```python
import torch

def _softmax(x, axis=-1):
    return torch.softmax(x, dim=axis)

def _gelu(x):
    return torch.nn.functional.gelu(x, approximate='tanh')

def _silu(x):
    return torch.nn.functional.silu(x)

def _sigmoid(x):
    return torch.sigmoid(x)

def _layernorm(x, axis=-1, eps=1e-05):
    m = x.mean(dim=axis, keepdim=True)
    v = x.var(dim=axis, keepdim=True, unbiased=False)
    return (x - m) / torch.sqrt(v + eps)

def _scatter_add(tgt, idx, src, axis=0):
    return tgt.clone().index_add_(axis, idx, src)

def _segment_sum(src, seg, n):
    out = torch.zeros((n,) + src.shape[1:], dtype=src.dtype)
    return out.index_add_(0, seg, src)

def _pad(x, pw):
    flat = tuple((v for pair in reversed(pw) for v in pair))
    return torch.nn.functional.pad(x, flat)
kernel_name = 'interleave_layers__memory_aug_recurrent_mem_tr__glu_gated_glu__prompt'

def setup_inputs():

    _w = lambda a: (lambda t: t.to(torch.bfloat16) if t.is_floating_point() else t)(torch.as_tensor(a))

    def _A_setup():
        B, S, Dm = (1, 2048, 640)
        H, Dh = (32, 192)
        M, seg = (8, 32)
        seg = min(seg, S)
        S = S // seg * seg
        return {'x': _w((lambda *s: torch.randn(*s, dtype=torch.bfloat16) * 0.05)(B, S, Dm)), 'mem0': _w((lambda *s: torch.randn(*s, dtype=torch.bfloat16) * 0.8)(B, M, Dm)), 'Wq': _w((lambda *s: torch.randn(*s, dtype=torch.bfloat16) * 0.8)(Dm, H, Dh) * Dm ** (-0.5)), 'Wk': _w((lambda *s: torch.randn(*s, dtype=torch.bfloat16) * 0.8)(Dm, H, Dh) * Dm ** (-0.5)), 'Wv': _w((lambda *s: torch.randn(*s, dtype=torch.bfloat16) * 0.8)(Dm, H, Dh) * Dm ** (-0.5)), 'Wo': _w((lambda *s: torch.randn(*s, dtype=torch.bfloat16) * 0.8)(H, Dh, Dm) * (H * Dh) ** (-0.5)), 'seg_len': seg}
    _A_in = _A_setup()

    def _B_setup():
        B, S, D, F = (1, 2048, 640, 1536)
        return {'x': _w((lambda *s: torch.randn(*s, dtype=torch.bfloat16) * 0.05)(B, S, D)), 'w_gate': _w((lambda *s: torch.randn(*s, dtype=torch.bfloat16) * 0.05)(D, F)), 'w_up': _w((lambda *s: torch.randn(*s, dtype=torch.bfloat16) * 0.05)(D, F)), 'w_down': _w((lambda *s: torch.randn(*s, dtype=torch.bfloat16) * 0.05)(F, D))}
    _B_in = _B_setup()
    return {'A_x': _A_in['x'], 'A_mem0': _A_in['mem0'], 'A_Wq': _A_in['Wq'], 'A_Wk': _A_in['Wk'], 'A_Wv': _A_in['Wv'], 'A_Wo': _A_in['Wo'], 'A_seg_len': _A_in['seg_len'], 'B_x': _B_in['x'], 'B_w_gate': _B_in['w_gate'], 'B_w_up': _B_in['w_up'], 'B_w_down': _B_in['w_down']}

def reference(A_x, A_mem0, A_Wq, A_Wk, A_Wv, A_Wo, A_seg_len, B_x, B_w_gate, B_w_up, B_w_down):

    def _A_ref(x, mem0, Wq, Wk, Wv, Wo, seg_len):
        B, S, Dm = x.shape
        H, Dh = (Wq.shape[1], Wq.shape[2])
        M = mem0.shape[1]
        scale = Dh ** (-0.5)
        n_seg = S // seg_len
        L = M + seg_len
        idx = torch.arange(L)
        mask = (idx[:, None] < M) | (idx[:, None] >= idx[None, :])
        mem = mem0
        outs = []
        for s in range(n_seg):
            xs = x[:, s * seg_len:(s + 1) * seg_len, :].clone()
            h = torch.concatenate([mem, xs], axis=1)
            q = torch.einsum('btd,dhe->bhte', h, Wq)
            k = torch.einsum('btd,dhe->bhte', h, Wk)
            v = torch.einsum('btd,dhe->bhte', h, Wv)
            scores = torch.einsum('bhqe,bhke->bhqk', q, k) * scale
            scores = torch.where(mask[None, None], scores, -1000000000.0)
            p = _softmax(scores, axis=-1)
            o = torch.einsum('bhqk,bhke->bhqe', p, v)
            o = torch.einsum('bhte,hed->btd', o, Wo)
            mem = o[:, :M, :].clone()
            outs.append(o[:, M:, :])
        return torch.concatenate(outs, axis=1)

    def _B_ref(x, w_gate, w_up, w_down):
        g = torch.einsum('bsd,df->bsf', x, w_gate)
        u = torch.einsum('bsd,df->bsf', x, w_up)
        h = _sigmoid(g) * u
        return torch.einsum('bsf,fd->bsd', h, w_down)
    x = A_x
    x = x + _A_ref(_layernorm(x, axis=-1), A_mem0, A_Wq, A_Wk, A_Wv, A_Wo, A_seg_len)
    x = x + _B_ref(_layernorm(x, axis=-1), B_w_gate, B_w_up, B_w_down)
    x = x + _A_ref(_layernorm(x, axis=-1), A_mem0, A_Wq, A_Wk, A_Wv, A_Wo, A_seg_len)
    x = x + _B_ref(_layernorm(x, axis=-1), B_w_gate, B_w_up, B_w_down)
    return x
```

```cpp
#include <hip/hip_runtime.h>
#include <hip/hip_bf16.h>
#include <stdint.h>
#include <utility>

typedef unsigned short u16;
typedef __attribute__((ext_vector_type(16))) short bf16x16;
typedef __attribute__((ext_vector_type(8))) short bf16x8;
typedef __attribute__((ext_vector_type(4))) short bf16x4;
typedef __attribute__((ext_vector_type(8))) __bf16 hbf8;
typedef __attribute__((ext_vector_type(16))) __bf16 hbf16;
typedef __attribute__((ext_vector_type(4))) float f32x4;
typedef __attribute__((ext_vector_type(2))) float f32x2;
typedef __attribute__((ext_vector_type(8))) unsigned u32x8;
typedef __attribute__((ext_vector_type(4))) unsigned u32x4;
typedef __attribute__((ext_vector_type(2))) unsigned u32x2;
typedef __attribute__((ext_vector_type(2))) __bf16 bf16x2_t;
#define LAS __attribute__((address_space(3)))
#define GAS __attribute__((address_space(1)))
#define DEVI __device__ __forceinline__

#define PSLEEP 8
#define WDELAY 700
#define CALR 3
#define SPG 3
#define BPOS 25
#define RDLY 0
#define NOYST 0
#define NOTOK 0
#define NOYLD 0
#define GPOSY 6
#define GPOS 19

#define EXP(o) ((((unsigned)(o) >> 13) << 14) | ((unsigned)(o) & 8191u))
enum { C_XN = 0, C_Y = 1, C_X1 = 2, C_XB = 3, C_H = 4, C_X2 = 5, C_BC = 6 };
#define CNT(type, s) ((unsigned)((type) * 16384 + (s) * 128))
constexpr unsigned PR_B = EXP(65536), PR_SZ = EXP(12 * 8192);
constexpr unsigned YM_B = EXP(458752);
constexpr unsigned DUMMY_B = EXP(0);
constexpr int PG_MEMF = 2;
#define YXR 8
constexpr int PG_YX = 3;
constexpr int PG_XNA = PG_YX + 2 * YXR;
constexpr int PG_XNB = PG_XNA + 4, PG_X1 = PG_XNB + 4, PG_X2 = PG_X1 + 4;
constexpr int PG_H = PG_X2 + 4;
constexpr int NPAGES = PG_H + 8;
static_assert(NPAGES <= 64, "map");
constexpr unsigned CM_TAB = 0, CM_TICKET = 1024, CM_TMO = 4096, CM_BYTES = 8192;
constexpr unsigned SPIN_CAP = 1u << 20;
#define AUX_SC1 16
#define LDS_BYTES (160 * 1024)

struct Params {
    const u16* x; const u16* mem0; const u16* Wq; const u16* Wk; const u16* Wv; const u16* Wo;
    const u16* wg; const u16* wu; const u16* wd; u16* out; char* cm; char* ws;
};
struct Ctx {
    char* ws; char* cm; __amdgpu_buffer_rsrc_t rs; unsigned long long par;
    int c, l, w, role;
    unsigned p0;
    DEVI unsigned pg(int page) const { return ((unsigned)page << 21) | (((unsigned)(par >> page) & 1u) << 13); }
    DEVI unsigned pg0(int page) const { return pg(page) ^ ((unsigned)c << 13); }
    DEVI unsigned seg(int page, int s) const { return pg(page + (s >> 4)) + (unsigned)(s & 15) * 131072u; }
    DEVI unsigned seg0(int page, int s) const { return seg(page, s) ^ ((unsigned)c << 13); }
    DEVI unsigned hseg(int s) const { return pg(PG_H + (s >> 3)) + (unsigned)(s & 7) * 262144u; }
    DEVI unsigned yx(int sg, int wq) const { return pg(PG_YX + (sg % YXR) * 2 + (wq >> 1)) + (unsigned)(wq & 1) * 1048576u; }
    DEVI unsigned memf(int s) const { return pg(PG_MEMF) + (unsigned)s * 32768u; }
    DEVI char* cnt(int type, int s) const { return ws + (p0 + CNT(type, s)); }
    DEVI char* cnt0(int type, int s) const { return ws + ((p0 ^ ((unsigned)c << 13)) + CNT(type, s)); }
};

DEVI unsigned cvtpk(float a, float b) { return __builtin_bit_cast(unsigned, __builtin_convertvector((f32x2){a, b}, bf16x2_t)); }
DEVI unsigned packs(float a, float b) { return min(cvtpk(a, b), 0xFFFFFFFEu); }
DEVI float blo(unsigned x) { return __uint_as_float(x << 16); }
DEVI float bhi(unsigned x) { return __uint_as_float(x & 0xFFFF0000u); }
DEVI float bfr(float f) { return blo(cvtpk(f, f)); }
DEVI u32x4 ld128(__amdgpu_buffer_rsrc_t rs, unsigned v, unsigned s) { return __builtin_amdgcn_raw_buffer_load_b128(rs, (int)v, (int)s, AUX_SC1); }
DEVI u32x2 ld64(__amdgpu_buffer_rsrc_t rs, unsigned v, unsigned s) { return __builtin_amdgcn_raw_buffer_load_b64(rs, (int)v, (int)s, AUX_SC1); }
DEVI unsigned ld32(__amdgpu_buffer_rsrc_t rs, unsigned v, unsigned s) { return __builtin_amdgcn_raw_buffer_load_b32(rs, (int)v, (int)s, AUX_SC1); }
DEVI void st128(__amdgpu_buffer_rsrc_t rs, unsigned v, unsigned s, u32x4 x) {
    __builtin_amdgcn_raw_buffer_store_b128(x, rs, (int)v, (int)s, AUX_SC1);
    asm volatile("s_nop 1" :: "v"(x) : "memory");
}
DEVI void st64(__amdgpu_buffer_rsrc_t rs, unsigned v, unsigned s, u32x2 x) { __builtin_amdgcn_raw_buffer_store_b64(x, rs, (int)v, (int)s, AUX_SC1); }
DEVI void st32(__amdgpu_buffer_rsrc_t rs, unsigned v, unsigned s, unsigned x) { __builtin_amdgcn_raw_buffer_store_b32(x, rs, (int)v, (int)s, AUX_SC1); }
#define RAWBAR() { __builtin_amdgcn_s_waitcnt(0xc07f); __builtin_amdgcn_s_barrier(); }
#define DRAIN() asm volatile("s_waitcnt vmcnt(0)" ::: "memory")
#define SCHED0() __builtin_amdgcn_sched_barrier(0)
#define OOB 0x80000000u
DEVI int opq(int x) { asm volatile("" : "+v"(x)); return x; }
#define LANE const int l = opq(k.l), g = l >> 4, n = l & 15; (void)g; (void)n;
DEVI unsigned umax3(unsigned a, unsigned b, unsigned c) { return max(max(a, b), c); }
DEVI unsigned umax4(u32x4 v) { return max(umax3(v[0], v[1], v[2]), v[3]); }
DEVI unsigned rclk() { return (unsigned)__builtin_amdgcn_s_memrealtime(); }
DEVI void sleep_ticks(int d) { unsigned target = rclk() + d; while ((int)(rclk() - target) < 0) __builtin_amdgcn_s_sleep(4); }
DEVI float xor16max(float x) { auto r = __builtin_amdgcn_permlane16_swap(__float_as_uint(x), __float_as_uint(x), false, false); return __builtin_fmaxf(__uint_as_float(r[0]), __uint_as_float(r[1])); }
DEVI float xor32max(float x) { auto r = __builtin_amdgcn_permlane32_swap(__float_as_uint(x), __float_as_uint(x), false, false); return __builtin_fmaxf(__uint_as_float(r[0]), __uint_as_float(r[1])); }
DEVI float xor16add(float x) { auto r = __builtin_amdgcn_permlane16_swap(__float_as_uint(x), __float_as_uint(x), false, false); return __uint_as_float(r[0]) + __uint_as_float(r[1]); }
DEVI float xor32add(float x) { auto r = __builtin_amdgcn_permlane32_swap(__float_as_uint(x), __float_as_uint(x), false, false); return __uint_as_float(r[0]) + __uint_as_float(r[1]); }
DEVI float fold32(float a, float b) { auto r = __builtin_amdgcn_permlane32_swap(__float_as_uint(a), __float_as_uint(b), false, false); return __uint_as_float(r[0]) + __uint_as_float(r[1]); }
#define ROR16(x, n) __builtin_bit_cast(float, __builtin_amdgcn_update_dpp(0, __builtin_bit_cast(int, (x)), 0x120 + (n), 0xf, 0xf, false))
DEVI float row16sum(float x) { x += ROR16(x, 8); x += ROR16(x, 4); x += ROR16(x, 2); x += ROR16(x, 1); return x; }
DEVI void cnt_add(char* p) { __hip_atomic_fetch_add((GAS unsigned*)p, 1u, __ATOMIC_RELAXED, __HIP_MEMORY_SCOPE_AGENT); }
DEVI unsigned cnt_get(char* p) { return __hip_atomic_load((GAS unsigned*)p, __ATOMIC_RELAXED, __HIP_MEMORY_SCOPE_AGENT) + 1u; }
DEVI bool spin_fail(const Ctx& k, unsigned spins) {
    if (spins > SPIN_CAP || ((spins & 255u) == 255u && cnt_get(k.cm + CM_TMO) != 0u)) { cnt_add(k.cm + CM_TMO); return true; }
    return false;
}
DEVI void cnt_wait(const Ctx& k, char* c, unsigned need) {
_Pragma("unroll 1")
    for (unsigned spins = 0; (unsigned)__builtin_amdgcn_readfirstlane(cnt_get(c)) < need; ++spins) {
        __builtin_amdgcn_s_sleep(PSLEEP);
        if (spin_fail(k, spins)) break;
    }
    __builtin_amdgcn_fence(__ATOMIC_ACQUIRE, "wavefront");
}
DEVI void dma1(const void* sbase, unsigned voff, unsigned lds) {
    asm volatile("s_mov_b32 m0, %2\n\ts_nop 0\n\tglobal_load_lds_dwordx4 %0, %1" :: "v"(voff), "s"(sbase), "s"(lds) : "memory");
}
DEVI void set_m0(unsigned lds) { asm volatile("s_mov_b32 m0, %0" :: "s"(lds) : "memory"); }
template <int OFF> DEVI void dmab(__amdgpu_buffer_rsrc_t rs, unsigned voff, unsigned soff) {
    asm volatile("buffer_load_dwordx4 %0, %1, %2 offen offset:%3 sc1 lds" :: "v"(voff), "s"(rs), "s"(soff), "n"(OFF) : "memory");
}
#define PIN(x) asm volatile("" : "+v"(x))
#define PINS(x) asm volatile("" : "+s"(x))
DEVI float csel(float a, float b, unsigned long long m) { float d; asm volatile("v_cndmask_b32_e64 %0, %1, %2, %3" : "=v"(d) : "v"(a), "v"(b), "s"(m)); return d; }
#define LO8(x) __builtin_shufflevector(x, x, 0, 1, 2, 3, 4, 5, 6, 7)
#define HI8(x) __builtin_shufflevector(x, x, 8, 9, 10, 11, 12, 13, 14, 15)
#define CAT8(a, b) __builtin_shufflevector(a, b, 0, 1, 2, 3, 4, 5, 6, 7, 8, 9, 10, 11, 12, 13, 14, 15)
template <bool AG> DEVI void smf(f32x4& c, const bf16x8& a, const bf16x16& w, unsigned ix) {
    if constexpr (AG) asm volatile("v_smfmac_f32_16x16x64_bf16 %0, %1, %2, %3" : "+v"(c) : "v"(a), "a"(w), "v"(ix));
    else asm volatile("v_smfmac_f32_16x16x64_bf16 %0, %1, %2, %3" : "+v"(c) : "v"(a), "v"(w), "v"(ix));
}
template <bool AG, bool FIRST> DEVI void mfa(f32x4& c, const bf16x8& w, const bf16x8& x) {
    if constexpr (AG && FIRST) asm volatile("v_mfma_f32_16x16x32_bf16 %0, %1, %2, 0" : "=&v"(c) : "a"(w), "v"(x));
    else if constexpr (AG) asm volatile("v_mfma_f32_16x16x32_bf16 %0, %1, %2, %0" : "+v"(c) : "a"(w), "v"(x));
    else if constexpr (FIRST) asm volatile("v_mfma_f32_16x16x32_bf16 %0, %1, %2, 0" : "=&v"(c) : "v"(w), "v"(x));
    else asm volatile("v_mfma_f32_16x16x32_bf16 %0, %1, %2, %0" : "+v"(c) : "v"(w), "v"(x));
}
template <bool AG, bool FIRST> DEVI void mfb(f32x4& c, const bf16x8& x, const bf16x8& w) {
    if constexpr (AG && FIRST) asm volatile("v_mfma_f32_16x16x32_bf16 %0, %1, %2, 0" : "=&v"(c) : "v"(x), "a"(w));
    else if constexpr (AG) asm volatile("v_mfma_f32_16x16x32_bf16 %0, %1, %2, %0" : "+v"(c) : "v"(x), "a"(w));
    else if constexpr (FIRST) asm volatile("v_mfma_f32_16x16x32_bf16 %0, %1, %2, 0" : "=&v"(c) : "v"(x), "v"(w));
    else asm volatile("v_mfma_f32_16x16x32_bf16 %0, %1, %2, %0" : "+v"(c) : "v"(x), "v"(w));
}
DEVI f32x4 mf(const bf16x8& a, const bf16x8& b, const f32x4& c) { return __builtin_amdgcn_mfma_f32_16x16x32_bf16(__builtin_bit_cast(hbf8, a), __builtin_bit_cast(hbf8, b), c, 0, 0, 0); }
template <int N, class F> DEVI void sfor(F f) { [&]<int... I>(std::integer_sequence<int, I...>) { (f(std::integral_constant<int, I>{}), ...); }(std::make_integer_sequence<int, N>{}); }
DEVI bf16x8 lds128(const char* p) { return *(const bf16x8*)p; }
DEVI bf16x4 ldstr(const char* p) { return __builtin_amdgcn_ds_read_tr16_b64_v4i16((LAS bf16x4*)p); }
DEVI bf16x8 ldstr2(const char* p, int d) { bf16x4 a = ldstr(p), b = ldstr(p + d); return __builtin_shufflevector(a, b, 0, 1, 2, 3, 4, 5, 6, 7); }

#define STAMP(s, k)
#define STOP(s)
#define PROF_DECL
#define PCOUNT(i)
#define PROF_OUT

template <int NT> DEVI void wp_issue(const u16* W, int ld, unsigned slot, int l) {
#pragma unroll
    for (int i = 0; i < NT; ++i) {
        const int id = i * 64 + l, row = id / (2 * NT), cc = id % (2 * NT);
        dma1(W, (unsigned)(row * ld + cc * 8) * 2u, slot + i * 1024);
    }
}
template <int NT, bool STD> DEVI bf16x8 wp_read(const char* slot, int T, int l) {
    const int row = (STD ? 8 : 4) * (l >> 4) + ((l >> 2) & 3);
    return ldstr2(slot + row * (NT * 32) + T * 32 + (l & 3) * 8, (STD ? 4 : 16) * NT * 32);
}
template <int NU, int NI, int D, class ISSUE, class READ> DEVI void wstream(char* ring, ISSUE issue, READ read) {
    const unsigned base = (unsigned)(size_t)(LAS char*)ring;
    sfor<D>([&](auto U) { constexpr int u = decltype(U)::value; if constexpr (u < NU) issue(U, base + u * NI * 1024); });
    sfor<NU>([&](auto U) {
        constexpr int u = decltype(U)::value, left = (NU - u < D ? NU - u : D) - 1;
        asm volatile("s_waitcnt vmcnt(%0)" :: "n"(left * NI) : "memory");
        read(U, ring + (u % D) * NI * 1024);
        __builtin_amdgcn_s_waitcnt(0xc07f);
        asm volatile("" ::: "memory");
        if constexpr (u + D < NU) issue(std::integral_constant<int, u + D>{}, base + (u % D) * NI * 1024);
    });
}

constexpr int L_XN = 0;
constexpr int L_K = 40960;
constexpr int L_MEM = L_K + 12288;
constexpr int L_QM = L_MEM + 10240;
constexpr int L_Q = L_QM + 6144;
constexpr int L_W = L_Q + 12288;
#define IC(x) std::integral_constant<int, (x)>{}
#define CV(X) decltype(X)::value
DEVI bf16x8 lr128(unsigned a) { return *(const LAS bf16x8*)(size_t)a; }
DEVI bf16x4 lrtr(unsigned a) { return __builtin_amdgcn_ds_read_tr16_b64_v4i16((LAS bf16x4*)(size_t)a); }
#define CAT4(a, b) __builtin_shufflevector(a, b, 0, 1, 2, 3, 4, 5, 6, 7)
DEVI void lw128(unsigned a, u32x4 v) { *(LAS u32x4*)(size_t)a = v; }
DEVI void lw64(unsigned a, u32x2 v) { *(LAS u32x2*)(size_t)a = v; }
DEVI void lw32(unsigned a, unsigned v) { *(LAS unsigned*)(size_t)a = v; }
template <int N> DEVI void bar_lgkm() { asm volatile("s_waitcnt lgkmcnt(%0)\n\ts_barrier" :: "n"(N) : "memory"); }
DEVI void atom1(__amdgpu_buffer_rsrc_t rs, unsigned voff, unsigned soff) {
    unsigned one; asm volatile("v_mov_b32 %0, 1\n\tbuffer_atomic_add %0, %1, %2, %3 offen" : "=&v"(one) : "v"(voff), "s"(rs), "s"(soff) : "memory");
}
DEVI float vzero() { float z; asm volatile("v_mov_b32 %0, 0" : "=v"(z)); return z; }
template <int B, int I> DEVI void xn_op(const Ctx& k, unsigned v, unsigned src, unsigned dst) {
    if constexpr (I == 0) set_m0(dst + B * 8192);
    else if constexpr (I == 1) dmab<0>(k.rs, v, src + B * 16384);
    else dmab<1024>(k.rs, v, src + B * 16384);
}
DEVI void xn_all(const Ctx& k, unsigned v, unsigned src, unsigned dst) {
    sfor<5>([&](auto B) { xn_op<CV(B), 0>(k, v, src, dst); asm volatile("s_nop 0"); xn_op<CV(B), 1>(k, v, src, dst); xn_op<CV(B), 2>(k, v, src, dst); });
}
struct Gather {
    u32x4 v0, v1, v2;
    template <int I> DEVI void op(const Ctx& k, unsigned l16, unsigned a) {
        if constexpr (I == 0) v0 = ld128(k.rs, l16, a);
        if constexpr (I == 1) v1 = ld128(k.rs, l16 + 1024, a);
        if constexpr (I == 2) v2 = ld128(k.rs, l16, a + 16384 - (k.w >> 1) * 3072 - (k.w & 1) * 2048);
    }
    DEVI bool ok() const { return __all(umax3(umax4(v0), umax4(v1), umax4(v2)) != 0xFFFFFFFFu); }
};
#define GATHER_FINISH(G, a) { unsigned spins_ = 0; \
    _Pragma("unroll 1") while (!G.ok()) { PCOUNT(0) asm volatile("" ::: "memory"); if (spin_fail(k, spins_++)) break; G.op<0>(k, l16, a); G.op<1>(k, l16, a); G.op<2>(k, l16, a); } \
    lw128(lwv + (L_MEM - L_W) - w * 14336, G.v0); lw128(lwv + (L_MEM - L_W) - w * 14336 + 1024, G.v1); lw128(l16 + L_MEM + 8192 + (w >> 1) * 1024, G.v2); }
#define NOP7_3(a) asm volatile("s_nop 7" : "+v"(a[0]), "+v"(a[1]), "+v"(a[2]))

struct G3 {
    f32x4 a0[3], a1[3];
    bf16x8 x0[20], x1[20], wl[20];
    unsigned l16, lwv;
    template <int KS> DEVI void r0() { x0[KS] = lr128(l16 + L_XN + KS * 2048); }
    template <int KS> DEVI void r1() { x1[KS] = lr128(l16 + L_XN + KS * 2048 + 1024); }
    template <int KS> DEVI void rw() { if constexpr (KS >= 4) wl[KS] = lr128(lwv + (KS - 4) * 1024); }
    template <int ID0, bool WL, int ks, int m> DEVI void mm(const bf16x16 (&W)[52]) {
        constexpr int b = ks >> 1, t = m % 3, id = ID0 + t * 10 + b;
        f32x4& c = m < 3 ? a0[t] : a1[t];
        const bf16x8& x = m < 3 ? x0[ks] : x1[ks];
        if constexpr (WL && t == 2 && b >= 2) mfa<false, false>(c, wl[ks], x);
        else if constexpr (ks & 1) mfa<(id < 32), false>(c, HI8(W[id % 52]), x);
        else mfa<(id < 32), ks == 0>(c, LO8(W[id % 52]), x);
        SCHED0();
    }
    template <int ID0, bool WL, int K0, int K1, int PF, class HOOK> DEVI void run(const bf16x16 (&W)[52], HOOK hook) {
        sfor<K1 - K0>([&](auto KK) {
            constexpr int kk = CV(KK), ks = K0 + kk;
            sfor<6>([&](auto M) {
                constexpr int m = CV(M);
                mm<ID0, WL, ks, m>(W);
                if constexpr (ks + 2 < PF) {
                    if constexpr (m == 0) r0<(ks + 2) % 20>();
                    if constexpr (m == 1) r1<(ks + 2) % 20>();
                    if constexpr (m == 3 && WL) rw<(ks + 2) % 20>();
                }
                hook(IC(kk * 7 + (m < 5 ? m : 6)));
                if constexpr (m == 4) hook(IC(kk * 7 + 5));
                SCHED0();
            });
        });
    }
    u32x2 o;
    template <int I, int OFF> DEVI void out(unsigned d0, unsigned d1) {
        if constexpr (I >= 0 && I < 18) {
            constexpr int t = I / 6, rt = (I / 3) & 1;
            const f32x4& a = rt ? a1[t] : a0[t];
            if constexpr (I % 3 == 0) { o[0] = cvtpk(a[0], a[1]); PIN(o[0]); }
            else if constexpr (I % 3 == 1) { o[1] = cvtpk(a[2], a[3]); PIN(o[1]); }
            else lw64((t == 1 ? d1 : d0) + OFF + rt * 6144 + (t == 2 ? 1024 : 0), o);
        }
    }
};
struct Fold {
    float f[4], h[4]; u32x2 o;
    template <int I, bool GUARD> DEVI void op(const f32x4& a, const f32x4& b) {
        if constexpr (I < 0) {}
        else if constexpr (I < 4) { auto r = __builtin_amdgcn_permlane32_swap(__float_as_uint(a[I]), __float_as_uint(b[I]), false, false); f[I] = __uint_as_float(r[0]); h[I] = __uint_as_float(r[1]); PIN(f[I]); PIN(h[I]); }
        else if constexpr (I < 8) { f[I - 4] += h[I - 4]; PIN(f[I - 4]); }
        else if constexpr (I < 10) { o[I - 8] = cvtpk(f[2 * (I - 8)], f[2 * (I - 8) + 1]); PIN(o[I - 8]); }
        else if constexpr (I < 12 && GUARD) { o[I - 10] = min(o[I - 10], 0xFFFFFFFEu); PIN(o[I - 10]); }
    }
};

struct Soft {
    unsigned long long mA, mB, mC[4], mM[2], mD;
    unsigned v0, vA, vB;
    float neg;
    float x[8], mx, t, u; unsigned pk[6];
    static constexpr int NP = 107, N = 115;
    DEVI static unsigned usel(unsigned a, unsigned b, unsigned long long m) { return __float_as_uint(csel(__uint_as_float(a), __uint_as_float(b), m)); }
    template <int I> DEVI void op(const Ctx& k, const f32x4 (&sc)[3], unsigned a0, unsigned a1, unsigned b0, unsigned b1) {
        const float L2E = 1.4426950408889634f, scale = 0.07216878364870322f;
#define SWAP16(p, q) { auto r = __builtin_amdgcn_permlane16_swap(__float_as_uint(p), __float_as_uint(q), false, false); p = __uint_as_float(r[0]); q = __uint_as_float(r[1]); PIN(p); PIN(q); }
#define SWAP32(p, q) { auto r = __builtin_amdgcn_permlane32_swap(__float_as_uint(p), __float_as_uint(q), false, false); p = __uint_as_float(r[0]); q = __uint_as_float(r[1]); PIN(p); PIN(q); }
        if constexpr (I < 6) { constexpr int j = (I + 4) % 6; pk[j] = cvtpk(sc[(I >> 1)][2 * (I & 1)], sc[(I >> 1)][2 * (I & 1) + 1]); PIN(pk[j]);
        } else if constexpr (I == 6) { t = __uint_as_float(usel(pk[0], pk[5], mB));
        } else if constexpr (I == 7) { u = __uint_as_float(usel(pk[5], pk[0], mB));
        } else if constexpr (I == 8) { pk[0] = usel(pk[4], __float_as_uint(t), mA);
        } else if constexpr (I == 9) { pk[1] = usel(__float_as_uint(u), pk[1], mA); asm volatile("v_mov_b32 %0, 0xf149f149" : "=v"(neg));
        } else if constexpr (I < 18) { constexpr int i = (I - 10 + 4) & 7; x[i] = (i & 1) ? bhi(pk[i >> 1]) : blo(pk[i >> 1]); PIN(x[i]);
        } else if constexpr (I < 26) { constexpr int i = (I - 18 + 4) & 7; x[i] *= scale; PIN(x[i]);
        } else if constexpr (I < 30) { constexpr int i = (I - 26 + 2) & 3; pk[i] = cvtpk(x[2 * i], x[2 * i + 1]); PIN(pk[i]);
        } else if constexpr (I < 32) { constexpr int i = I - 30; pk[2 + i] = usel(__float_as_uint(neg), pk[2 + i], mM[i]);
        } else if constexpr (I < 40) { constexpr int i = I - 32; x[i] = (i & 1) ? bhi(pk[i >> 1]) : blo(pk[i >> 1]); PIN(x[i]);
        } else if constexpr (I < 44) { constexpr int i = I - 40; x[i] = csel(neg, x[i], mC[i]);
        } else if constexpr (I == 44) { mx = __builtin_fmaxf(__builtin_fmaxf(x[4], x[5]), x[6]); PIN(mx);
        } else if constexpr (I == 45) { t = __builtin_fmaxf(__builtin_fmaxf(x[7], x[0]), x[1]); PIN(t);
        } else if constexpr (I == 46) { mx = __builtin_fmaxf(__builtin_fmaxf(mx, x[2]), x[3]); PIN(mx);
        } else if constexpr (I == 47) { mx = __builtin_fmaxf(mx, t); PIN(mx);
        } else if constexpr (I == 48) { t = ROR16(mx, 8); PIN(t);
        } else if constexpr (I == 49) { t = csel(mx, t, mD);
        } else if constexpr (I == 50) { mx = __builtin_fmaxf(mx, t); PIN(mx);
        } else if constexpr (I == 51) { t = mx; PIN(t);
        } else if constexpr (I == 52) { SWAP16(mx, t)
        } else if constexpr (I == 53) { mx = __builtin_fmaxf(mx, t); PIN(mx);
        } else if constexpr (I == 54) { t = mx; PIN(t);
        } else if constexpr (I == 55) { SWAP32(mx, t)
        } else if constexpr (I == 56) { mx = __builtin_fmaxf(mx, t); PIN(mx);
        } else if constexpr (I == 57) { mx = -mx * L2E; PIN(mx);
        } else if constexpr (I < 74) { constexpr int j = I - 58, i = (j >> 2) * 2 + (j & 1);
            if constexpr (j & 2) x[i] = __builtin_amdgcn_exp2f(x[i]); else x[i] = __builtin_fmaf(x[i], L2E, mx);
            PIN(x[i]);
        } else if constexpr (I == 74) { t = x[0] + x[1]; PIN(t);
        } else if constexpr (I == 75) { u = x[2] + x[3]; PIN(u);
        } else if constexpr (I == 76) { t += x[4]; PIN(t);
        } else if constexpr (I == 77) { u += x[5]; PIN(u);
        } else if constexpr (I == 78) { t += x[6]; PIN(t);
        } else if constexpr (I == 79) { u += x[7]; PIN(u);
        } else if constexpr (I == 80) { u += t; PIN(u);
        } else if constexpr (I == 81) { t = ROR16(u, 8); PIN(t);
        } else if constexpr (I == 82) { asm volatile("v_cndmask_b32_e64 %0, 0, %0, %1" : "+v"(t) : "s"(mD));
        } else if constexpr (I == 83) { u += t; PIN(u);
        } else if constexpr (I == 84) { t = u; PIN(t);
        } else if constexpr (I == 85) { SWAP16(u, t)
        } else if constexpr (I == 86) { u += t; PIN(u);
        } else if constexpr (I == 87) { t = u; PIN(t);
        } else if constexpr (I == 88) { SWAP32(u, t)
        } else if constexpr (I == 89) { u += t; PIN(u);
        } else if constexpr (I == 90) { u = __builtin_amdgcn_rcpf(u); PIN(u);
        } else if constexpr (I < 99) { constexpr int i = I - 91; x[i] *= u; PIN(x[i]);
        } else if constexpr (I < 103) { constexpr int i = I - 99; pk[i] = min(cvtpk(x[2 * i], x[2 * i + 1]), 0xFFFFFFFEu); PIN(pk[i]);
        } else if constexpr (I == 104) { st64(k.rs, v0, a0, (u32x2){pk[0], pk[1]});
        } else if constexpr (I == 105) { st32(k.rs, vA, a1, pk[2]);
        } else if constexpr (I == 106) { st32(k.rs, vB, a1, pk[3]);
        } else if constexpr (I == 108) { pk[0] = 0xFFFFFFFFu; PIN(pk[0]);
        } else if constexpr (I == 109) { pk[1] = 0xFFFFFFFFu; PIN(pk[1]);
        } else if constexpr (I == 110) { st64(k.rs, v0, b0, (u32x2){pk[0], pk[1]});
        } else if constexpr (I == 112) { st32(k.rs, vA, b1, pk[0]);
        } else if constexpr (I == 114) { st32(k.rs, vB, b1, pk[0]);
        }
    }
};
#define QIDX(t, b) ((t) * 10 + (b))
DEVI void qk_role(const Params& p, const Ctx& k, char* smem, int h) {
    PROF_DECL
    const int w = k.w;
    if (k.c) sleep_ticks(WDELAY);
    bf16x16 W[52];
    {
        const int l = k.l;
        bf16x8 lo[6], wt[16];
        const int col = h * 192 + w * 48;
        wstream<20, 6, 6>(smem + w * 36864,
            [&](auto U, unsigned sl) { constexpr int u = CV(U);
                wp_issue<3>(p.Wk + (size_t)u * 32 * 6144 + col, 6144, sl, l); wp_issue<3>(p.Wq + (size_t)u * 32 * 6144 + col, 6144, sl + 3072, l); },
            [&](auto U, char* sp) { constexpr int u = CV(U), b = u >> 1;
#pragma unroll
                for (int t = 0; t < 6; ++t) {
                    bf16x8 f = wp_read<3, true>(sp + (t / 3) * 3072, t % 3, l);
                    if (t == 5 && b >= 2) wt[(u - 4) & 15] = f;
                    else if (u & 1) W[QIDX(t, b) % 52] = CAT8(lo[t], f);
                    else lo[t] = f;
                } });
        RAWBAR()
#pragma unroll
        for (int i = 0; i < 16; ++i) *(bf16x8*)(smem + L_W + w * 16384 + i * 1024 + l * 16) = wt[i];
    }
    Gather G;
    Soft S;
    const int hw = h >> 3, hh = h & 7;
    unsigned l16, lwv, ix, dqm, trp, qp, d0, d1, vbc;
    {
        const int l = k.l, g = l >> 4, n = l & 15, r = n & 7, u = w & 1;
        const bool hf = n >= 8, dup = w != 1;
        S.mA = __ballot(dup && hf); S.mB = __ballot(w == 0); S.mD = __ballot(dup);
        const int lim = w == 1 ? n - 4 * g : (w == 0 || !hf) ? 64 : 8 * u + r - 4 * g;
#pragma unroll
        for (int i = 0; i < 4; ++i) S.mC[i] = __ballot(i <= lim);
        const bool okA = g < 2 && (!dup || !hf), okB = g < 2 && (!dup || hf);
        S.mM[0] = __ballot(okA); S.mM[1] = __ballot(okB);
        S.v0 = w == 0 ? hh * 512 + ((g >> 1) * 16 + n) * 16 + (g & 1) * 8 : w == 1 ? hh * 512 + l * 8 : hh * 1024 + (g * 16 + 8 * u + r) * 16 + hf * 8;
        const unsigned vm = (w == 0 ? 4096 + hh * 128 + r * 16 : w == 1 ? 4096 + hh * 256 + n * 16 : 6144 + hh * 256 + (8 * u + r) * 16) + g * 8;
        S.vA = okA ? vm : OOB; S.vB = okB ? vm + 4 : OOB;
        l16 = l * 16; lwv = l16 + L_W + w * 16384; ix = (l & 8) ? 0xEEEEu : 0x4444u;
        dqm = L_QM + (g >> 1) * 3072 + (48 * w + n) * 16 + (g & 1) * 8;
        trp = L_QM + (4 * g + ((l >> 2) & 3)) * 16 + (l & 1) * 8;
        qp = w == 1 ? L_Q + l16 : L_Q + 6144 + (g * 16 + 8 * u + r) * 16;
        d0 = L_K + l16 + ((3 * w) >> 1) * 1024 + ((3 * w) & 1) * 8; d1 = L_K + l16 + ((3 * w + 1) >> 1) * 1024 + ((3 * w + 1) & 1) * 8;
        vbc = (hh == 0 && w == 1 && l == 0) ? 0u : OOB;
        PIN(S.v0); PIN(S.vA); PIN(S.vB); PIN(l16); PIN(lwv); PIN(ix); PIN(dqm); PIN(trp); PIN(qp); PIN(d0); PIN(d1); PIN(vbc);
    }
    const unsigned po0 = k.p0 + PR_B + hw * 49152 + (w == 0 ? 0 : w == 1 ? 32768 : 16384), po1 = k.p0 + PR_B + hw * 49152 + (w == 0 ? 0 : 32768);
    const unsigned pdum = k.pg(1) + DUMMY_B, xdst = L_XN + w * 2048;
    bool xn_ok = false;
    G3 A, B;
    A.l16 = l16; A.lwv = lwv; B.l16 = l16; B.lwv = lwv;
#pragma unroll
    for (int t = 0; t < 3; ++t) { B.a0[t] = (f32x4){0.f, 0.f, 0.f, 0.f}; B.a1[t] = B.a0[t]; }
    B.x0[18] = lr128(l16); B.x1[18] = B.x0[18]; B.wl[18] = B.x0[18]; B.x0[19] = B.x0[18]; B.x1[19] = B.x0[18]; B.wl[19] = B.x0[18];
    for (int s = -1; s < 64; ++s) {
        RAWBAR()
        if (s >= 0) { STOP(s) } STAMP(s, 0)
        const unsigned xsrc = k.seg(PG_XNA, (s + 1) & 63) + w * 2048;
        f32x4 sc[3];
        bf16x8 q[6], k0[6], k1[6], km[6];
        {
            bf16x8 m[10], wa[10], wb[10];
            f32x2 z[12];
            f32x4 ac[6];
            Fold F;
            m[0] = lr128(l16 + L_MEM); m[1] = lr128(l16 + L_MEM + 1024);
            SCHED0();
            B.run<30, true, 18, 20, 0>(W, [&](auto I) { constexpr int i = CV(I);
                if constexpr (i < 12) { asm volatile("v_mov_b64 %0, 0" : "=v"(z[i])); if constexpr (i & 1) ac[i >> 1] = __builtin_shufflevector(z[i - 1], z[i], 0, 1, 2, 3); } });
            sfor<60>([&](auto I) {
                constexpr int i = CV(I), u = i - 42, b = i < 42 ? i / 6 : 7 + (u % 6) / 2, t = i < 42 ? i % 6 : (u / 6 + 2) % 3 + 3 * (u & 1), id = QIDX(t, b);
                if constexpr (t == 5 && b >= 2) smf<false>(ac[5], m[b], CAT8(wa[b], wb[b]), ix);
                else smf<(id < 32)>(ac[t], m[b], W[id % 52], ix);
                SCHED0();
                if constexpr (i < 42) {
                    constexpr int bb = i / 6 + 2;
                    if constexpr (i % 6 == 0 && bb < 9) m[bb] = lr128(l16 + L_MEM + bb * 1024);
                    if constexpr (i % 6 == 1) wa[bb] = lr128(lwv + (bb - 2) * 2048);
                    if constexpr (i % 6 == 2) wb[bb] = lr128(lwv + (bb - 2) * 2048 + 1024);
                }
                if constexpr (i == 33) m[9] = lr128(l16 + L_MEM + 9216);
                if constexpr (i == 39) wa[9] = lr128(lwv + 14336);
                if constexpr (i == 40) wb[9] = lr128(lwv + 15360);
                if constexpr (i >= 2 && i < 20) B.out<i - 2, L_Q - L_K>(d0, d1);
                if constexpr (u == 6) { k0[0] = lr128(l16 + L_K); k1[0] = lr128(l16 + L_K + 6144); }
                if constexpr (u == 7) { k0[1] = lr128(l16 + L_K + 1024); k1[1] = lr128(l16 + L_K + 7168); }
                if constexpr (u >= 9 && u < 14) { F.op<2 * (u - 9), false>(ac[5], ac[2]); F.op<2 * (u - 9) + 1, false>(ac[5], ac[2]); }
                if constexpr (u == 14) lw64(dqm + 512, F.o);
                if constexpr (u >= 15) { F.op<2 * (u - 15), false>(ac[3], ac[0]); F.op<2 * (u - 15) + 1, false>(ac[3], ac[0]); }
                SCHED0();
            });
            sfor<4>([&](auto I) { F.op<6 + CV(I), false>(ac[3], ac[0]); });
            lw64(dqm, F.o);
            asm volatile("s_nop 0" : "+v"(ac[1]), "+v"(ac[4]));
            sfor<10>([&](auto I) { F.op<CV(I), false>(ac[4], ac[1]); });
            lw64(dqm + 256, F.o);
        }
        if (!xn_ok && s < 63) {
            PCOUNT(1)
            cnt_wait(k, k.cnt(C_XN, s + 1), 8u);
            sfor<3>([&](auto Bk) { xn_op<CV(Bk), 0>(k, l16, xsrc, xdst); asm volatile("s_nop 0"); xn_op<CV(Bk), 1>(k, l16, xsrc, xdst); xn_op<CV(Bk), 2>(k, l16, xsrc, xdst); });
            DRAIN();
        }
        RAWBAR()
        STAMP(s, 1)
        const unsigned xv = s < 63 ? l16 : OOB;
        auto scores = [&](auto MODE) {
            constexpr int mode = CV(MODE);
            auto rq = [&](int ks) { if (mode == 0) q[ks] = CAT4(lrtr(trp + ks * 512), lrtr(trp + ks * 512 + 256)); else q[ks] = lr128(qp + ks * 1024); };
            auto rm = [&](int ks) { km[ks] = CAT4(lrtr(trp + 3072 + ks * 512), lrtr(trp + 3072 + ks * 512 + 256)); };
            rq(0); rm(0); rq(1); rm(1);
            SCHED0();
            sfor<6>([&](auto I) { xn_op<3 + CV(I) / 3, CV(I) % 3>(k, xv, xsrc, xdst); if constexpr (CV(I) % 3 == 0) asm volatile("s_nop 0"); });
            SCHED0();
            sfor<6>([&](auto KS) { constexpr int ks = CV(KS);
                mfa<false, ks == 0>(sc[0], k0[ks], q[ks]);
                SCHED0();
                if constexpr (ks + 2 < 6) { rq(ks + 2); k0[(ks + 2) % 6] = lr128(l16 + L_K + (ks + 2) * 1024); }
                if constexpr (ks == 4) A.r0<0>();
                if constexpr (ks == 5) A.r0<1>();
                SCHED0();
                mfa<false, ks == 0>(sc[1], k1[ks], q[ks]);
                SCHED0();
                if constexpr (ks + 2 < 6) k1[(ks + 2) % 6] = lr128(l16 + L_K + (8 + ks) * 1024);
                if constexpr (ks == 4) A.r1<0>();
                if constexpr (ks == 5) A.r1<1>();
                SCHED0();
                mfa<false, ks == 0>(sc[2], km[ks], q[ks]);
                SCHED0();
                if constexpr (ks + 2 < 6) rm(ks + 2);
                SCHED0();
            });
            NOP7_3(sc);
        };
        if (w == 0) scores(IC(0)); else scores(IC(1));
        STAMP(s, 2)
        {
            const unsigned a0 = s < 0 ? pdum : po0 + (s & 3) * PR_SZ, a1 = s < 0 ? pdum : po1 + (s & 3) * PR_SZ;
            const unsigned b0 = s < 0 ? pdum : po0 + ((s + 2) & 3) * PR_SZ, b1 = s < 0 ? pdum : po1 + ((s + 2) & 3) * PR_SZ;
            unsigned xc;
            constexpr int H0 = (Soft::NP + SPG - 1) / SPG;
            static_assert(H0 + 10 <= 70 && BPOS < H0, "K pass");
            SCHED0();
            A.run<0, false, 0, 10, 12>(W, [&](auto I) { constexpr int i = CV(I);
                if constexpr (i < H0) sfor<SPG>([&](auto J) { if constexpr (i * SPG + CV(J) < Soft::NP) S.op<i * SPG + CV(J)>(k, sc, a0, a1, b0, b1); });
                if constexpr (i == BPOS) st32(k.rs, vbc, k.p0 + CNT(C_BC, hw), (unsigned)s);
                if constexpr (i >= H0 && i < H0 + 8) S.op<Soft::NP + i - H0>(k, sc, a0, a1, b0, b1);
                if constexpr (i == H0 + 9) xc = ld32(k.rs, 0, k.p0 + CNT(C_XN, (s + 2) & 63)); });
            asm volatile("s_waitcnt vmcnt(8)\n\ts_barrier" ::: "memory");
            STAMP(s, 3)
            A.run<0, false, 10, 20, 20>(W, [&](auto I) { constexpr int i = CV(I);
                if constexpr (i == 56) B.r0<0>();
                if constexpr (i == 57) B.r1<0>();
                if constexpr (i == 63) B.r0<1>();
                if constexpr (i == 64) B.r1<1>(); });
            B.run<30, true, 0, 12, 20>(W, [&](auto I) { constexpr int i = CV(I); A.out<i - 1, 0>(d0, d1); });
            bar_lgkm<6>();
            xn_ok = s < 62 && (unsigned)__builtin_amdgcn_readfirstlane(xc) + 1u >= 8u;
            __builtin_amdgcn_fence(__ATOMIC_ACQUIRE, "wavefront");
            const unsigned xv2 = xn_ok ? l16 : OOB, xsrc2 = k.seg(PG_XNA, (s + 2) & 63) + w * 2048, ga = k.memf((s + 1) & 63) + w * 2048;
            SCHED0();
            B.run<30, true, 12, 18, 20>(W, [&](auto I) { constexpr int i = CV(I);
                if constexpr (i < 18 && !(i & 1)) xn_op<(i / 2) / 3, (i / 2) % 3>(k, xv2, xsrc2, xdst);
                if constexpr (i >= GPOS && i < GPOS + 6 && !((i - GPOS) & 1)) G.op<(i - GPOS) / 2>(k, l16, ga); });
            STAMP(s, 4)
            GATHER_FINISH(G, ga)
            STAMP(s, 5)
        }
    }
    PROF_OUT
}

#define OIDX(t, b) (22 + (t) * 3 + (b))
DEVI void vo_role(const Params& p, const Ctx& k, char* smem, int h) {
    PROF_DECL
    const int w = k.w;
    if (k.c) sleep_ticks(WDELAY);
    bf16x16 W[52];
    {
        const int l = k.l;
        bf16x8 lo[10], wt[16];
        const u16* wv = p.Wv + h * 192 + w * 48;
        const u16* wo = p.Wo + (size_t)h * 192 * 640 + w * 160;
        wstream<22, 6, 6>(smem + w * 36864,
            [&](auto U, unsigned sl) { constexpr int u = CV(U);
                if constexpr (u >= 12) { wp_issue<3>(wv + (size_t)(u - 12) * 64 * 6144, 6144, sl, l); wp_issue<3>(wv + (size_t)((u - 12) * 64 + 32) * 6144, 6144, sl + 3072, l); }
                else { wp_issue<5>(wo + (u >> 1) * 32 * 640 + (u & 1) * 80, 640, sl, l); dma1(wo, l * 16, sl + 5120); } },
            [&](auto U, char* sp) { constexpr int u = CV(U);
                if constexpr (u >= 12) {
                    constexpr int b = u - 12;
#pragma unroll
                    for (int t = 0; t < 3; ++t) {
                        bf16x8 x = wp_read<3, true>(sp, t, l), y = wp_read<3, true>(sp + 3072, t, l);
                        if (t == 2 && b >= 2) { wt[(2 * (b - 2)) & 15] = x; wt[(2 * (b - 2) + 1) & 15] = y; } else W[(t * 10 + b) % 52] = CAT8(x, y);
                    }
                } else {
                    constexpr int ks = u >> 1, b = ks >> 1;
#pragma unroll
                    for (int i = 0; i < 5; ++i) {
                        const int t = (u & 1) * 5 + i;
                        bf16x8 f = wp_read<5, false>(sp, i, l);
                        if (ks & 1) W[OIDX(t, b)] = CAT8(lo[t], f); else lo[t] = f;
                    }
                } });
        RAWBAR()
#pragma unroll
        for (int i = 0; i < 16; ++i) *(bf16x8*)(smem + L_W + w * 16384 + i * 1024 + l * 16) = wt[i];
    }
    unsigned l16, lwv, ix, dq0, dq1, trp, vo, d0, d1, vsig;
    {
        const int l = k.l, g = l >> 4, n = l & 15, q = (l >> 2) & 3, pp = l & 3;
        l16 = l * 16; lwv = l16 + L_W + w * 16384; ix = (l & 8) ? 0xEEEEu : 0x4444u;
        dq0 = L_QM + (48 * w + 16 * (g >> 1) + n) * 16 + (g & 1) * 8;
        dq1 = L_QM + (48 * w + 32 + n) * 16 + (g & 1) * 8 + (g >> 1) * 4;
        trp = L_QM + (32 * (g >> 1) + 8 * (g & 1) + 16 * (q >> 1) + 2 * (pp >> 1) + (q & 1)) * 16 + (pp & 1) * 8;
        vo = h * 256 + n * 16 + (g & 1) * 8 + (g >> 1) * 16384;
        d0 = L_K + l16 + ((3 * w) >> 1) * 1024 + ((3 * w) & 1) * 8; d1 = L_K + l16 + ((3 * w + 1) >> 1) * 1024 + ((3 * w + 1) & 1) * 8;
        vsig = (w == 3 && l == 0) ? 0u : OOB;
        PIN(l16); PIN(lwv); PIN(ix); PIN(dq0); PIN(dq1); PIN(trp); PIN(vo); PIN(d0); PIN(d1); PIN(vsig);
    }
    const unsigned pdum = k.pg(1) + DUMMY_B, xdst = L_XN + w * 2048;
    Gather G;
    G3 V;
    V.l16 = l16; V.lwv = lwv;
    bool xn_ok = false;
    for (int s = -2; s < 64; ++s) {
        if (!xn_ok && s + 2 < 64) { PCOUNT(1) cnt_wait(k, k.cnt(C_XN, s + 2), 8u); xn_all(k, l16, k.seg(PG_XNA, s + 2) + w * 2048, xdst); DRAIN(); }
        RAWBAR()
        if (s >= 0) { STOP(s) } STAMP(s, 0)
        Fold F;
        unsigned xc;
        {
            bf16x8 m[10], wa[10], wb[10];
            m[0] = lr128(l16 + L_MEM); m[1] = lr128(l16 + L_MEM + 1024); m[2] = lr128(l16 + L_MEM + 2048); wa[2] = lr128(lwv); wb[2] = lr128(lwv + 1024);
            f32x4 acc[3];
#pragma unroll
            for (int t = 0; t < 3; ++t) acc[t] = (f32x4){0.f, 0.f, 0.f, 0.f};
            asm volatile("s_nop 1" : "+v"(acc[0]), "+v"(acc[1]), "+v"(acc[2]));
            SCHED0();
            sfor<30>([&](auto I) {
                constexpr int i = CV(I), b = i / 3, t = i % 3;
                if constexpr (t == 2 && b >= 2) smf<false>(acc[2], m[b], CAT8(wa[b], wb[b]), ix); else smf<true>(acc[t], m[b], W[t * 10 + b], ix);
                SCHED0();
                if constexpr (b + 3 < 10) {
                    if constexpr (t == 0) m[(b + 3) % 10] = lr128(l16 + L_MEM + (b + 3) * 1024);
                    if constexpr (t == 1) wa[(b + 3) % 10] = lr128(lwv + (b + 1) * 2048);
                    if constexpr (t == 2) wb[(b + 3) % 10] = lr128(lwv + (b + 1) * 2048 + 1024);
                }
                if constexpr (i == 21) V.r0<0>();
                if constexpr (i == 22) V.r1<0>();
                if constexpr (i == 24) V.r0<1>();
                if constexpr (i == 25) V.r1<1>();
                SCHED0();
            });
            float e0, e1, e2, e3;
            V.run<0, true, 0, 4, 6>(W, [&](auto I) { constexpr int i = CV(I);
                if constexpr (i >= 5 && i < 15) F.op<i - 5, false>(acc[0], acc[1]);
                if constexpr (i == 15) lw64(dq0, F.o);
                if constexpr (i == 16) { e0 = acc[2][0]; e2 = acc[2][2]; SWAP32(e0, e2) }
                if constexpr (i == 17) { e1 = acc[2][1]; e3 = acc[2][3]; SWAP32(e1, e3) }
                if constexpr (i == 18) { e0 += e2; PIN(e0); }
                if constexpr (i == 19) { e1 += e3; PIN(e1); }
                if constexpr (i == 20) { F.o[0] = cvtpk(e0, e1); PIN(F.o[0]); }
                if constexpr (i == 21) lw32(dq1, F.o[0]); });
        }
        asm volatile("s_waitcnt vmcnt(0) lgkmcnt(2)\n\ts_barrier" ::: "memory");
        STAMP(s, 1)
        {
            bf16x4 al[3], ah[3];
            f32x4 acc[10];
            unsigned yb;
            auto clr = [&](auto T, auto I) { acc[CV(T) + (CV(I) >> 2)][CV(I) & 3] = vzero(); };
            auto fin = [&](auto TP, auto J) { constexpr int tp = CV(TP), j = CV(J);
                if constexpr (j < 12) F.op<j % 12, true>(acc[2 * tp], acc[2 * tp + 1]); else if constexpr (j == 12) st64(k.rs, vo, yb + tp * 32768, F.o); };
            al[0] = lrtr(trp); ah[0] = lrtr(trp + 64);
            SCHED0();
            V.run<0, true, 4, 6, 7>(W, [&](auto I) { constexpr int i = CV(I);
                if constexpr (i == 2) al[1] = lrtr(trp + 1024);
                if constexpr (i == 4) ah[1] = lrtr(trp + 1088);
                if constexpr (i == 5) al[2] = lrtr(trp + 2048);
                if constexpr (i == 6) ah[2] = lrtr(trp + 2112);
                if constexpr (i == 0 || i == 1 || i == 3) clr(IC(0), IC(i == 3 ? 2 : i));
                if constexpr (i >= 7 && i < 12) clr(IC(0), IC(i - 4));
                if constexpr (i == 7) atom1(k.rs, vsig, (s >= -1 && s < 63) ? k.p0 + CNT(C_Y, s + 1) : pdum);
                if constexpr (i == 9) { yb = (s < 0 ? pdum : k.pg(s & 1) + YM_B) + w * 163840; PINS(yb); } });
            sfor<5>([&](auto TP) {
                constexpr int tp = CV(TP), ks = 6 + tp;
                sfor<6>([&](auto J) { constexpr int j = CV(J), b = j >> 1, t = 2 * tp + (j & 1), id = OIDX(t, b);
                    smf<(id < 32)>(acc[t], CAT4(al[b], ah[b]), W[id], ix);
                    SCHED0();
                    if constexpr (tp > 0) { fin(IC((tp + 4) % 5), IC(1 + 2 * j)); fin(IC((tp + 4) % 5), IC(2 + 2 * j)); }
                    SCHED0(); });
                sfor<6>([&](auto J) { constexpr int j = CV(J);
                    V.mm<0, true, ks, j>(W);
                    if constexpr (j == 0) V.r0<ks + 1>();
                    if constexpr (j == 1) V.r1<ks + 1>();
                    if constexpr (j == 2) V.rw<ks + 1>();
                    if constexpr (tp < 4) {
                        if constexpr (j < 3) clr(IC((2 * tp + 2) % 10), J);
                        else if constexpr (j < 5) { clr(IC((2 * tp + 2) % 10), IC(2 * j - 3)); clr(IC((2 * tp + 2) % 10), IC(2 * j - 2)); }
                        else clr(IC((2 * tp + 2) % 10), IC(7));
                    } else {
                        if constexpr (j == 3) V.r0<ks + 2>();
                        if constexpr (j == 4) V.r1<ks + 2>();
                        if constexpr (j == 5) V.rw<ks + 2>();
                    }
                    if constexpr (j == 5) fin(TP, IC(0));
                    SCHED0(); });
            });
            STAMP(s, 2)
            V.run<0, true, 11, 20, 20>(W, [&](auto I) { constexpr int i = CV(I);
                if constexpr (i < 12) fin(IC(4), IC(1 + i));
                if constexpr (i == 14) xc = ld32(k.rs, 0, k.p0 + CNT(C_XN, (s + 3) & 63)); });
            NOP7_3(V.a0); NOP7_3(V.a1);
            sfor<18>([&](auto I) { V.out<CV(I), 0>(d0, d1); });
        }
        RAWBAR()
        STAMP(s, 3)
        {
            bf16x8 v[12];
#pragma unroll
            for (int i = 0; i < 6; ++i) { v[i] = lr128(l16 + L_K + i * 1024); v[6 + i] = lr128(l16 + L_K + (6 + i) * 1024); }
            xn_ok = s + 3 < 64 && (unsigned)__builtin_amdgcn_readfirstlane(xc) + 1u >= 8u;
            __builtin_amdgcn_fence(__ATOMIC_ACQUIRE, "wavefront");
            const unsigned xv = xn_ok ? l16 : OOB, xsrc = k.seg(PG_XNA, (s + 3) & 63) + w * 2048, ga = k.memf(s < 0 ? 0 : (s + 1) & 63) + w * 2048;
            f32x4 acc[10][2];
            u32x4 o;
            const unsigned yb = (s + 2 < 64 ? k.yx(s + 2, w) : pdum) + EXP(h * 1024);
            SCHED0();
            sfor<120>([&](auto I) {
                constexpr int i = CV(I), t = i / 12, j = i % 12, ks = j >> 1, rt = j & 1, b = ks >> 1, id = OIDX(t, b);
                if constexpr (ks & 1) mfb<(id < 32), false>(acc[t][rt], v[rt * 6 + ks], HI8(W[id]));
                else mfb<(id < 32), ks == 0>(acc[t][rt], v[rt * 6 + ks], LO8(W[id]));
                SCHED0();
                if constexpr (t > 0 && j >= 6 && j < 10) { constexpr int c = j - 6; o[c] = cvtpk(acc[t - 1][c >> 1][2 * (c & 1)], acc[t - 1][c >> 1][2 * (c & 1) + 1]); PIN(o[c]); }
                if constexpr (t > 0 && j == 10) __builtin_amdgcn_raw_buffer_store_b128(o, k.rs, (int)l16, (int)(yb + (t - 1) * 65536), AUX_SC1);
                if constexpr (t > 1 && j == 0) asm volatile("" :: "v"(o));
                if constexpr (t < 5 && (j == 1 || j == 3 || j == 5)) xn_op<t % 5, j / 2>(k, xv, xsrc, xdst);
                if constexpr (t == GPOSY && (j == 1 || j == 3 || j == 5)) G.op<j / 2>(k, l16, ga);
                SCHED0();
            });
            asm volatile("s_nop 7" : "+v"(acc[9][0]), "+v"(acc[9][1]) : "v"(o));
            o[0] = cvtpk(acc[9][0][0], acc[9][0][1]); o[1] = cvtpk(acc[9][0][2], acc[9][0][3]); o[2] = cvtpk(acc[9][1][0], acc[9][1][1]); o[3] = cvtpk(acc[9][1][2], acc[9][1][3]);
            st128(k.rs, l16, yb + 9 * 65536, o);
            STAMP(s, 4)
            GATHER_FINISH(G, ga)
            STAMP(s, 5)
        }
    }
    PROF_OUT
}

DEVI void ln_store(const Ctx& k, const float (&v)[40], unsigned dst, int row  ) {
    f32x2 s2[4] = {{0.f, 0.f}, {0.f, 0.f}, {0.f, 0.f}, {0.f, 0.f}};
#pragma unroll
    for (int i = 0; i < 40; i += 2) s2[(i >> 1) & 3] += (f32x2){v[i], v[i + 1]};
    const f32x2 st = (s2[0] + s2[1]) + (s2[2] + s2[3]);
    float m = row16sum(st[0] + st[1]) * (1.f / 640.f);
    f32x2 q2[4] = {{0.f, 0.f}, {0.f, 0.f}, {0.f, 0.f}, {0.f, 0.f}};
#pragma unroll
    for (int i = 0; i < 40; i += 2) { const f32x2 d = (f32x2){v[i], v[i + 1]} - (f32x2){m, m}; q2[(i >> 1) & 3] += d * d; }
    const f32x2 qt = (q2[0] + q2[1]) + (q2[2] + q2[3]);
    const float q = row16sum(qt[0] + qt[1]);
    const float var = bfr(q * (1.f / 640.f));
    m = bfr(m);
    const float rden = __builtin_amdgcn_rcpf(bfr(sqrtf(bfr(var + 1e-5f))));
    const int n = k.l & 15;
    const unsigned vo = (n >> 2) * 2048 + (row >> 4) * 1024 + ((n & 3) * 16 + (row & 15)) * 16;
#pragma unroll
    for (int c = 0; c < 5; ++c) {
        u32x4 o;
#pragma unroll
        for (int i = 0; i < 4; ++i) { unsigned d = cvtpk(v[c * 8 + 2 * i] - m, v[c * 8 + 2 * i + 1] - m); o[i] = cvtpk(blo(d) * rden, bhi(d) * rden); }
        st128(k.rs, vo, dst + c * 16384, o);
    }
}
DEVI void unpack8(float* v, u32x4 a) {
#pragma unroll
    for (int i = 0; i < 4; ++i) { v[2 * i] = blo(a[i]); v[2 * i + 1] = bhi(a[i]); }
}
DEVI void ln_x_batch(const Params& p, const Ctx& k, int bw) {
    const int row = bw * 4 + (k.l >> 4), sg = bw >> 3;
    float v[40];
#pragma unroll
    for (int c = 0; c < 5; ++c) unpack8(v + c * 8, *(const u32x4*)(p.x + (size_t)row * 640 + (c * 16 + (k.l & 15)) * 8));
    ln_store(k, v, k.seg0(PG_XNA, sg), row & 31);
    DRAIN();
    if (k.l == 0) cnt_add(k.cnt0(C_XN, sg));
}
DEVI void row_role(const Params& p, const Ctx& k, char* smem, int id) {
    PROF_DECL
    const int row = (id * 4 + k.w) * 4 + (k.l >> 4), n = k.l & 15;
    const unsigned vo = (n >> 2) * 2048 + (row >> 4) * 1024 + ((n & 3) * 16 + (row & 15)) * 16;
    for (int sg = k.c; sg < 64; sg += 2) {
        cnt_wait(k, k.cnt0(C_X2, sg), 40u);
        STOP(sg) STOP(sg ^ 1)
        const unsigned src = k.seg0(PG_X2, sg);
        float v[40];
        u32x4 a[5];
#pragma unroll
        for (int c = 0; c < 5; ++c) a[c] = ld128(k.rs, vo, src + c * 16384);
#pragma unroll
        for (int c = 0; c < 5; ++c) unpack8(v + c * 8, a[c]);
        ln_store(k, v, k.seg0(PG_XNA, sg) ^ 8192u, row);
        DRAIN();
        if (k.l == 0) cnt_add(k.ws + ((k.pg0(0) ^ 8192u) + CNT(C_XN, sg)));
    }
    PROF_OUT
}

DEVI void red_role(const Params& p, const Ctx& k, char* smem, int T) {
    PROF_DECL
    const int l = k.l, w = k.w, g = l >> 4, n = l & 15;
    const unsigned ix = (l & 8) ? 0xEEEEu : 0x4444u;
    const u32x4 ones = {0xFFFFFFFFu, 0xFFFFFFFFu, 0xFFFFFFFFu, 0xFFFFFFFFu};
    u32x4 Y[8], Yn[8];
    auto yload = [&](u32x4 (&y)[8], int sg) {
        const unsigned a = k.yx(sg, T / 10) + (T % 10) * 65536 + w * 16384;
#pragma unroll
        for (int i = 0; i < 8; ++i) y[i] = ld128(k.rs, NOYLD ? OOB : l * 16 + (i & 3) * 1024, a + (i >> 2) * 4096);
    };
    const unsigned v8 = (g >> 1) * 512 + n * 16 + (g & 1) * 8;
    const unsigned xo = EXP((T >> 1) * 2048) + (T & 1) * 512, xv = ((g >> 1) * 16 + n) * 16 + (g & 1) * 8;
    cnt_wait(k, k.cnt(C_Y, 0), 32u);
    yload(Y, 0);
    for (int s = 0; s < 64; ++s) {
        STOP(s) STAMP(s, 0)
        const unsigned ap = k.p0 + PR_B + (s & 3) * PR_SZ + w * 49152, ap1 = ap + 16384, ap2 = ap + 32768;
        const unsigned am = k.pg(s & 1) + YM_B + T * 16384 + w * 2048;
        unsigned yc;
        u32x2 ym[4];
        u32x4 pm[5];
        {
            unsigned spins = 0;
_Pragma("unroll 1")
            for (;; ++spins) {
                const unsigned bc = ld32(k.rs, 0, k.p0 + CNT(C_BC, w));
                DRAIN();
                if (spins == 0 && w == 1) st32(k.rs, l == 0 ? 0u : OOB, k.p0 + CNT(C_X1, T), (unsigned)(s - 1));
                if ((int)__builtin_amdgcn_readfirstlane(bc) >= s) break;
                if (spin_fail(k, spins)) break;
            }
            if (RDLY) __builtin_amdgcn_s_sleep(RDLY);
            STAMP(s, 1)
_Pragma("unroll 1")
            for (;; ++spins) {
#pragma unroll
                for (int i = 0; i < 4; ++i) { ym[i] = ld64(k.rs, v8 + (i >> 1) * 1024 + (i & 1) * 256, am); }
#pragma unroll
                for (int i = 0; i < 4; ++i) { pm[i] = ld128(k.rs, l * 16 + i * 1024, ap); }
                pm[4] = ld128(k.rs, l * 16, ap + 4096);
                yc = ld32(k.rs, 0, k.p0 + CNT(C_Y, (s + 1) & 63));
                unsigned mx = umax4(pm[4]);
#pragma unroll
                for (int i = 0; i < 4; ++i) mx = max(umax3(mx, umax4(pm[i]), ym[i][0]), ym[i][1]);
                if (__all(mx != 0xFFFFFFFFu)) break;
                PCOUNT(0)
                asm volatile("" ::: "memory");
                if (spin_fail(k, spins)) break;
            }
        }
        STAMP(s, 2)
        {
            f32x4 acc = {0.f, 0.f, 0.f, 0.f};
            const u32x8 yy = {ym[0][0], ym[1][0], ym[0][1], ym[1][1], ym[2][0], ym[3][0], ym[2][1], ym[3][1]};
            asm volatile("s_nop 1" : "+v"(acc) : "v"(yy), "v"(ix));
#pragma unroll
            for (int i = 0; i < 4; ++i)
                smf<false>(acc, __builtin_bit_cast(bf16x8, pm[i]), __builtin_bit_cast(bf16x16, __builtin_shufflevector(Y[2 * i], Y[2 * i + 1], 0, 1, 2, 3, 4, 5, 6, 7)), ix);
            smf<false>(acc, __builtin_bit_cast(bf16x8, pm[4]), __builtin_bit_cast(bf16x16, yy), ix);
            asm volatile("s_nop 7" : "+v"(acc));
            const float a = fold32(acc[0], acc[2]), b = fold32(acc[1], acc[3]);
            float* rp = (float*)(smem + w * 512 + (4 * (g & 1) + 2 * (g >> 1)) * 64 + n * 4);
            rp[0] = a; rp[16] = b;
        }
        RAWBAR()
        if (w == 0 && s < 63) {
            const char* rp = smem + ((l >> 2) & 7) * 64 + ((l & 3) * 4 + (l >> 5) * 2) * 4;
            const f32x2 a = *(const f32x2*)rp, b = *(const f32x2*)(rp + 512), c = *(const f32x2*)(rp + 1024), d = *(const f32x2*)(rp + 1536);
            const f32x2 r = (a + b) + (c + d);
            st32(k.rs, l * 4, k.memf(s + 1) + EXP(T * 256), packs(r[0], r[1]));
        }
        STAMP(s, 3)
        u32x4 p1[8]; u32x2 p0[8], pq[8];
        auto pload = [&]() {
#pragma unroll
            for (int i = 0; i < 8; ++i) {
                if (NOTOK) { p1[i] = pm[0]; p0[i] = ym[0]; pq[i] = ym[1]; continue; }
                p1[i] = ld128(k.rs, l * 16 + (i & 3) * 1024, ap1 + (i >> 2) * 4096);
                p0[i] = ld64(k.rs, l * 8 + i * 512, ap2);
                pq[i] = ld64(k.rs, v8 + ((i >> 1) & 1) * 1024 + (i & 1) * 256 + (i >> 2) * 2048, ap2 + 4096);
            }
        };
        auto pbad = [&]() { unsigned mx = 0;
#pragma unroll
            for (int i = 0; i < 8; ++i) mx = max(umax3(umax3(mx, umax4(p1[i]), p0[i][0]), p0[i][1], pq[i][0]), pq[i][1]);
            return !__all(mx != 0xFFFFFFFFu); };
        u32x2 res[2];
#pragma unroll
        for (int rt = 0; rt < 2; ++rt) {
            if (k.c) res[rt] = ld64(k.rs, xv + rt * 1024, k.seg0(PG_X2, s) + xo);
            else res[rt] = *(const u32x2*)(p.x + (size_t)(s * 32 + rt * 16 + n) * 640 + T * 16 + g * 4);
        }
        pload();
        const bool ynow = s < 63 && (unsigned)__builtin_amdgcn_readfirstlane(yc) + 1u >= 32u;
        __builtin_amdgcn_fence(__ATOMIC_ACQUIRE, "wavefront");
        yload(Yn, (s + 1) & 63);
        st128(k.rs, l * 16, am, ones); st128(k.rs, l * 16 + 1024, am, ones);
        if (pbad()) {
            unsigned spins = 0;
_Pragma("unroll 1")
            do {
                PCOUNT(1)
                asm volatile("" ::: "memory");
                __builtin_amdgcn_s_sleep(2);
                if (spin_fail(k, spins++)) break;
                pload();
            } while (pbad());
        }
        {
            f32x4 a0 = {0.f, 0.f, 0.f, 0.f}, a1 = a0;
#pragma unroll
            for (int i = 0; i < 8; ++i) {
                a0 = mf(__builtin_bit_cast(bf16x8, Y[i]), __builtin_bit_cast(bf16x8, (u32x4){p0[i][0], p0[i][1], 0u, 0u}), a0);
                a1 = mf(__builtin_bit_cast(bf16x8, Y[i]), __builtin_bit_cast(bf16x8, p1[i]), a1);
            }
#pragma unroll
            for (int f = 0; f < 2; ++f) {
                const u32x4 yf = {ym[2 * f][0], ym[2 * f][1], ym[2 * f + 1][0], ym[2 * f + 1][1]};
                a0 = mf(__builtin_bit_cast(bf16x8, yf), __builtin_bit_cast(bf16x8, (u32x4){pq[2 * f][0], pq[2 * f][1], pq[2 * f + 1][0], pq[2 * f + 1][1]}), a0);
                a1 = mf(__builtin_bit_cast(bf16x8, yf), __builtin_bit_cast(bf16x8, (u32x4){pq[4 + 2 * f][0], pq[4 + 2 * f][1], pq[5 + 2 * f][0], pq[5 + 2 * f][1]}), a1);
            }
            *(f32x4*)(smem + 4096 + w * 2048 + l * 16) = a0; *(f32x4*)(smem + 4096 + w * 2048 + 1024 + l * 16) = a1;
        }
        RAWBAR()
        if (w == 1) {
#pragma unroll
            for (int rt = 0; rt < 2; ++rt) {
                const char* rp = smem + 4096 + rt * 1024 + l * 16;
                const f32x4 r = (*(const f32x4*)rp + *(const f32x4*)(rp + 2048)) + (*(const f32x4*)(rp + 4096) + *(const f32x4*)(rp + 6144));
                const unsigned d0 = cvtpk(r[0], r[1]), d1 = cvtpk(r[2], r[3]);
                u32x2 o; o[0] = cvtpk(blo(res[rt][0]) + blo(d0), bhi(res[rt][0]) + bhi(d0)); o[1] = cvtpk(blo(res[rt][1]) + blo(d1), bhi(res[rt][1]) + bhi(d1));
                st64(k.rs, xv + rt * 1024, k.seg(PG_X1, s) + xo, o);
            }
        }
        STAMP(s, 4)
        if (s < 63) {
            if (!ynow) { PCOUNT(2) cnt_wait(k, k.cnt(C_Y, s + 1), 32u); yload(Yn, s + 1); }
#pragma unroll
            for (int i = 0; i < 8; ++i) Y[i] = Yn[i];
        }
        STAMP(s, 5)
    }
    DRAIN();
    if (w == 1) st32(k.rs, l == 0 ? 0u : OOB, k.p0 + CNT(C_X1, T), 63u);
    PROF_OUT
}

DEVI void up_role(const Params& p, const Ctx& k, char* smem, int id) {
    PROF_DECL
    const int l = k.l, w = k.w, g = l >> 4, n = l & 15;
    sleep_ticks(WDELAY);
    bf16x8 Wt[80];
    const int tile0 = id * 8 + w * 2;
    wstream<20, 4, 8>(smem + w * 32768,
        [&](auto U, unsigned sl) { constexpr int u = CV(U);
            wp_issue<2>(p.wg + (size_t)u * 32 * 1536 + tile0 * 16, 1536, sl, l); wp_issue<2>(p.wu + (size_t)u * 32 * 1536 + tile0 * 16, 1536, sl + 2048, l); },
        [&](auto U, char* sp) { constexpr int u = CV(U);
            Wt[u] = wp_read<2, true>(sp, 0, l); Wt[20 + u] = wp_read<2, true>(sp, 1, l); Wt[40 + u] = wp_read<2, true>(sp + 2048, 0, l); Wt[60 + u] = wp_read<2, true>(sp + 2048, 1, l); });
    RAWBAR()
    bf16x8 one8 = {0x3F80, 0x3F80, 0x3F80, 0x3F80, 0x3F80, 0x3F80, 0x3F80, 0x3F80};
    asm volatile("s_nop 1" : "+v"(one8));
    f32x4 cs[4];
    sfor<80>([&](auto I) { constexpr int f = CV(I); mfa<(f < 64), f % 20 == 0>(cs[f / 20], Wt[f], one8); });
    asm volatile("s_nop 7" : "+v"(cs[0]), "+v"(cs[1]), "+v"(cs[2]), "+v"(cs[3]));
    const unsigned lds0 = (unsigned)(size_t)(LAS char*)smem;
    const unsigned ho = EXP((tile0 >> 1) * 1024), fv = (l < 40 ? l : 0) * 128;
    constexpr int L_ST = 81920;
    bool issued = false;
    for (int s = 0; s < 64; ++s) {
        if (!issued) {
_Pragma("unroll 1")
            for (unsigned spins = 0; !__all((int)ld32(k.rs, fv, k.p0 + CNT(C_X1, 0)) >= s); ++spins) {
                asm volatile("" ::: "memory");
                __builtin_amdgcn_s_sleep(PSLEEP);
                if (spin_fail(k, spins)) break;
            }
            __builtin_amdgcn_fence(__ATOMIC_ACQUIRE, "wavefront");
            xn_all(k, l * 16, k.seg(PG_X1, s) + w * 2048, lds0 + (s & 1) * 40960 + w * 2048);
        }
        STOP(s)
        const int nf = (int)ld32(k.rs, fv, k.p0 + CNT(C_X1, 0));
        DRAIN();
        RAWBAR()
        issued = s < 63 && __all(nf >= s + 1);
        __builtin_amdgcn_fence(__ATOMIC_ACQUIRE, "wavefront");
        const unsigned xv = issued ? l * 16 : OOB, xsrc = k.seg(PG_X1, (s + 1) & 63) + w * 2048, xdst = lds0 + ((s + 1) & 1) * 40960 + w * 2048;
        const char* xb = smem + (s & 1) * 40960 + l * 16;
        f32x4 acc[2][4], st[4];
        bf16x8 x0[20], x1[20], y0[5], y1[5];
#pragma unroll
        for (int i = 0; i < 5; ++i) { y0[i] = lds128(xb + (i * 4 + w) * 2048); y1[i] = lds128(xb + (i * 4 + w) * 2048 + 1024); }
        x0[0] = lds128(xb); x1[0] = lds128(xb + 1024); x0[1] = lds128(xb + 2048); x1[1] = lds128(xb + 3072);
        SCHED0();
        sfor<5>([&](auto I) { constexpr int i = CV(I);
            mfa<false, i == 0>(st[0], one8, y0[i]); mfa<false, i == 0>(st[1], y0[i], y0[i]); mfa<false, i == 0>(st[2], one8, y1[i]); mfa<false, i == 0>(st[3], y1[i], y1[i]); });
        SCHED0();
        float* sp = (float*)(smem + L_ST + (s & 1) * 1024);
        sfor<160>([&](auto I) {
            constexpr int i = CV(I), ks = i / 8, rt = (i >> 2) & 1, nt = i & 3, f = nt * 20 + ks;
            mfa<(f < 64), ks == 0>(acc[rt][nt], Wt[f], rt ? x1[ks] : x0[ks]);
            SCHED0();
            if constexpr (ks + 2 < 20 && (i & 7) == 0) x0[(ks + 2) % 20] = lds128(xb + (ks + 2) * 2048);
            if constexpr (ks + 2 < 20 && (i & 7) == 1) x1[(ks + 2) % 20] = lds128(xb + 1024 + (ks + 2) * 2048);
            if constexpr ((i & 7) == 5 && ks < 15) xn_op<(ks / 3) % 5, ks % 3>(k, xv, xsrc, xdst);
            if constexpr (i == 10 || i == 18) {
                constexpr int rt2 = i == 18;
                const f32x4& q = st[2 * rt2 + 1];
                const float d = (n & 2) ? ((n & 1) ? q[3] : q[2]) : ((n & 1) ? q[1] : q[0]);
                float* dp = sp + (g == (n >> 2) ? n * 4 + w : 1024 + l);
                dp[rt2 * 128] = st[2 * rt2][0]; dp[rt2 * 128 + 64] = d;
            }
            if constexpr (i == 79) bar_lgkm<4>();
            SCHED0();
        });
        float mean[2], rden[2];
#pragma unroll
        for (int rt = 0; rt < 2; ++rt) {
            const f32x4 a = *(const f32x4*)(sp + rt * 128 + n * 4), b = *(const f32x4*)(sp + rt * 128 + 64 + n * 4);
            const float m = ((a[0] + a[1]) + (a[2] + a[3])) * (1.f / 640.f);
            const float var = __builtin_fmaxf(((b[0] + b[1]) + (b[2] + b[3])) * (1.f / 640.f) - m * m, 0.f);
            mean[rt] = bfr(m);
            rden[rt] = __builtin_amdgcn_rcpf(bfr(sqrtf(bfr(bfr(var) + 1e-5f))));
        }
        asm volatile("s_nop 7" : "+v"(acc[0][0]), "+v"(acc[0][1]), "+v"(acc[0][2]), "+v"(acc[0][3]), "+v"(acc[1][0]), "+v"(acc[1][1]), "+v"(acc[1][2]), "+v"(acc[1][3]));
#pragma unroll
        for (int rt = 0; rt < 2; ++rt) {
            float hv[8];
#pragma unroll
            for (int i = 0; i < 8; ++i) {
                const float gg = bfr(__builtin_fmaf(-mean[rt], cs[i >> 2][i & 3], acc[rt][i >> 2][i & 3]) * rden[rt]);
                const float u = bfr(__builtin_fmaf(-mean[rt], cs[2 + (i >> 2)][i & 3], acc[rt][2 + (i >> 2)][i & 3]) * rden[rt]);
                hv[i] = bfr(__builtin_amdgcn_rcpf(1.f + __expf(-gg))) * u;
            }
            u32x4 o; o[0] = cvtpk(hv[0], hv[1]); o[1] = cvtpk(hv[2], hv[3]); o[2] = cvtpk(hv[4], hv[5]); o[3] = cvtpk(hv[6], hv[7]);
            st128(k.rs, l * 16, k.hseg(s) + rt * EXP(49152) + ho, o);
        }
        DRAIN();
        if (l == 0) cnt_add(k.cnt(C_H, s));
    }
    PROF_OUT
}

DEVI void down_role(const Params& p, const Ctx& k, char* smem, int id) {
    PROF_DECL
    const int l = k.l, w = k.w, g = l >> 4, n = l & 15;
    sleep_ticks(WDELAY);
    bf16x8 Wt[48];
    wstream<12, 4, 8>(smem + w * 32768,
        [&](auto U, unsigned sl) { constexpr int u = decltype(U)::value; wp_issue<4>(p.wd + (size_t)(w * 12 + u) * 32 * 640 + id * 64, 640, sl, l); },
        [&](auto U, char* sp) { constexpr int u = decltype(U)::value;
#pragma unroll
            for (int nt = 0; nt < 4; ++nt) Wt[u * 4 + nt] = wp_read<4, false>(sp, nt, l); });
    RAWBAR()
    const int T = id * 4 + w;
    const unsigned xo = EXP((T >> 1) * 2048) + (T & 1) * 512, xv = ((g >> 1) * 16 + n) * 16 + (g & 1) * 8;
    for (int s = 0; s < 64; ++s) {
        cnt_wait(k, k.cnt(C_H, s), 48u);
        STOP(s)
        u32x4 hx[2][12];
#pragma unroll
        for (int q = 0; q < 3; ++q)
#pragma unroll
            for (int rt = 0; rt < 2; ++rt) {
                const unsigned a = k.hseg(s) + rt * EXP(49152) + EXP((w * 12 + q * 4) * 1024);
#pragma unroll
                for (int i = 0; i < 4; ++i) hx[rt][q * 4 + i] = ld128(k.rs, l * 16 + i * 1024, a);
            }
        u32x2 res[2];
#pragma unroll
        for (int rt = 0; rt < 2; ++rt) res[rt] = ld64(k.rs, xv + rt * 1024, k.seg(PG_X1, s) + xo);
        f32x4 acc[2][4];
#pragma unroll
        for (int rt = 0; rt < 2; ++rt)
#pragma unroll
            for (int nt = 0; nt < 4; ++nt) acc[rt][nt] = (f32x4){0.f, 0.f, 0.f, 0.f};
#pragma unroll
        for (int q = 0; q < 3; ++q)
#pragma unroll
            for (int rt = 0; rt < 2; ++rt)
#pragma unroll
                for (int i = 0; i < 4; ++i)
#pragma unroll
                    for (int nt = 0; nt < 4; ++nt) acc[rt][nt] = mf(Wt[(q * 4 + i) * 4 + nt], __builtin_bit_cast(bf16x8, hx[rt][q * 4 + i]), acc[rt][nt]);
        char* rb = smem + (s & 1) * 32768;
#pragma unroll
        for (int rt = 0; rt < 2; ++rt)
#pragma unroll
            for (int nt = 0; nt < 4; ++nt) *(f32x4*)(rb + ((nt * 2 + rt) * 4 + w) * 1024 + l * 16) = acc[rt][nt];
        RAWBAR()
#pragma unroll
        for (int rt = 0; rt < 2; ++rt) {
            const char* rp = rb + (w * 2 + rt) * 4096 + l * 16;
            const f32x4 r = (*(const f32x4*)rp + *(const f32x4*)(rp + 1024)) + (*(const f32x4*)(rp + 2048) + *(const f32x4*)(rp + 3072));
            const unsigned d0 = cvtpk(r[0], r[1]), d1 = cvtpk(r[2], r[3]);
            u32x2 o; o[0] = cvtpk(blo(res[rt][0]) + blo(d0), bhi(res[rt][0]) + bhi(d0)); o[1] = cvtpk(blo(res[rt][1]) + blo(d1), bhi(res[rt][1]) + bhi(d1));
            if (k.c == 0) st64(k.rs, xv + rt * 1024, k.seg(PG_X2, s) + xo, o);
            else *(u32x2*)(p.out + (size_t)(s * 32 + rt * 16 + n) * 640 + T * 16 + g * 4) = o;
        }
        if (k.c == 0) { DRAIN(); if (l == 0) cnt_add(k.cnt(C_X2, s)); }
    }
    PROF_OUT
}

DEVI int xcc_id() { return __builtin_amdgcn_s_getreg(20 | (0 << 6) | (3 << 11)) & 7; }

__global__ __launch_bounds__(256) void init_kernel(Params p) {
    const int b = blockIdx.x, tid = threadIdx.x, l = tid & 63;
    const __amdgpu_buffer_rsrc_t rs = __builtin_amdgcn_make_buffer_rsrc((void*)p.ws, 0, 0x7fffffff, 0x00020000);
    const u32x4 ones = {0xFFFFFFFFu, 0xFFFFFFFFu, 0xFFFFFFFFu, 0xFFFFFFFFu};
    if (b < 64 && tid < 64) {
        unsigned bt[2] = {0xFFFFFFFFu, 0xFFFFFFFFu};
        if (b < NPAGES) {
            const unsigned off = ((unsigned)b << 21) + 7 * 16384 + (l & 7) * 16;
#pragma unroll 1
            for (int r = 0; r < CALR; ++r) {
#pragma unroll
                for (int hf = 0; hf < 2; ++hf) {
                    unsigned long long c0, c1;
                    asm volatile("s_memtime %0\n\ts_waitcnt lgkmcnt(0)" : "=s"(c0) :: "memory");
                    st128(rs, off + hf * 8192, 0, ones);
                    asm volatile("s_waitcnt vmcnt(0)\n\ts_memtime %0\n\ts_waitcnt lgkmcnt(0)" : "=s"(c1) :: "memory");
                    bt[hf] = min(bt[hf], (unsigned)(c1 - c0));
                }
            }
        }
        if (l == 0) ((unsigned*)(p.cm + CM_TAB))[b] = (unsigned)(bt[1] < bt[0]) ^ (unsigned)(xcc_id() >> 2);
    }
    constexpr unsigned N0 = PR_B + 2 * PR_SZ, N1 = 40 * 16384, N2 = 64 * 20480;
    for (unsigned i = (b * 256 + tid) * 16; i < N0 + 2 * N1 + N2; i += 256 * 256 * 16) {
        unsigned o;
        u32x4 v = ones;
        if (i < N0) o = i;
        else if (i < N0 + N1) o = YM_B + (i - N0);
        else if (i < N0 + 2 * N1) o = (1u << 21) + YM_B + (i - N0 - N1);
        else {
            const unsigned j = i - N0 - 2 * N1, s = j / 20480, r = j % 20480;
            const unsigned q = r < 16384 ? r : 16384 + ((r - 16384) >> 11) * 8192 + (r & 2047);
            o = (2u << 21) + s * 32768 + q;
            if (s == 0) {
                const int e = (((q >> 14) << 13) | (q & 8191u)) >> 4, T = e >> 4, hf = (e >> 3) & 1, rr = e & 7;
#pragma unroll
                for (int c = 0; c < 4; ++c) v[c] = min(*(const unsigned*)(p.mem0 + rr * 640 + T * 16 + 4 * c + 2 * hf), 0xFFFFFFFEu);
            }
        }
        *(u32x4*)(p.ws + o) = v;
    }
    if (b == 255) for (unsigned o = CM_TICKET + tid * 16; o < CM_BYTES; o += 4096) *(u32x4*)(p.cm + o) = ones;
}

__global__ __launch_bounds__(256, 1) void mega_kernel(Params p) {
    extern __shared__ __attribute__((aligned(16))) char smem[];
    Ctx k;
    k.ws = p.ws; k.cm = p.cm;
    k.rs = __builtin_amdgcn_make_buffer_rsrc((void*)p.ws, 0, 0x7fffffff, 0x00020000);
    k.l = threadIdx.x & 63; k.w = __builtin_amdgcn_readfirstlane(threadIdx.x >> 6);
    k.par = __ballot(((const unsigned*)(p.cm + CM_TAB))[k.l] != 0u);
    if (threadIdx.x == 0) {
        int x = xcc_id(), role = 0;
#pragma unroll 1
        for (int i = 0; i < 8; ++i) {
            const unsigned t = __hip_atomic_fetch_add((GAS unsigned*)(p.cm + CM_TICKET + x * 128), 1u, __ATOMIC_RELAXED, __HIP_MEMORY_SCOPE_AGENT) + 1u;
            if (t < 32u) { role = x * 32 + (int)t; break; }
            x = (x + 1) & 7;
        }
        *(volatile LAS int*)smem = role;
    }
    RAWBAR()
    k.role = __builtin_amdgcn_readfirstlane(*(volatile LAS int*)smem);
    RAWBAR()
    const int x = k.role >> 5, i = k.role & 31, xl = x & 3;
    k.c = x >> 2;
    if (k.c) k.par = ~k.par;
    k.p0 = k.pg(0);
    if (i < 8) { qk_role(p, k, smem, xl * 8 + i); return; }
    if (i < 16) { vo_role(p, k, smem, xl * 8 + i - 8); return; }
    ln_x_batch(p, k, ((i - 16) * 8 + x) * 4 + k.w);
    if (i < 26) { red_role(p, k, smem, xl * 10 + i - 16); return; }
    const int o = xl * 6 + i - 26;
    if (o < 12) up_role(p, k, smem, o);
    else if (o < 22) down_role(p, k, smem, o - 12);
    else row_role(p, k, smem, o - 22);
}

extern "C" void kernel_launch(void* const* d_in, const int* in_sizes, int n_in,
                              void* d_out, int out_size, void* d_ws, size_t ws_size,
                              hipStream_t stream) {
    Params p;
    p.x = (const u16*)d_in[0]; p.mem0 = (const u16*)d_in[1];
    p.Wq = (const u16*)d_in[2]; p.Wk = (const u16*)d_in[3]; p.Wv = (const u16*)d_in[4]; p.Wo = (const u16*)d_in[5];
    p.wg = (const u16*)d_in[8]; p.wu = (const u16*)d_in[9]; p.wd = (const u16*)d_in[10];
    p.out = (u16*)d_out; p.cm = (char*)d_ws;
    p.ws = (char*)(((uintptr_t)d_ws + CM_BYTES + ((1u << 21) - 1)) & ~(uintptr_t)((1u << 21) - 1));
    (void)hipFuncSetAttribute((const void*)mega_kernel, hipFuncAttributeMaxDynamicSharedMemorySize, LDS_BYTES);
    init_kernel<<<256, 256, 0, stream>>>(p);
    mega_kernel<<<256, 256, LDS_BYTES, stream>>>(p);
}
```

```cpp
#include <hip/hip_runtime.h>
#include <hip/hip_bf16.h>
#include <stdint.h>
#include <utility>

typedef unsigned short u16;
typedef __attribute__((ext_vector_type(16))) short bf16x16;
typedef __attribute__((ext_vector_type(8))) short bf16x8;
typedef __attribute__((ext_vector_type(4))) short bf16x4;
typedef __attribute__((ext_vector_type(8))) __bf16 hbf8;
typedef __attribute__((ext_vector_type(16))) __bf16 hbf16;
typedef __attribute__((ext_vector_type(4))) float f32x4;
typedef __attribute__((ext_vector_type(2))) float f32x2;
typedef __attribute__((ext_vector_type(8))) unsigned u32x8;
typedef __attribute__((ext_vector_type(4))) unsigned u32x4;
typedef __attribute__((ext_vector_type(2))) unsigned u32x2;
typedef __attribute__((ext_vector_type(2))) __bf16 bf16x2_t;
#define LAS __attribute__((address_space(3)))
#define GAS __attribute__((address_space(1)))
#define DEVI __device__ __forceinline__

#define PSLEEP 8
#define WDELAY 700
#define CALR 3
#define SPG 3
#define DQ 12
#define DV 12
#define BSLP 6
#define RDLY 30
#define NOYST 0
#define NOTOK 0
#define NOYLD 0
#define GPOSY 6
#define GPOS 75

#define EXP(o) ((((unsigned)(o) >> 13) << 14) | ((unsigned)(o) & 8191u))
enum { C_XN = 0, C_Y = 1, C_X1 = 2, C_XB = 3, C_H = 4, C_X2 = 5, C_BC = 6 };
#define CNT(type, s) ((unsigned)((type) * 16384 + (s) * 128))
constexpr unsigned PR_B = EXP(65536), PR_SZ = EXP(12 * 8192);
constexpr unsigned DUMMY_B = EXP(0);
constexpr int PG_MEMF = 2;
#define YXR 8
constexpr int PG_YX = 3;
constexpr int PG_XNA = PG_YX + 2 * YXR;
constexpr int PG_XNB = PG_XNA + 4, PG_X1 = PG_XNB + 4, PG_X2 = PG_X1 + 4;
constexpr int PG_H = PG_X2 + 4;
constexpr int PG_YM = PG_H + 8;
constexpr int NPAGES = PG_YM + 2;
static_assert(NPAGES <= 64, "map");
constexpr unsigned CM_TAB = 0, CM_TICKET = 1024, CM_TMO = 4096, CM_BYTES = 8192;
constexpr unsigned SPIN_CAP = 1u << 20;
#define AUX_SC1 16
#define LDS_BYTES (160 * 1024)

struct Params {
    const u16* x; const u16* mem0; const u16* Wq; const u16* Wk; const u16* Wv; const u16* Wo;
    const u16* wg; const u16* wu; const u16* wd; u16* out; char* cm; char* ws;
};
struct Ctx {
    char* ws; char* cm; __amdgpu_buffer_rsrc_t rs; unsigned long long par;
    int c, l, w, role;
    unsigned p0;
    DEVI unsigned pg(int page) const { return ((unsigned)page << 21) | (((unsigned)(par >> page) & 1u) << 13); }
    DEVI unsigned pg0(int page) const { return pg(page) ^ ((unsigned)c << 13); }
    DEVI unsigned seg(int page, int s) const { return pg(page + (s >> 4)) + (unsigned)(s & 15) * 131072u; }
    DEVI unsigned seg0(int page, int s) const { return seg(page, s) ^ ((unsigned)c << 13); }
    DEVI unsigned hseg(int s) const { return pg(PG_H + (s >> 3)) + (unsigned)(s & 7) * 262144u; }
    DEVI unsigned yx(int sg, int wq) const { return pg(PG_YX + (sg % YXR) * 2 + (wq >> 1)) + (unsigned)(wq & 1) * 1048576u; }
    DEVI unsigned ym(int s) const { return pg(PG_YM + ((s >> 1) & 1)) + (unsigned)(s & 1) * EXP(327680); }
    DEVI unsigned memf(int s) const { return pg(PG_MEMF) + (unsigned)s * 32768u; }
    DEVI char* cnt(int type, int s) const { return ws + (p0 + CNT(type, s)); }
    DEVI char* cnt0(int type, int s) const { return ws + ((p0 ^ ((unsigned)c << 13)) + CNT(type, s)); }
};

DEVI unsigned cvtpk(float a, float b) { return __builtin_bit_cast(unsigned, __builtin_convertvector((f32x2){a, b}, bf16x2_t)); }
DEVI unsigned packs(float a, float b) { return min(cvtpk(a, b), 0xFFFFFFFEu); }
DEVI float blo(unsigned x) { return __uint_as_float(x << 16); }
DEVI float bhi(unsigned x) { return __uint_as_float(x & 0xFFFF0000u); }
DEVI float bfr(float f) { return blo(cvtpk(f, f)); }
DEVI u32x4 ld128(__amdgpu_buffer_rsrc_t rs, unsigned v, unsigned s) { return __builtin_amdgcn_raw_buffer_load_b128(rs, (int)v, (int)s, AUX_SC1); }
DEVI u32x2 ld64(__amdgpu_buffer_rsrc_t rs, unsigned v, unsigned s) { return __builtin_amdgcn_raw_buffer_load_b64(rs, (int)v, (int)s, AUX_SC1); }
DEVI unsigned ld32(__amdgpu_buffer_rsrc_t rs, unsigned v, unsigned s) { return __builtin_amdgcn_raw_buffer_load_b32(rs, (int)v, (int)s, AUX_SC1); }
DEVI void st128(__amdgpu_buffer_rsrc_t rs, unsigned v, unsigned s, u32x4 x) {
    __builtin_amdgcn_raw_buffer_store_b128(x, rs, (int)v, (int)s, AUX_SC1);
    asm volatile("s_nop 1" :: "v"(x) : "memory");
}
DEVI void st64(__amdgpu_buffer_rsrc_t rs, unsigned v, unsigned s, u32x2 x) { __builtin_amdgcn_raw_buffer_store_b64(x, rs, (int)v, (int)s, AUX_SC1); }
DEVI void st32(__amdgpu_buffer_rsrc_t rs, unsigned v, unsigned s, unsigned x) { __builtin_amdgcn_raw_buffer_store_b32(x, rs, (int)v, (int)s, AUX_SC1); }
#define RAWBAR() { __builtin_amdgcn_s_waitcnt(0xc07f); __builtin_amdgcn_s_barrier(); }
#define DRAIN() asm volatile("s_waitcnt vmcnt(0)" ::: "memory")
#define SCHED0() __builtin_amdgcn_sched_barrier(0)
#define OOB 0x80000000u
DEVI int opq(int x) { asm volatile("" : "+v"(x)); return x; }
#define LANE const int l = opq(k.l), g = l >> 4, n = l & 15; (void)g; (void)n;
DEVI unsigned umax3(unsigned a, unsigned b, unsigned c) { return max(max(a, b), c); }
DEVI unsigned umax4(u32x4 v) { return max(umax3(v[0], v[1], v[2]), v[3]); }
DEVI unsigned rclk() { return (unsigned)__builtin_amdgcn_s_memrealtime(); }
DEVI unsigned long long rt_async() { unsigned long long t; asm volatile("s_memrealtime %0" : "=s"(t)); return t; }
DEVI void sleep_ticks(int d) { unsigned target = rclk() + d; while ((int)(rclk() - target) < 0) __builtin_amdgcn_s_sleep(4); }
DEVI float xor16max(float x) { auto r = __builtin_amdgcn_permlane16_swap(__float_as_uint(x), __float_as_uint(x), false, false); return __builtin_fmaxf(__uint_as_float(r[0]), __uint_as_float(r[1])); }
DEVI float xor32max(float x) { auto r = __builtin_amdgcn_permlane32_swap(__float_as_uint(x), __float_as_uint(x), false, false); return __builtin_fmaxf(__uint_as_float(r[0]), __uint_as_float(r[1])); }
DEVI float xor16add(float x) { auto r = __builtin_amdgcn_permlane16_swap(__float_as_uint(x), __float_as_uint(x), false, false); return __uint_as_float(r[0]) + __uint_as_float(r[1]); }
DEVI float xor32add(float x) { auto r = __builtin_amdgcn_permlane32_swap(__float_as_uint(x), __float_as_uint(x), false, false); return __uint_as_float(r[0]) + __uint_as_float(r[1]); }
DEVI float fold32(float a, float b) { auto r = __builtin_amdgcn_permlane32_swap(__float_as_uint(a), __float_as_uint(b), false, false); return __uint_as_float(r[0]) + __uint_as_float(r[1]); }
#define ROR16(x, n) __builtin_bit_cast(float, __builtin_amdgcn_update_dpp(0, __builtin_bit_cast(int, (x)), 0x120 + (n), 0xf, 0xf, false))
DEVI float row16sum(float x) { x += ROR16(x, 8); x += ROR16(x, 4); x += ROR16(x, 2); x += ROR16(x, 1); return x; }
DEVI unsigned z0() { unsigned z; asm volatile("v_mov_b32 %0, 0" : "=v"(z)); return z; }
DEVI void cnt_add(char* p) { __hip_atomic_fetch_add((GAS unsigned*)(p + z0()), 1u, __ATOMIC_RELAXED, __HIP_MEMORY_SCOPE_AGENT); }
DEVI unsigned cnt_get(char* p) { return __hip_atomic_load((GAS unsigned*)(p + z0()), __ATOMIC_RELAXED, __HIP_MEMORY_SCOPE_AGENT) + 1u; }
DEVI bool spin_fail(const Ctx& k, unsigned spins) {
    if (spins > SPIN_CAP || ((spins & 255u) == 255u && __builtin_amdgcn_readfirstlane(cnt_get(k.cm + CM_TMO)) != 0)) { cnt_add(k.cm + CM_TMO); return true; }
    return false;
}
DEVI void cnt_wait(const Ctx& k, char* c, unsigned need) {
_Pragma("unroll 1")
    for (unsigned spins = 0; (unsigned)__builtin_amdgcn_readfirstlane(cnt_get(c)) < need; ++spins) {
        __builtin_amdgcn_s_sleep(PSLEEP);
        if (spin_fail(k, spins)) break;
    }
    __builtin_amdgcn_fence(__ATOMIC_ACQUIRE, "wavefront");
}
DEVI void dma1(const void* sbase, unsigned voff, unsigned lds) {
    asm volatile("s_mov_b32 m0, %2\n\ts_nop 0\n\tglobal_load_lds_dwordx4 %0, %1" :: "v"(voff), "s"(sbase), "s"(lds) : "memory");
}
DEVI void set_m0(unsigned lds) { asm volatile("s_mov_b32 m0, %0" :: "s"(lds) : "memory"); }
template <int OFF> DEVI void dmab(__amdgpu_buffer_rsrc_t rs, unsigned voff, unsigned soff) {
    asm volatile("buffer_load_dwordx4 %0, %1, %2 offen offset:%3 sc1 lds" :: "v"(voff), "s"(rs), "s"(soff), "n"(OFF) : "memory");
}
#define PIN(x) asm volatile("" : "+v"(x))
#define PINS(x) asm volatile("" : "+s"(x))
DEVI float csel(float a, float b, unsigned long long m) { float d; asm volatile("v_cndmask_b32_e64 %0, %1, %2, %3" : "=v"(d) : "v"(a), "v"(b), "s"(m)); return d; }
#define LO8(x) __builtin_shufflevector(x, x, 0, 1, 2, 3, 4, 5, 6, 7)
#define HI8(x) __builtin_shufflevector(x, x, 8, 9, 10, 11, 12, 13, 14, 15)
#define CAT8(a, b) __builtin_shufflevector(a, b, 0, 1, 2, 3, 4, 5, 6, 7, 8, 9, 10, 11, 12, 13, 14, 15)
template <bool AG> DEVI void smf(f32x4& c, const bf16x8& a, const bf16x16& w, unsigned ix) {
    if constexpr (AG) asm volatile("v_smfmac_f32_16x16x64_bf16 %0, %1, %2, %3" : "+v"(c) : "v"(a), "a"(w), "v"(ix));
    else asm volatile("v_smfmac_f32_16x16x64_bf16 %0, %1, %2, %3" : "+v"(c) : "v"(a), "v"(w), "v"(ix));
}
template <bool AG, bool FIRST> DEVI void mfa(f32x4& c, const bf16x8& w, const bf16x8& x) {
    if constexpr (AG && FIRST) asm volatile("v_mfma_f32_16x16x32_bf16 %0, %1, %2, 0" : "=&v"(c) : "a"(w), "v"(x));
    else if constexpr (AG) asm volatile("v_mfma_f32_16x16x32_bf16 %0, %1, %2, %0" : "+v"(c) : "a"(w), "v"(x));
    else if constexpr (FIRST) asm volatile("v_mfma_f32_16x16x32_bf16 %0, %1, %2, 0" : "=&v"(c) : "v"(w), "v"(x));
    else asm volatile("v_mfma_f32_16x16x32_bf16 %0, %1, %2, %0" : "+v"(c) : "v"(w), "v"(x));
}
template <bool AG, bool FIRST> DEVI void mfb(f32x4& c, const bf16x8& x, const bf16x8& w) {
    if constexpr (AG && FIRST) asm volatile("v_mfma_f32_16x16x32_bf16 %0, %1, %2, 0" : "=&v"(c) : "v"(x), "a"(w));
    else if constexpr (AG) asm volatile("v_mfma_f32_16x16x32_bf16 %0, %1, %2, %0" : "+v"(c) : "v"(x), "a"(w));
    else if constexpr (FIRST) asm volatile("v_mfma_f32_16x16x32_bf16 %0, %1, %2, 0" : "=&v"(c) : "v"(x), "v"(w));
    else asm volatile("v_mfma_f32_16x16x32_bf16 %0, %1, %2, %0" : "+v"(c) : "v"(x), "v"(w));
}
DEVI f32x4 mf(const bf16x8& a, const bf16x8& b, const f32x4& c) { return __builtin_amdgcn_mfma_f32_16x16x32_bf16(__builtin_bit_cast(hbf8, a), __builtin_bit_cast(hbf8, b), c, 0, 0, 0); }
template <int N, class F> DEVI void sfor(F f) { [&]<int... I>(std::integer_sequence<int, I...>) { (f(std::integral_constant<int, I>{}), ...); }(std::make_integer_sequence<int, N>{}); }
DEVI bf16x8 lds128(const char* p) { return *(const bf16x8*)p; }
DEVI bf16x4 ldstr(const char* p) { return __builtin_amdgcn_ds_read_tr16_b64_v4i16((LAS bf16x4*)p); }
DEVI bf16x8 ldstr2(const char* p, int d) { bf16x4 a = ldstr(p), b = ldstr(p + d); return __builtin_shufflevector(a, b, 0, 1, 2, 3, 4, 5, 6, 7); }

#define STAMP(s, k)
#define QSTAMP(f, i)
#define STOP(s)
#define PFLUSH(s)
#define PROF_DECL
#define RSTAMP(i)
#define PCOUNT(i)
#define PROF_OUT

template <int NT> DEVI void wp_issue(const u16* W, int ld, unsigned slot, int l) {
#pragma unroll
    for (int i = 0; i < NT; ++i) {
        const int id = i * 64 + l, row = id / (2 * NT), cc = id % (2 * NT);
        dma1(W, (unsigned)(row * ld + cc * 8) * 2u, slot + i * 1024);
    }
}
template <int NT, bool STD> DEVI bf16x8 wp_read(const char* slot, int T, int l) {
    const int row = (STD ? 8 : 4) * (l >> 4) + ((l >> 2) & 3);
    return ldstr2(slot + row * (NT * 32) + T * 32 + (l & 3) * 8, (STD ? 4 : 16) * NT * 32);
}
template <int NU, int NI, int D, class ISSUE, class READ> DEVI void wstream(char* ring, ISSUE issue, READ read) {
    const unsigned base = (unsigned)(size_t)(LAS char*)ring;
    sfor<D>([&](auto U) { constexpr int u = decltype(U)::value; if constexpr (u < NU) issue(U, base + u * NI * 1024); });
    sfor<NU>([&](auto U) {
        constexpr int u = decltype(U)::value, left = (NU - u < D ? NU - u : D) - 1;
        asm volatile("s_waitcnt vmcnt(%0)" :: "n"(left * NI) : "memory");
        read(U, ring + (u % D) * NI * 1024);
        __builtin_amdgcn_s_waitcnt(0xc07f);
        asm volatile("" ::: "memory");
        if constexpr (u + D < NU) issue(std::integral_constant<int, u + D>{}, base + (u % D) * NI * 1024);
    });
}

constexpr int L_XN = 0;
constexpr int L_K = 40960;
constexpr int L_MEM = L_K + 12288;
constexpr int L_QM = L_MEM + 10240;
constexpr int L_Q = L_QM + 12288;
constexpr int L_W = L_Q + 12288;
#define IC(x) std::integral_constant<int, (x)>{}
#define CV(X) decltype(X)::value
DEVI bf16x8 lr128(unsigned a) { return *(const LAS bf16x8*)(size_t)a; }
DEVI bf16x4 lrtr(unsigned a) { return __builtin_amdgcn_ds_read_tr16_b64_v4i16((LAS bf16x4*)(size_t)a); }
#define CAT4(a, b) __builtin_shufflevector(a, b, 0, 1, 2, 3, 4, 5, 6, 7)
DEVI void lw128(unsigned a, u32x4 v) { *(LAS u32x4*)(size_t)a = v; }
DEVI void lw64(unsigned a, u32x2 v) { *(LAS u32x2*)(size_t)a = v; }
DEVI void lw32(unsigned a, unsigned v) { *(LAS unsigned*)(size_t)a = v; }
template <int N> DEVI void bar_lgkm() { asm volatile("s_waitcnt lgkmcnt(%0)\n\ts_barrier" :: "n"(N) : "memory"); }
DEVI void atom1(__amdgpu_buffer_rsrc_t rs, unsigned voff, unsigned soff) {
    unsigned one; asm volatile("v_mov_b32 %0, 1\n\tbuffer_atomic_add %0, %1, %2, %3 offen" : "=&v"(one) : "v"(voff), "s"(rs), "s"(soff) : "memory");
}
DEVI float vzero() { float z; asm volatile("v_mov_b32 %0, 0" : "=v"(z)); return z; }
template <int B, int I> DEVI void xn_op(const Ctx& k, unsigned v, unsigned src, unsigned dst) {
    if constexpr (I == 0) set_m0(dst + B * 8192);
    else if constexpr (I == 1) dmab<0>(k.rs, v, src + B * 16384);
    else dmab<1024>(k.rs, v, src + B * 16384);
}
DEVI void xn_all(const Ctx& k, unsigned v, unsigned src, unsigned dst) {
    sfor<5>([&](auto B) { xn_op<CV(B), 0>(k, v, src, dst); asm volatile("s_nop 0"); xn_op<CV(B), 1>(k, v, src, dst); xn_op<CV(B), 2>(k, v, src, dst); });
}
DEVI u32x4 ald128(__amdgpu_buffer_rsrc_t rs, unsigned v, unsigned s) { u32x4 d; asm volatile("buffer_load_dwordx4 %0, %1, %2, %3 offen sc1" : "=v"(d) : "v"(v), "s"(rs), "s"(s) : "memory"); return d; }
struct Gather {
    u32x4 v0, v1, v2;
    template <int I> DEVI void op(const Ctx& k, unsigned l16, unsigned a) {
        if constexpr (I == 0) v0 = ald128(k.rs, l16, a);
        if constexpr (I == 1) v1 = ald128(k.rs, l16, a + 1024);
        if constexpr (I == 2) v2 = ald128(k.rs, l16, a + 16384 - (k.w >> 1) * 3072 - (k.w & 1) * 2048);
    }
    template <int N> DEVI bool ok() {
        asm volatile("s_waitcnt vmcnt(%3)" : "+v"(v0), "+v"(v1), "+v"(v2) : "n"(N) : "memory");
        return __all(umax3(umax4(v0), umax4(v1), umax4(v2)) != 0xFFFFFFFFu);
    }
};
#define GATHER_FINISH(G, a, N) { unsigned spins_ = 0; \
    if (!G.ok<N>()) { _Pragma("unroll 1") do { PCOUNT(0) if (spin_fail(k, spins_++)) break; G.op<0>(k, l16, a); G.op<1>(k, l16, a); G.op<2>(k, l16, a); } while (!G.ok<0>()); } \
    lw128(lwv + (L_MEM - L_W) - w * 14336, G.v0); lw128(lwv + (L_MEM - L_W) - w * 14336 + 1024, G.v1); lw128(l16 + L_MEM + 8192 + (w >> 1) * 1024, G.v2); }
#define NOP7_3(a) asm volatile("s_nop 7" : "+v"(a[0]), "+v"(a[1]), "+v"(a[2]))

struct G3 {
    f32x4 a0[3], a1[3];
    bf16x8 x0[20], x1[20], wl[20];
    unsigned l16, lwv;
    template <int KS> DEVI void r0() { x0[KS] = lr128(l16 + L_XN + KS * 2048); }
    template <int KS> DEVI void r1() { x1[KS] = lr128(l16 + L_XN + KS * 2048 + 1024); }
    template <int KS> DEVI void rw() { if constexpr (KS >= 4) wl[KS] = lr128(lwv + (KS - 4) * 1024); }
    template <int ID0, bool WL, int ks, int m> DEVI void mm(const bf16x16 (&W)[52]) {
        constexpr int b = ks >> 1, t = m % 3, id = ID0 + t * 10 + b;
        f32x4& c = m < 3 ? a0[t] : a1[t];
        const bf16x8& x = m < 3 ? x0[ks] : x1[ks];
        if constexpr (ID0 == 30) {
            if constexpr (WL && t == 2 && b >= 2) mfb<false, false>(c, x, wl[ks]);
            else if constexpr (ks & 1) mfb<(id < 32), false>(c, x, HI8(W[id % 52]));
            else mfb<(id < 32), ks == 0>(c, x, LO8(W[id % 52]));
        } else {
            if constexpr (WL && t == 2 && b >= 2) mfa<false, false>(c, wl[ks], x);
            else if constexpr (ks & 1) mfa<(id < 32), false>(c, HI8(W[id % 52]), x);
            else mfa<(id < 32), ks == 0>(c, LO8(W[id % 52]), x);
        }
        SCHED0();
    }
    template <int ID0, bool WL, int K0, int K1, int PF, bool H6 = false, class HOOK> DEVI void run(const bf16x16 (&W)[52], HOOK hook) {
        sfor<K1 - K0>([&](auto KK) {
            constexpr int kk = CV(KK), ks = K0 + kk;
            sfor<6>([&](auto M) {
                constexpr int m = CV(M);
                mm<ID0, WL, ks, m>(W);
                if constexpr (ks + 2 < PF) {
                    if constexpr (m == 0) r0<(ks + 2) % 20>();
                    if constexpr (m == 1) r1<(ks + 2) % 20>();
                    if constexpr (m == 3 && WL) rw<(ks + 2) % 20>();
                }
                if constexpr (H6) hook(IC(kk * 6 + m));
                else { hook(IC(kk * 7 + (m < 5 ? m : 6))); if constexpr (m == 4) hook(IC(kk * 7 + 5)); }
                SCHED0();
            });
        });
    }
    u32x2 o;
    template <int I, int OFF> DEVI void out(unsigned d0, unsigned d1) {
        if constexpr (I >= 0 && I < 18) {
            constexpr int t = I / 6, rt = (I / 3) & 1;
            const f32x4& a = rt ? a1[t] : a0[t];
            if constexpr (I % 3 == 0) { o[0] = cvtpk(a[0], a[1]); PIN(o[0]); }
            else if constexpr (I % 3 == 1) { o[1] = cvtpk(a[2], a[3]); PIN(o[1]); }
            else if constexpr (OFF > 0) lw64(d0 + (OFF - 1) + rt * 6144 + t * 1024, o);
            else lw64((t == 1 ? d1 : d0) + OFF + rt * 6144 + (t == 2 ? 1024 : 0), o);
        }
    }
};
#define SPIX(l) (((l) & 2) ? 0xEEEEu : 0x4444u)
template <int I> DEVI void fold_op(f32x4& a, unsigned& o) {
    if constexpr (I == 0) asm volatile("v_add_f32 %0, %0, %1" : "+v"(a[0]) : "v"(a[2]));
    if constexpr (I == 1) asm volatile("v_add_f32 %0, %0, %1" : "+v"(a[1]) : "v"(a[3]));
    if constexpr (I == 2) asm volatile("v_cvt_pk_bf16_f32 %0, %1, %2" : "=v"(o) : "v"(a[0]), "v"(a[1]));
}

#define ANNOUNCE(s, now, d) (((unsigned)(s) << 16) | (((unsigned)(now) + (d)) & 0xFFFFu))

struct Soft {
    unsigned long long mA, mY, mC[4], mM, mD;
    unsigned v0, v1;
    float a[6], m, ml, M, t, u, sc; unsigned pk[3], msk;
    static constexpr int N = 30;
    template <int I> DEVI void op(const Ctx& k, unsigned a0, unsigned a1, unsigned ph) {
#define A3(o) "+v"(a[o]), "+v"(a[o + 1]), "+v"(a[o + 2])
#define CVT3 "v_cvt_pk_bf16_f32 %0, 0, %0\n\tv_cvt_pk_bf16_f32 %1, 0, %1\n\tv_cvt_pk_bf16_f32 %2, 0, %2"
#define MUL3 "v_mul_f32 %0, 0x3d93cd3a, %0\n\tv_mul_f32 %1, 0x3d93cd3a, %1\n\tv_mul_f32 %2, 0x3d93cd3a, %2"
#define FMA(x) "v_fmamk_f32 " x ", " x ", 0x3fb8aa3b, "
        if constexpr (I == 0 || I == 4) asm volatile(CVT3 : A3(0));
        if constexpr (I == 1) asm volatile(CVT3 : A3(3));
        if constexpr (I == 2) asm volatile(MUL3 : A3(0));
        if constexpr (I == 3) asm volatile(MUL3 : A3(3));
        if constexpr (I == 5) asm volatile(CVT3 "\n\tv_mov_b32 %3, 0xf1800000" : A3(3), "=v"(t));
        if constexpr (I == 6) asm volatile("v_cndmask_b32_e64 %0, %3, %0, %4\n\tv_cndmask_b32_e64 %1, %3, %1, %5\n\tv_cndmask_b32_e64 %2, %3, %2, %6" : A3(0) : "v"(t), "s"(mC[0]), "s"(mC[1]), "s"(mC[2]));
        if constexpr (I == 7) asm volatile("v_cndmask_b32_e64 %0, %3, %0, %4\n\tv_cndmask_b32_e64 %1, %3, %1, %5\n\tv_cndmask_b32_e64 %2, %3, %2, %5" : A3(3) : "v"(t), "s"(mC[3]), "s"(mM));
        if constexpr (I == 8) asm volatile("v_max3_f32 %0, %1, %2, %3\n\tv_max3_f32 %0, %0, %4, %5\n\tv_max_f32 %0, %0, %6" : "=&v"(m) : "v"(a[0]), "v"(a[1]), "v"(a[2]), "v"(a[3]), "v"(a[4]), "v"(a[5]));
        if constexpr (I == 9) asm volatile("v_mul_f32 %0, 0xbfb8aa3b, %3\n\t" FMA("%1") "%0\n\t" FMA("%2") "%0" : "=&v"(ml), "+v"(a[0]), "+v"(a[1]) : "v"(m));
        if constexpr (I == 10) asm volatile("v_max_f32_dpp %0, %3, %3 row_ror:8 row_mask:0xf bank_mask:0xf\n\t" FMA("%1") "%4\n\tv_cndmask_b32_e64 %2, %3, %0, %5" : "=&v"(t), "+v"(a[2]), "=&v"(M) : "v"(m), "v"(ml), "s"(mD));
        if constexpr (I == 11) asm volatile("v_mov_b32 %0, %3\n\tv_exp_f32 %1, %1\n\t" FMA("%2") "%4" : "=&v"(u), "+v"(a[0]), "+v"(a[3]) : "v"(M), "v"(ml));
        if constexpr (I == 12) asm volatile("v_permlane16_swap_b32 %0, %1\n\t" FMA("%2") "%3" : "+v"(M), "+v"(u), "+v"(a[4]) : "v"(ml));
        if constexpr (I == 13) asm volatile("v_max_f32 %0, %0, %1\n\tv_mov_b32 %1, %0\n\tv_exp_f32 %2, %2" : "+v"(M), "+v"(u), "+v"(a[1]));
        if constexpr (I == 14) asm volatile(FMA("%2") "%3\n\tv_permlane32_swap_b32 %0, %1" : "+v"(M), "+v"(u), "+v"(a[5]) : "v"(ml));
        if constexpr (I == 15) asm volatile("v_max_f32 %0, %0, %1\n\tv_exp_f32 %2, %2" : "+v"(M), "+v"(u), "+v"(a[2]));
        if constexpr (I == 16) asm volatile("v_sub_f32 %0, %2, %0\n\tv_mul_f32 %0, 0x3fb8aa3b, %0\n\tv_exp_f32 %1, %1" : "+v"(M), "+v"(a[3]) : "v"(m));
        if constexpr (I == 17) asm volatile("v_exp_f32 %0, %2\n\tv_exp_f32 %1, %1" : "=&v"(sc), "+v"(a[4]) : "v"(M));
        if constexpr (I == 18) asm volatile("v_exp_f32 %2, %2\n\tv_add_f32 %0, %3, %4\n\tv_add_f32 %1, %5, %6" : "=&v"(u), "=&v"(t), "+v"(a[5]) : "v"(a[0]), "v"(a[1]), "v"(a[2]), "v"(a[3]));
        if constexpr (I == 19) asm volatile("v_add_f32 %0, %0, %2\n\tv_add_f32 %1, %1, %3\n\tv_add_f32 %0, %0, %1" : "+v"(u), "+v"(t) : "v"(a[4]), "v"(a[5]));
        if constexpr (I == 20) asm volatile("v_mul_f32 %0, %0, %2\n\tv_mov_b32 %1, 0x7fff7fff" : "+v"(u), "=v"(msk) : "v"(sc));
        if constexpr (I == 21) asm volatile("v_add_f32_dpp %1, %0, %0 row_ror:8 row_mask:0xf bank_mask:0xf\n\tv_cndmask_b32_e64 %0, %0, %1, %2\n\tv_mov_b32 %1, %0" : "+v"(u), "=&v"(t) : "s"(mD));
        if constexpr (I == 22) asm volatile("s_nop 0\n\tv_permlane16_swap_b32 %0, %1\n\tv_add_f32 %0, %0, %1\n\tv_mov_b32 %1, %0" : "+v"(u), "+v"(t));
        if constexpr (I == 23) asm volatile("s_nop 0\n\tv_permlane32_swap_b32 %0, %1\n\tv_add_f32 %0, %0, %1\n\tv_rcp_f32 %0, %0" : "+v"(u), "+v"(t));
        if constexpr (I == 24) asm volatile("v_mul_f32 %0, %0, %3\n\tv_mul_f32 %1, %1, %0\n\tv_mul_f32 %2, %2, %0" : "+v"(u), "+v"(a[0]), "+v"(a[1]) : "v"(sc));
        if constexpr (I == 25) asm volatile("v_mul_f32 %0, %0, %3\n\tv_mul_f32 %1, %1, %3\n\tv_mul_f32 %2, %2, %3" : A3(2) : "v"(u));
        if constexpr (I == 26) asm volatile("v_mul_f32 %2, %2, %3\n\tv_cvt_pk_bf16_f32 %0, %4, %5\n\tv_cvt_pk_bf16_f32 %1, %6, %7" : "=&v"(pk[0]), "=&v"(pk[1]), "+v"(a[5]) : "v"(u), "v"(a[0]), "v"(a[1]), "v"(a[2]), "v"(a[3]));
        if constexpr (I == 27) asm volatile("v_cvt_pk_bf16_f32 %2, %3, %4\n\tv_bfi_b32 %0, %5, %0, %6\n\tv_bfi_b32 %1, %5, %1, %6" : "+v"(pk[0]), "+v"(pk[1]), "=&v"(pk[2]) : "v"(a[4]), "v"(a[5]), "v"(msk), "s"(ph));
        if constexpr (I == 28) { asm volatile("v_bfi_b32 %0, %1, %0, %2" : "+v"(pk[2]) : "v"(msk), "s"(ph)); st64(k.rs, v0, a0, (u32x2){pk[0], pk[1]}); }
        if constexpr (I == 29) st32(k.rs, v1, a1, pk[2]);
    }
};
#define QIDX(t, b) ((t) * 10 + (b))
DEVI void qk_role(const Params& p, const Ctx& k, char* smem, int h) {
    PROF_DECL
    const int w = k.w;
    if (k.c) sleep_ticks(WDELAY);
    bf16x16 W[52];
    {
        const int l = k.l;
        bf16x8 lo[6], wt[16];
        const int col = h * 192 + w * 48;
        wstream<20, 6, 6>(smem + w * 36864,
            [&](auto U, unsigned sl) { constexpr int u = CV(U);
                wp_issue<3>(p.Wk + (size_t)u * 32 * 6144 + col, 6144, sl, l); wp_issue<3>(p.Wq + (size_t)u * 32 * 6144 + col, 6144, sl + 3072, l); },
            [&](auto U, char* sp) { constexpr int u = CV(U), b = u >> 1;
#pragma unroll
                for (int t = 0; t < 6; ++t) {
                    bf16x8 f = wp_read<3, true>(sp + (t / 3) * 3072, t % 3, l);
                    if (t == 5 && b >= 2) wt[(u - 4) & 15] = f;
                    else if (u & 1) W[QIDX(t, b) % 52] = CAT8(lo[t], f);
                    else lo[t] = f;
                } });
        RAWBAR()
#pragma unroll
        for (int i = 0; i < 16; ++i) *(bf16x8*)(smem + L_W + w * 16384 + i * 1024 + l * 16) = wt[i];
    }
    RSTAMP(2)
    Gather G;
    Soft S;
    const int hw = h >> 3, hh = h & 7;
    unsigned l16, lwv, ix, dqm, dq, trp, qp, d0, vz;
    {
        const int l = k.l, g = l >> 4, n = l & 15, r = n & 7, u = w & 1;
        const bool hf = n >= 8, dup = w != 1;
        S.mA = __ballot(dup && hf); S.mD = __ballot(dup);
        const int lim = w == 1 ? n - 4 * g : (w == 0 || !hf) ? 64 : 8 * u + r - 4 * g;
#pragma unroll
        for (int i = 0; i < 4; ++i) S.mC[i] = __ballot(i <= lim);
        const bool okm = !(dup && hf);
        S.mY = __ballot((dup && hf) == (w != 0)); S.mM = __ballot(okm);
        S.v0 = w == 0 ? hh * 512 + ((g >> 1) * 16 + (r >> 1) * 4 + hf * 2 + (r & 1)) * 16 + (g & 1) * 8 : w == 1 ? hh * 512 + l * 8 : hh * 1024 + (g * 16 + 8 * u + r) * 16 + hf * 8;
        S.v1 = !okm ? OOB : w == 0 ? 4096 + hh * 128 + r * 16 + g * 4 : (w == 1 ? 4096 + n * 8 : 6144 + (8 * u + r) * 8) + hh * 256 + (g >> 1) * 128 + (g & 1) * 4;
        l16 = l * 16; lwv = l16 + L_W + w * 16384; ix = SPIX(l);
        dqm = L_QM + (w >> 1) * 3072 + u * 512 + (g >> 1) * 128 + n * 8 + (g & 1) * 4;
        dq = L_QM + 6144 + (w >> 1) * 3072 + u * 512 + g * 128 + ((n * 8) ^ ((g >> 1) * 64));
        trp = L_QM + 6144 + (l & 3) * 128 + (((l >> 2) * 8) ^ ((l & 2) * 32));
        qp = w == 1 ? trp + 6144 : (w == 0 ? L_QM : L_Q + 6144 + u * 256) + (l & 1) * 128 + (((l >> 2) * 8) ^ (w == 3 ? 64 : 0));
        d0 = L_K + l16 + (w >> 1) * 3072 + u * 8;
        vz = l == 0 ? 0u : OOB;
        PIN(S.v0); PIN(S.v1); PIN(l16); PIN(lwv); PIN(ix); PIN(dqm); PIN(dq); PIN(trp); PIN(qp); PIN(d0); PIN(vz);
    }
    const unsigned po0 = k.p0 + PR_B + hw * 49152 + (w == 0 ? 0 : w == 1 ? 32768 : 16384), po1 = k.p0 + PR_B + hw * 49152 + (w == 0 ? 0 : 32768);
    const unsigned pdum = k.pg(1) + DUMMY_B, xdst = L_XN + w * 2048, pbc = w == 0 ? k.p0 + CNT(C_BC, hw * 16 + hh) : pdum + 65536 + h * 512 + w * 128;
    bool xn_ok = false;
    unsigned long long now = 0, done = 0;
    unsigned xsrc = k.seg(PG_XNA, 0) + w * 2048, xv = OOB;
    G3 A, B;
    A.l16 = l16; A.lwv = lwv; B.l16 = l16; B.lwv = lwv;
#pragma unroll
    for (int t = 0; t < 3; ++t) { B.a0[t] = (f32x4){0.f, 0.f, 0.f, 0.f}; B.a1[t] = B.a0[t]; }
    B.x0[18] = lr128(l16); B.x1[18] = B.x0[18]; B.wl[18] = B.x0[18]; B.x0[19] = B.x0[18]; B.x1[19] = B.x0[18]; B.wl[19] = B.x0[18];
    for (int s = -1; s < 64; ++s) {
        asm volatile("s_waitcnt lgkmcnt(0)\n\ts_barrier" : "+s"(done) :: "memory");
        const unsigned dly = min((unsigned)done - (unsigned)now, 200u) + DQ;
        now = rt_async();
        STOP(s) STAMP(s, 0)
        bf16x8 q[6], k0[6], k1[6], km[6];
        {
            bf16x8 m[10], wa[10], wb[10];
            f32x2 z[12];
            f32x4 ac[6];
            unsigned fo[6];
            m[0] = lr128(l16 + L_MEM); m[1] = lr128(l16 + L_MEM + 1024);
            SCHED0();
            B.run<30, true, 18, 20, 0>(W, [&](auto I) { constexpr int i = CV(I);
                if constexpr (i % 7 == 0 || i % 7 == 1 || i % 7 == 3) xn_op<3 + i / 7, (i % 7 + 1) / 2>(k, xv, xsrc, xdst);
                if constexpr (i < 12) { asm volatile("v_mov_b64 %0, 0" : "=v"(z[i])); if constexpr (i & 1) ac[i >> 1] = __builtin_shufflevector(z[i - 1], z[i], 0, 1, 2, 3); } });
            QSTAMP(1, 1)
            asm volatile("s_waitcnt lgkmcnt(0)" : "+s"(now));
            auto fin = [&](auto J) { constexpr int j = CV(J), t = 5 - j / 3;
                if constexpr (j >= 0 && j < 18) fold_op<j % 3>(ac[t], fo[t]); };
            auto put = [&](auto T) { constexpr int t = CV(T); if constexpr (t < 3) lw32(dq + t * 1024, fo[t]); else lw32(dqm + (t - 3) * 1024, fo[t]); };
            sfor<60>([&](auto I) {
                constexpr int i = CV(I), u = i - 42, b = i < 42 ? i / 6 : 7 + u % 3, t = i < 42 ? (i + 5) % 6 : 5 - u / 3, id = QIDX(t, b);
                if constexpr (t == 5 && b >= 2) smf<false>(ac[5], m[b], CAT8(wa[b], wb[b]), ix);
                else smf<(id < 32)>(ac[t], m[b], W[id % 52], ix);
                SCHED0();
                if constexpr (i < 36) {
                    constexpr int bb = i / 6 + 2;
                    if constexpr (i % 6 == 0) m[bb] = lr128(l16 + L_MEM + bb * 1024);
                    if constexpr (i % 6 == 3) wa[bb] = lr128(lwv + (bb - 2) * 2048);
                    if constexpr (i % 6 == 4) wb[bb] = lr128(lwv + (bb - 2) * 2048 + 1024);
                }
                if constexpr (i == 32) m[8] = lr128(l16 + L_MEM + 8192);
                if constexpr (i == 31) m[9] = lr128(l16 + L_MEM + 9216);
                if constexpr (i == 36) wa[8] = lr128(lwv + 12288);
                if constexpr (i == 37) wb[8] = lr128(lwv + 13312);
                if constexpr (i == 38) wa[9] = lr128(lwv + 14336);
                if constexpr (i == 39) wb[9] = lr128(lwv + 15360);
                if constexpr (i == 8) st32(k.rs, vz, pbc, fo[0]);
                if constexpr (i < 36 && (i % 6 == 1 || i % 6 == 2 || i % 6 == 5)) B.out<(i / 6) * 3 + (i % 6) / 2, 6145>(dq, dq);
                if constexpr (i == 3) { fo[0] = ANNOUNCE(s, now, dly); PIN(fo[0]); }
                if constexpr (u >= 3 && u < 11) { constexpr int f = (u - 3) >> 1; if constexpr (u & 1) k0[f] = lr128(l16 + L_K + f * 1024); else k1[f] = lr128(l16 + L_K + (6 + f) * 1024); }
                if constexpr (u == 11 || u == 14 || u == 17) put(IC(5 - (u - 11) / 3));
                if constexpr (u >= 7) fin(IC(u - 7));
                if constexpr (i == 0) { QSTAMP(1, 2) }
                if constexpr (i == 20) { QSTAMP(1, 3) }
                if constexpr (i == 41) { QSTAMP(1, 4) }
                if constexpr (i == 59) { QSTAMP(1, 5) }
                SCHED0();
            });
            fin(IC(11)); fin(IC(12)); fin(IC(13)); put(IC(2)); fin(IC(14));
            asm volatile("s_nop 0" : "+v"(ac[0]));
            put(IC(1)); fin(IC(15)); fin(IC(16)); fin(IC(17)); put(IC(0));
        }
        if (!xn_ok && s < 63) {
            PCOUNT(1)
            cnt_wait(k, k.cnt(C_XN, s + 1), 8u);
            xn_all(k, l16, xsrc, xdst);
            DRAIN();
        }
        asm volatile("s_waitcnt vmcnt(5) lgkmcnt(0)\n\ts_barrier" ::: "memory");
        QSTAMP(0, 1)
        {
            auto rq = [&](int ks) { q[ks] = CAT4(lrtr(qp + ks * 1024), lrtr(qp + ks * 1024 + 512)); };
            auto rm = [&](int ks) { km[ks] = CAT4(lrtr(trp + ks * 1024), lrtr(trp + ks * 1024 + 512)); };
            f32x4 sc[3];
            rq(0); rq(1); rm(0); rq(2); rm(1);
            SCHED0();
            sfor<6>([&](auto KS) { constexpr int ks = CV(KS);
                mfa<false, ks == 0>(sc[0], k0[ks], q[ks]);
                SCHED0();
                if constexpr (ks + 3 < 6) rq(ks + 3);
                if constexpr (ks == 3) A.r0<0>();
                if constexpr (ks == 4) A.r0<1>();
                SCHED0();
                mfa<false, ks == 0>(sc[1], k1[ks], q[ks]);
                SCHED0();
                if constexpr (ks < 2) { k0[(ks + 4) % 6] = lr128(l16 + L_K + (ks + 4) * 1024); k1[(ks + 4) % 6] = lr128(l16 + L_K + (10 + ks) * 1024); }
                if constexpr (ks == 3) A.r1<0>();
                if constexpr (ks == 4) A.r1<1>();
                SCHED0();
                mfa<false, ks == 0>(sc[2], km[ks], q[ks]);
                SCHED0();
                if constexpr (ks + 2 < 6) rm(ks + 2);
                SCHED0();
            });
            asm volatile("s_nop 5" : "+v"(sc[0]), "+v"(sc[1]));
            const float y0 = csel(sc[0][2], sc[1][0], S.mY), y1 = csel(sc[0][3], sc[1][1], S.mY);
            S.a[0] = csel(sc[0][0], y0, S.mA); S.a[1] = csel(sc[0][1], y1, S.mA); S.a[2] = csel(y0, sc[1][2], S.mA); S.a[3] = csel(y1, sc[1][3], S.mA);
            asm volatile("" : "+v"(sc[2]));
            S.a[4] = sc[2][0]; S.a[5] = sc[2][1];
        }
        QSTAMP(0, 2)
        {
            unsigned xc, a0, a1, ph;
            SCHED0();
            A.run<0, false, 0, 10, 12, true>(W, [&](auto I) { constexpr int i = CV(I);
                S.op<i>(k, a0, a1, ph);
                if constexpr (i == 12) { a0 = po0 + (s & 3) * PR_SZ; ph = (s & 4) ? 0x80008000u : 0u; PINS(a0); PINS(ph); }
                if constexpr (i == 15) { a1 = a0 + (po1 - po0); if (s < 0) { a0 = pdum; a1 = pdum; } PINS(a0); PINS(a1); }
                if constexpr (i == 30) done = rt_async();
                if constexpr (i == 34) xc = ld32(k.rs, vz, k.p0 + CNT(C_XN, (s + 2) & 63)); });
            asm volatile("s_waitcnt vmcnt(3)\n\ts_barrier" ::: "memory");
            QSTAMP(0, 3)
            A.run<0, false, 10, 20, 20>(W, [&](auto I) { constexpr int i = CV(I);
                if constexpr (i == 56) B.r0<0>();
                if constexpr (i == 57) B.r1<0>();
                if constexpr (i == 63) B.r0<1>();
                if constexpr (i == 64) B.r1<1>(); });
            unsigned ga;
            auto gat = [&](auto I) { constexpr int i = CV(I);
                if constexpr (i == GPOS - 1) { ga = k.memf((s + 1) & 63) + w * 2048; PINS(ga); }
                if constexpr (i >= GPOS && i < GPOS + 6 && !((i - GPOS) & 1)) G.op<(i - GPOS) / 2>(k, l16, ga); };
            B.run<30, true, 0, 12, 20>(W, [&](auto I) { constexpr int i = CV(I); A.out<i - 1, 1>(d0, d0); gat(I);
                if constexpr (i == 30) {
                    xn_ok = s < 62 && (unsigned)__builtin_amdgcn_readfirstlane(xc) + 1u >= 8u;
                    __builtin_amdgcn_fence(__ATOMIC_ACQUIRE, "wavefront");
                    xv = l16 | (xn_ok ? 0u : OOB); PIN(xv);
                }
                if constexpr (i == 33) { xsrc = k.seg(PG_XNA, (s + 2) & 63) + w * 2048; PINS(xsrc); } });
            bar_lgkm<6>();
            constexpr int X0 = GPOS + 6 > 84 ? GPOS + 6 - 84 : 0;
            static_assert(X0 + 18 <= 42, "GPOS");
            B.run<30, true, 12, 18, 20>(W, [&](auto I) { constexpr int i = CV(I), x = i - X0; gat(IC(i + 84));
                if constexpr (x >= 0 && x < 18 && !(x & 1)) xn_op<(x / 2) / 3, (x / 2) % 3>(k, xv, xsrc, xdst); });
            QSTAMP(0, 4)
            GATHER_FINISH(G, ga, 6)
            QSTAMP(0, 5)
            PFLUSH(s)
        }
    }
    PROF_OUT
}

#define OIDX(t, b) (22 + (t) * 3 + (b))
DEVI void vo_role(const Params& p, const Ctx& k, char* smem, int h) {
    PROF_DECL
    const int w = k.w;
    if (k.c) sleep_ticks(WDELAY);
    bf16x16 W[52];
    {
        const int l = k.l;
        bf16x8 lo[10], wt[16];
        const u16* wv = p.Wv + h * 192 + w * 48;
        const u16* wo = p.Wo + (size_t)h * 192 * 640 + w * 160;
        wstream<22, 6, 6>(smem + w * 36864,
            [&](auto U, unsigned sl) { constexpr int u = CV(U);
                if constexpr (u >= 12) { wp_issue<3>(wv + (size_t)(u - 12) * 64 * 6144, 6144, sl, l); wp_issue<3>(wv + (size_t)((u - 12) * 64 + 32) * 6144, 6144, sl + 3072, l); }
                else { wp_issue<5>(wo + (u >> 1) * 32 * 640 + (u & 1) * 80, 640, sl, l); dma1(wo, l * 16, sl + 5120); } },
            [&](auto U, char* sp) { constexpr int u = CV(U);
                if constexpr (u >= 12) {
                    constexpr int b = u - 12;
#pragma unroll
                    for (int t = 0; t < 3; ++t) {
                        bf16x8 x = wp_read<3, true>(sp, t, l), y = wp_read<3, true>(sp + 3072, t, l);
                        if (t == 2 && b >= 2) { wt[(2 * (b - 2)) & 15] = x; wt[(2 * (b - 2) + 1) & 15] = y; } else W[(t * 10 + b) % 52] = CAT8(x, y);
                    }
                } else {
                    constexpr int ks = u >> 1, b = ks >> 1;
#pragma unroll
                    for (int i = 0; i < 5; ++i) {
                        const int t = (u & 1) * 5 + i;
                        bf16x8 f = wp_read<5, false>(sp, i, l);
                        if (ks & 1) W[OIDX(t, b)] = CAT8(lo[t], f); else lo[t] = f;
                    }
                } });
        RAWBAR()
#pragma unroll
        for (int i = 0; i < 16; ++i) *(bf16x8*)(smem + L_W + w * 16384 + i * 1024 + l * 16) = wt[i];
    }
    RSTAMP(2)
    unsigned l16, lwv, ix, dq0, trp, vo, d0, d1, vsig;
    {
        const int l = k.l, g = l >> 4, n = l & 15, q = (l >> 2) & 3, pp = l & 3;
        l16 = l * 16; lwv = l16 + L_W + w * 16384; ix = SPIX(l);
        dq0 = L_QM + (24 * w + (n >> 2) * 2 + (n & 1)) * 32 + g * 8 + ((n >> 1) & 1) * 4;
        trp = L_QM + (16 * (g >> 1) + 4 * (g & 1) + 8 * (q >> 1) + (q & 1)) * 32 + pp * 8;
        vo = EXP(h * 512) + (g >> 1) * 256 + n * 16 + (g & 1) * 8;
        d0 = L_K + l16 + ((3 * w) >> 1) * 1024 + ((3 * w) & 1) * 8; d1 = L_K + l16 + ((3 * w + 1) >> 1) * 1024 + ((3 * w + 1) & 1) * 8;
        vsig = l == 0 ? 0u : OOB;
        PIN(l16); PIN(lwv); PIN(ix); PIN(dq0); PIN(trp); PIN(vo); PIN(d0); PIN(d1); PIN(vsig);
    }
    const unsigned pdum = k.pg(1) + DUMMY_B, xdst = L_XN + w * 2048, pw = pdum + 98304 + h * 512 + w * 128, pbc = w == 0 ? k.p0 + CNT(C_BC, (h >> 3) * 16 + 8 + (h & 7)) : pw;
    Gather G;
    G3 V;
    V.l16 = l16; V.lwv = lwv;
    bool xn_ok = false;
    unsigned long long now = 0, done = 0;
    for (int s = -2; s < 64; ++s) {
        if (!xn_ok && s + 2 < 64) { PCOUNT(1) cnt_wait(k, k.cnt(C_XN, s + 2), 8u); xn_all(k, l16, k.seg(PG_XNA, s + 2) + w * 2048, xdst); DRAIN(); }
        asm volatile("s_waitcnt lgkmcnt(0)\n\ts_barrier" : "+s"(done) :: "memory");
        const unsigned dly = min((unsigned)done - (unsigned)now, 200u) + DV;
        now = rt_async();
        STOP(s) STAMP(s, 0)
        unsigned xc;
        {
            bf16x8 m[10], wa[10], wb[10];
            m[0] = lr128(l16 + L_MEM); m[1] = lr128(l16 + L_MEM + 1024); m[2] = lr128(l16 + L_MEM + 2048); wa[2] = lr128(lwv); wb[2] = lr128(lwv + 1024);
            f32x4 acc[3];
#pragma unroll
            for (int t = 0; t < 3; ++t) acc[t] = (f32x4){0.f, 0.f, 0.f, 0.f};
            asm volatile("s_waitcnt lgkmcnt(0)" : "+v"(acc[0]), "+v"(acc[1]), "+v"(acc[2]), "+s"(now));
            unsigned an;
            SCHED0();
            sfor<30>([&](auto I) {
                constexpr int i = CV(I), b = i / 3, t = i % 3;
                if constexpr (t == 2 && b >= 2) smf<false>(acc[2], m[b], CAT8(wa[b], wb[b]), ix); else smf<true>(acc[t], m[b], W[t * 10 + b], ix);
                SCHED0();
                if constexpr (b + 3 < 10) {
                    if constexpr (t == 0) m[(b + 3) % 10] = lr128(l16 + L_MEM + (b + 3) * 1024);
                    if constexpr (t == 1) wa[(b + 3) % 10] = lr128(lwv + (b + 1) * 2048);
                    if constexpr (t == 2) wb[(b + 3) % 10] = lr128(lwv + (b + 1) * 2048 + 1024);
                }
                if constexpr (i == 0) { an = ANNOUNCE(s, now, dly); PIN(an); }
                if constexpr (i == 2) st32(k.rs, vsig, pbc, an);
                if constexpr (i == 21) V.r0<0>();
                if constexpr (i == 22) V.r1<0>();
                if constexpr (i == 24) V.r0<1>();
                if constexpr (i == 25) V.r1<1>();
                SCHED0();
            });
            unsigned fo;
            V.run<0, true, 0, 3, 5>(W, [&](auto I) { constexpr int i = CV(I), j = i - 4;
                if constexpr (j >= 0 && j < 12) { if constexpr (j % 4 < 3) fold_op<j % 4>(acc[j / 4], fo); else lw32(dq0 + (j / 4) * 256, fo); } });
        }
        asm volatile("s_waitcnt vmcnt(1) lgkmcnt(1)\n\ts_barrier" ::: "memory");
        STAMP(s, 1)
        {
            bf16x4 al[3], ah[3];
            f32x2 z[20];
            f32x4 acc[10];
            unsigned yb, fo[2];
            auto clr = [&](auto I) { constexpr int i = CV(I); if constexpr (i < 20) { asm volatile("v_mov_b64 %0, 0" : "=v"(z[i])); if constexpr (i & 1) acc[i >> 1] = __builtin_shufflevector(z[i - 1], z[i], 0, 1, 2, 3); } };
            auto fin = [&](auto TP, auto J) { constexpr int tp = CV(TP), j = CV(J);
                if constexpr (j < 6) fold_op<j / 2>(acc[2 * tp + (j & 1)], fo[j & 1]);
                else if constexpr (j < 8) asm volatile("v_min_u32 %0, -2, %0" : "+v"(fo[j & 1]));
                else if constexpr (j == 9) asm volatile("v_permlane16_swap_b32 %0, %1" : "+v"(fo[0]), "+v"(fo[1]));
                else if constexpr (j == 10) st64(k.rs, vo, yb + tp * 32768, (u32x2){fo[0], fo[1]}); };
            al[0] = lrtr(trp); ah[0] = lrtr(trp + 64);
            SCHED0();
            V.run<0, true, 3, 5, 6>(W, [&](auto I) { constexpr int i = CV(I);
                if constexpr (i == 2) al[1] = lrtr(trp + 1024);
                if constexpr (i == 4) ah[1] = lrtr(trp + 1088);
                if constexpr (i == 5) al[2] = lrtr(trp + 2048);
                if constexpr (i == 6) ah[2] = lrtr(trp + 2112);
                if constexpr (i < 4) clr(I);
                if constexpr (i == 7) atom1(k.rs, vsig, (w == 3 && s >= -1 && s < 63) ? k.p0 + CNT(C_Y, s + 1) : pw);
                if constexpr (i == 9) { yb = (s < 0 ? pdum : k.ym(s)) + w * 163840; PINS(yb); } });
            sfor<5>([&](auto TP) {
                constexpr int tp = CV(TP), ks = 5 + tp;
                sfor<6>([&](auto J) { constexpr int j = CV(J), b = j >> 1, t = 2 * tp + (j & 1), id = OIDX(t, b);
                    smf<(id < 32)>(acc[t], CAT4(al[b], ah[b]), W[id], ix);
                    SCHED0();
                    if constexpr (j == 0) V.r0<ks + 1>();
                    if constexpr (j == 1) V.r1<ks + 1>();
                    if constexpr (j == 2) V.rw<ks + 1>();
                    if constexpr (tp > 0 && j < 5) fin(IC((tp + 4) % 5), IC(6 + j));
                    SCHED0(); });
                sfor<6>([&](auto J) { constexpr int j = CV(J);
                    V.mm<0, true, ks, j>(W);
                    if constexpr (tp == 4) {
                        if constexpr (j == 0) V.r0<ks + 2>();
                        if constexpr (j == 1) V.r1<ks + 2>();
                        if constexpr (j == 2) V.rw<ks + 2>();
                    }
                    if constexpr (j < 2) { clr(IC(4 * tp + 4 + 2 * j)); clr(IC(4 * tp + 5 + 2 * j)); }
                    if constexpr (j == 2) asm volatile("s_nop 1");
                    if constexpr (j >= 3) { fin(TP, IC(2 * (j - 3))); fin(TP, IC(2 * (j - 3) + 1)); }
                    SCHED0(); });
            });
            STAMP(s, 2)
            V.run<0, true, 10, 20, 20>(W, [&](auto I) { constexpr int i = CV(I);
                if constexpr (i < 5) fin(IC(4), IC(6 + i));
                if constexpr (i == 5) done = rt_async();
                if constexpr (i == 14) xc = ld32(k.rs, 0, k.p0 + CNT(C_XN, (s + 3) & 63)); });
            NOP7_3(V.a0); NOP7_3(V.a1);
            sfor<18>([&](auto I) { V.out<CV(I), 0>(d0, d1); });
        }
        RAWBAR()
        STAMP(s, 3)
        {
            bf16x8 v[12];
#pragma unroll
            for (int i = 0; i < 6; ++i) { v[i] = lr128(l16 + L_K + i * 1024); v[6 + i] = lr128(l16 + L_K + (6 + i) * 1024); }
            xn_ok = s + 3 < 64 && (unsigned)__builtin_amdgcn_readfirstlane(xc) + 1u >= 8u;
            __builtin_amdgcn_fence(__ATOMIC_ACQUIRE, "wavefront");
            const unsigned xv = xn_ok ? l16 : OOB, xsrc = k.seg(PG_XNA, (s + 3) & 63) + w * 2048, ga = k.memf(s < 0 ? 0 : (s + 1) & 63) + w * 2048;
            f32x4 acc[10][2];
            u32x4 o;
            const unsigned yb = (s + 2 < 64 ? k.yx(s + 2, w) : pdum) + EXP(h * 1024);
            SCHED0();
            sfor<120>([&](auto I) {
                constexpr int i = CV(I), t = i / 12, j = i % 12, ks = j >> 1, rt = j & 1, b = ks >> 1, id = OIDX(t, b);
                if constexpr (ks & 1) mfb<(id < 32), false>(acc[t][rt], v[rt * 6 + ks], HI8(W[id]));
                else mfb<(id < 32), ks == 0>(acc[t][rt], v[rt * 6 + ks], LO8(W[id]));
                SCHED0();
                if constexpr (t > 0 && j >= 6 && j < 10) { constexpr int c = j - 6; o[c] = cvtpk(acc[t - 1][c >> 1][2 * (c & 1)], acc[t - 1][c >> 1][2 * (c & 1) + 1]); PIN(o[c]); }
                if constexpr (t > 0 && j == 10) __builtin_amdgcn_raw_buffer_store_b128(o, k.rs, (int)l16, (int)(yb + (t - 1) * 65536), AUX_SC1);
                if constexpr (t > 1 && j == 0) asm volatile("" :: "v"(o));
                if constexpr (t < 5 && (j == 1 || j == 3 || j == 5)) xn_op<t % 5, j / 2>(k, xv, xsrc, xdst);
                if constexpr (t == GPOSY && (j == 1 || j == 3 || j == 5)) G.op<j / 2>(k, l16, ga);
                SCHED0();
            });
            asm volatile("s_nop 7" : "+v"(acc[9][0]), "+v"(acc[9][1]) : "v"(o));
            o[0] = cvtpk(acc[9][0][0], acc[9][0][1]); o[1] = cvtpk(acc[9][0][2], acc[9][0][3]); o[2] = cvtpk(acc[9][1][0], acc[9][1][1]); o[3] = cvtpk(acc[9][1][2], acc[9][1][3]);
            st128(k.rs, l16, yb + 9 * 65536, o);
            STAMP(s, 4)
            GATHER_FINISH(G, ga, 11 - GPOSY)
            STAMP(s, 5)
            PFLUSH(s)
        }
    }
    PROF_OUT
}

DEVI void ln_store(const Ctx& k, const float (&v)[40], unsigned dst, int row  ) {
    f32x2 s2[4] = {{0.f, 0.f}, {0.f, 0.f}, {0.f, 0.f}, {0.f, 0.f}};
#pragma unroll
    for (int i = 0; i < 40; i += 2) s2[(i >> 1) & 3] += (f32x2){v[i], v[i + 1]};
    const f32x2 st = (s2[0] + s2[1]) + (s2[2] + s2[3]);
    float m = row16sum(st[0] + st[1]) * (1.f / 640.f);
    f32x2 q2[4] = {{0.f, 0.f}, {0.f, 0.f}, {0.f, 0.f}, {0.f, 0.f}};
#pragma unroll
    for (int i = 0; i < 40; i += 2) { const f32x2 d = (f32x2){v[i], v[i + 1]} - (f32x2){m, m}; q2[(i >> 1) & 3] += d * d; }
    const f32x2 qt = (q2[0] + q2[1]) + (q2[2] + q2[3]);
    const float q = row16sum(qt[0] + qt[1]);
    const float var = bfr(q * (1.f / 640.f));
    m = bfr(m);
    const float rden = __builtin_amdgcn_rcpf(bfr(sqrtf(bfr(var + 1e-5f))));
    const int n = k.l & 15;
    const unsigned vo = (n >> 2) * 2048 + (row >> 4) * 1024 + ((n & 3) * 16 + (row & 15)) * 16;
#pragma unroll
    for (int c = 0; c < 5; ++c) {
        u32x4 o;
#pragma unroll
        for (int i = 0; i < 4; ++i) { unsigned d = cvtpk(v[c * 8 + 2 * i] - m, v[c * 8 + 2 * i + 1] - m); o[i] = cvtpk(blo(d) * rden, bhi(d) * rden); }
        st128(k.rs, vo, dst + c * 16384, o);
    }
}
DEVI void unpack8(float* v, u32x4 a) {
#pragma unroll
    for (int i = 0; i < 4; ++i) { v[2 * i] = blo(a[i]); v[2 * i + 1] = bhi(a[i]); }
}
DEVI void ln_x_batch(const Params& p, const Ctx& k, int bw) {
    const int row = bw * 4 + (k.l >> 4), sg = bw >> 3;
    float v[40];
#pragma unroll
    for (int c = 0; c < 5; ++c) unpack8(v + c * 8, *(const u32x4*)(p.x + (size_t)row * 640 + (c * 16 + (k.l & 15)) * 8));
    ln_store(k, v, k.seg0(PG_XNA, sg), row & 31);
    DRAIN();
    if (k.l == 0) cnt_add(k.cnt0(C_XN, sg));
}
DEVI void row_role(const Params& p, const Ctx& k, char* smem, int id) {
    PROF_DECL
    const int row = (id * 4 + k.w) * 4 + (k.l >> 4), n = k.l & 15;
    const unsigned vo = (n >> 2) * 2048 + (row >> 4) * 1024 + ((n & 3) * 16 + (row & 15)) * 16;
    RSTAMP(2)
    for (int sg = k.c; sg < 64; sg += 2) {
        cnt_wait(k, k.cnt0(C_X2, sg), 40u);
        STOP(sg) PFLUSH(sg) PFLUSH(sg ^ 1)
        const unsigned src = k.seg0(PG_X2, sg);
        float v[40];
        u32x4 a[5];
#pragma unroll
        for (int c = 0; c < 5; ++c) a[c] = ld128(k.rs, vo, src + c * 16384);
#pragma unroll
        for (int c = 0; c < 5; ++c) unpack8(v + c * 8, a[c]);
        ln_store(k, v, k.seg0(PG_XNA, sg) ^ 8192u, row);
        DRAIN();
        if (k.l == 0) cnt_add(k.ws + ((k.pg0(0) ^ 8192u) + CNT(C_XN, sg)));
    }
    PROF_OUT
}

DEVI int imin4(u32x4 v) { return min(min((int)v[0], (int)v[1]), min((int)v[2], (int)v[3])); }
struct Red {
    u32x4 Y[16];
    DEVI void yload(const Ctx& k, int j, int sg) {
        const unsigned a = k.yx(sg, j / 5) + (j % 5) * 131072 + k.w * 16384;
#pragma unroll
        for (int i = 0; i < 16; ++i) Y[i] = ld128(k.rs, k.l * 16 + (i & 3) * 1024, a + ((i >> 2) & 1) * 4096 + (i >> 3) * 65536);
    }
};
DEVI void wait_until(unsigned t) { while ((int)((rclk() - t) << 16) < 0) { } }
#define DPPMAX(x, c) x = max(x, __builtin_amdgcn_update_dpp(0, x, c, 0xf, 0xf, false))
#define WAIT_BC(s) { \
    unsigned bc; \
    _Pragma("unroll 1") for (;; ++spins) { \
        bc = ld32(k.rs, (l & 15) * 128, k.p0 + CNT(C_BC, w * 16)); \
        DRAIN(); \
        if (__all(((int)bc >> 16) >= s)) break; \
        __builtin_amdgcn_s_sleep(BSLP); \
        if (spin_fail(k, spins)) break; \
    } \
    const unsigned now = rclk(); \
    int d = ((int)bc >> 16) > s ? 0 : (int)((bc - now) << 16); \
    DPPMAX(d, 0xB1); DPPMAX(d, 0x4E); DPPMAX(d, 0x141); \
    tq = now + (__builtin_amdgcn_readlane(d, 0) >> 16); tv = now + (__builtin_amdgcn_readlane(d, 8) >> 16); }
DEVI void mred_role(const Params& p, const Ctx& k, char* smem, int j) {
    PROF_DECL
    const int l = k.l, w = k.w, g = l >> 4, n = l & 15;
    const unsigned ix = SPIX(l);
    const unsigned vp = (g * 16 + ((n >> 1) & 1) * 8 + (n >> 2) * 2 + (n & 1)) * 16;
    const unsigned v16 = (g >> 1) * 1024 + (g & 1) * 256 + n * 16, yo = j * 32768 + EXP(w * 4096);
    const u32x4 ones = {0xFFFFFFFFu, 0xFFFFFFFFu, 0xFFFFFFFFu, 0xFFFFFFFFu};
    auto arm = [&](int s) {
#pragma unroll
        for (int i = 0; i < 4; ++i) st128(k.rs, l * 16 + i * 1024, k.ym(s) + yo, ones);
    };
    Red R;
    RSTAMP(2)
    arm(1);
    cnt_wait(k, k.cnt(C_Y, 0), 32u);
    R.yload(k, j, 0);
    for (int s = 0; s < 63; ++s) {
        STOP(s) STAMP(s, 0)
        const unsigned ap = k.p0 + PR_B + (s & 3) * PR_SZ + w * 49152, am = k.ym(s) + yo;
        const unsigned ph = (s & 4) ? 0x80008000u : 0u;
        unsigned spins = 0, tq, tv, yc;
        u32x4 fl;
        WAIT_BC(s)
        STAMP(s, 5)
        char* rb = smem + (s & 1) * 4096;
_Pragma("unroll 1")
        for (spins = 0;; ++spins) {
            u32x4 ym[4], pm[5];
            f32x4 acc[2];
            if (spins == 0) wait_until(tv);
            asm volatile("" ::: "memory");
#pragma unroll
            for (int i = 0; i < 4; ++i) ym[i] = ld128(k.rs, v16 + (i >> 1) * 2048 + (i & 1) * 512, am);
            yc = ld32(k.rs, 0, k.p0 + CNT(C_Y, s + 1));
            fl = ld128(k.rs, 0, k.p0 + CNT(C_X1, j));
            asm volatile("" ::: "memory");
            if (spins == 0) { wait_until(tq); STAMP(s, 1) }
            asm volatile("" ::: "memory");
#pragma unroll
            for (int i = 0; i < 5; ++i) pm[i] = ld128(k.rs, i < 4 ? l * 16 + i * 1024 : vp, ap + (i >> 2) * 4096);
            acc[0] = (f32x4){0.f, 0.f, 0.f, 0.f}; acc[1] = acc[0];
            const u32x8 y0 = {ym[0][0], ym[1][0], ym[0][1], ym[1][1], ym[2][0], ym[3][0], ym[2][1], ym[3][1]};
            const u32x8 y1 = {ym[0][2], ym[1][2], ym[0][3], ym[1][3], ym[2][2], ym[3][2], ym[2][3], ym[3][3]};
            unsigned mx = 0;
#pragma unroll
            for (int i = 0; i < 4; ++i) mx = max(mx, umax4(ym[i]));
            unsigned tg = mx == 0xFFFFFFFFu ? 0x80000000u : 0u;
            asm volatile("s_nop 1" : "+v"(acc[0]), "+v"(acc[1]), "+v"(tg) : "v"(y0), "v"(y1), "v"(ix));
            SCHED0();
#pragma unroll
            for (int i = 0; i < 5; ++i) {
                const unsigned u0 = __builtin_amdgcn_bitop3_b32(pm[i][0], pm[i][1], ph, 0x7e), u1 = __builtin_amdgcn_bitop3_b32(pm[i][2], pm[i][3], ph, 0x7e);
                SCHED0();
                if (i < 4) smf<false>(acc[0], __builtin_bit_cast(bf16x8, pm[i]), __builtin_bit_cast(bf16x16, __builtin_shufflevector(R.Y[2 * i], R.Y[2 * i + 1], 0, 1, 2, 3, 4, 5, 6, 7)), ix);
                else smf<false>(acc[0], __builtin_bit_cast(bf16x8, pm[4]), __builtin_bit_cast(bf16x16, y0), ix);
                tg = (tg | u0) | u1;
                SCHED0();
                if (i < 4) smf<false>(acc[1], __builtin_bit_cast(bf16x8, pm[i]), __builtin_bit_cast(bf16x16, __builtin_shufflevector(R.Y[8 + 2 * i], R.Y[9 + 2 * i], 0, 1, 2, 3, 4, 5, 6, 7)), ix);
                else smf<false>(acc[1], __builtin_bit_cast(bf16x8, pm[4]), __builtin_bit_cast(bf16x16, y1), ix);
                SCHED0();
            }
            const unsigned long long bad = __ballot((int)tg < 0);
            asm volatile("s_nop 5" : "+v"(acc[0]), "+v"(acc[1]) : "s"(bad));
#pragma unroll
            for (int t = 0; t < 2; ++t) { float* rp = (float*)(rb + t * 2048 + w * 512 + g * 128 + n * 4); rp[0] = acc[t][0] + acc[t][2]; rp[16] = acc[t][1] + acc[t][3]; }
            if (bad == 0) break;
            PCOUNT(0)
            asm volatile("" ::: "memory");
            if (spin_fail(k, spins)) break;
        }
        STAMP(s, 2)
        RAWBAR()
        if (w < 2) {
            const char* rp = rb + w * 2048 + ((l >> 4) * 2 + ((l >> 2) & 1)) * 64 + ((l & 3) * 4 + ((l >> 3) & 1) * 2) * 4;
            const f32x2 a = *(const f32x2*)rp, b = *(const f32x2*)(rp + 512), c = *(const f32x2*)(rp + 1024), d = *(const f32x2*)(rp + 1536);
            const f32x2 r = (a + b) + (c + d);
            st32(k.rs, l * 4, k.memf(s + 1) + EXP((2 * j + w) * 256), min(cvtpk(r[0], r[1]) ^ ph, 0xFFFFFFFEu));
        }
        STAMP(s, 3)
        if ((unsigned)__builtin_amdgcn_readfirstlane(yc) + 1u < 32u) { PCOUNT(2) cnt_wait(k, k.cnt(C_Y, s + 1), 32u); }
        __builtin_amdgcn_fence(__ATOMIC_ACQUIRE, "wavefront");
        R.yload(k, j, s + 1);
_Pragma("unroll 1")
        for (spins = 0; __builtin_amdgcn_readfirstlane(imin4(fl)) < s - 2; ++spins) {
            PCOUNT(1)
            __builtin_amdgcn_s_sleep(PSLEEP);
            if (spin_fail(k, spins)) break;
            fl = ld128(k.rs, 0, k.p0 + CNT(C_X1, j));
        }
        __builtin_amdgcn_fence(__ATOMIC_ACQUIRE, "wavefront");
        arm(s + 2);
        STAMP(s, 4)
        PFLUSH(s)
    }
    PFLUSH(63)
    PROF_OUT
}
DEVI void tred_role(const Params& p, const Ctx& k, char* smem, int j) {
    PROF_DECL
    const int l = k.l, w = k.w, g = l >> 4, n = l & 15, T = 2 * j + (w >> 1), rt = w & 1;
    const unsigned v16 = (g >> 1) * 1024 + (g & 1) * 256 + n * 16, yo = j * 32768 + EXP(w * 4096);
    const unsigned v8 = (g >> 1) * 512 + (g & 1) * 128 + n * 8;
    const unsigned xo = EXP(j * 2048) + (T & 1) * 512 + rt * 1024, xv = ((g >> 1) * 16 + n) * 16 + (g & 1) * 8;
    Red R;
    RSTAMP(2)
    cnt_wait(k, k.cnt(C_Y, 0), 32u);
    R.yload(k, j, 0);
    for (int s = 0; s < 64; ++s) {
        STOP(s) STAMP(s, 0)
        const unsigned ap1 = k.p0 + PR_B + (s & 3) * PR_SZ + w * 49152 + 16384, ap2 = ap1 + 16384, am = k.ym(s) + yo;
        const unsigned ph = (s & 4) ? 0x80008000u : 0u;
        unsigned yc, spins = 0, tq, tv;
        u32x2 res;
        if (k.c) res = ld64(k.rs, xv, k.seg0(PG_X2, s) + xo);
        else res = *(const u32x2*)(p.x + (size_t)(s * 32 + rt * 16 + n) * 640 + T * 16 + g * 4);
        f32x4 acc[2][2];
        WAIT_BC(s)
        wait_until(tv); wait_until(tq + RDLY);
        asm volatile("" ::: "memory");
        STAMP(s, 1)
_Pragma("unroll 1")
        for (;; ++spins) {
            u32x4 ym[4], p1[8]; u32x2 p0[8], pq[8];
#pragma unroll
            for (int i = 0; i < 4; ++i) ym[i] = ld128(k.rs, v16 + (i >> 1) * 2048 + (i & 1) * 512, am);
#pragma unroll
            for (int i = 0; i < 8; ++i) pq[i] = ld64(k.rs, v8 + ((i >> 1) & 1) * 1024 + (i & 1) * 256 + (i >> 2) * 2048, ap2 + 4096);
#pragma unroll
            for (int i = 0; i < 8; ++i) { p0[i] = ld64(k.rs, l * 8 + i * 512, ap2); p1[i] = ld128(k.rs, l * 16 + (i & 3) * 1024, ap1 + (i >> 2) * 4096); }
            yc = ld32(k.rs, 0, k.p0 + CNT(C_Y, (s + 1) & 63));
            unsigned mx = 0, tg = 0;
#pragma unroll
            for (int t = 0; t < 2; ++t) {
                acc[t][0] = (f32x4){0.f, 0.f, 0.f, 0.f}; acc[t][1] = acc[t][0];
#pragma unroll
                for (int f = 0; f < 2; ++f) {
                    const u32x4 yf = {ym[2 * f][2 * t], ym[2 * f][2 * t + 1], ym[2 * f + 1][2 * t], ym[2 * f + 1][2 * t + 1]};
#pragma unroll
                    for (int r = 0; r < 2; ++r)
                        acc[t][r] = mf(__builtin_bit_cast(bf16x8, yf), __builtin_bit_cast(bf16x8, (u32x4){pq[4 * r + 2 * f][0], pq[4 * r + 2 * f][1], pq[4 * r + 2 * f + 1][0], pq[4 * r + 2 * f + 1][1]}), acc[t][r]);
                }
            }
#pragma unroll
            for (int i = 0; i < 8; ++i)
#pragma unroll
                for (int t = 0; t < 2; ++t) {
                    acc[t][0] = mf(__builtin_bit_cast(bf16x8, R.Y[t * 8 + i]), __builtin_bit_cast(bf16x8, (u32x4){p0[i][0], p0[i][1], 0u, 0u}), acc[t][0]);
                    acc[t][1] = mf(__builtin_bit_cast(bf16x8, R.Y[t * 8 + i]), __builtin_bit_cast(bf16x8, p1[i]), acc[t][1]);
                }
#pragma unroll
            for (int i = 0; i < 4; ++i) mx = max(mx, umax4(ym[i]));
#pragma unroll
            for (int i = 0; i < 8; ++i) tg |= (((p1[i][0] ^ ph) | (p1[i][1] ^ ph)) | ((p1[i][2] ^ ph) | (p1[i][3] ^ ph))) | (((p0[i][0] ^ ph) | (p0[i][1] ^ ph)) | ((pq[i][0] ^ ph) | (pq[i][1] ^ ph)));
            if (__all(mx != 0xFFFFFFFFu && (int)tg >= 0)) break;
            PCOUNT(0)
            asm volatile("" ::: "memory");
            if (spin_fail(k, spins)) break;
        }
        STAMP(s, 2)
        char* rb = smem + (s & 1) * 16384;
#pragma unroll
        for (int i = 0; i < 4; ++i) *(f32x4*)(rb + i * 4096 + w * 1024 + l * 16) = acc[i >> 1][i & 1];
        RAWBAR()
        {
            const char* rp = rb + w * 4096 + l * 16;
            const f32x4 r = (*(const f32x4*)rp + *(const f32x4*)(rp + 1024)) + (*(const f32x4*)(rp + 2048) + *(const f32x4*)(rp + 3072));
            const unsigned d0 = cvtpk(r[0], r[1]) ^ ph, d1 = cvtpk(r[2], r[3]) ^ ph;
            u32x2 o; o[0] = cvtpk(blo(res[0]) + blo(d0), bhi(res[0]) + bhi(d0)); o[1] = cvtpk(blo(res[1]) + blo(d1), bhi(res[1]) + bhi(d1));
            st64(k.rs, xv, k.seg(PG_X1, s) + xo, o);
        }
        STAMP(s, 3)
        const bool ynow = s == 63 || (unsigned)__builtin_amdgcn_readfirstlane(yc) + 1u >= 32u;
        asm volatile("" ::: "memory");
        R.yload(k, j, (s + 1) & 63);
        asm volatile("s_waitcnt vmcnt(16)" ::: "memory");
        st32(k.rs, l == 0 ? 0u : OOB, k.p0 + CNT(C_X1, j) + w * 4, (unsigned)s);
        STAMP(s, 4)
        if (!ynow) { PCOUNT(2) cnt_wait(k, k.cnt(C_Y, s + 1), 32u); R.yload(k, j, s + 1); }
        STAMP(s, 5)
        PFLUSH(s)
    }
    PROF_OUT
}

DEVI void up_role(const Params& p, const Ctx& k, char* smem, int id) {
    PROF_DECL
    const int l = k.l, w = k.w, g = l >> 4, n = l & 15;
    sleep_ticks(WDELAY);
    bf16x8 Wt[80];
    const int tile0 = id * 8 + w * 2;
    wstream<20, 4, 8>(smem + w * 32768,
        [&](auto U, unsigned sl) { constexpr int u = CV(U);
            wp_issue<2>(p.wg + (size_t)u * 32 * 1536 + tile0 * 16, 1536, sl, l); wp_issue<2>(p.wu + (size_t)u * 32 * 1536 + tile0 * 16, 1536, sl + 2048, l); },
        [&](auto U, char* sp) { constexpr int u = CV(U);
            Wt[u] = wp_read<2, true>(sp, 0, l); Wt[20 + u] = wp_read<2, true>(sp, 1, l); Wt[40 + u] = wp_read<2, true>(sp + 2048, 0, l); Wt[60 + u] = wp_read<2, true>(sp + 2048, 1, l); });
    RAWBAR()
    RSTAMP(2)
    bf16x8 one8 = {0x3F80, 0x3F80, 0x3F80, 0x3F80, 0x3F80, 0x3F80, 0x3F80, 0x3F80};
    asm volatile("s_nop 1" : "+v"(one8));
    f32x4 cs[4];
    sfor<80>([&](auto I) { constexpr int f = CV(I); mfa<(f < 64), f % 20 == 0>(cs[f / 20], Wt[f], one8); });
    asm volatile("s_nop 7" : "+v"(cs[0]), "+v"(cs[1]), "+v"(cs[2]), "+v"(cs[3]));
    const unsigned lds0 = (unsigned)(size_t)(LAS char*)smem;
    const unsigned ho = EXP((tile0 >> 1) * 1024), fv = (l < 20 ? l : 0) * 128;
    constexpr int L_ST = 81920;
    bool issued = false;
    for (int s = 0; s < 64; ++s) {
        if (!issued) {
_Pragma("unroll 1")
            for (unsigned spins = 0; !__all(imin4(ld128(k.rs, fv, k.p0 + CNT(C_X1, 0))) >= s); ++spins) {
                asm volatile("" ::: "memory");
                __builtin_amdgcn_s_sleep(PSLEEP);
                if (spin_fail(k, spins)) break;
            }
            __builtin_amdgcn_fence(__ATOMIC_ACQUIRE, "wavefront");
            xn_all(k, l * 16, k.seg(PG_X1, s) + w * 2048, lds0 + (s & 1) * 40960 + w * 2048);
        }
        STOP(s) PFLUSH(s)
        const int nf = imin4(ld128(k.rs, fv, k.p0 + CNT(C_X1, 0)));
        DRAIN();
        RAWBAR()
        issued = s < 63 && __all(nf >= s + 1);
        __builtin_amdgcn_fence(__ATOMIC_ACQUIRE, "wavefront");
        const unsigned xv = issued ? l * 16 : OOB, xsrc = k.seg(PG_X1, (s + 1) & 63) + w * 2048, xdst = lds0 + ((s + 1) & 1) * 40960 + w * 2048;
        const char* xb = smem + (s & 1) * 40960 + l * 16;
        f32x4 acc[2][4], st[4];
        bf16x8 x0[20], x1[20], y0[5], y1[5];
#pragma unroll
        for (int i = 0; i < 5; ++i) { y0[i] = lds128(xb + (i * 4 + w) * 2048); y1[i] = lds128(xb + (i * 4 + w) * 2048 + 1024); }
        x0[0] = lds128(xb); x1[0] = lds128(xb + 1024); x0[1] = lds128(xb + 2048); x1[1] = lds128(xb + 3072);
        SCHED0();
        sfor<5>([&](auto I) { constexpr int i = CV(I);
            mfa<false, i == 0>(st[0], one8, y0[i]); mfa<false, i == 0>(st[1], y0[i], y0[i]); mfa<false, i == 0>(st[2], one8, y1[i]); mfa<false, i == 0>(st[3], y1[i], y1[i]); });
        SCHED0();
        float* sp = (float*)(smem + L_ST + (s & 1) * 1024);
        sfor<160>([&](auto I) {
            constexpr int i = CV(I), ks = i / 8, rt = (i >> 2) & 1, nt = i & 3, f = nt * 20 + ks;
            mfa<(f < 64), ks == 0>(acc[rt][nt], Wt[f], rt ? x1[ks] : x0[ks]);
            SCHED0();
            if constexpr (ks + 2 < 20 && (i & 7) == 0) x0[(ks + 2) % 20] = lds128(xb + (ks + 2) * 2048);
            if constexpr (ks + 2 < 20 && (i & 7) == 1) x1[(ks + 2) % 20] = lds128(xb + 1024 + (ks + 2) * 2048);
            if constexpr ((i & 7) == 5 && ks < 15) xn_op<(ks / 3) % 5, ks % 3>(k, xv, xsrc, xdst);
            if constexpr (i == 10 || i == 18) {
                constexpr int rt2 = i == 18;
                const f32x4& q = st[2 * rt2 + 1];
                const float d = (n & 2) ? ((n & 1) ? q[3] : q[2]) : ((n & 1) ? q[1] : q[0]);
                float* dp = sp + (g == (n >> 2) ? n * 4 + w : 1024 + l);
                dp[rt2 * 128] = st[2 * rt2][0]; dp[rt2 * 128 + 64] = d;
            }
            if constexpr (i == 79) bar_lgkm<4>();
            SCHED0();
        });
        float mean[2], rden[2];
#pragma unroll
        for (int rt = 0; rt < 2; ++rt) {
            const f32x4 a = *(const f32x4*)(sp + rt * 128 + n * 4), b = *(const f32x4*)(sp + rt * 128 + 64 + n * 4);
            const float m = ((a[0] + a[1]) + (a[2] + a[3])) * (1.f / 640.f);
            const float var = __builtin_fmaxf(((b[0] + b[1]) + (b[2] + b[3])) * (1.f / 640.f) - m * m, 0.f);
            mean[rt] = bfr(m);
            rden[rt] = __builtin_amdgcn_rcpf(bfr(sqrtf(bfr(bfr(var) + 1e-5f))));
        }
        asm volatile("s_nop 7" : "+v"(acc[0][0]), "+v"(acc[0][1]), "+v"(acc[0][2]), "+v"(acc[0][3]), "+v"(acc[1][0]), "+v"(acc[1][1]), "+v"(acc[1][2]), "+v"(acc[1][3]));
#pragma unroll
        for (int rt = 0; rt < 2; ++rt) {
            float hv[8];
#pragma unroll
            for (int i = 0; i < 8; ++i) {
                const float gg = bfr(__builtin_fmaf(-mean[rt], cs[i >> 2][i & 3], acc[rt][i >> 2][i & 3]) * rden[rt]);
                const float u = bfr(__builtin_fmaf(-mean[rt], cs[2 + (i >> 2)][i & 3], acc[rt][2 + (i >> 2)][i & 3]) * rden[rt]);
                hv[i] = bfr(__builtin_amdgcn_rcpf(1.f + __expf(-gg))) * u;
            }
            u32x4 o; o[0] = cvtpk(hv[0], hv[1]); o[1] = cvtpk(hv[2], hv[3]); o[2] = cvtpk(hv[4], hv[5]); o[3] = cvtpk(hv[6], hv[7]);
            st128(k.rs, l * 16, k.hseg(s) + rt * EXP(49152) + ho, o);
        }
        DRAIN();
        if (l == 0) cnt_add(k.cnt(C_H, s));
    }
    PROF_OUT
}

DEVI void down_role(const Params& p, const Ctx& k, char* smem, int id) {
    PROF_DECL
    const int l = k.l, w = k.w, g = l >> 4, n = l & 15, rt = id & 1, cg = id >> 1;
    sleep_ticks(WDELAY);
    bf16x8 Wt[96];
    wstream<12, 8, 4>(smem + w * 32768,
        [&](auto U, unsigned sl) { constexpr int u = decltype(U)::value; wp_issue<8>(p.wd + (size_t)(w * 12 + u) * 32 * 640 + cg * 128, 640, sl, l); },
        [&](auto U, char* sp) { constexpr int u = decltype(U)::value;
#pragma unroll
            for (int nt = 0; nt < 8; ++nt) Wt[u * 8 + nt] = wp_read<8, false>(sp, nt, l); });
    RAWBAR()
    RSTAMP(2)
    const unsigned xo = EXP((cg * 4 + w) * 2048) + rt * 1024, xv = ((g >> 1) * 16 + n) * 16 + (g & 1) * 8;
    u32x4 hx[12];
    u32x2 rn[2];
    auto fetch = [&](int s, unsigned off) {
#pragma unroll
        for (int q = 0; q < 3; ++q) {
            const unsigned a = k.hseg(s) + rt * EXP(49152) + EXP((w * 12 + q * 4) * 1024);
#pragma unroll
            for (int i = 0; i < 4; ++i) hx[q * 4 + i] = ld128(k.rs, (l * 16 + i * 1024) | off, a);
        }
#pragma unroll
        for (int t = 0; t < 2; ++t) rn[t] = ld64(k.rs, (xv + t * 512) | off, k.seg(PG_X1, s) + xo);
    };
    bool have = false;
    for (int s = 0; s < 64; ++s) {
        if (!have) { cnt_wait(k, k.cnt(C_H, s), 48u); fetch(s, 0); }
        STOP(s) PFLUSH(s)
        const unsigned nc = ld32(k.rs, 0, k.p0 + CNT(C_H, (s + 1) & 63));
        f32x4 acc[8];
        sfor<96>([&](auto I) { constexpr int i = CV(I); mfa<(i < 64), (i < 8)>(acc[i & 7], Wt[i], __builtin_bit_cast(bf16x8, hx[i >> 3])); });
        const u32x2 r0 = rn[0], r1 = rn[1];
        have = s < 63 && (unsigned)__builtin_amdgcn_readfirstlane(nc) + 1u >= 48u;
        __builtin_amdgcn_fence(__ATOMIC_ACQUIRE, "wavefront");
        fetch((s + 1) & 63, have ? 0u : OOB);
        asm volatile("s_nop 7" : "+v"(acc[0]), "+v"(acc[1]), "+v"(acc[2]), "+v"(acc[3]), "+v"(acc[4]), "+v"(acc[5]), "+v"(acc[6]), "+v"(acc[7]));
        char* rb = smem + (s & 1) * 32768;
#pragma unroll
        for (int nt = 0; nt < 8; ++nt) *(f32x4*)(rb + (nt * 4 + w) * 1024 + l * 16) = acc[nt];
        RAWBAR()
#pragma unroll
        for (int t = 0; t < 2; ++t) {
            const char* rp = rb + (w * 2 + t) * 4096 + l * 16;
            const f32x4 r = (*(const f32x4*)rp + *(const f32x4*)(rp + 1024)) + (*(const f32x4*)(rp + 2048) + *(const f32x4*)(rp + 3072));
            const unsigned d0 = cvtpk(r[0], r[1]), d1 = cvtpk(r[2], r[3]);
            const u32x2 res = t ? r1 : r0;
            u32x2 o; o[0] = cvtpk(blo(res[0]) + blo(d0), bhi(res[0]) + bhi(d0)); o[1] = cvtpk(blo(res[1]) + blo(d1), bhi(res[1]) + bhi(d1));
            if (k.c == 0) st64(k.rs, xv + t * 512, k.seg(PG_X2, s) + xo, o);
            else *(u32x2*)(p.out + (size_t)(s * 32 + rt * 16 + n) * 640 + (cg * 8 + w * 2 + t) * 16 + g * 4) = o;
        }
        if (k.c == 0) { DRAIN(); if (l == 0) cnt_add(k.cnt(C_X2, s)); }
    }
    PROF_OUT
}

DEVI int xcc_id() { return __builtin_amdgcn_s_getreg(20 | (0 << 6) | (3 << 11)) & 7; }

__global__ __launch_bounds__(256) void init_kernel(Params p) {
    const int b = blockIdx.x, tid = threadIdx.x, l = tid & 63;
    const __amdgpu_buffer_rsrc_t rs = __builtin_amdgcn_make_buffer_rsrc((void*)p.ws, 0, 0x7fffffff, 0x00020000);
    const u32x4 ones = {0xFFFFFFFFu, 0xFFFFFFFFu, 0xFFFFFFFFu, 0xFFFFFFFFu};
    if (b < 64 && tid < 64) {
        unsigned bt[2] = {0xFFFFFFFFu, 0xFFFFFFFFu};
        if (b < NPAGES) {
            const unsigned off = ((unsigned)b << 21) + 7 * 16384 + (l & 7) * 16;
#pragma unroll 1
            for (int r = 0; r < CALR; ++r) {
#pragma unroll
                for (int hf = 0; hf < 2; ++hf) {
                    unsigned long long c0, c1;
                    asm volatile("s_memtime %0\n\ts_waitcnt lgkmcnt(0)" : "=s"(c0) :: "memory");
                    st128(rs, off + hf * 8192, 0, ones);
                    asm volatile("s_waitcnt vmcnt(0)\n\ts_memtime %0\n\ts_waitcnt lgkmcnt(0)" : "=s"(c1) :: "memory");
                    bt[hf] = min(bt[hf], (unsigned)(c1 - c0));
                }
            }
        }
        if (l == 0) ((unsigned*)(p.cm + CM_TAB))[b] = (unsigned)(bt[1] < bt[0]) ^ (unsigned)(xcc_id() >> 2);
    }
    constexpr unsigned N0 = PR_B + 4 * PR_SZ, N1 = 40 * 16384, N2 = 64 * 20480;
    for (unsigned i = (b * 256 + tid) * 16; i < N0 + N1 + N2; i += 256 * 256 * 16) {
        unsigned o;
        u32x4 v = ones;
        if (i < N0) o = i;
        else if (i < N0 + N1) o = ((unsigned)PG_YM << 21) + (i - N0);
        else {
            const unsigned j = i - N0 - N1, s = j / 20480, r = j % 20480;
            const unsigned q = r < 16384 ? r : 16384 + ((r - 16384) >> 11) * 8192 + (r & 2047);
            o = (2u << 21) + s * 32768 + q;
            if (s == 0) {
                const int e = (((q >> 14) << 13) | (q & 8191u)) >> 4, T = e >> 4, hf = (e >> 1) & 1, rr = ((e >> 2) & 3) * 2 + (e & 1);
#pragma unroll
                for (int c = 0; c < 4; ++c) v[c] = min(*(const unsigned*)(p.mem0 + rr * 640 + T * 16 + 4 * c + 2 * hf), 0xFFFFFFFEu);
            }
        }
        *(u32x4*)(p.ws + o) = v;
    }
    if (b == 255) for (unsigned o = CM_TICKET + tid * 16; o < CM_BYTES; o += 4096) *(u32x4*)(p.cm + o) = ones;
}

__global__ __launch_bounds__(256, 1) void mega_kernel(Params p) {
    extern __shared__ __attribute__((aligned(16))) char smem[];
    Ctx k;
    k.ws = p.ws; k.cm = p.cm;
    k.rs = __builtin_amdgcn_make_buffer_rsrc((void*)p.ws, 0, 0x7fffffff, 0x00020000);
    k.l = threadIdx.x & 63; k.w = __builtin_amdgcn_readfirstlane(threadIdx.x >> 6);
    RSTAMP(0)
    k.par = __ballot(((const unsigned*)(p.cm + CM_TAB))[k.l] != 0u);
    if (threadIdx.x == 0) {
        int x = xcc_id(), role = 0;
#pragma unroll 1
        for (int i = 0; i < 8; ++i) {
            const unsigned t = __hip_atomic_fetch_add((GAS unsigned*)(p.cm + CM_TICKET + x * 128), 1u, __ATOMIC_RELAXED, __HIP_MEMORY_SCOPE_AGENT) + 1u;
            if (t < 32u) { role = x * 32 + (int)t; break; }
            x = (x + 1) & 7;
        }
        *(volatile LAS int*)smem = role;
    }
    RAWBAR()
    k.role = __builtin_amdgcn_readfirstlane(*(volatile LAS int*)smem);
    RAWBAR()
    const int x = k.role >> 5, i = k.role & 31, xl = x & 3;
    k.c = x >> 2;
    if (k.c) k.par = ~k.par;
    k.p0 = k.pg(0);
    RSTAMP(1)
    if (i < 8) { qk_role(p, k, smem, xl * 8 + i); return; }
    if (i < 16) { vo_role(p, k, smem, xl * 8 + i - 8); return; }
    ln_x_batch(p, k, ((i - 16) * 8 + x) * 4 + k.w);
    if (i < 21) { mred_role(p, k, smem, xl * 5 + i - 16); return; }
    if (i < 26) { tred_role(p, k, smem, xl * 5 + i - 21); return; }
    const int o = xl * 6 + i - 26;
    if (o < 12) up_role(p, k, smem, o);
    else if (o < 22) down_role(p, k, smem, o - 12);
    else row_role(p, k, smem, o - 22);
}

extern "C" void kernel_launch(void* const* d_in, const int* in_sizes, int n_in,
                              void* d_out, int out_size, void* d_ws, size_t ws_size,
                              hipStream_t stream) {
    Params p;
    p.x = (const u16*)d_in[0]; p.mem0 = (const u16*)d_in[1];
    p.Wq = (const u16*)d_in[2]; p.Wk = (const u16*)d_in[3]; p.Wv = (const u16*)d_in[4]; p.Wo = (const u16*)d_in[5];
    p.wg = (const u16*)d_in[8]; p.wu = (const u16*)d_in[9]; p.wd = (const u16*)d_in[10];
    p.out = (u16*)d_out; p.cm = (char*)d_ws;
    p.ws = (char*)(((uintptr_t)d_ws + CM_BYTES + ((1u << 21) - 1)) & ~(uintptr_t)((1u << 21) - 1));
    (void)hipFuncSetAttribute((const void*)mega_kernel, hipFuncAttributeMaxDynamicSharedMemorySize, LDS_BYTES);
    init_kernel<<<256, 256, 0, stream>>>(p);
    mega_kernel<<<256, 256, LDS_BYTES, stream>>>(p);
}
```

```cpp
#include <hip/hip_runtime.h>
#include <hip/hip_bf16.h>
#include <stdint.h>
#include <utility>

typedef unsigned short u16;
typedef __attribute__((ext_vector_type(16))) short bf16x16;
typedef __attribute__((ext_vector_type(8))) short bf16x8;
typedef __attribute__((ext_vector_type(4))) short bf16x4;
typedef __attribute__((ext_vector_type(8))) __bf16 hbf8;
typedef __attribute__((ext_vector_type(16))) __bf16 hbf16;
typedef __attribute__((ext_vector_type(4))) float f32x4;
typedef __attribute__((ext_vector_type(2))) float f32x2;
typedef __attribute__((ext_vector_type(8))) unsigned u32x8;
typedef __attribute__((ext_vector_type(4))) unsigned u32x4;
typedef __attribute__((ext_vector_type(2))) unsigned u32x2;
typedef __attribute__((ext_vector_type(2))) __bf16 bf16x2_t;
#define LAS __attribute__((address_space(3)))
#define GAS __attribute__((address_space(1)))
#define DEVI __device__ __forceinline__

#define PSLEEP 8
#define WD1 800
#define PACE 65
#define XPOS 62
#define YPOS 46
#define XPOSY 1
#define QDLY 300
#define CALR 3
#define SPG 3
#define DQ 12
#define DV 12
#define BSLP 6
#define SDLY 0
#define UPOS 79
#define RDLY 30
#define NOYST 0
#define NOTOK 0
#define NOYLD 0
#define GPOSY 62
#define GPOS 80

#define EXP(o) ((((unsigned)(o) >> 13) << 14) | ((unsigned)(o) & 8191u))
enum { C_XN = 0, C_Y = 1, C_X1 = 2, C_XB = 3, C_H = 4, C_X2 = 5, C_BC = 6 };
#define CNT(type, s) ((unsigned)((type) * 16384 + (s) * 128))
constexpr unsigned PR_B = EXP(65536), PR_SZ = EXP(12 * 8192);
constexpr unsigned DUMMY_B = EXP(0);
constexpr int PG_MEMF = 2;
#define YXR 8
constexpr int PG_YX = 3;
constexpr int PG_XNA = PG_YX + 2 * YXR;
constexpr int PG_X1 = PG_XNA + 4, PG_X2 = PG_X1 + 4;
constexpr int PG_H = PG_X2 + 4;
constexpr int PG_YM = PG_H + 8;
constexpr int NPAGES = PG_YM + 2;
static_assert(NPAGES <= 64, "map");
constexpr unsigned CM_TAB = 0, CM_TICKET = 1024, CM_TMO = 4096, CM_BYTES = 8192;
constexpr unsigned SPIN_CAP = 1u << 20;
#define AUX_SC1 16
#define LDS_BYTES (160 * 1024)

struct Params {
    const u16* x; const u16* mem0; const u16* Wq; const u16* Wk; const u16* Wv; const u16* Wo;
    const u16* wg; const u16* wu; const u16* wd; u16* out; char* cm; char* ws;
};
struct Ctx {
    char* ws; char* cm; __amdgpu_buffer_rsrc_t rs; unsigned long long par;
    int c, l, w, role;
    unsigned p0;
    DEVI unsigned pg(int page) const { return ((unsigned)page << 21) | (((unsigned)(par >> page) & 1u) << 13); }
    DEVI unsigned pg0(int page) const { return pg(page) ^ ((unsigned)c << 13); }
    DEVI unsigned seg(int page, int s) const { return pg(page + (s >> 4)) + (unsigned)(s & 15) * 131072u; }
    DEVI unsigned seg0(int page, int s) const { return seg(page, s) ^ ((unsigned)c << 13); }
    DEVI unsigned hseg(int s) const { return pg(PG_H + (s >> 3)) + (unsigned)(s & 7) * 262144u; }
    DEVI unsigned yx(int sg, int wq) const { return pg(PG_YX + (sg % YXR) * 2 + (wq >> 1)) + (unsigned)(wq & 1) * 1048576u; }
    DEVI unsigned ym(int s) const { return pg(PG_YM + ((s >> 1) & 1)) + (unsigned)(s & 1) * EXP(327680); }
    DEVI unsigned memf(int s) const { return pg(PG_MEMF) + (unsigned)s * 32768u; }
    DEVI char* cnt(int type, int s) const { return ws + (p0 + CNT(type, s)); }
    DEVI char* cnt0(int type, int s) const { return ws + ((p0 ^ ((unsigned)c << 13)) + CNT(type, s)); }
};

DEVI unsigned cvtpk(float a, float b) { return __builtin_bit_cast(unsigned, __builtin_convertvector((f32x2){a, b}, bf16x2_t)); }
DEVI unsigned packs(float a, float b) { return min(cvtpk(a, b), 0xFFFFFFFEu); }
DEVI float blo(unsigned x) { return __uint_as_float(x << 16); }
DEVI float bhi(unsigned x) { return __uint_as_float(x & 0xFFFF0000u); }
DEVI float bfr(float f) { return __uint_as_float(cvtpk(0.f, f)); }
DEVI u32x4 ld128(__amdgpu_buffer_rsrc_t rs, unsigned v, unsigned s) { return __builtin_amdgcn_raw_buffer_load_b128(rs, (int)v, (int)s, AUX_SC1); }
DEVI u32x2 ld64(__amdgpu_buffer_rsrc_t rs, unsigned v, unsigned s) { return __builtin_amdgcn_raw_buffer_load_b64(rs, (int)v, (int)s, AUX_SC1); }
DEVI unsigned ld32(__amdgpu_buffer_rsrc_t rs, unsigned v, unsigned s) { return __builtin_amdgcn_raw_buffer_load_b32(rs, (int)v, (int)s, AUX_SC1); }
DEVI void st128(__amdgpu_buffer_rsrc_t rs, unsigned v, unsigned s, u32x4 x) {
    __builtin_amdgcn_raw_buffer_store_b128(x, rs, (int)v, (int)s, AUX_SC1);
    asm volatile("s_nop 1" :: "v"(x) : "memory");
}
DEVI void st64(__amdgpu_buffer_rsrc_t rs, unsigned v, unsigned s, u32x2 x) { __builtin_amdgcn_raw_buffer_store_b64(x, rs, (int)v, (int)s, AUX_SC1); }
DEVI void st32(__amdgpu_buffer_rsrc_t rs, unsigned v, unsigned s, unsigned x) { __builtin_amdgcn_raw_buffer_store_b32(x, rs, (int)v, (int)s, AUX_SC1); }
#define RAWBAR() { __builtin_amdgcn_s_waitcnt(0xc07f); __builtin_amdgcn_s_barrier(); }
#define DRAIN() asm volatile("s_waitcnt vmcnt(0)" ::: "memory")
#define SCHED0() __builtin_amdgcn_sched_barrier(0)
#define OOB 0x80000000u
DEVI int opq(int x) { asm volatile("" : "+v"(x)); return x; }
#define LANE const int l = opq(k.l), g = l >> 4, n = l & 15; (void)g; (void)n;
DEVI unsigned umax3(unsigned a, unsigned b, unsigned c) { return max(max(a, b), c); }
DEVI unsigned umax4(u32x4 v) { return max(umax3(v[0], v[1], v[2]), v[3]); }
DEVI unsigned rclk() { return (unsigned)__builtin_amdgcn_s_memrealtime(); }
DEVI unsigned long long rt_async() { unsigned long long t; asm volatile("s_memrealtime %0" : "=s"(t)); return t; }
DEVI void lgkm0(unsigned long long& a, unsigned long long& b) { asm volatile("s_waitcnt lgkmcnt(0)" : "+s"(a), "+s"(b)); }
DEVI void sleep_ticks(int d) { unsigned target = rclk() + d; while ((int)(rclk() - target) < 0) __builtin_amdgcn_s_sleep(4); }
DEVI float xor16max(float x) { auto r = __builtin_amdgcn_permlane16_swap(__float_as_uint(x), __float_as_uint(x), false, false); return __builtin_fmaxf(__uint_as_float(r[0]), __uint_as_float(r[1])); }
DEVI float xor32max(float x) { auto r = __builtin_amdgcn_permlane32_swap(__float_as_uint(x), __float_as_uint(x), false, false); return __builtin_fmaxf(__uint_as_float(r[0]), __uint_as_float(r[1])); }
DEVI float xor16add(float x) { auto r = __builtin_amdgcn_permlane16_swap(__float_as_uint(x), __float_as_uint(x), false, false); return __uint_as_float(r[0]) + __uint_as_float(r[1]); }
DEVI float xor32add(float x) { auto r = __builtin_amdgcn_permlane32_swap(__float_as_uint(x), __float_as_uint(x), false, false); return __uint_as_float(r[0]) + __uint_as_float(r[1]); }
DEVI float fold32(float a, float b) { auto r = __builtin_amdgcn_permlane32_swap(__float_as_uint(a), __float_as_uint(b), false, false); return __uint_as_float(r[0]) + __uint_as_float(r[1]); }
#define ROR16(x, n) __builtin_bit_cast(float, __builtin_amdgcn_update_dpp(0, __builtin_bit_cast(int, (x)), 0x120 + (n), 0xf, 0xf, false))
DEVI float row16sum(float x) { x += ROR16(x, 8); x += ROR16(x, 4); x += ROR16(x, 2); x += ROR16(x, 1); return x; }
DEVI unsigned z0() { unsigned z; asm volatile("v_mov_b32 %0, 0" : "=v"(z)); return z; }
DEVI void cnt_add(char* p) { __hip_atomic_fetch_add((GAS unsigned*)(p + z0()), z0() + 1u, __ATOMIC_RELAXED, __HIP_MEMORY_SCOPE_AGENT); }
DEVI unsigned cnt_get(char* p) { return __hip_atomic_load((GAS unsigned*)(p + z0()), __ATOMIC_RELAXED, __HIP_MEMORY_SCOPE_AGENT) + 1u; }
DEVI bool spin_fail(const Ctx& k, unsigned spins) {
    if (spins > SPIN_CAP || ((spins & 255u) == 255u && __builtin_amdgcn_readfirstlane(cnt_get(k.cm + CM_TMO)) != 0)) { cnt_add(k.cm + CM_TMO); return true; }
    return false;
}
DEVI void cnt_wait(const Ctx& k, char* c, unsigned need) {
_Pragma("unroll 1")
    for (unsigned spins = 0; (unsigned)__builtin_amdgcn_readfirstlane(cnt_get(c)) < need; ++spins) {
        __builtin_amdgcn_s_sleep(PSLEEP);
        if (spin_fail(k, spins)) break;
    }
    __builtin_amdgcn_fence(__ATOMIC_ACQUIRE, "wavefront");
}
DEVI unsigned xn_get(const Ctx& k, unsigned l16, int s) { return ld32(k.rs, l16 & (k.c ? 128u : 0u), k.p0 + CNT(C_XN, k.c ? (s & 1) * 2 : s & 63)); }
DEVI bool xn_has(const Ctx& k, unsigned v, int s) { return __all((int)v >= (k.c ? s : 7)); }
DEVI void xn_wait(const Ctx& k, unsigned l16, int s) {
_Pragma("unroll 1")
    for (unsigned spins = 0; !xn_has(k, xn_get(k, l16, s), s); ++spins) {
        asm volatile("" ::: "memory");
        __builtin_amdgcn_s_sleep(2);
        if (spin_fail(k, spins)) break;
    }
    __builtin_amdgcn_fence(__ATOMIC_ACQUIRE, "wavefront");
}
template <int N> DEVI void prog_wait(const Ctx& k, unsigned a, int s) {
_Pragma("unroll 1")
    for (unsigned spins = 0; !__all((int)ld32(k.rs, min(k.l, N - 1) * 128, a) >= s); ++spins) {
        asm volatile("" ::: "memory");
        __builtin_amdgcn_s_sleep(1);
        if (spin_fail(k, spins)) break;
    }
    __builtin_amdgcn_fence(__ATOMIC_ACQUIRE, "wavefront");
}
DEVI void dma1(const void* sbase, unsigned voff, unsigned lds) {
    asm volatile("s_mov_b32 m0, %2\n\ts_nop 0\n\tglobal_load_lds_dwordx4 %0, %1" :: "v"(voff), "s"(sbase), "s"(lds) : "memory");
}
DEVI void set_m0(unsigned lds) { asm volatile("s_mov_b32 m0, %0" :: "s"(lds) : "memory"); }
template <int OFF> DEVI void dmab(__amdgpu_buffer_rsrc_t rs, unsigned voff, unsigned soff) {
    asm volatile("buffer_load_dwordx4 %0, %1, %2 offen offset:%3 sc1 lds" :: "v"(voff), "s"(rs), "s"(soff), "n"(OFF) : "memory");
}
#define PIN(x) asm volatile("" : "+v"(x))
#define PINS(x) asm volatile("" : "+s"(x))
DEVI float csel(float a, float b, unsigned long long m) { float d; asm volatile("v_cndmask_b32_e64 %0, %1, %2, %3" : "=v"(d) : "v"(a), "v"(b), "s"(m)); return d; }
#define LO8(x) __builtin_shufflevector(x, x, 0, 1, 2, 3, 4, 5, 6, 7)
#define HI8(x) __builtin_shufflevector(x, x, 8, 9, 10, 11, 12, 13, 14, 15)
#define CAT8(a, b) __builtin_shufflevector(a, b, 0, 1, 2, 3, 4, 5, 6, 7, 8, 9, 10, 11, 12, 13, 14, 15)
template <bool AG> DEVI void smf(f32x4& c, const bf16x8& a, const bf16x16& w, unsigned ix) {
    if constexpr (AG) asm volatile("v_smfmac_f32_16x16x64_bf16 %0, %1, %2, %3" : "+v"(c) : "v"(a), "a"(w), "v"(ix));
    else asm volatile("v_smfmac_f32_16x16x64_bf16 %0, %1, %2, %3" : "+v"(c) : "v"(a), "v"(w), "v"(ix));
}
template <bool AG, bool FIRST> DEVI void mfa(f32x4& c, const bf16x8& w, const bf16x8& x) {
    if constexpr (AG && FIRST) asm volatile("v_mfma_f32_16x16x32_bf16 %0, %1, %2, 0" : "=&v"(c) : "a"(w), "v"(x));
    else if constexpr (AG) asm volatile("v_mfma_f32_16x16x32_bf16 %0, %1, %2, %0" : "+v"(c) : "a"(w), "v"(x));
    else if constexpr (FIRST) asm volatile("v_mfma_f32_16x16x32_bf16 %0, %1, %2, 0" : "=&v"(c) : "v"(w), "v"(x));
    else asm volatile("v_mfma_f32_16x16x32_bf16 %0, %1, %2, %0" : "+v"(c) : "v"(w), "v"(x));
}
template <bool AG, bool FIRST> DEVI void mfb(f32x4& c, const bf16x8& x, const bf16x8& w) {
    if constexpr (AG && FIRST) asm volatile("v_mfma_f32_16x16x32_bf16 %0, %1, %2, 0" : "=&v"(c) : "v"(x), "a"(w));
    else if constexpr (AG) asm volatile("v_mfma_f32_16x16x32_bf16 %0, %1, %2, %0" : "+v"(c) : "v"(x), "a"(w));
    else if constexpr (FIRST) asm volatile("v_mfma_f32_16x16x32_bf16 %0, %1, %2, 0" : "=&v"(c) : "v"(x), "v"(w));
    else asm volatile("v_mfma_f32_16x16x32_bf16 %0, %1, %2, %0" : "+v"(c) : "v"(x), "v"(w));
}
DEVI f32x4 mf(const bf16x8& a, const bf16x8& b, const f32x4& c) { return __builtin_amdgcn_mfma_f32_16x16x32_bf16(__builtin_bit_cast(hbf8, a), __builtin_bit_cast(hbf8, b), c, 0, 0, 0); }
template <int N, class F> DEVI void sfor(F f) { [&]<int... I>(std::integer_sequence<int, I...>) { (f(std::integral_constant<int, I>{}), ...); }(std::make_integer_sequence<int, N>{}); }
DEVI bf16x8 lds128(const char* p) { return *(const bf16x8*)p; }
DEVI bf16x4 ldstr(const char* p) { return __builtin_amdgcn_ds_read_tr16_b64_v4i16((LAS bf16x4*)p); }
DEVI bf16x8 ldstr2(const char* p, int d) { bf16x4 a = ldstr(p), b = ldstr(p + d); return __builtin_shufflevector(a, b, 0, 1, 2, 3, 4, 5, 6, 7); }

#define STAMP(s, k)
#define QSTAMP(f, i)
#define STOP(s)
#define PFLUSH(s)
#define PROF_DECL
#define RSTAMP(i)
#define PCOUNT(i)
#define PROF_OUT

template <int NT> DEVI void wp_issue(const u16* W, int ld, unsigned slot, int l) {
#pragma unroll
    for (int i = 0; i < NT; ++i) {
        const int id = i * 64 + l, row = id / (2 * NT), cc = id % (2 * NT);
        dma1(W, (unsigned)(row * ld + cc * 8) * 2u, slot + i * 1024);
    }
}
template <int NT, bool STD> DEVI bf16x8 wp_read(const char* slot, int T, int l) {
    const int row = (STD ? 8 : 4) * (l >> 4) + ((l >> 2) & 3);
    return ldstr2(slot + row * (NT * 32) + T * 32 + (l & 3) * 8, (STD ? 4 : 16) * NT * 32);
}
template <int NU, int NI, int D, class ISSUE, class READ> DEVI void wstream(char* ring, ISSUE issue, READ read) {
    const unsigned base = (unsigned)(size_t)(LAS char*)ring;
    sfor<D>([&](auto U) { constexpr int u = decltype(U)::value; if constexpr (u < NU) issue(U, base + u * NI * 1024); });
    sfor<NU>([&](auto U) {
        constexpr int u = decltype(U)::value, left = (NU - u < D ? NU - u : D) - 1;
        asm volatile("s_waitcnt vmcnt(%0)" :: "n"(left * NI) : "memory");
        read(U, ring + (u % D) * NI * 1024);
        __builtin_amdgcn_s_waitcnt(0xc07f);
        asm volatile("" ::: "memory");
        if constexpr (u + D < NU) issue(std::integral_constant<int, u + D>{}, base + (u % D) * NI * 1024);
    });
}

constexpr int L_XN = 0;
constexpr int L_K = 40960;
constexpr int L_MEM = L_K + 12288;
constexpr int L_QM = L_MEM + 10240;
constexpr int L_Q = L_QM + 12288;
constexpr int L_W = L_Q + 12288;
#define IC(x) std::integral_constant<int, (x)>{}
#define CV(X) decltype(X)::value
DEVI bf16x8 lr128(unsigned a) { return *(const LAS bf16x8*)(size_t)a; }
DEVI bf16x4 lrtr(unsigned a) { return __builtin_amdgcn_ds_read_tr16_b64_v4i16((LAS bf16x4*)(size_t)a); }
#define CAT4(a, b) __builtin_shufflevector(a, b, 0, 1, 2, 3, 4, 5, 6, 7)
DEVI void lw128(unsigned a, u32x4 v) { *(LAS u32x4*)(size_t)a = v; }
DEVI void lw64(unsigned a, u32x2 v) { *(LAS u32x2*)(size_t)a = v; }
DEVI void lw32(unsigned a, unsigned v) { *(LAS unsigned*)(size_t)a = v; }
template <int N> DEVI void bar_lgkm() { asm volatile("s_waitcnt lgkmcnt(%0)\n\ts_barrier" :: "n"(N) : "memory"); }
DEVI void atom1(__amdgpu_buffer_rsrc_t rs, unsigned voff, unsigned soff) {
    unsigned one; asm volatile("v_mov_b32 %0, 1\n\tbuffer_atomic_add %0, %1, %2, %3 offen" : "=&v"(one) : "v"(voff), "s"(rs), "s"(soff) : "memory");
}
DEVI unsigned vadd(unsigned v, unsigned sc) { unsigned d; asm volatile("v_add_u32 %0, %1, %2" : "=v"(d) : "s"(sc), "v"(v)); return d; }
DEVI float vzero() { float z; asm volatile("v_mov_b32 %0, 0" : "=v"(z)); return z; }
template <int B, int I> DEVI void xn_op(const Ctx& k, unsigned v, unsigned src, unsigned dst) {
    if constexpr (I == 0) set_m0(dst + B * 8192);
    else if constexpr (I == 1) dmab<0>(k.rs, v, src + B * 16384);
    else dmab<1024>(k.rs, v, src + B * 16384);
}
DEVI void xn_all(const Ctx& k, unsigned v, unsigned src, unsigned dst) {
    sfor<5>([&](auto B) { xn_op<CV(B), 0>(k, v, src, dst); asm volatile("s_nop 0"); xn_op<CV(B), 1>(k, v, src, dst); xn_op<CV(B), 2>(k, v, src, dst); });
}
DEVI u32x4 ald128(__amdgpu_buffer_rsrc_t rs, unsigned v, unsigned s) { u32x4 d; asm volatile("buffer_load_dwordx4 %0, %1, %2, %3 offen sc1" : "=v"(d) : "v"(v), "s"(rs), "s"(s) : "memory"); return d; }
struct Gather {
    u32x4 p0, p1, v0, v1;
    unsigned gw, mx, a;
    unsigned long long bad;
    template <int I> DEVI void op(const Ctx& k, unsigned l16, int s) {
        if constexpr (I == 0) { a = k.memf(s); PINS(a); }
        if constexpr (I == 1) p0 = ald128(k.rs, l16, a);
        if constexpr (I == 2) v0 = ald128(k.rs, l16, a + 2048 + k.w * 1024);
        if constexpr (I == 3) v1 = ald128(k.rs, l16, a + 6144 + k.w * 1024 + (k.w >> 1) * 8192);
        if constexpr (I == 4) p1 = ald128(k.rs, l16, a + 1024);
        if constexpr (I == 5) gw = vadd(l16, k.w * 1024);
    }
    template <int N> DEVI void wait() { asm volatile("s_waitcnt vmcnt(%4)" : "+v"(p0), "+v"(p1), "+v"(v0), "+v"(v1) : "n"(N) : "memory"); }
    template <int I> DEVI void fin() {
#define MX2(A, B, C, D) asm volatile("v_max3_u32 %0, %0, %1, %2\n\tv_max3_u32 %0, %0, %3, %4" : "+v"(mx) : "v"(A), "v"(B), "v"(C), "v"(D))
        if constexpr (I == 0) { lw128(gw + L_MEM + 2048, v0); asm volatile("v_max3_u32 %0, %1, %2, %3\n\tv_max3_u32 %0, %0, %4, %5" : "=&v"(mx) : "v"(p0[0]), "v"(p0[1]), "v"(p0[2]), "v"(p0[3]), "v"(p1[0])); }
        if constexpr (I == 1) { lw128(gw + L_MEM + 6144, v1); MX2(p1[1], p1[2], p1[3], v0[0]); }
        if constexpr (I == 2) MX2(v0[1], v0[2], v0[3], v1[0]);
        if constexpr (I == 3) MX2(v1[1], v1[2], v1[3], v1[3]);
        if constexpr (I == 4) asm volatile("v_cmp_eq_u32_e64 %0, -1, %1" : "=s"(bad) : "v"(mx));
    }
    DEVI void again(const Ctx& k, unsigned l16) {
        asm volatile("buffer_load_dwordx4 %0, %4, %5, %6 offen sc1\n\tbuffer_load_dwordx4 %1, %4, %5, %6 offen offset:1024 sc1\n\tbuffer_load_dwordx4 %2, %4, %5, %7 offen sc1\n\tbuffer_load_dwordx4 %3, %4, %5, %8 offen sc1\n\ts_waitcnt vmcnt(0)"
            : "+v"(p0), "+v"(p1), "+v"(v0), "+v"(v1) : "v"(l16), "s"(k.rs), "s"(a), "s"(a + 2048 + k.w * 1024), "s"(a + 6144 + k.w * 1024 + (k.w >> 1) * 8192) : "memory");
        fin<0>(); fin<1>(); fin<2>(); fin<3>(); fin<4>();
        asm volatile("s_nop 3");
    }
};
DEVI void clr2(f32x4& c) { f32x2 a, b; asm volatile("v_mov_b64 %0, 0\n\tv_mov_b64 %1, 0" : "=v"(a), "=v"(b)); c = __builtin_shufflevector(a, b, 0, 1, 2, 3); }
#define NOP7_3(a) asm volatile("s_nop 7" : "+v"(a[0]), "+v"(a[1]), "+v"(a[2]))

struct G3 {
    f32x4 a0[3], a1[3];
    bf16x8 x0[20], x1[20], wl[20];
    unsigned l16, lwv;
    template <int KS> DEVI void r0() { x0[KS] = lr128(l16 + L_XN + KS * 2048); }
    template <int KS> DEVI void r1() { x1[KS] = lr128(l16 + L_XN + KS * 2048 + 1024); }
    template <int KS> DEVI void rw() { if constexpr (KS >= 4) wl[KS] = lr128(lwv + (KS - 4) * 1024); }
    template <int ID0, bool WL, int ks, int m> DEVI void mm(const bf16x16 (&W)[52]) {
        constexpr int b = ks >> 1, t = m % 3, id = ID0 + t * 10 + b;
        f32x4& c = m < 3 ? a0[t] : a1[t];
        const bf16x8& x = m < 3 ? x0[ks] : x1[ks];
        if constexpr (ID0 == 30) {
            if constexpr (WL && t == 2 && b >= 2) mfb<false, false>(c, x, wl[ks]);
            else if constexpr (ks & 1) mfb<(id < 32), false>(c, x, HI8(W[id % 52]));
            else mfb<(id < 32), ks == 0>(c, x, LO8(W[id % 52]));
        } else {
            if constexpr (WL && t == 2 && b >= 2) mfa<false, false>(c, wl[ks], x);
            else if constexpr (ks & 1) mfa<(id < 32), false>(c, HI8(W[id % 52]), x);
            else mfa<(id < 32), ks == 0>(c, LO8(W[id % 52]), x);
        }
        SCHED0();
    }
    template <int ID0, bool WL, int K0, int K1, int PF, bool H6 = false, class HOOK> DEVI void run(const bf16x16 (&W)[52], HOOK hook) {
        sfor<K1 - K0>([&](auto KK) {
            constexpr int kk = CV(KK), ks = K0 + kk;
            sfor<6>([&](auto M) {
                constexpr int m = CV(M);
                mm<ID0, WL, ks, m>(W);
                if constexpr (ks + 2 < PF) {
                    if constexpr (m == 0) r0<(ks + 2) % 20>();
                    if constexpr (m == 1) r1<(ks + 2) % 20>();
                    if constexpr (m == 3 && WL) rw<(ks + 2) % 20>();
                }
                if constexpr (H6) hook(IC(kk * 6 + m));
                else { hook(IC(kk * 7 + (m < 5 ? m : 6))); if constexpr (m == 4) hook(IC(kk * 7 + 5)); }
                SCHED0();
            });
        });
    }
    u32x2 o;
    template <int I, int OFF> DEVI void out(unsigned d0, unsigned d1) {
        if constexpr (I >= 0 && I < 18) {
            constexpr int t = I / 6, rt = (I / 3) & 1;
            const f32x4& a = rt ? a1[t] : a0[t];
            if constexpr (I % 3 == 0) { o[0] = cvtpk(a[0], a[1]); PIN(o[0]); }
            else if constexpr (I % 3 == 1) { o[1] = cvtpk(a[2], a[3]); PIN(o[1]); }
            else if constexpr (OFF > 0) lw64(d0 + (OFF - 1) + rt * 6144 + t * 1024, o);
            else lw64((t == 1 ? d1 : d0) + OFF + rt * 6144 + (t == 2 ? 1024 : 0), o);
        }
    }
};
#define SPIX(l) (((l) & 2) ? 0xEEEEu : 0x4444u)
template <int I> DEVI void fold_op(f32x4& a, unsigned& o) {
    if constexpr (I == 0) asm volatile("v_add_f32 %0, %0, %1" : "+v"(a[0]) : "v"(a[2]));
    if constexpr (I == 1) asm volatile("v_add_f32 %0, %0, %1" : "+v"(a[1]) : "v"(a[3]));
    if constexpr (I == 2) asm volatile("v_cvt_pk_bf16_f32 %0, %1, %2" : "=v"(o) : "v"(a[0]), "v"(a[1]));
}

#define ANNOUNCE(s, now, d) (((unsigned)(s) << 16) | (((unsigned)(now) + (d)) & 0xFFFFu))

struct Soft {
    unsigned long long mA, mY, mC[4], mM, mD;
    unsigned v0, v1;
    float a[6], m, ml, M, t, u, sc; unsigned pk[3], msk;
    template <int I> DEVI void op(const Ctx& k, unsigned a0, unsigned a1, unsigned ph) {
#define A3(o) "+v"(a[o]), "+v"(a[o + 1]), "+v"(a[o + 2])
#define FMA(x) "v_fmamk_f32 " x ", " x ", 0x3dd53b95, "
        if constexpr (I == 5) asm volatile("v_mov_b32 %0, 0xf1800000" : "=v"(t));
        if constexpr (I == 6) asm volatile("v_cndmask_b32_e64 %0, %3, %0, %4\n\tv_cndmask_b32_e64 %1, %3, %1, %5\n\tv_cndmask_b32_e64 %2, %3, %2, %6" : A3(0) : "v"(t), "s"(mC[0]), "s"(mC[1]), "s"(mC[2]));
        if constexpr (I == 7) asm volatile("v_cndmask_b32_e64 %0, %3, %0, %4\n\tv_cndmask_b32_e64 %1, %3, %1, %5\n\tv_cndmask_b32_e64 %2, %3, %2, %5" : A3(3) : "v"(t), "s"(mC[3]), "s"(mM));
        if constexpr (I == 8) asm volatile("v_max3_f32 %0, %1, %2, %3\n\tv_max3_f32 %0, %0, %4, %5\n\tv_max_f32 %0, %0, %6" : "=&v"(m) : "v"(a[0]), "v"(a[1]), "v"(a[2]), "v"(a[3]), "v"(a[4]), "v"(a[5]));
        if constexpr (I == 9) asm volatile("v_mul_f32 %0, 0xbdd53b95, %3\n\t" FMA("%1") "%0\n\t" FMA("%2") "%0" : "=&v"(ml), "+v"(a[0]), "+v"(a[1]) : "v"(m));
        if constexpr (I == 10) asm volatile("v_max_f32_dpp %0, %3, %3 row_ror:8 row_mask:0xf bank_mask:0xf\n\t" FMA("%1") "%4\n\tv_cndmask_b32_e64 %2, %3, %0, %5" : "=&v"(t), "+v"(a[2]), "=&v"(M) : "v"(m), "v"(ml), "s"(mD));
        if constexpr (I == 11) asm volatile("v_mov_b32 %0, %3\n\tv_exp_f32 %1, %1\n\t" FMA("%2") "%4" : "=&v"(u), "+v"(a[0]), "+v"(a[3]) : "v"(M), "v"(ml));
        if constexpr (I == 12) asm volatile("v_permlane16_swap_b32 %0, %1\n\t" FMA("%2") "%3" : "+v"(M), "+v"(u), "+v"(a[4]) : "v"(ml));
        if constexpr (I == 13) asm volatile("v_max_f32 %0, %0, %1\n\tv_mov_b32 %1, %0\n\tv_exp_f32 %2, %2" : "+v"(M), "+v"(u), "+v"(a[1]));
        if constexpr (I == 14) asm volatile(FMA("%2") "%3\n\tv_permlane32_swap_b32 %0, %1" : "+v"(M), "+v"(u), "+v"(a[5]) : "v"(ml));
        if constexpr (I == 15) asm volatile("v_max_f32 %0, %0, %1\n\tv_exp_f32 %2, %2" : "+v"(M), "+v"(u), "+v"(a[2]));
        if constexpr (I == 16) asm volatile("v_sub_f32 %0, %2, %0\n\tv_mul_f32 %0, 0x3dd53b95, %0\n\tv_exp_f32 %1, %1" : "+v"(M), "+v"(a[3]) : "v"(m));
        if constexpr (I == 17) asm volatile("v_exp_f32 %0, %2\n\tv_exp_f32 %1, %1" : "=&v"(sc), "+v"(a[4]) : "v"(M));
        if constexpr (I == 18) asm volatile("v_exp_f32 %2, %2\n\tv_add_f32 %0, %3, %4\n\tv_add_f32 %1, %5, %6" : "=&v"(u), "=&v"(t), "+v"(a[5]) : "v"(a[0]), "v"(a[1]), "v"(a[2]), "v"(a[3]));
        if constexpr (I == 19) asm volatile("v_add_f32 %0, %0, %2\n\tv_add_f32 %1, %1, %3\n\tv_add_f32 %0, %0, %1" : "+v"(u), "+v"(t) : "v"(a[4]), "v"(a[5]));
        if constexpr (I == 20) asm volatile("v_mul_f32 %0, %0, %2\n\tv_mov_b32 %1, 0x7fff7fff" : "+v"(u), "=v"(msk) : "v"(sc));
        if constexpr (I == 21) asm volatile("v_add_f32_dpp %1, %0, %0 row_ror:8 row_mask:0xf bank_mask:0xf\n\tv_cndmask_b32_e64 %0, %0, %1, %2\n\tv_mov_b32 %1, %0" : "+v"(u), "=&v"(t) : "s"(mD));
        if constexpr (I == 22) asm volatile("s_nop 0\n\tv_permlane16_swap_b32 %0, %1\n\tv_add_f32 %0, %0, %1\n\tv_mov_b32 %1, %0" : "+v"(u), "+v"(t));
        if constexpr (I == 23) asm volatile("s_nop 0\n\tv_permlane32_swap_b32 %0, %1\n\tv_add_f32 %0, %0, %1\n\tv_rcp_f32 %0, %0" : "+v"(u), "+v"(t));
        if constexpr (I == 24) asm volatile("v_mul_f32 %0, %0, %3\n\tv_mul_f32 %1, %1, %0\n\tv_mul_f32 %2, %2, %0" : "+v"(u), "+v"(a[0]), "+v"(a[1]) : "v"(sc));
        if constexpr (I == 25) asm volatile("v_mul_f32 %0, %0, %3\n\tv_mul_f32 %1, %1, %3\n\tv_mul_f32 %2, %2, %3" : A3(2) : "v"(u));
        if constexpr (I == 26) asm volatile("v_mul_f32 %2, %2, %3\n\tv_cvt_pk_bf16_f32 %0, %4, %5\n\tv_cvt_pk_bf16_f32 %1, %6, %7" : "=&v"(pk[0]), "=&v"(pk[1]), "+v"(a[5]) : "v"(u), "v"(a[0]), "v"(a[1]), "v"(a[2]), "v"(a[3]));
        if constexpr (I == 27) asm volatile("v_cvt_pk_bf16_f32 %2, %3, %4\n\tv_bfi_b32 %0, %5, %0, %6\n\tv_bfi_b32 %1, %5, %1, %6" : "+v"(pk[0]), "+v"(pk[1]), "=&v"(pk[2]) : "v"(a[4]), "v"(a[5]), "v"(msk), "s"(ph));
        if constexpr (I == 28) { asm volatile("v_bfi_b32 %0, %1, %0, %2" : "+v"(pk[2]) : "v"(msk), "s"(ph)); st64(k.rs, v0, a0, (u32x2){pk[0], pk[1]}); }
        if constexpr (I == 29) st32(k.rs, v1, a1, pk[2]);
    }
};
#define QIDX(t, b) ((t) * 10 + (b))
DEVI void qk_role(const Params& p, const Ctx& k, char* smem, int h) {
    PROF_DECL
    const int w = k.w;
    sleep_ticks(k.c ? WD1 + QDLY : QDLY);
    bf16x16 W[52];
    {
        const int l = k.l;
        bf16x8 lo[6], wt[16];
        const int col = h * 192 + w * 48;
        wstream<20, 6, 6>(smem + w * 36864,
            [&](auto U, unsigned sl) { constexpr int u = CV(U);
                wp_issue<3>(p.Wk + (size_t)u * 32 * 6144 + col, 6144, sl, l); wp_issue<3>(p.Wq + (size_t)u * 32 * 6144 + col, 6144, sl + 3072, l); },
            [&](auto U, char* sp) { constexpr int u = CV(U), b = u >> 1;
#pragma unroll
                for (int t = 0; t < 6; ++t) {
                    bf16x8 f = wp_read<3, true>(sp + (t / 3) * 3072, t % 3, l);
                    if (t == 5 && b >= 2) wt[(u - 4) & 15] = f;
                    else if (u & 1) W[QIDX(t, b) % 52] = CAT8(lo[t], f);
                    else lo[t] = f;
                } });
        RAWBAR()
#pragma unroll
        for (int i = 0; i < 16; ++i) *(bf16x8*)(smem + L_W + w * 16384 + i * 1024 + l * 16) = wt[i];
    }
    RSTAMP(2)
    Gather G;
    Soft S;
    const int hw = h >> 3, hh = h & 7;
    unsigned l16, ix, dqm, dq, trp, qp, vz;
    {
        const int l = k.l, g = l >> 4, n = l & 15, r = n & 7, u = w & 1;
        const bool hf = n >= 8, dup = w != 1;
        S.mA = __ballot(dup && hf); S.mD = __ballot(dup);
        const int lim = w == 1 ? n - 4 * g : (w == 0 || !hf) ? 64 : 8 * u + r - 4 * g;
#pragma unroll
        for (int i = 0; i < 4; ++i) S.mC[i] = __ballot(i <= lim);
        const bool okm = !(dup && hf);
        S.mY = __ballot((dup && hf) == (w != 0)); S.mM = __ballot(okm);
        S.v0 = w == 0 ? hh * 512 + ((g >> 1) * 16 + (r >> 1) * 4 + hf * 2 + (r & 1)) * 16 + (g & 1) * 8 : w == 1 ? hh * 512 + l * 8 : hh * 1024 + (g * 16 + 8 * u + r) * 16 + hf * 8;
        S.v1 = !okm ? OOB : w == 0 ? 4096 + hh * 128 + r * 16 + g * 4 : (w == 1 ? 4096 + n * 8 : 6144 + (8 * u + r) * 8) + hh * 256 + (g >> 1) * 128 + (g & 1) * 4;
        l16 = l * 16; ix = SPIX(l);
        dqm = L_QM + (w >> 1) * 3072 + u * 512 + (g >> 1) * 128 + n * 8 + (g & 1) * 4;
        dq = L_QM + 6144 + (w >> 1) * 3072 + u * 512 + g * 128 + ((n * 8) ^ ((g >> 1) * 64));
        trp = L_QM + 6144 + (l & 3) * 128 + (((l >> 2) * 8) ^ ((l & 2) * 32));
        qp = w == 1 ? trp + 6144 : (w == 0 ? L_QM : L_Q + 6144 + u * 256) + (l & 1) * 128 + (((l >> 2) * 8) ^ (w == 3 ? 64 : 0));
        vz = l == 0 ? 0u : OOB;
        PIN(S.v0); PIN(S.v1); PIN(l16); PIN(ix); PIN(dqm); PIN(dq); PIN(trp); PIN(qp); PIN(vz);
    }
    const unsigned po0 = k.p0 + PR_B + hw * 49152 + (w == 0 ? 0 : w == 1 ? 32768 : 16384), po1 = k.p0 + PR_B + hw * 49152 + (w == 0 ? 0 : 32768);
    const unsigned pdum = k.pg(1) + DUMMY_B, xdst = L_XN + w * 2048, pbc = w == 0 ? k.p0 + CNT(C_BC, hw * 16 + hh) : pdum + 65536 + h * 512 + w * 128;
    bool xn_ok = false;
    unsigned long long now = 0, done = 0;
    unsigned xsrc = k.seg(PG_XNA, 0) + w * 2048, xv = OOB, a0 = pdum, a1 = pdum, ph = 0;
    G3 A, B;
    A.l16 = l16; B.l16 = l16; B.lwv = vadd(l16, L_W + w * 16384);
#pragma unroll
    for (int t = 0; t < 3; ++t) { B.a0[t] = (f32x4){0.f, 0.f, 0.f, 0.f}; B.a1[t] = B.a0[t]; }
    f32x4 ac[6];
#pragma unroll
    for (int t = 0; t < 6; ++t) clr2(ac[t]);
    sfor<6>([&](auto I) { G.op<CV(I)>(k, l16, 0); });
    for (int s = -1; s < 64; ++s) {
        if (!xn_ok && s < 63) {
            PCOUNT(1)
            xn_wait(k, l16, s + 1);
            sfor<4>([&](auto Bk) { xn_op<CV(Bk), 0>(k, l16, xsrc, xdst); asm volatile("s_nop 0"); xn_op<CV(Bk), 1>(k, l16, xsrc, xdst); xn_op<CV(Bk), 2>(k, l16, xsrc, xdst); });
            DRAIN();
            xv = l16;
        }
        bf16x8 k0[6], k1[6];
        {
            bf16x8 m[10], wa[10], wb[10];
            unsigned fo[6];
            G.wait<4>();
            STOP(s) STAMP(s, 0)
            sfor<6>([&](auto I) { constexpr int i = CV(I), t = (i + 5) % 6, id = QIDX(t, 0);
                smf<(id < 32)>(ac[t], __builtin_bit_cast(bf16x8, G.p0), W[id], ix);
                SCHED0();
                G.fin<i>();
                SCHED0(); });
            if (G.bad != 0) {
                unsigned spins = 0;
_Pragma("unroll 1")
                do { PCOUNT(0) G.again(k, l16); } while (G.bad != 0 && !spin_fail(k, spins++));
                STOP(s) STAMP(s, 0)
#pragma unroll
                for (int t = 0; t < 6; ++t) clr2(ac[t]);
                asm volatile("s_nop 1" : "+v"(ac[0]), "+v"(ac[1]), "+v"(ac[2]), "+v"(ac[3]), "+v"(ac[4]), "+v"(ac[5]));
                sfor<6>([&](auto I) { constexpr int t = (CV(I) + 5) % 6, id = QIDX(t, 0);
                    smf<(id < 32)>(ac[t], __builtin_bit_cast(bf16x8, G.p0), W[id], ix); });
            }
            bar_lgkm<0>();
            const unsigned then = (unsigned)now;
            unsigned an;
            now = rt_async();
            m[1] = __builtin_bit_cast(bf16x8, G.p1);
            m[2] = lr128(l16 + L_MEM + 2048);
            SCHED0();
            auto fin = [&](auto J) { constexpr int j = CV(J), t = 5 - j / 3;
                if constexpr (j >= 0 && j < 18) fold_op<j % 3>(ac[t], fo[t]); };
            auto put = [&](auto T) { constexpr int t = CV(T); if constexpr (t < 3) lw32(dq + t * 1024, fo[t]); else lw32(dqm + (t - 3) * 1024, fo[t]); };
            sfor<54>([&](auto I) {
                constexpr int i = CV(I) + 6, u = i - 42, b = i < 42 ? i / 6 : 7 + u % 3, t = b == 2 ? i % 6 : i < 42 ? (i + 5) % 6 : 5 - u / 3, id = QIDX(t, b);
                if constexpr (i == 44) lgkm0(now, done);
                if constexpr (t == 5 && b >= 2) smf<false>(ac[5], m[b], CAT8(wa[b], wb[b]), ix);
                else smf<(id < 32)>(ac[t], m[b], W[id % 52], ix);
                SCHED0();
                if constexpr (i < 36) {
                    constexpr int bb = i / 6 + 2;
                    if constexpr (i % 6 == 0) m[bb] = lr128(l16 + L_MEM + bb * 1024);
                    if constexpr (i % 6 == 3) wa[bb] = lr128(B.lwv + (bb - 2) * 2048);
                    if constexpr (i % 6 == 4) wb[bb] = lr128(B.lwv + (bb - 2) * 2048 + 1024);
                }
                if constexpr (i == 7) wa[2] = lr128(B.lwv);
                if constexpr (i == 8) wb[2] = lr128(B.lwv + 1024);
                if constexpr (i == 32) m[8] = lr128(l16 + L_MEM + 8192);
                if constexpr (i == 31) m[9] = lr128(l16 + L_MEM + 9216);
                if constexpr (i == 36) wa[8] = lr128(B.lwv + 12288);
                if constexpr (i == 37) wb[8] = lr128(B.lwv + 13312);
                if constexpr (i == 38) wa[9] = lr128(B.lwv + 14336);
                if constexpr (i == 39) wb[9] = lr128(B.lwv + 15360);
                if constexpr (i == 47) st32(k.rs, vz, pbc, fo[0]);
                B.out<i - 9, 6145>(dq, dq);
                if constexpr (i == 44) { an = min((unsigned)done - then, 200u) + DQ + (unsigned)now; PINS(an); }
                if constexpr (i == 45) { an = ANNOUNCE(s, an, 0); PINS(an); }
                if constexpr (i == 46) { fo[0] = an; PIN(fo[0]); }
                if constexpr (u >= 6 && u % 3 != 2) {
                    constexpr int r = (u / 3 - 2) * 2 + u % 3, f = (r + 6) / 2 % 4;
                    if constexpr (r < 2) A.r0<r>(); else if constexpr (r & 1) k1[f] = lr128(l16 + L_K + (6 + f) * 1024); else k0[f] = lr128(l16 + L_K + f * 1024);
                }
                if constexpr (u >= 8 && u % 3 == 2) put(IC(5 - (u - 8) / 3));
                if constexpr (u >= 5) fin(IC(u - 5));
                if constexpr (i == 6) { QSTAMP(1, 1) }
                if constexpr (i == 12) { QSTAMP(1, 2) }
                if constexpr (i == 41) { QSTAMP(1, 3) }
                if constexpr (i == 59) { QSTAMP(1, 4) }
                SCHED0();
            });
            sfor<9>([&](auto I) { constexpr int i = CV(I);
                A.mm<0, false, i / 3, i % 3>(W);
                if constexpr (i == 0) { A.r0<2>(); fin(IC(13)); }
                if constexpr (i == 1) { fin(IC(14)); put(IC(1)); }
                if constexpr (i == 2) { fin(IC(15)); fin(IC(16)); }
                if constexpr (i == 3) { fin(IC(17)); put(IC(0)); }
                if constexpr (i == 4) A.r0<3>();
                if constexpr (i == 5) A.r0<4>();
                if constexpr (i >= 6) xn_op<4, i % 3>(k, xv, xsrc, xdst);
                SCHED0(); });
        }
        asm volatile("s_waitcnt vmcnt(3) lgkmcnt(2)\n\ts_barrier" ::: "memory");
        QSTAMP(0, 1) QSTAMP(1, 5)
        f32x4 sc[3];
        {
            bf16x4 q[12], km[12];
            auto rd = [&](auto R) { constexpr int r = CV(R);
                constexpr int tab[34] = {0, 1, 2, 3, 12, 13, 4, 5, 14, 15, 6, 7, 24, 25, 16, 17, 30, 8, 9, 26, 27, 18, 19, 10, 11, 28, 29, 20, 21, 22, 23, 31, 32, 33};
                constexpr int c = tab[r % 34];
                if constexpr (r >= 34) { }
                else if constexpr (c < 12) q[c] = lrtr(qp + c * 512);
                else if constexpr (c < 24) km[c - 12] = lrtr(trp + (c - 12) * 512);
                else if constexpr (c < 30) { if constexpr (c & 1) k1[3 + (c - 24) / 2] = lr128(l16 + L_K + (9 + (c - 24) / 2) * 1024); else k0[3 + (c - 24) / 2] = lr128(l16 + L_K + (3 + (c - 24) / 2) * 1024); }
                else if constexpr (c == 30) A.r0<5>();
                else A.r1<(c - 31) % 20>(); };
            rd(IC(0)); rd(IC(1));
            SCHED0();
            sfor<27>([&](auto I) { constexpr int i = CV(I);
                constexpr int seq[27] = {-9, -10, -11, -12, -13, -14, 0, 1, 3, 4, 2, 6, 7, 5, 9, 10, 8, -15, 12, 13, 11, -16, 15, 16, 14, -17, 17};
                constexpr int c = seq[i], ks = (c < 0 ? -c : c) / 3, j = (c < 0 ? -c : c) % 3;
                if constexpr (c < 0) A.mm<0, false, ks, j>(W);
                else mfa<false, ks == 0>(sc[j], j == 0 ? k0[ks] : j == 1 ? k1[ks] : CAT4(km[2 * ks], km[2 * ks + 1]), CAT4(q[2 * ks], q[2 * ks + 1]));
                SCHED0();
                constexpr int r0 = 2 + i + (i + 2) / 3, r1 = 2 + (i + 1) + (i + 3) / 3;
                sfor<r1 - r0>([&](auto J) { rd(IC(r0 + CV(J))); });
                if constexpr (i == 25) S.op<5>(k, 0, 0, 0);
                SCHED0(); });
        }
        QSTAMP(0, 2)
        {
            unsigned xc, d0;
            float y0, y1;
            auto soft = [&](auto I) { constexpr int i = CV(I);
                if constexpr (i == 0) { asm volatile("" : "+v"(sc[0]), "+v"(sc[1])); y0 = csel(sc[0][2], sc[1][0], S.mY); y1 = csel(sc[0][3], sc[1][1], S.mY); S.a[0] = csel(sc[0][0], y0, S.mA); }
                if constexpr (i == 1) { S.a[1] = csel(sc[0][1], y1, S.mA); S.a[2] = csel(y0, sc[1][2], S.mA); S.a[3] = csel(y1, sc[1][3], S.mA); }
                if constexpr (i == 3) { asm volatile("" : "+v"(sc[2])); S.a[4] = sc[2][0]; S.a[5] = sc[2][1]; }
                if constexpr (i >= 2) S.op<i + 4>(k, a0, a1, ph);
                if constexpr (i == 26) done = rt_async(); };
            sfor<18>([&](auto I) { constexpr int i = CV(I);
                A.mm<0, false, i / 3, 3 + i % 3>(W);
                if constexpr (i == 2 || i == 5 || i == 8) A.r1<3 + i / 3>();
                if constexpr (i == 10) A.r0<6>();
                if constexpr (i == 12) A.r1<6>();
                if constexpr (i == 14) A.r0<7>();
                if constexpr (i == 17) A.r1<7>();
                soft(I);
                SCHED0(); });
            A.run<0, false, 6, 20, 20, true>(W, [&](auto I) { constexpr int i = CV(I);
                soft(IC(18 + i));
                if constexpr (i == 8) { QSTAMP(0, 3) }
                if constexpr (i == 40) asm volatile("s_waitcnt vmcnt(2)\n\ts_barrier" ::: "memory");
                if constexpr (i == 44) { a0 = po0 + ((s + 1) & 3) * PR_SZ; ph = ((s + 1) & 4) ? 0x80008000u : 0u; PINS(a0); PINS(ph); }
                if constexpr (i == 46) { a1 = a0 + (po1 - po0); PINS(a1); }
                if constexpr (i == XPOS) xc = xn_get(k, l16, s + 2);
                if constexpr (i == 81) { d0 = vadd(l16, L_K + (w >> 1) * 3072 + (w & 1) * 8); B.lwv = vadd(l16, L_W + w * 16384); }
                if constexpr (i == 72) B.r0<0>();
                if constexpr (i == 73) B.r1<0>();
                if constexpr (i == 78) B.r0<1>();
                if constexpr (i == 79) B.r1<1>(); });
            B.run<30, true, 0, 20, 20, true>(W, [&](auto I) { constexpr int i = CV(I);
                if constexpr (i < 36 && i % 6 != 0 && i % 6 != 1 && i % 6 != 3) A.out<(i / 6) * 3 + (i % 6) / 2 - 1 + (i % 6) / 5, 1>(d0, d0);
                if constexpr (i == 38) {
                    xn_ok = s < 62 && xn_has(k, xc, s + 2);
                    __builtin_amdgcn_fence(__ATOMIC_ACQUIRE, "wavefront");
                    xv = l16 | (xn_ok ? 0u : OOB); PIN(xv);
                }
                if constexpr (i == 41) { xsrc = k.seg(PG_XNA, (s + 2) & 63) + w * 2048; PINS(xsrc); }
                if constexpr (i == 47 || i == 95) __builtin_amdgcn_s_barrier();
                if constexpr (i % 48 < 12 && i >= 48 && (i % 6 == 2 || i % 6 == 4 || i % 6 == 5)) xn_op<(i / 48 - 1) * 2 + (i / 6) % 2, (i % 6) / 2 - 1 + (i % 6) / 5>(k, xv, xsrc, xdst);
                if constexpr (i >= GPOS && i < GPOS + 10 && !((i - GPOS) & 1)) G.op<(i - GPOS) / 2>(k, l16, (s + 1) & 63);
                if constexpr (i == 111) G.op<5>(k, l16, 0);
                if constexpr (i >= 113 && i < 119) clr2(ac[(i + 6 - 113 + 5) % 6]); });
            static_assert(GPOS >= 59 && GPOS + 9 <= 98, "GPOS");
            QSTAMP(0, 4) QSTAMP(0, 5)
            PFLUSH(s)
        }
    }
    PROF_OUT
}

#define OIDX(t, b) (22 + (t) * 3 + (b))
DEVI void vo_role(const Params& p, const Ctx& k, char* smem, int h) {
    PROF_DECL
    const int w = k.w;
    if (k.c) sleep_ticks(WD1);
    bf16x16 W[52];
    {
        const int l = k.l;
        bf16x8 lo[10], wt[16];
        const u16* wv = p.Wv + h * 192 + w * 48;
        const u16* wo = p.Wo + (size_t)h * 192 * 640 + w * 160;
        wstream<22, 6, 6>(smem + w * 36864,
            [&](auto U, unsigned sl) { constexpr int u = CV(U);
                if constexpr (u >= 12) { wp_issue<3>(wv + (size_t)(u - 12) * 64 * 6144, 6144, sl, l); wp_issue<3>(wv + (size_t)((u - 12) * 64 + 32) * 6144, 6144, sl + 3072, l); }
                else { wp_issue<5>(wo + (u >> 1) * 32 * 640 + (u & 1) * 80, 640, sl, l); dma1(wo, l * 16, sl + 5120); } },
            [&](auto U, char* sp) { constexpr int u = CV(U);
                if constexpr (u >= 12) {
                    constexpr int b = u - 12;
#pragma unroll
                    for (int t = 0; t < 3; ++t) {
                        bf16x8 x = wp_read<3, true>(sp, t, l), y = wp_read<3, true>(sp + 3072, t, l);
                        if (t == 2 && b >= 2) { wt[(2 * (b - 2)) & 15] = x; wt[(2 * (b - 2) + 1) & 15] = y; } else W[(t * 10 + b) % 52] = CAT8(x, y);
                    }
                } else {
                    constexpr int ks = u >> 1, b = ks >> 1;
#pragma unroll
                    for (int i = 0; i < 5; ++i) {
                        const int t = (u & 1) * 5 + i;
                        bf16x8 f = wp_read<5, false>(sp, i, l);
                        if (ks & 1) W[OIDX(t, b)] = CAT8(lo[t], f); else lo[t] = f;
                    }
                } });
        RAWBAR()
#pragma unroll
        for (int i = 0; i < 16; ++i) *(bf16x8*)(smem + L_W + w * 16384 + i * 1024 + l * 16) = wt[i];
    }
    RSTAMP(2)
    unsigned l16, lwv, ix, dq0, trp, vo, vsig;
    {
        const int l = k.l, g = l >> 4, n = l & 15, q = (l >> 2) & 3, pp = l & 3;
        l16 = l * 16; lwv = l16 + L_W + w * 16384; ix = SPIX(l);
        dq0 = L_QM + (24 * w + (n >> 2) * 2 + (n & 1)) * 32 + g * 8 + ((n >> 1) & 1) * 4;
        trp = L_QM + (16 * (g >> 1) + 4 * (g & 1) + 8 * (q >> 1) + (q & 1)) * 32 + pp * 8;
        vo = EXP(h * 512) + (g >> 1) * 256 + n * 16 + (g & 1) * 8;
        vsig = l == 0 ? 0u : OOB;
        PIN(l16); PIN(lwv); PIN(ix); PIN(dq0); PIN(trp); PIN(vo); PIN(vsig);
    }
    const unsigned pdum = k.pg(1) + DUMMY_B, xdst = L_XN + w * 2048, pw = pdum + 98304 + h * 512 + w * 128, pbc = w == 0 ? k.p0 + CNT(C_BC, (h >> 3) * 16 + 8 + (h & 7)) : pw;
    Gather G;
    G3 V;
    V.l16 = l16; V.lwv = lwv;
    unsigned long long now = 0, done = 0;
    f32x4 yl = {0.f, 0.f, 0.f, 0.f};
    u32x2 lo = {0u, 0u};
    u32x4 o;
    unsigned yb = pdum;
    auto pack = [&](const f32x4& y) { o = (u32x4){lo[0], lo[1], cvtpk(y[0], y[1]), cvtpk(y[2], y[3])}; PIN(o); };
    xn_wait(k, l16, 0);
    xn_all(k, l16, k.seg(PG_XNA, 0) + w * 2048, xdst);
    DRAIN();
    RAWBAR()
    V.r0<0>(); V.r1<0>(); V.r0<1>(); V.r1<1>();
    for (int s = -1; s < 64; ++s) {
        f32x4 ac[3];
        SCHED0();
        V.run<0, true, 0, 16, 17, true>(W, [&](auto I) { constexpr int i = CV(I);
            if constexpr (i == 2) pack(yl);
            if constexpr (i == 4) __builtin_amdgcn_raw_buffer_store_b128(o, k.rs, (int)l16, (int)(yb + 9 * 65536), AUX_SC1);
            if constexpr (i == 5) asm volatile("" :: "v"(o));
            if constexpr (i == YPOS) { asm volatile("s_waitcnt vmcnt(0)\n\ts_barrier" ::: "memory"); G.mx = s; PIN(G.mx); }
            if constexpr (i == YPOS + 2) st32(k.rs, vsig, pbc + 4, G.mx);
            if constexpr (i >= GPOSY && i < GPOSY + 10 && !((i - GPOSY) & 1)) G.op<(i - GPOSY) / 2>(k, l16, s < 0 ? 0 : s);
            if constexpr (i == 91) G.op<5>(k, l16, 0);
            if constexpr (i >= 92 && i < 95) clr2(ac[(i - 92) % 3]); });
        static_assert(GPOSY > YPOS + 2 && GPOSY + 9 <= 96, "GPOSY");
        STAMP(s, 5)
        PFLUSH(s - 1)
        f32x4 acc[10];
        f32x2 z[20];
        auto clr = [&](auto I) { constexpr int i = CV(I); asm volatile("v_mov_b64 %0, 0" : "=v"(z[i])); if constexpr (i & 1) acc[i >> 1] = __builtin_shufflevector(z[i - 1], z[i], 0, 1, 2, 3); };
        bf16x4 al[3], ah[3];
        unsigned an;
        {
            bf16x8 m[10], wa[10], wb[10];
            auto first = [&](auto I) { constexpr int i = CV(I); smf<true>(ac[i % 3], __builtin_bit_cast(bf16x8, i < 3 ? G.p0 : G.p1), W[(i % 3) * 10 + i / 3], ix); };
            G.wait<0>();
            STOP(s) STAMP(s, 0)
            sfor<6>([&](auto I) { constexpr int i = CV(I);
                first(I);
                SCHED0();
                if constexpr (i == 2) wa[2] = lr128(lwv);
                if constexpr (i == 3) wb[2] = lr128(lwv + 1024);
                G.fin<i>();
                SCHED0(); });
            if (G.bad != 0) {
                unsigned spins = 0;
_Pragma("unroll 1")
                do { PCOUNT(0) G.again(k, l16); } while (G.bad != 0 && !spin_fail(k, spins++));
                STOP(s) STAMP(s, 0)
                clr2(ac[0]); clr2(ac[1]); clr2(ac[2]);
                asm volatile("s_nop 1" : "+v"(ac[0]), "+v"(ac[1]), "+v"(ac[2]));
                sfor<6>(first);
                asm volatile("s_waitcnt lgkmcnt(0)" ::: "memory");
            }
            bar_lgkm<2>();
            an = (unsigned)now;
            now = rt_async();
            m[2] = lr128(l16 + L_MEM + 2048);
            auto rd = [&](auto R) { constexpr int r = CV(R), b = 3 + r / 3;
                if constexpr (b < 10) {
                    if constexpr (r % 3 == 0) wa[b] = lr128(lwv + (b - 2) * 2048);
                    if constexpr (r % 3 == 1) wb[b] = lr128(lwv + (b - 2) * 2048 + 1024);
                    if constexpr (r % 3 == 2) m[b] = lr128(l16 + L_MEM + b * 1024);
                } else if constexpr (b < 13) {
                    if constexpr (r % 3 == 0) V.r0<b + 7>();
                    if constexpr (r % 3 == 1) V.rw<b + 7>();
                    if constexpr (r % 3 == 2) V.r1<b + 7>();
                } };
            SCHED0();
            V.run<0, true, 16, 17, 0, true>(W, [&](auto I) { constexpr int i = CV(I); if constexpr (i < 3) rd(I); if constexpr (i == 5) { rd(IC(5)); rd(IC(3)); } });
            sfor<24>([&](auto I) {
                constexpr int i = CV(I), b = 2 + i / 3, t = (i + 2) % 3;
                if constexpr (t == 2) smf<false>(ac[2], m[b], CAT8(wa[b], wb[b]), ix); else smf<true>(ac[t], m[b], W[t * 10 + b], ix);
                SCHED0();
                if constexpr (i == 0) rd(IC(4));
                if constexpr (i < 21) rd(IC(6 + i));
                SCHED0();
            });
            unsigned fo[3];
            V.run<0, true, 17, 19, 0, true>(W, [&](auto I) { constexpr int i = CV(I);
                if constexpr (i < 3) rd(IC(27 + i));
                if constexpr (i >= 1 && i < 10) fold_op<(i - 1) % 3>(ac[((i + 5) / 3) % 3], fo[((i + 5) / 3) % 3]);
                if constexpr (i == 3 || i == 6 || i == 9) lw32(dq0 + ((i / 3 + 1) % 3) * 256, fo[(i / 3 + 1) % 3]);
                if constexpr (i == 4 || i == 5 || i == 7 || i == 8) clr(IC((i / 7) * 2 + i % 3 - 1));
                if constexpr (i >= 10) clr2(acc[i - 8]); });
        }
        lgkm0(now, done);
        __builtin_amdgcn_s_barrier();
        STAMP(s, 1)
        {
            unsigned ym, fo[2], xc, xv, xsrc, d0, d1;
            bool xn_ok;
            bf16x8 v[12];
            u32x2 pk[10];
            auto fin = [&](auto Q) { constexpr int tp = CV(Q) / 11, j = CV(Q) % 11;
                if constexpr (tp >= 5) { }
                else if constexpr (j < 6) fold_op<j / 2>(acc[2 * tp + (j & 1)], fo[j & 1]);
                else if constexpr (j < 8) asm volatile("v_min_u32 %0, -2, %0" : "+v"(fo[j & 1]));
                else if constexpr (j == 8) asm volatile("s_nop 0");
                else if constexpr (j == 9) asm volatile("v_permlane16_swap_b32 %0, %1" : "+v"(fo[0]), "+v"(fo[1]));
                else st64(k.rs, vo, ym + tp * 32768, (u32x2){fo[0], fo[1]}); };
            al[0] = lrtr(trp); ah[0] = lrtr(trp + 64);
            SCHED0();
            V.run<0, true, 19, 20, 0, true>(W, [&](auto I) { constexpr int i = CV(I);
                if constexpr (i == 0) al[1] = lrtr(trp + 1024);
                if constexpr (i == 1) ah[1] = lrtr(trp + 1088);
                if constexpr (i == 2) al[2] = lrtr(trp + 2048);
                if constexpr (i == 3) ah[2] = lrtr(trp + 2112);
                if constexpr (i == 0) { an = min((unsigned)done - an, 200u) + DV + (unsigned)now; PINS(an); }
                if constexpr (i == 1) { an = ANNOUNCE(s, an, 0); PINS(an); }
                if constexpr (i == 0) d0 = vadd(l16, L_K + ((3 * w) >> 1) * 1024 + ((3 * w) & 1) * 8);
                if constexpr (i == 1) d1 = vadd(l16, L_K + ((3 * w + 1) >> 1) * 1024 + ((3 * w + 1) & 1) * 8);
                if constexpr (i == 2) { fo[0] = an; PIN(fo[0]); }
                if constexpr (i == 3) st32(k.rs, vsig, pbc, fo[0]);
                if constexpr (i >= 4) clr2(acc[i]); });
            sfor<30>([&](auto I) { constexpr int i = CV(I), b = (i % 6) >> 1, t = 2 * (i / 6) + (i & 1), id = OIDX(t, b);
                smf<(id < 32)>(acc[t], CAT4(al[b], ah[b]), W[id], ix);
                SCHED0();
                V.out<i - 1, 0>(d0, d1);
                if constexpr ((i % 3 == 0 && i < 21) || i == 19) clr(IC(12 + (i == 19 ? 7 : i / 3)));
                if constexpr (i == 21) bar_lgkm<0>();
                if constexpr (i >= 21 && i < 27) v[i - 21] = lr128(l16 + L_K + (i - 21) * 1024);
                if constexpr (i == 28) { ym = (s < 0 ? pdum : k.ym(s)) + w * 163840; PINS(ym); }
                if constexpr (i >= 20 && i < 28) fin(IC(i - 20));
                SCHED0(); });
            STAMP(s, 2)
            f32x4 y[20];
            sfor<120>([&](auto I) {
                constexpr int i = CV(I), rt = i / 60, t = (i / 6) % 10, ks = i % 6, u = i / 6, id = OIDX(t, ks >> 1);
                if constexpr (ks & 1) mfb<(id < 32), false>(y[u], v[rt * 6 + ks], HI8(W[id]));
                else mfb<(id < 32), ks == 0>(y[u], v[rt * 6 + ks], LO8(W[id]));
                SCHED0();
                if constexpr (i < 32) { constexpr int q0 = 9 + (3 * i) / 2, q1 = 9 + (3 * i + 3) / 2; sfor<q1 - q0>([&](auto J) { fin(IC(q0 + CV(J))); }); }
                if constexpr (i == 32) done = rt_async();
                if constexpr (u > 0 && u <= 10 && (ks == 2 || ks == 4)) { pk[u - 1][ks >> 2] = cvtpk(y[u - 1][ks - 2], y[u - 1][ks - 1]); PIN(pk[u - 1][ks >> 2]); }
                if constexpr (u > 10 && ks == 2) pack(y[u - 1]);
                if constexpr (u > 10 && ks == 3) __builtin_amdgcn_raw_buffer_store_b128(o, k.rs, (int)l16, (int)(yb + (t - 1) * 65536), AUX_SC1);
                if constexpr (u > 10 && ks == 4) asm volatile("" :: "v"(o));
                if constexpr (u >= 10 && ks == 5) { u32x2 d; asm volatile("v_mov_b64 %0, %1" : "=v"(d) : "v"(pk[u % 10])); lo = d; }
                if constexpr (i == XPOSY) xc = xn_get(k, l16, s + 2);
                if constexpr (i == 35) { xsrc = k.seg(PG_XNA, (s + 2) & 63) + w * 2048; PINS(xsrc); }
                if constexpr (i == 37) { yb = (s < 63 ? k.yx(s + 1, w) : pdum) + EXP(h * 1024); PINS(yb); }
                if constexpr (i == 39) {
                    xn_ok = s < 62 && xn_has(k, xc, s + 2);
                    __builtin_amdgcn_fence(__ATOMIC_ACQUIRE, "wavefront");
                    xv = l16 | (xn_ok ? 0u : OOB); PIN(xv);
                }
                if constexpr (i >= 40 && i < 65 && i % 5 != 1 && i % 5 != 3) xn_op<(i / 5 - 8) % 5, (i % 5) / 2>(k, xv, xsrc, xdst);
                if constexpr (i >= 46 && i < 60 && (i % 5 == 1 || i % 5 == 3)) v[6 + (i - 46) / 5 * 2 + (i % 5) / 3] = lr128(l16 + L_K + (6 + (i - 46) / 5 * 2 + (i % 5) / 3) * 1024);
                if constexpr (i == 60) { STAMP(s, 3) }
                if constexpr (i == 106) {
                    if (!xn_ok && s < 62) { PCOUNT(1) asm volatile("s_nop 7"); xn_wait(k, l16, s + 2); xn_all(k, l16, xsrc, xdst); DRAIN(); }
                    asm volatile("s_waitcnt vmcnt(7)\n\ts_barrier" ::: "memory");
                }
                if constexpr (i == 108) V.r0<0>();
                if constexpr (i == 109) V.r1<0>();
                if constexpr (i == 114) V.r0<1>();
                if constexpr (i == 115) V.r1<1>();
                SCHED0();
            });
            yl = y[19];
            STAMP(s, 4)
        }
    }
    PFLUSH(63)
    PROF_OUT
}

DEVI void ln_store(const Ctx& k, const float (&v)[40], unsigned dst, int row  ) {
    f32x2 s2[4] = {{0.f, 0.f}, {0.f, 0.f}, {0.f, 0.f}, {0.f, 0.f}};
#pragma unroll
    for (int i = 0; i < 40; i += 2) s2[(i >> 1) & 3] += (f32x2){v[i], v[i + 1]};
    const f32x2 st = (s2[0] + s2[1]) + (s2[2] + s2[3]);
    float m = row16sum(st[0] + st[1]) * (1.f / 640.f);
    f32x2 q2[4] = {{0.f, 0.f}, {0.f, 0.f}, {0.f, 0.f}, {0.f, 0.f}};
#pragma unroll
    for (int i = 0; i < 40; i += 2) { const f32x2 d = (f32x2){v[i], v[i + 1]} - (f32x2){m, m}; q2[(i >> 1) & 3] += d * d; }
    const f32x2 qt = (q2[0] + q2[1]) + (q2[2] + q2[3]);
    const float q = row16sum(qt[0] + qt[1]);
    const float var = bfr(q * (1.f / 640.f));
    m = bfr(m);
    const float rden = __builtin_amdgcn_rcpf(bfr(__builtin_amdgcn_sqrtf(bfr(var + 1e-5f))));
    const int n = k.l & 15;
    const unsigned vo = (n >> 2) * 2048 + (row >> 4) * 1024 + ((n & 3) * 16 + (row & 15)) * 16;
#pragma unroll
    for (int c = 0; c < 5; ++c) {
        u32x4 o;
#pragma unroll
        for (int i = 0; i < 4; ++i) { unsigned d = cvtpk(v[c * 8 + 2 * i] - m, v[c * 8 + 2 * i + 1] - m); o[i] = cvtpk(blo(d) * rden, bhi(d) * rden); }
        st128(k.rs, vo, dst + c * 16384, o);
    }
}
DEVI void unpack8(float* v, u32x4 a) {
#pragma unroll
    for (int i = 0; i < 4; ++i) { v[2 * i] = blo(a[i]); v[2 * i + 1] = bhi(a[i]); }
}
DEVI void ln_x_batch(const Params& p, const Ctx& k, int bw) {
    const int row = bw * 4 + (k.l >> 4), sg = bw >> 3;
    float v[40];
#pragma unroll
    for (int c = 0; c < 5; ++c) unpack8(v + c * 8, *(const u32x4*)(p.x + (size_t)row * 640 + (c * 16 + (k.l & 15)) * 8));
    ln_store(k, v, k.seg0(PG_XNA, sg), row & 31);
    DRAIN();
    if (k.l == 0) cnt_add(k.cnt0(C_XN, sg));
}

DEVI void row_role(const Params& p, const Ctx& k, char* smem, int rt) {
    PROF_DECL
    const int l = k.l, n = l & 15;
    const unsigned fo = k.w * 2048 + rt * 1024;
    RSTAMP(2)
    for (int sg = k.c; sg < 64; sg += 2) {
        prog_wait<5>(k, k.pg0(0) + CNT(C_X2, rt * 5), sg);
        STOP(sg) STAMP(sg, 0)
        const unsigned src = k.seg0(PG_X2, sg) + fo, dst = (k.seg0(PG_XNA, sg) ^ 8192u) + fo;
        u32x4 a[5];
_Pragma("unroll 1")
        for (unsigned spins = 0;; ++spins) {
            unsigned mx = 0;
#pragma unroll
            for (int c = 0; c < 5; ++c) a[c] = ld128(k.rs, l * 16, src + c * 16384);
#pragma unroll
            for (int c = 0; c < 5; ++c) mx = max(mx, umax4(a[c]));
            if (__all(mx != 0xFFFFFFFFu) || spin_fail(k, spins)) break;
            PCOUNT(0)
            asm volatile("" ::: "memory");
        }
        STAMP(sg, 1)
        f32x2 v[20], s2[4], q2[4];
#pragma unroll
        for (int i = 0; i < 20; ++i) { v[i] = (f32x2){blo(a[i >> 2][i & 3]), bhi(a[i >> 2][i & 3])}; s2[i & 3] = i < 4 ? v[i] : s2[i & 3] + v[i]; }
        const f32x2 st = (s2[0] + s2[1]) + (s2[2] + s2[3]);
        const float sm = xor32add(xor16add(st[0] + st[1])), mu = sm * (1.f / 160.f);
#pragma unroll
        for (int i = 0; i < 20; ++i) { const f32x2 d = v[i] - mu; q2[i & 3] = i < 4 ? d * d : __builtin_elementwise_fma(d, d, q2[i & 3]); }
        const f32x2 qt = (q2[0] + q2[1]) + (q2[2] + q2[3]);
        *(f32x2*)(smem + n * 32 + k.w * 8) = (f32x2){sm, xor32add(xor16add(qt[0] + qt[1]))};
        RAWBAR()
        const f32x4 e0 = *(const f32x4*)(smem + n * 32), e1 = *(const f32x4*)(smem + n * 32 + 16);
        float m = ((e0[0] + e0[2]) + (e1[0] + e1[2])) * (1.f / 640.f);
        const f32x4 d = (f32x4){e0[0], e0[2], e1[0], e1[2]} - 160.f * m;
        const float var = bfr((((e0[1] + e0[3]) + (e1[1] + e1[3])) + ((d[0] * d[0] + d[1] * d[1]) + (d[2] * d[2] + d[3] * d[3])) * (1.f / 160.f)) * (1.f / 640.f));
        m = bfr(m);
        const float rden = __builtin_amdgcn_rcpf(bfr(__builtin_amdgcn_sqrtf(bfr(var + 1e-5f))));
#pragma unroll
        for (int c = 0; c < 5; ++c) {
            u32x4 o;
#pragma unroll
            for (int i = 0; i < 4; ++i) { const f32x2 x = v[c * 4 + i] - m; o[i] = cvtpk(bfr(x[0]) * rden, bfr(x[1]) * rden); }
            st128(k.rs, l * 16, dst + c * 16384, o);
        }
        STAMP(sg, 2)
        DRAIN();
        __builtin_amdgcn_s_barrier();
        st32(k.rs, (k.w == 0 && l == 0) ? 0u : OOB, (k.pg0(0) ^ 8192u) + CNT(C_XN, k.c * 2 + rt), (unsigned)sg);
        STAMP(sg, 3) STAMP(sg, 4) STAMP(sg, 5)
        PFLUSH(sg) PFLUSH(sg ^ 1)
    }
    PROF_OUT
}

struct Red {
    u32x4 Y[16];
    DEVI void yload(const Ctx& k, int j, int sg) {
        const unsigned a = k.yx(sg, j / 5) + (j % 5) * 131072 + k.w * 16384;
#pragma unroll
        for (int i = 0; i < 16; ++i) Y[i] = ld128(k.rs, k.l * 16 + (i & 3) * 1024, a + ((i >> 2) & 1) * 4096 + (i >> 3) * 65536);
    }
};
DEVI unsigned y_get(const Ctx& k) { return ld32(k.rs, (k.l & 7) * 128, k.p0 + CNT(C_BC, k.w * 16 + 8) + 4); }
DEVI void y_wait(const Ctx& k, int s) {
_Pragma("unroll 1")
    for (unsigned spins = 0; !__all((int)y_get(k) >= s); ++spins) {
        asm volatile("" ::: "memory");
        __builtin_amdgcn_s_sleep(2);
        if (spin_fail(k, spins)) break;
    }
    __builtin_amdgcn_fence(__ATOMIC_ACQUIRE, "wavefront");
}
DEVI void wait_until(unsigned t) { while ((int)((rclk() - t) << 16) < 0) { } }
#define DPPMAX(x, c) x = max(x, __builtin_amdgcn_update_dpp(0, x, c, 0xf, 0xf, false))
#define WAIT_BC(s) { \
    unsigned bc; \
    _Pragma("unroll 1") for (;; ++spins) { \
        bc = ld32(k.rs, (l & 15) * 128, k.p0 + CNT(C_BC, w * 16)); \
        DRAIN(); \
        if (__all(((int)bc >> 16) >= s)) break; \
        __builtin_amdgcn_s_sleep(BSLP); \
        if (spin_fail(k, spins)) break; \
    } \
    const unsigned now = rclk(); \
    int d = ((int)bc >> 16) > s ? 0 : (int)((bc - now) << 16); \
    DPPMAX(d, 0xB1); DPPMAX(d, 0x4E); DPPMAX(d, 0x141); \
    tq = now + (__builtin_amdgcn_readlane(d, 0) >> 16); tv = now + (__builtin_amdgcn_readlane(d, 8) >> 16); }
DEVI void mred_role(const Params& p, const Ctx& k, char* smem, int j) {
    PROF_DECL
    const int l = k.l, w = k.w, g = l >> 4, n = l & 15;
    const unsigned ix = SPIX(l);
    const unsigned vp = (g * 16 + ((n >> 1) & 1) * 8 + (n >> 2) * 2 + (n & 1)) * 16;
    const unsigned v16 = (g >> 1) * 1024 + (g & 1) * 256 + n * 16, yo = j * 32768 + EXP(w * 4096);
    const u32x4 ones = {0xFFFFFFFFu, 0xFFFFFFFFu, 0xFFFFFFFFu, 0xFFFFFFFFu};
    auto arm = [&](int s) {
#pragma unroll
        for (int i = 0; i < 4; ++i) st128(k.rs, l * 16 + i * 1024, k.ym(s) + yo, ones);
    };
    Red R;
    RSTAMP(2)
    arm(1);
    y_wait(k, 0);
    R.yload(k, j, 0);
    for (int s = 0; s < 63; ++s) {
        STOP(s) STAMP(s, 0)
        const unsigned ap = k.p0 + PR_B + (s & 3) * PR_SZ + w * 49152, am = k.ym(s) + yo;
        const unsigned ph = (s & 4) ? 0x80008000u : 0u;
        unsigned spins = 0, tq, tv;
        unsigned fl;
        WAIT_BC(s)
        STAMP(s, 5)
        char* rb = smem + (s & 1) * 4096;
_Pragma("unroll 1")
        for (spins = 0;; ++spins) {
            u32x4 ym[4], pm[5];
            f32x4 acc[2];
            if (spins == 0) wait_until(tv);
            asm volatile("" ::: "memory");
#pragma unroll
            for (int i = 0; i < 4; ++i) ym[i] = ld128(k.rs, v16 + (i >> 1) * 2048 + (i & 1) * 512, am);
            fl = ld32(k.rs, 0, k.p0 + CNT(C_X1, j));
            asm volatile("" ::: "memory");
            if (spins == 0) { wait_until(tq); STAMP(s, 1) }
            asm volatile("" ::: "memory");
#pragma unroll
            for (int i = 0; i < 5; ++i) pm[i] = ld128(k.rs, i < 4 ? l * 16 + i * 1024 : vp, ap + (i >> 2) * 4096);
            acc[0] = (f32x4){0.f, 0.f, 0.f, 0.f}; acc[1] = acc[0];
            const u32x8 y0 = {ym[0][0], ym[1][0], ym[0][1], ym[1][1], ym[2][0], ym[3][0], ym[2][1], ym[3][1]};
            const u32x8 y1 = {ym[0][2], ym[1][2], ym[0][3], ym[1][3], ym[2][2], ym[3][2], ym[2][3], ym[3][3]};
            unsigned mx = 0;
#pragma unroll
            for (int i = 0; i < 4; ++i) mx = max(mx, umax4(ym[i]));
            unsigned tg = mx == 0xFFFFFFFFu ? 0x80000000u : 0u;
            asm volatile("s_nop 1" : "+v"(acc[0]), "+v"(acc[1]), "+v"(tg) : "v"(y0), "v"(y1), "v"(ix));
            SCHED0();
#pragma unroll
            for (int i = 0; i < 5; ++i) {
                const unsigned u0 = __builtin_amdgcn_bitop3_b32(pm[i][0], pm[i][1], ph, 0x7e), u1 = __builtin_amdgcn_bitop3_b32(pm[i][2], pm[i][3], ph, 0x7e);
                SCHED0();
                if (i < 4) smf<false>(acc[0], __builtin_bit_cast(bf16x8, pm[i]), __builtin_bit_cast(bf16x16, __builtin_shufflevector(R.Y[2 * i], R.Y[2 * i + 1], 0, 1, 2, 3, 4, 5, 6, 7)), ix);
                else smf<false>(acc[0], __builtin_bit_cast(bf16x8, pm[4]), __builtin_bit_cast(bf16x16, y0), ix);
                tg = (tg | u0) | u1;
                SCHED0();
                if (i < 4) smf<false>(acc[1], __builtin_bit_cast(bf16x8, pm[i]), __builtin_bit_cast(bf16x16, __builtin_shufflevector(R.Y[8 + 2 * i], R.Y[9 + 2 * i], 0, 1, 2, 3, 4, 5, 6, 7)), ix);
                else smf<false>(acc[1], __builtin_bit_cast(bf16x8, pm[4]), __builtin_bit_cast(bf16x16, y1), ix);
                SCHED0();
            }
            const unsigned long long bad = __ballot((int)tg < 0);
            asm volatile("s_nop 5" : "+v"(acc[0]), "+v"(acc[1]) : "s"(bad));
#pragma unroll
            for (int t = 0; t < 2; ++t) { float* rp = (float*)(rb + t * 2048 + w * 512 + g * 128 + n * 4); rp[0] = acc[t][0] + acc[t][2]; rp[16] = acc[t][1] + acc[t][3]; }
            if (bad == 0) break;
            PCOUNT(0)
            asm volatile("" ::: "memory");
            if (spin_fail(k, spins)) break;
        }
        STAMP(s, 2)
        RAWBAR()
        if (w < 2) {
            const char* rp = rb + w * 2048 + ((l >> 4) * 2 + ((l >> 2) & 1)) * 64 + ((l & 3) * 4 + ((l >> 3) & 1) * 2) * 4;
            const f32x2 a = *(const f32x2*)rp, b = *(const f32x2*)(rp + 512), c = *(const f32x2*)(rp + 1024), d = *(const f32x2*)(rp + 1536);
            const f32x2 r = (a + b) + (c + d);
            st32(k.rs, l * 4, k.memf(s + 1) + EXP((2 * j + w) * 256), min(cvtpk(r[0], r[1]) ^ ph, 0xFFFFFFFEu));
        }
        STAMP(s, 3)
_Pragma("unroll 1")
        for (spins = 0; (int)__builtin_amdgcn_readfirstlane(fl) < s - 2; ++spins) {
            PCOUNT(1)
            __builtin_amdgcn_s_sleep(PSLEEP);
            if (spin_fail(k, spins)) break;
            fl = ld32(k.rs, 0, k.p0 + CNT(C_X1, j));
        }
        __builtin_amdgcn_fence(__ATOMIC_ACQUIRE, "wavefront");
        arm(s + 2);
        y_wait(k, s + 1);
        R.yload(k, j, s + 1);
        STAMP(s, 4)
        PFLUSH(s)
    }
    PFLUSH(63)
    PROF_OUT
}
DEVI void tred_role(const Params& p, const Ctx& k, char* smem, int j) {
    PROF_DECL
    const int l = k.l, w = k.w, g = l >> 4, n = l & 15, T = 2 * j + (w >> 1), rt = w & 1;
    const unsigned v16 = (g >> 1) * 1024 + (g & 1) * 256 + n * 16, yo = j * 32768 + EXP(w * 4096);
    const unsigned v8 = (g >> 1) * 512 + (g & 1) * 128 + n * 8;
    const unsigned xo = EXP(j * 2048) + (T & 1) * 512 + rt * 1024, xv = ((g >> 1) * 16 + n) * 16 + (g & 1) * 8;
    const u32x2 ones = {0xFFFFFFFFu, 0xFFFFFFFFu};
    Red R;
    RSTAMP(2)
    st64(k.rs, xv, k.seg(PG_X1, 0) + xo, ones); st64(k.rs, xv, k.seg(PG_X1, 1) + xo, ones);
    y_wait(k, 0);
    R.yload(k, j, 0);
    for (int s = 0; s < 64; ++s) {
        STOP(s) STAMP(s, 0)
        const unsigned ap1 = k.p0 + PR_B + (s & 3) * PR_SZ + w * 49152 + 16384, ap2 = ap1 + 16384, am = k.ym(s) + yo;
        const unsigned ph = (s & 4) ? 0x80008000u : 0u;
        unsigned spins = 0, tq, tv;
        u32x2 res;
        if (k.c) res = ld64(k.rs, xv, k.seg0(PG_X2, s) + xo);
        else res = *(const u32x2*)(p.x + (size_t)(s * 32 + rt * 16 + n) * 640 + T * 16 + g * 4);
        f32x4 acc[2][2];
        WAIT_BC(s)
        wait_until(tv); wait_until(tq + RDLY);
        asm volatile("" ::: "memory");
        STAMP(s, 1)
_Pragma("unroll 1")
        for (;; ++spins) {
            u32x4 ym[4], p1[8]; u32x2 p0[8], pq[8];
#pragma unroll
            for (int i = 0; i < 4; ++i) ym[i] = ld128(k.rs, v16 + (i >> 1) * 2048 + (i & 1) * 512, am);
#pragma unroll
            for (int i = 0; i < 8; ++i) pq[i] = ld64(k.rs, v8 + ((i >> 1) & 1) * 1024 + (i & 1) * 256 + (i >> 2) * 2048, ap2 + 4096);
#pragma unroll
            for (int i = 0; i < 8; ++i) { p0[i] = ld64(k.rs, l * 8 + i * 512, ap2); p1[i] = ld128(k.rs, l * 16 + (i & 3) * 1024, ap1 + (i >> 2) * 4096); }
            unsigned mx = 0, tg = 0;
#pragma unroll
            for (int t = 0; t < 2; ++t) {
                acc[t][0] = (f32x4){0.f, 0.f, 0.f, 0.f}; acc[t][1] = acc[t][0];
#pragma unroll
                for (int f = 0; f < 2; ++f) {
                    const u32x4 yf = {ym[2 * f][2 * t], ym[2 * f][2 * t + 1], ym[2 * f + 1][2 * t], ym[2 * f + 1][2 * t + 1]};
#pragma unroll
                    for (int r = 0; r < 2; ++r)
                        acc[t][r] = mf(__builtin_bit_cast(bf16x8, yf), __builtin_bit_cast(bf16x8, (u32x4){pq[4 * r + 2 * f][0], pq[4 * r + 2 * f][1], pq[4 * r + 2 * f + 1][0], pq[4 * r + 2 * f + 1][1]}), acc[t][r]);
                }
            }
#pragma unroll
            for (int i = 0; i < 8; ++i)
#pragma unroll
                for (int t = 0; t < 2; ++t) {
                    acc[t][0] = mf(__builtin_bit_cast(bf16x8, R.Y[t * 8 + i]), __builtin_bit_cast(bf16x8, (u32x4){p0[i][0], p0[i][1], 0u, 0u}), acc[t][0]);
                    acc[t][1] = mf(__builtin_bit_cast(bf16x8, R.Y[t * 8 + i]), __builtin_bit_cast(bf16x8, p1[i]), acc[t][1]);
                }
#pragma unroll
            for (int i = 0; i < 4; ++i) mx = max(mx, umax4(ym[i]));
#pragma unroll
            for (int i = 0; i < 8; ++i) tg |= (((p1[i][0] ^ ph) | (p1[i][1] ^ ph)) | ((p1[i][2] ^ ph) | (p1[i][3] ^ ph))) | (((p0[i][0] ^ ph) | (p0[i][1] ^ ph)) | ((pq[i][0] ^ ph) | (pq[i][1] ^ ph)));
            if (__all(mx != 0xFFFFFFFFu && (int)tg >= 0)) break;
            PCOUNT(0)
            asm volatile("" ::: "memory");
            if (spin_fail(k, spins)) break;
        }
        STAMP(s, 2)
        const unsigned yc = y_get(k);
        char* rb = smem + (s & 1) * 16384;
#pragma unroll
        for (int i = 0; i < 4; ++i) *(f32x4*)(rb + i * 4096 + w * 1024 + l * 16) = acc[i >> 1][i & 1];
        RAWBAR()
        st32(k.rs, (w == 0 && l == 0) ? 0u : OOB, k.p0 + CNT(C_X1, j), (unsigned)s);
        {
            const char* rp = rb + w * 4096 + l * 16;
            const f32x4 r = (*(const f32x4*)rp + *(const f32x4*)(rp + 1024)) + (*(const f32x4*)(rp + 2048) + *(const f32x4*)(rp + 3072));
            const unsigned d0 = cvtpk(r[0], r[1]) ^ ph, d1 = cvtpk(r[2], r[3]) ^ ph;
            u32x2 o; o[0] = packs(blo(res[0]) + blo(d0), bhi(res[0]) + bhi(d0)); o[1] = packs(blo(res[1]) + blo(d1), bhi(res[1]) + bhi(d1));
            st64(k.rs, xv, k.seg(PG_X1, s) + xo, o);
            st64(k.rs, s < 62 ? xv : OOB, k.seg(PG_X1, (s + 2) & 63) + xo, ones);
        }
        STAMP(s, 3)
        if (s < 63) {
            if (!__all((int)yc >= s + 1)) { PCOUNT(2) y_wait(k, s + 1); }
            __builtin_amdgcn_fence(__ATOMIC_ACQUIRE, "wavefront");
            R.yload(k, j, s + 1);
        }
        STAMP(s, 4)
        STAMP(s, 5)
        PFLUSH(s)
    }
    PROF_OUT
}

DEVI void up_role(const Params& p, const Ctx& k, char* smem, int id) {
    PROF_DECL
    const int l = k.l, w = k.w, g = l >> 4, n = l & 15;
    bf16x8 Wt[80];
    const int tile0 = id * 8 + w * 2;
    wstream<20, 4, 8>(smem + w * 32768,
        [&](auto U, unsigned sl) { constexpr int u = CV(U);
            wp_issue<2>(p.wg + (size_t)u * 32 * 1536 + tile0 * 16, 1536, sl, l); wp_issue<2>(p.wu + (size_t)u * 32 * 1536 + tile0 * 16, 1536, sl + 2048, l); },
        [&](auto U, char* sp) { constexpr int u = CV(U);
            Wt[u] = wp_read<2, true>(sp, 0, l); Wt[20 + u] = wp_read<2, true>(sp, 1, l); Wt[40 + u] = wp_read<2, true>(sp + 2048, 0, l); Wt[60 + u] = wp_read<2, true>(sp + 2048, 1, l); });
    RAWBAR()
    RSTAMP(2)
    bf16x8 one8 = {0x3F80, 0x3F80, 0x3F80, 0x3F80, 0x3F80, 0x3F80, 0x3F80, 0x3F80};
    asm volatile("s_nop 1" : "+v"(one8));
    f32x4 cs[4];
    sfor<80>([&](auto I) { constexpr int f = CV(I); mfa<(f < 64), f % 20 == 0>(cs[f / 20], Wt[f], one8); });
    asm volatile("s_nop 7" : "+v"(cs[0]), "+v"(cs[1]), "+v"(cs[2]), "+v"(cs[3]));
    const unsigned ho = EXP((tile0 >> 1) * 1024), l16 = l * 16;
    constexpr int L_ST = 81920;
    const u32x4 ones = {0xFFFFFFFFu, 0xFFFFFFFFu, 0xFFFFFFFFu, 0xFFFFFFFFu};
#pragma unroll
    for (int i = 0; i < 4; ++i) st128(k.rs, l16, k.hseg(i >> 1) + (i & 1) * EXP(49152) + ho, ones);
    unsigned fails = 0;
    const unsigned fl = (w == 0 && l == 0) ? 0u : OOB;
    const unsigned long long m1 = __ballot(n & 1), m2 = __ballot(n & 2);
    for (int s = 0; s < 64; ) {
        prog_wait<20>(k, k.p0 + CNT(C_X1, 0), s);
        STOP(s) STAMP(s, 0)
        const unsigned xb = (s & 1) * 40960 + l16, yb = xb + w * 2048, sp = L_ST + (s & 1) * 2048, rp = sp + n * 16;
        const unsigned dp = sp + (g == (n >> 2) ? n * 16 + w * 4 : 4096 + l * 4);
        DRAIN();
        xn_all(k, l16, k.seg(PG_X1, s) + w * 2048, (s & 1) * 40960 + w * 2048);
        f32x4 acc[2][4], st[4];
        bf16x8 x[20], y0[5], y1[5];
        unsigned mx = 0;
        auto blk = [&](auto B, auto J) { constexpr int b = CV(B), j = CV(J);
            if constexpr (j == 0) asm volatile("s_waitcnt vmcnt(%0)\n\ts_barrier" :: "n"(8 - 2 * b) : "memory");
            if constexpr (j >= 1 && j < 5) x[4 * b + j - 1] = lr128(xb + (4 * b + j - 1) * 2048);
            if constexpr (j == 5) y0[b] = lr128(yb + b * 8192);
            if constexpr (j == 6) y1[b] = lr128(yb + b * 8192 + 1024); };
        sfor<7>([&](auto J) { blk(IC(0), J); });
        SCHED0();
        STAMP(s, 1)
        sfor<5>([&](auto B) { constexpr int b = CV(B);
            sfor<20>([&](auto I) { constexpr int i = CV(I), ks = 4 * b + ((i >> 2) & 3), nt = i & 3, f = nt * 20 + ks;
                if constexpr (i < 16) mfa<(f < 64), ks == 0>(acc[0][nt], Wt[f], x[ks]);
                else mfa<false, b == 0>(st[nt], (nt & 1) ? (nt & 2 ? y1[b] : y0[b]) : one8, nt & 2 ? y1[b] : y0[b]);
                SCHED0();
                if constexpr (i >= 8 && i < 15 && b < 4) blk(IC((b + 1) % 5), IC(i - 8));
                if constexpr (i >= 16) { const u32x4 v = __builtin_bit_cast(u32x4, (i & 1) ? y1[b] : y0[b]); mx = umax3(mx, v[i & 2], v[(i & 2) + 1]); PIN(mx); }
                if constexpr (b == 4 && i >= 9 && i < 12) x[i - 9] = lr128(xb + 1024 + (i - 9) * 2048);
                SCHED0(); }); });
        STAMP(s, 2)
        float mean[2], rden[2], G[2], U[2];
        f32x4 sa[2], sb[2];
        u32x4 bad, o[2];
        bool redo;
        auto stat = [&](auto I) { constexpr int i = CV(I), rt = i / 8, j = i % 8;
            if constexpr (j == 0) asm volatile("v_add_f32 %0, %2, %3\n\tv_add_f32 %1, %4, %5" : "=&v"(mean[rt]), "=&v"(rden[rt]) : "v"(sa[rt][0]), "v"(sa[rt][1]), "v"(sb[rt][0]), "v"(sb[rt][1]));
            if constexpr (j == 1 || j == 2) asm volatile("v_add_f32 %0, %0, %2\n\tv_add_f32 %1, %1, %3" : "+v"(mean[rt]), "+v"(rden[rt]) : "v"(sa[rt][j + 1]), "v"(sb[rt][j + 1]));
            if constexpr (j == 3) asm volatile("v_mul_f32 %0, 0xbaccccCD, %0\n\tv_mul_f32 %1, 0x3acccccd, %1" : "+v"(mean[rt]), "+v"(rden[rt]));
            if constexpr (j == 4) asm volatile("v_fma_f32 %1, -%0, %0, %1\n\tv_cvt_pk_bf16_f32 %0, 0, %0\n\tv_max_f32 %1, 0, %1" : "+v"(mean[rt]), "+v"(rden[rt]));
            if constexpr (j == 5) asm volatile("v_cvt_pk_bf16_f32 %0, 0, %0\n\tv_add_f32 %0, 0x3727c5ac, %0\n\tv_cvt_pk_bf16_f32 %0, 0, %0" : "+v"(rden[rt]));
            if constexpr (j == 6) asm volatile("v_sqrt_f32 %0, %0" : "+v"(rden[rt]));
            if constexpr (j == 7) asm volatile("v_cvt_pk_bf16_f32 %0, 0, %0\n\tv_rcp_f32 %0, %0" : "+v"(rden[rt])); };
#define GU "+v"(G[0]), "+v"(G[1]), "+v"(U[0]), "+v"(U[1])
        auto ep = [&](auto I) { constexpr int i = CV(I), q = i / 15, j = i % 15, rt = q >> 2, t = (q >> 1) & 1, c = (q & 1) * 2;
            if constexpr (j == 0) asm volatile("v_fma_f32 %0, %2, %3, %4\n\tv_fma_f32 %1, %2, %5, %6" : "=&v"(G[0]), "=&v"(G[1]) : "v"(mean[rt]), "v"(cs[t][c]), "v"(acc[rt][t][c]), "v"(cs[t][c + 1]), "v"(acc[rt][t][c + 1]));
            if constexpr (j == 1) asm volatile("v_mul_f32 %0, %0, %2\n\tv_mul_f32 %1, %1, %2" : "+v"(G[0]), "+v"(G[1]) : "v"(rden[rt]));
            if constexpr (j == 2) asm volatile("v_cvt_pk_bf16_f32 %0, 0, %0\n\tv_cvt_pk_bf16_f32 %1, 0, %1" : "+v"(G[0]), "+v"(G[1]));
            if constexpr (j == 3) asm volatile("v_mul_f32 %0, 0xbfb8aa3b, %0\n\tv_mul_f32 %1, 0xbfb8aa3b, %1" : "+v"(G[0]), "+v"(G[1]));
            if constexpr (j == 4 || j == 5) asm volatile("v_exp_f32 %0, %0" : "+v"(G[j & 1]));
            if constexpr (j == 6) asm volatile("v_fma_f32 %0, %2, %3, %4\n\tv_fma_f32 %1, %2, %5, %6" : "=&v"(U[0]), "=&v"(U[1]) : "v"(mean[rt]), "v"(cs[2 + t][c]), "v"(acc[rt][2 + t][c]), "v"(cs[2 + t][c + 1]), "v"(acc[rt][2 + t][c + 1]));
            if constexpr (j == 7) asm volatile("v_add_f32 %0, 1.0, %0\n\tv_add_f32 %1, 1.0, %1" : "+v"(G[0]), "+v"(G[1]));
            if constexpr (j == 8 || j == 9) asm volatile("v_rcp_f32 %0, %0" : "+v"(G[j & 1]));
            if constexpr (j == 10) asm volatile("v_mul_f32 %0, %0, %2\n\tv_mul_f32 %1, %1, %2" : "+v"(U[0]), "+v"(U[1]) : "v"(rden[rt]));
            if constexpr (j == 11) asm volatile("v_cvt_pk_bf16_f32 %0, 0, %0\n\tv_cvt_pk_bf16_f32 %1, 0, %1" : "+v"(U[0]), "+v"(U[1]));
            if constexpr (j == 12) asm volatile("v_cvt_pk_bf16_f32 %0, 0, %0\n\tv_cvt_pk_bf16_f32 %1, 0, %1" : "+v"(G[0]), "+v"(G[1]));
            if constexpr (j == 13) asm volatile("v_mul_f32 %0, %0, %2\n\tv_mul_f32 %1, %1, %3" : "+v"(G[0]), "+v"(G[1]) : "v"(U[0]), "v"(U[1]));
            if constexpr (j == 14) { unsigned d; asm volatile("v_cvt_pk_bf16_f32 %0, %1, %2\n\tv_min_u32 %0, -2, %0" : "=&v"(d) : "v"(G[0]), "v"(G[1])); o[rt][q & 3] = d; } };
        sfor<80>([&](auto I) {
            constexpr int i = CV(I), ks = i >> 2, nt = i & 3, f = nt * 20 + ks;
            mfa<(f < 64), ks == 0>(acc[1][nt], Wt[f], x[ks]);
            SCHED0();
            if constexpr (nt == 0 && ks + 3 < 20) x[(ks + 3) % 20] = lr128(xb + 1024 + ((ks + 3) % 20) * 2048);
            if constexpr (i == 1 || i == 3) {
                constexpr int rt2 = i == 3;
                asm volatile("" : "+v"(st[2 * rt2]), "+v"(st[2 * rt2 + 1]));
                const f32x4& q = st[2 * rt2 + 1];
                const float d = csel(csel(q[0], q[1], m1), csel(q[2], q[3], m1), m2);
                lw32(dp + rt2 * 512, __float_as_uint(st[2 * rt2][0])); lw32(dp + rt2 * 512 + 256, __float_as_uint(d));
            }
            if constexpr (i == 2) lw32(sp + 1024 + w * 4, __any(mx == 0xFFFFFFFFu) ? 1u : 0u);
            if constexpr (i == 6) bar_lgkm<1>();
            if constexpr (i == 6) sa[0] = __builtin_bit_cast(f32x4, lr128(rp));
            if constexpr (i == 7) sb[0] = __builtin_bit_cast(f32x4, lr128(rp + 256));
            if constexpr (i == 9) sa[1] = __builtin_bit_cast(f32x4, lr128(rp + 512));
            if constexpr (i == 10) sb[1] = __builtin_bit_cast(f32x4, lr128(rp + 768));
            if constexpr (i == 11) bad = __builtin_bit_cast(u32x4, lr128(sp + 1024));
            if constexpr (i >= 12 && i < 20) stat(IC(i - 12));
            if constexpr (i >= 20) ep(IC(i - 20));
            if constexpr (i >= 21 && i < 53 && nt == 1) stat(IC(8 + (i - 21) / 4));
            if constexpr (i == 54) { redo = __builtin_amdgcn_readfirstlane((bad[0] | bad[1]) | (bad[2] | bad[3])) != 0 && fails++ < 4096u; }
            if constexpr (i == UPOS) st32(k.rs, fl, k.p0 + CNT(C_H, id), (unsigned)(redo ? s - 1 : s));
            SCHED0();
        });
        STAMP(s, 3)
        asm volatile("s_nop 7" : "+v"(acc[1][0]), "+v"(acc[1][1]), "+v"(acc[1][2]), "+v"(acc[1][3]));
#pragma unroll
        for (int q = 0; q < 4; ++q) {
            const int t = q >> 1, c = (q & 1) * 2;
            float hv[2];
#pragma unroll
            for (int i = 0; i < 2; ++i) {
                const float gg = bfr(__builtin_fmaf(mean[1], cs[t][c + i], acc[1][t][c + i]) * rden[1]), u = bfr(__builtin_fmaf(mean[1], cs[2 + t][c + i], acc[1][2 + t][c + i]) * rden[1]);
                hv[i] = bfr(__builtin_amdgcn_rcpf(1.f + __builtin_amdgcn_exp2f(gg * -1.44269504f))) * u;
            }
            o[1][q] = packs(hv[0], hv[1]);
        }
        if (redo) { PCOUNT(0) RAWBAR() continue; }
        st128(k.rs, l16, k.hseg(s) + ho, o[0]); st128(k.rs, l16, k.hseg(s) + EXP(49152) + ho, o[1]);
        st128(k.rs, s < 62 ? l16 : OOB, k.hseg((s + 2) & 63) + ho, ones); st128(k.rs, s < 62 ? l16 : OOB, k.hseg((s + 2) & 63) + EXP(49152) + ho, ones);
        STAMP(s, 4) STAMP(s, 5)
        PFLUSH(s)
        ++s;
    }
    PROF_OUT
}

DEVI void down_role(const Params& p, const Ctx& k, char* smem, int id) {
    PROF_DECL
    const int l = k.l, w = k.w, g = l >> 4, n = l & 15, rt = id & 1, cg = id >> 1;
    bf16x8 Wt[96];
    wstream<12, 8, 4>(smem + w * 32768,
        [&](auto U, unsigned sl) { constexpr int u = decltype(U)::value; wp_issue<8>(p.wd + (size_t)(w * 12 + u) * 32 * 640 + cg * 128, 640, sl, l); },
        [&](auto U, char* sp) { constexpr int u = decltype(U)::value;
#pragma unroll
            for (int nt = 0; nt < 8; ++nt) Wt[u * 8 + nt] = wp_read<8, false>(sp, (nt + 2 * w) & 7, l); });
    RAWBAR()
    RSTAMP(2)
    const unsigned xo = EXP((cg * 4 + w) * 2048) + rt * 1024, xv = ((g >> 1) * 16 + n) * 16 + (g & 1) * 8;
    const u32x2 ones = {0xFFFFFFFFu, 0xFFFFFFFFu};
    const unsigned x2v = k.c ? OOB : xv;
#pragma unroll
    for (int i = 0; i < 4; ++i) st64(k.rs, x2v, k.seg(PG_X2, i >> 1) + xo + (i & 1) * 512, ones);
    for (int s = 0; s < 64; ++s) {
        prog_wait<12>(k, k.p0 + CNT(C_H, 0), s);
        STOP(s) STAMP(s, 0)
        f32x4 acc[8];
        u32x2 res[2];
        const unsigned rb = (s & 1) * 32768 + l * 16;
_Pragma("unroll 1")
        for (unsigned spins = 0;; ++spins) {
            u32x4 hx[12];
#pragma unroll
            for (int q = 0; q < 3; ++q) {
                const unsigned a = k.hseg(s) + rt * EXP(49152) + EXP((w * 12 + q * 4) * 1024);
#pragma unroll
                for (int i = 0; i < 4; ++i) hx[q * 4 + i] = ld128(k.rs, l * 16 + i * 1024, a);
            }
#pragma unroll
            for (int t = 0; t < 2; ++t) res[t] = ld64(k.rs, xv + t * 512, k.seg(PG_X1, s) + xo);
            unsigned mx = 0;
            sfor<96>([&](auto I) { constexpr int i = CV(I), ks = i < 72 ? i / 6 : (i - 72) / 2, j = i < 72 ? 2 + i % 6 : i & 1, f = ks * 8 + j;
                mfa<(f < 64), ks == 0>(acc[j], Wt[f], __builtin_bit_cast(bf16x8, hx[ks]));
                SCHED0();
                if constexpr (i < 72 && i % 6 < 2) { mx = umax3(mx, hx[ks][(i & 1) * 2], hx[ks][(i & 1) * 2 + 1]); PIN(mx); }
                if constexpr (i >= 75 && i < 93 && i % 3 == 0) { constexpr int jj = 2 + (i - 75) / 3;
                    asm volatile("" : "+v"(acc[jj])); lw128(rb + ((w + jj / 2) & 3) * 6144 + ((jj / 2 - 1) * 2 + (jj & 1)) * 1024, __builtin_bit_cast(u32x4, acc[jj])); }
                SCHED0(); });
            asm volatile("s_nop 7" : "+v"(acc[0]), "+v"(acc[1]), "+v"(res[0]), "+v"(res[1]));
            if (__all(mx != 0xFFFFFFFFu) || spin_fail(k, spins)) break;
            PCOUNT(0)
            asm volatile("" ::: "memory");
        }
        STAMP(s, 1)
        RAWBAR()
        st32(k.rs, (w == 0 && l == 0) ? 0u : OOB, k.p0 + CNT(C_X2, rt * 5 + cg), (unsigned)s);
#pragma unroll
        for (int t = 0; t < 2; ++t) {
            const unsigned rp = rb + w * 6144 + t * 1024;
            const f32x4 r = (acc[t] + __builtin_bit_cast(f32x4, lr128(rp))) + (__builtin_bit_cast(f32x4, lr128(rp + 2048)) + __builtin_bit_cast(f32x4, lr128(rp + 4096)));
            const unsigned d0 = cvtpk(r[0], r[1]), d1 = cvtpk(r[2], r[3]);
            u32x2 o; o[0] = cvtpk(blo(res[t][0]) + blo(d0), bhi(res[t][0]) + bhi(d0)); o[1] = cvtpk(blo(res[t][1]) + blo(d1), bhi(res[t][1]) + bhi(d1));
            if (k.c) *(u32x2*)(p.out + (size_t)(s * 32 + rt * 16 + n) * 640 + (cg * 8 + w * 2 + t) * 16 + g * 4) = o;
            else st64(k.rs, xv + t * 512, k.seg(PG_X2, s) + xo, (u32x2){min(o[0], 0xFFFFFFFEu), min(o[1], 0xFFFFFFFEu)});
        }
        STAMP(s, 2)
#pragma unroll
        for (int t = 0; t < 2; ++t) st64(k.rs, s < 62 ? x2v : OOB, k.seg(PG_X2, (s + 2) & 63) + xo + t * 512, ones);
        STAMP(s, 3) STAMP(s, 4) STAMP(s, 5)
        PFLUSH(s)
    }
    PROF_OUT
}

DEVI void warm(const Params& p, const Ctx& k, int xl, int hw) {
    if (k.c) sleep_ticks(WD1);
    set_m0(k.w * 256);
    const unsigned t0 = rclk();
_Pragma("unroll 1")
    for (int it = hw; it < 872; it += 64) {
        const u16* b; unsigned o;
        while ((int)(rclk() - t0) < (it >> 6) * PACE) __builtin_amdgcn_s_sleep(1);
        if (it < 200) { b = p.Wo; o = ((xl * 8 + (it / 5) % 8) * 192 + (it / 40 + 1) * 32) * 1280 + (it % 5) * 8192 + k.l * 128; }
        else if (it < 440) { const unsigned ln = (it - 200) * 64 + k.l; b = p.Wv; o = (ln / 24) * 12288 + xl * 3072 + (ln % 24) * 128; }
        else { const unsigned ln = ((it - 440) >> 1) * 64 + k.l; b = (it & 1) ? p.Wq : p.Wk; o = (64 + ln / 24) * 12288 + xl * 3072 + (ln % 24) * 128; }
        asm volatile("global_load_lds_dword %0, %1" :: "v"(o), "s"(b) : "memory");
    }
    DRAIN();
    RAWBAR()
}

DEVI int xcc_id() { return __builtin_amdgcn_s_getreg(20 | (0 << 6) | (3 << 11)) & 7; }

__global__ __launch_bounds__(256) void init_kernel(Params p) {
    const int b = blockIdx.x, tid = threadIdx.x, l = tid & 63;
    const __amdgpu_buffer_rsrc_t rs = __builtin_amdgcn_make_buffer_rsrc((void*)p.ws, 0, 0x7fffffff, 0x00020000);
    const u32x4 ones = {0xFFFFFFFFu, 0xFFFFFFFFu, 0xFFFFFFFFu, 0xFFFFFFFFu};
    if (b < 64 && tid < 64) {
        unsigned bt[2] = {0xFFFFFFFFu, 0xFFFFFFFFu};
        if (b < NPAGES) {
            const unsigned off = ((unsigned)b << 21) + 7 * 16384 + (l & 7) * 16;
#pragma unroll 1
            for (int r = 0; r < CALR; ++r) {
#pragma unroll
                for (int hf = 0; hf < 2; ++hf) {
                    unsigned long long c0, c1;
                    asm volatile("s_memtime %0\n\ts_waitcnt lgkmcnt(0)" : "=s"(c0) :: "memory");
                    st128(rs, off + hf * 8192, 0, ones);
                    asm volatile("s_waitcnt vmcnt(0)\n\ts_memtime %0\n\ts_waitcnt lgkmcnt(0)" : "=s"(c1) :: "memory");
                    bt[hf] = min(bt[hf], (unsigned)(c1 - c0));
                }
            }
        }
        if (l == 0) ((unsigned*)(p.cm + CM_TAB))[b] = (unsigned)(bt[1] < bt[0]) ^ (unsigned)(xcc_id() >> 2);
    }
    constexpr unsigned N0 = PR_B + 4 * PR_SZ, N1 = 40 * 16384, N2 = 64 * 20480;
    for (unsigned i = (b * 256 + tid) * 16; i < N0 + N1 + N2; i += 256 * 256 * 16) {
        unsigned o;
        u32x4 v = ones;
        if (i < N0) o = i;
        else if (i < N0 + N1) o = ((unsigned)PG_YM << 21) + (i - N0);
        else {
            const unsigned j = i - N0 - N1, s = j / 20480, r = j % 20480;
            const unsigned q = r < 16384 ? r : 16384 + ((r - 16384) >> 11) * 8192 + (r & 2047);
            o = (2u << 21) + s * 32768 + q;
            if (s == 0) {
                const int e = (((q >> 14) << 13) | (q & 8191u)) >> 4, T = e >> 4, hf = (e >> 1) & 1, rr = ((e >> 2) & 3) * 2 + (e & 1);
#pragma unroll
                for (int c = 0; c < 4; ++c) v[c] = min(*(const unsigned*)(p.mem0 + rr * 640 + T * 16 + 4 * c + 2 * hf), 0xFFFFFFFEu);
            }
        }
        *(u32x4*)(p.ws + o) = v;
    }
    if (b == 255) for (unsigned o = CM_TICKET + tid * 16; o < CM_BYTES; o += 4096) *(u32x4*)(p.cm + o) = ones;
}

__global__ __launch_bounds__(256, 1) void mega_kernel(Params p) {
    extern __shared__ __attribute__((aligned(16))) char smem[];
    Ctx k;
    k.ws = p.ws; k.cm = p.cm;
    k.rs = __builtin_amdgcn_make_buffer_rsrc((void*)p.ws, 0, 0x7fffffff, 0x00020000);
    k.l = threadIdx.x & 63; k.w = __builtin_amdgcn_readfirstlane(threadIdx.x >> 6);
    RSTAMP(0)
    k.par = __ballot(((const unsigned*)(p.cm + CM_TAB))[k.l] != 0u);
    if (threadIdx.x == 0) {
        int x = xcc_id(), role = 0;
#pragma unroll 1
        for (int i = 0; i < 8; ++i) {
            const unsigned t = __hip_atomic_fetch_add((GAS unsigned*)(p.cm + CM_TICKET + x * 128), 1u, __ATOMIC_RELAXED, __HIP_MEMORY_SCOPE_AGENT) + 1u;
            if (t < 32u) { role = x * 32 + (int)t; break; }
            x = (x + 1) & 7;
        }
        *(volatile LAS int*)smem = role;
    }
    RAWBAR()
    k.role = __builtin_amdgcn_readfirstlane(*(volatile LAS int*)smem);
    RAWBAR()
    const int x = k.role >> 5, i = k.role & 31, xl = x & 3;
    k.c = x >> 2;
    if (k.c) k.par = ~k.par;
    k.p0 = k.pg(0);
    RSTAMP(1)
    if (k.c) ln_x_batch(p, k, (i * 4 + xl) * 4 + k.w);
    if (i < 8) { qk_role(p, k, smem, xl * 8 + i); return; }
    if (i < 16) { vo_role(p, k, smem, xl * 8 + i - 8); return; }
    warm(p, k, xl, (i - 16) * 4 + k.w);
    if (i < 21) { mred_role(p, k, smem, xl * 5 + i - 16); return; }
    if (i < 26) { tred_role(p, k, smem, xl * 5 + i - 21); return; }
    const int o = xl * 6 + i - 26;
    if (o < 12) up_role(p, k, smem, o);
    else if (o < 22) down_role(p, k, smem, o - 12);
    else row_role(p, k, smem, o - 22);
}

extern "C" void kernel_launch(void* const* d_in, const int* in_sizes, int n_in,
                              void* d_out, int out_size, void* d_ws, size_t ws_size,
                              hipStream_t stream) {
    Params p;
    p.x = (const u16*)d_in[0]; p.mem0 = (const u16*)d_in[1];
    p.Wq = (const u16*)d_in[2]; p.Wk = (const u16*)d_in[3]; p.Wv = (const u16*)d_in[4]; p.Wo = (const u16*)d_in[5];
    p.wg = (const u16*)d_in[8]; p.wu = (const u16*)d_in[9]; p.wd = (const u16*)d_in[10];
    p.out = (u16*)d_out; p.cm = (char*)d_ws;
    p.ws = (char*)(((uintptr_t)d_ws + CM_BYTES + ((1u << 21) - 1)) & ~(uintptr_t)((1u << 21) - 1));
    (void)hipFuncSetAttribute((const void*)mega_kernel, hipFuncAttributeMaxDynamicSharedMemorySize, LDS_BYTES);
    init_kernel<<<256, 256, 0, stream>>>(p);
    mega_kernel<<<256, 256, LDS_BYTES, stream>>>(p);
}
```

```cpp
#include <hip/hip_runtime.h>
#include <hip/hip_bf16.h>
#include <stdint.h>
#include <utility>

typedef unsigned short u16;
typedef __attribute__((ext_vector_type(16))) short bf16x16;
typedef __attribute__((ext_vector_type(8))) short bf16x8;
typedef __attribute__((ext_vector_type(4))) short bf16x4;
typedef __attribute__((ext_vector_type(8))) __bf16 hbf8;
typedef __attribute__((ext_vector_type(16))) __bf16 hbf16;
typedef __attribute__((ext_vector_type(4))) float f32x4;
typedef __attribute__((ext_vector_type(2))) float f32x2;
typedef __attribute__((ext_vector_type(8))) unsigned u32x8;
typedef __attribute__((ext_vector_type(4))) unsigned u32x4;
typedef __attribute__((ext_vector_type(2))) unsigned u32x2;
typedef __attribute__((ext_vector_type(2))) __bf16 bf16x2_t;
#define LAS __attribute__((address_space(3)))
#define GAS __attribute__((address_space(1)))
#define DEVI __device__ __forceinline__

#define PSLEEP 8
#define WD1 800
#define PACE 65
#define XPOS 62
#define YPOS 46
#define XPOSY 1
#define PWS 1
#define WUS 0
#define QDLY 300
#define CALR 3
#define SPG 3
#define DQ 12
#define DV 12
#define BSLP 6
#define SDLY 0
#define UPOS 79
#define RDLY 30
#define NOYST 0
#define NOTOK 0
#define NOYLD 0
#define GPOSY 10
#define GPOS 13

#define EXP(o) ((((unsigned)(o) >> 13) << 14) | ((unsigned)(o) & 8191u))
enum { C_XN = 0, C_Y = 1, C_X1 = 2, C_XB = 3, C_H = 4, C_X2 = 5, C_BC = 6 };
#define CNT(type, s) ((unsigned)((type) * 16384 + (s) * 128))
constexpr unsigned PR_B = EXP(65536), PR_SZ = EXP(12 * 8192);
constexpr unsigned DUMMY_B = EXP(0);
constexpr int PG_MEMF = 2;
#define YXR 8
constexpr int PG_YX = 3;
constexpr int PG_XNA = PG_YX + 2 * YXR;
constexpr int PG_X1 = PG_XNA + 4, PG_X2 = PG_X1 + 4;
constexpr int PG_H = PG_X2 + 4;
constexpr int PG_YM = PG_H + 8;
constexpr int NPAGES = PG_YM + 2;
static_assert(NPAGES <= 64, "map");
constexpr unsigned CM_TAB = 0, CM_TICKET = 1024, CM_TMO = 4096, CM_BYTES = 8192;
constexpr unsigned SPIN_CAP = 1u << 20;
#define AUX_SC1 16
#define LDS_BYTES (160 * 1024)

struct Params {
    const u16* x; const u16* mem0; const u16* Wq; const u16* Wk; const u16* Wv; const u16* Wo;
    const u16* wg; const u16* wu; const u16* wd; u16* out; char* cm; char* ws;
};
struct Ctx {
    char* ws; char* cm; __amdgpu_buffer_rsrc_t rs; unsigned long long par;
    int c, l, w, role;
    unsigned p0;
    DEVI unsigned pg(int page) const { return ((unsigned)page << 21) | (((unsigned)(par >> page) & 1u) << 13); }
    DEVI unsigned pg0(int page) const { return pg(page) ^ ((unsigned)c << 13); }
    DEVI unsigned seg(int page, int s) const { return pg(page + (s >> 4)) + (unsigned)(s & 15) * 131072u; }
    DEVI unsigned seg0(int page, int s) const { return seg(page, s) ^ ((unsigned)c << 13); }
    DEVI unsigned hseg(int s) const { return pg(PG_H + (s >> 3)) + (unsigned)(s & 7) * 262144u; }
    DEVI unsigned yx(int sg, int wq) const { return pg(PG_YX + (sg % YXR) * 2 + (wq >> 1)) + (unsigned)(wq & 1) * 1048576u; }
    DEVI unsigned ym(int s) const { return pg(PG_YM + ((s >> 1) & 1)) + (unsigned)(s & 1) * EXP(327680); }
    DEVI unsigned memf(int s) const { return pg(PG_MEMF) + (unsigned)s * 32768u; }
    DEVI char* cnt(int type, int s) const { return ws + (p0 + CNT(type, s)); }
    DEVI char* cnt0(int type, int s) const { return ws + ((p0 ^ ((unsigned)c << 13)) + CNT(type, s)); }
};

DEVI unsigned cvtpk(float a, float b) { return __builtin_bit_cast(unsigned, __builtin_convertvector((f32x2){a, b}, bf16x2_t)); }
DEVI unsigned packs(float a, float b) { return min(cvtpk(a, b), 0xFFFFFFFEu); }
DEVI float blo(unsigned x) { return __uint_as_float(x << 16); }
DEVI float bhi(unsigned x) { return __uint_as_float(x & 0xFFFF0000u); }
DEVI float bfr(float f) { return __uint_as_float(cvtpk(0.f, f)); }
DEVI u32x4 ld128(__amdgpu_buffer_rsrc_t rs, unsigned v, unsigned s) { return __builtin_amdgcn_raw_buffer_load_b128(rs, (int)v, (int)s, AUX_SC1); }
DEVI u32x2 ld64(__amdgpu_buffer_rsrc_t rs, unsigned v, unsigned s) { return __builtin_amdgcn_raw_buffer_load_b64(rs, (int)v, (int)s, AUX_SC1); }
DEVI unsigned ld32(__amdgpu_buffer_rsrc_t rs, unsigned v, unsigned s) { return __builtin_amdgcn_raw_buffer_load_b32(rs, (int)v, (int)s, AUX_SC1); }
DEVI void st128(__amdgpu_buffer_rsrc_t rs, unsigned v, unsigned s, u32x4 x) {
    __builtin_amdgcn_raw_buffer_store_b128(x, rs, (int)v, (int)s, AUX_SC1);
    asm volatile("s_nop 1" :: "v"(x) : "memory");
}
DEVI void st64(__amdgpu_buffer_rsrc_t rs, unsigned v, unsigned s, u32x2 x) { __builtin_amdgcn_raw_buffer_store_b64(x, rs, (int)v, (int)s, AUX_SC1); }
DEVI void st32(__amdgpu_buffer_rsrc_t rs, unsigned v, unsigned s, unsigned x) { __builtin_amdgcn_raw_buffer_store_b32(x, rs, (int)v, (int)s, AUX_SC1); }
#define RAWBAR() { __builtin_amdgcn_s_waitcnt(0xc07f); __builtin_amdgcn_s_barrier(); }
#define DRAIN() asm volatile("s_waitcnt vmcnt(0)" ::: "memory")
#define SCHED0() __builtin_amdgcn_sched_barrier(0)
#define OOB 0x80000000u
DEVI int opq(int x) { asm volatile("" : "+v"(x)); return x; }
#define LANE const int l = opq(k.l), g = l >> 4, n = l & 15; (void)g; (void)n;
DEVI unsigned umax3(unsigned a, unsigned b, unsigned c) { return max(max(a, b), c); }
DEVI unsigned umax4(u32x4 v) { return max(umax3(v[0], v[1], v[2]), v[3]); }
DEVI unsigned rclk() { return (unsigned)__builtin_amdgcn_s_memrealtime(); }
DEVI unsigned long long rt_async() { unsigned long long t; asm volatile("s_memrealtime %0" : "=s"(t)); return t; }
DEVI void lgkm0(unsigned long long& a) { asm volatile("s_waitcnt lgkmcnt(0)" : "+s"(a) :: "memory"); }
DEVI unsigned spack(unsigned lo, unsigned hi) { unsigned d; asm volatile("s_pack_ll_b32_b16 %0, %1, %2" : "=s"(d) : "s"(lo), "s"(hi)); return d; }
DEVI void quit_if(int s, int last) {
    asm volatile("s_cmp_lg_u32 %0, %1\n\ts_cbranch_scc1 .Lgo%=\n\ts_endpgm\n.Lgo%=:" :: "s"(s), "n"(63) : "scc", "memory");
}
DEVI void sleep_ticks(int d) { unsigned target = rclk() + d; while ((int)(rclk() - target) < 0) __builtin_amdgcn_s_sleep(4); }
DEVI float xor16max(float x) { auto r = __builtin_amdgcn_permlane16_swap(__float_as_uint(x), __float_as_uint(x), false, false); return __builtin_fmaxf(__uint_as_float(r[0]), __uint_as_float(r[1])); }
DEVI float xor32max(float x) { auto r = __builtin_amdgcn_permlane32_swap(__float_as_uint(x), __float_as_uint(x), false, false); return __builtin_fmaxf(__uint_as_float(r[0]), __uint_as_float(r[1])); }
DEVI float xor16add(float x) { auto r = __builtin_amdgcn_permlane16_swap(__float_as_uint(x), __float_as_uint(x), false, false); return __uint_as_float(r[0]) + __uint_as_float(r[1]); }
DEVI float xor32add(float x) { auto r = __builtin_amdgcn_permlane32_swap(__float_as_uint(x), __float_as_uint(x), false, false); return __uint_as_float(r[0]) + __uint_as_float(r[1]); }
DEVI float fold32(float a, float b) { auto r = __builtin_amdgcn_permlane32_swap(__float_as_uint(a), __float_as_uint(b), false, false); return __uint_as_float(r[0]) + __uint_as_float(r[1]); }
#define ROR16(x, n) __builtin_bit_cast(float, __builtin_amdgcn_update_dpp(0, __builtin_bit_cast(int, (x)), 0x120 + (n), 0xf, 0xf, false))
DEVI float row16sum(float x) { x += ROR16(x, 8); x += ROR16(x, 4); x += ROR16(x, 2); x += ROR16(x, 1); return x; }
DEVI unsigned z0() { unsigned z; asm volatile("v_mov_b32 %0, 0" : "=v"(z)); return z; }
DEVI void cnt_add(char* p) { __hip_atomic_fetch_add((GAS unsigned*)(p + z0()), z0() + 1u, __ATOMIC_RELAXED, __HIP_MEMORY_SCOPE_AGENT); }
DEVI unsigned cnt_get(char* p) { return __hip_atomic_load((GAS unsigned*)(p + z0()), __ATOMIC_RELAXED, __HIP_MEMORY_SCOPE_AGENT) + 1u; }
DEVI bool spin_fail(const Ctx& k, unsigned spins) {
    if (spins > SPIN_CAP || ((spins & 255u) == 255u && __builtin_amdgcn_readfirstlane(cnt_get(k.cm + CM_TMO)) != 0)) { cnt_add(k.cm + CM_TMO); return true; }
    return false;
}
DEVI void cnt_wait(const Ctx& k, char* c, unsigned need) {
_Pragma("unroll 1")
    for (unsigned spins = 0; (unsigned)__builtin_amdgcn_readfirstlane(cnt_get(c)) < need; ++spins) {
        __builtin_amdgcn_s_sleep(PSLEEP);
        if (spin_fail(k, spins)) break;
    }
    __builtin_amdgcn_fence(__ATOMIC_ACQUIRE, "wavefront");
}
DEVI unsigned xn_get(const Ctx& k, unsigned l16, int s) { return ld32(k.rs, l16 & (k.c ? 128u : 0u), k.p0 + CNT(C_XN, k.c ? (s & 1) * 2 : s & 63)); }
DEVI bool xn_has(const Ctx& k, unsigned v, int s) { return __all((int)v >= (k.c ? s : 7)); }
DEVI void xn_wait(const Ctx& k, unsigned l16, int s) {
_Pragma("unroll 1")
    for (unsigned spins = 0; !xn_has(k, xn_get(k, l16, s), s); ++spins) {
        asm volatile("" ::: "memory");
        __builtin_amdgcn_s_sleep(2);
        if (spin_fail(k, spins)) break;
    }
    __builtin_amdgcn_fence(__ATOMIC_ACQUIRE, "wavefront");
}
template <int N> DEVI void prog_wait(const Ctx& k, unsigned a, int s) {
_Pragma("unroll 1")
    for (unsigned spins = 0; !__all((int)ld32(k.rs, min(k.l, N - 1) * 128, a) >= s); ++spins) {
        asm volatile("" ::: "memory");
        __builtin_amdgcn_s_sleep(PWS);
        if (spin_fail(k, spins)) break;
    }
    __builtin_amdgcn_fence(__ATOMIC_ACQUIRE, "wavefront");
}
template <int OFF> DEVI void dma1(const void* sbase, unsigned voff, unsigned lds) {
    asm volatile("s_add_i32 m0, %2, %3\n\ts_nop 0\n\tglobal_load_lds_dwordx4 %0, %1" :: "v"(voff), "s"(sbase), "s"(lds), "n"(OFF) : "memory", "scc");
}
DEVI void set_m0(unsigned lds) { asm volatile("s_mov_b32 m0, %0" :: "s"(lds) : "memory"); }
template <int OFF> DEVI void dmab(__amdgpu_buffer_rsrc_t rs, unsigned voff, unsigned soff) {
    asm volatile("buffer_load_dwordx4 %0, %1, %2 offen offset:%3 sc1 lds" :: "v"(voff), "s"(rs), "s"(soff), "n"(OFF) : "memory");
}
#define PIN(x) asm volatile("" : "+v"(x))
#define PINS(x) asm volatile("" : "+s"(x))
DEVI float csel(float a, float b, unsigned long long m) { float d; asm volatile("v_cndmask_b32_e64 %0, %1, %2, %3" : "=v"(d) : "v"(a), "v"(b), "s"(m)); return d; }
#define LO8(x) __builtin_shufflevector(x, x, 0, 1, 2, 3, 4, 5, 6, 7)
#define HI8(x) __builtin_shufflevector(x, x, 8, 9, 10, 11, 12, 13, 14, 15)
#define CAT8(a, b) __builtin_shufflevector(a, b, 0, 1, 2, 3, 4, 5, 6, 7, 8, 9, 10, 11, 12, 13, 14, 15)
template <bool AG> DEVI void smf(f32x4& c, const bf16x8& a, const bf16x16& w, unsigned ix) {
    if constexpr (AG) asm volatile("v_smfmac_f32_16x16x64_bf16 %0, %1, %2, %3" : "+v"(c) : "v"(a), "a"(w), "v"(ix));
    else asm volatile("v_smfmac_f32_16x16x64_bf16 %0, %1, %2, %3" : "+v"(c) : "v"(a), "v"(w), "v"(ix));
}
template <bool AG, bool FIRST> DEVI void mfa(f32x4& c, const bf16x8& w, const bf16x8& x) {
    if constexpr (AG && FIRST) asm volatile("v_mfma_f32_16x16x32_bf16 %0, %1, %2, 0" : "=&v"(c) : "a"(w), "v"(x));
    else if constexpr (AG) asm volatile("v_mfma_f32_16x16x32_bf16 %0, %1, %2, %0" : "+v"(c) : "a"(w), "v"(x));
    else if constexpr (FIRST) asm volatile("v_mfma_f32_16x16x32_bf16 %0, %1, %2, 0" : "=&v"(c) : "v"(w), "v"(x));
    else asm volatile("v_mfma_f32_16x16x32_bf16 %0, %1, %2, %0" : "+v"(c) : "v"(w), "v"(x));
}
template <bool AG, bool FIRST> DEVI void mfb(f32x4& c, const bf16x8& x, const bf16x8& w) {
    if constexpr (AG && FIRST) asm volatile("v_mfma_f32_16x16x32_bf16 %0, %1, %2, 0" : "=&v"(c) : "v"(x), "a"(w));
    else if constexpr (AG) asm volatile("v_mfma_f32_16x16x32_bf16 %0, %1, %2, %0" : "+v"(c) : "v"(x), "a"(w));
    else if constexpr (FIRST) asm volatile("v_mfma_f32_16x16x32_bf16 %0, %1, %2, 0" : "=&v"(c) : "v"(x), "v"(w));
    else asm volatile("v_mfma_f32_16x16x32_bf16 %0, %1, %2, %0" : "+v"(c) : "v"(x), "v"(w));
}
DEVI f32x4 mf(const bf16x8& a, const bf16x8& b, const f32x4& c) { return __builtin_amdgcn_mfma_f32_16x16x32_bf16(__builtin_bit_cast(hbf8, a), __builtin_bit_cast(hbf8, b), c, 0, 0, 0); }
template <int N, class F> DEVI void sfor(F f) { [&]<int... I>(std::integer_sequence<int, I...>) { (f(std::integral_constant<int, I>{}), ...); }(std::make_integer_sequence<int, N>{}); }
DEVI bf16x8 lds128(const char* p) { return *(const bf16x8*)p; }
DEVI bf16x4 ldstr(const char* p) { return __builtin_amdgcn_ds_read_tr16_b64_v4i16((LAS bf16x4*)p); }
DEVI bf16x8 ldstr2(const char* p, int d) { bf16x4 a = ldstr(p), b = ldstr(p + d); return __builtin_shufflevector(a, b, 0, 1, 2, 3, 4, 5, 6, 7); }

#define STAMP(s, k)
#define QSTAMP(f, i)
#define STOP(s)
#define PFLUSH(s)
#define PROF_DECL
#define RSTAMP(i)
#define PCOUNT(i)
#define PROF_OUT

template <int NT, int OFF> DEVI void wp_issue(const u16* W, int ld, unsigned ring, int l) {
    sfor<NT>([&](auto I) { constexpr int i = decltype(I)::value;
        const int id = i * 64 + l, row = id / (2 * NT), cc = id % (2 * NT);
        dma1<OFF + i * 1024>(W, (unsigned)(row * ld + cc * 8) * 2u, ring); });
}
template <int NT, bool STD> DEVI bf16x8 wp_read(const char* slot, int T, int l) {
    const int row = (STD ? 8 : 4) * (l >> 4) + ((l >> 2) & 3);
    return ldstr2(slot + row * (NT * 32) + T * 32 + (l & 3) * 8, (STD ? 4 : 16) * NT * 32);
}
template <int NU, int NI, int D, class ISSUE, class READ> DEVI void wstream(char* ring, ISSUE issue, READ read) {
    const unsigned base = (unsigned)(size_t)(LAS char*)ring;
    sfor<D>([&](auto U) { constexpr int u = decltype(U)::value; if constexpr (u < NU) issue(U, base); });
    sfor<NU>([&](auto U) {
        constexpr int u = decltype(U)::value, left = (NU - u < D ? NU - u : D) - 1;
        asm volatile("s_waitcnt vmcnt(%0)" :: "n"(left * NI) : "memory");
        read(U, ring + (u % D) * NI * 1024);
        __builtin_amdgcn_s_waitcnt(0xc07f);
        asm volatile("" ::: "memory");
        if constexpr (u + D < NU) issue(std::integral_constant<int, u + D>{}, base);
    });
}

constexpr int L_XN = 0;
constexpr int L_K = 40960;
constexpr int L_MEM = L_K + 12288;
constexpr int L_QM = L_MEM + 10240;
constexpr int L_Q = L_QM + 12288;
constexpr int L_W = L_Q + 12288;
#define IC(x) std::integral_constant<int, (x)>{}
#define CV(X) decltype(X)::value
DEVI bf16x8 lr128(unsigned a) { return *(const LAS bf16x8*)(size_t)a; }
DEVI bf16x4 lrtr(unsigned a) { return __builtin_amdgcn_ds_read_tr16_b64_v4i16((LAS bf16x4*)(size_t)a); }
#define CAT4(a, b) __builtin_shufflevector(a, b, 0, 1, 2, 3, 4, 5, 6, 7)
DEVI void lw128(unsigned a, u32x4 v) { *(LAS u32x4*)(size_t)a = v; }
DEVI void lw64(unsigned a, u32x2 v) { *(LAS u32x2*)(size_t)a = v; }
DEVI void lw32(unsigned a, unsigned v) { *(LAS unsigned*)(size_t)a = v; }
template <int N> DEVI void bar_lgkm() { asm volatile("s_waitcnt lgkmcnt(%0)\n\ts_barrier" :: "n"(N) : "memory"); }
DEVI void atom1(__amdgpu_buffer_rsrc_t rs, unsigned voff, unsigned soff) {
    unsigned one; asm volatile("v_mov_b32 %0, 1\n\tbuffer_atomic_add %0, %1, %2, %3 offen" : "=&v"(one) : "v"(voff), "s"(rs), "s"(soff) : "memory");
}
DEVI unsigned vadd(unsigned v, unsigned sc) { unsigned d; asm volatile("v_add_u32 %0, %1, %2" : "=v"(d) : "s"(sc), "v"(v)); return d; }
DEVI float vzero() { float z; asm volatile("v_mov_b32 %0, 0" : "=v"(z)); return z; }
template <int B, int I> DEVI void xn_op(const Ctx& k, unsigned v, unsigned src, unsigned dst) {
    if constexpr (I == 0) set_m0(dst + B * 8192);
    else if constexpr (I == 1) dmab<0>(k.rs, v, src + B * 16384);
    else dmab<1024>(k.rs, v, src + B * 16384);
}
DEVI void xn_all(const Ctx& k, unsigned v, unsigned src, unsigned dst) {
    sfor<5>([&](auto B) { xn_op<CV(B), 0>(k, v, src, dst); asm volatile("s_nop 0"); xn_op<CV(B), 1>(k, v, src, dst); xn_op<CV(B), 2>(k, v, src, dst); });
}
struct Xn {
    unsigned src, dst, fa, fi, fx, sg, need, t, sb, v, c, off;
    unsigned long long flip, m;
    DEVI void init(const Ctx& k) {
        src = k.seg(PG_XNA, 0) + k.w * 2048; dst = L_XN + k.w * 2048; sg = 0; off = OOB;
        fa = k.p0 + CNT(C_XN, 0); fi = k.c ? 0u : 128u; fx = k.c ? 256u : 0u;
        const unsigned q = (unsigned)(k.par >> PG_XNA), d = q ^ (q >> 1);
        flip = ((unsigned long long)(d & 1) << 16) | ((unsigned long long)(d & 2) << 31) | ((unsigned long long)(d & 4) << 46);
    }
    template <int I> DEVI void next(const Ctx& k, unsigned l16) {
        if constexpr (I == 0) { sg += 1; src += 131072; PINS(sg); PINS(src); }
        if constexpr (I == 1) asm volatile("s_bitcmp1_b64 %1, %2\n\ts_cselect_b32 %0, 0x2000, 0" : "=s"(t) : "s"(flip), "s"(sg) : "scc");
        if constexpr (I == 2) { src ^= t; fa += fi; PINS(src); PINS(fa); }
        if constexpr (I == 3) { fa ^= fx; PINS(fa); asm volatile("v_and_b32 %0, %1, %2" : "=v"(c) : "s"(fx >> 1), "v"(l16)); }
        if constexpr (I == 4) c = ld32(k.rs, c, fa);
        if constexpr (I == 5) { need = k.c ? sg : 7u; PINS(need); }
        if constexpr (I == 6) { need = sg < 64u ? need : 0x7FFFFFFFu; PINS(need); }
        if constexpr (I == 7) asm volatile("v_cmp_gt_i32_e64 %0, %1, %2" : "=s"(m) : "s"(need), "v"(c) : "memory");
        if constexpr (I == 8) { off = m != 0 ? OOB : 0u; PINS(off); }
        if constexpr (I == 9) asm volatile("v_or_b32 %0, %1, %2" : "=v"(v) : "s"(off), "v"(l16));
        if constexpr (I == 10) { off = sg < 64u ? off : 0u; PINS(off); }
    }
    template <int B, int I> DEVI void op(const Ctx& k) {
        if constexpr (I == 0) { set_m0(dst + B * 8192); sb = src + B * 16384; PINS(sb); }
        else dmab<(I - 1) * 1024>(k.rs, v, sb);
    }
    template <int NB> DEVI void late(const Ctx& k, unsigned l16) {
        xn_wait(k, l16, sg);
        v = l16;
        sfor<NB>([&](auto B) { op<CV(B), 0>(k); asm volatile("s_nop 0"); op<CV(B), 1>(k); op<CV(B), 2>(k); });
        DRAIN();
    }
};
template <int OFF> DEVI u32x4 ald128(__amdgpu_buffer_rsrc_t rs, unsigned v, unsigned s) { u32x4 d; asm volatile("buffer_load_dwordx4 %0, %1, %2, %3 offen offset:%4 sc1" : "=v"(d) : "v"(v), "s"(rs), "s"(s), "n"(OFF) : "memory"); return d; }
struct Gather {
    u32x4 p0, p1, v0, v1;
    unsigned gw, mx, a;
    unsigned long long bad;
    unsigned b, base;
    DEVI void init(const Ctx& k) { base = k.memf(0); }
    template <int I> DEVI void op(const Ctx& k, unsigned l16, int s) {
        if constexpr (I == 0) { a = ((unsigned)s << 15) & 0x1F8000u; PINS(a); }
        if constexpr (I == 1) { a += base; b = a + 6144 + (k.w >> 1) * 8192; PINS(a); PINS(b); }
        if constexpr (I == 2) gw = vadd(l16, k.w * 1024);
        if constexpr (I == 3) p0 = ald128<0>(k.rs, l16, a);
        if constexpr (I == 4) v0 = ald128<2048>(k.rs, gw, a);
        if constexpr (I == 5) v1 = ald128<0>(k.rs, gw, b);
        if constexpr (I == 6) p1 = ald128<1024>(k.rs, l16, a);
    }
    template <int N> DEVI void wait() { asm volatile("s_waitcnt vmcnt(%4)" : "+v"(p0), "+v"(p1), "+v"(v0), "+v"(v1) : "n"(N) : "memory"); }
    template <int I> DEVI void fin() {
#define MX2(A, B, C, D) asm volatile("v_max3_u32 %0, %0, %1, %2\n\tv_max3_u32 %0, %0, %3, %4" : "+v"(mx) : "v"(A), "v"(B), "v"(C), "v"(D))
        if constexpr (I == 0) { lw128(gw + L_MEM + 2048, v0); asm volatile("v_max3_u32 %0, %1, %2, %3\n\tv_max3_u32 %0, %0, %4, %5" : "=&v"(mx) : "v"(p0[0]), "v"(p0[1]), "v"(p0[2]), "v"(p0[3]), "v"(p1[0])); }
        if constexpr (I == 1) { lw128(gw + L_MEM + 6144, v1); MX2(p1[1], p1[2], p1[3], v0[0]); }
        if constexpr (I == 2) MX2(v0[1], v0[2], v0[3], v1[0]);
        if constexpr (I == 3) MX2(v1[1], v1[2], v1[3], v1[3]);
        if constexpr (I == 4) asm volatile("v_cmp_eq_u32_e64 %0, -1, %1" : "=s"(bad) : "v"(mx));
    }
    DEVI void again(const Ctx& k, unsigned l16) {
        asm volatile("buffer_load_dwordx4 %0, %4, %5, %6 offen sc1\n\tbuffer_load_dwordx4 %1, %4, %5, %6 offen offset:1024 sc1\n\tbuffer_load_dwordx4 %2, %7, %5, %6 offen offset:2048 sc1\n\tbuffer_load_dwordx4 %3, %7, %5, %8 offen sc1\n\ts_waitcnt vmcnt(0)"
            : "+v"(p0), "+v"(p1), "+v"(v0), "+v"(v1) : "v"(l16), "s"(k.rs), "s"(a), "v"(gw), "s"(b) : "memory");
        fin<0>(); fin<1>(); fin<2>(); fin<3>(); fin<4>();
        asm volatile("s_nop 3");
    }
};
DEVI void clr2(f32x4& c) { f32x2 a, b; asm volatile("v_mov_b64 %0, 0\n\tv_mov_b64 %1, 0" : "=v"(a), "=v"(b)); c = __builtin_shufflevector(a, b, 0, 1, 2, 3); }
#define NOP7_3(a) asm volatile("s_nop 7" : "+v"(a[0]), "+v"(a[1]), "+v"(a[2]))

struct G3 {
    f32x4 a0[3], a1[3];
    bf16x8 x0[20], x1[20], wl[20];
    unsigned l16, lwv;
    template <int KS> DEVI void r0() { x0[KS] = lr128(l16 + L_XN + KS * 2048); }
    template <int KS> DEVI void r1() { x1[KS] = lr128(l16 + L_XN + KS * 2048 + 1024); }
    template <int KS> DEVI void rw() { if constexpr (KS >= 4) wl[KS] = lr128(lwv + (KS - 4) * 1024); }
    template <int ID0, bool WL, int ks, int m> DEVI void mm(const bf16x16 (&W)[52]) {
        constexpr int b = ks >> 1, t = m % 3, id = ID0 + t * 10 + b;
        f32x4& c = m < 3 ? a0[t] : a1[t];
        const bf16x8& x = m < 3 ? x0[ks] : x1[ks];
        if constexpr (ID0 == 30) {
            if constexpr (WL && t == 2 && b >= 2) mfb<false, false>(c, x, wl[ks]);
            else if constexpr (ks & 1) mfb<(id < 32), false>(c, x, HI8(W[id % 52]));
            else mfb<(id < 32), ks == 0>(c, x, LO8(W[id % 52]));
        } else {
            if constexpr (WL && t == 2 && b >= 2) mfa<false, false>(c, wl[ks], x);
            else if constexpr (ks & 1) mfa<(id < 32), false>(c, HI8(W[id % 52]), x);
            else mfa<(id < 32), ks == 0>(c, LO8(W[id % 52]), x);
        }
        SCHED0();
    }
    template <int ID0, bool WL, int K0, int K1, int PF, bool H6 = false, class HOOK> DEVI void run(const bf16x16 (&W)[52], HOOK hook) {
        sfor<K1 - K0>([&](auto KK) {
            constexpr int kk = CV(KK), ks = K0 + kk;
            sfor<6>([&](auto M) {
                constexpr int m = CV(M);
                mm<ID0, WL, ks, m>(W);
                if constexpr (ks + 2 < PF) {
                    if constexpr (m == 0) r0<(ks + 2) % 20>();
                    if constexpr (m == 1) r1<(ks + 2) % 20>();
                    if constexpr (m == 3 && WL) rw<(ks + 2) % 20>();
                }
                if constexpr (H6) hook(IC(kk * 6 + m));
                else { hook(IC(kk * 7 + (m < 5 ? m : 6))); if constexpr (m == 4) hook(IC(kk * 7 + 5)); }
                SCHED0();
            });
        });
    }
    u32x2 o;
    template <int I, int OFF> DEVI void out(unsigned d0, unsigned d1) {
        if constexpr (I >= 0 && I < 18) {
            constexpr int t = I / 6, rt = (I / 3) & 1;
            const f32x4& a = rt ? a1[t] : a0[t];
            if constexpr (I % 3 == 0) { o[0] = cvtpk(a[0], a[1]); PIN(o[0]); }
            else if constexpr (I % 3 == 1) { o[1] = cvtpk(a[2], a[3]); PIN(o[1]); }
            else if constexpr (OFF > 0) lw64(d0 + (OFF - 1) + rt * 6144 + t * 1024, o);
            else lw64((t == 1 ? d1 : d0) + OFF + rt * 6144 + (t == 2 ? 1024 : 0), o);
        }
    }
};
#define SPIX(l) (((l) & 2) ? 0xEEEEu : 0x4444u)
template <int I> DEVI void fold_op(f32x4& a, unsigned& o) {
    if constexpr (I == 0) asm volatile("v_add_f32 %0, %0, %1" : "+v"(a[0]) : "v"(a[2]));
    if constexpr (I == 1) asm volatile("v_add_f32 %0, %0, %1" : "+v"(a[1]) : "v"(a[3]));
    if constexpr (I == 2) asm volatile("v_cvt_pk_bf16_f32 %0, %1, %2" : "=v"(o) : "v"(a[0]), "v"(a[1]));
}

#define ANNOUNCE(s, now, d) (((unsigned)(s) << 16) | (((unsigned)(now) + (d)) & 0xFFFFu))

struct Soft {
    unsigned long long mA, mY, mC[4], mM, mD;
    unsigned v0, v1;
    float a[6], m, ml, M, t, u, sc; unsigned pk[3], msk;
    template <int I> DEVI void op(const Ctx& k, unsigned a0, unsigned a1, unsigned ph) {
#define A3(o) "+v"(a[o]), "+v"(a[o + 1]), "+v"(a[o + 2])
#define FMA(x) "v_fmamk_f32 " x ", " x ", 0x3dd53b95, "
        if constexpr (I == 5) asm volatile("v_mov_b32 %0, 0xf1800000" : "=v"(t));
        if constexpr (I == 6) asm volatile("v_cndmask_b32_e64 %0, %3, %0, %4\n\tv_cndmask_b32_e64 %1, %3, %1, %5\n\tv_cndmask_b32_e64 %2, %3, %2, %6" : A3(0) : "v"(t), "s"(mC[0]), "s"(mC[1]), "s"(mC[2]));
        if constexpr (I == 7) asm volatile("v_cndmask_b32_e64 %0, %3, %0, %4\n\tv_cndmask_b32_e64 %1, %3, %1, %5\n\tv_cndmask_b32_e64 %2, %3, %2, %5" : A3(3) : "v"(t), "s"(mC[3]), "s"(mM));
        if constexpr (I == 8) asm volatile("v_max3_f32 %0, %1, %2, %3\n\tv_max3_f32 %0, %0, %4, %5\n\tv_max_f32 %0, %0, %6" : "=&v"(m) : "v"(a[0]), "v"(a[1]), "v"(a[2]), "v"(a[3]), "v"(a[4]), "v"(a[5]));
        if constexpr (I == 9) asm volatile("v_mul_f32 %0, 0xbdd53b95, %3\n\t" FMA("%1") "%0\n\t" FMA("%2") "%0" : "=&v"(ml), "+v"(a[0]), "+v"(a[1]) : "v"(m));
        if constexpr (I == 10) asm volatile("v_max_f32_dpp %0, %3, %3 row_ror:8 row_mask:0xf bank_mask:0xf\n\t" FMA("%1") "%4\n\tv_cndmask_b32_e64 %2, %3, %0, %5" : "=&v"(t), "+v"(a[2]), "=&v"(M) : "v"(m), "v"(ml), "s"(mD));
        if constexpr (I == 11) asm volatile("v_mov_b32 %0, %3\n\tv_exp_f32 %1, %1\n\t" FMA("%2") "%4" : "=&v"(u), "+v"(a[0]), "+v"(a[3]) : "v"(M), "v"(ml));
        if constexpr (I == 12) asm volatile("v_permlane16_swap_b32 %0, %1\n\t" FMA("%2") "%3" : "+v"(M), "+v"(u), "+v"(a[4]) : "v"(ml));
        if constexpr (I == 13) asm volatile("v_max_f32 %0, %0, %1\n\tv_mov_b32 %1, %0\n\tv_exp_f32 %2, %2" : "+v"(M), "+v"(u), "+v"(a[1]));
        if constexpr (I == 14) asm volatile(FMA("%2") "%3\n\tv_permlane32_swap_b32 %0, %1" : "+v"(M), "+v"(u), "+v"(a[5]) : "v"(ml));
        if constexpr (I == 15) asm volatile("v_max_f32 %0, %0, %1\n\tv_exp_f32 %2, %2" : "+v"(M), "+v"(u), "+v"(a[2]));
        if constexpr (I == 16) asm volatile("v_sub_f32 %0, %2, %0\n\tv_mul_f32 %0, 0x3dd53b95, %0\n\tv_exp_f32 %1, %1" : "+v"(M), "+v"(a[3]) : "v"(m));
        if constexpr (I == 17) asm volatile("v_exp_f32 %0, %2\n\tv_exp_f32 %1, %1" : "=&v"(sc), "+v"(a[4]) : "v"(M));
        if constexpr (I == 18) asm volatile("v_exp_f32 %2, %2\n\tv_add_f32 %0, %3, %4\n\tv_add_f32 %1, %5, %6" : "=&v"(u), "=&v"(t), "+v"(a[5]) : "v"(a[0]), "v"(a[1]), "v"(a[2]), "v"(a[3]));
        if constexpr (I == 19) asm volatile("v_add_f32 %0, %0, %2\n\tv_add_f32 %1, %1, %3\n\tv_add_f32 %0, %0, %1" : "+v"(u), "+v"(t) : "v"(a[4]), "v"(a[5]));
        if constexpr (I == 20) asm volatile("v_mul_f32 %0, %0, %2\n\tv_mov_b32 %1, 0x7fff7fff" : "+v"(u), "=v"(msk) : "v"(sc));
        if constexpr (I == 21) asm volatile("v_add_f32_dpp %1, %0, %0 row_ror:8 row_mask:0xf bank_mask:0xf\n\tv_cndmask_b32_e64 %0, %0, %1, %2\n\tv_mov_b32 %1, %0" : "+v"(u), "=&v"(t) : "s"(mD));
        if constexpr (I == 22) asm volatile("s_nop 0\n\tv_permlane16_swap_b32 %0, %1\n\tv_add_f32 %0, %0, %1\n\tv_mov_b32 %1, %0" : "+v"(u), "+v"(t));
        if constexpr (I == 23) asm volatile("s_nop 0\n\tv_permlane32_swap_b32 %0, %1\n\tv_add_f32 %0, %0, %1\n\tv_rcp_f32 %0, %0" : "+v"(u), "+v"(t));
        if constexpr (I == 24) asm volatile("v_mul_f32 %0, %0, %3\n\tv_mul_f32 %1, %1, %0\n\tv_mul_f32 %2, %2, %0" : "+v"(u), "+v"(a[0]), "+v"(a[1]) : "v"(sc));
        if constexpr (I == 25) asm volatile("v_mul_f32 %0, %0, %3\n\tv_mul_f32 %1, %1, %3\n\tv_mul_f32 %2, %2, %3" : A3(2) : "v"(u));
        if constexpr (I == 26) asm volatile("v_mul_f32 %2, %2, %3\n\tv_cvt_pk_bf16_f32 %0, %4, %5\n\tv_cvt_pk_bf16_f32 %1, %6, %7" : "=&v"(pk[0]), "=&v"(pk[1]), "+v"(a[5]) : "v"(u), "v"(a[0]), "v"(a[1]), "v"(a[2]), "v"(a[3]));
        if constexpr (I == 27) asm volatile("v_cvt_pk_bf16_f32 %2, %3, %4\n\tv_bfi_b32 %0, %5, %0, %6\n\tv_bfi_b32 %1, %5, %1, %6" : "+v"(pk[0]), "+v"(pk[1]), "=&v"(pk[2]) : "v"(a[4]), "v"(a[5]), "v"(msk), "s"(ph));
        if constexpr (I == 28) { asm volatile("v_bfi_b32 %0, %1, %0, %2" : "+v"(pk[2]) : "v"(msk), "s"(ph)); st64(k.rs, v0, a0, (u32x2){pk[0], pk[1]}); }
        if constexpr (I == 29) st32(k.rs, v1, a1, pk[2]);
    }
};
#define QIDX(t, b) ((t) * 10 + (b))
DEVI void qk_role(const Params& p, const Ctx& k, char* smem, int h) {
    PROF_DECL
    const int w = k.w;
    sleep_ticks(k.c ? WD1 + QDLY : QDLY);
    bf16x16 W[52];
    {
        const int l = k.l;
        bf16x8 lo[6], wt[16];
        const int col = h * 192 + w * 48;
        wstream<20, 6, 6>(smem + w * 36864,
            [&](auto U, unsigned sl) { constexpr int u = CV(U);
                wp_issue<3, (u % 6) * 6144>(p.Wk + (size_t)u * 32 * 6144 + col, 6144, sl, l); wp_issue<3, (u % 6) * 6144 + 3072>(p.Wq + (size_t)u * 32 * 6144 + col, 6144, sl, l); },
            [&](auto U, char* sp) { constexpr int u = CV(U), b = u >> 1;
#pragma unroll
                for (int t = 0; t < 6; ++t) {
                    bf16x8 f = wp_read<3, true>(sp + (t / 3) * 3072, t % 3, l);
                    if (t == 5 && b >= 2) wt[(u - 4) & 15] = f;
                    else if (u & 1) W[QIDX(t, b) % 52] = CAT8(lo[t], f);
                    else lo[t] = f;
                } });
        RAWBAR()
#pragma unroll
        for (int i = 0; i < 16; ++i) *(bf16x8*)(smem + L_W + w * 16384 + i * 1024 + l * 16) = wt[i];
    }
    RSTAMP(2)
    Gather G;
    Soft S;
    const int hw = h >> 3, hh = h & 7;
    unsigned l16, ix, dqm, dq, trp, qp, vz;
    {
        const int l = k.l, g = l >> 4, n = l & 15, r = n & 7, u = w & 1;
        const bool hf = n >= 8, dup = w != 1;
        S.mA = __ballot(dup && hf); S.mD = __ballot(dup);
        const int lim = w == 1 ? n - 4 * g : (w == 0 || !hf) ? 64 : 8 * u + r - 4 * g;
#pragma unroll
        for (int i = 0; i < 4; ++i) S.mC[i] = __ballot(i <= lim);
        const bool okm = !(dup && hf);
        S.mY = __ballot((dup && hf) == (w != 0)); S.mM = __ballot(okm);
        S.v0 = w == 0 ? hh * 512 + ((g >> 1) * 16 + (r >> 1) * 4 + hf * 2 + (r & 1)) * 16 + (g & 1) * 8 : w == 1 ? hh * 512 + l * 8 : hh * 1024 + (g * 16 + 8 * u + r) * 16 + hf * 8;
        S.v1 = !okm ? OOB : w == 0 ? 4096 + hh * 128 + r * 16 + g * 4 : (w == 1 ? 4096 + n * 8 : 6144 + (8 * u + r) * 8) + hh * 256 + (g >> 1) * 128 + (g & 1) * 4;
        l16 = l * 16; ix = SPIX(l);
        dqm = L_QM + (w >> 1) * 3072 + u * 512 + (g >> 1) * 128 + n * 8 + (g & 1) * 4;
        dq = L_QM + 6144 + (w >> 1) * 3072 + u * 512 + g * 128 + ((n * 8) ^ ((g >> 1) * 64));
        trp = L_QM + 6144 + (l & 3) * 128 + (((l >> 2) * 8) ^ ((l & 2) * 32));
        qp = w == 1 ? trp + 6144 : (w == 0 ? L_QM : L_Q + 6144 + u * 256) + (l & 1) * 128 + (((l >> 2) * 8) ^ (w == 3 ? 64 : 0));
        vz = l == 0 ? 0u : OOB;
        PIN(S.v0); PIN(S.v1); PIN(l16); PIN(ix); PIN(dqm); PIN(dq); PIN(trp); PIN(qp); PIN(vz);
    }
    const unsigned po0 = k.p0 + PR_B + hw * 49152 + (w == 0 ? 0 : w == 1 ? 32768 : 16384), po1 = k.p0 + PR_B + hw * 49152 + (w == 0 ? 0 : 32768);
    const unsigned pdum = k.pg(1) + DUMMY_B, pbc = w == 0 ? k.p0 + CNT(C_BC, hw * 16 + hh) : pdum + 65536 + h * 512 + w * 128;
    Xn X;
    X.init(k); G.init(k);
    unsigned long long now = 0, done = 0;
    unsigned a0 = pdum + (po0 - k.p0 - PR_B), a1 = pdum + (po1 - k.p0 - PR_B), ph = 0;
    G3 A, B;
    A.l16 = l16; B.l16 = l16; B.lwv = vadd(l16, L_W + w * 16384);
#pragma unroll
    for (int t = 0; t < 3; ++t) { B.a0[t] = (f32x4){0.f, 0.f, 0.f, 0.f}; B.a1[t] = B.a0[t]; }
    f32x4 ac[6];
#pragma unroll
    for (int t = 0; t < 6; ++t) clr2(ac[t]);
    sfor<7>([&](auto I) { G.op<CV(I)>(k, l16, 0); });
    for (int s = -1; s < 64; ++s) {
        if (X.off != 0) { PCOUNT(1) X.late<4>(k, l16); }
        bf16x8 k0[6], k1[6];
        {
            bf16x8 m[10], wa[10], wb[10];
            unsigned fo[6];
            G.wait<4>();
            STOP(s) STAMP(s, 0)
            sfor<6>([&](auto I) { constexpr int i = CV(I), t = (i + 5) % 6, id = QIDX(t, 0);
                smf<(id < 32)>(ac[t], __builtin_bit_cast(bf16x8, G.p0), W[id], ix);
                SCHED0();
                G.fin<i>();
                SCHED0(); });
            if (G.bad != 0) {
                unsigned spins = 0;
_Pragma("unroll 1")
                do { PCOUNT(0) G.again(k, l16); } while (G.bad != 0 && !spin_fail(k, spins++));
                STOP(s) STAMP(s, 0)
#pragma unroll
                for (int t = 0; t < 6; ++t) clr2(ac[t]);
                asm volatile("s_nop 1" : "+v"(ac[0]), "+v"(ac[1]), "+v"(ac[2]), "+v"(ac[3]), "+v"(ac[4]), "+v"(ac[5]));
                sfor<6>([&](auto I) { constexpr int t = (CV(I) + 5) % 6, id = QIDX(t, 0);
                    smf<(id < 32)>(ac[t], __builtin_bit_cast(bf16x8, G.p0), W[id], ix); });
            }
            asm volatile("s_waitcnt lgkmcnt(0)\n\ts_barrier" : "+s"(done) :: "memory");
            unsigned an = (unsigned)done - (unsigned)now;
            PINS(an);
            now = rt_async();
            m[1] = __builtin_bit_cast(bf16x8, G.p1);
            m[2] = lr128(l16 + L_MEM + 2048);
            SCHED0();
            auto fin = [&](auto J) { constexpr int j = CV(J), t = 5 - j / 3;
                if constexpr (j >= 0 && j < 18) fold_op<j % 3>(ac[t], fo[t]); };
            auto put = [&](auto T) { constexpr int t = CV(T); if constexpr (t < 3) lw32(dq + t * 1024, fo[t]); else lw32(dqm + (t - 3) * 1024, fo[t]); };
            sfor<54>([&](auto I) {
                constexpr int i = CV(I) + 6, u = i - 42, b = i < 42 ? i / 6 : 7 + u % 3, t = b == 2 ? i % 6 : i < 42 ? (i + 5) % 6 : 5 - u / 3, id = QIDX(t, b);
                if constexpr (i == 44) lgkm0(now);
                if constexpr (t == 5 && b >= 2) smf<false>(ac[5], m[b], CAT8(wa[b], wb[b]), ix);
                else smf<(id < 32)>(ac[t], m[b], W[id % 52], ix);
                SCHED0();
                if constexpr (i < 36) {
                    constexpr int bb = i / 6 + 2;
                    if constexpr (i % 6 == 0) m[bb] = lr128(l16 + L_MEM + bb * 1024);
                    if constexpr (i % 6 == 3) wa[bb] = lr128(B.lwv + (bb - 2) * 2048);
                    if constexpr (i % 6 == 4) wb[bb] = lr128(B.lwv + (bb - 2) * 2048 + 1024);
                }
                if constexpr (i == 7) wa[2] = lr128(B.lwv);
                if constexpr (i == 8) wb[2] = lr128(B.lwv + 1024);
                if constexpr (i == 32) m[8] = lr128(l16 + L_MEM + 8192);
                if constexpr (i == 31) m[9] = lr128(l16 + L_MEM + 9216);
                if constexpr (i == 36) wa[8] = lr128(B.lwv + 12288);
                if constexpr (i == 37) wb[8] = lr128(B.lwv + 13312);
                if constexpr (i == 38) wa[9] = lr128(B.lwv + 14336);
                if constexpr (i == 39) wb[9] = lr128(B.lwv + 15360);
                if constexpr (i == 46) st32(k.rs, vz, pbc, fo[0]);
                B.out<i - 9, 6145>(dq, dq);
                if constexpr (i == 29) { an = min(an, 200u) + DQ; PINS(an); }
                if constexpr (i == 44) an = spack(an + (unsigned)now, s);
                if constexpr (i == 45) { fo[0] = an; PIN(fo[0]); }
                if constexpr (u >= 6 && u % 3 != 2) {
                    constexpr int r = (u / 3 - 2) * 2 + u % 3, f = (r + 6) / 2 % 4;
                    if constexpr (r < 2) A.r0<r>(); else if constexpr (r & 1) k1[f] = lr128(l16 + L_K + (6 + f) * 1024); else k0[f] = lr128(l16 + L_K + f * 1024);
                }
                if constexpr (u >= 8 && u % 3 == 2) put(IC(5 - (u - 8) / 3));
                if constexpr (u >= 5) fin(IC(u - 5));
                if constexpr (i == 6) { QSTAMP(1, 1) }
                if constexpr (i == 12) { QSTAMP(1, 2) }
                if constexpr (i == 41) { QSTAMP(1, 3) }
                if constexpr (i == 59) { QSTAMP(1, 4) }
                SCHED0();
            });
            sfor<9>([&](auto I) { constexpr int i = CV(I);
                A.mm<0, false, i / 3, i % 3>(W);
                if constexpr (i == 0) { A.r0<2>(); fin(IC(13)); }
                if constexpr (i == 1) { fin(IC(14)); put(IC(1)); }
                if constexpr (i == 2) { fin(IC(15)); fin(IC(16)); }
                if constexpr (i == 3) { fin(IC(17)); put(IC(0)); }
                if constexpr (i == 4) A.r0<3>();
                if constexpr (i == 5) A.r0<4>();
                if constexpr (i >= 6) X.op<4, i % 3>(k);
                SCHED0(); });
        }
        asm volatile("s_waitcnt vmcnt(3) lgkmcnt(2)\n\ts_barrier" ::: "memory");
        QSTAMP(0, 1) QSTAMP(1, 5)
        f32x4 sc[3];
        {
            bf16x4 q[12], km[12];
            auto rd = [&](auto R) { constexpr int r = CV(R);
                constexpr int tab[34] = {0, 1, 2, 3, 12, 13, 4, 5, 14, 15, 6, 7, 24, 25, 16, 17, 30, 8, 9, 26, 27, 18, 19, 10, 11, 28, 29, 20, 21, 22, 23, 31, 32, 33};
                constexpr int c = tab[r % 34];
                if constexpr (r >= 34) { }
                else if constexpr (c < 12) q[c] = lrtr(qp + c * 512);
                else if constexpr (c < 24) km[c - 12] = lrtr(trp + (c - 12) * 512);
                else if constexpr (c < 30) { if constexpr (c & 1) k1[3 + (c - 24) / 2] = lr128(l16 + L_K + (9 + (c - 24) / 2) * 1024); else k0[3 + (c - 24) / 2] = lr128(l16 + L_K + (3 + (c - 24) / 2) * 1024); }
                else if constexpr (c == 30) A.r0<5>();
                else A.r1<(c - 31) % 20>(); };
            rd(IC(0)); rd(IC(1));
            SCHED0();
            sfor<27>([&](auto I) { constexpr int i = CV(I);
                constexpr int seq[27] = {-9, -10, -11, -12, -13, -14, 0, 1, 3, 4, 2, 6, 7, 5, 9, 10, 8, -15, 12, 13, 11, -16, 15, 16, 14, -17, 17};
                constexpr int c = seq[i], ks = (c < 0 ? -c : c) / 3, j = (c < 0 ? -c : c) % 3;
                if constexpr (c < 0) A.mm<0, false, ks, j>(W);
                else mfa<false, ks == 0>(sc[j], j == 0 ? k0[ks] : j == 1 ? k1[ks] : CAT4(km[2 * ks], km[2 * ks + 1]), CAT4(q[2 * ks], q[2 * ks + 1]));
                SCHED0();
                constexpr int r0 = 2 + i + (i + 2) / 3, r1 = 2 + (i + 1) + (i + 3) / 3;
                sfor<r1 - r0>([&](auto J) { rd(IC(r0 + CV(J))); });
                if constexpr (i == 25) S.op<5>(k, 0, 0, 0);
                SCHED0(); });
        }
        QSTAMP(0, 2)
        {
            unsigned d0;
            float y0, y1;
            auto soft = [&](auto I) { constexpr int i = CV(I);
                if constexpr (i == 0) { asm volatile("" : "+v"(sc[0]), "+v"(sc[1])); y0 = csel(sc[0][2], sc[1][0], S.mY); y1 = csel(sc[0][3], sc[1][1], S.mY); S.a[0] = csel(sc[0][0], y0, S.mA); }
                if constexpr (i == 1) { S.a[1] = csel(sc[0][1], y1, S.mA); S.a[2] = csel(y0, sc[1][2], S.mA); S.a[3] = csel(y1, sc[1][3], S.mA); }
                if constexpr (i == 3) { asm volatile("" : "+v"(sc[2])); S.a[4] = sc[2][0]; S.a[5] = sc[2][1]; }
                if constexpr (i >= 2) S.op<i + 4>(k, a0, a1, ph);
                if constexpr (i == 26) done = rt_async();
                if constexpr (i == 27) quit_if(s, 63); };
            sfor<18>([&](auto I) { constexpr int i = CV(I);
                A.mm<0, false, i / 3, 3 + i % 3>(W);
                if constexpr (i == 2 || i == 5 || i == 8) A.r1<3 + i / 3>();
                if constexpr (i == 10) A.r0<6>();
                if constexpr (i == 12) A.r1<6>();
                if constexpr (i == 14) A.r0<7>();
                if constexpr (i == 17) A.r1<7>();
                soft(I);
                SCHED0(); });
            A.run<0, false, 6, 20, 20, true>(W, [&](auto I) { constexpr int i = CV(I);
                soft(IC(18 + i));
                if constexpr (i == 8) { QSTAMP(0, 3) }
                if constexpr (i == 40) asm volatile("s_waitcnt vmcnt(2)\n\ts_barrier" ::: "memory");
                if constexpr (i == 15) { a0 = (X.sg & 3) * PR_SZ; PINS(a0); }
                if constexpr (i == 16) { a1 = a0 + po1; a0 += po0; PINS(a0); PINS(a1); }
                if constexpr (i == 21) { ph = (X.sg & 4) ? 0x80008000u : 0u; PINS(ph); }
                if constexpr (i == 22 || i == 27) G.op<i / 27>(k, l16, X.sg);
                if constexpr (i >= 45 && i < 64 && (i % 6 == 3 || i % 6 == 4)) X.next<(i / 6 - 7) * 2 + i % 6 - 3>(k, l16);
                if constexpr (i == 81) { d0 = vadd(l16, L_K + (w >> 1) * 3072 + (w & 1) * 8); B.lwv = vadd(l16, L_W + w * 16384); }
                if constexpr (i == 72) B.r0<0>();
                if constexpr (i == 73) B.r1<0>();
                if constexpr (i == 78) B.r0<1>();
                if constexpr (i == 79) B.r1<1>(); });
            B.run<30, true, 0, 20, 20, true>(W, [&](auto I) { constexpr int i = CV(I);
                if constexpr (i < 36 && i % 6 != 0 && i % 6 != 1 && i % 6 != 3) A.out<(i / 6) * 3 + (i % 6) / 2 - 1 + (i % 6) / 5, 1>(d0, d0);
                if constexpr (i == 38 || i == 40) X.next<7 + (i - 38) / 2>(k, l16);
                if constexpr (i == 44 || i == 46) X.next<9 + (i - 44) / 2>(k, l16);
                if constexpr (i == 47 || i == 95) __builtin_amdgcn_s_barrier();
                if constexpr (i % 48 < 18 && i >= 48 && (i % 6 == 2 || i % 6 == 4)) X.op<(i / 48 - 1) * 2 + ((i % 48) / 3) / 3, ((i % 48) / 3) % 3>(k);
                if constexpr (i / 6 >= GPOS && i / 6 < GPOS + 3 && (i % 6 == 2 || i % 6 == 4) && i != GPOS * 6 + 2) G.op<1 + (i / 6 - GPOS) * 2 + (i % 6) / 4>(k, l16, 0);
                if constexpr (i >= 113 && i < 119) clr2(ac[(i + 6 - 113 + 5) % 6]); });
            static_assert(GPOS >= 11 && GPOS <= 13, "GPOS");
            QSTAMP(0, 4) QSTAMP(0, 5)
            PFLUSH(s)
        }
    }
    PROF_OUT
}

#define OIDX(t, b) (22 + (t) * 3 + (b))
DEVI void vo_role(const Params& p, const Ctx& k, char* smem, int h) {
    PROF_DECL
    const int w = k.w;
    if (k.c) sleep_ticks(WD1);
    bf16x16 W[52];
    {
        const int l = k.l;
        bf16x8 lo[10], wt[16];
        const u16* wv = p.Wv + h * 192 + w * 48;
        const u16* wo = p.Wo + (size_t)h * 192 * 640 + w * 160;
        wstream<22, 6, 6>(smem + w * 36864,
            [&](auto U, unsigned sl) { constexpr int u = CV(U);
                if constexpr (u >= 12) { wp_issue<3, (u % 6) * 6144>(wv + (size_t)(u - 12) * 64 * 6144, 6144, sl, l); wp_issue<3, (u % 6) * 6144 + 3072>(wv + (size_t)((u - 12) * 64 + 32) * 6144, 6144, sl, l); }
                else { wp_issue<5, (u % 6) * 6144>(wo + (u >> 1) * 32 * 640 + (u & 1) * 80, 640, sl, l); dma1<(u % 6) * 6144 + 5120>(wo, l * 16, sl); } },
            [&](auto U, char* sp) { constexpr int u = CV(U);
                if constexpr (u >= 12) {
                    constexpr int b = u - 12;
#pragma unroll
                    for (int t = 0; t < 3; ++t) {
                        bf16x8 x = wp_read<3, true>(sp, t, l), y = wp_read<3, true>(sp + 3072, t, l);
                        if (t == 2 && b >= 2) { wt[(2 * (b - 2)) & 15] = x; wt[(2 * (b - 2) + 1) & 15] = y; } else W[(t * 10 + b) % 52] = CAT8(x, y);
                    }
                } else {
                    constexpr int ks = u >> 1, b = ks >> 1;
#pragma unroll
                    for (int i = 0; i < 5; ++i) {
                        const int t = (u & 1) * 5 + i;
                        bf16x8 f = wp_read<5, false>(sp, i, l);
                        if (ks & 1) W[OIDX(t, b)] = CAT8(lo[t], f); else lo[t] = f;
                    }
                } });
        RAWBAR()
#pragma unroll
        for (int i = 0; i < 16; ++i) *(bf16x8*)(smem + L_W + w * 16384 + i * 1024 + l * 16) = wt[i];
    }
    RSTAMP(2)
    unsigned l16, lwv, ix, dq0, trp, vo, vsig;
    {
        const int l = k.l, g = l >> 4, n = l & 15, q = (l >> 2) & 3, pp = l & 3;
        l16 = l * 16; lwv = l16 + L_W + w * 16384; ix = SPIX(l);
        dq0 = L_QM + (24 * w + (n >> 2) * 2 + (n & 1)) * 32 + g * 8 + ((n >> 1) & 1) * 4;
        trp = L_QM + (16 * (g >> 1) + 4 * (g & 1) + 8 * (q >> 1) + (q & 1)) * 32 + pp * 8;
        vo = EXP(h * 512) + (g >> 1) * 256 + n * 16 + (g & 1) * 8;
        vsig = l == 0 ? 0u : OOB;
        PIN(l16); PIN(lwv); PIN(ix); PIN(dq0); PIN(trp); PIN(vo); PIN(vsig);
    }
    const unsigned pdum = k.pg(1) + DUMMY_B, pw = pdum + 98304 + h * 512 + w * 128, pbc = w == 0 ? k.p0 + CNT(C_BC, (h >> 3) * 16 + 8 + (h & 7)) : pw;
    Gather G;
    G3 V;
    V.l16 = l16; V.lwv = lwv;
    unsigned long long now = 0, done = 0;
    f32x4 yl = {0.f, 0.f, 0.f, 0.f};
    u32x2 lo = {0u, 0u};
    u32x4 o;
    unsigned yb = k.yx(7, w) + EXP(h * 1024);
    auto pack = [&](const f32x4& y) { o = (u32x4){lo[0], lo[1], cvtpk(y[0], y[1]), cvtpk(y[2], y[3])}; PIN(o); };
    Xn X;
    X.init(k); G.init(k);
    X.late<5>(k, l16);
    RAWBAR()
    const unsigned ypar = (unsigned)(k.par >> (PG_YX + (w >> 1))), yo = ((unsigned)(PG_YX + (w >> 1)) << 21) + (w & 1) * 1048576 + EXP(h * 1024);
    const unsigned ym0 = k.ym(0) + w * 163840, ym2 = k.ym(2) + w * 163840;
    unsigned ym = pdum + w * 163840, ys, t;
    V.r0<0>(); V.r1<0>(); V.r0<1>(); V.r1<1>();
    for (int s = -1; s < 64; ++s) {
        f32x4 ac[3];
        SCHED0();
        V.run<0, true, 0, 16, 17, true>(W, [&](auto I) { constexpr int i = CV(I);
            if constexpr (i == 2) pack(yl);
            if constexpr (i == 4) __builtin_amdgcn_raw_buffer_store_b128(o, k.rs, (int)l16, (int)yb, AUX_SC1);
            if constexpr (i == 8) { t = (X.sg & 7) * 2; PINS(t); }
            if constexpr (i == 10) { yb = (ypar >> t) & 1; PINS(yb); }
            if constexpr (i == 14) { t = (t << 21) + yo; PINS(t); }
            if constexpr (i == 16) { yb = (yb << 13) + t; PINS(yb); }
            if constexpr (i == 22) { t = max(s, 0); PINS(t); }
            if constexpr (i == 26 || i == 28) G.op<i / 28>(k, l16, t);
            if constexpr (i == 32 || i == 34 || i == 38) X.next<(i - 31) / 3>(k, l16);
            if constexpr (i == 5) asm volatile("" :: "v"(o));
            if constexpr (i == YPOS) { asm volatile("s_waitcnt vmcnt(0)\n\ts_barrier" ::: "memory"); G.mx = s; PIN(G.mx); }
            if constexpr (i == YPOS + 4) st32(k.rs, vsig, pbc + 4, G.mx);
            if constexpr (i / 6 >= GPOSY && i / 6 < GPOSY + 3 && (i % 6 == 2 || i % 6 == 4) && i != GPOSY * 6 + 2) G.op<1 + (i / 6 - GPOSY) * 2 + (i % 6) / 4>(k, l16, 0);
            if constexpr (i >= 92 && i < 95) clr2(ac[(i - 92) % 3]); });
        static_assert(GPOSY * 6 + 8 > YPOS + 4 && GPOSY <= 13, "GPOSY");
        STAMP(s, 5)
        PFLUSH(s - 1)
        f32x4 acc[10];
        f32x2 z[20];
        auto clr = [&](auto I) { constexpr int i = CV(I); asm volatile("v_mov_b64 %0, 0" : "=v"(z[i])); if constexpr (i & 1) acc[i >> 1] = __builtin_shufflevector(z[i - 1], z[i], 0, 1, 2, 3); };
        bf16x4 al[3], ah[3];
        unsigned an, d0, d1;
        {
            bf16x8 m[10], wa[10], wb[10];
            auto first = [&](auto I) { constexpr int i = CV(I); smf<true>(ac[i % 3], __builtin_bit_cast(bf16x8, i < 3 ? G.p0 : G.p1), W[(i % 3) * 10 + i / 3], ix); };
            G.wait<0>();
            STOP(s) STAMP(s, 0)
            sfor<6>([&](auto I) { constexpr int i = CV(I);
                first(I);
                SCHED0();
                if constexpr (i == 2) wa[2] = lr128(lwv);
                if constexpr (i == 3) wb[2] = lr128(lwv + 1024);
                G.fin<i>();
                SCHED0(); });
            if (G.bad != 0) {
                unsigned spins = 0;
_Pragma("unroll 1")
                do { PCOUNT(0) G.again(k, l16); } while (G.bad != 0 && !spin_fail(k, spins++));
                STOP(s) STAMP(s, 0)
                clr2(ac[0]); clr2(ac[1]); clr2(ac[2]);
                asm volatile("s_nop 1" : "+v"(ac[0]), "+v"(ac[1]), "+v"(ac[2]));
                sfor<6>(first);
                asm volatile("s_waitcnt lgkmcnt(0)" ::: "memory");
            }
            bar_lgkm<2>();
            an = (unsigned)done - (unsigned)now;
            PINS(an);
            now = rt_async();
            m[2] = lr128(l16 + L_MEM + 2048);
            auto rd = [&](auto R) { constexpr int r = CV(R), b = 3 + r / 3;
                if constexpr (b < 10) {
                    if constexpr (r % 3 == 0) wa[b] = lr128(lwv + (b - 2) * 2048);
                    if constexpr (r % 3 == 1) wb[b] = lr128(lwv + (b - 2) * 2048 + 1024);
                    if constexpr (r % 3 == 2) m[b] = lr128(l16 + L_MEM + b * 1024);
                } else if constexpr (b < 13) {
                    if constexpr (r % 3 == 0) V.r0<b + 7>();
                    if constexpr (r % 3 == 1) V.rw<b + 7>();
                    if constexpr (r % 3 == 2) V.r1<b + 7>();
                } };
            SCHED0();
            V.run<0, true, 16, 17, 0, true>(W, [&](auto I) { constexpr int i = CV(I); if constexpr (i < 3) rd(I); if constexpr (i == 5) { rd(IC(5)); rd(IC(3)); }
                if constexpr (i == 3) { an = min(an, 200u) + DV; PINS(an); } });
            sfor<24>([&](auto I) {
                constexpr int i = CV(I), b = 2 + i / 3, t = (i + 2) % 3;
                if constexpr (t == 2) smf<false>(ac[2], m[b], CAT8(wa[b], wb[b]), ix); else smf<true>(ac[t], m[b], W[t * 10 + b], ix);
                SCHED0();
                if constexpr (i == 0) rd(IC(4));
                if constexpr (i < 21) rd(IC(6 + i));
                if constexpr (i == 22) d0 = vadd(l16, L_K + ((3 * w) >> 1) * 1024 + ((3 * w) & 1) * 8);
                if constexpr (i == 23) d1 = vadd(l16, L_K + ((3 * w + 1) >> 1) * 1024 + ((3 * w + 1) & 1) * 8);
                SCHED0();
            });
            unsigned fo[3];
            V.run<0, true, 17, 19, 0, true>(W, [&](auto I) { constexpr int i = CV(I);
                if constexpr (i < 3) rd(IC(27 + i));
                if constexpr (i >= 1 && i < 10) fold_op<(i - 1) % 3>(ac[((i + 5) / 3) % 3], fo[((i + 5) / 3) % 3]);
                if constexpr (i == 3 || i == 6 || i == 9) lw32(dq0 + ((i / 3 + 1) % 3) * 256, fo[(i / 3 + 1) % 3]);
                if constexpr (i == 4 || i == 5 || i == 7 || i == 8) clr(IC((i / 7) * 2 + i % 3 - 1));
                if constexpr (i >= 10) clr2(acc[i - 8]); });
        }
        lgkm0(now);
        __builtin_amdgcn_s_barrier();
        STAMP(s, 1)
        {
            unsigned fo[2];
            bf16x8 v[12];
            u32x2 pk[10];
            auto fin = [&](auto Q) { constexpr int tp = CV(Q) / 11, j = CV(Q) % 11;
                if constexpr (tp >= 5) { }
                else if constexpr (j < 6) fold_op<j / 2>(acc[2 * tp + (j & 1)], fo[j & 1]);
                else if constexpr (j < 8) asm volatile("v_min_u32 %0, -2, %0" : "+v"(fo[j & 1]));
                else if constexpr (j == 8) { ys = ym + tp * 32768; PINS(ys); }
                else if constexpr (j == 9) asm volatile("v_permlane16_swap_b32 %0, %1" : "+v"(fo[0]), "+v"(fo[1]));
                else st64(k.rs, vo, ys, (u32x2){fo[0], fo[1]}); };
            al[0] = lrtr(trp); ah[0] = lrtr(trp + 64);
            SCHED0();
            V.run<0, true, 19, 20, 0, true>(W, [&](auto I) { constexpr int i = CV(I);
                if constexpr (i == 0) al[1] = lrtr(trp + 1024);
                if constexpr (i == 1) ah[1] = lrtr(trp + 1088);
                if constexpr (i == 2) al[2] = lrtr(trp + 2048);
                if constexpr (i == 3) ah[2] = lrtr(trp + 2112);
                if constexpr (i == 0) { an += (unsigned)now; PINS(an); }
                if constexpr (i == 1) an = spack(an, s);
                if constexpr (i == 2) { fo[0] = an; PIN(fo[0]); }
                if constexpr (i == 3) st32(k.rs, vsig, pbc, fo[0]);
                if constexpr (i >= 4) clr2(acc[i]); });
            sfor<30>([&](auto I) { constexpr int i = CV(I), b = (i % 6) >> 1, t = 2 * (i / 6) + (i & 1), id = OIDX(t, b);
                smf<(id < 32)>(acc[t], CAT4(al[b], ah[b]), W[id], ix);
                SCHED0();
                V.out<i - 1, 0>(d0, d1);
                if constexpr ((i % 3 == 0 && i < 21) || i == 19) clr(IC(12 + (i == 19 ? 7 : i / 3)));
                if constexpr (i == 21) bar_lgkm<0>();
                if constexpr (i >= 21 && i < 27) v[i - 21] = lr128(l16 + L_K + (i - 21) * 1024);
                if constexpr (i >= 20 && i < 29) fin(IC(i - 20));
                if constexpr (i == 28 || i == 29) X.next<i - 25>(k, l16);
                SCHED0(); });
            STAMP(s, 2)
            f32x4 y[20];
            sfor<120>([&](auto I) {
                constexpr int i = CV(I), rt = i / 60, t = (i / 6) % 10, ks = i % 6, u = i / 6, id = OIDX(t, ks >> 1);
                if constexpr (ks & 1) mfb<(id < 32), false>(y[u], v[rt * 6 + ks], HI8(W[id]));
                else mfb<(id < 32), ks == 0>(y[u], v[rt * 6 + ks], LO8(W[id]));
                SCHED0();
                if constexpr (i < 32) { constexpr int q0 = 9 + (3 * i) / 2, q1 = 9 + (3 * i + 3) / 2; sfor<q1 - q0>([&](auto J) { fin(IC(q0 + CV(J))); }); }
                if constexpr (i == 32) done = rt_async();
                if constexpr (i == 31) quit_if(s, 63);
                if constexpr (u > 0 && u <= 10 && (ks == 2 || ks == 4)) { pk[u - 1][ks >> 2] = cvtpk(y[u - 1][ks - 2], y[u - 1][ks - 1]); PIN(pk[u - 1][ks >> 2]); }
                if constexpr (u > 10 && ks == 2) pack(y[u - 1]);
                if constexpr (u > 10 && ks == 3) __builtin_amdgcn_raw_buffer_store_b128(o, k.rs, (int)l16, (int)yb, AUX_SC1);
                if constexpr (u > 10 && ks == 4) { yb += 65536; PINS(yb); asm volatile("" :: "v"(o)); }
                if constexpr (u >= 10 && ks == 5) { u32x2 d; asm volatile("v_mov_b64 %0, %1" : "=v"(d) : "v"(pk[u % 10])); lo = d; }
                if constexpr (i == 33 || i == 35 || i == 36 || i == 37 || i == 39 || i == 41) X.next<(i == 33 ? 5 : i < 38 ? i - 29 : i == 39 ? 9 : 10)>(k, l16);
                if constexpr (i >= 40 && i < 65 && i % 5 != 1 && i % 5 != 3) X.op<(i / 5 - 8) % 5, (i % 5) / 2>(k);
                if constexpr (i == 66) { ym = (s & 2) ? ym0 : ym2; PINS(ym); }
                if constexpr (i == 67) { ys = (s & 2) ? ym2 : ym0; PINS(ys); }
                if constexpr (i == 72) { ys += EXP(327680); PINS(ys); }
                if constexpr (i == 73) { ym = (s & 1) ? ym : ys; PINS(ym); }
                if constexpr (i >= 46 && i < 60 && (i % 5 == 1 || i % 5 == 3)) v[6 + (i - 46) / 5 * 2 + (i % 5) / 3] = lr128(l16 + L_K + (6 + (i - 46) / 5 * 2 + (i % 5) / 3) * 1024);
                if constexpr (i == 60) { STAMP(s, 3) }
                if constexpr (i == 106) {
                    if (X.off != 0) { PCOUNT(1) X.late<5>(k, l16); }
                    asm volatile("s_waitcnt vmcnt(7)\n\ts_barrier" ::: "memory");
                    lgkm0(done);
                }
                if constexpr (i == 108) V.r0<0>();
                if constexpr (i == 109) V.r1<0>();
                if constexpr (i == 114) V.r0<1>();
                if constexpr (i == 115) V.r1<1>();
                SCHED0();
            });
            yl = y[19];
            STAMP(s, 4)
        }
    }
    PFLUSH(63)
    PROF_OUT
}

DEVI void ln_store(const Ctx& k, const float (&v)[40], unsigned dst, int row  ) {
    f32x2 s2[4] = {{0.f, 0.f}, {0.f, 0.f}, {0.f, 0.f}, {0.f, 0.f}};
#pragma unroll
    for (int i = 0; i < 40; i += 2) s2[(i >> 1) & 3] += (f32x2){v[i], v[i + 1]};
    const f32x2 st = (s2[0] + s2[1]) + (s2[2] + s2[3]);
    float m = row16sum(st[0] + st[1]) * (1.f / 640.f);
    f32x2 q2[4] = {{0.f, 0.f}, {0.f, 0.f}, {0.f, 0.f}, {0.f, 0.f}};
#pragma unroll
    for (int i = 0; i < 40; i += 2) { const f32x2 d = (f32x2){v[i], v[i + 1]} - (f32x2){m, m}; q2[(i >> 1) & 3] += d * d; }
    const f32x2 qt = (q2[0] + q2[1]) + (q2[2] + q2[3]);
    const float q = row16sum(qt[0] + qt[1]);
    const float var = bfr(q * (1.f / 640.f));
    m = bfr(m);
    const float rden = __builtin_amdgcn_rcpf(bfr(__builtin_amdgcn_sqrtf(bfr(var + 1e-5f))));
    const int n = k.l & 15;
    const unsigned vo = (n >> 2) * 2048 + (row >> 4) * 1024 + ((n & 3) * 16 + (row & 15)) * 16;
#pragma unroll
    for (int c = 0; c < 5; ++c) {
        u32x4 o;
#pragma unroll
        for (int i = 0; i < 4; ++i) { unsigned d = cvtpk(v[c * 8 + 2 * i] - m, v[c * 8 + 2 * i + 1] - m); o[i] = cvtpk(blo(d) * rden, bhi(d) * rden); }
        st128(k.rs, vo, dst + c * 16384, o);
    }
}
DEVI void unpack8(float* v, u32x4 a) {
#pragma unroll
    for (int i = 0; i < 4; ++i) { v[2 * i] = blo(a[i]); v[2 * i + 1] = bhi(a[i]); }
}
DEVI void ln_x_batch(const Params& p, const Ctx& k, int bw) {
    const int row = bw * 4 + (k.l >> 4), sg = bw >> 3;
    float v[40];
#pragma unroll
    for (int c = 0; c < 5; ++c) unpack8(v + c * 8, *(const u32x4*)(p.x + (size_t)row * 640 + (c * 16 + (k.l & 15)) * 8));
    ln_store(k, v, k.seg0(PG_XNA, sg), row & 31);
    DRAIN();
    if (k.l == 0) cnt_add(k.cnt0(C_XN, sg));
}

DEVI void row_role(const Params& p, const Ctx& k, char* smem, int rt) {
    PROF_DECL
    const int l = k.l, n = l & 15;
    const unsigned fo = k.w * 2048 + rt * 1024;
    RSTAMP(2)
    for (int sg = k.c; sg < 64; sg += 2) {
        prog_wait<5>(k, k.pg0(0) + CNT(C_X2, rt * 5), sg);
        STOP(sg) STAMP(sg, 0)
        const unsigned src = k.seg0(PG_X2, sg) + fo, dst = (k.seg0(PG_XNA, sg) ^ 8192u) + fo;
        u32x4 a[5];
_Pragma("unroll 1")
        for (unsigned spins = 0;; ++spins) {
            unsigned mx = 0;
#pragma unroll
            for (int c = 0; c < 5; ++c) a[c] = ld128(k.rs, l * 16, src + c * 16384);
#pragma unroll
            for (int c = 0; c < 5; ++c) mx = max(mx, umax4(a[c]));
            if (__all(mx != 0xFFFFFFFFu) || spin_fail(k, spins)) break;
            PCOUNT(0)
            asm volatile("" ::: "memory");
        }
        STAMP(sg, 1)
        f32x2 v[20], s2[4], q2[4];
#pragma unroll
        for (int i = 0; i < 20; ++i) { v[i] = (f32x2){blo(a[i >> 2][i & 3]), bhi(a[i >> 2][i & 3])}; s2[i & 3] = i < 4 ? v[i] : s2[i & 3] + v[i]; }
        const f32x2 st = (s2[0] + s2[1]) + (s2[2] + s2[3]);
        const float sm = xor32add(xor16add(st[0] + st[1])), mu = sm * (1.f / 160.f);
#pragma unroll
        for (int i = 0; i < 20; ++i) { const f32x2 d = v[i] - mu; q2[i & 3] = i < 4 ? d * d : __builtin_elementwise_fma(d, d, q2[i & 3]); }
        const f32x2 qt = (q2[0] + q2[1]) + (q2[2] + q2[3]);
        *(f32x2*)(smem + n * 32 + k.w * 8) = (f32x2){sm, xor32add(xor16add(qt[0] + qt[1]))};
        RAWBAR()
        const f32x4 e0 = *(const f32x4*)(smem + n * 32), e1 = *(const f32x4*)(smem + n * 32 + 16);
        float m = ((e0[0] + e0[2]) + (e1[0] + e1[2])) * (1.f / 640.f);
        const f32x4 d = (f32x4){e0[0], e0[2], e1[0], e1[2]} - 160.f * m;
        const float var = bfr((((e0[1] + e0[3]) + (e1[1] + e1[3])) + ((d[0] * d[0] + d[1] * d[1]) + (d[2] * d[2] + d[3] * d[3])) * (1.f / 160.f)) * (1.f / 640.f));
        m = bfr(m);
        const float rden = __builtin_amdgcn_rcpf(bfr(__builtin_amdgcn_sqrtf(bfr(var + 1e-5f))));
#pragma unroll
        for (int c = 0; c < 5; ++c) {
            u32x4 o;
#pragma unroll
            for (int i = 0; i < 4; ++i) { const f32x2 x = v[c * 4 + i] - m; o[i] = cvtpk(bfr(x[0]) * rden, bfr(x[1]) * rden); }
            st128(k.rs, l * 16, dst + c * 16384, o);
        }
        STAMP(sg, 2)
        DRAIN();
        __builtin_amdgcn_s_barrier();
        st32(k.rs, (k.w == 0 && l == 0) ? 0u : OOB, (k.pg0(0) ^ 8192u) + CNT(C_XN, k.c * 2 + rt), (unsigned)sg);
        STAMP(sg, 3) STAMP(sg, 4) STAMP(sg, 5)
        PFLUSH(sg) PFLUSH(sg ^ 1)
    }
    PROF_OUT
}

struct Red {
    u32x4 Y[16];
    DEVI void yload(const Ctx& k, int j, int sg) {
        const unsigned a = k.yx(sg, j / 5) + (j % 5) * 131072 + k.w * 16384;
#pragma unroll
        for (int i = 0; i < 16; ++i) Y[i] = ld128(k.rs, k.l * 16 + (i & 3) * 1024, a + ((i >> 2) & 1) * 4096 + (i >> 3) * 65536);
    }
};
DEVI unsigned y_get(const Ctx& k) { return ld32(k.rs, (k.l & 7) * 128, k.p0 + CNT(C_BC, k.w * 16 + 8) + 4); }
DEVI void y_wait(const Ctx& k, int s) {
_Pragma("unroll 1")
    for (unsigned spins = 0; !__all((int)y_get(k) >= s); ++spins) {
        asm volatile("" ::: "memory");
        __builtin_amdgcn_s_sleep(2);
        if (spin_fail(k, spins)) break;
    }
    __builtin_amdgcn_fence(__ATOMIC_ACQUIRE, "wavefront");
}
DEVI void wait_until(unsigned t) { while ((int)((rclk() - t) << 16) < 0) { if (WUS) __builtin_amdgcn_s_sleep(WUS); } }
#define DPPMAX(x, c) x = max(x, __builtin_amdgcn_update_dpp(0, x, c, 0xf, 0xf, false))
#define WAIT_BC(s) { \
    unsigned bc; \
    _Pragma("unroll 1") for (;; ++spins) { \
        bc = ld32(k.rs, (l & 15) * 128, k.p0 + CNT(C_BC, w * 16)); \
        DRAIN(); \
        if (__all(((int)bc >> 16) >= s)) break; \
        __builtin_amdgcn_s_sleep(BSLP); \
        if (spin_fail(k, spins)) break; \
    } \
    const unsigned now = rclk(); \
    int d = ((int)bc >> 16) > s ? 0 : (int)((bc - now) << 16); \
    DPPMAX(d, 0xB1); DPPMAX(d, 0x4E); DPPMAX(d, 0x141); \
    tq = now + (__builtin_amdgcn_readlane(d, 0) >> 16); tv = now + (__builtin_amdgcn_readlane(d, 8) >> 16); }
DEVI void mred_role(const Params& p, const Ctx& k, char* smem, int j) {
    PROF_DECL
    const int l = k.l, w = k.w, g = l >> 4, n = l & 15;
    const unsigned ix = SPIX(l);
    const unsigned vp = (g * 16 + ((n >> 1) & 1) * 8 + (n >> 2) * 2 + (n & 1)) * 16;
    const unsigned v16 = (g >> 1) * 1024 + (g & 1) * 256 + n * 16, yo = j * 32768 + EXP(w * 4096);
    const u32x4 ones = {0xFFFFFFFFu, 0xFFFFFFFFu, 0xFFFFFFFFu, 0xFFFFFFFFu};
    auto arm = [&](int s) {
#pragma unroll
        for (int i = 0; i < 4; ++i) st128(k.rs, l * 16 + i * 1024, k.ym(s) + yo, ones);
    };
    Red R;
    RSTAMP(2)
    arm(1);
    y_wait(k, 0);
    R.yload(k, j, 0);
    for (int s = 0; s < 63; ++s) {
        STOP(s) STAMP(s, 0)
        const unsigned ap = k.p0 + PR_B + (s & 3) * PR_SZ + w * 49152, am = k.ym(s) + yo;
        const unsigned ph = (s & 4) ? 0x80008000u : 0u;
        unsigned spins = 0, tq, tv;
        unsigned fl;
        WAIT_BC(s)
        STAMP(s, 5)
        char* rb = smem + (s & 1) * 4096;
_Pragma("unroll 1")
        for (spins = 0;; ++spins) {
            u32x4 ym[4], pm[5];
            f32x4 acc[2];
            if (spins == 0) wait_until(tv);
            asm volatile("" ::: "memory");
#pragma unroll
            for (int i = 0; i < 4; ++i) ym[i] = ld128(k.rs, v16 + (i >> 1) * 2048 + (i & 1) * 512, am);
            fl = ld32(k.rs, 0, k.p0 + CNT(C_X1, j));
            asm volatile("" ::: "memory");
            if (spins == 0) { wait_until(tq); STAMP(s, 1) }
            asm volatile("" ::: "memory");
#pragma unroll
            for (int i = 0; i < 5; ++i) pm[i] = ld128(k.rs, i < 4 ? l * 16 + i * 1024 : vp, ap + (i >> 2) * 4096);
            acc[0] = (f32x4){0.f, 0.f, 0.f, 0.f}; acc[1] = acc[0];
            const u32x8 y0 = {ym[0][0], ym[1][0], ym[0][1], ym[1][1], ym[2][0], ym[3][0], ym[2][1], ym[3][1]};
            const u32x8 y1 = {ym[0][2], ym[1][2], ym[0][3], ym[1][3], ym[2][2], ym[3][2], ym[2][3], ym[3][3]};
            unsigned mx = 0;
#pragma unroll
            for (int i = 0; i < 4; ++i) mx = max(mx, umax4(ym[i]));
            unsigned tg = mx == 0xFFFFFFFFu ? 0x80000000u : 0u;
            asm volatile("s_nop 1" : "+v"(acc[0]), "+v"(acc[1]), "+v"(tg) : "v"(y0), "v"(y1), "v"(ix));
            SCHED0();
#pragma unroll
            for (int i = 0; i < 5; ++i) {
                const unsigned u0 = __builtin_amdgcn_bitop3_b32(pm[i][0], pm[i][1], ph, 0x7e), u1 = __builtin_amdgcn_bitop3_b32(pm[i][2], pm[i][3], ph, 0x7e);
                SCHED0();
                if (i < 4) smf<false>(acc[0], __builtin_bit_cast(bf16x8, pm[i]), __builtin_bit_cast(bf16x16, __builtin_shufflevector(R.Y[2 * i], R.Y[2 * i + 1], 0, 1, 2, 3, 4, 5, 6, 7)), ix);
                else smf<false>(acc[0], __builtin_bit_cast(bf16x8, pm[4]), __builtin_bit_cast(bf16x16, y0), ix);
                tg = (tg | u0) | u1;
                SCHED0();
                if (i < 4) smf<false>(acc[1], __builtin_bit_cast(bf16x8, pm[i]), __builtin_bit_cast(bf16x16, __builtin_shufflevector(R.Y[8 + 2 * i], R.Y[9 + 2 * i], 0, 1, 2, 3, 4, 5, 6, 7)), ix);
                else smf<false>(acc[1], __builtin_bit_cast(bf16x8, pm[4]), __builtin_bit_cast(bf16x16, y1), ix);
                SCHED0();
            }
            const unsigned long long bad = __ballot((int)tg < 0);
            asm volatile("s_nop 5" : "+v"(acc[0]), "+v"(acc[1]) : "s"(bad));
#pragma unroll
            for (int t = 0; t < 2; ++t) { float* rp = (float*)(rb + t * 2048 + w * 512 + g * 128 + n * 4); rp[0] = acc[t][0] + acc[t][2]; rp[16] = acc[t][1] + acc[t][3]; }
            if (bad == 0) break;
            PCOUNT(0)
            asm volatile("" ::: "memory");
            if (spin_fail(k, spins)) break;
        }
        STAMP(s, 2)
        RAWBAR()
        if (w < 2) {
            const char* rp = rb + w * 2048 + ((l >> 4) * 2 + ((l >> 2) & 1)) * 64 + ((l & 3) * 4 + ((l >> 3) & 1) * 2) * 4;
            const f32x2 a = *(const f32x2*)rp, b = *(const f32x2*)(rp + 512), c = *(const f32x2*)(rp + 1024), d = *(const f32x2*)(rp + 1536);
            const f32x2 r = (a + b) + (c + d);
            st32(k.rs, l * 4, k.memf(s + 1) + EXP((2 * j + w) * 256), min(cvtpk(r[0], r[1]) ^ ph, 0xFFFFFFFEu));
        }
        STAMP(s, 3)
_Pragma("unroll 1")
        for (spins = 0; (int)__builtin_amdgcn_readfirstlane(fl) < s - 2; ++spins) {
            PCOUNT(1)
            __builtin_amdgcn_s_sleep(PSLEEP);
            if (spin_fail(k, spins)) break;
            fl = ld32(k.rs, 0, k.p0 + CNT(C_X1, j));
        }
        __builtin_amdgcn_fence(__ATOMIC_ACQUIRE, "wavefront");
        arm(s + 2);
        y_wait(k, s + 1);
        R.yload(k, j, s + 1);
        STAMP(s, 4)
        PFLUSH(s)
    }
    PFLUSH(63)
    PROF_OUT
}
DEVI void tred_role(const Params& p, const Ctx& k, char* smem, int j) {
    PROF_DECL
    const int l = k.l, w = k.w, g = l >> 4, n = l & 15, T = 2 * j + (w >> 1), rt = w & 1;
    const unsigned v16 = (g >> 1) * 1024 + (g & 1) * 256 + n * 16, yo = j * 32768 + EXP(w * 4096);
    const unsigned v8 = (g >> 1) * 512 + (g & 1) * 128 + n * 8;
    const unsigned xo = EXP(j * 2048) + (T & 1) * 512 + rt * 1024, xv = ((g >> 1) * 16 + n) * 16 + (g & 1) * 8;
    const u32x2 ones = {0xFFFFFFFFu, 0xFFFFFFFFu};
    Red R;
    RSTAMP(2)
    st64(k.rs, xv, k.seg(PG_X1, 0) + xo, ones); st64(k.rs, xv, k.seg(PG_X1, 1) + xo, ones);
    y_wait(k, 0);
    R.yload(k, j, 0);
    for (int s = 0; s < 64; ++s) {
        STOP(s) STAMP(s, 0)
        const unsigned ap1 = k.p0 + PR_B + (s & 3) * PR_SZ + w * 49152 + 16384, ap2 = ap1 + 16384, am = k.ym(s) + yo;
        const unsigned ph = (s & 4) ? 0x80008000u : 0u;
        unsigned spins = 0, tq, tv;
        u32x2 res;
        if (k.c) res = ld64(k.rs, xv, k.seg0(PG_X2, s) + xo);
        else res = *(const u32x2*)(p.x + (size_t)(s * 32 + rt * 16 + n) * 640 + T * 16 + g * 4);
        f32x4 acc[2][2];
        WAIT_BC(s)
        wait_until(tv); wait_until(tq + RDLY);
        asm volatile("" ::: "memory");
        STAMP(s, 1)
_Pragma("unroll 1")
        for (;; ++spins) {
            u32x4 ym[4], p1[8]; u32x2 p0[8], pq[8];
#pragma unroll
            for (int i = 0; i < 4; ++i) ym[i] = ld128(k.rs, v16 + (i >> 1) * 2048 + (i & 1) * 512, am);
#pragma unroll
            for (int i = 0; i < 8; ++i) pq[i] = ld64(k.rs, v8 + ((i >> 1) & 1) * 1024 + (i & 1) * 256 + (i >> 2) * 2048, ap2 + 4096);
#pragma unroll
            for (int i = 0; i < 8; ++i) { p0[i] = ld64(k.rs, l * 8 + i * 512, ap2); p1[i] = ld128(k.rs, l * 16 + (i & 3) * 1024, ap1 + (i >> 2) * 4096); }
            unsigned mx = 0, tg = 0;
#pragma unroll
            for (int t = 0; t < 2; ++t) {
                acc[t][0] = (f32x4){0.f, 0.f, 0.f, 0.f}; acc[t][1] = acc[t][0];
#pragma unroll
                for (int f = 0; f < 2; ++f) {
                    const u32x4 yf = {ym[2 * f][2 * t], ym[2 * f][2 * t + 1], ym[2 * f + 1][2 * t], ym[2 * f + 1][2 * t + 1]};
#pragma unroll
                    for (int r = 0; r < 2; ++r)
                        acc[t][r] = mf(__builtin_bit_cast(bf16x8, yf), __builtin_bit_cast(bf16x8, (u32x4){pq[4 * r + 2 * f][0], pq[4 * r + 2 * f][1], pq[4 * r + 2 * f + 1][0], pq[4 * r + 2 * f + 1][1]}), acc[t][r]);
                }
            }
#pragma unroll
            for (int i = 0; i < 8; ++i)
#pragma unroll
                for (int t = 0; t < 2; ++t) {
                    acc[t][0] = mf(__builtin_bit_cast(bf16x8, R.Y[t * 8 + i]), __builtin_bit_cast(bf16x8, (u32x4){p0[i][0], p0[i][1], 0u, 0u}), acc[t][0]);
                    acc[t][1] = mf(__builtin_bit_cast(bf16x8, R.Y[t * 8 + i]), __builtin_bit_cast(bf16x8, p1[i]), acc[t][1]);
                }
#pragma unroll
            for (int i = 0; i < 4; ++i) mx = max(mx, umax4(ym[i]));
#pragma unroll
            for (int i = 0; i < 8; ++i) tg |= (((p1[i][0] ^ ph) | (p1[i][1] ^ ph)) | ((p1[i][2] ^ ph) | (p1[i][3] ^ ph))) | (((p0[i][0] ^ ph) | (p0[i][1] ^ ph)) | ((pq[i][0] ^ ph) | (pq[i][1] ^ ph)));
            if (__all(mx != 0xFFFFFFFFu && (int)tg >= 0)) break;
            PCOUNT(0)
            asm volatile("" ::: "memory");
            if (spin_fail(k, spins)) break;
        }
        STAMP(s, 2)
        const unsigned yc = y_get(k);
        char* rb = smem + (s & 1) * 16384;
#pragma unroll
        for (int i = 0; i < 4; ++i) *(f32x4*)(rb + i * 4096 + w * 1024 + l * 16) = acc[i >> 1][i & 1];
        RAWBAR()
        st32(k.rs, (w == 0 && l == 0) ? 0u : OOB, k.p0 + CNT(C_X1, j), (unsigned)s);
        {
            const char* rp = rb + w * 4096 + l * 16;
            const f32x4 r = (*(const f32x4*)rp + *(const f32x4*)(rp + 1024)) + (*(const f32x4*)(rp + 2048) + *(const f32x4*)(rp + 3072));
            const unsigned d0 = cvtpk(r[0], r[1]) ^ ph, d1 = cvtpk(r[2], r[3]) ^ ph;
            u32x2 o; o[0] = packs(blo(res[0]) + blo(d0), bhi(res[0]) + bhi(d0)); o[1] = packs(blo(res[1]) + blo(d1), bhi(res[1]) + bhi(d1));
            st64(k.rs, xv, k.seg(PG_X1, s) + xo, o);
            st64(k.rs, s < 62 ? xv : OOB, k.seg(PG_X1, (s + 2) & 63) + xo, ones);
        }
        STAMP(s, 3)
        if (s < 63) {
            if (!__all((int)yc >= s + 1)) { PCOUNT(2) y_wait(k, s + 1); }
            __builtin_amdgcn_fence(__ATOMIC_ACQUIRE, "wavefront");
            R.yload(k, j, s + 1);
        }
        STAMP(s, 4)
        STAMP(s, 5)
        PFLUSH(s)
    }
    PROF_OUT
}

DEVI void up_role(const Params& p, const Ctx& k, char* smem, int id) {
    PROF_DECL
    const int l = k.l, w = k.w, g = l >> 4, n = l & 15;
    bf16x8 Wt[80];
    const int tile0 = id * 8 + w * 2;
    wstream<20, 4, 8>(smem + w * 32768,
        [&](auto U, unsigned sl) { constexpr int u = CV(U);
            wp_issue<2, (u % 8) * 4096>(p.wg + (size_t)u * 32 * 1536 + tile0 * 16, 1536, sl, l); wp_issue<2, (u % 8) * 4096 + 2048>(p.wu + (size_t)u * 32 * 1536 + tile0 * 16, 1536, sl, l); },
        [&](auto U, char* sp) { constexpr int u = CV(U);
            Wt[u] = wp_read<2, true>(sp, 0, l); Wt[20 + u] = wp_read<2, true>(sp, 1, l); Wt[40 + u] = wp_read<2, true>(sp + 2048, 0, l); Wt[60 + u] = wp_read<2, true>(sp + 2048, 1, l); });
    RAWBAR()
    RSTAMP(2)
    bf16x8 one8 = {0x3F80, 0x3F80, 0x3F80, 0x3F80, 0x3F80, 0x3F80, 0x3F80, 0x3F80};
    asm volatile("s_nop 1" : "+v"(one8));
    f32x4 cs[4];
    sfor<80>([&](auto I) { constexpr int f = CV(I); mfa<(f < 64), f % 20 == 0>(cs[f / 20], Wt[f], one8); });
    asm volatile("s_nop 7" : "+v"(cs[0]), "+v"(cs[1]), "+v"(cs[2]), "+v"(cs[3]));
    const unsigned ho = EXP((tile0 >> 1) * 1024), l16 = l * 16;
    constexpr int L_ST = 81920;
    const u32x4 ones = {0xFFFFFFFFu, 0xFFFFFFFFu, 0xFFFFFFFFu, 0xFFFFFFFFu};
#pragma unroll
    for (int i = 0; i < 4; ++i) st128(k.rs, l16, k.hseg(i >> 1) + (i & 1) * EXP(49152) + ho, ones);
    unsigned fails = 0;
    const unsigned fl = (w == 0 && l == 0) ? 0u : OOB;
    const unsigned long long m1 = __ballot(n & 1), m2 = __ballot(n & 2);
    for (int s = 0; s < 64; ) {
        prog_wait<20>(k, k.p0 + CNT(C_X1, 0), s);
        STOP(s) STAMP(s, 0)
        const unsigned xb = (s & 1) * 40960 + l16, yb = xb + w * 2048, sp = L_ST + (s & 1) * 2048, rp = sp + n * 16;
        const unsigned dp = sp + (g == (n >> 2) ? n * 16 + w * 4 : 4096 + l * 4);
        DRAIN();
        xn_all(k, l16, k.seg(PG_X1, s) + w * 2048, (s & 1) * 40960 + w * 2048);
        f32x4 acc[2][4], st[4];
        bf16x8 x[20], y0[5], y1[5];
        unsigned mx = 0;
        auto blk = [&](auto B, auto J) { constexpr int b = CV(B), j = CV(J);
            if constexpr (j == 0) asm volatile("s_waitcnt vmcnt(%0)\n\ts_barrier" :: "n"(8 - 2 * b) : "memory");
            if constexpr (j >= 1 && j < 5) x[4 * b + j - 1] = lr128(xb + (4 * b + j - 1) * 2048);
            if constexpr (j == 5) y0[b] = lr128(yb + b * 8192);
            if constexpr (j == 6) y1[b] = lr128(yb + b * 8192 + 1024); };
        sfor<7>([&](auto J) { blk(IC(0), J); });
        SCHED0();
        STAMP(s, 1)
        sfor<5>([&](auto B) { constexpr int b = CV(B);
            sfor<20>([&](auto I) { constexpr int i = CV(I), ks = 4 * b + ((i >> 2) & 3), nt = i & 3, f = nt * 20 + ks;
                if constexpr (i < 16) mfa<(f < 64), ks == 0>(acc[0][nt], Wt[f], x[ks]);
                else mfa<false, b == 0>(st[nt], (nt & 1) ? (nt & 2 ? y1[b] : y0[b]) : one8, nt & 2 ? y1[b] : y0[b]);
                SCHED0();
                if constexpr (i >= 8 && i < 15 && b < 4) blk(IC((b + 1) % 5), IC(i - 8));
                if constexpr (i >= 16) { const u32x4 v = __builtin_bit_cast(u32x4, (i & 1) ? y1[b] : y0[b]); mx = umax3(mx, v[i & 2], v[(i & 2) + 1]); PIN(mx); }
                if constexpr (b == 4 && i >= 9 && i < 12) x[i - 9] = lr128(xb + 1024 + (i - 9) * 2048);
                SCHED0(); }); });
        STAMP(s, 2)
        float mean[2], rden[2], G[2], U[2];
        f32x4 sa[2], sb[2];
        u32x4 bad, o[2];
        bool redo;
        auto stat = [&](auto I) { constexpr int i = CV(I), rt = i / 8, j = i % 8;
            if constexpr (j == 0) asm volatile("v_add_f32 %0, %2, %3\n\tv_add_f32 %1, %4, %5" : "=&v"(mean[rt]), "=&v"(rden[rt]) : "v"(sa[rt][0]), "v"(sa[rt][1]), "v"(sb[rt][0]), "v"(sb[rt][1]));
            if constexpr (j == 1 || j == 2) asm volatile("v_add_f32 %0, %0, %2\n\tv_add_f32 %1, %1, %3" : "+v"(mean[rt]), "+v"(rden[rt]) : "v"(sa[rt][j + 1]), "v"(sb[rt][j + 1]));
            if constexpr (j == 3) asm volatile("v_mul_f32 %0, 0xbaccccCD, %0\n\tv_mul_f32 %1, 0x3acccccd, %1" : "+v"(mean[rt]), "+v"(rden[rt]));
            if constexpr (j == 4) asm volatile("v_fma_f32 %1, -%0, %0, %1\n\tv_cvt_pk_bf16_f32 %0, 0, %0\n\tv_max_f32 %1, 0, %1" : "+v"(mean[rt]), "+v"(rden[rt]));
            if constexpr (j == 5) asm volatile("v_cvt_pk_bf16_f32 %0, 0, %0\n\tv_add_f32 %0, 0x3727c5ac, %0\n\tv_cvt_pk_bf16_f32 %0, 0, %0" : "+v"(rden[rt]));
            if constexpr (j == 6) asm volatile("v_sqrt_f32 %0, %0" : "+v"(rden[rt]));
            if constexpr (j == 7) asm volatile("v_cvt_pk_bf16_f32 %0, 0, %0\n\tv_rcp_f32 %0, %0" : "+v"(rden[rt])); };
#define GU "+v"(G[0]), "+v"(G[1]), "+v"(U[0]), "+v"(U[1])
        auto ep = [&](auto I) { constexpr int i = CV(I), q = i / 15, j = i % 15, rt = q >> 2, t = (q >> 1) & 1, c = (q & 1) * 2;
            if constexpr (j == 0) asm volatile("v_fma_f32 %0, %2, %3, %4\n\tv_fma_f32 %1, %2, %5, %6" : "=&v"(G[0]), "=&v"(G[1]) : "v"(mean[rt]), "v"(cs[t][c]), "v"(acc[rt][t][c]), "v"(cs[t][c + 1]), "v"(acc[rt][t][c + 1]));
            if constexpr (j == 1) asm volatile("v_mul_f32 %0, %0, %2\n\tv_mul_f32 %1, %1, %2" : "+v"(G[0]), "+v"(G[1]) : "v"(rden[rt]));
            if constexpr (j == 2) asm volatile("v_cvt_pk_bf16_f32 %0, 0, %0\n\tv_cvt_pk_bf16_f32 %1, 0, %1" : "+v"(G[0]), "+v"(G[1]));
            if constexpr (j == 3) asm volatile("v_mul_f32 %0, 0xbfb8aa3b, %0\n\tv_mul_f32 %1, 0xbfb8aa3b, %1" : "+v"(G[0]), "+v"(G[1]));
            if constexpr (j == 4 || j == 5) asm volatile("v_exp_f32 %0, %0" : "+v"(G[j & 1]));
            if constexpr (j == 6) asm volatile("v_fma_f32 %0, %2, %3, %4\n\tv_fma_f32 %1, %2, %5, %6" : "=&v"(U[0]), "=&v"(U[1]) : "v"(mean[rt]), "v"(cs[2 + t][c]), "v"(acc[rt][2 + t][c]), "v"(cs[2 + t][c + 1]), "v"(acc[rt][2 + t][c + 1]));
            if constexpr (j == 7) asm volatile("v_add_f32 %0, 1.0, %0\n\tv_add_f32 %1, 1.0, %1" : "+v"(G[0]), "+v"(G[1]));
            if constexpr (j == 8 || j == 9) asm volatile("v_rcp_f32 %0, %0" : "+v"(G[j & 1]));
            if constexpr (j == 10) asm volatile("v_mul_f32 %0, %0, %2\n\tv_mul_f32 %1, %1, %2" : "+v"(U[0]), "+v"(U[1]) : "v"(rden[rt]));
            if constexpr (j == 11) asm volatile("v_cvt_pk_bf16_f32 %0, 0, %0\n\tv_cvt_pk_bf16_f32 %1, 0, %1" : "+v"(U[0]), "+v"(U[1]));
            if constexpr (j == 12) asm volatile("v_cvt_pk_bf16_f32 %0, 0, %0\n\tv_cvt_pk_bf16_f32 %1, 0, %1" : "+v"(G[0]), "+v"(G[1]));
            if constexpr (j == 13) asm volatile("v_mul_f32 %0, %0, %2\n\tv_mul_f32 %1, %1, %3" : "+v"(G[0]), "+v"(G[1]) : "v"(U[0]), "v"(U[1]));
            if constexpr (j == 14) { unsigned d; asm volatile("v_cvt_pk_bf16_f32 %0, %1, %2\n\tv_min_u32 %0, -2, %0" : "=&v"(d) : "v"(G[0]), "v"(G[1])); o[rt][q & 3] = d; } };
        sfor<80>([&](auto I) {
            constexpr int i = CV(I), ks = i >> 2, nt = i & 3, f = nt * 20 + ks;
            mfa<(f < 64), ks == 0>(acc[1][nt], Wt[f], x[ks]);
            SCHED0();
            if constexpr (nt == 0 && ks + 3 < 20) x[(ks + 3) % 20] = lr128(xb + 1024 + ((ks + 3) % 20) * 2048);
            if constexpr (i == 1 || i == 3) {
                constexpr int rt2 = i == 3;
                asm volatile("" : "+v"(st[2 * rt2]), "+v"(st[2 * rt2 + 1]));
                const f32x4& q = st[2 * rt2 + 1];
                const float d = csel(csel(q[0], q[1], m1), csel(q[2], q[3], m1), m2);
                lw32(dp + rt2 * 512, __float_as_uint(st[2 * rt2][0])); lw32(dp + rt2 * 512 + 256, __float_as_uint(d));
            }
            if constexpr (i == 2) lw32(sp + 1024 + w * 4, __any(mx == 0xFFFFFFFFu) ? 1u : 0u);
            if constexpr (i == 6) bar_lgkm<1>();
            if constexpr (i == 6) sa[0] = __builtin_bit_cast(f32x4, lr128(rp));
            if constexpr (i == 7) sb[0] = __builtin_bit_cast(f32x4, lr128(rp + 256));
            if constexpr (i == 9) sa[1] = __builtin_bit_cast(f32x4, lr128(rp + 512));
            if constexpr (i == 10) sb[1] = __builtin_bit_cast(f32x4, lr128(rp + 768));
            if constexpr (i == 11) bad = __builtin_bit_cast(u32x4, lr128(sp + 1024));
            if constexpr (i >= 12 && i < 20) stat(IC(i - 12));
            if constexpr (i >= 20) ep(IC(i - 20));
            if constexpr (i >= 21 && i < 53 && nt == 1) stat(IC(8 + (i - 21) / 4));
            if constexpr (i == 54) { redo = __builtin_amdgcn_readfirstlane((bad[0] | bad[1]) | (bad[2] | bad[3])) != 0 && fails++ < 4096u; }
            if constexpr (i == UPOS) st32(k.rs, fl, k.p0 + CNT(C_H, id), (unsigned)(redo ? s - 1 : s));
            SCHED0();
        });
        STAMP(s, 3)
        asm volatile("s_nop 7" : "+v"(acc[1][0]), "+v"(acc[1][1]), "+v"(acc[1][2]), "+v"(acc[1][3]));
#pragma unroll
        for (int q = 0; q < 4; ++q) {
            const int t = q >> 1, c = (q & 1) * 2;
            float hv[2];
#pragma unroll
            for (int i = 0; i < 2; ++i) {
                const float gg = bfr(__builtin_fmaf(mean[1], cs[t][c + i], acc[1][t][c + i]) * rden[1]), u = bfr(__builtin_fmaf(mean[1], cs[2 + t][c + i], acc[1][2 + t][c + i]) * rden[1]);
                hv[i] = bfr(__builtin_amdgcn_rcpf(1.f + __builtin_amdgcn_exp2f(gg * -1.44269504f))) * u;
            }
            o[1][q] = packs(hv[0], hv[1]);
        }
        if (redo) { PCOUNT(0) RAWBAR() continue; }
        st128(k.rs, l16, k.hseg(s) + ho, o[0]); st128(k.rs, l16, k.hseg(s) + EXP(49152) + ho, o[1]);
        st128(k.rs, s < 62 ? l16 : OOB, k.hseg((s + 2) & 63) + ho, ones); st128(k.rs, s < 62 ? l16 : OOB, k.hseg((s + 2) & 63) + EXP(49152) + ho, ones);
        STAMP(s, 4) STAMP(s, 5)
        PFLUSH(s)
        ++s;
    }
    PROF_OUT
}

DEVI void down_role(const Params& p, const Ctx& k, char* smem, int id) {
    PROF_DECL
    const int l = k.l, w = k.w, g = l >> 4, n = l & 15, rt = id / 5, cg = id % 5;
    bf16x8 Wt[96];
    wstream<12, 8, 4>(smem + w * 32768,
        [&](auto U, unsigned sl) { constexpr int u = decltype(U)::value; wp_issue<8, (u % 4) * 8192>(p.wd + (size_t)(w * 12 + u) * 32 * 640 + cg * 128, 640, sl, l); },
        [&](auto U, char* sp) { constexpr int u = decltype(U)::value;
#pragma unroll
            for (int nt = 0; nt < 8; ++nt) Wt[u * 8 + nt] = wp_read<8, false>(sp, (nt + 2 * w) & 7, l); });
    RAWBAR()
    RSTAMP(2)
    const unsigned xo = EXP((cg * 4 + w) * 2048) + rt * 1024, xv = ((g >> 1) * 16 + n) * 16 + (g & 1) * 8;
    const u32x2 ones = {0xFFFFFFFFu, 0xFFFFFFFFu};
    const unsigned x2v = k.c ? OOB : xv;
#pragma unroll
    for (int i = 0; i < 4; ++i) st64(k.rs, x2v, k.seg(PG_X2, i >> 1) + xo + (i & 1) * 512, ones);
    for (int s = 0; s < 64; ++s) {
        prog_wait<12>(k, k.p0 + CNT(C_H, 0), s);
        STOP(s) STAMP(s, 0)
        f32x4 acc[8];
        u32x2 res[2];
        const unsigned rb = (s & 1) * 32768 + l * 16;
_Pragma("unroll 1")
        for (unsigned spins = 0;; ++spins) {
            u32x4 hx[12];
#pragma unroll
            for (int q = 0; q < 3; ++q) {
                const unsigned a = k.hseg(s) + rt * EXP(49152) + EXP((w * 12 + q * 4) * 1024);
#pragma unroll
                for (int i = 0; i < 4; ++i) hx[q * 4 + i] = ld128(k.rs, l * 16 + i * 1024, a);
            }
#pragma unroll
            for (int t = 0; t < 2; ++t) res[t] = ld64(k.rs, xv + t * 512, k.seg(PG_X1, s) + xo);
            unsigned mx = 0;
            sfor<96>([&](auto I) { constexpr int i = CV(I), ks = i < 72 ? i / 6 : (i - 72) / 2, j = i < 72 ? 2 + i % 6 : i & 1, f = ks * 8 + j;
                mfa<(f < 64), ks == 0>(acc[j], Wt[f], __builtin_bit_cast(bf16x8, hx[ks]));
                SCHED0();
                if constexpr (i < 72 && i % 6 < 2) { mx = umax3(mx, hx[ks][(i & 1) * 2], hx[ks][(i & 1) * 2 + 1]); PIN(mx); }
                if constexpr (i >= 75 && i < 93 && i % 3 == 0) { constexpr int jj = 2 + (i - 75) / 3;
                    asm volatile("" : "+v"(acc[jj])); lw128(rb + ((w + jj / 2) & 3) * 6144 + ((jj / 2 - 1) * 2 + (jj & 1)) * 1024, __builtin_bit_cast(u32x4, acc[jj])); }
                SCHED0(); });
            asm volatile("s_nop 7" : "+v"(acc[0]), "+v"(acc[1]), "+v"(res[0]), "+v"(res[1]));
            if (__all(mx != 0xFFFFFFFFu) || spin_fail(k, spins)) break;
            PCOUNT(0)
            asm volatile("" ::: "memory");
        }
        STAMP(s, 1)
        RAWBAR()
        st32(k.rs, (w == 0 && l == 0) ? 0u : OOB, k.p0 + CNT(C_X2, rt * 5 + cg), (unsigned)s);
#pragma unroll
        for (int t = 0; t < 2; ++t) {
            const unsigned rp = rb + w * 6144 + t * 1024;
            const f32x4 r = (acc[t] + __builtin_bit_cast(f32x4, lr128(rp))) + (__builtin_bit_cast(f32x4, lr128(rp + 2048)) + __builtin_bit_cast(f32x4, lr128(rp + 4096)));
            const unsigned d0 = cvtpk(r[0], r[1]), d1 = cvtpk(r[2], r[3]);
            u32x2 o; o[0] = cvtpk(blo(res[t][0]) + blo(d0), bhi(res[t][0]) + bhi(d0)); o[1] = cvtpk(blo(res[t][1]) + blo(d1), bhi(res[t][1]) + bhi(d1));
            if (k.c) *(u32x2*)(p.out + (size_t)(s * 32 + rt * 16 + n) * 640 + (cg * 8 + w * 2 + t) * 16 + g * 4) = o;
            else st64(k.rs, xv + t * 512, k.seg(PG_X2, s) + xo, (u32x2){min(o[0], 0xFFFFFFFEu), min(o[1], 0xFFFFFFFEu)});
        }
        STAMP(s, 2)
#pragma unroll
        for (int t = 0; t < 2; ++t) st64(k.rs, s < 62 ? x2v : OOB, k.seg(PG_X2, (s + 2) & 63) + xo + t * 512, ones);
        STAMP(s, 3) STAMP(s, 4) STAMP(s, 5)
        PFLUSH(s)
    }
    PROF_OUT
}

DEVI void warm(const Params& p, const Ctx& k, int xl, int hw) {
    if (k.c) sleep_ticks(WD1);
    set_m0(k.w * 256);
    const unsigned t0 = rclk();
_Pragma("unroll 1")
    for (int it = hw; it < 872; it += 64) {
        const u16* b; unsigned o;
        while ((int)(rclk() - t0) < (it >> 6) * PACE) __builtin_amdgcn_s_sleep(1);
        if (it < 200) { b = p.Wo; o = ((xl * 8 + (it / 5) % 8) * 192 + (it / 40 + 1) * 32) * 1280 + (it % 5) * 8192 + k.l * 128; }
        else if (it < 440) { const unsigned ln = (it - 200) * 64 + k.l; b = p.Wv; o = (ln / 24) * 12288 + xl * 3072 + (ln % 24) * 128; }
        else { const unsigned ln = ((it - 440) >> 1) * 64 + k.l; b = (it & 1) ? p.Wq : p.Wk; o = (64 + ln / 24) * 12288 + xl * 3072 + (ln % 24) * 128; }
        asm volatile("global_load_lds_dword %0, %1" :: "v"(o), "s"(b) : "memory");
    }
    DRAIN();
    RAWBAR()
}

DEVI int xcc_id() { return __builtin_amdgcn_s_getreg(20 | (0 << 6) | (3 << 11)) & 7; }

__global__ __launch_bounds__(256) void init_kernel(Params p) {
    const int b = blockIdx.x, tid = threadIdx.x, l = tid & 63;
    const __amdgpu_buffer_rsrc_t rs = __builtin_amdgcn_make_buffer_rsrc((void*)p.ws, 0, 0x7fffffff, 0x00020000);
    const u32x4 ones = {0xFFFFFFFFu, 0xFFFFFFFFu, 0xFFFFFFFFu, 0xFFFFFFFFu};
    if (b < 64 && tid < 64) {
        unsigned bt[2] = {0xFFFFFFFFu, 0xFFFFFFFFu};
        if (b < NPAGES) {
            const unsigned off = ((unsigned)b << 21) + 7 * 16384 + (l & 7) * 16;
#pragma unroll 1
            for (int r = 0; r < CALR; ++r) {
#pragma unroll
                for (int hf = 0; hf < 2; ++hf) {
                    unsigned long long c0, c1;
                    asm volatile("s_memtime %0\n\ts_waitcnt lgkmcnt(0)" : "=s"(c0) :: "memory");
                    st128(rs, off + hf * 8192, 0, ones);
                    asm volatile("s_waitcnt vmcnt(0)\n\ts_memtime %0\n\ts_waitcnt lgkmcnt(0)" : "=s"(c1) :: "memory");
                    bt[hf] = min(bt[hf], (unsigned)(c1 - c0));
                }
            }
        }
        if (l == 0) ((unsigned*)(p.cm + CM_TAB))[b] = (unsigned)(bt[1] < bt[0]) ^ (unsigned)(xcc_id() >> 2);
    }
    constexpr unsigned N0 = PR_B + 4 * PR_SZ, N1 = 40 * 16384, N2 = 64 * 20480;
    for (unsigned i = (b * 256 + tid) * 16; i < N0 + N1 + N2; i += 256 * 256 * 16) {
        unsigned o;
        u32x4 v = ones;
        if (i < N0) o = i;
        else if (i < N0 + N1) o = ((unsigned)PG_YM << 21) + (i - N0);
        else {
            const unsigned j = i - N0 - N1, s = j / 20480, r = j % 20480;
            const unsigned q = r < 16384 ? r : 16384 + ((r - 16384) >> 11) * 8192 + (r & 2047);
            o = (2u << 21) + s * 32768 + q;
            if (s == 0) {
                const int e = (((q >> 14) << 13) | (q & 8191u)) >> 4, T = e >> 4, hf = (e >> 1) & 1, rr = ((e >> 2) & 3) * 2 + (e & 1);
#pragma unroll
                for (int c = 0; c < 4; ++c) v[c] = min(*(const unsigned*)(p.mem0 + rr * 640 + T * 16 + 4 * c + 2 * hf), 0xFFFFFFFEu);
            }
        }
        *(u32x4*)(p.ws + o) = v;
    }
    if (b == 255) for (unsigned o = CM_TICKET + tid * 16; o < CM_BYTES; o += 4096) *(u32x4*)(p.cm + o) = ones;
}

__global__ __launch_bounds__(256, 1) void mega_kernel(Params p) {
    extern __shared__ __attribute__((aligned(16))) char smem[];
    Ctx k;
    k.ws = p.ws; k.cm = p.cm;
    k.rs = __builtin_amdgcn_make_buffer_rsrc((void*)p.ws, 0, 0x7fffffff, 0x00020000);
    k.l = threadIdx.x & 63; k.w = __builtin_amdgcn_readfirstlane(threadIdx.x >> 6);
    RSTAMP(0)
    k.par = __ballot(((const unsigned*)(p.cm + CM_TAB))[k.l] != 0u);
    if (threadIdx.x == 0) {
        int x = xcc_id(), role = 0;
#pragma unroll 1
        for (int i = 0; i < 8; ++i) {
            const unsigned t = __hip_atomic_fetch_add((GAS unsigned*)(p.cm + CM_TICKET + x * 128), 1u, __ATOMIC_RELAXED, __HIP_MEMORY_SCOPE_AGENT) + 1u;
            if (t < 32u) { role = x * 32 + (int)t; break; }
            x = (x + 1) & 7;
        }
        *(volatile LAS int*)smem = role;
    }
    RAWBAR()
    k.role = __builtin_amdgcn_readfirstlane(*(volatile LAS int*)smem);
    RAWBAR()
    const int x = k.role >> 5, i = k.role & 31, xl = x & 3;
    k.c = x >> 2;
    if (k.c) k.par = ~k.par;
    k.p0 = k.pg(0);
    RSTAMP(1)
    if (k.c) ln_x_batch(p, k, (i * 4 + xl) * 4 + k.w);
    if (i < 8) { qk_role(p, k, smem, xl * 8 + i); return; }
    if (i < 16) { vo_role(p, k, smem, xl * 8 + i - 8); return; }
    warm(p, k, xl, (i - 16) * 4 + k.w);
    if (i < 21) { mred_role(p, k, smem, xl * 5 + i - 16); return; }
    if (i < 26) { tred_role(p, k, smem, xl * 5 + i - 21); return; }
    if (!(xl & 1)) up_role(p, k, smem, xl * 3 + i - 26);
    else if (i < 31) down_role(p, k, smem, (xl >> 1) * 5 + i - 26);
    else row_role(p, k, smem, xl >> 1);
}

extern "C" void kernel_launch(void* const* d_in, const int* in_sizes, int n_in,
                              void* d_out, int out_size, void* d_ws, size_t ws_size,
                              hipStream_t stream) {
    Params p;
    p.x = (const u16*)d_in[0]; p.mem0 = (const u16*)d_in[1];
    p.Wq = (const u16*)d_in[2]; p.Wk = (const u16*)d_in[3]; p.Wv = (const u16*)d_in[4]; p.Wo = (const u16*)d_in[5];
    p.wg = (const u16*)d_in[8]; p.wu = (const u16*)d_in[9]; p.wd = (const u16*)d_in[10];
    p.out = (u16*)d_out; p.cm = (char*)d_ws;
    p.ws = (char*)(((uintptr_t)d_ws + CM_BYTES + ((1u << 21) - 1)) & ~(uintptr_t)((1u << 21) - 1));
    (void)hipFuncSetAttribute((const void*)mega_kernel, hipFuncAttributeMaxDynamicSharedMemorySize, LDS_BYTES);
    init_kernel<<<256, 256, 0, stream>>>(p);
    mega_kernel<<<256, 256, LDS_BYTES, stream>>>(p);
}
```

```cpp
#include <hip/hip_runtime.h>
#include <hip/hip_bf16.h>
#include <stdint.h>
#include <utility>

typedef unsigned short u16;
typedef __attribute__((ext_vector_type(16))) short bf16x16;
typedef __attribute__((ext_vector_type(8))) short bf16x8;
typedef __attribute__((ext_vector_type(4))) short bf16x4;
typedef __attribute__((ext_vector_type(8))) __bf16 hbf8;
typedef __attribute__((ext_vector_type(16))) __bf16 hbf16;
typedef __attribute__((ext_vector_type(4))) float f32x4;
typedef __attribute__((ext_vector_type(2))) float f32x2;
typedef __attribute__((ext_vector_type(8))) unsigned u32x8;
typedef __attribute__((ext_vector_type(4))) unsigned u32x4;
typedef __attribute__((ext_vector_type(2))) unsigned u32x2;
typedef __attribute__((ext_vector_type(2))) __bf16 bf16x2_t;
#define LAS __attribute__((address_space(3)))
#define GAS __attribute__((address_space(1)))
#define DEVI __device__ __forceinline__

#define PSLEEP 8
#define WD1 400
#define PACE 65
#define XPOS 62
#define YPOS 46
#define XPOSY 1
#define PWS 1
#define WUS 0
#define WMIX 1
#define PACE2 30
#define GDLY 80
#define QDLY 0
#define CALR 3
#define SPG 3
#define DQ 12
#define DV 12
#define BSLP 1
#define SDLY 0
#define UPOS 79
#define RDLY 30
#define NOYST 0
#define NOTOK 0
#define NOYLD 0
#define GPOSY 10
#define GPOS 13

#define EXP(o) ((((unsigned)(o) >> 13) << 14) | ((unsigned)(o) & 8191u))
enum { C_XN = 0, C_Y = 1, C_X1 = 2, C_XB = 3, C_H = 4, C_X2 = 5, C_BC = 6 };
#define CNT(type, s) ((unsigned)((type) * 16384 + (s) * 128))
constexpr unsigned PR_B = EXP(65536), PR_SZ = EXP(12 * 8192);
constexpr unsigned DUMMY_B = EXP(0);
constexpr int PG_MEMF = 2;
#define YXR 8
constexpr int PG_YX = 3;
constexpr int PG_XNA = PG_YX + 2 * YXR;
constexpr int PG_X1 = PG_XNA + 4, PG_X2 = PG_X1 + 4;
constexpr int PG_H = PG_X2 + 4;
constexpr int PG_YM = PG_H + 8;
constexpr int NPAGES = PG_YM + 2;
static_assert(NPAGES <= 64, "map");
constexpr unsigned CM_TAB = 0, CM_TICKET = 1024, CM_TMO = 4096, CM_BYTES = 8192;
constexpr unsigned SPIN_CAP = 1u << 20;
#define AUX_SC1 16
#define LDS_BYTES (160 * 1024)

struct Params {
    const u16* x; const u16* mem0; const u16* Wq; const u16* Wk; const u16* Wv; const u16* Wo;
    const u16* wg; const u16* wu; const u16* wd; u16* out; char* cm; char* ws;
};
struct Ctx {
    char* ws; char* cm; __amdgpu_buffer_rsrc_t rs; unsigned long long par;
    int c, l, w, role;
    unsigned p0;
    DEVI unsigned pg(int page) const { return ((unsigned)page << 21) | (((unsigned)(par >> page) & 1u) << 13); }
    DEVI unsigned pg0(int page) const { return pg(page) ^ ((unsigned)c << 13); }
    DEVI unsigned seg(int page, int s) const { return pg(page + (s >> 4)) + (unsigned)(s & 15) * 131072u; }
    DEVI unsigned seg0(int page, int s) const { return seg(page, s) ^ ((unsigned)c << 13); }
    DEVI unsigned hseg(int s) const { return pg(PG_H + (s >> 3)) + (unsigned)(s & 7) * 262144u; }
    DEVI unsigned yx(int sg, int wq) const { return pg(PG_YX + (sg % YXR) * 2 + (wq >> 1)) + (unsigned)(wq & 1) * 1048576u; }
    DEVI unsigned ym(int s) const { return pg(PG_YM + ((s >> 1) & 1)) + (unsigned)(s & 1) * EXP(327680); }
    DEVI unsigned memf(int s) const { return pg(PG_MEMF) + (unsigned)s * 32768u; }
    DEVI char* cnt(int type, int s) const { return ws + (p0 + CNT(type, s)); }
    DEVI char* cnt0(int type, int s) const { return ws + ((p0 ^ ((unsigned)c << 13)) + CNT(type, s)); }
};

DEVI unsigned cvtpk(float a, float b) { return __builtin_bit_cast(unsigned, __builtin_convertvector((f32x2){a, b}, bf16x2_t)); }
DEVI unsigned packs(float a, float b) { return min(cvtpk(a, b), 0xFFFFFFFEu); }
DEVI float blo(unsigned x) { return __uint_as_float(x << 16); }
DEVI float bhi(unsigned x) { return __uint_as_float(x & 0xFFFF0000u); }
DEVI float bfr(float f) { return __uint_as_float(cvtpk(0.f, f)); }
DEVI u32x4 ld128(__amdgpu_buffer_rsrc_t rs, unsigned v, unsigned s) { return __builtin_amdgcn_raw_buffer_load_b128(rs, (int)v, (int)s, AUX_SC1); }
DEVI u32x2 ld64(__amdgpu_buffer_rsrc_t rs, unsigned v, unsigned s) { return __builtin_amdgcn_raw_buffer_load_b64(rs, (int)v, (int)s, AUX_SC1); }
DEVI unsigned ld32(__amdgpu_buffer_rsrc_t rs, unsigned v, unsigned s) { return __builtin_amdgcn_raw_buffer_load_b32(rs, (int)v, (int)s, AUX_SC1); }
DEVI void st128(__amdgpu_buffer_rsrc_t rs, unsigned v, unsigned s, u32x4 x) {
    __builtin_amdgcn_raw_buffer_store_b128(x, rs, (int)v, (int)s, AUX_SC1);
    asm volatile("s_nop 1" :: "v"(x) : "memory");
}
DEVI void st64(__amdgpu_buffer_rsrc_t rs, unsigned v, unsigned s, u32x2 x) { __builtin_amdgcn_raw_buffer_store_b64(x, rs, (int)v, (int)s, AUX_SC1); }
DEVI void st32(__amdgpu_buffer_rsrc_t rs, unsigned v, unsigned s, unsigned x) { __builtin_amdgcn_raw_buffer_store_b32(x, rs, (int)v, (int)s, AUX_SC1); }
#define RAWBAR() { __builtin_amdgcn_s_waitcnt(0xc07f); __builtin_amdgcn_s_barrier(); }
#define DRAIN() asm volatile("s_waitcnt vmcnt(0)" ::: "memory")
#define SCHED0() __builtin_amdgcn_sched_barrier(0)
#define OOB 0x80000000u
DEVI int opq(int x) { asm volatile("" : "+v"(x)); return x; }
#define LANE const int l = opq(k.l), g = l >> 4, n = l & 15; (void)g; (void)n;
DEVI unsigned umax3(unsigned a, unsigned b, unsigned c) { return max(max(a, b), c); }
DEVI unsigned umax4(u32x4 v) { return max(umax3(v[0], v[1], v[2]), v[3]); }
DEVI unsigned rclk() { return (unsigned)__builtin_amdgcn_s_memrealtime(); }
DEVI unsigned long long rt_async() { unsigned long long t; asm volatile("s_memrealtime %0" : "=s"(t)); return t; }
DEVI void lgkm0(unsigned long long& a) { asm volatile("s_waitcnt lgkmcnt(0)" : "+s"(a) :: "memory"); }
DEVI unsigned spack(unsigned lo, unsigned hi) { unsigned d; asm volatile("s_pack_ll_b32_b16 %0, %1, %2" : "=s"(d) : "s"(lo), "s"(hi)); return d; }
DEVI void quit_if(int s, int last) {
    asm volatile("s_cmp_lg_u32 %0, %1\n\ts_cbranch_scc1 .Lgo%=\n\ts_endpgm\n.Lgo%=:" :: "s"(s), "n"(63) : "scc", "memory");
}
DEVI void sleep_ticks(int d) { unsigned target = rclk() + d; while ((int)(rclk() - target) < 0) __builtin_amdgcn_s_sleep(4); }
DEVI float xor16max(float x) { auto r = __builtin_amdgcn_permlane16_swap(__float_as_uint(x), __float_as_uint(x), false, false); return __builtin_fmaxf(__uint_as_float(r[0]), __uint_as_float(r[1])); }
DEVI float xor32max(float x) { auto r = __builtin_amdgcn_permlane32_swap(__float_as_uint(x), __float_as_uint(x), false, false); return __builtin_fmaxf(__uint_as_float(r[0]), __uint_as_float(r[1])); }
DEVI float xor16add(float x) { auto r = __builtin_amdgcn_permlane16_swap(__float_as_uint(x), __float_as_uint(x), false, false); return __uint_as_float(r[0]) + __uint_as_float(r[1]); }
DEVI float xor32add(float x) { auto r = __builtin_amdgcn_permlane32_swap(__float_as_uint(x), __float_as_uint(x), false, false); return __uint_as_float(r[0]) + __uint_as_float(r[1]); }
DEVI float fold32(float a, float b) { auto r = __builtin_amdgcn_permlane32_swap(__float_as_uint(a), __float_as_uint(b), false, false); return __uint_as_float(r[0]) + __uint_as_float(r[1]); }
#define ROR16(x, n) __builtin_bit_cast(float, __builtin_amdgcn_update_dpp(0, __builtin_bit_cast(int, (x)), 0x120 + (n), 0xf, 0xf, false))
DEVI float row16sum(float x) { x += ROR16(x, 8); x += ROR16(x, 4); x += ROR16(x, 2); x += ROR16(x, 1); return x; }
DEVI unsigned z0() { unsigned z; asm volatile("v_mov_b32 %0, 0" : "=v"(z)); return z; }
DEVI void cnt_add(char* p) { __hip_atomic_fetch_add((GAS unsigned*)(p + z0()), z0() + 1u, __ATOMIC_RELAXED, __HIP_MEMORY_SCOPE_AGENT); }
DEVI unsigned cnt_get(char* p) { return __hip_atomic_load((GAS unsigned*)(p + z0()), __ATOMIC_RELAXED, __HIP_MEMORY_SCOPE_AGENT) + 1u; }
DEVI bool spin_fail(const Ctx& k, unsigned spins) {
    if (spins > SPIN_CAP || ((spins & 255u) == 255u && __builtin_amdgcn_readfirstlane(cnt_get(k.cm + CM_TMO)) != 0)) { cnt_add(k.cm + CM_TMO); return true; }
    return false;
}
DEVI void cnt_wait(const Ctx& k, char* c, unsigned need) {
_Pragma("unroll 1")
    for (unsigned spins = 0; (unsigned)__builtin_amdgcn_readfirstlane(cnt_get(c)) < need; ++spins) {
        __builtin_amdgcn_s_sleep(PSLEEP);
        if (spin_fail(k, spins)) break;
    }
    __builtin_amdgcn_fence(__ATOMIC_ACQUIRE, "wavefront");
}
DEVI unsigned xn_get(const Ctx& k, unsigned l16, int s) { return ld32(k.rs, l16 & (k.c ? 128u : 0u), k.p0 + CNT(C_XN, k.c ? (s & 1) * 2 : s & 63)); }
DEVI bool xn_has(const Ctx& k, unsigned v, int s) { return __all((int)v >= (k.c ? s : 7)); }
DEVI void xn_wait(const Ctx& k, unsigned l16, int s) {
_Pragma("unroll 1")
    for (unsigned spins = 0; !xn_has(k, xn_get(k, l16, s), s); ++spins) {
        asm volatile("" ::: "memory");
        __builtin_amdgcn_s_sleep(2);
        if (spin_fail(k, spins)) break;
    }
    __builtin_amdgcn_fence(__ATOMIC_ACQUIRE, "wavefront");
}
template <int N> DEVI void prog_wait(const Ctx& k, unsigned a, int s) {
_Pragma("unroll 1")
    for (unsigned spins = 0; !__all((int)ld32(k.rs, min(k.l, N - 1) * 128, a) >= s); ++spins) {
        asm volatile("" ::: "memory");
        __builtin_amdgcn_s_sleep(PWS);
        if (spin_fail(k, spins)) break;
    }
    __builtin_amdgcn_fence(__ATOMIC_ACQUIRE, "wavefront");
}
template <int OFF> DEVI void dma1(const void* sbase, unsigned voff, unsigned lds) {
    asm volatile("s_add_i32 m0, %2, %3\n\ts_nop 0\n\tglobal_load_lds_dwordx4 %0, %1" :: "v"(voff), "s"(sbase), "s"(lds), "n"(OFF) : "memory", "scc");
}
DEVI void set_m0(unsigned lds) { asm volatile("s_mov_b32 m0, %0" :: "s"(lds) : "memory"); }
template <int OFF> DEVI void dmab(__amdgpu_buffer_rsrc_t rs, unsigned voff, unsigned soff) {
    asm volatile("buffer_load_dwordx4 %0, %1, %2 offen offset:%3 sc1 lds" :: "v"(voff), "s"(rs), "s"(soff), "n"(OFF) : "memory");
}
#define PIN(x) asm volatile("" : "+v"(x))
#define PINS(x) asm volatile("" : "+s"(x))
DEVI float csel(float a, float b, unsigned long long m) { float d; asm volatile("v_cndmask_b32_e64 %0, %1, %2, %3" : "=v"(d) : "v"(a), "v"(b), "s"(m)); return d; }
#define LO8(x) __builtin_shufflevector(x, x, 0, 1, 2, 3, 4, 5, 6, 7)
#define HI8(x) __builtin_shufflevector(x, x, 8, 9, 10, 11, 12, 13, 14, 15)
#define CAT8(a, b) __builtin_shufflevector(a, b, 0, 1, 2, 3, 4, 5, 6, 7, 8, 9, 10, 11, 12, 13, 14, 15)
template <bool AG> DEVI void smf(f32x4& c, const bf16x8& a, const bf16x16& w, unsigned ix) {
    if constexpr (AG) asm volatile("v_smfmac_f32_16x16x64_bf16 %0, %1, %2, %3" : "+v"(c) : "v"(a), "a"(w), "v"(ix));
    else asm volatile("v_smfmac_f32_16x16x64_bf16 %0, %1, %2, %3" : "+v"(c) : "v"(a), "v"(w), "v"(ix));
}
template <bool AG, bool FIRST> DEVI void mfa(f32x4& c, const bf16x8& w, const bf16x8& x) {
    if constexpr (AG && FIRST) asm volatile("v_mfma_f32_16x16x32_bf16 %0, %1, %2, 0" : "=&v"(c) : "a"(w), "v"(x));
    else if constexpr (AG) asm volatile("v_mfma_f32_16x16x32_bf16 %0, %1, %2, %0" : "+v"(c) : "a"(w), "v"(x));
    else if constexpr (FIRST) asm volatile("v_mfma_f32_16x16x32_bf16 %0, %1, %2, 0" : "=&v"(c) : "v"(w), "v"(x));
    else asm volatile("v_mfma_f32_16x16x32_bf16 %0, %1, %2, %0" : "+v"(c) : "v"(w), "v"(x));
}
template <bool AG, bool FIRST> DEVI void mfb(f32x4& c, const bf16x8& x, const bf16x8& w) {
    if constexpr (AG && FIRST) asm volatile("v_mfma_f32_16x16x32_bf16 %0, %1, %2, 0" : "=&v"(c) : "v"(x), "a"(w));
    else if constexpr (AG) asm volatile("v_mfma_f32_16x16x32_bf16 %0, %1, %2, %0" : "+v"(c) : "v"(x), "a"(w));
    else if constexpr (FIRST) asm volatile("v_mfma_f32_16x16x32_bf16 %0, %1, %2, 0" : "=&v"(c) : "v"(x), "v"(w));
    else asm volatile("v_mfma_f32_16x16x32_bf16 %0, %1, %2, %0" : "+v"(c) : "v"(x), "v"(w));
}
DEVI f32x4 mf(const bf16x8& a, const bf16x8& b, const f32x4& c) { return __builtin_amdgcn_mfma_f32_16x16x32_bf16(__builtin_bit_cast(hbf8, a), __builtin_bit_cast(hbf8, b), c, 0, 0, 0); }
template <int N, class F> DEVI void sfor(F f) { [&]<int... I>(std::integer_sequence<int, I...>) { (f(std::integral_constant<int, I>{}), ...); }(std::make_integer_sequence<int, N>{}); }
DEVI bf16x8 lds128(const char* p) { return *(const bf16x8*)p; }
DEVI bf16x4 ldstr(const char* p) { return __builtin_amdgcn_ds_read_tr16_b64_v4i16((LAS bf16x4*)p); }
DEVI bf16x8 ldstr2(const char* p, int d) { bf16x4 a = ldstr(p), b = ldstr(p + d); return __builtin_shufflevector(a, b, 0, 1, 2, 3, 4, 5, 6, 7); }

#define STAMP(s, k)
#define QSTAMP(f, i)
#define STOP(s)
#define PFLUSH(s)
#define PROF_DECL
#define RSTAMP(i)
#define PCOUNT(i)
#define PROF_OUT

template <int NT, int OFF> DEVI void wp_issue(const u16* W, int ld, unsigned ring, int l) {
    sfor<NT>([&](auto I) { constexpr int i = decltype(I)::value;
        const int id = i * 64 + l, row = id / (2 * NT), cc = id % (2 * NT);
        dma1<OFF + i * 1024>(W, (unsigned)(row * ld + cc * 8) * 2u, ring); });
}
template <int NT, bool STD> DEVI bf16x8 wp_read(const char* slot, int T, int l) {
    const int row = (STD ? 8 : 4) * (l >> 4) + ((l >> 2) & 3);
    return ldstr2(slot + row * (NT * 32) + T * 32 + (l & 3) * 8, (STD ? 4 : 16) * NT * 32);
}
template <int NU, int NI, int D, class ISSUE, class READ> DEVI void wstream(char* ring, ISSUE issue, READ read) {
    const unsigned base = (unsigned)(size_t)(LAS char*)ring;
    sfor<D>([&](auto U) { constexpr int u = decltype(U)::value; if constexpr (u < NU) issue(U, base); });
    sfor<NU>([&](auto U) {
        constexpr int u = decltype(U)::value, left = (NU - u < D ? NU - u : D) - 1;
        asm volatile("s_waitcnt vmcnt(%0)" :: "n"(left * NI) : "memory");
        read(U, ring + (u % D) * NI * 1024);
        __builtin_amdgcn_s_waitcnt(0xc07f);
        asm volatile("" ::: "memory");
        if constexpr (u + D < NU) issue(std::integral_constant<int, u + D>{}, base);
    });
}

constexpr int L_XN = 0;
constexpr int L_K = 40960;
constexpr int L_MEM = L_K + 12288;
constexpr int L_QM = L_MEM + 10240;
constexpr int L_Q = L_QM + 12288;
constexpr int L_W = L_Q + 12288;
#define IC(x) std::integral_constant<int, (x)>{}
#define CV(X) decltype(X)::value
DEVI bf16x8 lr128(unsigned a) { return *(const LAS bf16x8*)(size_t)a; }
DEVI bf16x4 lrtr(unsigned a) { return __builtin_amdgcn_ds_read_tr16_b64_v4i16((LAS bf16x4*)(size_t)a); }
#define CAT4(a, b) __builtin_shufflevector(a, b, 0, 1, 2, 3, 4, 5, 6, 7)
DEVI void lw128(unsigned a, u32x4 v) { *(LAS u32x4*)(size_t)a = v; }
DEVI void lw64(unsigned a, u32x2 v) { *(LAS u32x2*)(size_t)a = v; }
DEVI void lw32(unsigned a, unsigned v) { *(LAS unsigned*)(size_t)a = v; }
template <int N> DEVI void bar_lgkm() { asm volatile("s_waitcnt lgkmcnt(%0)\n\ts_barrier" :: "n"(N) : "memory"); }
DEVI void atom1(__amdgpu_buffer_rsrc_t rs, unsigned voff, unsigned soff) {
    unsigned one; asm volatile("v_mov_b32 %0, 1\n\tbuffer_atomic_add %0, %1, %2, %3 offen" : "=&v"(one) : "v"(voff), "s"(rs), "s"(soff) : "memory");
}
DEVI unsigned vadd(unsigned v, unsigned sc) { unsigned d; asm volatile("v_add_u32 %0, %1, %2" : "=v"(d) : "s"(sc), "v"(v)); return d; }
DEVI float vzero() { float z; asm volatile("v_mov_b32 %0, 0" : "=v"(z)); return z; }
template <int B, int I> DEVI void xn_op(const Ctx& k, unsigned v, unsigned src, unsigned dst) {
    if constexpr (I == 0) set_m0(dst + B * 8192);
    else if constexpr (I == 1) dmab<0>(k.rs, v, src + B * 16384);
    else dmab<1024>(k.rs, v, src + B * 16384);
}
DEVI void xn_all(const Ctx& k, unsigned v, unsigned src, unsigned dst) {
    sfor<5>([&](auto B) { xn_op<CV(B), 0>(k, v, src, dst); asm volatile("s_nop 0"); xn_op<CV(B), 1>(k, v, src, dst); xn_op<CV(B), 2>(k, v, src, dst); });
}
struct Xn {
    unsigned src, dst, fa, fi, fx, sg, need, t, sb, v, c, off;
    unsigned long long flip, m;
    DEVI void init(const Ctx& k) {
        src = k.seg(PG_XNA, 0) + k.w * 2048; dst = L_XN + k.w * 2048; sg = 0; off = OOB;
        fa = k.p0 + CNT(C_XN, 0); fi = k.c ? 0u : 128u; fx = k.c ? 256u : 0u;
        const unsigned q = (unsigned)(k.par >> PG_XNA), d = q ^ (q >> 1);
        flip = ((unsigned long long)(d & 1) << 16) | ((unsigned long long)(d & 2) << 31) | ((unsigned long long)(d & 4) << 46);
    }
    template <int I> DEVI void next(const Ctx& k, unsigned l16) {
        if constexpr (I == 0) { sg += 1; src += 131072; PINS(sg); PINS(src); }
        if constexpr (I == 1) asm volatile("s_bitcmp1_b64 %1, %2\n\ts_cselect_b32 %0, 0x2000, 0" : "=s"(t) : "s"(flip), "s"(sg) : "scc");
        if constexpr (I == 2) { src ^= t; fa += fi; PINS(src); PINS(fa); }
        if constexpr (I == 3) { fa ^= fx; PINS(fa); asm volatile("v_and_b32 %0, %1, %2" : "=v"(c) : "s"(fx >> 1), "v"(l16)); }
        if constexpr (I == 4) c = ld32(k.rs, c, fa);
        if constexpr (I == 5) { need = k.c ? sg : 7u; PINS(need); }
        if constexpr (I == 6) { need = sg < 64u ? need : 0x7FFFFFFFu; PINS(need); }
        if constexpr (I == 7) asm volatile("v_cmp_gt_i32_e64 %0, %1, %2" : "=s"(m) : "s"(need), "v"(c) : "memory");
        if constexpr (I == 8) { off = m != 0 ? OOB : 0u; PINS(off); }
        if constexpr (I == 9) asm volatile("v_or_b32 %0, %1, %2" : "=v"(v) : "s"(off), "v"(l16));
        if constexpr (I == 10) { off = sg < 64u ? off : 0u; PINS(off); }
    }
    template <int B, int I> DEVI void op(const Ctx& k) {
        if constexpr (I == 0) { set_m0(dst + B * 8192); sb = src + B * 16384; PINS(sb); }
        else dmab<(I - 1) * 1024>(k.rs, v, sb);
    }
    template <int NB> DEVI void late(const Ctx& k, unsigned l16) {
        xn_wait(k, l16, sg);
        v = l16;
        sfor<NB>([&](auto B) { op<CV(B), 0>(k); asm volatile("s_nop 0"); op<CV(B), 1>(k); op<CV(B), 2>(k); });
        DRAIN();
    }
};
template <int OFF> DEVI u32x4 ald128(__amdgpu_buffer_rsrc_t rs, unsigned v, unsigned s) { u32x4 d; asm volatile("buffer_load_dwordx4 %0, %1, %2, %3 offen offset:%4 sc1" : "=v"(d) : "v"(v), "s"(rs), "s"(s), "n"(OFF) : "memory"); return d; }
struct Gather {
    u32x4 p0, p1, v0, v1;
    unsigned gw, mx, a;
    unsigned long long bad;
    unsigned b, base;
    DEVI void init(const Ctx& k) { base = k.memf(0); }
    template <int I> DEVI void op(const Ctx& k, unsigned l16, int s) {
        if constexpr (I == 0) { a = ((unsigned)s << 15) & 0x1F8000u; PINS(a); }
        if constexpr (I == 1) { a += base; b = a + 6144 + (k.w >> 1) * 8192; PINS(a); PINS(b); }
        if constexpr (I == 2) gw = vadd(l16, k.w * 1024);
        if constexpr (I == 3) p0 = ald128<0>(k.rs, l16, a);
        if constexpr (I == 4) v0 = ald128<2048>(k.rs, gw, a);
        if constexpr (I == 5) v1 = ald128<0>(k.rs, gw, b);
        if constexpr (I == 6) p1 = ald128<1024>(k.rs, l16, a);
    }
    template <int N> DEVI void wait() { asm volatile("s_waitcnt vmcnt(%4)" : "+v"(p0), "+v"(p1), "+v"(v0), "+v"(v1) : "n"(N) : "memory"); }
    template <int I> DEVI void fin() {
#define MX2(A, B, C, D) asm volatile("v_max3_u32 %0, %0, %1, %2\n\tv_max3_u32 %0, %0, %3, %4" : "+v"(mx) : "v"(A), "v"(B), "v"(C), "v"(D))
        if constexpr (I == 0) { lw128(gw + L_MEM + 2048, v0); asm volatile("v_max3_u32 %0, %1, %2, %3\n\tv_max3_u32 %0, %0, %4, %5" : "=&v"(mx) : "v"(p0[0]), "v"(p0[1]), "v"(p0[2]), "v"(p0[3]), "v"(p1[0])); }
        if constexpr (I == 1) { lw128(gw + L_MEM + 6144, v1); MX2(p1[1], p1[2], p1[3], v0[0]); }
        if constexpr (I == 2) MX2(v0[1], v0[2], v0[3], v1[0]);
        if constexpr (I == 3) MX2(v1[1], v1[2], v1[3], v1[3]);
        if constexpr (I == 4) asm volatile("v_cmp_eq_u32_e64 %0, -1, %1" : "=s"(bad) : "v"(mx));
    }
    DEVI void again(const Ctx& k, unsigned l16) {
        asm volatile("buffer_load_dwordx4 %0, %4, %5, %6 offen sc1\n\tbuffer_load_dwordx4 %1, %4, %5, %6 offen offset:1024 sc1\n\tbuffer_load_dwordx4 %2, %7, %5, %6 offen offset:2048 sc1\n\tbuffer_load_dwordx4 %3, %7, %5, %8 offen sc1\n\ts_waitcnt vmcnt(0)"
            : "+v"(p0), "+v"(p1), "+v"(v0), "+v"(v1) : "v"(l16), "s"(k.rs), "s"(a), "v"(gw), "s"(b) : "memory");
        fin<0>(); fin<1>(); fin<2>(); fin<3>(); fin<4>();
        asm volatile("s_nop 3");
    }
};
DEVI void clr2(f32x4& c) { f32x2 a, b; asm volatile("v_mov_b64 %0, 0\n\tv_mov_b64 %1, 0" : "=v"(a), "=v"(b)); c = __builtin_shufflevector(a, b, 0, 1, 2, 3); }
#define NOP7_3(a) asm volatile("s_nop 7" : "+v"(a[0]), "+v"(a[1]), "+v"(a[2]))

struct G3 {
    f32x4 a0[3], a1[3];
    bf16x8 x0[20], x1[20], wl[20];
    unsigned l16, lwv;
    template <int KS> DEVI void r0() { x0[KS] = lr128(l16 + L_XN + KS * 2048); }
    template <int KS> DEVI void r1() { x1[KS] = lr128(l16 + L_XN + KS * 2048 + 1024); }
    template <int KS> DEVI void rw() { if constexpr (KS >= 4) wl[KS] = lr128(lwv + (KS - 4) * 1024); }
    template <int ID0, bool WL, int ks, int m> DEVI void mm(const bf16x16 (&W)[52]) {
        constexpr int b = ks >> 1, t = m % 3, id = ID0 + t * 10 + b;
        f32x4& c = m < 3 ? a0[t] : a1[t];
        const bf16x8& x = m < 3 ? x0[ks] : x1[ks];
        if constexpr (ID0 == 30) {
            if constexpr (WL && t == 2 && b >= 2) mfb<false, false>(c, x, wl[ks]);
            else if constexpr (ks & 1) mfb<(id < 32), false>(c, x, HI8(W[id % 52]));
            else mfb<(id < 32), ks == 0>(c, x, LO8(W[id % 52]));
        } else {
            if constexpr (WL && t == 2 && b >= 2) mfa<false, false>(c, wl[ks], x);
            else if constexpr (ks & 1) mfa<(id < 32), false>(c, HI8(W[id % 52]), x);
            else mfa<(id < 32), ks == 0>(c, LO8(W[id % 52]), x);
        }
        SCHED0();
    }
    template <int ID0, bool WL, int K0, int K1, int PF, bool H6 = false, class HOOK> DEVI void run(const bf16x16 (&W)[52], HOOK hook) {
        sfor<K1 - K0>([&](auto KK) {
            constexpr int kk = CV(KK), ks = K0 + kk;
            sfor<6>([&](auto M) {
                constexpr int m = CV(M);
                mm<ID0, WL, ks, m>(W);
                if constexpr (ks + 2 < PF) {
                    if constexpr (m == 0) r0<(ks + 2) % 20>();
                    if constexpr (m == 1) r1<(ks + 2) % 20>();
                    if constexpr (m == 3 && WL) rw<(ks + 2) % 20>();
                }
                if constexpr (H6) hook(IC(kk * 6 + m));
                else { hook(IC(kk * 7 + (m < 5 ? m : 6))); if constexpr (m == 4) hook(IC(kk * 7 + 5)); }
                SCHED0();
            });
        });
    }
    u32x2 o;
    template <int I, int OFF> DEVI void out(unsigned d0, unsigned d1) {
        if constexpr (I >= 0 && I < 18) {
            constexpr int t = I / 6, rt = (I / 3) & 1;
            const f32x4& a = rt ? a1[t] : a0[t];
            if constexpr (I % 3 == 0) { o[0] = cvtpk(a[0], a[1]); PIN(o[0]); }
            else if constexpr (I % 3 == 1) { o[1] = cvtpk(a[2], a[3]); PIN(o[1]); }
            else if constexpr (OFF > 0) lw64(d0 + (OFF - 1) + rt * 6144 + t * 1024, o);
            else lw64((t == 1 ? d1 : d0) + OFF + rt * 6144 + (t == 2 ? 1024 : 0), o);
        }
    }
};
#define SPIX(l) (((l) & 2) ? 0xEEEEu : 0x4444u)
template <int I> DEVI void fold_op(f32x4& a, unsigned& o) {
    if constexpr (I == 0) asm volatile("v_add_f32 %0, %0, %1" : "+v"(a[0]) : "v"(a[2]));
    if constexpr (I == 1) asm volatile("v_add_f32 %0, %0, %1" : "+v"(a[1]) : "v"(a[3]));
    if constexpr (I == 2) asm volatile("v_cvt_pk_bf16_f32 %0, %1, %2" : "=v"(o) : "v"(a[0]), "v"(a[1]));
}

#define ANNOUNCE(s, now, d) (((unsigned)(s) << 16) | (((unsigned)(now) + (d)) & 0xFFFFu))

struct Soft {
    unsigned long long mA, mY, mC[4], mM, mD;
    unsigned v0, v1;
    float a[6], m, ml, M, t, u, sc; unsigned pk[3], msk;
    template <int I> DEVI void op(const Ctx& k, unsigned a0, unsigned a1, unsigned ph) {
#define A3(o) "+v"(a[o]), "+v"(a[o + 1]), "+v"(a[o + 2])
#define FMA(x) "v_fmamk_f32 " x ", " x ", 0x3dd53b95, "
        if constexpr (I == 5) asm volatile("v_mov_b32 %0, 0xf1800000" : "=v"(t));
        if constexpr (I == 6) asm volatile("v_cndmask_b32_e64 %0, %3, %0, %4\n\tv_cndmask_b32_e64 %1, %3, %1, %5\n\tv_cndmask_b32_e64 %2, %3, %2, %6" : A3(0) : "v"(t), "s"(mC[0]), "s"(mC[1]), "s"(mC[2]));
        if constexpr (I == 7) asm volatile("v_cndmask_b32_e64 %0, %3, %0, %4\n\tv_cndmask_b32_e64 %1, %3, %1, %5\n\tv_cndmask_b32_e64 %2, %3, %2, %5" : A3(3) : "v"(t), "s"(mC[3]), "s"(mM));
        if constexpr (I == 8) asm volatile("v_max3_f32 %0, %1, %2, %3\n\tv_max3_f32 %0, %0, %4, %5\n\tv_max_f32 %0, %0, %6" : "=&v"(m) : "v"(a[0]), "v"(a[1]), "v"(a[2]), "v"(a[3]), "v"(a[4]), "v"(a[5]));
        if constexpr (I == 9) asm volatile("v_mul_f32 %0, 0xbdd53b95, %3\n\t" FMA("%1") "%0\n\t" FMA("%2") "%0" : "=&v"(ml), "+v"(a[0]), "+v"(a[1]) : "v"(m));
        if constexpr (I == 10) asm volatile("v_max_f32_dpp %0, %3, %3 row_ror:8 row_mask:0xf bank_mask:0xf\n\t" FMA("%1") "%4\n\tv_cndmask_b32_e64 %2, %3, %0, %5" : "=&v"(t), "+v"(a[2]), "=&v"(M) : "v"(m), "v"(ml), "s"(mD));
        if constexpr (I == 11) asm volatile("v_mov_b32 %0, %3\n\tv_exp_f32 %1, %1\n\t" FMA("%2") "%4" : "=&v"(u), "+v"(a[0]), "+v"(a[3]) : "v"(M), "v"(ml));
        if constexpr (I == 12) asm volatile("v_permlane16_swap_b32 %0, %1\n\t" FMA("%2") "%3" : "+v"(M), "+v"(u), "+v"(a[4]) : "v"(ml));
        if constexpr (I == 13) asm volatile("v_max_f32 %0, %0, %1\n\tv_mov_b32 %1, %0\n\tv_exp_f32 %2, %2" : "+v"(M), "+v"(u), "+v"(a[1]));
        if constexpr (I == 14) asm volatile(FMA("%2") "%3\n\tv_permlane32_swap_b32 %0, %1" : "+v"(M), "+v"(u), "+v"(a[5]) : "v"(ml));
        if constexpr (I == 15) asm volatile("v_max_f32 %0, %0, %1\n\tv_exp_f32 %2, %2" : "+v"(M), "+v"(u), "+v"(a[2]));
        if constexpr (I == 16) asm volatile("v_sub_f32 %0, %2, %0\n\tv_mul_f32 %0, 0x3dd53b95, %0\n\tv_exp_f32 %1, %1" : "+v"(M), "+v"(a[3]) : "v"(m));
        if constexpr (I == 17) asm volatile("v_exp_f32 %0, %2\n\tv_exp_f32 %1, %1" : "=&v"(sc), "+v"(a[4]) : "v"(M));
        if constexpr (I == 18) asm volatile("v_exp_f32 %2, %2\n\tv_add_f32 %0, %3, %4\n\tv_add_f32 %1, %5, %6" : "=&v"(u), "=&v"(t), "+v"(a[5]) : "v"(a[0]), "v"(a[1]), "v"(a[2]), "v"(a[3]));
        if constexpr (I == 19) asm volatile("v_add_f32 %0, %0, %2\n\tv_add_f32 %1, %1, %3\n\tv_add_f32 %0, %0, %1" : "+v"(u), "+v"(t) : "v"(a[4]), "v"(a[5]));
        if constexpr (I == 20) asm volatile("v_mul_f32 %0, %0, %2\n\tv_mov_b32 %1, 0x7fff7fff" : "+v"(u), "=v"(msk) : "v"(sc));
        if constexpr (I == 21) asm volatile("v_add_f32_dpp %1, %0, %0 row_ror:8 row_mask:0xf bank_mask:0xf\n\tv_cndmask_b32_e64 %0, %0, %1, %2\n\tv_mov_b32 %1, %0" : "+v"(u), "=&v"(t) : "s"(mD));
        if constexpr (I == 22) asm volatile("s_nop 0\n\tv_permlane16_swap_b32 %0, %1\n\tv_add_f32 %0, %0, %1\n\tv_mov_b32 %1, %0" : "+v"(u), "+v"(t));
        if constexpr (I == 23) asm volatile("s_nop 0\n\tv_permlane32_swap_b32 %0, %1\n\tv_add_f32 %0, %0, %1\n\tv_rcp_f32 %0, %0" : "+v"(u), "+v"(t));
        if constexpr (I == 24) asm volatile("v_mul_f32 %0, %0, %3\n\tv_mul_f32 %1, %1, %0\n\tv_mul_f32 %2, %2, %0" : "+v"(u), "+v"(a[0]), "+v"(a[1]) : "v"(sc));
        if constexpr (I == 25) asm volatile("v_mul_f32 %0, %0, %3\n\tv_mul_f32 %1, %1, %3\n\tv_mul_f32 %2, %2, %3" : A3(2) : "v"(u));
        if constexpr (I == 26) asm volatile("v_mul_f32 %2, %2, %3\n\tv_cvt_pk_bf16_f32 %0, %4, %5\n\tv_cvt_pk_bf16_f32 %1, %6, %7" : "=&v"(pk[0]), "=&v"(pk[1]), "+v"(a[5]) : "v"(u), "v"(a[0]), "v"(a[1]), "v"(a[2]), "v"(a[3]));
        if constexpr (I == 27) asm volatile("v_cvt_pk_bf16_f32 %2, %3, %4\n\tv_bfi_b32 %0, %5, %0, %6\n\tv_bfi_b32 %1, %5, %1, %6" : "+v"(pk[0]), "+v"(pk[1]), "=&v"(pk[2]) : "v"(a[4]), "v"(a[5]), "v"(msk), "s"(ph));
        if constexpr (I == 28) { asm volatile("v_bfi_b32 %0, %1, %0, %2" : "+v"(pk[2]) : "v"(msk), "s"(ph)); st64(k.rs, v0, a0, (u32x2){pk[0], pk[1]}); }
        if constexpr (I == 29) st32(k.rs, v1, a1, pk[2]);
    }
};
#define QIDX(t, b) ((t) * 10 + (b))
DEVI void qk_role(const Params& p, const Ctx& k, char* smem, int h) {
    PROF_DECL
    const int w = k.w;
    sleep_ticks(k.c ? WD1 + QDLY : QDLY);
    bf16x16 W[52];
    {
        const int l = k.l;
        bf16x8 lo[6], wt[16];
        const int col = h * 192 + w * 48;
        wstream<20, 6, 6>(smem + w * 36864,
            [&](auto U, unsigned sl) { constexpr int u = CV(U);
                wp_issue<3, (u % 6) * 6144>(p.Wk + (size_t)u * 32 * 6144 + col, 6144, sl, l); wp_issue<3, (u % 6) * 6144 + 3072>(p.Wq + (size_t)u * 32 * 6144 + col, 6144, sl, l); },
            [&](auto U, char* sp) { constexpr int u = CV(U), b = u >> 1;
#pragma unroll
                for (int t = 0; t < 6; ++t) {
                    bf16x8 f = wp_read<3, true>(sp + (t / 3) * 3072, t % 3, l);
                    if (t == 5 && b >= 2) wt[(u - 4) & 15] = f;
                    else if (u & 1) W[QIDX(t, b) % 52] = CAT8(lo[t], f);
                    else lo[t] = f;
                } });
        RAWBAR()
#pragma unroll
        for (int i = 0; i < 16; ++i) *(bf16x8*)(smem + L_W + w * 16384 + i * 1024 + l * 16) = wt[i];
    }
    RSTAMP(2)
    Gather G;
    Soft S;
    const int hw = h >> 3, hh = h & 7;
    unsigned l16, ix, dqm, dq, trp, qp, vz;
    {
        const int l = k.l, g = l >> 4, n = l & 15, r = n & 7, u = w & 1;
        const bool hf = n >= 8, dup = w != 1;
        S.mA = __ballot(dup && hf); S.mD = __ballot(dup);
        const int lim = w == 1 ? n - 4 * g : (w == 0 || !hf) ? 64 : 8 * u + r - 4 * g;
#pragma unroll
        for (int i = 0; i < 4; ++i) S.mC[i] = __ballot(i <= lim);
        const bool okm = !(dup && hf);
        S.mY = __ballot((dup && hf) == (w != 0)); S.mM = __ballot(okm);
        S.v0 = w == 0 ? hh * 512 + ((g >> 1) * 16 + (r >> 1) * 4 + hf * 2 + (r & 1)) * 16 + (g & 1) * 8 : w == 1 ? hh * 512 + l * 8 : hh * 1024 + (g * 16 + 8 * u + r) * 16 + hf * 8;
        S.v1 = !okm ? OOB : w == 0 ? 4096 + hh * 128 + r * 16 + g * 4 : (w == 1 ? 4096 + n * 8 : 6144 + (8 * u + r) * 8) + hh * 256 + (g >> 1) * 128 + (g & 1) * 4;
        l16 = l * 16; ix = SPIX(l);
        dqm = L_QM + (w >> 1) * 3072 + u * 512 + (g >> 1) * 128 + n * 8 + (g & 1) * 4;
        dq = L_QM + 6144 + (w >> 1) * 3072 + u * 512 + g * 128 + ((n * 8) ^ ((g >> 1) * 64));
        trp = L_QM + 6144 + (l & 3) * 128 + (((l >> 2) * 8) ^ ((l & 2) * 32));
        qp = w == 1 ? trp + 6144 : (w == 0 ? L_QM : L_Q + 6144 + u * 256) + (l & 1) * 128 + (((l >> 2) * 8) ^ (w == 3 ? 64 : 0));
        vz = l == 0 ? 0u : OOB;
        PIN(S.v0); PIN(S.v1); PIN(l16); PIN(ix); PIN(dqm); PIN(dq); PIN(trp); PIN(qp); PIN(vz);
    }
    const unsigned po0 = k.p0 + PR_B + hw * 49152 + (w == 0 ? 0 : w == 1 ? 32768 : 16384), po1 = k.p0 + PR_B + hw * 49152 + (w == 0 ? 0 : 32768);
    const unsigned pdum = k.pg(1) + DUMMY_B, pbc = w == 0 ? k.p0 + CNT(C_BC, hw * 16 + hh) : pdum + 65536 + h * 512 + w * 128;
    Xn X;
    X.init(k); G.init(k);
    unsigned long long now = 0, done = 0;
    unsigned a0 = pdum + (po0 - k.p0 - PR_B), a1 = pdum + (po1 - k.p0 - PR_B), ph = 0;
    G3 A, B;
    A.l16 = l16; B.l16 = l16; B.lwv = vadd(l16, L_W + w * 16384);
#pragma unroll
    for (int t = 0; t < 3; ++t) { B.a0[t] = (f32x4){0.f, 0.f, 0.f, 0.f}; B.a1[t] = B.a0[t]; }
    f32x4 ac[6];
#pragma unroll
    for (int t = 0; t < 6; ++t) clr2(ac[t]);
    sfor<7>([&](auto I) { G.op<CV(I)>(k, l16, 0); });
    for (int s = -1; s < 64; ++s) {
        if (X.off != 0) { PCOUNT(1) X.late<4>(k, l16); }
        bf16x8 k0[6], k1[6];
        {
            bf16x8 m[10], wa[10], wb[10];
            unsigned fo[6];
            G.wait<4>();
            STOP(s) STAMP(s, 0)
            sfor<6>([&](auto I) { constexpr int i = CV(I), t = (i + 5) % 6, id = QIDX(t, 0);
                smf<(id < 32)>(ac[t], __builtin_bit_cast(bf16x8, G.p0), W[id], ix);
                SCHED0();
                G.fin<i>();
                SCHED0(); });
            if (G.bad != 0) {
                unsigned spins = 0;
_Pragma("unroll 1")
                do { PCOUNT(0) G.again(k, l16); } while (G.bad != 0 && !spin_fail(k, spins++));
                STOP(s) STAMP(s, 0)
#pragma unroll
                for (int t = 0; t < 6; ++t) clr2(ac[t]);
                asm volatile("s_nop 1" : "+v"(ac[0]), "+v"(ac[1]), "+v"(ac[2]), "+v"(ac[3]), "+v"(ac[4]), "+v"(ac[5]));
                sfor<6>([&](auto I) { constexpr int t = (CV(I) + 5) % 6, id = QIDX(t, 0);
                    smf<(id < 32)>(ac[t], __builtin_bit_cast(bf16x8, G.p0), W[id], ix); });
            }
            asm volatile("s_waitcnt lgkmcnt(0)\n\ts_barrier" : "+s"(done) :: "memory");
            unsigned an = (unsigned)done - (unsigned)now;
            PINS(an);
            now = rt_async();
            m[1] = __builtin_bit_cast(bf16x8, G.p1);
            m[2] = lr128(l16 + L_MEM + 2048);
            SCHED0();
            auto fin = [&](auto J) { constexpr int j = CV(J), t = 5 - j / 3;
                if constexpr (j >= 0 && j < 18) fold_op<j % 3>(ac[t], fo[t]); };
            auto put = [&](auto T) { constexpr int t = CV(T); if constexpr (t < 3) lw32(dq + t * 1024, fo[t]); else lw32(dqm + (t - 3) * 1024, fo[t]); };
            sfor<54>([&](auto I) {
                constexpr int i = CV(I) + 6, u = i - 42, b = i < 42 ? i / 6 : 7 + u % 3, t = b == 2 ? i % 6 : i < 42 ? (i + 5) % 6 : 5 - u / 3, id = QIDX(t, b);
                if constexpr (i == 44) lgkm0(now);
                if constexpr (t == 5 && b >= 2) smf<false>(ac[5], m[b], CAT8(wa[b], wb[b]), ix);
                else smf<(id < 32)>(ac[t], m[b], W[id % 52], ix);
                SCHED0();
                if constexpr (i < 36) {
                    constexpr int bb = i / 6 + 2;
                    if constexpr (i % 6 == 0) m[bb] = lr128(l16 + L_MEM + bb * 1024);
                    if constexpr (i % 6 == 3) wa[bb] = lr128(B.lwv + (bb - 2) * 2048);
                    if constexpr (i % 6 == 4) wb[bb] = lr128(B.lwv + (bb - 2) * 2048 + 1024);
                }
                if constexpr (i == 7) wa[2] = lr128(B.lwv);
                if constexpr (i == 8) wb[2] = lr128(B.lwv + 1024);
                if constexpr (i == 32) m[8] = lr128(l16 + L_MEM + 8192);
                if constexpr (i == 31) m[9] = lr128(l16 + L_MEM + 9216);
                if constexpr (i == 36) wa[8] = lr128(B.lwv + 12288);
                if constexpr (i == 37) wb[8] = lr128(B.lwv + 13312);
                if constexpr (i == 38) wa[9] = lr128(B.lwv + 14336);
                if constexpr (i == 39) wb[9] = lr128(B.lwv + 15360);
                if constexpr (i == 46) st32(k.rs, vz, pbc, fo[0]);
                B.out<i - 9, 6145>(dq, dq);
                if constexpr (i == 29) { an = min(an, 200u) + DQ; PINS(an); }
                if constexpr (i == 44) an = spack(an + (unsigned)now, s);
                if constexpr (i == 45) { fo[0] = an; PIN(fo[0]); }
                if constexpr (u >= 6 && u % 3 != 2) {
                    constexpr int r = (u / 3 - 2) * 2 + u % 3, f = (r + 6) / 2 % 4;
                    if constexpr (r < 2) A.r0<r>(); else if constexpr (r & 1) k1[f] = lr128(l16 + L_K + (6 + f) * 1024); else k0[f] = lr128(l16 + L_K + f * 1024);
                }
                if constexpr (u >= 8 && u % 3 == 2) put(IC(5 - (u - 8) / 3));
                if constexpr (u >= 5) fin(IC(u - 5));
                if constexpr (i == 6) { QSTAMP(1, 1) }
                if constexpr (i == 12) { QSTAMP(1, 2) }
                if constexpr (i == 41) { QSTAMP(1, 3) }
                if constexpr (i == 59) { QSTAMP(1, 4) }
                SCHED0();
            });
            sfor<9>([&](auto I) { constexpr int i = CV(I);
                A.mm<0, false, i / 3, i % 3>(W);
                if constexpr (i == 0) { A.r0<2>(); fin(IC(13)); }
                if constexpr (i == 1) { fin(IC(14)); put(IC(1)); }
                if constexpr (i == 2) { fin(IC(15)); fin(IC(16)); }
                if constexpr (i == 3) { fin(IC(17)); put(IC(0)); }
                if constexpr (i == 4) A.r0<3>();
                if constexpr (i == 5) A.r0<4>();
                if constexpr (i >= 6) X.op<4, i % 3>(k);
                SCHED0(); });
        }
        asm volatile("s_waitcnt vmcnt(3) lgkmcnt(2)\n\ts_barrier" ::: "memory");
        QSTAMP(0, 1) QSTAMP(1, 5)
        f32x4 sc[3];
        {
            bf16x4 q[12], km[12];
            auto rd = [&](auto R) { constexpr int r = CV(R);
                constexpr int tab[34] = {0, 1, 2, 3, 12, 13, 4, 5, 14, 15, 6, 7, 24, 25, 16, 17, 30, 8, 9, 26, 27, 18, 19, 10, 11, 28, 29, 20, 21, 22, 23, 31, 32, 33};
                constexpr int c = tab[r % 34];
                if constexpr (r >= 34) { }
                else if constexpr (c < 12) q[c] = lrtr(qp + c * 512);
                else if constexpr (c < 24) km[c - 12] = lrtr(trp + (c - 12) * 512);
                else if constexpr (c < 30) { if constexpr (c & 1) k1[3 + (c - 24) / 2] = lr128(l16 + L_K + (9 + (c - 24) / 2) * 1024); else k0[3 + (c - 24) / 2] = lr128(l16 + L_K + (3 + (c - 24) / 2) * 1024); }
                else if constexpr (c == 30) A.r0<5>();
                else A.r1<(c - 31) % 20>(); };
            rd(IC(0)); rd(IC(1));
            SCHED0();
            sfor<27>([&](auto I) { constexpr int i = CV(I);
                constexpr int seq[27] = {-9, -10, -11, -12, -13, -14, 0, 1, 3, 4, 2, 6, 7, 5, 9, 10, 8, -15, 12, 13, 11, -16, 15, 16, 14, -17, 17};
                constexpr int c = seq[i], ks = (c < 0 ? -c : c) / 3, j = (c < 0 ? -c : c) % 3;
                if constexpr (c < 0) A.mm<0, false, ks, j>(W);
                else mfa<false, ks == 0>(sc[j], j == 0 ? k0[ks] : j == 1 ? k1[ks] : CAT4(km[2 * ks], km[2 * ks + 1]), CAT4(q[2 * ks], q[2 * ks + 1]));
                SCHED0();
                constexpr int r0 = 2 + i + (i + 2) / 3, r1 = 2 + (i + 1) + (i + 3) / 3;
                sfor<r1 - r0>([&](auto J) { rd(IC(r0 + CV(J))); });
                if constexpr (i == 25) S.op<5>(k, 0, 0, 0);
                SCHED0(); });
        }
        QSTAMP(0, 2)
        {
            unsigned d0;
            float y0, y1;
            auto soft = [&](auto I) { constexpr int i = CV(I);
                if constexpr (i == 0) { asm volatile("" : "+v"(sc[0]), "+v"(sc[1])); y0 = csel(sc[0][2], sc[1][0], S.mY); y1 = csel(sc[0][3], sc[1][1], S.mY); S.a[0] = csel(sc[0][0], y0, S.mA); }
                if constexpr (i == 1) { S.a[1] = csel(sc[0][1], y1, S.mA); S.a[2] = csel(y0, sc[1][2], S.mA); S.a[3] = csel(y1, sc[1][3], S.mA); }
                if constexpr (i == 3) { asm volatile("" : "+v"(sc[2])); S.a[4] = sc[2][0]; S.a[5] = sc[2][1]; }
                if constexpr (i >= 2) S.op<i + 4>(k, a0, a1, ph);
                if constexpr (i == 26) done = rt_async();
                if constexpr (i == 27) quit_if(s, 63); };
            sfor<18>([&](auto I) { constexpr int i = CV(I);
                A.mm<0, false, i / 3, 3 + i % 3>(W);
                if constexpr (i == 2 || i == 5 || i == 8) A.r1<3 + i / 3>();
                if constexpr (i == 10) A.r0<6>();
                if constexpr (i == 12) A.r1<6>();
                if constexpr (i == 14) A.r0<7>();
                if constexpr (i == 17) A.r1<7>();
                soft(I);
                SCHED0(); });
            A.run<0, false, 6, 20, 20, true>(W, [&](auto I) { constexpr int i = CV(I);
                soft(IC(18 + i));
                if constexpr (i == 8) { QSTAMP(0, 3) }
                if constexpr (i == 40) asm volatile("s_waitcnt vmcnt(2)\n\ts_barrier" ::: "memory");
                if constexpr (i == 15) { a0 = (X.sg & 3) * PR_SZ; PINS(a0); }
                if constexpr (i == 16) { a1 = a0 + po1; a0 += po0; PINS(a0); PINS(a1); }
                if constexpr (i == 21) { ph = (X.sg & 4) ? 0x80008000u : 0u; PINS(ph); }
                if constexpr (i == 22 || i == 27) G.op<i / 27>(k, l16, X.sg);
                if constexpr (i >= 45 && i < 64 && (i % 6 == 3 || i % 6 == 4)) X.next<(i / 6 - 7) * 2 + i % 6 - 3>(k, l16);
                if constexpr (i == 81) { d0 = vadd(l16, L_K + (w >> 1) * 3072 + (w & 1) * 8); B.lwv = vadd(l16, L_W + w * 16384); }
                if constexpr (i == 72) B.r0<0>();
                if constexpr (i == 73) B.r1<0>();
                if constexpr (i == 78) B.r0<1>();
                if constexpr (i == 79) B.r1<1>(); });
            B.run<30, true, 0, 20, 20, true>(W, [&](auto I) { constexpr int i = CV(I);
                if constexpr (i < 36 && i % 6 != 0 && i % 6 != 1 && i % 6 != 3) A.out<(i / 6) * 3 + (i % 6) / 2 - 1 + (i % 6) / 5, 1>(d0, d0);
                if constexpr (i == 38 || i == 40) X.next<7 + (i - 38) / 2>(k, l16);
                if constexpr (i == 44 || i == 46) X.next<9 + (i - 44) / 2>(k, l16);
                if constexpr (i == 47 || i == 95) __builtin_amdgcn_s_barrier();
                if constexpr (i % 48 < 18 && i >= 48 && (i % 6 == 2 || i % 6 == 4)) X.op<(i / 48 - 1) * 2 + ((i % 48) / 3) / 3, ((i % 48) / 3) % 3>(k);
                if constexpr (i / 6 >= GPOS && i / 6 < GPOS + 3 && (i % 6 == 2 || i % 6 == 4) && i != GPOS * 6 + 2) G.op<1 + (i / 6 - GPOS) * 2 + (i % 6) / 4>(k, l16, 0);
                if constexpr (i >= 113 && i < 119) clr2(ac[(i + 6 - 113 + 5) % 6]); });
            static_assert(GPOS >= 11 && GPOS <= 13, "GPOS");
            QSTAMP(0, 4) QSTAMP(0, 5)
            PFLUSH(s)
        }
    }
    PROF_OUT
}

#define OIDX(t, b) (22 + (t) * 3 + (b))
DEVI void vo_role(const Params& p, const Ctx& k, char* smem, int h) {
    PROF_DECL
    const int w = k.w;
    if (k.c) sleep_ticks(WD1);
    bf16x16 W[52];
    {
        const int l = k.l;
        bf16x8 lo[10], wt[16];
        const u16* wv = p.Wv + h * 192 + w * 48;
        const u16* wo = p.Wo + (size_t)h * 192 * 640 + w * 160;
        wstream<22, 6, 6>(smem + w * 36864,
            [&](auto U, unsigned sl) { constexpr int u = CV(U);
                if constexpr (u >= 12) { wp_issue<3, (u % 6) * 6144>(wv + (size_t)(u - 12) * 64 * 6144, 6144, sl, l); wp_issue<3, (u % 6) * 6144 + 3072>(wv + (size_t)((u - 12) * 64 + 32) * 6144, 6144, sl, l); }
                else { wp_issue<5, (u % 6) * 6144>(wo + (u >> 1) * 32 * 640 + (u & 1) * 80, 640, sl, l); dma1<(u % 6) * 6144 + 5120>(wo, l * 16, sl); } },
            [&](auto U, char* sp) { constexpr int u = CV(U);
                if constexpr (u >= 12) {
                    constexpr int b = u - 12;
#pragma unroll
                    for (int t = 0; t < 3; ++t) {
                        bf16x8 x = wp_read<3, true>(sp, t, l), y = wp_read<3, true>(sp + 3072, t, l);
                        if (t == 2 && b >= 2) { wt[(2 * (b - 2)) & 15] = x; wt[(2 * (b - 2) + 1) & 15] = y; } else W[(t * 10 + b) % 52] = CAT8(x, y);
                    }
                } else {
                    constexpr int ks = u >> 1, b = ks >> 1;
#pragma unroll
                    for (int i = 0; i < 5; ++i) {
                        const int t = (u & 1) * 5 + i;
                        bf16x8 f = wp_read<5, false>(sp, i, l);
                        if (ks & 1) W[OIDX(t, b)] = CAT8(lo[t], f); else lo[t] = f;
                    }
                } });
        RAWBAR()
#pragma unroll
        for (int i = 0; i < 16; ++i) *(bf16x8*)(smem + L_W + w * 16384 + i * 1024 + l * 16) = wt[i];
    }
    RSTAMP(2)
    unsigned l16, lwv, ix, dq0, trp, vo, vsig;
    {
        const int l = k.l, g = l >> 4, n = l & 15, q = (l >> 2) & 3, pp = l & 3;
        l16 = l * 16; lwv = l16 + L_W + w * 16384; ix = SPIX(l);
        dq0 = L_QM + (24 * w + (n >> 2) * 2 + (n & 1)) * 32 + g * 8 + ((n >> 1) & 1) * 4;
        trp = L_QM + (16 * (g >> 1) + 4 * (g & 1) + 8 * (q >> 1) + (q & 1)) * 32 + pp * 8;
        vo = EXP(h * 512) + (g >> 1) * 256 + n * 16 + (g & 1) * 8;
        vsig = l == 0 ? 0u : OOB;
        PIN(l16); PIN(lwv); PIN(ix); PIN(dq0); PIN(trp); PIN(vo); PIN(vsig);
    }
    const unsigned pdum = k.pg(1) + DUMMY_B, pw = pdum + 98304 + h * 512 + w * 128, pbc = w == 0 ? k.p0 + CNT(C_BC, (h >> 3) * 16 + 8 + (h & 7)) : pw;
    Gather G;
    G3 V;
    V.l16 = l16; V.lwv = lwv;
    unsigned long long now = 0, done = 0;
    f32x4 yl = {0.f, 0.f, 0.f, 0.f};
    u32x2 lo = {0u, 0u};
    u32x4 o;
    unsigned yb = k.yx(7, w) + EXP(h * 1024);
    auto pack = [&](const f32x4& y) { o = (u32x4){lo[0], lo[1], cvtpk(y[0], y[1]), cvtpk(y[2], y[3])}; PIN(o); };
    Xn X;
    X.init(k); G.init(k);
    X.late<5>(k, l16);
    RAWBAR()
    const unsigned ypar = (unsigned)(k.par >> (PG_YX + (w >> 1))), yo = ((unsigned)(PG_YX + (w >> 1)) << 21) + (w & 1) * 1048576 + EXP(h * 1024);
    const unsigned ym0 = k.ym(0) + w * 163840, ym2 = k.ym(2) + w * 163840;
    unsigned ym = pdum + w * 163840, ys, t;
    V.r0<0>(); V.r1<0>(); V.r0<1>(); V.r1<1>();
    for (int s = -1; s < 64; ++s) {
        f32x4 ac[3];
        SCHED0();
        V.run<0, true, 0, 16, 17, true>(W, [&](auto I) { constexpr int i = CV(I);
            if constexpr (i == 2) pack(yl);
            if constexpr (i == 4) __builtin_amdgcn_raw_buffer_store_b128(o, k.rs, (int)l16, (int)yb, AUX_SC1);
            if constexpr (i == 8) { t = (X.sg & 7) * 2; PINS(t); }
            if constexpr (i == 10) { yb = (ypar >> t) & 1; PINS(yb); }
            if constexpr (i == 14) { t = (t << 21) + yo; PINS(t); }
            if constexpr (i == 16) { yb = (yb << 13) + t; PINS(yb); }
            if constexpr (i == 22) { t = max(s, 0); PINS(t); }
            if constexpr (i == 26 || i == 28) G.op<i / 28>(k, l16, t);
            if constexpr (i == 32 || i == 34 || i == 38) X.next<(i - 31) / 3>(k, l16);
            if constexpr (i == 5) asm volatile("" :: "v"(o));
            if constexpr (i == YPOS) { asm volatile("s_waitcnt vmcnt(0)\n\ts_barrier" ::: "memory"); G.mx = s; PIN(G.mx); }
            if constexpr (i == YPOS + 4) st32(k.rs, vsig, pbc + 4, G.mx);
            if constexpr (i / 6 >= GPOSY && i / 6 < GPOSY + 3 && (i % 6 == 2 || i % 6 == 4) && i != GPOSY * 6 + 2) G.op<1 + (i / 6 - GPOSY) * 2 + (i % 6) / 4>(k, l16, 0);
            if constexpr (i >= 92 && i < 95) clr2(ac[(i - 92) % 3]); });
        static_assert(GPOSY * 6 + 8 > YPOS + 4 && GPOSY <= 13, "GPOSY");
        STAMP(s, 5)
        PFLUSH(s - 1)
        f32x4 acc[10];
        f32x2 z[20];
        auto clr = [&](auto I) { constexpr int i = CV(I); asm volatile("v_mov_b64 %0, 0" : "=v"(z[i])); if constexpr (i & 1) acc[i >> 1] = __builtin_shufflevector(z[i - 1], z[i], 0, 1, 2, 3); };
        bf16x4 al[3], ah[3];
        unsigned an, d0, d1;
        {
            bf16x8 m[10], wa[10], wb[10];
            auto first = [&](auto I) { constexpr int i = CV(I); smf<true>(ac[i % 3], __builtin_bit_cast(bf16x8, i < 3 ? G.p0 : G.p1), W[(i % 3) * 10 + i / 3], ix); };
            G.wait<0>();
            STOP(s) STAMP(s, 0)
            sfor<6>([&](auto I) { constexpr int i = CV(I);
                first(I);
                SCHED0();
                if constexpr (i == 2) wa[2] = lr128(lwv);
                if constexpr (i == 3) wb[2] = lr128(lwv + 1024);
                G.fin<i>();
                SCHED0(); });
            if (G.bad != 0) {
                unsigned spins = 0;
_Pragma("unroll 1")
                do { PCOUNT(0) G.again(k, l16); } while (G.bad != 0 && !spin_fail(k, spins++));
                STOP(s) STAMP(s, 0)
                clr2(ac[0]); clr2(ac[1]); clr2(ac[2]);
                asm volatile("s_nop 1" : "+v"(ac[0]), "+v"(ac[1]), "+v"(ac[2]));
                sfor<6>(first);
                asm volatile("s_waitcnt lgkmcnt(0)" ::: "memory");
            }
            bar_lgkm<2>();
            an = (unsigned)done - (unsigned)now;
            PINS(an);
            now = rt_async();
            m[2] = lr128(l16 + L_MEM + 2048);
            auto rd = [&](auto R) { constexpr int r = CV(R), b = 3 + r / 3;
                if constexpr (b < 10) {
                    if constexpr (r % 3 == 0) wa[b] = lr128(lwv + (b - 2) * 2048);
                    if constexpr (r % 3 == 1) wb[b] = lr128(lwv + (b - 2) * 2048 + 1024);
                    if constexpr (r % 3 == 2) m[b] = lr128(l16 + L_MEM + b * 1024);
                } else if constexpr (b < 13) {
                    if constexpr (r % 3 == 0) V.r0<b + 7>();
                    if constexpr (r % 3 == 1) V.rw<b + 7>();
                    if constexpr (r % 3 == 2) V.r1<b + 7>();
                } };
            SCHED0();
            V.run<0, true, 16, 17, 0, true>(W, [&](auto I) { constexpr int i = CV(I); if constexpr (i < 3) rd(I); if constexpr (i == 5) { rd(IC(5)); rd(IC(3)); }
                if constexpr (i == 3) { an = min(an, 200u) + DV; PINS(an); } });
            sfor<24>([&](auto I) {
                constexpr int i = CV(I), b = 2 + i / 3, t = (i + 2) % 3;
                if constexpr (t == 2) smf<false>(ac[2], m[b], CAT8(wa[b], wb[b]), ix); else smf<true>(ac[t], m[b], W[t * 10 + b], ix);
                SCHED0();
                if constexpr (i == 0) rd(IC(4));
                if constexpr (i < 21) rd(IC(6 + i));
                if constexpr (i == 22) d0 = vadd(l16, L_K + ((3 * w) >> 1) * 1024 + ((3 * w) & 1) * 8);
                if constexpr (i == 23) d1 = vadd(l16, L_K + ((3 * w + 1) >> 1) * 1024 + ((3 * w + 1) & 1) * 8);
                SCHED0();
            });
            unsigned fo[3];
            V.run<0, true, 17, 19, 0, true>(W, [&](auto I) { constexpr int i = CV(I);
                if constexpr (i < 3) rd(IC(27 + i));
                if constexpr (i >= 1 && i < 10) fold_op<(i - 1) % 3>(ac[((i + 5) / 3) % 3], fo[((i + 5) / 3) % 3]);
                if constexpr (i == 3 || i == 6 || i == 9) lw32(dq0 + ((i / 3 + 1) % 3) * 256, fo[(i / 3 + 1) % 3]);
                if constexpr (i == 4 || i == 5 || i == 7 || i == 8) clr(IC((i / 7) * 2 + i % 3 - 1));
                if constexpr (i >= 10) clr2(acc[i - 8]); });
        }
        lgkm0(now);
        __builtin_amdgcn_s_barrier();
        STAMP(s, 1)
        {
            unsigned fo[2];
            bf16x8 v[12];
            u32x2 pk[10];
            auto fin = [&](auto Q) { constexpr int tp = CV(Q) / 11, j = CV(Q) % 11;
                if constexpr (tp >= 5) { }
                else if constexpr (j < 6) fold_op<j / 2>(acc[2 * tp + (j & 1)], fo[j & 1]);
                else if constexpr (j < 8) asm volatile("v_min_u32 %0, -2, %0" : "+v"(fo[j & 1]));
                else if constexpr (j == 8) { ys = ym + tp * 32768; PINS(ys); }
                else if constexpr (j == 9) asm volatile("v_permlane16_swap_b32 %0, %1" : "+v"(fo[0]), "+v"(fo[1]));
                else st64(k.rs, vo, ys, (u32x2){fo[0], fo[1]}); };
            al[0] = lrtr(trp); ah[0] = lrtr(trp + 64);
            SCHED0();
            V.run<0, true, 19, 20, 0, true>(W, [&](auto I) { constexpr int i = CV(I);
                if constexpr (i == 0) al[1] = lrtr(trp + 1024);
                if constexpr (i == 1) ah[1] = lrtr(trp + 1088);
                if constexpr (i == 2) al[2] = lrtr(trp + 2048);
                if constexpr (i == 3) ah[2] = lrtr(trp + 2112);
                if constexpr (i == 0) { an += (unsigned)now; PINS(an); }
                if constexpr (i == 1) an = spack(an, s);
                if constexpr (i == 2) { fo[0] = an; PIN(fo[0]); }
                if constexpr (i == 3) st32(k.rs, vsig, pbc, fo[0]);
                if constexpr (i >= 4) clr2(acc[i]); });
            sfor<30>([&](auto I) { constexpr int i = CV(I), b = (i % 6) >> 1, t = 2 * (i / 6) + (i & 1), id = OIDX(t, b);
                smf<(id < 32)>(acc[t], CAT4(al[b], ah[b]), W[id], ix);
                SCHED0();
                V.out<i - 1, 0>(d0, d1);
                if constexpr ((i % 3 == 0 && i < 21) || i == 19) clr(IC(12 + (i == 19 ? 7 : i / 3)));
                if constexpr (i == 21) bar_lgkm<0>();
                if constexpr (i >= 21 && i < 27) v[i - 21] = lr128(l16 + L_K + (i - 21) * 1024);
                if constexpr (i >= 20 && i < 29) fin(IC(i - 20));
                if constexpr (i == 28 || i == 29) X.next<i - 25>(k, l16);
                SCHED0(); });
            STAMP(s, 2)
            f32x4 y[20];
            sfor<120>([&](auto I) {
                constexpr int i = CV(I), rt = i / 60, t = (i / 6) % 10, ks = i % 6, u = i / 6, id = OIDX(t, ks >> 1);
                if constexpr (ks & 1) mfb<(id < 32), false>(y[u], v[rt * 6 + ks], HI8(W[id]));
                else mfb<(id < 32), ks == 0>(y[u], v[rt * 6 + ks], LO8(W[id]));
                SCHED0();
                if constexpr (i < 32) { constexpr int q0 = 9 + (3 * i) / 2, q1 = 9 + (3 * i + 3) / 2; sfor<q1 - q0>([&](auto J) { fin(IC(q0 + CV(J))); }); }
                if constexpr (i == 32) done = rt_async();
                if constexpr (i == 31) quit_if(s, 63);
                if constexpr (u > 0 && u <= 10 && (ks == 2 || ks == 4)) { pk[u - 1][ks >> 2] = cvtpk(y[u - 1][ks - 2], y[u - 1][ks - 1]); PIN(pk[u - 1][ks >> 2]); }
                if constexpr (u > 10 && ks == 2) pack(y[u - 1]);
                if constexpr (u > 10 && ks == 3) __builtin_amdgcn_raw_buffer_store_b128(o, k.rs, (int)l16, (int)yb, AUX_SC1);
                if constexpr (u > 10 && ks == 4) { yb += 65536; PINS(yb); asm volatile("" :: "v"(o)); }
                if constexpr (u >= 10 && ks == 5) { u32x2 d; asm volatile("v_mov_b64 %0, %1" : "=v"(d) : "v"(pk[u % 10])); lo = d; }
                if constexpr (i == 33 || i == 35 || i == 36 || i == 37 || i == 39 || i == 41) X.next<(i == 33 ? 5 : i < 38 ? i - 29 : i == 39 ? 9 : 10)>(k, l16);
                if constexpr (i >= 40 && i < 65 && i % 5 != 1 && i % 5 != 3) X.op<(i / 5 - 8) % 5, (i % 5) / 2>(k);
                if constexpr (i == 66) { ym = (s & 2) ? ym0 : ym2; PINS(ym); }
                if constexpr (i == 67) { ys = (s & 2) ? ym2 : ym0; PINS(ys); }
                if constexpr (i == 72) { ys += EXP(327680); PINS(ys); }
                if constexpr (i == 73) { ym = (s & 1) ? ym : ys; PINS(ym); }
                if constexpr (i >= 46 && i < 60 && (i % 5 == 1 || i % 5 == 3)) v[6 + (i - 46) / 5 * 2 + (i % 5) / 3] = lr128(l16 + L_K + (6 + (i - 46) / 5 * 2 + (i % 5) / 3) * 1024);
                if constexpr (i == 60) { STAMP(s, 3) }
                if constexpr (i == 106) {
                    if (X.off != 0) { PCOUNT(1) X.late<5>(k, l16); }
                    asm volatile("s_waitcnt vmcnt(7)\n\ts_barrier" ::: "memory");
                    lgkm0(done);
                }
                if constexpr (i == 108) V.r0<0>();
                if constexpr (i == 109) V.r1<0>();
                if constexpr (i == 114) V.r0<1>();
                if constexpr (i == 115) V.r1<1>();
                SCHED0();
            });
            yl = y[19];
            STAMP(s, 4)
        }
    }
    PFLUSH(63)
    PROF_OUT
}

DEVI void ln_store(const Ctx& k, const float (&v)[40], unsigned dst, int row  ) {
    f32x2 s2[4] = {{0.f, 0.f}, {0.f, 0.f}, {0.f, 0.f}, {0.f, 0.f}};
#pragma unroll
    for (int i = 0; i < 40; i += 2) s2[(i >> 1) & 3] += (f32x2){v[i], v[i + 1]};
    const f32x2 st = (s2[0] + s2[1]) + (s2[2] + s2[3]);
    float m = row16sum(st[0] + st[1]) * (1.f / 640.f);
    f32x2 q2[4] = {{0.f, 0.f}, {0.f, 0.f}, {0.f, 0.f}, {0.f, 0.f}};
#pragma unroll
    for (int i = 0; i < 40; i += 2) { const f32x2 d = (f32x2){v[i], v[i + 1]} - (f32x2){m, m}; q2[(i >> 1) & 3] += d * d; }
    const f32x2 qt = (q2[0] + q2[1]) + (q2[2] + q2[3]);
    const float q = row16sum(qt[0] + qt[1]);
    const float var = bfr(q * (1.f / 640.f));
    m = bfr(m);
    const float rden = __builtin_amdgcn_rcpf(bfr(__builtin_amdgcn_sqrtf(bfr(var + 1e-5f))));
    const int n = k.l & 15;
    const unsigned vo = (n >> 2) * 2048 + (row >> 4) * 1024 + ((n & 3) * 16 + (row & 15)) * 16;
#pragma unroll
    for (int c = 0; c < 5; ++c) {
        u32x4 o;
#pragma unroll
        for (int i = 0; i < 4; ++i) { unsigned d = cvtpk(v[c * 8 + 2 * i] - m, v[c * 8 + 2 * i + 1] - m); o[i] = cvtpk(blo(d) * rden, bhi(d) * rden); }
        st128(k.rs, vo, dst + c * 16384, o);
    }
}
DEVI void unpack8(float* v, u32x4 a) {
#pragma unroll
    for (int i = 0; i < 4; ++i) { v[2 * i] = blo(a[i]); v[2 * i + 1] = bhi(a[i]); }
}
DEVI void ln_x_batch(const Params& p, const Ctx& k, int bw) {
    const int row = bw * 4 + (k.l >> 4), sg = bw >> 3;
    float v[40];
#pragma unroll
    for (int c = 0; c < 5; ++c) unpack8(v + c * 8, *(const u32x4*)(p.x + (size_t)row * 640 + (c * 16 + (k.l & 15)) * 8));
    ln_store(k, v, k.seg0(PG_XNA, sg), row & 31);
    DRAIN();
    if (k.l == 0) cnt_add(k.cnt0(C_XN, sg));
}

DEVI void row_role(const Params& p, const Ctx& k, char* smem, int rt) {
    PROF_DECL
    const int l = k.l, n = l & 15;
    const unsigned fo = k.w * 2048 + rt * 1024;
    RSTAMP(2)
    for (int sg = k.c; sg < 64; sg += 2) {
        prog_wait<5>(k, k.pg0(0) + CNT(C_X2, rt * 5), sg);
        STOP(sg) STAMP(sg, 0)
        const unsigned src = k.seg0(PG_X2, sg) + fo, dst = (k.seg0(PG_XNA, sg) ^ 8192u) + fo;
        u32x4 a[5];
_Pragma("unroll 1")
        for (unsigned spins = 0;; ++spins) {
            unsigned mx = 0;
#pragma unroll
            for (int c = 0; c < 5; ++c) a[c] = ld128(k.rs, l * 16, src + c * 16384);
#pragma unroll
            for (int c = 0; c < 5; ++c) mx = max(mx, umax4(a[c]));
            if (__all(mx != 0xFFFFFFFFu) || spin_fail(k, spins)) break;
            PCOUNT(0)
            asm volatile("" ::: "memory");
        }
        STAMP(sg, 1)
        f32x2 v[20], s2[4], q2[4];
#pragma unroll
        for (int i = 0; i < 20; ++i) { v[i] = (f32x2){blo(a[i >> 2][i & 3]), bhi(a[i >> 2][i & 3])}; s2[i & 3] = i < 4 ? v[i] : s2[i & 3] + v[i]; }
        const f32x2 st = (s2[0] + s2[1]) + (s2[2] + s2[3]);
        const float sm = xor32add(xor16add(st[0] + st[1])), mu = sm * (1.f / 160.f);
#pragma unroll
        for (int i = 0; i < 20; ++i) { const f32x2 d = v[i] - mu; q2[i & 3] = i < 4 ? d * d : __builtin_elementwise_fma(d, d, q2[i & 3]); }
        const f32x2 qt = (q2[0] + q2[1]) + (q2[2] + q2[3]);
        *(f32x2*)(smem + n * 32 + k.w * 8) = (f32x2){sm, xor32add(xor16add(qt[0] + qt[1]))};
        RAWBAR()
        const f32x4 e0 = *(const f32x4*)(smem + n * 32), e1 = *(const f32x4*)(smem + n * 32 + 16);
        float m = ((e0[0] + e0[2]) + (e1[0] + e1[2])) * (1.f / 640.f);
        const f32x4 d = (f32x4){e0[0], e0[2], e1[0], e1[2]} - 160.f * m;
        const float var = bfr((((e0[1] + e0[3]) + (e1[1] + e1[3])) + ((d[0] * d[0] + d[1] * d[1]) + (d[2] * d[2] + d[3] * d[3])) * (1.f / 160.f)) * (1.f / 640.f));
        m = bfr(m);
        const float rden = __builtin_amdgcn_rcpf(bfr(__builtin_amdgcn_sqrtf(bfr(var + 1e-5f))));
#pragma unroll
        for (int c = 0; c < 5; ++c) {
            u32x4 o;
#pragma unroll
            for (int i = 0; i < 4; ++i) { const f32x2 x = v[c * 4 + i] - m; o[i] = cvtpk(bfr(x[0]) * rden, bfr(x[1]) * rden); }
            st128(k.rs, l * 16, dst + c * 16384, o);
        }
        STAMP(sg, 2)
        DRAIN();
        __builtin_amdgcn_s_barrier();
        st32(k.rs, (k.w == 0 && l == 0) ? 0u : OOB, (k.pg0(0) ^ 8192u) + CNT(C_XN, k.c * 2 + rt), (unsigned)sg);
        STAMP(sg, 3) STAMP(sg, 4) STAMP(sg, 5)
        PFLUSH(sg) PFLUSH(sg ^ 1)
    }
    PROF_OUT
}

struct Red {
    u32x4 Y[16];
    DEVI void yload(const Ctx& k, int j, int sg) {
        const unsigned a = k.yx(sg, j / 5) + (j % 5) * 131072 + k.w * 16384;
#pragma unroll
        for (int i = 0; i < 16; ++i) Y[i] = ld128(k.rs, k.l * 16 + (i & 3) * 1024, a + ((i >> 2) & 1) * 4096 + (i >> 3) * 65536);
    }
};
DEVI unsigned y_get(const Ctx& k) { return ld32(k.rs, (k.l & 7) * 128, k.p0 + CNT(C_BC, k.w * 16 + 8) + 4); }
DEVI void y_wait(const Ctx& k, int s) {
_Pragma("unroll 1")
    for (unsigned spins = 0; !__all((int)y_get(k) >= s); ++spins) {
        asm volatile("" ::: "memory");
        __builtin_amdgcn_s_sleep(2);
        if (spin_fail(k, spins)) break;
    }
    __builtin_amdgcn_fence(__ATOMIC_ACQUIRE, "wavefront");
}
DEVI void wait_until(unsigned t) { while ((int)((rclk() - t) << 16) < 0) { if (WUS) __builtin_amdgcn_s_sleep(WUS); } }
#define DPPMAX(x, c) x = max(x, __builtin_amdgcn_update_dpp(0, x, c, 0xf, 0xf, false))
#define WAIT_BC(s) { \
    unsigned bc; \
    _Pragma("unroll 1") for (;; ++spins) { \
        bc = ld32(k.rs, (l & 15) * 128, k.p0 + CNT(C_BC, w * 16)); \
        DRAIN(); \
        if (__all(((int)bc >> 16) >= s)) break; \
        __builtin_amdgcn_s_sleep(BSLP); \
        if (spin_fail(k, spins)) break; \
    } \
    const unsigned now = rclk(); \
    int d = ((int)bc >> 16) > s ? 0 : (int)((bc - now) << 16); \
    DPPMAX(d, 0xB1); DPPMAX(d, 0x4E); DPPMAX(d, 0x141); \
    tq = now + (__builtin_amdgcn_readlane(d, 0) >> 16); tv = now + (__builtin_amdgcn_readlane(d, 8) >> 16); }
DEVI void mred_role(const Params& p, const Ctx& k, char* smem, int j) {
    PROF_DECL
    const int l = k.l, w = k.w, g = l >> 4, n = l & 15;
    const unsigned ix = SPIX(l);
    const unsigned vp = (g * 16 + ((n >> 1) & 1) * 8 + (n >> 2) * 2 + (n & 1)) * 16;
    const unsigned v16 = (g >> 1) * 1024 + (g & 1) * 256 + n * 16, yo = j * 32768 + EXP(w * 4096);
    const u32x4 ones = {0xFFFFFFFFu, 0xFFFFFFFFu, 0xFFFFFFFFu, 0xFFFFFFFFu};
    auto arm = [&](int s) {
#pragma unroll
        for (int i = 0; i < 4; ++i) st128(k.rs, l * 16 + i * 1024, k.ym(s) + yo, ones);
    };
    Red R;
    RSTAMP(2)
    arm(1);
    y_wait(k, 0);
    R.yload(k, j, 0);
    for (int s = 0; s < 63; ++s) {
        STOP(s) STAMP(s, 0)
        const unsigned ap = k.p0 + PR_B + (s & 3) * PR_SZ + w * 49152, am = k.ym(s) + yo;
        const unsigned ph = (s & 4) ? 0x80008000u : 0u;
        unsigned spins = 0, tq, tv;
        unsigned fl;
        WAIT_BC(s)
        STAMP(s, 5)
        char* rb = smem + (s & 1) * 4096;
_Pragma("unroll 1")
        for (spins = 0;; ++spins) {
            u32x4 ym[4], pm[5];
            f32x4 acc[2];
            if (spins == 0) wait_until(tv);
            asm volatile("" ::: "memory");
#pragma unroll
            for (int i = 0; i < 4; ++i) ym[i] = ld128(k.rs, v16 + (i >> 1) * 2048 + (i & 1) * 512, am);
            fl = ld32(k.rs, 0, k.p0 + CNT(C_X1, j));
            asm volatile("" ::: "memory");
            if (spins == 0) { wait_until(tq); STAMP(s, 1) }
            asm volatile("" ::: "memory");
#pragma unroll
            for (int i = 0; i < 5; ++i) pm[i] = ld128(k.rs, i < 4 ? l * 16 + i * 1024 : vp, ap + (i >> 2) * 4096);
            acc[0] = (f32x4){0.f, 0.f, 0.f, 0.f}; acc[1] = acc[0];
            const u32x8 y0 = {ym[0][0], ym[1][0], ym[0][1], ym[1][1], ym[2][0], ym[3][0], ym[2][1], ym[3][1]};
            const u32x8 y1 = {ym[0][2], ym[1][2], ym[0][3], ym[1][3], ym[2][2], ym[3][2], ym[2][3], ym[3][3]};
            unsigned mx = 0;
#pragma unroll
            for (int i = 0; i < 4; ++i) mx = max(mx, umax4(ym[i]));
            unsigned tg = mx == 0xFFFFFFFFu ? 0x80000000u : 0u;
            asm volatile("s_nop 1" : "+v"(acc[0]), "+v"(acc[1]), "+v"(tg) : "v"(y0), "v"(y1), "v"(ix));
            SCHED0();
#pragma unroll
            for (int i = 0; i < 5; ++i) {
                const unsigned u0 = __builtin_amdgcn_bitop3_b32(pm[i][0], pm[i][1], ph, 0x7e), u1 = __builtin_amdgcn_bitop3_b32(pm[i][2], pm[i][3], ph, 0x7e);
                SCHED0();
                if (i < 4) smf<false>(acc[0], __builtin_bit_cast(bf16x8, pm[i]), __builtin_bit_cast(bf16x16, __builtin_shufflevector(R.Y[2 * i], R.Y[2 * i + 1], 0, 1, 2, 3, 4, 5, 6, 7)), ix);
                else smf<false>(acc[0], __builtin_bit_cast(bf16x8, pm[4]), __builtin_bit_cast(bf16x16, y0), ix);
                tg = (tg | u0) | u1;
                SCHED0();
                if (i < 4) smf<false>(acc[1], __builtin_bit_cast(bf16x8, pm[i]), __builtin_bit_cast(bf16x16, __builtin_shufflevector(R.Y[8 + 2 * i], R.Y[9 + 2 * i], 0, 1, 2, 3, 4, 5, 6, 7)), ix);
                else smf<false>(acc[1], __builtin_bit_cast(bf16x8, pm[4]), __builtin_bit_cast(bf16x16, y1), ix);
                SCHED0();
            }
            const unsigned long long bad = __ballot((int)tg < 0);
            asm volatile("s_nop 5" : "+v"(acc[0]), "+v"(acc[1]) : "s"(bad));
#pragma unroll
            for (int t = 0; t < 2; ++t) { float* rp = (float*)(rb + t * 2048 + w * 512 + g * 128 + n * 4); rp[0] = acc[t][0] + acc[t][2]; rp[16] = acc[t][1] + acc[t][3]; }
            if (bad == 0) break;
            PCOUNT(0)
            asm volatile("" ::: "memory");
            if (spin_fail(k, spins)) break;
        }
        STAMP(s, 2)
        RAWBAR()
        if (w < 2) {
            const char* rp = rb + w * 2048 + ((l >> 4) * 2 + ((l >> 2) & 1)) * 64 + ((l & 3) * 4 + ((l >> 3) & 1) * 2) * 4;
            const f32x2 a = *(const f32x2*)rp, b = *(const f32x2*)(rp + 512), c = *(const f32x2*)(rp + 1024), d = *(const f32x2*)(rp + 1536);
            const f32x2 r = (a + b) + (c + d);
            st32(k.rs, l * 4, k.memf(s + 1) + EXP((2 * j + w) * 256), min(cvtpk(r[0], r[1]) ^ ph, 0xFFFFFFFEu));
        }
        STAMP(s, 3)
_Pragma("unroll 1")
        for (spins = 0; (int)__builtin_amdgcn_readfirstlane(fl) < s - 2; ++spins) {
            PCOUNT(1)
            __builtin_amdgcn_s_sleep(PSLEEP);
            if (spin_fail(k, spins)) break;
            fl = ld32(k.rs, 0, k.p0 + CNT(C_X1, j));
        }
        __builtin_amdgcn_fence(__ATOMIC_ACQUIRE, "wavefront");
        arm(s + 2);
        y_wait(k, s + 1);
        R.yload(k, j, s + 1);
        STAMP(s, 4)
        PFLUSH(s)
    }
    PFLUSH(63)
    PROF_OUT
}
DEVI void tred_role(const Params& p, const Ctx& k, char* smem, int j) {
    PROF_DECL
    const int l = k.l, w = k.w, g = l >> 4, n = l & 15, T = 2 * j + (w >> 1), rt = w & 1;
    const unsigned v16 = (g >> 1) * 1024 + (g & 1) * 256 + n * 16, yo = j * 32768 + EXP(w * 4096);
    const unsigned v8 = (g >> 1) * 512 + (g & 1) * 128 + n * 8;
    const unsigned xo = EXP(j * 2048) + (T & 1) * 512 + rt * 1024, xv = ((g >> 1) * 16 + n) * 16 + (g & 1) * 8;
    const u32x2 ones = {0xFFFFFFFFu, 0xFFFFFFFFu};
    Red R;
    RSTAMP(2)
    st64(k.rs, xv, k.seg(PG_X1, 0) + xo, ones); st64(k.rs, xv, k.seg(PG_X1, 1) + xo, ones);
    y_wait(k, 0);
    R.yload(k, j, 0);
    for (int s = 0; s < 64; ++s) {
        STOP(s) STAMP(s, 0)
        const unsigned ap1 = k.p0 + PR_B + (s & 3) * PR_SZ + w * 49152 + 16384, ap2 = ap1 + 16384, am = k.ym(s) + yo;
        const unsigned ph = (s & 4) ? 0x80008000u : 0u;
        unsigned spins = 0, tq, tv;
        u32x2 res;
        if (k.c) res = ld64(k.rs, xv, k.seg0(PG_X2, s) + xo);
        else res = *(const u32x2*)(p.x + (size_t)(s * 32 + rt * 16 + n) * 640 + T * 16 + g * 4);
        f32x4 acc[2][2];
        WAIT_BC(s)
        wait_until(tv); wait_until(tq + RDLY);
        asm volatile("" ::: "memory");
        STAMP(s, 1)
_Pragma("unroll 1")
        for (;; ++spins) {
            u32x4 ym[4], p1[8]; u32x2 p0[8], pq[8];
#pragma unroll
            for (int i = 0; i < 4; ++i) ym[i] = ld128(k.rs, v16 + (i >> 1) * 2048 + (i & 1) * 512, am);
#pragma unroll
            for (int i = 0; i < 8; ++i) pq[i] = ld64(k.rs, v8 + ((i >> 1) & 1) * 1024 + (i & 1) * 256 + (i >> 2) * 2048, ap2 + 4096);
#pragma unroll
            for (int i = 0; i < 8; ++i) { p0[i] = ld64(k.rs, l * 8 + i * 512, ap2); p1[i] = ld128(k.rs, l * 16 + (i & 3) * 1024, ap1 + (i >> 2) * 4096); }
            unsigned mx = 0, tg = 0;
#pragma unroll
            for (int t = 0; t < 2; ++t) {
                acc[t][0] = (f32x4){0.f, 0.f, 0.f, 0.f}; acc[t][1] = acc[t][0];
#pragma unroll
                for (int f = 0; f < 2; ++f) {
                    const u32x4 yf = {ym[2 * f][2 * t], ym[2 * f][2 * t + 1], ym[2 * f + 1][2 * t], ym[2 * f + 1][2 * t + 1]};
#pragma unroll
                    for (int r = 0; r < 2; ++r)
                        acc[t][r] = mf(__builtin_bit_cast(bf16x8, yf), __builtin_bit_cast(bf16x8, (u32x4){pq[4 * r + 2 * f][0], pq[4 * r + 2 * f][1], pq[4 * r + 2 * f + 1][0], pq[4 * r + 2 * f + 1][1]}), acc[t][r]);
                }
            }
#pragma unroll
            for (int i = 0; i < 8; ++i)
#pragma unroll
                for (int t = 0; t < 2; ++t) {
                    acc[t][0] = mf(__builtin_bit_cast(bf16x8, R.Y[t * 8 + i]), __builtin_bit_cast(bf16x8, (u32x4){p0[i][0], p0[i][1], 0u, 0u}), acc[t][0]);
                    acc[t][1] = mf(__builtin_bit_cast(bf16x8, R.Y[t * 8 + i]), __builtin_bit_cast(bf16x8, p1[i]), acc[t][1]);
                }
#pragma unroll
            for (int i = 0; i < 4; ++i) mx = max(mx, umax4(ym[i]));
#pragma unroll
            for (int i = 0; i < 8; ++i) tg |= (((p1[i][0] ^ ph) | (p1[i][1] ^ ph)) | ((p1[i][2] ^ ph) | (p1[i][3] ^ ph))) | (((p0[i][0] ^ ph) | (p0[i][1] ^ ph)) | ((pq[i][0] ^ ph) | (pq[i][1] ^ ph)));
            if (__all(mx != 0xFFFFFFFFu && (int)tg >= 0)) break;
            PCOUNT(0)
            asm volatile("" ::: "memory");
            if (spin_fail(k, spins)) break;
        }
        STAMP(s, 2)
        const unsigned yc = y_get(k);
        char* rb = smem + (s & 1) * 16384;
#pragma unroll
        for (int i = 0; i < 4; ++i) *(f32x4*)(rb + i * 4096 + w * 1024 + l * 16) = acc[i >> 1][i & 1];
        RAWBAR()
        st32(k.rs, (w == 0 && l == 0) ? 0u : OOB, k.p0 + CNT(C_X1, j), (unsigned)s);
        {
            const char* rp = rb + w * 4096 + l * 16;
            const f32x4 r = (*(const f32x4*)rp + *(const f32x4*)(rp + 1024)) + (*(const f32x4*)(rp + 2048) + *(const f32x4*)(rp + 3072));
            const unsigned d0 = cvtpk(r[0], r[1]) ^ ph, d1 = cvtpk(r[2], r[3]) ^ ph;
            u32x2 o; o[0] = packs(blo(res[0]) + blo(d0), bhi(res[0]) + bhi(d0)); o[1] = packs(blo(res[1]) + blo(d1), bhi(res[1]) + bhi(d1));
            st64(k.rs, xv, k.seg(PG_X1, s) + xo, o);
            st64(k.rs, s < 62 ? xv : OOB, k.seg(PG_X1, (s + 2) & 63) + xo, ones);
        }
        STAMP(s, 3)
        if (s < 63) {
            if (!__all((int)yc >= s + 1)) { PCOUNT(2) y_wait(k, s + 1); }
            __builtin_amdgcn_fence(__ATOMIC_ACQUIRE, "wavefront");
            R.yload(k, j, s + 1);
        }
        STAMP(s, 4)
        STAMP(s, 5)
        PFLUSH(s)
    }
    PROF_OUT
}

DEVI void up_role(const Params& p, const Ctx& k, char* smem, int id) {
    PROF_DECL
    const int l = k.l, w = k.w, g = l >> 4, n = l & 15;
    bf16x8 Wt[80];
    const int tile0 = id * 8 + w * 2;
    wstream<20, 4, 8>(smem + w * 32768,
        [&](auto U, unsigned sl) { constexpr int u = CV(U);
            wp_issue<2, (u % 8) * 4096>(p.wg + (size_t)u * 32 * 1536 + tile0 * 16, 1536, sl, l); wp_issue<2, (u % 8) * 4096 + 2048>(p.wu + (size_t)u * 32 * 1536 + tile0 * 16, 1536, sl, l); },
        [&](auto U, char* sp) { constexpr int u = CV(U);
            Wt[u] = wp_read<2, true>(sp, 0, l); Wt[20 + u] = wp_read<2, true>(sp, 1, l); Wt[40 + u] = wp_read<2, true>(sp + 2048, 0, l); Wt[60 + u] = wp_read<2, true>(sp + 2048, 1, l); });
    RAWBAR()
    RSTAMP(2)
    bf16x8 one8 = {0x3F80, 0x3F80, 0x3F80, 0x3F80, 0x3F80, 0x3F80, 0x3F80, 0x3F80};
    asm volatile("s_nop 1" : "+v"(one8));
    f32x4 cs[4];
    sfor<80>([&](auto I) { constexpr int f = CV(I); mfa<(f < 64), f % 20 == 0>(cs[f / 20], Wt[f], one8); });
    asm volatile("s_nop 7" : "+v"(cs[0]), "+v"(cs[1]), "+v"(cs[2]), "+v"(cs[3]));
    const unsigned ho = EXP((tile0 >> 1) * 1024), l16 = l * 16;
    constexpr int L_ST = 81920;
    const u32x4 ones = {0xFFFFFFFFu, 0xFFFFFFFFu, 0xFFFFFFFFu, 0xFFFFFFFFu};
#pragma unroll
    for (int i = 0; i < 4; ++i) st128(k.rs, l16, k.hseg(i >> 1) + (i & 1) * EXP(49152) + ho, ones);
    unsigned fails = 0;
    const unsigned fl = (w == 0 && l == 0) ? 0u : OOB;
    const unsigned long long m1 = __ballot(n & 1), m2 = __ballot(n & 2);
    for (int s = 0; s < 64; ) {
        prog_wait<20>(k, k.p0 + CNT(C_X1, 0), s);
        STOP(s) STAMP(s, 0)
        const unsigned xb = (s & 1) * 40960 + l16, yb = xb + w * 2048, sp = L_ST + (s & 1) * 2048, rp = sp + n * 16;
        const unsigned dp = sp + (g == (n >> 2) ? n * 16 + w * 4 : 4096 + l * 4);
        DRAIN();
        xn_all(k, l16, k.seg(PG_X1, s) + w * 2048, (s & 1) * 40960 + w * 2048);
        f32x4 acc[2][4], st[4];
        bf16x8 x[20], y0[5], y1[5];
        unsigned mx = 0;
        auto blk = [&](auto B, auto J) { constexpr int b = CV(B), j = CV(J);
            if constexpr (j == 0) asm volatile("s_waitcnt vmcnt(%0)\n\ts_barrier" :: "n"(8 - 2 * b) : "memory");
            if constexpr (j >= 1 && j < 5) x[4 * b + j - 1] = lr128(xb + (4 * b + j - 1) * 2048);
            if constexpr (j == 5) y0[b] = lr128(yb + b * 8192);
            if constexpr (j == 6) y1[b] = lr128(yb + b * 8192 + 1024); };
        sfor<7>([&](auto J) { blk(IC(0), J); });
        SCHED0();
        STAMP(s, 1)
        sfor<5>([&](auto B) { constexpr int b = CV(B);
            sfor<20>([&](auto I) { constexpr int i = CV(I), ks = 4 * b + ((i >> 2) & 3), nt = i & 3, f = nt * 20 + ks;
                if constexpr (i < 16) mfa<(f < 64), ks == 0>(acc[0][nt], Wt[f], x[ks]);
                else mfa<false, b == 0>(st[nt], (nt & 1) ? (nt & 2 ? y1[b] : y0[b]) : one8, nt & 2 ? y1[b] : y0[b]);
                SCHED0();
                if constexpr (i >= 8 && i < 15 && b < 4) blk(IC((b + 1) % 5), IC(i - 8));
                if constexpr (i >= 16) { const u32x4 v = __builtin_bit_cast(u32x4, (i & 1) ? y1[b] : y0[b]); mx = umax3(mx, v[i & 2], v[(i & 2) + 1]); PIN(mx); }
                if constexpr (b == 4 && i >= 9 && i < 12) x[i - 9] = lr128(xb + 1024 + (i - 9) * 2048);
                SCHED0(); }); });
        STAMP(s, 2)
        float mean[2], rden[2], G[2], U[2];
        f32x4 sa[2], sb[2];
        u32x4 bad, o[2];
        bool redo;
        auto stat = [&](auto I) { constexpr int i = CV(I), rt = i / 8, j = i % 8;
            if constexpr (j == 0) asm volatile("v_add_f32 %0, %2, %3\n\tv_add_f32 %1, %4, %5" : "=&v"(mean[rt]), "=&v"(rden[rt]) : "v"(sa[rt][0]), "v"(sa[rt][1]), "v"(sb[rt][0]), "v"(sb[rt][1]));
            if constexpr (j == 1 || j == 2) asm volatile("v_add_f32 %0, %0, %2\n\tv_add_f32 %1, %1, %3" : "+v"(mean[rt]), "+v"(rden[rt]) : "v"(sa[rt][j + 1]), "v"(sb[rt][j + 1]));
            if constexpr (j == 3) asm volatile("v_mul_f32 %0, 0xbaccccCD, %0\n\tv_mul_f32 %1, 0x3acccccd, %1" : "+v"(mean[rt]), "+v"(rden[rt]));
            if constexpr (j == 4) asm volatile("v_fma_f32 %1, -%0, %0, %1\n\tv_cvt_pk_bf16_f32 %0, 0, %0\n\tv_max_f32 %1, 0, %1" : "+v"(mean[rt]), "+v"(rden[rt]));
            if constexpr (j == 5) asm volatile("v_cvt_pk_bf16_f32 %0, 0, %0\n\tv_add_f32 %0, 0x3727c5ac, %0\n\tv_cvt_pk_bf16_f32 %0, 0, %0" : "+v"(rden[rt]));
            if constexpr (j == 6) asm volatile("v_sqrt_f32 %0, %0" : "+v"(rden[rt]));
            if constexpr (j == 7) asm volatile("v_cvt_pk_bf16_f32 %0, 0, %0\n\tv_rcp_f32 %0, %0" : "+v"(rden[rt])); };
#define GU "+v"(G[0]), "+v"(G[1]), "+v"(U[0]), "+v"(U[1])
        auto ep = [&](auto I) { constexpr int i = CV(I), q = i / 15, j = i % 15, rt = q >> 2, t = (q >> 1) & 1, c = (q & 1) * 2;
            if constexpr (j == 0) asm volatile("v_fma_f32 %0, %2, %3, %4\n\tv_fma_f32 %1, %2, %5, %6" : "=&v"(G[0]), "=&v"(G[1]) : "v"(mean[rt]), "v"(cs[t][c]), "v"(acc[rt][t][c]), "v"(cs[t][c + 1]), "v"(acc[rt][t][c + 1]));
            if constexpr (j == 1) asm volatile("v_mul_f32 %0, %0, %2\n\tv_mul_f32 %1, %1, %2" : "+v"(G[0]), "+v"(G[1]) : "v"(rden[rt]));
            if constexpr (j == 2) asm volatile("v_cvt_pk_bf16_f32 %0, 0, %0\n\tv_cvt_pk_bf16_f32 %1, 0, %1" : "+v"(G[0]), "+v"(G[1]));
            if constexpr (j == 3) asm volatile("v_mul_f32 %0, 0xbfb8aa3b, %0\n\tv_mul_f32 %1, 0xbfb8aa3b, %1" : "+v"(G[0]), "+v"(G[1]));
            if constexpr (j == 4 || j == 5) asm volatile("v_exp_f32 %0, %0" : "+v"(G[j & 1]));
            if constexpr (j == 6) asm volatile("v_fma_f32 %0, %2, %3, %4\n\tv_fma_f32 %1, %2, %5, %6" : "=&v"(U[0]), "=&v"(U[1]) : "v"(mean[rt]), "v"(cs[2 + t][c]), "v"(acc[rt][2 + t][c]), "v"(cs[2 + t][c + 1]), "v"(acc[rt][2 + t][c + 1]));
            if constexpr (j == 7) asm volatile("v_add_f32 %0, 1.0, %0\n\tv_add_f32 %1, 1.0, %1" : "+v"(G[0]), "+v"(G[1]));
            if constexpr (j == 8 || j == 9) asm volatile("v_rcp_f32 %0, %0" : "+v"(G[j & 1]));
            if constexpr (j == 10) asm volatile("v_mul_f32 %0, %0, %2\n\tv_mul_f32 %1, %1, %2" : "+v"(U[0]), "+v"(U[1]) : "v"(rden[rt]));
            if constexpr (j == 11) asm volatile("v_cvt_pk_bf16_f32 %0, 0, %0\n\tv_cvt_pk_bf16_f32 %1, 0, %1" : "+v"(U[0]), "+v"(U[1]));
            if constexpr (j == 12) asm volatile("v_cvt_pk_bf16_f32 %0, 0, %0\n\tv_cvt_pk_bf16_f32 %1, 0, %1" : "+v"(G[0]), "+v"(G[1]));
            if constexpr (j == 13) asm volatile("v_mul_f32 %0, %0, %2\n\tv_mul_f32 %1, %1, %3" : "+v"(G[0]), "+v"(G[1]) : "v"(U[0]), "v"(U[1]));
            if constexpr (j == 14) { unsigned d; asm volatile("v_cvt_pk_bf16_f32 %0, %1, %2\n\tv_min_u32 %0, -2, %0" : "=&v"(d) : "v"(G[0]), "v"(G[1])); o[rt][q & 3] = d; } };
        sfor<80>([&](auto I) {
            constexpr int i = CV(I), ks = i >> 2, nt = i & 3, f = nt * 20 + ks;
            mfa<(f < 64), ks == 0>(acc[1][nt], Wt[f], x[ks]);
            SCHED0();
            if constexpr (nt == 0 && ks + 3 < 20) x[(ks + 3) % 20] = lr128(xb + 1024 + ((ks + 3) % 20) * 2048);
            if constexpr (i == 1 || i == 3) {
                constexpr int rt2 = i == 3;
                asm volatile("" : "+v"(st[2 * rt2]), "+v"(st[2 * rt2 + 1]));
                const f32x4& q = st[2 * rt2 + 1];
                const float d = csel(csel(q[0], q[1], m1), csel(q[2], q[3], m1), m2);
                lw32(dp + rt2 * 512, __float_as_uint(st[2 * rt2][0])); lw32(dp + rt2 * 512 + 256, __float_as_uint(d));
            }
            if constexpr (i == 2) lw32(sp + 1024 + w * 4, __any(mx == 0xFFFFFFFFu) ? 1u : 0u);
            if constexpr (i == 6) bar_lgkm<1>();
            if constexpr (i == 6) sa[0] = __builtin_bit_cast(f32x4, lr128(rp));
            if constexpr (i == 7) sb[0] = __builtin_bit_cast(f32x4, lr128(rp + 256));
            if constexpr (i == 9) sa[1] = __builtin_bit_cast(f32x4, lr128(rp + 512));
            if constexpr (i == 10) sb[1] = __builtin_bit_cast(f32x4, lr128(rp + 768));
            if constexpr (i == 11) bad = __builtin_bit_cast(u32x4, lr128(sp + 1024));
            if constexpr (i >= 12 && i < 20) stat(IC(i - 12));
            if constexpr (i >= 20) ep(IC(i - 20));
            if constexpr (i >= 21 && i < 53 && nt == 1) stat(IC(8 + (i - 21) / 4));
            if constexpr (i == 54) { redo = __builtin_amdgcn_readfirstlane((bad[0] | bad[1]) | (bad[2] | bad[3])) != 0 && fails++ < 4096u; }
            if constexpr (i == UPOS) st32(k.rs, fl, k.p0 + CNT(C_H, id), (unsigned)(redo ? s - 1 : s));
            SCHED0();
        });
        STAMP(s, 3)
        asm volatile("s_nop 7" : "+v"(acc[1][0]), "+v"(acc[1][1]), "+v"(acc[1][2]), "+v"(acc[1][3]));
#pragma unroll
        for (int q = 0; q < 4; ++q) {
            const int t = q >> 1, c = (q & 1) * 2;
            float hv[2];
#pragma unroll
            for (int i = 0; i < 2; ++i) {
                const float gg = bfr(__builtin_fmaf(mean[1], cs[t][c + i], acc[1][t][c + i]) * rden[1]), u = bfr(__builtin_fmaf(mean[1], cs[2 + t][c + i], acc[1][2 + t][c + i]) * rden[1]);
                hv[i] = bfr(__builtin_amdgcn_rcpf(1.f + __builtin_amdgcn_exp2f(gg * -1.44269504f))) * u;
            }
            o[1][q] = packs(hv[0], hv[1]);
        }
        if (redo) { PCOUNT(0) RAWBAR() continue; }
        st128(k.rs, l16, k.hseg(s) + ho, o[0]); st128(k.rs, l16, k.hseg(s) + EXP(49152) + ho, o[1]);
        st128(k.rs, s < 62 ? l16 : OOB, k.hseg((s + 2) & 63) + ho, ones); st128(k.rs, s < 62 ? l16 : OOB, k.hseg((s + 2) & 63) + EXP(49152) + ho, ones);
        STAMP(s, 4) STAMP(s, 5)
        PFLUSH(s)
        ++s;
    }
    PROF_OUT
}

DEVI void down_role(const Params& p, const Ctx& k, char* smem, int id) {
    PROF_DECL
    const int l = k.l, w = k.w, g = l >> 4, n = l & 15, rt = id / 5, cg = id % 5;
    bf16x8 Wt[96];
    wstream<12, 8, 4>(smem + w * 32768,
        [&](auto U, unsigned sl) { constexpr int u = decltype(U)::value; wp_issue<8, (u % 4) * 8192>(p.wd + (size_t)(w * 12 + u) * 32 * 640 + cg * 128, 640, sl, l); },
        [&](auto U, char* sp) { constexpr int u = decltype(U)::value;
#pragma unroll
            for (int nt = 0; nt < 8; ++nt) Wt[u * 8 + nt] = wp_read<8, false>(sp, (nt + 2 * w) & 7, l); });
    RAWBAR()
    RSTAMP(2)
    const unsigned xo = EXP((cg * 4 + w) * 2048) + rt * 1024, xv = ((g >> 1) * 16 + n) * 16 + (g & 1) * 8;
    const u32x2 ones = {0xFFFFFFFFu, 0xFFFFFFFFu};
    const unsigned x2v = k.c ? OOB : xv;
#pragma unroll
    for (int i = 0; i < 4; ++i) st64(k.rs, x2v, k.seg(PG_X2, i >> 1) + xo + (i & 1) * 512, ones);
    for (int s = 0; s < 64; ++s) {
        prog_wait<12>(k, k.p0 + CNT(C_H, 0), s);
        STOP(s) STAMP(s, 0)
        f32x4 acc[8];
        u32x2 res[2];
        const unsigned rb = (s & 1) * 32768 + l * 16;
_Pragma("unroll 1")
        for (unsigned spins = 0;; ++spins) {
            u32x4 hx[12];
#pragma unroll
            for (int q = 0; q < 3; ++q) {
                const unsigned a = k.hseg(s) + rt * EXP(49152) + EXP((w * 12 + q * 4) * 1024);
#pragma unroll
                for (int i = 0; i < 4; ++i) hx[q * 4 + i] = ld128(k.rs, l * 16 + i * 1024, a);
            }
#pragma unroll
            for (int t = 0; t < 2; ++t) res[t] = ld64(k.rs, xv + t * 512, k.seg(PG_X1, s) + xo);
            unsigned mx = 0;
            sfor<96>([&](auto I) { constexpr int i = CV(I), ks = i < 72 ? i / 6 : (i - 72) / 2, j = i < 72 ? 2 + i % 6 : i & 1, f = ks * 8 + j;
                mfa<(f < 64), ks == 0>(acc[j], Wt[f], __builtin_bit_cast(bf16x8, hx[ks]));
                SCHED0();
                if constexpr (i < 72 && i % 6 < 2) { mx = umax3(mx, hx[ks][(i & 1) * 2], hx[ks][(i & 1) * 2 + 1]); PIN(mx); }
                if constexpr (i >= 75 && i < 93 && i % 3 == 0) { constexpr int jj = 2 + (i - 75) / 3;
                    asm volatile("" : "+v"(acc[jj])); lw128(rb + ((w + jj / 2) & 3) * 6144 + ((jj / 2 - 1) * 2 + (jj & 1)) * 1024, __builtin_bit_cast(u32x4, acc[jj])); }
                SCHED0(); });
            asm volatile("s_nop 7" : "+v"(acc[0]), "+v"(acc[1]), "+v"(res[0]), "+v"(res[1]));
            if (__all(mx != 0xFFFFFFFFu) || spin_fail(k, spins)) break;
            PCOUNT(0)
            asm volatile("" ::: "memory");
        }
        STAMP(s, 1)
        RAWBAR()
        st32(k.rs, (w == 0 && l == 0) ? 0u : OOB, k.p0 + CNT(C_X2, rt * 5 + cg), (unsigned)s);
#pragma unroll
        for (int t = 0; t < 2; ++t) {
            const unsigned rp = rb + w * 6144 + t * 1024;
            const f32x4 r = (acc[t] + __builtin_bit_cast(f32x4, lr128(rp))) + (__builtin_bit_cast(f32x4, lr128(rp + 2048)) + __builtin_bit_cast(f32x4, lr128(rp + 4096)));
            const unsigned d0 = cvtpk(r[0], r[1]), d1 = cvtpk(r[2], r[3]);
            u32x2 o; o[0] = cvtpk(blo(res[t][0]) + blo(d0), bhi(res[t][0]) + bhi(d0)); o[1] = cvtpk(blo(res[t][1]) + blo(d1), bhi(res[t][1]) + bhi(d1));
            if (k.c) *(u32x2*)(p.out + (size_t)(s * 32 + rt * 16 + n) * 640 + (cg * 8 + w * 2 + t) * 16 + g * 4) = o;
            else st64(k.rs, xv + t * 512, k.seg(PG_X2, s) + xo, (u32x2){min(o[0], 0xFFFFFFFEu), min(o[1], 0xFFFFFFFEu)});
        }
        STAMP(s, 2)
#pragma unroll
        for (int t = 0; t < 2; ++t) st64(k.rs, s < 62 ? x2v : OOB, k.seg(PG_X2, (s + 2) & 63) + xo + t * 512, ones);
        STAMP(s, 3) STAMP(s, 4) STAMP(s, 5)
        PFLUSH(s)
    }
    PROF_OUT
}

DEVI void warm(const Params& p, const Ctx& k, int xl, int hw) {
    if (k.c) sleep_ticks(WD1);
    set_m0(k.w * 256);
    const unsigned t0 = rclk();
_Pragma("unroll 1")
    for (int jt = hw; jt < 879; jt += 64) {
        const u16* b; unsigned o;
        const int it = WMIX ? ((jt & 1) ? min(440 + (jt >> 1), 871) : jt >> 1) : min(jt, 871);
        while ((int)(rclk() - t0) < (jt >> 6) * PACE) __builtin_amdgcn_s_sleep(1);
        if (it < 200) { b = p.Wo; o = ((xl * 8 + (it / 5) % 8) * 192 + (it / 40 + 1) * 32) * 1280 + (it % 5) * 8192 + k.l * 128; }
        else if (it < 440) { const unsigned ln = (it - 200) * 64 + k.l; b = p.Wv; o = (ln / 24) * 12288 + xl * 3072 + (ln % 24) * 128; }
        else { const unsigned ln = ((it - 440) >> 1) * 64 + k.l; b = (it & 1) ? p.Wq : p.Wk; o = (64 + ln / 24) * 12288 + xl * 3072 + (ln % 24) * 128; }
        asm volatile("global_load_lds_dword %0, %1" :: "v"(o), "s"(b) : "memory");
    }
    if (hw >= 20) {
_Pragma("unroll 1")
        for (int it = hw - 20; it < 240; it += 44) {
            const unsigned ln = (it >> 1) * 64 + k.l;
            const u16* b = (xl & 1) ? p.wd : (it & 1) ? p.wu : p.wg;
            const unsigned o = (xl & 1) ? ((it & 3) * 60 + (it >> 2)) * 8192 + k.l * 128 : (ln / 12) * 3072 + xl * 768 + (ln % 12) * 128;
            while ((int)(rclk() - t0) < 14 * PACE + GDLY + (it / 44) * PACE2) __builtin_amdgcn_s_sleep(1);
            asm volatile("global_load_lds_dword %0, %1" :: "v"(o), "s"(b) : "memory");
        }
    }
    DRAIN();
    RAWBAR()
}

DEVI int xcc_id() { return __builtin_amdgcn_s_getreg(20 | (0 << 6) | (3 << 11)) & 7; }

__global__ __launch_bounds__(256) void init_kernel(Params p) {
    const int b = blockIdx.x, tid = threadIdx.x, l = tid & 63;
    const __amdgpu_buffer_rsrc_t rs = __builtin_amdgcn_make_buffer_rsrc((void*)p.ws, 0, 0x7fffffff, 0x00020000);
    const u32x4 ones = {0xFFFFFFFFu, 0xFFFFFFFFu, 0xFFFFFFFFu, 0xFFFFFFFFu};
    if (b < 64 && tid < 64) {
        unsigned bt[2] = {0xFFFFFFFFu, 0xFFFFFFFFu};
        if (b < NPAGES) {
            const unsigned off = ((unsigned)b << 21) + 7 * 16384 + (l & 7) * 16;
#pragma unroll 1
            for (int r = 0; r < CALR; ++r) {
#pragma unroll
                for (int hf = 0; hf < 2; ++hf) {
                    unsigned long long c0, c1;
                    asm volatile("s_memtime %0\n\ts_waitcnt lgkmcnt(0)" : "=s"(c0) :: "memory");
                    st128(rs, off + hf * 8192, 0, ones);
                    asm volatile("s_waitcnt vmcnt(0)\n\ts_memtime %0\n\ts_waitcnt lgkmcnt(0)" : "=s"(c1) :: "memory");
                    bt[hf] = min(bt[hf], (unsigned)(c1 - c0));
                }
            }
        }
        if (l == 0) ((unsigned*)(p.cm + CM_TAB))[b] = (unsigned)(bt[1] < bt[0]) ^ (unsigned)(xcc_id() >> 2);
    }
    constexpr unsigned N0 = PR_B + 4 * PR_SZ, N1 = 40 * 16384, N2 = 64 * 20480;
    for (unsigned i = (b * 256 + tid) * 16; i < N0 + N1 + N2; i += 256 * 256 * 16) {
        unsigned o;
        u32x4 v = ones;
        if (i < N0) o = i;
        else if (i < N0 + N1) o = ((unsigned)PG_YM << 21) + (i - N0);
        else {
            const unsigned j = i - N0 - N1, s = j / 20480, r = j % 20480;
            const unsigned q = r < 16384 ? r : 16384 + ((r - 16384) >> 11) * 8192 + (r & 2047);
            o = (2u << 21) + s * 32768 + q;
            if (s == 0) {
                const int e = (((q >> 14) << 13) | (q & 8191u)) >> 4, T = e >> 4, hf = (e >> 1) & 1, rr = ((e >> 2) & 3) * 2 + (e & 1);
#pragma unroll
                for (int c = 0; c < 4; ++c) v[c] = min(*(const unsigned*)(p.mem0 + rr * 640 + T * 16 + 4 * c + 2 * hf), 0xFFFFFFFEu);
            }
        }
        *(u32x4*)(p.ws + o) = v;
    }
    if (b == 255) for (unsigned o = CM_TICKET + tid * 16; o < CM_BYTES; o += 4096) *(u32x4*)(p.cm + o) = ones;
}

__global__ __launch_bounds__(256, 1) void mega_kernel(Params p) {
    extern __shared__ __attribute__((aligned(16))) char smem[];
    Ctx k;
    k.ws = p.ws; k.cm = p.cm;
    k.rs = __builtin_amdgcn_make_buffer_rsrc((void*)p.ws, 0, 0x7fffffff, 0x00020000);
    k.l = threadIdx.x & 63; k.w = __builtin_amdgcn_readfirstlane(threadIdx.x >> 6);
    RSTAMP(0)
    k.par = __ballot(((const unsigned*)(p.cm + CM_TAB))[k.l] != 0u);
    if (threadIdx.x == 0) {
        int x = xcc_id(), role = 0;
#pragma unroll 1
        for (int i = 0; i < 8; ++i) {
            const unsigned t = __hip_atomic_fetch_add((GAS unsigned*)(p.cm + CM_TICKET + x * 128), 1u, __ATOMIC_RELAXED, __HIP_MEMORY_SCOPE_AGENT) + 1u;
            if (t < 32u) { role = x * 32 + (int)t; break; }
            x = (x + 1) & 7;
        }
        *(volatile LAS int*)smem = role;
    }
    RAWBAR()
    k.role = __builtin_amdgcn_readfirstlane(*(volatile LAS int*)smem);
    RAWBAR()
    const int x = k.role >> 5, i = k.role & 31, xl = x & 3;
    k.c = x >> 2;
    if (k.c) k.par = ~k.par;
    k.p0 = k.pg(0);
    RSTAMP(1)
    if (k.c) ln_x_batch(p, k, (i * 4 + xl) * 4 + k.w);
    if (i < 8) { qk_role(p, k, smem, xl * 8 + i); return; }
    if (i < 16) { vo_role(p, k, smem, xl * 8 + i - 8); return; }
    warm(p, k, xl, (i - 16) * 4 + k.w);
    if (i < 21) { mred_role(p, k, smem, xl * 5 + i - 16); return; }
    if (i < 26) { tred_role(p, k, smem, xl * 5 + i - 21); return; }
    if (!(xl & 1)) up_role(p, k, smem, xl * 3 + i - 26);
    else if (i < 31) down_role(p, k, smem, (xl >> 1) * 5 + i - 26);
    else row_role(p, k, smem, xl >> 1);
}

extern "C" void kernel_launch(void* const* d_in, const int* in_sizes, int n_in,
                              void* d_out, int out_size, void* d_ws, size_t ws_size,
                              hipStream_t stream) {
    Params p;
    p.x = (const u16*)d_in[0]; p.mem0 = (const u16*)d_in[1];
    p.Wq = (const u16*)d_in[2]; p.Wk = (const u16*)d_in[3]; p.Wv = (const u16*)d_in[4]; p.Wo = (const u16*)d_in[5];
    p.wg = (const u16*)d_in[8]; p.wu = (const u16*)d_in[9]; p.wd = (const u16*)d_in[10];
    p.out = (u16*)d_out; p.cm = (char*)d_ws;
    p.ws = (char*)(((uintptr_t)d_ws + CM_BYTES + ((1u << 21) - 1)) & ~(uintptr_t)((1u << 21) - 1));
    (void)hipFuncSetAttribute((const void*)mega_kernel, hipFuncAttributeMaxDynamicSharedMemorySize, LDS_BYTES);
    init_kernel<<<256, 256, 0, stream>>>(p);
    mega_kernel<<<256, 256, LDS_BYTES, stream>>>(p);
}
```

```cpp
#include <hip/hip_runtime.h>
#include <hip/hip_bf16.h>
#include <stdint.h>
#include <utility>

typedef unsigned short u16;
typedef __attribute__((ext_vector_type(16))) short bf16x16;
typedef __attribute__((ext_vector_type(8))) short bf16x8;
typedef __attribute__((ext_vector_type(4))) short bf16x4;
typedef __attribute__((ext_vector_type(8))) __bf16 hbf8;
typedef __attribute__((ext_vector_type(16))) __bf16 hbf16;
typedef __attribute__((ext_vector_type(4))) float f32x4;
typedef __attribute__((ext_vector_type(2))) float f32x2;
typedef __attribute__((ext_vector_type(8))) unsigned u32x8;
typedef __attribute__((ext_vector_type(4))) unsigned u32x4;
typedef __attribute__((ext_vector_type(2))) unsigned u32x2;
typedef __attribute__((ext_vector_type(2))) __bf16 bf16x2_t;
#define LAS __attribute__((address_space(3)))
#define GAS __attribute__((address_space(1)))
#define DEVI __device__ __forceinline__

#define PSLEEP 8
#define WD1 400
#define PACE 65
#define XPOS 74
#define YPOS 40
#define XPOSY 1
#define PWS 1
#define WUS 0
#define WMIX 1
#define PACE2 30
#define GDLY 80
#define QDLY 0
#define CALR 3
#define SPG 3
#define DQ 12
#define DV 12
#define BSLP 1
#define SDLY 0
#define UPOS 79
#define RDLY 45
#define NOYST 0
#define NOTOK 0
#define NOYLD 0
#define GPOSY 9
#define OLDMAP 1
#define UPODD 0
#define XLD 10
#define TROT 8
#define KOUT 1
#define APOS 33
#define GPOS 73

#define EXP(o) ((((unsigned)(o) >> 13) << 14) | ((unsigned)(o) & 8191u))
enum { C_XN = 0, C_Y = 1, C_X1 = 2, C_XB = 3, C_H = 4, C_X2 = 5, C_BC = 6 };
#define CNT(type, s) ((unsigned)((type) * 16384 + (s) * 128))
constexpr unsigned PR_B = EXP(65536), PR_SZ = EXP(12 * 8192);
constexpr unsigned DUMMY_B = EXP(0);
constexpr int PG_MEMF = 2;
#define YXR 8
constexpr int PG_YX = 3;
constexpr int PG_XNA = PG_YX + 2 * YXR;
constexpr int PG_X1 = PG_XNA + 4, PG_X2 = PG_X1 + 4;
constexpr int PG_H = PG_X2 + 4;
constexpr int PG_YM = PG_H + 8;
constexpr int NPAGES = PG_YM + 2;
static_assert(NPAGES <= 64, "map");
constexpr unsigned CM_TAB = 0, CM_TICKET = 1024, CM_TMO = 4096, CM_BYTES = 8192;
constexpr unsigned SPIN_CAP = 1u << 20;
#define AUX_SC1 16
#define LDS_BYTES (160 * 1024)

struct Params {
    const u16* x; const u16* mem0; const u16* Wq; const u16* Wk; const u16* Wv; const u16* Wo;
    const u16* wg; const u16* wu; const u16* wd; u16* out; char* cm; char* ws;
};
struct Ctx {
    char* ws; char* cm; __amdgpu_buffer_rsrc_t rs; unsigned long long par;
    int c, l, w, role;
    unsigned p0;
    DEVI unsigned pg(int page) const { return ((unsigned)page << 21) | (((unsigned)(par >> page) & 1u) << 13); }
    DEVI unsigned pg0(int page) const { return pg(page) ^ ((unsigned)c << 13); }
    DEVI unsigned seg(int page, int s) const { return pg(page + (s >> 4)) + (unsigned)(s & 15) * 131072u; }
    DEVI unsigned seg0(int page, int s) const { return seg(page, s) ^ ((unsigned)c << 13); }
    DEVI unsigned hseg(int s) const { return pg(PG_H + (s >> 3)) + (unsigned)(s & 7) * 262144u; }
    DEVI unsigned yx(int sg, int wq) const { return pg(PG_YX + (sg % YXR) * 2 + (wq >> 1)) + (unsigned)(wq & 1) * 1048576u; }
    DEVI unsigned ym(int s) const { return pg(PG_YM + ((s >> 1) & 1)) + (unsigned)(s & 1) * EXP(327680); }
    DEVI unsigned memf(int s) const { return pg(PG_MEMF) + (unsigned)s * 32768u; }
    DEVI char* cnt(int type, int s) const { return ws + (p0 + CNT(type, s)); }
    DEVI char* cnt0(int type, int s) const { return ws + ((p0 ^ ((unsigned)c << 13)) + CNT(type, s)); }
};

DEVI unsigned cvtpk(float a, float b) { return __builtin_bit_cast(unsigned, __builtin_convertvector((f32x2){a, b}, bf16x2_t)); }
DEVI unsigned packs(float a, float b) { return min(cvtpk(a, b), 0xFFFFFFFEu); }
DEVI float blo(unsigned x) { return __uint_as_float(x << 16); }
DEVI float bhi(unsigned x) { return __uint_as_float(x & 0xFFFF0000u); }
DEVI float bfr(float f) { return __uint_as_float(cvtpk(0.f, f)); }
DEVI u32x4 ld128(__amdgpu_buffer_rsrc_t rs, unsigned v, unsigned s) { return __builtin_amdgcn_raw_buffer_load_b128(rs, (int)v, (int)s, AUX_SC1); }
DEVI u32x2 ld64(__amdgpu_buffer_rsrc_t rs, unsigned v, unsigned s) { return __builtin_amdgcn_raw_buffer_load_b64(rs, (int)v, (int)s, AUX_SC1); }
DEVI unsigned ld32(__amdgpu_buffer_rsrc_t rs, unsigned v, unsigned s) { return __builtin_amdgcn_raw_buffer_load_b32(rs, (int)v, (int)s, AUX_SC1); }
DEVI void st128(__amdgpu_buffer_rsrc_t rs, unsigned v, unsigned s, u32x4 x) {
    __builtin_amdgcn_raw_buffer_store_b128(x, rs, (int)v, (int)s, AUX_SC1);
    asm volatile("s_nop 1" :: "v"(x) : "memory");
}
DEVI void st64(__amdgpu_buffer_rsrc_t rs, unsigned v, unsigned s, u32x2 x) { __builtin_amdgcn_raw_buffer_store_b64(x, rs, (int)v, (int)s, AUX_SC1); }
DEVI void st32(__amdgpu_buffer_rsrc_t rs, unsigned v, unsigned s, unsigned x) { __builtin_amdgcn_raw_buffer_store_b32(x, rs, (int)v, (int)s, AUX_SC1); }
#define RAWBAR() { __builtin_amdgcn_s_waitcnt(0xc07f); __builtin_amdgcn_s_barrier(); }
#define BARL(n) { asm volatile("" ::: "memory"); __builtin_amdgcn_s_waitcnt(0xc07f | ((n) << 8)); __builtin_amdgcn_s_barrier(); asm volatile("" ::: "memory"); }
#define DRAIN() asm volatile("s_waitcnt vmcnt(0)" ::: "memory")
#define SCHED0() __builtin_amdgcn_sched_barrier(0)
#define OOB 0x80000000u
DEVI int opq(int x) { asm volatile("" : "+v"(x)); return x; }
#define LANE const int l = opq(k.l), g = l >> 4, n = l & 15; (void)g; (void)n;
DEVI unsigned umax3(unsigned a, unsigned b, unsigned c) { return max(max(a, b), c); }
DEVI unsigned umax4(u32x4 v) { return max(umax3(v[0], v[1], v[2]), v[3]); }
DEVI unsigned rclk() { return (unsigned)__builtin_amdgcn_s_memrealtime(); }
DEVI unsigned long long rt_async() { unsigned long long t; asm volatile("s_memrealtime %0" : "=s"(t)); return t; }
DEVI void lgkm0(unsigned long long& a) { asm volatile("s_waitcnt lgkmcnt(0)" : "+s"(a) :: "memory"); }
DEVI unsigned spack(unsigned lo, unsigned hi) { unsigned d; asm volatile("s_pack_ll_b32_b16 %0, %1, %2" : "=s"(d) : "s"(lo), "s"(hi)); return d; }
DEVI int lane() { int l; asm volatile("v_mbcnt_lo_u32_b32 %0, -1, 0\n\tv_mbcnt_hi_u32_b32 %0, -1, %0" : "=v"(l)); __builtin_assume(l >= 0 && l < 64); return l; }
DEVI void sleep_ticks(int d) { unsigned target = rclk() + d; while ((int)(rclk() - target) < 0) __builtin_amdgcn_s_sleep(4); }
DEVI float xor16max(float x) { auto r = __builtin_amdgcn_permlane16_swap(__float_as_uint(x), __float_as_uint(x), false, false); return __builtin_fmaxf(__uint_as_float(r[0]), __uint_as_float(r[1])); }
DEVI float xor32max(float x) { auto r = __builtin_amdgcn_permlane32_swap(__float_as_uint(x), __float_as_uint(x), false, false); return __builtin_fmaxf(__uint_as_float(r[0]), __uint_as_float(r[1])); }
DEVI float xor16add(float x) { auto r = __builtin_amdgcn_permlane16_swap(__float_as_uint(x), __float_as_uint(x), false, false); return __uint_as_float(r[0]) + __uint_as_float(r[1]); }
DEVI float xor32add(float x) { auto r = __builtin_amdgcn_permlane32_swap(__float_as_uint(x), __float_as_uint(x), false, false); return __uint_as_float(r[0]) + __uint_as_float(r[1]); }
DEVI float fold32(float a, float b) { auto r = __builtin_amdgcn_permlane32_swap(__float_as_uint(a), __float_as_uint(b), false, false); return __uint_as_float(r[0]) + __uint_as_float(r[1]); }
#define ROR16(x, n) __builtin_bit_cast(float, __builtin_amdgcn_update_dpp(0, __builtin_bit_cast(int, (x)), 0x120 + (n), 0xf, 0xf, false))
DEVI float row16sum(float x) { x += ROR16(x, 8); x += ROR16(x, 4); x += ROR16(x, 2); x += ROR16(x, 1); return x; }
DEVI unsigned z0() { unsigned z; asm volatile("v_mov_b32 %0, 0" : "=v"(z)); return z; }
DEVI void cnt_add(char* p) { __hip_atomic_fetch_add((GAS unsigned*)(p + z0()), z0() + 1u, __ATOMIC_RELAXED, __HIP_MEMORY_SCOPE_AGENT); }
DEVI unsigned cnt_get(char* p) { return __hip_atomic_load((GAS unsigned*)(p + z0()), __ATOMIC_RELAXED, __HIP_MEMORY_SCOPE_AGENT) + 1u; }
DEVI bool spin_fail(const Ctx& k, unsigned spins) {
    if (spins > SPIN_CAP || ((spins & 255u) == 255u && __builtin_amdgcn_readfirstlane(cnt_get(k.cm + CM_TMO)) != 0)) { cnt_add(k.cm + CM_TMO); return true; }
    return false;
}
DEVI void cnt_wait(const Ctx& k, char* c, unsigned need) {
_Pragma("unroll 1")
    for (unsigned spins = 0; (unsigned)__builtin_amdgcn_readfirstlane(cnt_get(c)) < need; ++spins) {
        __builtin_amdgcn_s_sleep(PSLEEP);
        if (spin_fail(k, spins)) break;
    }
    __builtin_amdgcn_fence(__ATOMIC_ACQUIRE, "wavefront");
}
DEVI unsigned xn_get(const Ctx& k, unsigned l16, int s) { return ld32(k.rs, opq(l16) & (k.c ? 128u : 0u), k.p0 + CNT(C_XN, k.c ? (s & 1) * 2 : s & 63)); }
DEVI bool xn_has(const Ctx& k, unsigned v, int s) { return __all((int)v >= (k.c ? s : 7)); }
DEVI void xn_wait(const Ctx& k, unsigned l16, int s) {
_Pragma("unroll 1")
    for (unsigned spins = 0; !xn_has(k, xn_get(k, l16, s), s); ++spins) {
        asm volatile("" ::: "memory");
        __builtin_amdgcn_s_sleep(2);
        if (spin_fail(k, spins)) break;
    }
    __builtin_amdgcn_fence(__ATOMIC_ACQUIRE, "wavefront");
}
template <int N> DEVI void prog_wait(const Ctx& k, unsigned a, int s) {
_Pragma("unroll 1")
    for (unsigned spins = 0; !__all((int)ld32(k.rs, min(k.l, N - 1) * 128, a) >= s); ++spins) {
        asm volatile("" ::: "memory");
        __builtin_amdgcn_s_sleep(PWS);
        if (spin_fail(k, spins)) break;
    }
    __builtin_amdgcn_fence(__ATOMIC_ACQUIRE, "wavefront");
}
template <int OFF> DEVI void dma1(const void* sbase, unsigned voff, unsigned lds) {
    asm volatile("s_add_i32 m0, %2, %3\n\ts_nop 0\n\tglobal_load_lds_dwordx4 %0, %1" :: "v"(voff), "s"(sbase), "s"(lds), "n"(OFF) : "memory", "scc");
}
DEVI void set_m0(unsigned lds) { asm volatile("s_mov_b32 m0, %0" :: "s"(lds) : "memory"); }
template <int OFF> DEVI void dmab(__amdgpu_buffer_rsrc_t rs, unsigned voff, unsigned soff) {
    asm volatile("buffer_load_dwordx4 %0, %1, %2 offen offset:%3 sc1 lds" :: "v"(voff), "s"(rs), "s"(soff), "n"(OFF) : "memory");
}
#define PIN(x) asm volatile("" : "+v"(x))
#define PINS(x) asm volatile("" : "+s"(x))
#define LEAD()
#define TOUCH(x) asm volatile("" :: "v"(x))
DEVI float csel(float a, float b, unsigned long long m) { float d; asm volatile("v_cndmask_b32_e64 %0, %1, %2, %3" : "=v"(d) : "v"(a), "v"(b), "s"(m)); return d; }
#define LO8(x) __builtin_shufflevector(x, x, 0, 1, 2, 3, 4, 5, 6, 7)
#define HI8(x) __builtin_shufflevector(x, x, 8, 9, 10, 11, 12, 13, 14, 15)
#define CAT8(a, b) __builtin_shufflevector(a, b, 0, 1, 2, 3, 4, 5, 6, 7, 8, 9, 10, 11, 12, 13, 14, 15)
template <bool AG> DEVI void smf(f32x4& c, const bf16x8& a, const bf16x16& w, unsigned ix) {
    if constexpr (AG) asm volatile("v_smfmac_f32_16x16x64_bf16 %0, %1, %2, %3" : "+v"(c) : "v"(a), "a"(w), "v"(ix));
    else asm volatile("v_smfmac_f32_16x16x64_bf16 %0, %1, %2, %3" : "+v"(c) : "v"(a), "v"(w), "v"(ix));
}
#define WTAB(X) X(0, "{a[0:7]}", "{a[0:3]}", "{a[4:7]}") X(1, "{a[8:15]}", "{a[8:11]}", "{a[12:15]}") X(2, "{a[16:23]}", "{a[16:19]}", "{a[20:23]}") X(3, "{a[24:31]}", "{a[24:27]}", "{a[28:31]}") X(4, "{a[32:39]}", "{a[32:35]}", "{a[36:39]}") X(5, "{a[40:47]}", "{a[40:43]}", "{a[44:47]}") X(6, "{a[48:55]}", "{a[48:51]}", "{a[52:55]}") X(7, "{a[56:63]}", "{a[56:59]}", "{a[60:63]}") X(8, "{a[64:71]}", "{a[64:67]}", "{a[68:71]}") X(9, "{a[72:79]}", "{a[72:75]}", "{a[76:79]}") X(10, "{a[80:87]}", "{a[80:83]}", "{a[84:87]}") X(11, "{a[88:95]}", "{a[88:91]}", "{a[92:95]}") X(12, "{a[96:103]}", "{a[96:99]}", "{a[100:103]}") X(13, "{a[104:111]}", "{a[104:107]}", "{a[108:111]}") X(14, "{a[112:119]}", "{a[112:115]}", "{a[116:119]}") X(15, "{a[120:127]}", "{a[120:123]}", "{a[124:127]}") X(16, "{a[128:135]}", "{a[128:131]}", "{a[132:135]}") X(17, "{a[136:143]}", "{a[136:139]}", "{a[140:143]}") X(18, "{a[144:151]}", "{a[144:147]}", "{a[148:151]}") X(19, "{a[152:159]}", "{a[152:155]}", "{a[156:159]}") X(20, "{a[160:167]}", "{a[160:163]}", "{a[164:167]}") X(21, "{a[168:175]}", "{a[168:171]}", "{a[172:175]}") X(22, "{a[176:183]}", "{a[176:179]}", "{a[180:183]}") X(23, "{a[184:191]}", "{a[184:187]}", "{a[188:191]}") X(24, "{a[192:199]}", "{a[192:195]}", "{a[196:199]}") X(25, "{a[200:207]}", "{a[200:203]}", "{a[204:207]}") X(26, "{a[208:215]}", "{a[208:211]}", "{a[212:215]}") X(27, "{a[216:223]}", "{a[216:219]}", "{a[220:223]}") X(28, "{a[224:231]}", "{a[224:227]}", "{a[228:231]}") X(29, "{a[232:239]}", "{a[232:235]}", "{a[236:239]}") X(30, "{a[240:247]}", "{a[240:243]}", "{a[244:247]}") X(31, "{a[248:255]}", "{a[248:251]}", "{a[252:255]}") X(32, "{v[96:103]}", "{v[96:99]}", "{v[100:103]}") X(33, "{v[104:111]}", "{v[104:107]}", "{v[108:111]}") X(34, "{v[112:119]}", "{v[112:115]}", "{v[116:119]}") X(35, "{v[120:127]}", "{v[120:123]}", "{v[124:127]}") X(36, "{v[128:135]}", "{v[128:131]}", "{v[132:135]}") X(37, "{v[136:143]}", "{v[136:139]}", "{v[140:143]}") X(38, "{v[144:151]}", "{v[144:147]}", "{v[148:151]}") X(39, "{v[152:159]}", "{v[152:155]}", "{v[156:159]}") X(40, "{v[160:167]}", "{v[160:163]}", "{v[164:167]}") X(41, "{v[168:175]}", "{v[168:171]}", "{v[172:175]}") X(42, "{v[176:183]}", "{v[176:179]}", "{v[180:183]}") X(43, "{v[184:191]}", "{v[184:187]}", "{v[188:191]}") X(44, "{v[192:199]}", "{v[192:195]}", "{v[196:199]}") X(45, "{v[200:207]}", "{v[200:203]}", "{v[204:207]}") X(46, "{v[208:215]}", "{v[208:211]}", "{v[212:215]}") X(47, "{v[216:223]}", "{v[216:219]}", "{v[220:223]}") X(48, "{v[224:231]}", "{v[224:227]}", "{v[228:231]}") X(49, "{v[232:239]}", "{v[232:235]}", "{v[236:239]}") X(50, "{v[240:247]}", "{v[240:243]}", "{v[244:247]}") X(51, "{v[248:255]}", "{v[248:251]}", "{v[252:255]}")
template <int ID> DEVI void smfw(f32x4& c, const bf16x8& a, const bf16x16& w, unsigned ix) {
#define X(i, P, L, H) if constexpr (ID == i) asm volatile("v_smfmac_f32_16x16x64_bf16 %0, %1, %2, %3" : "+v"(c) : "v"(a), P(w), "v"(ix));
    WTAB(X)
#undef X
}
template <int ID, int HF, bool WB, bool FIRST> DEVI void mfw(f32x4& c, const bf16x16& w, const bf16x8& x) {
#define Y(R, W8) { if constexpr (FIRST && WB) asm volatile("v_mfma_f32_16x16x32_bf16 %0, %2, %1, 0" : "=&v"(c) : R(W8), "v"(x)); \
    else if constexpr (FIRST) asm volatile("v_mfma_f32_16x16x32_bf16 %0, %1, %2, 0" : "=&v"(c) : R(W8), "v"(x)); \
    else if constexpr (WB) asm volatile("v_mfma_f32_16x16x32_bf16 %0, %2, %1, %0" : "+v"(c) : R(W8), "v"(x)); \
    else asm volatile("v_mfma_f32_16x16x32_bf16 %0, %1, %2, %0" : "+v"(c) : R(W8), "v"(x)); }
#define X(i, P, L, H) if constexpr (ID == i) { if constexpr (HF) Y(H, HI8(w)) else Y(L, LO8(w)) }
    WTAB(X)
#undef X
#undef Y
}
template <bool AG, bool FIRST> DEVI void mfa(f32x4& c, const bf16x8& w, const bf16x8& x) {
    if constexpr (AG && FIRST) asm volatile("v_mfma_f32_16x16x32_bf16 %0, %1, %2, 0" : "=&v"(c) : "a"(w), "v"(x));
    else if constexpr (AG) asm volatile("v_mfma_f32_16x16x32_bf16 %0, %1, %2, %0" : "+v"(c) : "a"(w), "v"(x));
    else if constexpr (FIRST) asm volatile("v_mfma_f32_16x16x32_bf16 %0, %1, %2, 0" : "=&v"(c) : "v"(w), "v"(x));
    else asm volatile("v_mfma_f32_16x16x32_bf16 %0, %1, %2, %0" : "+v"(c) : "v"(w), "v"(x));
}
template <bool AG, bool FIRST> DEVI void mfb(f32x4& c, const bf16x8& x, const bf16x8& w) {
    if constexpr (AG && FIRST) asm volatile("v_mfma_f32_16x16x32_bf16 %0, %1, %2, 0" : "=&v"(c) : "v"(x), "a"(w));
    else if constexpr (AG) asm volatile("v_mfma_f32_16x16x32_bf16 %0, %1, %2, %0" : "+v"(c) : "v"(x), "a"(w));
    else if constexpr (FIRST) asm volatile("v_mfma_f32_16x16x32_bf16 %0, %1, %2, 0" : "=&v"(c) : "v"(x), "v"(w));
    else asm volatile("v_mfma_f32_16x16x32_bf16 %0, %1, %2, %0" : "+v"(c) : "v"(x), "v"(w));
}
DEVI f32x4 mf(const bf16x8& a, const bf16x8& b, const f32x4& c) { return __builtin_amdgcn_mfma_f32_16x16x32_bf16(__builtin_bit_cast(hbf8, a), __builtin_bit_cast(hbf8, b), c, 0, 0, 0); }
template <int N, class F> DEVI void sfor(F f) { [&]<int... I>(std::integer_sequence<int, I...>) { (f(std::integral_constant<int, I>{}), ...); }(std::make_integer_sequence<int, N>{}); }
DEVI bf16x8 lds128(const char* p) { return *(const bf16x8*)p; }
DEVI bf16x4 ldstr(const char* p) { return __builtin_amdgcn_ds_read_tr16_b64_v4i16((LAS bf16x4*)p); }
DEVI bf16x8 ldstr2(const char* p, int d) { bf16x4 a = ldstr(p), b = ldstr(p + d); return __builtin_shufflevector(a, b, 0, 1, 2, 3, 4, 5, 6, 7); }

#define STAMP(s, k)
#define QSTAMP(f, i)
#define STOP(s)
#define PFLUSH(s)
#define PROF_DECL
#define RSTAMP(i)
#define PCOUNT(i)
#define PROF_OUT

#define AQ(m, i)
#define AC3(i, j, k) { }
#define AST(i)
#define AAC(i)
#define ACCM 0
#define HSTOP(m) if constexpr ((ACCM != 0) == m) { STOP(0) }
#define PC8(i, x)
#define PROF_ONLY(x)
#define PROF_S(x)
template <int NT, int OFF> DEVI void wp_issue(const u16* W, int ld, unsigned ring, int l) {
    sfor<NT>([&](auto I) { constexpr int i = decltype(I)::value;
        const int id = i * 64 + l, row = id / (2 * NT), cc = id % (2 * NT);
        dma1<OFF + i * 1024>(W, (unsigned)(row * ld + cc * 8) * 2u, ring); });
}
template <int NT, bool STD> DEVI bf16x8 wp_read(const char* slot, int T, int l) {
    const int row = (STD ? 8 : 4) * (l >> 4) + ((l >> 2) & 3);
    return ldstr2(slot + row * (NT * 32) + T * 32 + (l & 3) * 8, (STD ? 4 : 16) * NT * 32);
}
template <int NU, int NI, int D, class ISSUE, class READ> DEVI void wstream(char* ring, ISSUE issue, READ read) {
    const unsigned base = (unsigned)(size_t)(LAS char*)ring;
    sfor<D>([&](auto U) { constexpr int u = decltype(U)::value; if constexpr (u < NU) issue(U, base); });
    sfor<NU>([&](auto U) {
        constexpr int u = decltype(U)::value, left = (NU - u < D ? NU - u : D) - 1;
        asm volatile("s_waitcnt vmcnt(%0)" :: "n"(left * NI) : "memory");
        read(U, ring + (u % D) * NI * 1024);
        __builtin_amdgcn_s_waitcnt(0xc07f);
        asm volatile("" ::: "memory");
        if constexpr (u + D < NU) issue(std::integral_constant<int, u + D>{}, base);
    });
}

constexpr int L_XN = 0;
constexpr int L_K = 40960;
constexpr int L_MEM = L_K + 12288;
constexpr int L_QM = L_MEM + 10240;
constexpr int L_Q = L_QM + 12288;
constexpr int L_W = L_Q + 12288;
#define IC(x) std::integral_constant<int, (x)>{}
#define CV(X) decltype(X)::value
DEVI bf16x8 lr128(unsigned a) { return *(const LAS bf16x8*)(size_t)a; }
DEVI bf16x4 lrtr(unsigned a) { return __builtin_amdgcn_ds_read_tr16_b64_v4i16((LAS bf16x4*)(size_t)a); }
#define CAT4(a, b) __builtin_shufflevector(a, b, 0, 1, 2, 3, 4, 5, 6, 7)
DEVI void lw128(unsigned a, u32x4 v) { *(LAS u32x4*)(size_t)a = v; }
DEVI void lw64(unsigned a, u32x2 v) { *(LAS u32x2*)(size_t)a = v; }
DEVI void lw32(unsigned a, unsigned v) { *(LAS unsigned*)(size_t)a = v; }
template <int N> DEVI void bar_lgkm() { asm volatile("s_waitcnt lgkmcnt(%0)\n\ts_barrier" :: "n"(N) : "memory"); }
DEVI void atom1(__amdgpu_buffer_rsrc_t rs, unsigned voff, unsigned soff) {
    unsigned one; asm volatile("v_mov_b32 %0, 1\n\tbuffer_atomic_add %0, %1, %2, %3 offen" : "=&v"(one) : "v"(voff), "s"(rs), "s"(soff) : "memory");
}
DEVI unsigned vadd(unsigned v, unsigned sc) { unsigned d; asm volatile("v_add_u32 %0, %1, %2" : "=v"(d) : "s"(sc), "v"(v)); return d; }
DEVI float vzero() { float z; asm volatile("v_mov_b32 %0, 0" : "=v"(z)); return z; }
template <int B, int I> DEVI void xn_op(const Ctx& k, unsigned v, unsigned src, unsigned dst) {
    if constexpr (I == 0) set_m0(dst + B * 8192);
    else if constexpr (I == 1) dmab<0>(k.rs, v, src + B * 16384);
    else dmab<1024>(k.rs, v, src + B * 16384);
}
DEVI void xn_all(const Ctx& k, unsigned v, unsigned src, unsigned dst) {
    sfor<5>([&](auto B) { xn_op<CV(B), 0>(k, v, src, dst); asm volatile("s_nop 0"); xn_op<CV(B), 1>(k, v, src, dst); xn_op<CV(B), 2>(k, v, src, dst); });
}
struct Xn {
    unsigned src, dst, fa, fi, fx, sg, need, t, sb, se, l16, c, off;
    unsigned long long flip, m;
    DEVI void init(const Ctx& k) {
        src = k.seg(PG_XNA, 0) + k.w * 2048; dst = L_XN + k.w * 2048; sg = 0; off = ~0u;
        fa = k.p0 + CNT(C_XN, 0); fi = k.c ? 0u : 128u; fx = k.c ? 256u : 0u;
        const unsigned q = (unsigned)(k.par >> PG_XNA), d = q ^ (q >> 1);
        flip = ((unsigned long long)(d & 1) << 16) | ((unsigned long long)(d & 2) << 31) | ((unsigned long long)(d & 4) << 46);
    }
    template <int I> DEVI void next(const Ctx& k, unsigned l16) {
        if constexpr (I == 0) { sg += 1; src += 131072; PINS(sg); PINS(src); }
        if constexpr (I == 1) asm volatile("s_bitcmp1_b64 %1, %2\n\ts_cselect_b32 %0, 0x2000, 0" : "=s"(t) : "s"(flip), "s"(sg) : "scc");
        if constexpr (I == 2) { src ^= t; fa += fi; PINS(src); PINS(fa); }
        if constexpr (I == 3) { fa ^= fx; PINS(fa); asm volatile("v_and_b32 %0, %1, %2" : "=v"(c) : "s"(fx >> 1), "v"(l16)); }
        if constexpr (I == 4) c = ld32(k.rs, c, fa);
        if constexpr (I == 5) { need = k.c ? sg : 7u; PINS(need); }
        if constexpr (I == 6) { need = sg < 64u ? need : 0x7FFFFFFFu; PINS(need); }
        if constexpr (I == 7) asm volatile("v_cmp_gt_i32_e64 %0, %1, %2" : "=s"(m) : "s"(need), "v"(c) : "memory");
        if constexpr (I == 8) asm volatile("s_cmp_lg_u64 %2, 0\n\ts_cselect_b32 %0, -1, 0\n\ts_cselect_b32 %1, 0x200000, %3" : "=&s"(off), "=s"(se) : "s"(m), "s"(src) : "scc");
        if constexpr (I == 10) asm volatile("s_cmp_lg_u32 %1, 64\n\ts_cselect_b32 %0, %0, 0" : "+s"(off) : "s"(sg) : "scc");
    }
    template <int B, int I> DEVI void op(const Ctx& k) {
        if constexpr (I == 0) { set_m0(dst + B * 8192); sb = se + B * 16384; PINS(sb); }
        else dmab<(I - 1) * 1024>(k.rs, l16, sb);
    }
    template <int NB> DEVI void late(const Ctx& k, unsigned l16) {
        xn_wait(k, l16, sg);
        se = src;
        sfor<NB>([&](auto B) { op<CV(B), 0>(k); asm volatile("s_nop 0"); op<CV(B), 1>(k); op<CV(B), 2>(k); });
        DRAIN();
    }
};
template <int OFF> DEVI u32x4 ald128(__amdgpu_buffer_rsrc_t rs, unsigned v, unsigned s) { u32x4 d; asm volatile("buffer_load_dwordx4 %0, %1, %2, %3 offen offset:%4 sc1" : "=v"(d) : "v"(v), "s"(rs), "s"(s), "n"(OFF) : "memory"); return d; }
#define MADR(b) (L_MEM + ((b) < 6 ? ((b) - 1) * 2048 : ((b) - 6) * 2048 + 3072))
struct Gather {
    u32x4 p0, p1, v0, v1;
    unsigned mx, a, aw, b, base, wo, dst;
    unsigned long long bad;
    DEVI void init(const Ctx& k) { base = k.memf(0); wo = k.w * 1024; dst = L_MEM + k.w * 2048; }
    template <int I> DEVI void op(const Ctx& k, unsigned l16, int s) {
        if constexpr (I == 0) { a = ((unsigned)s << 15) & 0x1F8000u; PINS(a); }
        if constexpr (I == 1) { a += base; aw = a + wo; PINS(a); PINS(aw); }
        if constexpr (I == 2) { b = aw + 3072 + (k.w >> 1) * 8192; PINS(b); set_m0(dst); }
        if constexpr (I == 3) v0 = ald128<2048>(k.rs, l16, aw);
        if constexpr (I == 4) v1 = ald128<3072>(k.rs, l16, b);
        if constexpr (I == 5) dmab<2048>(k.rs, l16, aw);
        if constexpr (I == 6) dmab<3072>(k.rs, l16, b);
        if constexpr (I == 7) p0 = ald128<0>(k.rs, l16, a);
        if constexpr (I == 8) p1 = ald128<1024>(k.rs, l16, a);
    }
    template <int N> DEVI void wait() { asm volatile("s_waitcnt vmcnt(%4)" : "+v"(p0), "+v"(p1), "+v"(v0), "+v"(v1) : "n"(N) : "memory"); }
    template <int I> DEVI void fin(unsigned other) {
#define MX3 "v_max3_u32 %0, %0, %3, %4\n\tv_max3_u32 %0, %0, %5, %6"
        if constexpr (I == 0) asm volatile("v_max3_u32 %0, %7, %1, %2\n\t" MX3 : "=&v"(mx) : "v"(v0[0]), "v"(v0[1]), "v"(v0[2]), "v"(v0[3]), "v"(v1[0]), "v"(v1[1]), "s"(other));
        if constexpr (I == 1) asm volatile("v_max3_u32 %0, %0, %1, %2\n\t" MX3 : "+v"(mx) : "v"(v1[2]), "v"(v1[3]), "v"(p0[0]), "v"(p0[1]), "v"(p0[2]), "v"(p0[3]));
        if constexpr (I == 2) asm volatile("v_max3_u32 %1, %1, %2, %3\n\tv_max3_u32 %1, %1, %4, %5\n\tv_cmp_eq_u32_e64 %0, -1, %1" : "=s"(bad) : "v"(mx), "v"(p1[0]), "v"(p1[1]), "v"(p1[2]), "v"(p1[3]));
    }
    DEVI void also(unsigned other) { asm volatile("v_mov_b32 %0, %1" : "=v"(mx) : "s"(other)); }
    DEVI void all() {
        asm volatile("s_waitcnt vmcnt(0)\n\tv_max3_u32 %1, %1, %2, %3\n\tv_max3_u32 %1, %1, %4, %5\n\tv_max3_u32 %1, %1, %6, %7\n\tv_max3_u32 %1, %1, %8, %9\n\tv_max3_u32 %1, %1, %10, %11\n\tv_max3_u32 %1, %1, %12, %13\n\t"
            "v_max3_u32 %1, %1, %14, %15\n\tv_max3_u32 %1, %1, %16, %17\n\tv_cmp_eq_u32_e64 %0, -1, %1" : "=s"(bad) : "v"(mx), "v"(v0[0]), "v"(v0[1]), "v"(v0[2]), "v"(v0[3]), "v"(v1[0]), "v"(v1[1]), "v"(v1[2]), "v"(v1[3]),
            "v"(p0[0]), "v"(p0[1]), "v"(p0[2]), "v"(p0[3]), "v"(p1[0]), "v"(p1[1]), "v"(p1[2]), "v"(p1[3]) : "memory");
    }
    DEVI void check() { also(0); all(); asm volatile("s_nop 3"); }
    template <int I> DEVI void at(const Ctx& k, unsigned l16) {
        if constexpr (I == 1) op<2>(k, l16, 0);
        if constexpr (I >= 2 && I < 15 && (I % 5 == 2 || I % 5 == 4)) op<3 + (I / 5) * 2 + (I % 5) / 4>(k, l16, 0);
    }
    DEVI void again(const Ctx& k, unsigned l16) {
        asm volatile("s_mov_b32 m0, %9\n\tbuffer_load_dwordx4 %2, %4, %5, %7 offen offset:2048 sc1\n\tbuffer_load_dwordx4 %3, %4, %5, %8 offen offset:3072 sc1\n\t"
            "buffer_load_dwordx4 %4, %5, %7 offen offset:2048 sc1 lds\n\tbuffer_load_dwordx4 %4, %5, %8 offen offset:3072 sc1 lds\n\t"
            "buffer_load_dwordx4 %0, %4, %5, %6 offen sc1\n\tbuffer_load_dwordx4 %1, %4, %5, %6 offen offset:1024 sc1\n\ts_waitcnt vmcnt(0)"
            : "+v"(p0), "+v"(p1), "+v"(v0), "+v"(v1) : "v"(l16), "s"(k.rs), "s"(a), "s"(aw), "s"(b), "s"(dst) : "memory");
        check();
    }
};
DEVI void clr2(f32x4& c) { f32x2 a, b; asm volatile("v_mov_b64 %0, 0\n\tv_mov_b64 %1, 0" : "=v"(a), "=v"(b)); c = __builtin_shufflevector(a, b, 0, 1, 2, 3); }
#define NOP7_3(a) asm volatile("s_nop 7" : "+v"(a[0]), "+v"(a[1]), "+v"(a[2]))

struct G3 {
    f32x4 a0[3], a1[3];
    bf16x8 x0[20], x1[20], wl[20];
    unsigned l16, lwv;
    template <int KS> DEVI void r0() { x0[KS] = lr128(l16 + L_XN + KS * 2048); }
    template <int KS> DEVI void r1() { x1[KS] = lr128(l16 + L_XN + KS * 2048 + 1024); }
    template <int KS> DEVI void rw() { if constexpr (KS >= 4) wl[KS] = lr128(lwv + (KS - 4) * 1024); }
    template <bool WL, int KS> DEVI void pre() { if constexpr (WL && KS >= 4) asm volatile("" :: "v"(wl[KS]), "v"(x0[KS]), "v"(x1[KS])); else asm volatile("" :: "v"(x0[KS]), "v"(x1[KS])); }
    template <int ID0, bool WL, int ks, int m> DEVI void mm(const bf16x16 (&W)[52]) {
        constexpr int b = ks >> 1, t = m % 3, id = ID0 + t * 10 + b;
        f32x4& c = m < 3 ? a0[t] : a1[t];
        const bf16x8& x = m < 3 ? x0[ks] : x1[ks];
        if constexpr (WL && t == 2 && b >= 2) { if constexpr (ID0 == 30) mfb<false, false>(c, x, wl[ks]); else mfa<false, false>(c, wl[ks], x); }
        else mfw<id % 52, ks & 1, ID0 == 30, ks == 0>(c, W[id % 52], x);
        SCHED0();
    }
    template <int ID0, bool WL, int K0, int K1, int PF, bool H6 = false, class HOOK> DEVI void run(const bf16x16 (&W)[52], HOOK hook) {
        sfor<K1 - K0>([&](auto KK) {
            constexpr int kk = CV(KK), ks = K0 + kk;
            sfor<6>([&](auto M) {
                constexpr int m = CV(M);
                mm<ID0, WL, ks, m>(W);
                if constexpr (m == 5 && ks + 1 < K1) { pre<WL, (ks + 1) % 20>(); SCHED0(); }
                if constexpr (ks + 2 < PF) {
                    if constexpr (m == 0) r0<(ks + 2) % 20>();
                    if constexpr (m == 1) r1<(ks + 2) % 20>();
                    if constexpr (m == 3 && WL) rw<(ks + 2) % 20>();
                }
                if constexpr (H6) hook(IC(kk * 6 + m));
                else { hook(IC(kk * 7 + (m < 5 ? m : 6))); if constexpr (m == 4) hook(IC(kk * 7 + 5)); }
                SCHED0();
            });
        });
    }
    template <int ID0, bool WL, int F0, int F1, int PF, class HOOK> DEVI void go(const bf16x16 (&W)[52], HOOK hook) {
        sfor<F1 - F0>([&](auto FF) {
            constexpr int f = F0 + CV(FF), ks = f / 6, m = f % 6;
            mm<ID0, WL, ks, m>(W);
            if constexpr (m == 5 && f + 1 < F1) { pre<WL, (ks + 1) % 20>(); SCHED0(); }
            if constexpr (ks + 2 < PF) rd<WL, (f + 12) % 120>();
            hook(IC(f));
            SCHED0();
        });
    }
    template <int P> DEVI void home() {
        if constexpr (P == 0) asm volatile("" : "+{v[12:15]}"(a0[0]), "+{v[16:19]}"(a0[1]), "+{v[20:23]}"(a0[2]), "+{v[24:27]}"(a1[0]), "+{v[28:31]}"(a1[1]), "+{v[32:35]}"(a1[2]));
        else asm volatile("" : "+{v[36:39]}"(a0[0]), "+{v[40:43]}"(a0[1]), "+{v[44:47]}"(a0[2]), "+{v[48:51]}"(a1[0]), "+{v[52:55]}"(a1[1]), "+{v[56:59]}"(a1[2]));
    }
    template <bool WL, int F> DEVI void rd() {
        if constexpr (F % 6 == 0) r0<F / 6>();
        if constexpr (F % 6 == 1) r1<F / 6>();
        if constexpr (F % 6 == 3 && WL) rw<F / 6>();
    }
    u32x2 o;
    template <int I, int OFF> DEVI void out4(unsigned d0) {
        if constexpr (I >= 0 && I < 24) {
            constexpr int t = I / 8, rt = (I / 4) & 1, j = I & 1;
            const f32x4& a = rt ? a1[t] : a0[t];
            if constexpr (I % 4 < 2) { o[j] = cvtpk(a[2 * j], a[2 * j + 1]); PIN(o[j]); }
            else lw32(d0 + OFF + rt * 6144 + t * 1024 + j * 4, o[j]);
        }
    }
    template <int I, int OFF> DEVI void out(unsigned d0, unsigned d1) {
        if constexpr (I >= 0 && I < 18) {
            constexpr int t = I / 6, rt = (I / 3) & 1;
            const f32x4& a = rt ? a1[t] : a0[t];
            if constexpr (I % 3 == 0) { o[0] = cvtpk(a[0], a[1]); PIN(o[0]); }
            else if constexpr (I % 3 == 1) { o[1] = cvtpk(a[2], a[3]); PIN(o[1]); }
            else if constexpr (OFF > 0) lw64(d0 + (OFF - 1) + rt * 6144 + t * 1024, o);
            else lw64((t == 1 ? d1 : d0) + OFF + rt * 6144 + (t == 2 ? 1024 : 0), o);
        }
    }
};
#define SPIX(l) (((l) & 2) ? 0xEEEEu : 0x4444u)
template <int I> DEVI void fold_op(f32x4& a, unsigned& o) {
    if constexpr (I == 0) asm volatile("v_add_f32 %0, %0, %1" : "+v"(a[0]) : "v"(a[2]));
    if constexpr (I == 1) asm volatile("v_add_f32 %0, %0, %1" : "+v"(a[1]) : "v"(a[3]));
    if constexpr (I == 2) asm volatile("v_cvt_pk_bf16_f32 %0, %1, %2" : "=v"(o) : "v"(a[0]), "v"(a[1]));
}

template <int J> DEVI void fold2(f32x4& a, f32x4& b, unsigned (&o)[2]) {
    if constexpr (J < 2) asm volatile("v_add_f32 %0, %0, %2\n\tv_add_f32 %1, %1, %3" : "+v"(a[J]), "+v"(b[J]) : "v"(a[J + 2]), "v"(b[J + 2]));
    else asm volatile("v_cvt_pk_bf16_f32 %0, %2, %3\n\tv_cvt_pk_bf16_f32 %1, %4, %5" : "=&v"(o[0]), "=&v"(o[1]) : "v"(a[0]), "v"(a[1]), "v"(b[0]), "v"(b[1]));
}
DEVI unsigned vshl1(unsigned x, unsigned b) { unsigned d; asm volatile("v_lshl_or_b32 %0, %1, 1, %2" : "=v"(d) : "s"(x), "n"(b)); return d; }

#define ANNOUNCE(s, now, d) (((unsigned)(s) << 16) | (((unsigned)(now) + (d)) & 0xFFFFu))

struct Soft {
    unsigned long long mA, mY, mC[8];
    unsigned v0, v1;
    unsigned sg, hs;
    float a[6], m, M, t, s; u32x2 pk;
    template <int I> DEVI void mask(f32x4 (&sc)[3]) {
        if constexpr (I == 0) asm volatile("v_mov_b32 %0, 0xfe800000" : "=v"(t));
        if constexpr (I >= 1 && I < 9) { float d; asm volatile("v_cndmask_b32_e64 %0, %1, 0, %2" : "=v"(d) : "v"(t), "s"(mC[I - 1])); sc[(I - 1) >> 2][(I - 1) & 3] = d; }
    }
    template <int I> DEVI void op(const Ctx& k, f32x4 (&sc)[3], unsigned a0, unsigned a1, unsigned hv) {
#define FMA(x, y) "v_fmamk_f32 " x ", " x ", 0x3dd53b95, " y "\n\t"
#define EXPC(x) "v_exp_f32_e64 " x ", " x " clamp\n\t"
#define OP3(o, d, x, y) o " " d ", " x ", " y "\n\t"
#define SA(txt) asm volatile(txt : "+v"(a[0]), "+v"(a[1]), "+v"(a[2]), "+v"(a[3]), "+v"(m), "+v"(t), "+v"(s) : "s"(sg))
#define SB(txt) asm volatile(txt : "+v"(a[0]), "+v"(a[1]), "+v"(a[2]), "+v"(a[3]), "+v"(m), "+v"(t), "+v"(s), "+v"(a[4]), "+v"(a[5]), "+v"(M) : "s"(hs), "s"(hv))
        if constexpr (I == 0) asm volatile("v_max3_f32 %0, %1, %2, %3\n\tv_max3_f32 %0, %0, %4, %5\n\tv_max3_f32 %0, %0, %6, %7" : "=&v"(m) : "v"(sc[0][0]), "v"(sc[0][1]), "v"(sc[0][2]), "v"(sc[0][3]), "v"(sc[1][0]), "v"(sc[1][1]), "v"(sc[1][2]));
        if constexpr (I == 1) asm volatile("v_cndmask_b32_e64 %0, %2, %3, %6\n\tv_cndmask_b32_e64 %1, %4, %5, %6" : "=&v"(a[2]), "=&v"(a[3]) : "v"(sc[0][2]), "v"(sc[1][0]), "v"(sc[0][3]), "v"(sc[1][1]), "s"(mY));
        if constexpr (I == 2) asm volatile("v_max_f32 %2, %2, %8\n\tv_cndmask_b32_e64 %0, %3, %4, %7\n\tv_cndmask_b32_e64 %1, %5, %6, %7" : "=&v"(a[0]), "=&v"(a[1]), "+v"(m) : "v"(sc[0][0]), "v"(a[2]), "v"(sc[0][1]), "v"(a[3]), "s"(mA), "v"(sc[1][3]));
        if constexpr (I == 3) asm volatile("v_cndmask_b32_e64 %0, %0, %4, %6\n\tv_cndmask_b32_e64 %1, %1, %5, %6\n\tv_mul_f32 %2, 0xbdd53b95, %7" : "+v"(a[2]), "+v"(a[3]), "=&v"(t), "=v"(s) : "v"(sc[1][2]), "v"(sc[1][3]), "s"(mA), "v"(m));
        if constexpr (I == 4) SA(FMA("%0", "%5") FMA("%1", "%5"));
        if constexpr (I == 5) SA(FMA("%2", "%5") EXPC("%0"));
        if constexpr (I == 6) SA(FMA("%3", "%5") EXPC("%1"));
        if constexpr (I == 7) SA(EXPC("%2"));
        if constexpr (I == 8) SA(OP3("v_add_f32", "%6", "%0", "%1") EXPC("%3"));
        if constexpr (I == 9) SA(OP3("v_add_f32", "%5", "%2", "%3") OP3("v_add_f32", "%6", "%6", "%5") OP3("v_mul_f32", "%6", "%7", "%6"));
        if constexpr (I == 10) { asm volatile("v_max3_f32 %0, %2, %3, %4\n\tv_max3_f32 %1, %2, %3, %4" : "=&v"(M), "=&v"(t) : "v"(m), "v"(sc[2][0]), "v"(sc[2][1]), "v"(sc[2])); a[4] = sc[2][0]; a[5] = sc[2][1]; }
        if constexpr (I == 11) { LEAD(); SB("v_permlane16_swap_b32 %9, %5\n\t" OP3("v_max_f32", "%9", "%9", "%5") "v_mov_b32 %5, %9"); }
        if constexpr (I == 12) { LEAD(); SB("v_permlane32_swap_b32 %9, %5\n\t" OP3("v_max_f32", "%9", "%9", "%5") "v_mul_f32 %9, 0xbdd53b95, %9"); }
        if constexpr (I == 13) SB(FMA("%7", "%9") FMA("%8", "%9"));
        if constexpr (I == 14) SB(FMA("%4", "%9") EXPC("%7"));
        if constexpr (I == 15) SB(EXPC("%8") EXPC("%4"));
        if constexpr (I == 16) SB(OP3("v_add_f32", "%5", "%7", "%8") OP3("v_mul_f32", "%6", "%6", "%4"));
        if constexpr (I == 17) SB(OP3("v_fmac_f32", "%6", "%10", "%5") "v_mov_b32 %9, %11");
        if constexpr (I == 18) { LEAD(); SB("v_fmac_f32_dpp %6, %6, %9 row_ror:8 row_mask:0xf bank_mask:0xf\n\tv_mov_b32 %5, %6"); }
        if constexpr (I == 19) { LEAD(); SB("v_permlane16_swap_b32 %6, %5\n\t" OP3("v_add_f32", "%6", "%6", "%5") "v_mov_b32 %5, %6"); }
        if constexpr (I == 20) { LEAD(); SB("v_permlane32_swap_b32 %6, %5\n\t" OP3("v_add_f32", "%6", "%6", "%5") "v_rcp_f32 %6, %6"); }
        if constexpr (I == 21) SB(OP3("v_mul_f32", "%4", "%4", "%6") OP3("v_mul_f32", "%0", "%0", "%4") OP3("v_mul_f32", "%1", "%1", "%4"));
        if constexpr (I == 22) SB(OP3("v_mul_f32", "%2", "%2", "%4") OP3("v_mul_f32", "%3", "%3", "%4"));
        if constexpr (I == 23) { unsigned x, y; asm volatile("v_cvt_pk_bf16_f32 %0, %3, %4\n\tv_cvt_pk_bf16_f32 %1, %5, %6\n\tv_mul_f32 %2, %2, %7" : "=&v"(x), "=&v"(y), "+v"(a[4]) : "v"(a[0]), "v"(a[1]), "v"(a[2]), "v"(a[3]), "v"(s)); pk = (u32x2){x, y}; }
        if constexpr (I == 24) { st64(k.rs, v0, a0, pk); SCHED0(); asm volatile("v_mul_f32 %1, %1, %2\n\tv_cvt_pk_bf16_f32 %0, %0, %1" : "+v"(a[4]), "+v"(a[5]) : "v"(s)); }
        if constexpr (I == 25) st32(k.rs, v1, a1, __float_as_uint(a[4]));
    }
};
#define QIDX(t, b) ((t) * 10 + (b))
DEVI void qk_role(const Params& p, const Ctx& k, char* smem, int h) {
    PROF_DECL
    const int w = k.w;
    sleep_ticks(k.c ? WD1 + QDLY : QDLY);
    bf16x16 W[52];
    {
        const int l = k.l;
        bf16x8 lo[6], wt[16];
        const int col = h * 192 + w * 48;
        wstream<20, 6, 6>(smem + w * 36864,
            [&](auto U, unsigned sl) { constexpr int u = CV(U);
                wp_issue<3, (u % 6) * 6144>(p.Wk + (size_t)u * 32 * 6144 + col, 6144, sl, l); wp_issue<3, (u % 6) * 6144 + 3072>(p.Wq + (size_t)u * 32 * 6144 + col, 6144, sl, l); },
            [&](auto U, char* sp) { constexpr int u = CV(U), b = u >> 1;
#pragma unroll
                for (int t = 0; t < 6; ++t) {
                    bf16x8 f = wp_read<3, true>(sp + (t / 3) * 3072, t % 3, l);
                    if (t == 5 && b >= 2) wt[(u - 4) & 15] = f;
                    else if (u & 1) W[QIDX(t, b) % 52] = CAT8(lo[t], f);
                    else lo[t] = f;
                } });
        RAWBAR()
#pragma unroll
        for (int i = 0; i < 16; ++i) *(bf16x8*)(smem + L_W + w * 16384 + i * 1024 + l * 16) = wt[i];
    }
    RSTAMP(2)
    Gather G;
    Soft S;
    const int hw = h >> 3, hh = h & 7;
    unsigned l16, ix, dqm, dq, trp, qp, vz;
    {
        const int l = k.l, g = l >> 4, n = l & 15, r = n & 7, u = w & 1;
        const bool hf = n >= 8, dup = w != 1;
        S.mA = __ballot(dup && hf);
        const int lim = (w == 0 ? 64 : w == 1 ? n : 16 + 8 * u + r) - 4 * g;
#pragma unroll
        for (int i = 0; i < 8; ++i) S.mC[i] = __ballot((i & 3) + (i >> 2) * 16 <= lim);
        const bool okm = !(dup && hf);
        S.mY = __ballot((dup && hf) == (w != 0));
        S.v0 = w == 0 ? hh * 512 + ((g >> 1) * 16 + (r >> 1) * 4 + hf * 2 + (r & 1)) * 16 + (g & 1) * 8 : w == 1 ? (hh >> 1) * 1024 + l * 16 + (hh & 1) * 8 : hh * 1024 + (g * 16 + 8 * u + r) * 16 + hf * 8;
        S.v1 = !okm ? OOB : w == 0 ? 4096 + hh * 128 + r * 16 + g * 4 : (w == 1 ? 4096 + n * 16 : 6144 + (8 * u + r) * 16) + (hh >> 1) * 512 + (g >> 1) * 256 + (hh & 1) * 8 + (g & 1) * 4;
        l16 = l * 16; ix = SPIX(l);
        const int uu = u ^ ((n >> 2) & 1);
        dqm = L_QM + (w >> 1) * 3072 + uu * 512 + (g >> 1) * 128 + n * 8 + (g & 1) * 4;
        dq = L_QM + 6144 + (w >> 1) * 3072 + uu * 512 + g * 128 + ((n * 8) ^ ((g >> 1) * 64));
        trp = L_QM + 6144 + (l & 3) * 128 + (((l >> 2) * 8) ^ ((l & 2) * 32));
        qp = w == 1 ? trp + 6144 : (w == 0 ? L_QM : L_Q + 6144 + u * 256) + (l & 1) * 128 + (((l >> 2) * 8) ^ (w == 3 ? 64 : 0));
        vz = l == 0 && w == 0 ? 0u : OOB;
        u32x8 rg = {S.v0, S.v1, vz, ix, dqm, dq, trp, qp};
        asm volatile("" : "+{v[0:7]}"(rg), "+{v8}"(l16));
        S.v0 = rg[0]; S.v1 = rg[1]; vz = rg[2]; ix = rg[3]; dqm = rg[4]; dq = rg[5]; trp = rg[6]; qp = rg[7];
    }
    const unsigned po0 = k.p0 + PR_B + hw * 49152 + (w == 0 ? 0 : w == 1 ? 32768 : 16384), po1 = k.p0 + PR_B + hw * 49152 + (w == 0 ? 0 : 32768);
    const unsigned pdum = k.pg(1) + DUMMY_B, pbc = k.p0 + CNT(C_BC, hw * 16 + hh);
    Xn X;
    X.init(k); G.init(k);
    X.l16 = l16; X.se = 0;
    unsigned long long now = 0, done = 0;
    unsigned a0 = pdum + (po0 - k.p0 - PR_B), a1 = pdum + (po1 - k.p0 - PR_B);
    const unsigned hv = w == 1 ? 0u : 0x3F800000u, hw2 = w == 1 ? 0u : 0x00800000u;
    S.sg = 0x3F800000u; S.hs = 0x3F800000u;
    G3 A, B;
    A.l16 = l16; B.l16 = l16; B.lwv = vadd(l16, L_W + w * 16384);
#pragma unroll
    for (int t = 0; t < 3; ++t) { B.a0[t] = (f32x4){0.f, 0.f, 0.f, 0.f}; B.a1[t] = B.a0[t]; }
    f32x4 ac[6];
#pragma unroll
    for (int t = 0; t < 6; ++t) clr2(ac[t]);
    sfor<9>([&](auto I) { G.op<CV(I)>(k, l16, 0); });
    G.bad = 0;
    unsigned an = 0;
    do {
        if (X.off != 0) {
            PC8(1, X.sg)
            X.late<2>(k, l16);
            G.check();
        }
_Pragma("unroll 1")
        for (unsigned spins = 0; G.bad != 0 && !spin_fail(k, spins); ++spins) { PC8(0, X.sg) G.again(k, l16); }
        HSTOP(0) STAMP(0, 0)
        clr2(ac[5]);
        asm volatile("s_nop 1" : "+v"(ac[5]));
        smfw<50>(ac[5], __builtin_bit_cast(bf16x8, G.p0), W[50], ix);
    do {
        PROF_S(X.sg - 1)
        f32x4 sc[3];
        {
            bf16x8 m[16], wa[10], wb[10], kf[12];
            bf16x4 q[12], km[12];
            unsigned fo[6];
            m[0] = __builtin_bit_cast(bf16x8, G.p0); m[1] = __builtin_bit_cast(bf16x8, G.p1);
            auto put = [&](auto T) { constexpr int t = CV(T); if constexpr (t < 3) lw32(dq + t * 1024, fo[t]); else lw32(dqm + (t - 3) * 1024, fo[t]); };
            auto rd = [&](auto C) { constexpr int c = CV(C), j = c % 20;
                if constexpr (c < 0) { }
                else if constexpr (c < 20) m[j] = lr128(l16 + MADR(j < 10 ? j : j - 8));
                else if constexpr (c < 40) wa[j] = lr128(B.lwv + (j - 2) * 2048);
                else if constexpr (c < 60) wb[j] = lr128(B.lwv + (j - 2) * 2048 + 1024);
                else if constexpr (c < 80) kf[j] = lr128(l16 + L_K + ((j & 1) * 6 + j / 2) * 1024);
                else if constexpr (c < 100) q[j] = lrtr(qp + j * 512);
                else if constexpr (c < 120) km[j] = lrtr(trp + j * 512);
                else if constexpr (c < 140) A.r0<j>();
                else A.r1<j>(); };
#define PB(i) ((i) < 12 ? (i) / 6 : (i) < 32 ? (i) / 4 - 1 : (i) < 44 ? 7 + ((i) - 32) % 3 : (i) < 54 ? 7 + ((i) - 44) / 2 : 12 + ((i) - 54) % 3)
#define PT(i) ((i) < 12 ? ((i) + 5) % 6 : (i) < 32 ? (3 + (i) % 4) % 6 : (i) < 44 ? (3 + ((i) - 32) / 3) % 6 : (i) < 54 ? 1 + ((i) & 1) : 1 + ((i) - 54) / 3)
            sfor<82>([&](auto I) {
                constexpr int i = CV(I) + 1, b = PB(i), t = PT(i), bn = PB(i + 1);
                constexpr int rt[83] = {-1, 2, -1, -1, -1, 22, 42, 3, -1, 23, 43, 4, -1, 24, 44, 5, -1, 25, 45, 6,
                    -1, 26, 46, 7, -1, 8, 9, 27, 47, 28, 48, 29, 49, -1, -1, 60, -1, 61, 62, 63,
                    -1, 10, 64, -1, 11, 12, -1, 13, 14, -1, -1, 80, 82, 83, 84, 85, 86, 87, 67, 88,
                    89, 69, -1, 91, -1, 71, -1, 121, -1, 100, 102, 104, 106, 108, 110, -1, 140, -1, -1, 141, -1, -1, 122};
                constexpr int r2[83] = {-1, -1, -1, -1, -1, -1, -1, -1, -1, -1, -1, -1, -1, -1, -1, -1, -1, -1, -1, -1,
                    -1, -1, -1, -1, -1, -1, -1, -1, -1, -1, -1, -1, -1, -1, -1, -1, -1, -1, -1, -1,
                    -1, -1, -1, -1, -1, -1, -1, -1, -1, -1, -1, 81, -1, 65, -1, 66, -1, -1, -1, -1,
                    68, 90, -1, 70, -1, 120, -1, -1, -1, 101, 103, 105, 107, 109, 111, -1, -1, -1, -1, -1, -1, -1, -1};
                if constexpr (i < 60) {
                    if constexpr (t == 5 && b >= 2) smf<false>(ac[5], m[b], CAT8(wa[b], wb[b]), ix);
                    else smfw<QIDX(t, b < 10 ? b : b - 8) % 52>(ac[t], m[b], W[QIDX(t, b < 10 ? b : b - 8) % 52], ix);
                } else if constexpr (i < 72) { constexpr int j = i - 60; mfa<false, false>(sc[j & 1], kf[j], CAT4(q[j & ~1], q[j | 1])); }
                else if constexpr (i < 77) A.mm<0, false, (i - 72) / 3, (i - 72) % 3>(W);
                else { constexpr int ks = i - 77; mfa<false, ks == 0>(sc[2], CAT4(km[2 * ks], km[2 * ks + 1]), CAT4(q[2 * ks], q[2 * ks + 1])); }
                SCHED0();
                if constexpr (i == 1) { AQ(3, 3) asm volatile("s_barrier" ::: "memory"); AQ(1, 2) AQ(3, 4) }
                else if constexpr (i == 50) { BARL(3); AQ(1, 3) }
                else if constexpr (i == 68) { BARL(3); AQ(1, 4) QSTAMP(0, 1) }
                else if constexpr (i >= 12 && i < 59) { if constexpr (PT(i + 1) == 5) TOUCH(wb[bn % 10]); TOUCH(m[bn]); }
                else if constexpr (i >= 59 && i < 71) { asm volatile("" :: "v"(kf[(i - 59) % 12]), "v"(q[((i - 59) | 1) % 12])); }
                else if constexpr (i == 71) TOUCH(A.x0[0]);
                else if constexpr (i == 74) TOUCH(A.x0[1]);
                else if constexpr (i >= 76 && i < 82) TOUCH(km[(2 * (i - 76) + 1) % 12]);
                if constexpr (i == 8 || i == 30) asm volatile("" :: "{v[60:63]}"(ac[0]), "{v[28:31]}"(ac[1]), "{v[32:35]}"(ac[2]), "{v[64:67]}"(ac[3]), "{v[68:71]}"(ac[4]), "{v[72:75]}"(ac[5]));
                if constexpr (i == 14) AC3(0, 0, 0)
                if constexpr (i == 16) AC3(1, 6, 1)
                if constexpr (i == 18) AC3(2, 7, 2)
                if constexpr (i == 20) AC3(-1, -1, 3)
                if constexpr (i == 22) AC3(-1, -1, 4)
                if constexpr (i == 24) AC3(-1, -1, 5)
                if constexpr (i == 66) AC3(3, -1, 6)
                if constexpr (i == 13) { now = rt_async(); HSTOP(1) }
                if constexpr (i == APOS) { PINS(now); an += (unsigned)now; PINS(an); }
                if constexpr (i == APOS + 1) an = spack(an, X.sg);
                SCHED0();
                rd(IC(rt[i])); rd(IC(r2[i]));
                if constexpr (i == APOS + 3) st32(k.rs, vz, pbc, fo[1]);
                if constexpr (i >= 40 && i < 50 && i % 3 == 1) put(IC(((i - 31) / 3) % 6));
                if constexpr (i == 62 || i == 64) put(IC(i / 2 - 30));
                SCHED0();
                if constexpr (i < 25 && i % 4 != 1) B.out<((i - 2) / 4) * 3 + (i - 2) % 4, 6145>(dq, dq);
                if constexpr (i == APOS + 2) { fo[1] = an; PIN(fo[1]); }
                if constexpr (i >= 37 && i < 49) { constexpr int tt = ((i - 28) / 3) % 6; fold_op<(i - 37) % 3>(ac[tt], fo[tt]); }
                if constexpr (i >= 59 && i < 62) fold_op<i - 59>(ac[1], fo[1]);
                if constexpr (i == 62) fold_op<0>(ac[2], fo[2]);
                if constexpr (i == 63) asm volatile("v_add_f32 %1, %1, %2\n\tv_cvt_pk_bf16_f32 %0, %3, %1" : "=v"(fo[2]), "+v"(ac[2][1]) : "v"(ac[2][3]), "v"(ac[2][0]));
                if constexpr (i == 33) S.mask<0>(sc);
                if constexpr (i >= 51 && i < 59) S.mask<i - 50>(sc);
                if constexpr (i >= 75) S.op<i - 75>(k, sc, a0, a1, hv);
                if constexpr (i == 12) { AQ(3, 5) }
                if constexpr (i == 43) { AQ(3, 6) }
                if constexpr (i == 59) { AQ(3, 7) }
                SCHED0();
            });
        }
        QSTAMP(0, 2)
        {
            unsigned d0;
            auto rest = [&](auto J) { constexpr int j = CV(J);
                if constexpr (j >= 0 && j < 18 && (j % 6 == 0 || j % 3 == 2)) X.op<2 + j / 6, (j % 6 + 1) / 3>(k); };
            sfor<25>([&](auto F) { constexpr int f = CV(F) + 5;
                constexpr int e = f < 6 ? f + 3 : f < 9 ? f - 3 : f;
                A.mm<0, false, e / 6, e % 6>(W);
                if constexpr (f % 6 == 2 || f == 5) TOUCH(A.x1[f / 6]);
                else if constexpr (f % 6 == 5) TOUCH(A.x0[f / 6 + 1]);
                if constexpr (f == 24) { done = rt_async(); QSTAMP(0, 3) AQ(1, 5) }
                if constexpr (f == 28) AC3(4, -1, 7)
                SCHED0();
                if constexpr ((f % 6 == 4 && f < 28) || f == 26) A.r0<f / 6 + 2>();
                if constexpr (f % 6 == 1 || f == 27) A.r1<(f + 3) / 6 + 1>();
                SCHED0();
                S.op<f + 3>(k, sc, a0, a1, hv);
                rest(IC(f - 23));
                SCHED0(); });
            A.go<0, false, 30, 48, 8>(W, [&](auto F) { constexpr int f = CV(F);
                if constexpr (f >= 36) B.rd<true, f - 36>();
                rest(IC(f - 23));
                if constexpr (f == 44) B.lwv = vadd(l16, L_W + w * 16384);
                if constexpr (f == 45) AC3(5, -1, -1)
                if constexpr (f == 47) { B.pre<true, 0>(); AQ(1, 6) AQ(2, 1) } });
            B.go<30, true, 0, 48, 8>(W, [&](auto F) { constexpr int f = CV(F);
                if constexpr (f == 2) { a0 = (X.sg & 3) * PR_SZ; PINS(a0); }
                if constexpr (f == 4) { a1 = a0 + po1; a0 += po0; PINS(a0); PINS(a1); }
                if constexpr (f == 8) { S.sg = (X.sg & 4) ? 0xBF800000u : 0x3F800000u; PINS(S.sg); }
                if constexpr (f == 10) { S.hs = S.sg - hw2; PINS(S.hs); }
                if constexpr (f == 14 || f == 16) G.op<f / 16>(k, l16, X.sg);
                if constexpr (f == 22 || f == 28 || f == 32) X.next<(f - 22) / 5>(k, l16);
                if constexpr (f == 35) asm volatile("s_waitcnt vmcnt(4)\n\ts_barrier" ::: "memory");
                if constexpr (f == 20) AC3(6, 1, -1)
                if constexpr (f == 26) A.home<0>();
                if constexpr (f >= 36) A.rd<false, f + 12>();
                if constexpr (f == 38 || f == 40 || f == 45 || f == 46) X.next<(f == 38 ? 5 : f == 40 ? 6 : f - 42)>(k, l16);
                if constexpr (f == 47) { A.pre<false, 8>(); AQ(1, 7) AQ(2, 2) } });
            A.go<0, false, 48, 120, 20>(W, [&](auto F) { constexpr int f = CV(F), i = f - XPOS;
                if constexpr (f == 56) B.home<1>();
                if constexpr (i == 0 || i == 2 || i == 6) X.next<7 + i / 2>(k, l16);
                if constexpr (i >= 3 && i < 15 && ((i - 3) % 6 == 0 || (i - 3) % 3 == 2)) X.op<(i - 3) / 6, ((i - 3) % 6 + 1) / 3>(k);
                if constexpr (f == 58) asm volatile("s_waitcnt vmcnt(1)\n\ts_barrier" ::: "memory");
                if constexpr (f == 57) AC3(7, 2, -1)
                if constexpr (f >= 108) B.rd<true, f - 60>();
                if constexpr (f == 116) asm volatile("v_bfe_u32 %0, %1, 8, 1\n\tv_lshl_or_b32 %0, %0, 3, %1" : "=&v"(d0) : "v"(l16));
                if constexpr (f == 118) asm volatile("v_xor_b32 %0, %1, %0" : "+v"(d0) : "s"(L_K + (w >> 1) * 3072 + (w & 1) * 8));
                if constexpr (f == 119) { B.pre<true, 8>(); AQ(2, 3) } });
            B.go<30, true, 48, 120, 20>(W, [&](auto F) { constexpr int f = CV(F), i = f - 48;
                constexpr int ko[26] = {0, 1, 2, 4, 3, 6, 5, 8, 7, 9, 10, 11, 12, 13, 14, 16, 15, 18, 17, 20, 19, 21, 22, 23, -1, -1};
                if constexpr (i < 24) { SCHED0(); A.out4<ko[i % 26], 0>(d0); }
                G.at<f - GPOS>(k, l16);
                if constexpr (f == 114) lgkm0(done);
                if constexpr (f == 115) { PINS(now); an = (unsigned)done - (unsigned)now; PINS(an); }
                if constexpr (f == 116) { an = min(an, 200u); PINS(an); }
                if constexpr (f == 117) { an += DQ; PINS(an); }
                SCHED0();
                if constexpr (i == 28) AC3(-1, 3, -1)
                if constexpr (i == 50) AC3(-1, 4, -1)
                if constexpr (i == 62) AC3(-1, 5, -1)
                if constexpr (i == 23) { AQ(2, 4) }
                if constexpr (i == 47) { AQ(2, 5) }
                if constexpr (i == 65) { AQ(2, 6) }
                if constexpr (f == 119) { AQ(2, 7) }
                if constexpr (f == 112) asm volatile("" :: "{v[12:15]}"(G.v0), "{v[16:19]}"(G.v1), "{v[20:23]}"(G.p0), "{v[24:27]}"(G.p1), "{v9}"(B.lwv), "{v8}"(l16));
                if constexpr (f == 119) G.also(X.off);
                if constexpr (f >= 113 && f < 119) clr2(ac[(f + 6 - 113 + 5) % 6]); });
            static_assert(GPOS >= 70 && GPOS <= 98 && XPOS % 6 == 2, "GPOS");
            QSTAMP(0, 4) QSTAMP(0, 5)
            PFLUSH(s)
        }
        AST(0)
        PROF_ONLY(G.wait<0>();)
        HSTOP(0) STAMP(s, 0) AQ(1, 1) AQ(3, 1)
        G.all();
        SCHED0();
        smfw<50>(ac[5], __builtin_bit_cast(bf16x8, G.p0), W[50], ix);
        AQ(3, 2)
        SCHED0();
    } while (G.bad == 0);
    } while (X.sg != 65u);
    PROF_OUT
}

#define OIDX(t, b) (22 + (t) * 3 + (b))
DEVI void vo_role(const Params& p, const Ctx& k, char* smem, int h) {
    PROF_DECL
    const int w = k.w;
    if (k.c) sleep_ticks(WD1);
    bf16x16 W[52];
    {
        const int l = k.l;
        bf16x8 lo[10], wt[16];
        const u16* wv = p.Wv + h * 192 + w * 48;
        const u16* wo = p.Wo + (size_t)h * 192 * 640 + w * 160;
        wstream<22, 6, 6>(smem + w * 36864,
            [&](auto U, unsigned sl) { constexpr int u = CV(U);
                if constexpr (u >= 12) { wp_issue<3, (u % 6) * 6144>(wv + (size_t)(u - 12) * 64 * 6144, 6144, sl, l); wp_issue<3, (u % 6) * 6144 + 3072>(wv + (size_t)((u - 12) * 64 + 32) * 6144, 6144, sl, l); }
                else { wp_issue<5, (u % 6) * 6144>(wo + (u >> 1) * 32 * 640 + (u & 1) * 80, 640, sl, l); dma1<(u % 6) * 6144 + 5120>(wo, l * 16, sl); } },
            [&](auto U, char* sp) { constexpr int u = CV(U);
                if constexpr (u >= 12) {
                    constexpr int b = u - 12;
#pragma unroll
                    for (int t = 0; t < 3; ++t) {
                        bf16x8 x = wp_read<3, true>(sp, t, l), y = wp_read<3, true>(sp + 3072, t, l);
                        if (t == 2 && b >= 2) { wt[(2 * (b - 2)) & 15] = x; wt[(2 * (b - 2) + 1) & 15] = y; } else W[(t * 10 + b) % 52] = CAT8(x, y);
                    }
                } else {
                    constexpr int ks = u >> 1, b = ks >> 1;
#pragma unroll
                    for (int i = 0; i < 5; ++i) {
                        const int t = (u & 1) * 5 + i;
                        bf16x8 f = wp_read<5, false>(sp, i, l);
                        if (ks & 1) W[OIDX(t, b)] = CAT8(lo[t], f); else lo[t] = f;
                    }
                } });
        RAWBAR()
#pragma unroll
        for (int i = 0; i < 16; ++i) *(bf16x8*)(smem + L_W + w * 16384 + i * 1024 + l * 16) = wt[i];
    }
    RSTAMP(2)
    unsigned l16, lwv, ix, dq0, trp, vo, vsig;
    {
        const int l = k.l, g = l >> 4, n = l & 15, q = (l >> 2) & 3, pp = l & 3;
        l16 = l * 16; lwv = l16 + L_W + w * 16384; ix = SPIX(l);
        dq0 = L_QM + (24 * w + (n >> 2) * 2 + (n & 1)) * 32 + g * 8 + ((n >> 1) & 1) * 4;
        trp = L_QM + (16 * (g >> 1) + 4 * (g & 1) + 8 * (q >> 1) + (q & 1)) * 32 + pp * 8;
        vo = EXP(h * 512) + (g >> 1) * 256 + n * 16 + (g & 1) * 8;
        vsig = l == 0 && w == 0 ? 0u : OOB;
        u32x8 r = {dq0, trp, vo, vsig, l16, lwv, ix, 0u};
        PIN(r);
        dq0 = r[0]; trp = r[1]; vo = r[2]; vsig = r[3]; l16 = r[4]; lwv = r[5]; ix = r[6];
    }
    const unsigned pdum = k.pg(1) + DUMMY_B, pbc = k.p0 + CNT(C_BC, (h >> 3) * 16 + 8 + (h & 7));
    Gather G;
    G3 V;
    V.l16 = l16; V.lwv = lwv;
    unsigned long long now = 0, done = 0;
    f32x4 y[20];
    bf16x8 v[12];
    u32x4 o[10];
    o[7] = (u32x4){0u, 0u, 0u, 0u};
    clr2(y[17]);
#pragma unroll
    for (int i = 0; i < 12; ++i) v[i] = __builtin_bit_cast(bf16x8, o[7]);
    unsigned yb = k.yx(7, w) + EXP(h * 1024);
    G.mx = ~0u;
    Xn X;
    X.init(k); G.init(k);
    X.l16 = l16; X.se = 0;
    G.base -= 32768;
    const unsigned ypar = (unsigned)(k.par >> (PG_YX + (w >> 1))), yo = ((unsigned)(PG_YX + (w >> 1)) << 21) + (w & 1) * 1048576 + EXP(h * 1024);
    const unsigned ym0 = k.ym(0) + w * 163840, ym2 = k.ym(2) + w * 163840;
    unsigned ym = pdum + w * 163840, ys, t, c = ~0u, an = 0;
#define YT(u) ((u) < 10 ? 2 * ((u) % 5) : ((u) - 10) | 1)
#define YR(u) ((u) < 10 ? (u) / 5 : (u) & 1)
    auto yout = [&](auto U, auto J) { constexpr int u = CV(U), j = CV(J), t = YT(u), rt = YR(u);
        if constexpr (j == 0) { o[t][2 * rt] = cvtpk(y[u][0], y[u][1]); o[t][2 * rt + 1] = cvtpk(y[u][2], y[u][3]); TOUCH(o[t][2 * rt]); TOUCH(o[t][2 * rt + 1]); }
        if constexpr (j == 1 && rt) __builtin_amdgcn_raw_buffer_store_b128(o[t], k.rs, (int)l16, (int)yb, AUX_SC1);
        if constexpr (j == 2 && rt) { yb += t == 8 ? -7 * 65536 : 131072; PINS(yb); TOUCH(o[t]); } };
    auto ymm = [&](auto I) {
        constexpr int i = CV(I), ks = i % 6, u = i / 6, id = OIDX(YT(u), ks >> 1);
        mfw<id, ks & 1, true, ks == 0>(y[u], W[id], v[YR(u) * 6 + ks]);
        SCHED0();
        if constexpr (u > 0 && ks >= 2) yout(IC((u + 19) % 20), IC(ks - 2)); };
    do {
        PC8(1, c)
        X.late<5>(k, l16);
    do {
        PROF_S(X.sg - 1)
        f32x4 ac[3];
        asm volatile("s_waitcnt vmcnt(7)\n\ts_barrier" ::: "memory");
        SCHED0();
        sfor<13>([&](auto I) { constexpr int i = CV(I) + 107;
            ymm(IC(i));
            if constexpr (i == 107) { AST(5) st32(k.rs, vsig, pbc + 4, G.mx); }
            if constexpr (i == 119) V.pre<true, 0>();
            if constexpr (i == 108) V.r0<0>();
            if constexpr (i == 109) V.r1<0>();
            if constexpr (i == 114) V.r0<1>();
            if constexpr (i == 115) V.r1<1>();
            SCHED0(); });
        STAMP(s, 4)
        V.run<0, true, 0, 16, 17, true>(W, [&](auto I) { constexpr int i = CV(I);
            if constexpr (i >= 2) yout(IC(19), IC(i - 2));
            if constexpr (i == 8) { t = (X.sg & 7) * 2; PINS(t); }
            if constexpr (i == 10) { yb = (ypar >> t) & 1; PINS(yb); }
            if constexpr (i == 14) { t = (t << 21) + yo; PINS(t); }
            if constexpr (i == 16) { yb = (yb << 13) + t; PINS(yb); }
            if constexpr (i == 20) { c = X.sg; PINS(c); }
            if constexpr (i == 22) { G.a = max(c, 1u) << 15; PINS(G.a); }
            if constexpr (i == 28) G.op<1>(k, l16, 0);
            if constexpr (i == 40) { an = min(an, 200u) + DV; PINS(an); }
            if constexpr (i == 32 || i == 34 || i == 38) X.next<(i - 31) / 3>(k, l16);
            if constexpr (i == 23 || i == 29) { AAC(4 + i / 29) }
            if constexpr (i == 83 || i == 89) { AAC(6 + i / 89) }
            if constexpr (i == YPOS) { AST(6) }
            if constexpr (i == YPOS + 1) { AST(7) }
            if constexpr (i == YPOS) { asm volatile("s_waitcnt vmcnt(0)\n\ts_barrier" ::: "memory"); }
            if constexpr (i == YPOS - 2) G.mx = vshl1(c, 0);
            if constexpr (i == YPOS + 1) st32(k.rs, vsig, pbc + 4, G.mx);
            G.at<i - GPOSY * 6>(k, l16);
            if constexpr (i == 95) V.pre<true, 16>();
            if constexpr (i >= 92 && i < 95) clr2(ac[(i - 92) % 3]); });
        static_assert(GPOSY * 6 + 2 > YPOS + 1 && GPOSY <= 13, "GPOSY");
        STAMP(s, 5)
        PFLUSH(s - 1)
        f32x4 acc[10];
        f32x2 z[20];
        auto clr = [&](auto I) { constexpr int i = CV(I); asm volatile("v_mov_b64 %0, 0" : "=v"(z[i])); if constexpr (i & 1) acc[i >> 1] = __builtin_shufflevector(z[i - 1], z[i], 0, 1, 2, 3); };
        bf16x4 al[3], ah[3];
        unsigned d0, d1;
        {
            bf16x8 m[10], wa[10], wb[10];
            auto rd = [&](auto R) { constexpr int r = CV(R), b = 2 + r / 3;
                if constexpr (b < 10) {
                    if constexpr (r % 3 == 0) m[b] = lr128(l16 + MADR(b));
                    if constexpr (r % 3 == 1) wa[b] = lr128(lwv + (b - 2) * 2048);
                    if constexpr (r % 3 == 2) wb[b] = lr128(lwv + (b - 2) * 2048 + 1024);
                } else if constexpr (b < 13) {
                    if constexpr (r % 3 == 0) V.r0<b + 7>();
                    if constexpr (r % 3 == 1) V.rw<b + 7>();
                    if constexpr (r % 3 == 2) V.r1<b + 7>();
                } };
            AST(0)
            G.wait<0>();
            HSTOP(0) STAMP(s, 0) AST(1)
            V.run<0, true, 16, 17, 0, true>(W, [&](auto I) { constexpr int i = CV(I);
                G.fin<i>(0);
                if constexpr (i == 3) {
                    if (__builtin_expect(G.bad != 0, 0)) {
                        unsigned spins = 0;
_Pragma("unroll 1")
                        do { PC8(0, c) G.again(k, l16); } while (G.bad != 0 && !spin_fail(k, spins++));
                        HSTOP(0) STAMP(s, 0)
                    }
                    asm volatile("s_barrier" ::: "memory");
                    AST(2)
                }
                if constexpr (i >= 3) rd(IC(i - 3)); });
            m[0] = __builtin_bit_cast(bf16x8, G.p0); m[1] = __builtin_bit_cast(bf16x8, G.p1);
            sfor<30>([&](auto I) {
                constexpr int i = CV(I), b = i / 3, t = i % 3;
                if constexpr (t == 2 && b >= 2) smf<false>(ac[2], m[b], CAT8(wa[b], wb[b]), ix); else smfw<t * 10 + b>(ac[t], m[b], W[t * 10 + b], ix);
                SCHED0();
                if constexpr (i >= 6 && i < 29 && t != 0) { if constexpr (t == 1) TOUCH(wb[b]); else TOUCH(m[(b + 1) % 10]); }
                if constexpr (i == 29) V.pre<true, 17>();
                if constexpr (i == 6) { now = rt_async(); HSTOP(1) }
                if constexpr (i == 21) { PINS(now); an += (unsigned)now; PINS(an); }
                if constexpr (i == 24) an = spack(an, c);
                SCHED0();
                if constexpr (i < 27) rd(IC(3 + i));
                if constexpr (i == 27) st32(k.rs, vsig, pbc, d0);
                SCHED0();
                if constexpr (i == 25) { d0 = an; PIN(d0); }
                if constexpr (i == 26) X.next<3>(k, l16);
                if constexpr (i == 28) d0 = vadd(l16, L_K + ((3 * w) >> 1) * 1024 + ((3 * w) & 1) * 8);
                if constexpr (i == 29) d1 = vadd(l16, L_K + ((3 * w + 1) >> 1) * 1024 + ((3 * w + 1) & 1) * 8);
                SCHED0();
            });
            unsigned fo[3];
            V.run<0, true, 17, 19, 0, true>(W, [&](auto I) { constexpr int i = CV(I);
                if constexpr (i < 3) { rd(IC(30 + i)); SCHED0(); fold_op<0>(ac[i], fo[i]); }
                if constexpr (i == 3 || i == 4 || i == 6) { constexpr int t = i / 2 - 1; asm volatile("v_add_f32 %1, %1, %2\n\tv_cvt_pk_bf16_f32 %0, %3, %1" : "=v"(fo[t]), "+v"(ac[t][1]) : "v"(ac[t][3]), "v"(ac[t][0])); }
                if constexpr (i == 5 || i == 7 || i == 8) lw32(dq0 + ((i - 4) / 2) * 256, fo[(i - 4) / 2]);
                if constexpr (i == 9 || i == 10) clr2(acc[i - 9]);
                if constexpr (i == 11) BARL(0) });
        }
        STAMP(s, 1) AST(3)
        {
            unsigned fo[2];
            auto fin2 = [&](auto P, auto J) { fold2<CV(J)>(acc[2 * CV(P)], acc[2 * CV(P) + 1], fo); };
            V.run<0, true, 19, 20, 0, true>(W, [&](auto I) { constexpr int i = CV(I);
                if constexpr (i == 0) { al[0] = lrtr(trp); ah[0] = lrtr(trp + 64); }
                if constexpr (i == 1) al[1] = lrtr(trp + 1024);
                if constexpr (i == 2) ah[1] = lrtr(trp + 1088);
                if constexpr (i == 3) al[2] = lrtr(trp + 2048);
                if constexpr (i == 4) ah[2] = lrtr(trp + 2112);
                SCHED0();
                if constexpr (i >= 1) clr(IC(3 + i)); });
            sfor<30>([&](auto I) { constexpr int i = CV(I), b = (i % 6) >> 1, t = 2 * (i / 6) + (i & 1), id = OIDX(t, b);
                smfw<id>(acc[t], CAT4(al[b], ah[b]), W[id], ix);
                SCHED0();
                if constexpr (i == 1 || i == 3) TOUCH(ah[(i + 1) / 2]);
                if constexpr (i == 29) TOUCH(v[0]);
                if constexpr (i == 21) BARL(0)
                SCHED0();
                if constexpr (i == XLD) X.next<4>(k, l16);
                if constexpr (i >= 21 && i < 27) v[i - 21] = lr128(l16 + L_K + (i - 21) * 1024);
                SCHED0();
                V.out<i - 1, 0>(d0, d1);
                SCHED0();
                if constexpr (i % 3 == 0 && i < 18) clr(IC(9 + i / 3));
                if constexpr (i == 16) clr(IC(15));
                if constexpr (i >= 18 && i < 21) clr(IC(i - 2));
                if constexpr (i == 22) clr(IC(19));
                if constexpr (i >= 23 && i < 29) fold_op<(i - 23) / 2>(acc[(i - 23) & 1], fo[(i - 23) & 1]);
                if constexpr (i == 29) { st64(k.rs, vo, ym, (u32x2){fo[0], fo[1]}); SCHED0(); fin2(IC(1), IC(0)); }
                SCHED0(); });
            STAMP(s, 2)
            sfor<107>([&](auto I) { constexpr int i = CV(I), q = i < 8 ? i + 4 : i + 3;
                ymm(I);
                if constexpr (i < 5 || (i >= 29 && i < 35)) TOUCH(v[(i < 5 ? i + 1 : i - 23) % 12]);
                if constexpr (i < 13 && i != 8) {
                    if constexpr (q % 3 == 1) { ym += 32768; PINS(ym); }
                    if constexpr (q % 3 == 2 && i >= 5) __builtin_amdgcn_s_setprio(0);
                    SCHED0();
                    if constexpr (q % 3 == 0) { st64(k.rs, vo, ym, (u32x2){fo[0], fo[1]}); SCHED0(); }
                    if constexpr (q < 15) fin2(IC(q / 3), IC(q % 3));
                }
                if constexpr (i == 13) done = rt_async();
                if constexpr (i >= 15 && i < 23 && i != 18 && i != 20) v[6 + i - 15 - (i > 18) - (i > 20)] = lr128(l16 + L_K + (6 + i - 15 - (i > 18) - (i > 20)) * 1024);
                if constexpr (i == 12 || i == 13) X.next<i - 7>(k, l16);
                if constexpr (i == 15) { SCHED0(); X.next<7>(k, l16); }
                if constexpr (i == 18 || i == 23) X.next<8 + 2 * (i / 23)>(k, l16);
                if constexpr (i >= 19 && i < 37 && (i % 6 == 1 || i % 3 == 0)) X.op<(i - 19) / 6, ((i - 19) % 6 + 1) / 3>(k);
                if constexpr (i >= 66 && i < 90 && i % 3 == 0 && i % 12 != 3) X.op<3 + (i - 66) / 12, ((i - 66) / 3) % 4>(k);
                if constexpr (i == 90) { ym = (c & 2) ? ym2 : ym0; PINS(ym); }
                if constexpr (i == 91) { ys = ym + EXP(327680); PINS(ys); }
                if constexpr (i == 96) { ym = (c & 1) ? ys : ym; PINS(ym); }
                if constexpr (i == 60) { STAMP(s, 3) }
                if constexpr (i == 42 || i == 43 || i == 48 || i == 49) { AAC((i - 42) / 3 + (i & 1)) }
                if constexpr (i == 102) lgkm0(done);
                if constexpr (i == 103) { PINS(now); an = (unsigned)done - (unsigned)now; PINS(an); }
                if constexpr (i == 105) G.mx = vshl1(c, 1);
                SCHED0(); });
            AST(4)
        }
    } while (X.off == 0);
    } while (X.sg != 65u);
    PFLUSH(63)
    PROF_OUT
}

DEVI void ln_store(const Ctx& k, const float (&v)[40], unsigned dst, int row  ) {
    f32x2 s2[4] = {{0.f, 0.f}, {0.f, 0.f}, {0.f, 0.f}, {0.f, 0.f}};
#pragma unroll
    for (int i = 0; i < 40; i += 2) s2[(i >> 1) & 3] += (f32x2){v[i], v[i + 1]};
    const f32x2 st = (s2[0] + s2[1]) + (s2[2] + s2[3]);
    float m = row16sum(st[0] + st[1]) * (1.f / 640.f);
    f32x2 q2[4] = {{0.f, 0.f}, {0.f, 0.f}, {0.f, 0.f}, {0.f, 0.f}};
#pragma unroll
    for (int i = 0; i < 40; i += 2) { const f32x2 d = (f32x2){v[i], v[i + 1]} - (f32x2){m, m}; q2[(i >> 1) & 3] += d * d; }
    const f32x2 qt = (q2[0] + q2[1]) + (q2[2] + q2[3]);
    const float q = row16sum(qt[0] + qt[1]);
    const float var = bfr(q * (1.f / 640.f));
    m = bfr(m);
    const float rden = __builtin_amdgcn_rcpf(bfr(__builtin_amdgcn_sqrtf(bfr(var + 1e-5f))));
    const int n = k.l & 15;
    const unsigned vo = (n >> 2) * 2048 + (row >> 4) * 1024 + ((n & 3) * 16 + (row & 15)) * 16;
#pragma unroll
    for (int c = 0; c < 5; ++c) {
        u32x4 o;
#pragma unroll
        for (int i = 0; i < 4; ++i) { unsigned d = cvtpk(v[c * 8 + 2 * i] - m, v[c * 8 + 2 * i + 1] - m); o[i] = cvtpk(blo(d) * rden, bhi(d) * rden); }
        st128(k.rs, vo, dst + c * 16384, o);
    }
}
DEVI void unpack8(float* v, u32x4 a) {
#pragma unroll
    for (int i = 0; i < 4; ++i) { v[2 * i] = blo(a[i]); v[2 * i + 1] = bhi(a[i]); }
}
DEVI void ln_x_batch(const Params& p, const Ctx& k, int bw) {
    const int row = bw * 4 + (k.l >> 4), sg = bw >> 3;
    float v[40];
#pragma unroll
    for (int c = 0; c < 5; ++c) unpack8(v + c * 8, *(const u32x4*)(p.x + (size_t)row * 640 + (c * 16 + (k.l & 15)) * 8));
    ln_store(k, v, k.seg0(PG_XNA, sg), row & 31);
    DRAIN();
    if (k.l == 0) cnt_add(k.cnt0(C_XN, sg));
}

DEVI void row_role(const Params& p, const Ctx& k, char* smem, int rt) {
    PROF_DECL
    const int l = k.l, n = l & 15;
    const unsigned fo = k.w * 2048 + rt * 1024;
    RSTAMP(2)
    for (int sg = k.c; sg < 64; sg += 2) {
        prog_wait<5>(k, k.pg0(0) + CNT(C_X2, rt * 5), sg);
        STOP(sg) STAMP(sg, 0)
        const unsigned src = k.seg0(PG_X2, sg) + fo, dst = (k.seg0(PG_XNA, sg) ^ 8192u) + fo;
        u32x4 a[5];
_Pragma("unroll 1")
        for (unsigned spins = 0;; ++spins) {
            unsigned mx = 0;
#pragma unroll
            for (int c = 0; c < 5; ++c) a[c] = ld128(k.rs, l * 16, src + c * 16384);
#pragma unroll
            for (int c = 0; c < 5; ++c) mx = max(mx, umax4(a[c]));
            if (__all(mx != 0xFFFFFFFFu) || spin_fail(k, spins)) break;
            PCOUNT(0)
            asm volatile("" ::: "memory");
        }
        STAMP(sg, 1)
        f32x2 v[20], s2[4], q2[4];
#pragma unroll
        for (int i = 0; i < 20; ++i) { v[i] = (f32x2){blo(a[i >> 2][i & 3]), bhi(a[i >> 2][i & 3])}; s2[i & 3] = i < 4 ? v[i] : s2[i & 3] + v[i]; }
        const f32x2 st = (s2[0] + s2[1]) + (s2[2] + s2[3]);
        const float sm = xor32add(xor16add(st[0] + st[1])), mu = sm * (1.f / 160.f);
#pragma unroll
        for (int i = 0; i < 20; ++i) { const f32x2 d = v[i] - mu; q2[i & 3] = i < 4 ? d * d : __builtin_elementwise_fma(d, d, q2[i & 3]); }
        const f32x2 qt = (q2[0] + q2[1]) + (q2[2] + q2[3]);
        *(f32x2*)(smem + n * 32 + k.w * 8) = (f32x2){sm, xor32add(xor16add(qt[0] + qt[1]))};
        RAWBAR()
        const f32x4 e0 = *(const f32x4*)(smem + n * 32), e1 = *(const f32x4*)(smem + n * 32 + 16);
        float m = ((e0[0] + e0[2]) + (e1[0] + e1[2])) * (1.f / 640.f);
        const f32x4 d = (f32x4){e0[0], e0[2], e1[0], e1[2]} - 160.f * m;
        const float var = bfr((((e0[1] + e0[3]) + (e1[1] + e1[3])) + ((d[0] * d[0] + d[1] * d[1]) + (d[2] * d[2] + d[3] * d[3])) * (1.f / 160.f)) * (1.f / 640.f));
        m = bfr(m);
        const float rden = __builtin_amdgcn_rcpf(bfr(__builtin_amdgcn_sqrtf(bfr(var + 1e-5f))));
#pragma unroll
        for (int c = 0; c < 5; ++c) {
            u32x4 o;
#pragma unroll
            for (int i = 0; i < 4; ++i) { const f32x2 x = v[c * 4 + i] - m; o[i] = cvtpk(bfr(x[0]) * rden, bfr(x[1]) * rden); }
            st128(k.rs, l * 16, dst + c * 16384, o);
        }
        STAMP(sg, 2)
        DRAIN();
        __builtin_amdgcn_s_barrier();
        st32(k.rs, (k.w == 0 && l == 0) ? 0u : OOB, (k.pg0(0) ^ 8192u) + CNT(C_XN, k.c * 2 + rt), (unsigned)sg);
        STAMP(sg, 3) STAMP(sg, 4) STAMP(sg, 5)
        PFLUSH(sg) PFLUSH(sg ^ 1)
    }
    PROF_OUT
}

struct Red {
    u32x4 Y[16];
    template <int I0, int I1> DEVI void yload(const Ctx& k, int j, int sg) {
        const unsigned a = k.yx(sg, j / 5) + (j % 5) * 131072 + k.w * 16384;
#pragma unroll
        for (int i = I0; i < I1; ++i) Y[i] = ld128(k.rs, k.l * 16 + (i & 3) * 1024, a + ((i >> 2) & 1) * 4096 + (i >> 3) * 65536);
    }
};
DEVI unsigned y_get(const Ctx& k) { return ld32(k.rs, (k.l & 7) * 128, k.p0 + CNT(C_BC, k.w * 16 + 8) + 4); }
DEVI void y_wait(const Ctx& k, int s) {
_Pragma("unroll 1")
    for (unsigned spins = 0; !__all((int)y_get(k) >= s); ++spins) {
        asm volatile("" ::: "memory");
        __builtin_amdgcn_s_sleep(2);
        if (spin_fail(k, spins)) break;
    }
    __builtin_amdgcn_fence(__ATOMIC_ACQUIRE, "wavefront");
}
DEVI void wait_until(unsigned t) { while ((int)((rclk() - t) << 16) < 0) { if (WUS) __builtin_amdgcn_s_sleep(WUS); } }
#define DPPMAX(x, c) x = max(x, __builtin_amdgcn_update_dpp(0, x, c, 0xf, 0xf, false))
#define TSLP 1
#define TWU 0
DEVI void wait_until2(unsigned t) { while ((int)((rclk() - t) << 16) < 0) { if (TWU) __builtin_amdgcn_s_sleep(TWU); } }
#define WAIT_BC(s, BSLP) { \
    unsigned bc; \
    _Pragma("unroll 1") for (;; ++spins) { \
        bc = ld32(k.rs, (l & 15) * 128, k.p0 + CNT(C_BC, w * 16)); \
        DRAIN(); \
        if (__all(((int)bc >> 16) > s)) break; \
        __builtin_amdgcn_s_sleep(BSLP); \
        if (spin_fail(k, spins)) break; \
    } \
    const unsigned now = rclk(); \
    int d = (int)((bc - now) << 16); \
    d = ((int)bc >> 16) > s + 1 || d > (256 << 16) ? -(64 << 16) : d; \
    DPPMAX(d, 0xB1); DPPMAX(d, 0x4E); DPPMAX(d, 0x141); \
    tq = now + (__builtin_amdgcn_readlane(d, 0) >> 16); tv = now + (__builtin_amdgcn_readlane(d, 8) >> 16); }
DEVI void mred_role(const Params& p, const Ctx& k, char* smem, int j) {
    PROF_DECL
    const int l = k.l, w = k.w, g = l >> 4, n = l & 15;
    const unsigned ix = SPIX(l);
    const unsigned vp = (g * 16 + ((n >> 1) & 1) * 8 + (n >> 2) * 2 + (n & 1)) * 16;
    const unsigned v16 = (g >> 1) * 1024 + (g & 1) * 256 + n * 16, yo = j * 32768 + EXP(w * 4096);
    const u32x4 ones = {0xFFFFFFFFu, 0xFFFFFFFFu, 0xFFFFFFFFu, 0xFFFFFFFFu};
    auto arm = [&](int s) {
#pragma unroll
        for (int i = 0; i < 4; ++i) st128(k.rs, l * 16 + i * 1024, k.ym(s) + yo, ones);
    };
    Red R;
    RSTAMP(2)
    arm(1);
    y_wait(k, 2);
    R.yload<0, 16>(k, j, 0);
    for (int s = 0; s < 63; ++s) {
        STOP(s) STAMP(s, 0)
        const unsigned ap = k.p0 + PR_B + (s & 3) * PR_SZ + w * 49152, am = k.ym(s) + yo;
        const unsigned ph = (s & 4) ? 0x80008000u : 0u;
        unsigned spins = 0, tq, tv;
        unsigned fl;
        WAIT_BC(s, BSLP)
        STAMP(s, 5)
        char* rb = smem + (s & 1) * 4096;
_Pragma("unroll 1")
        for (spins = 0;; ++spins) {
            u32x4 ym[4], pm[5];
            f32x4 acc[2];
            if (spins == 0) wait_until(tv);
            asm volatile("" ::: "memory");
#pragma unroll
            for (int i = 0; i < 4; ++i) ym[i] = ld128(k.rs, v16 + (i >> 1) * 2048 + (i & 1) * 512, am);
            fl = ld32(k.rs, 0, k.p0 + CNT(C_X1, j));
            asm volatile("" ::: "memory");
            if (spins == 0) { wait_until(tq); STAMP(s, 1) }
            asm volatile("" ::: "memory");
#pragma unroll
            for (int i = 0; i < 5; ++i) pm[i] = ld128(k.rs, i < 4 ? l * 16 + i * 1024 : vp, ap + (i >> 2) * 4096);
            acc[0] = (f32x4){0.f, 0.f, 0.f, 0.f}; acc[1] = acc[0];
            const u32x8 y0 = {ym[0][0], ym[1][0], ym[0][2], ym[1][2], ym[2][0], ym[3][0], ym[2][2], ym[3][2]};
            const u32x8 y1 = {ym[0][1], ym[1][1], ym[0][3], ym[1][3], ym[2][1], ym[3][1], ym[2][3], ym[3][3]};
            unsigned mx = 0;
#pragma unroll
            for (int i = 0; i < 4; ++i) mx = max(mx, umax4(ym[i]));
            unsigned tg = mx == 0xFFFFFFFFu ? 0x80000000u : 0u;
            asm volatile("s_nop 1" : "+v"(acc[0]), "+v"(acc[1]), "+v"(tg) : "v"(y0), "v"(y1), "v"(ix));
            SCHED0();
#pragma unroll
            for (int i = 0; i < 5; ++i) {
                const unsigned u0 = __builtin_amdgcn_bitop3_b32(pm[i][0], pm[i][1], ph, 0x7e), u1 = __builtin_amdgcn_bitop3_b32(pm[i][2], pm[i][3], ph, 0x7e);
                SCHED0();
                if (i < 4) smf<false>(acc[0], __builtin_bit_cast(bf16x8, pm[i]), __builtin_bit_cast(bf16x16, __builtin_shufflevector(R.Y[2 * i], R.Y[2 * i + 1], 0, 1, 2, 3, 4, 5, 6, 7)), ix);
                else smf<false>(acc[0], __builtin_bit_cast(bf16x8, pm[4]), __builtin_bit_cast(bf16x16, y0), ix);
                tg = (tg | u0) | u1;
                SCHED0();
                if (i < 4) smf<false>(acc[1], __builtin_bit_cast(bf16x8, pm[i]), __builtin_bit_cast(bf16x16, __builtin_shufflevector(R.Y[8 + 2 * i], R.Y[9 + 2 * i], 0, 1, 2, 3, 4, 5, 6, 7)), ix);
                else smf<false>(acc[1], __builtin_bit_cast(bf16x8, pm[4]), __builtin_bit_cast(bf16x16, y1), ix);
                SCHED0();
            }
            const unsigned long long bad = __ballot((int)tg < 0);
            asm volatile("s_nop 5" : "+v"(acc[0]), "+v"(acc[1]) : "s"(bad));
#pragma unroll
            for (int t = 0; t < 2; ++t) { float* rp = (float*)(rb + t * 2048 + w * 512 + g * 128 + n * 4); rp[0] = acc[t][0] + acc[t][2]; rp[16] = acc[t][1] + acc[t][3]; }
            if (bad == 0) break;
            PCOUNT(0)
            asm volatile("" ::: "memory");
            if (spin_fail(k, spins)) break;
        }
        STAMP(s, 2)
        RAWBAR()
        if (w < 2) {
            const char* rp = rb + w * 2048 + ((l >> 4) * 2 + ((l >> 2) & 1)) * 64 + ((l & 3) * 4 + ((l >> 3) & 1) * 2) * 4;
            const f32x2 a = *(const f32x2*)rp, b = *(const f32x2*)(rp + 512), c = *(const f32x2*)(rp + 1024), d = *(const f32x2*)(rp + 1536);
            const f32x2 r = (a + b) + (c + d);
            st32(k.rs, l * 4, k.memf(s + 1) + EXP((2 * j + w) * 256), min(cvtpk(r[0], r[1]) ^ ph, 0xFFFFFFFEu));
        }
        STAMP(s, 3)
_Pragma("unroll 1")
        for (spins = 0; (int)__builtin_amdgcn_readfirstlane(fl) < s - 2; ++spins) {
            PCOUNT(1)
            __builtin_amdgcn_s_sleep(PSLEEP);
            if (spin_fail(k, spins)) break;
            fl = ld32(k.rs, 0, k.p0 + CNT(C_X1, j));
        }
        __builtin_amdgcn_fence(__ATOMIC_ACQUIRE, "wavefront");
        y_wait(k, 2 * s + 3);
        R.yload<0, 8>(k, j, s + 1);
        arm(s + 2);
        y_wait(k, 2 * s + 4);
        R.yload<8, 16>(k, j, s + 1);
        STAMP(s, 4)
        PFLUSH(s)
    }
    PFLUSH(63)
    PROF_OUT
}
DEVI void tred_role(const Params& p, const Ctx& k, char* smem, int j) {
    PROF_DECL
    const int l = k.l, w = k.w, g = l >> 4, n = l & 15, T = 2 * j + (w >> 1), rt = w & 1;
    const unsigned v16 = (g >> 1) * 1024 + (g & 1) * 256 + n * 16, yo = j * 32768 + EXP(w * 4096);
    const unsigned xo = EXP(j * 2048) + (T & 1) * 512 + rt * 1024, xv = ((g >> 1) * 16 + n) * 16 + (g & 1) * 8;
    const u32x2 ones = {0xFFFFFFFFu, 0xFFFFFFFFu};
    Red R;
    RSTAMP(2)
    st64(k.rs, xv, k.seg(PG_X1, 0) + xo, ones); st64(k.rs, xv, k.seg(PG_X1, 1) + xo, ones);
    y_wait(k, 2);
    R.yload<0, 16>(k, j, 0);
    for (int s = 0; s < 64; ++s) {
        STOP(s) STAMP(s, 0)
        const unsigned ap1 = k.p0 + PR_B + (s & 3) * PR_SZ + w * 49152 + 16384, ap2 = ap1 + 16384, am = k.ym(s) + yo;
        const unsigned ph = (s & 4) ? 0x80008000u : 0u;
        unsigned spins = 0, tq, tv;
        u32x2 res;
        if (k.c) res = ld64(k.rs, xv, k.seg0(PG_X2, s) + xo);
        else res = *(const u32x2*)(p.x + (size_t)(s * 32 + rt * 16 + n) * 640 + T * 16 + g * 4);
        f32x4 acc[2][2];
        WAIT_BC(s, TSLP)
        u32x4 yl[8];
#pragma unroll
        for (int i = 0; i < 8; ++i) { yl[i] = __builtin_shufflevector(R.Y[2 * i], R.Y[2 * i + 1], 0, 1, 4, 5); PIN(yl[i]); }
        wait_until2(tv); wait_until2(tq + RDLY);
        asm volatile("" ::: "memory");
        STAMP(s, 1)
_Pragma("unroll 1")
        for (;; ++spins) {
            u32x4 ym[4], pp[16];
#pragma unroll
            for (int i = 0; i < 4; ++i) ym[i] = ld128(k.rs, v16 + (i >> 1) * 2048 + (i & 1) * 512, am);
#pragma unroll
            for (int i = 0; i < 16; ++i) pp[i ^ 4] = ld128(k.rs, l * 16 + (i & 3) * 1024, (i < 8 ? ap2 : ap1) + (((i ^ 4) >> 2) & 1) * 4096);
            unsigned mx = 0, tg = 0;
#pragma unroll
            for (int t = 0; t < 2; ++t) {
                acc[t][0] = (f32x4){0.f, 0.f, 0.f, 0.f}; acc[t][1] = acc[t][0];
#pragma unroll
                for (int f = 0; f < 2; ++f) {
                    const u32x4 yf = {ym[2 * f][t], ym[2 * f][t + 2], ym[2 * f + 1][t], ym[2 * f + 1][t + 2]};
#pragma unroll
                    for (int r = 0; r < 2; ++r) acc[t][r] = mf(__builtin_bit_cast(bf16x8, yf), __builtin_bit_cast(bf16x8, pp[4 + 2 * r + f]), acc[t][r]);
                }
            }
#pragma unroll
            for (int i = 0; i < 4; ++i)
#pragma unroll
                for (int t = 0; t < 2; ++t) acc[t][0] = mf(__builtin_bit_cast(bf16x8, yl[t * 4 + i]), __builtin_bit_cast(bf16x8, pp[i]), acc[t][0]);
#pragma unroll
            for (int i = 0; i < 8; ++i)
#pragma unroll
                for (int t = 0; t < 2; ++t) acc[t][1] = mf(__builtin_bit_cast(bf16x8, R.Y[t * 8 + (i ^ 4)]), __builtin_bit_cast(bf16x8, pp[8 + (i ^ 4)]), acc[t][1]);
#pragma unroll
            for (int i = 0; i < 4; ++i) mx = max(mx, umax4(ym[i]));
#pragma unroll
            for (int i = 0; i < 16; ++i) tg |= __builtin_amdgcn_bitop3_b32(pp[i ^ 4][0], pp[i ^ 4][1], ph, 0x7e) | __builtin_amdgcn_bitop3_b32(pp[i ^ 4][2], pp[i ^ 4][3], ph, 0x7e);
            if (__all(mx != 0xFFFFFFFFu && (int)tg >= 0)) break;
            PCOUNT(0)
            asm volatile("" ::: "memory");
            if (spin_fail(k, spins)) break;
        }
        STAMP(s, 2)
        const unsigned yc = y_get(k);
        char* rb = smem + (s & 1) * 16384;
#pragma unroll
        for (int i = 0; i < 4; ++i) *(f32x4*)(rb + i * 4096 + w * 1024 + l * 16) = acc[i >> 1][i & 1];
        RAWBAR()
        st32(k.rs, (w == 0 && l == 0) ? 0u : OOB, k.p0 + CNT(C_X1, j), (unsigned)s);
        {
            const char* rp = rb + w * 4096 + l * 16;
            const f32x4 r = (*(const f32x4*)rp + *(const f32x4*)(rp + 1024)) + (*(const f32x4*)(rp + 2048) + *(const f32x4*)(rp + 3072));
            const unsigned d0 = cvtpk(r[0], r[1]) ^ ph, d1 = cvtpk(r[2], r[3]) ^ ph;
            u32x2 o; o[0] = packs(blo(res[0]) + blo(d0), bhi(res[0]) + bhi(d0)); o[1] = packs(blo(res[1]) + blo(d1), bhi(res[1]) + bhi(d1));
            st64(k.rs, xv, k.seg(PG_X1, s) + xo, o);
            st64(k.rs, s < 62 ? xv : OOB, k.seg(PG_X1, (s + 2) & 63) + xo, ones);
        }
        STAMP(s, 3)
        if (s < 63) {
            if (!__all((int)yc >= 2 * s + 4)) { PCOUNT(2) y_wait(k, 2 * s + 4); }
            __builtin_amdgcn_fence(__ATOMIC_ACQUIRE, "wavefront");
            R.yload<0, 16>(k, j, s + 1);
        }
        STAMP(s, 4)
        STAMP(s, 5)
        PFLUSH(s)
    }
    PROF_OUT
}

DEVI void up_role(const Params& p, const Ctx& k, char* smem, int id) {
    PROF_DECL
    const int l = k.l, w = k.w, g = l >> 4, n = l & 15;
    bf16x8 Wt[80];
    const int tile0 = id * 8 + w * 2;
    wstream<20, 4, 8>(smem + w * 32768,
        [&](auto U, unsigned sl) { constexpr int u = CV(U);
            wp_issue<2, (u % 8) * 4096>(p.wg + (size_t)u * 32 * 1536 + tile0 * 16, 1536, sl, l); wp_issue<2, (u % 8) * 4096 + 2048>(p.wu + (size_t)u * 32 * 1536 + tile0 * 16, 1536, sl, l); },
        [&](auto U, char* sp) { constexpr int u = CV(U);
            Wt[u] = wp_read<2, true>(sp, 0, l); Wt[20 + u] = wp_read<2, true>(sp, 1, l); Wt[40 + u] = wp_read<2, true>(sp + 2048, 0, l); Wt[60 + u] = wp_read<2, true>(sp + 2048, 1, l); });
    RAWBAR()
    RSTAMP(2)
    bf16x8 one8 = {0x3F80, 0x3F80, 0x3F80, 0x3F80, 0x3F80, 0x3F80, 0x3F80, 0x3F80};
    asm volatile("s_nop 1" : "+v"(one8));
    f32x4 cs[4];
    sfor<80>([&](auto I) { constexpr int f = CV(I); mfa<(f < 64), f % 20 == 0>(cs[f / 20], Wt[f], one8); });
    asm volatile("s_nop 7" : "+v"(cs[0]), "+v"(cs[1]), "+v"(cs[2]), "+v"(cs[3]));
    const unsigned ho = EXP((tile0 >> 1) * 1024), l16 = l * 16;
    constexpr int L_ST = 81920;
    const u32x4 ones = {0xFFFFFFFFu, 0xFFFFFFFFu, 0xFFFFFFFFu, 0xFFFFFFFFu};
#pragma unroll
    for (int i = 0; i < 4; ++i) st128(k.rs, l16, k.hseg(i >> 1) + (i & 1) * EXP(49152) + ho, ones);
    unsigned fails = 0;
    const unsigned fl = (w == 0 && l == 0) ? 0u : OOB;
    const unsigned long long m1 = __ballot(n & 1), m2 = __ballot(n & 2);
    bool have = false;
    for (int s = 0; s < 64; ) {
        if (!have) prog_wait<20>(k, k.p0 + CNT(C_X1, 0), s);
        STOP(s) STAMP(s, 0)
        xn_all(k, l16, k.seg(PG_X1, s) + w * 2048, (s & 1) * 40960 + w * 2048);
        const unsigned xb = (s & 1) * 40960 + l16, yb = xb + w * 2048, sp = L_ST + (s & 1) * 2048, rp = sp + n * 16;
        const unsigned dp = sp + (g == (n >> 2) ? n * 16 + w * 4 : 4096 + l * 4);
        unsigned nx;
        f32x4 acc[2][4], st[4];
        bf16x8 x[20], y0[5], y1[5];
        unsigned mx = 0;
        auto blk = [&](auto B, auto J) { constexpr int b = CV(B), j = CV(J);
            if constexpr (j == 0) asm volatile("s_waitcnt vmcnt(%0)\n\ts_barrier" :: "n"(8 - 2 * b) : "memory");
            if constexpr (j >= 1 && j < 5) x[4 * b + j - 1] = lr128(xb + (4 * b + j - 1) * 2048);
            if constexpr (j == 5) y0[b] = lr128(yb + b * 8192);
            if constexpr (j == 6) y1[b] = lr128(yb + b * 8192 + 1024); };
        sfor<7>([&](auto J) { blk(IC(0), J); });
        SCHED0();
        STAMP(s, 1)
        sfor<5>([&](auto B) { constexpr int b = CV(B);
            sfor<20>([&](auto I) { constexpr int i = CV(I), ks = 4 * b + ((i >> 2) & 3), nt = i & 3, f = nt * 20 + ks;
                if constexpr (i < 16) mfa<(f < 64), ks == 0>(acc[0][nt], Wt[f], x[ks]);
                else mfa<false, b == 0>(st[nt], (nt & 1) ? (nt & 2 ? y1[b] : y0[b]) : one8, nt & 2 ? y1[b] : y0[b]);
                SCHED0();
                if constexpr (i >= 8 && i < 15 && b < 4) blk(IC((b + 1) % 5), IC(i - 8));
                if constexpr (i >= 16) { const u32x4 v = __builtin_bit_cast(u32x4, (i & 1) ? y1[b] : y0[b]); mx = umax3(mx, v[i & 2], v[(i & 2) + 1]); PIN(mx); }
                if constexpr (b == 4 && i >= 9 && i < 12) x[i - 9] = lr128(xb + 1024 + (i - 9) * 2048);
                SCHED0(); }); });
        STAMP(s, 2)
        float mean[2], rden[2], G[2], U[2];
        f32x4 sa[2], sb[2];
        u32x4 bad, o[2];
        bool redo;
        auto stat = [&](auto I) { constexpr int i = CV(I), rt = i / 8, j = i % 8;
            if constexpr (j == 0) asm volatile("v_add_f32 %0, %2, %3\n\tv_add_f32 %1, %4, %5" : "=&v"(mean[rt]), "=&v"(rden[rt]) : "v"(sa[rt][0]), "v"(sa[rt][1]), "v"(sb[rt][0]), "v"(sb[rt][1]));
            if constexpr (j == 1 || j == 2) asm volatile("v_add_f32 %0, %0, %2\n\tv_add_f32 %1, %1, %3" : "+v"(mean[rt]), "+v"(rden[rt]) : "v"(sa[rt][j + 1]), "v"(sb[rt][j + 1]));
            if constexpr (j == 3) asm volatile("v_mul_f32 %0, 0xbaccccCD, %0\n\tv_mul_f32 %1, 0x3acccccd, %1" : "+v"(mean[rt]), "+v"(rden[rt]));
            if constexpr (j == 4) asm volatile("v_fma_f32 %1, -%0, %0, %1\n\tv_cvt_pk_bf16_f32 %0, 0, %0\n\tv_max_f32 %1, 0, %1" : "+v"(mean[rt]), "+v"(rden[rt]));
            if constexpr (j == 5) asm volatile("v_cvt_pk_bf16_f32 %0, 0, %0\n\tv_add_f32 %0, 0x3727c5ac, %0\n\tv_cvt_pk_bf16_f32 %0, 0, %0" : "+v"(rden[rt]));
            if constexpr (j == 6) asm volatile("v_sqrt_f32 %0, %0" : "+v"(rden[rt]));
            if constexpr (j == 7) asm volatile("v_cvt_pk_bf16_f32 %0, 0, %0\n\tv_rcp_f32 %0, %0" : "+v"(rden[rt])); };
#define GU "+v"(G[0]), "+v"(G[1]), "+v"(U[0]), "+v"(U[1])
        auto ep = [&](auto I) { constexpr int i = CV(I), q = i / 15, j = i % 15, rt = q >> 2, t = (q >> 1) & 1, c = (q & 1) * 2;
            if constexpr (j == 0) asm volatile("v_fma_f32 %0, %2, %3, %4\n\tv_fma_f32 %1, %2, %5, %6" : "=&v"(G[0]), "=&v"(G[1]) : "v"(mean[rt]), "v"(cs[t][c]), "v"(acc[rt][t][c]), "v"(cs[t][c + 1]), "v"(acc[rt][t][c + 1]));
            if constexpr (j == 1) asm volatile("v_mul_f32 %0, %0, %2\n\tv_mul_f32 %1, %1, %2" : "+v"(G[0]), "+v"(G[1]) : "v"(rden[rt]));
            if constexpr (j == 2) asm volatile("v_cvt_pk_bf16_f32 %0, 0, %0\n\tv_cvt_pk_bf16_f32 %1, 0, %1" : "+v"(G[0]), "+v"(G[1]));
            if constexpr (j == 3) asm volatile("v_mul_f32 %0, 0xbfb8aa3b, %0\n\tv_mul_f32 %1, 0xbfb8aa3b, %1" : "+v"(G[0]), "+v"(G[1]));
            if constexpr (j == 4 || j == 5) asm volatile("v_exp_f32 %0, %0" : "+v"(G[j & 1]));
            if constexpr (j == 6) asm volatile("v_fma_f32 %0, %2, %3, %4\n\tv_fma_f32 %1, %2, %5, %6" : "=&v"(U[0]), "=&v"(U[1]) : "v"(mean[rt]), "v"(cs[2 + t][c]), "v"(acc[rt][2 + t][c]), "v"(cs[2 + t][c + 1]), "v"(acc[rt][2 + t][c + 1]));
            if constexpr (j == 7) asm volatile("v_add_f32 %0, 1.0, %0\n\tv_add_f32 %1, 1.0, %1" : "+v"(G[0]), "+v"(G[1]));
            if constexpr (j == 8 || j == 9) asm volatile("v_rcp_f32 %0, %0" : "+v"(G[j & 1]));
            if constexpr (j == 10) asm volatile("v_mul_f32 %0, %0, %2\n\tv_mul_f32 %1, %1, %2" : "+v"(U[0]), "+v"(U[1]) : "v"(rden[rt]));
            if constexpr (j == 11) asm volatile("v_cvt_pk_bf16_f32 %0, 0, %0\n\tv_cvt_pk_bf16_f32 %1, 0, %1" : "+v"(U[0]), "+v"(U[1]));
            if constexpr (j == 12) asm volatile("v_cvt_pk_bf16_f32 %0, 0, %0\n\tv_cvt_pk_bf16_f32 %1, 0, %1" : "+v"(G[0]), "+v"(G[1]));
            if constexpr (j == 13) asm volatile("v_mul_f32 %0, %0, %2\n\tv_mul_f32 %1, %1, %3" : "+v"(G[0]), "+v"(G[1]) : "v"(U[0]), "v"(U[1]));
            if constexpr (j == 14) { unsigned d; asm volatile("v_cvt_pk_bf16_f32 %0, %1, %2\n\tv_min_u32 %0, -2, %0" : "=&v"(d) : "v"(G[0]), "v"(G[1])); o[rt][q & 3] = d; } };
        sfor<80>([&](auto I) {
            constexpr int i = CV(I), ks = i >> 2, nt = i & 3, f = nt * 20 + ks;
            mfa<(f < 64), ks == 0>(acc[1][nt], Wt[f], x[ks]);
            SCHED0();
            if constexpr (nt == 0 && ks + 3 < 20) x[(ks + 3) % 20] = lr128(xb + 1024 + ((ks + 3) % 20) * 2048);
            if constexpr (i == 1 || i == 3) {
                constexpr int rt2 = i == 3;
                asm volatile("" : "+v"(st[2 * rt2]), "+v"(st[2 * rt2 + 1]));
                const f32x4& q = st[2 * rt2 + 1];
                const float d = csel(csel(q[0], q[1], m1), csel(q[2], q[3], m1), m2);
                lw32(dp + rt2 * 512, __float_as_uint(st[2 * rt2][0])); lw32(dp + rt2 * 512 + 256, __float_as_uint(d));
            }
            if constexpr (i == 2) lw32(sp + 1024 + w * 4, __any(mx == 0xFFFFFFFFu) ? 1u : 0u);
            if constexpr (i == 6) bar_lgkm<1>();
            if constexpr (i == 6) sa[0] = __builtin_bit_cast(f32x4, lr128(rp));
            if constexpr (i == 7) sb[0] = __builtin_bit_cast(f32x4, lr128(rp + 256));
            if constexpr (i == 9) sa[1] = __builtin_bit_cast(f32x4, lr128(rp + 512));
            if constexpr (i == 10) sb[1] = __builtin_bit_cast(f32x4, lr128(rp + 768));
            if constexpr (i == 11) bad = __builtin_bit_cast(u32x4, lr128(sp + 1024));
            if constexpr (i >= 12 && i < 20) stat(IC(i - 12));
            if constexpr (i >= 20) ep(IC(i - 20));
            if constexpr (i >= 21 && i < 53 && nt == 1) stat(IC(8 + (i - 21) / 4));
            if constexpr (i == 54) { redo = __builtin_amdgcn_readfirstlane((bad[0] | bad[1]) | (bad[2] | bad[3])) != 0 && fails++ < 4096u; }
            if constexpr (i == 78) nx = ld32(k.rs, min(l, 19) * 128, k.p0 + CNT(C_X1, 0));
            if constexpr (i == UPOS) st32(k.rs, fl, k.p0 + CNT(C_H, id), (unsigned)(redo ? s - 1 : s));
            SCHED0();
        });
        STAMP(s, 3)
        asm volatile("s_nop 7" : "+v"(acc[1][0]), "+v"(acc[1][1]), "+v"(acc[1][2]), "+v"(acc[1][3]));

#pragma unroll
        for (int q = 0; q < 4; ++q) {
            const int t = q >> 1, c = (q & 1) * 2;
            float hv[2];
#pragma unroll
            for (int i = 0; i < 2; ++i) {
                const float gg = bfr(__builtin_fmaf(mean[1], cs[t][c + i], acc[1][t][c + i]) * rden[1]), u = bfr(__builtin_fmaf(mean[1], cs[2 + t][c + i], acc[1][2 + t][c + i]) * rden[1]);
                hv[i] = bfr(__builtin_amdgcn_rcpf(1.f + __builtin_amdgcn_exp2f(gg * -1.44269504f))) * u;
            }
            o[1][q] = packs(hv[0], hv[1]);
        }
        if (redo) { PCOUNT(0) RAWBAR() continue; }
        st128(k.rs, l16, k.hseg(s) + ho, o[0]); st128(k.rs, l16, k.hseg(s) + EXP(49152) + ho, o[1]);
        st128(k.rs, s < 62 ? l16 : OOB, k.hseg((s + 2) & 63) + ho, ones); st128(k.rs, s < 62 ? l16 : OOB, k.hseg((s + 2) & 63) + EXP(49152) + ho, ones);
        PIN(nx);
        have = __all((int)nx > s);
        __builtin_amdgcn_fence(__ATOMIC_ACQUIRE, "wavefront");
        STAMP(s, 4) STAMP(s, 5)
        PFLUSH(s)
        ++s;
    }
    PROF_OUT
}

DEVI void down_role(const Params& p, const Ctx& k, char* smem, int id) {
    PROF_DECL
    const int l = k.l, w = k.w, g = l >> 4, n = l & 15, rt = id / 5, cg = id % 5;
    bf16x8 Wt[96];
    wstream<12, 8, 4>(smem + w * 32768,
        [&](auto U, unsigned sl) { constexpr int u = decltype(U)::value; wp_issue<8, (u % 4) * 8192>(p.wd + (size_t)(w * 12 + u) * 32 * 640 + cg * 128, 640, sl, l); },
        [&](auto U, char* sp) { constexpr int u = decltype(U)::value;
#pragma unroll
            for (int nt = 0; nt < 8; ++nt) Wt[u * 8 + nt] = wp_read<8, false>(sp, (nt + 2 * w) & 7, l); });
    RAWBAR()
    RSTAMP(2)
    const unsigned xo = EXP((cg * 4 + w) * 2048) + rt * 1024, xv = ((g >> 1) * 16 + n) * 16 + (g & 1) * 8;
    const u32x2 ones = {0xFFFFFFFFu, 0xFFFFFFFFu};
    const unsigned x2v = k.c ? OOB : xv;
#pragma unroll
    for (int i = 0; i < 4; ++i) st64(k.rs, x2v, k.seg(PG_X2, i >> 1) + xo + (i & 1) * 512, ones);
    for (int s = 0; s < 64; ++s) {
        prog_wait<12>(k, k.p0 + CNT(C_H, 0), s);
        STOP(s) STAMP(s, 0)
        f32x4 acc[8];
        u32x2 res[2];
        const unsigned rb = (s & 1) * 32768 + l * 16;
_Pragma("unroll 1")
        for (unsigned spins = 0;; ++spins) {
            u32x4 hx[12];
#pragma unroll
            for (int q = 0; q < 3; ++q) {
                const unsigned a = k.hseg(s) + rt * EXP(49152) + EXP((w * 12 + q * 4) * 1024);
#pragma unroll
                for (int i = 0; i < 4; ++i) hx[q * 4 + i] = ld128(k.rs, l * 16 + i * 1024, a);
            }
#pragma unroll
            for (int t = 0; t < 2; ++t) res[t] = ld64(k.rs, xv + t * 512, k.seg(PG_X1, s) + xo);
            unsigned mx = 0;
            sfor<96>([&](auto I) { constexpr int i = CV(I), ks = i < 72 ? i / 6 : (i - 72) / 2, j = i < 72 ? 2 + i % 6 : i & 1, f = ks * 8 + j;
                mfa<(f < 64), ks == 0>(acc[j], Wt[f], __builtin_bit_cast(bf16x8, hx[ks]));
                SCHED0();
                if constexpr (i < 72 && i % 6 < 2) { mx = umax3(mx, hx[ks][(i & 1) * 2], hx[ks][(i & 1) * 2 + 1]); PIN(mx); }
                if constexpr (i >= 75 && i < 93 && i % 3 == 0) { constexpr int jj = 2 + (i - 75) / 3;
                    asm volatile("" : "+v"(acc[jj])); lw128(rb + ((w + jj / 2) & 3) * 6144 + ((jj / 2 - 1) * 2 + (jj & 1)) * 1024, __builtin_bit_cast(u32x4, acc[jj])); }
                SCHED0(); });
            asm volatile("s_nop 7" : "+v"(acc[0]), "+v"(acc[1]), "+v"(res[0]), "+v"(res[1]));
            if (__all(mx != 0xFFFFFFFFu) || spin_fail(k, spins)) break;
            PCOUNT(0)
            asm volatile("" ::: "memory");
        }
        STAMP(s, 1)
        RAWBAR()
        st32(k.rs, (w == 0 && l == 0) ? 0u : OOB, k.p0 + CNT(C_X2, rt * 5 + cg), (unsigned)s);
#pragma unroll
        for (int t = 0; t < 2; ++t) {
            const unsigned rp = rb + w * 6144 + t * 1024;
            const f32x4 r = (acc[t] + __builtin_bit_cast(f32x4, lr128(rp))) + (__builtin_bit_cast(f32x4, lr128(rp + 2048)) + __builtin_bit_cast(f32x4, lr128(rp + 4096)));
            const unsigned d0 = cvtpk(r[0], r[1]), d1 = cvtpk(r[2], r[3]);
            u32x2 o; o[0] = cvtpk(blo(res[t][0]) + blo(d0), bhi(res[t][0]) + bhi(d0)); o[1] = cvtpk(blo(res[t][1]) + blo(d1), bhi(res[t][1]) + bhi(d1));
            if (k.c) *(u32x2*)(p.out + (size_t)(s * 32 + rt * 16 + n) * 640 + (cg * 8 + w * 2 + t) * 16 + g * 4) = o;
            else st64(k.rs, xv + t * 512, k.seg(PG_X2, s) + xo, (u32x2){min(o[0], 0xFFFFFFFEu), min(o[1], 0xFFFFFFFEu)});
        }
        STAMP(s, 2)
#pragma unroll
        for (int t = 0; t < 2; ++t) st64(k.rs, s < 62 ? x2v : OOB, k.seg(PG_X2, (s + 2) & 63) + xo + t * 512, ones);
        STAMP(s, 3) STAMP(s, 4) STAMP(s, 5)
        PFLUSH(s)
    }
    PROF_OUT
}

DEVI void warm(const Params& p, const Ctx& k, int xl, int hw) {
    if (k.c) sleep_ticks(WD1);
    set_m0(k.w * 256);
    const unsigned t0 = rclk();
_Pragma("unroll 1")
    for (int it = hw; it < 880; it += 64) {
        const u16* b; unsigned o;
        while ((int)(rclk() - t0) < (it >> 6) * PACE) __builtin_amdgcn_s_sleep(1);
        if (OLDMAP) { const int jt = (it & 1) ? min(440 + (it >> 1), 871) : it >> 1;
            if (jt < 200) { b = p.Wo; o = ((xl * 8 + (jt / 5) % 8) * 192 + (jt / 40 + 1) * 32) * 1280 + (jt % 5) * 8192 + k.l * 128; }
            else if (jt < 440) { const unsigned ln = (jt - 200) * 64 + k.l; b = p.Wv; o = (ln / 24) * 12288 + xl * 3072 + (ln % 24) * 128; }
            else { const unsigned ln = ((jt - 440) >> 1) * 64 + k.l; b = (jt & 1) ? p.Wq : p.Wk; o = (64 + ln / 24) * 12288 + xl * 3072 + (ln % 24) * 128; } }
        else if (!(xl & 1)) { const unsigned ln = (min(it, 863) >> 1) * 64 + k.l; b = (it & 1) ? p.Wq : p.Wk; o = (64 + ln / 48) * 12288 + xl * 3072 + (ln % 48) * 128; }
        else if (it < 400) { b = p.Wo; o = (((xl >> 1) * 16 + (it / 5) % 16) * 192 + (it / 80 + 1) * 32) * 1280 + (it % 5) * 8192 + k.l * 128; }
        else { const unsigned ln = (it - 400) * 64 + k.l; b = p.Wv; o = (ln / 48) * 12288 + (xl >> 1) * 6144 + (ln % 48) * 128; }
        asm volatile("global_load_lds_dword %0, %1" :: "v"(o), "s"(b) : "memory");
    }
    if (hw >= 20) {
_Pragma("unroll 1")
        for (int it = hw - 20; it < 240; it += 44) {
            const unsigned ln = (it >> 1) * 64 + k.l;
            const bool dn = (xl & 1) != UPODD;
            const u16* b = dn ? p.wd : (it & 1) ? p.wu : p.wg;
            const unsigned o = dn ? ((it & 3) * 60 + (it >> 2)) * 8192 + k.l * 128 : (ln / 12) * 3072 + (xl >> 1) * 1536 + (ln % 12) * 128;
            while ((int)(rclk() - t0) < 14 * PACE + GDLY + (it / 44) * PACE2) __builtin_amdgcn_s_sleep(1);
            asm volatile("global_load_lds_dword %0, %1" :: "v"(o), "s"(b) : "memory");
        }
    }
    DRAIN();
    RAWBAR()
}

DEVI int xcc_id() { return __builtin_amdgcn_s_getreg(20 | (0 << 6) | (3 << 11)) & 7; }

__global__ __launch_bounds__(256) void init_kernel(Params p) {
    const int b = blockIdx.x, tid = threadIdx.x, l = tid & 63;
    const __amdgpu_buffer_rsrc_t rs = __builtin_amdgcn_make_buffer_rsrc((void*)p.ws, 0, 0x7fffffff, 0x00020000);
    const u32x4 ones = {0xFFFFFFFFu, 0xFFFFFFFFu, 0xFFFFFFFFu, 0xFFFFFFFFu};
    if (b < 64 && tid < 64) {
        unsigned bt[2] = {0xFFFFFFFFu, 0xFFFFFFFFu};
        if (b < NPAGES) {
            const unsigned off = ((unsigned)b << 21) + 7 * 16384 + (l & 7) * 16;
#pragma unroll 1
            for (int r = 0; r < CALR; ++r) {
#pragma unroll
                for (int hf = 0; hf < 2; ++hf) {
                    unsigned long long c0, c1;
                    asm volatile("s_memtime %0\n\ts_waitcnt lgkmcnt(0)" : "=s"(c0) :: "memory");
                    st128(rs, off + hf * 8192, 0, ones);
                    asm volatile("s_waitcnt vmcnt(0)\n\ts_memtime %0\n\ts_waitcnt lgkmcnt(0)" : "=s"(c1) :: "memory");
                    bt[hf] = min(bt[hf], (unsigned)(c1 - c0));
                }
            }
        }
        if (l == 0) ((unsigned*)(p.cm + CM_TAB))[b] = (unsigned)(bt[1] < bt[0]) ^ (unsigned)(xcc_id() >> 2);
    }
    constexpr unsigned N0 = PR_B + 4 * PR_SZ, N1 = 40 * 16384, N2 = 64 * 20480;
    for (unsigned i = (b * 256 + tid) * 16; i < N0 + N1 + N2; i += 256 * 256 * 16) {
        unsigned o;
        u32x4 v = ones;
        if (i < N0) o = i;
        else if (i < N0 + N1) o = ((unsigned)PG_YM << 21) + (i - N0);
        else {
            const unsigned j = i - N0 - N1, s = j / 20480, r = j % 20480;
            const unsigned q = r < 16384 ? r : 16384 + ((r - 16384) >> 11) * 8192 + (r & 2047);
            o = (2u << 21) + s * 32768 + q;
            if (s == 0) {
                const int e = (((q >> 14) << 13) | (q & 8191u)) >> 4, T = e >> 4, hf = (e >> 1) & 1, rr = ((e >> 2) & 3) * 2 + (e & 1);
#pragma unroll
                for (int c = 0; c < 4; ++c) v[c] = min(*(const unsigned*)(p.mem0 + rr * 640 + T * 16 + 4 * c + 2 * hf), 0xFFFFFFFEu);
            }
        }
        *(u32x4*)(p.ws + o) = v;
    }
    if (b == 255) for (unsigned o = CM_TICKET + tid * 16; o < CM_BYTES; o += 4096) *(u32x4*)(p.cm + o) = ones;
}

__global__ __launch_bounds__(256, 1) void mega_kernel(Params p) {
    extern __shared__ __attribute__((aligned(16))) char smem[];
    Ctx k;
    k.ws = p.ws; k.cm = p.cm;
    k.rs = __builtin_amdgcn_make_buffer_rsrc((void*)p.ws, 0, 0x7fffffff, 0x00020000);
    k.l = threadIdx.x & 63; k.w = __builtin_amdgcn_readfirstlane(threadIdx.x >> 6);
    RSTAMP(0)
    k.par = __ballot(((const unsigned*)(p.cm + CM_TAB))[k.l] != 0u);
    if (threadIdx.x == 0) {
        int x = xcc_id(), role = 0;
#pragma unroll 1
        for (int i = 0; i < 8; ++i) {
            const unsigned t = __hip_atomic_fetch_add((GAS unsigned*)(p.cm + CM_TICKET + x * 128), 1u, __ATOMIC_RELAXED, __HIP_MEMORY_SCOPE_AGENT) + 1u;
            if (t < 32u) { role = x * 32 + (int)t; break; }
            x = (x + 1) & 7;
        }
        *(volatile LAS int*)smem = role;
    }
    RAWBAR()
    k.role = __builtin_amdgcn_readfirstlane(*(volatile LAS int*)smem);
    RAWBAR()
    const int x = k.role >> 5, i = k.role & 31, xl = x & 3;
    k.c = x >> 2;
    if (k.c) k.par = ~k.par;
    k.p0 = k.pg(0);
    RSTAMP(1)
    if (k.c) ln_x_batch(p, k, (i * 4 + xl) * 4 + k.w);
    if (OLDMAP ? i < 8 : i < 16 && !(xl & 1)) { k.l = lane(); qk_role(p, k, smem, xl * 8 + i); return; }
    if (i < 16) { k.l = lane(); vo_role(p, k, smem, OLDMAP ? xl * 8 + i - 8 : (xl >> 1) * 16 + i); return; }
    k.l = lane();
    warm(p, k, xl, (i - 16) * 4 + k.w);
    if (i < 21) { mred_role(p, k, smem, xl * 5 + i - 16); return; }
    if (i < 26) { tred_role(p, k, smem, xl * 5 + i - 21); return; }
    if ((xl & 1) == UPODD) up_role(p, k, smem, (xl >> 1) * 6 + i - 26);
    else if (i < 31) down_role(p, k, smem, (xl >> 1) * 5 + i - 26);
    else row_role(p, k, smem, xl >> 1);
}

extern "C" void kernel_launch(void* const* d_in, const int* in_sizes, int n_in,
                              void* d_out, int out_size, void* d_ws, size_t ws_size,
                              hipStream_t stream) {
    Params p;
    p.x = (const u16*)d_in[0]; p.mem0 = (const u16*)d_in[1];
    p.Wq = (const u16*)d_in[2]; p.Wk = (const u16*)d_in[3]; p.Wv = (const u16*)d_in[4]; p.Wo = (const u16*)d_in[5];
    p.wg = (const u16*)d_in[8]; p.wu = (const u16*)d_in[9]; p.wd = (const u16*)d_in[10];
    p.out = (u16*)d_out; p.cm = (char*)d_ws;
    p.ws = (char*)(((uintptr_t)d_ws + CM_BYTES + ((1u << 21) - 1)) & ~(uintptr_t)((1u << 21) - 1));
    (void)hipFuncSetAttribute((const void*)mega_kernel, hipFuncAttributeMaxDynamicSharedMemorySize, LDS_BYTES);
    init_kernel<<<256, 256, 0, stream>>>(p);
    mega_kernel<<<256, 256, LDS_BYTES, stream>>>(p);
}
```

```cpp
#include <hip/hip_runtime.h>
#include <hip/hip_bf16.h>
#include <stdint.h>
#include <utility>

typedef unsigned short u16;
typedef __attribute__((ext_vector_type(16))) short bf16x16;
typedef __attribute__((ext_vector_type(8))) short bf16x8;
typedef __attribute__((ext_vector_type(4))) short bf16x4;
typedef __attribute__((ext_vector_type(8))) __bf16 hbf8;
typedef __attribute__((ext_vector_type(16))) __bf16 hbf16;
typedef __attribute__((ext_vector_type(4))) float f32x4;
typedef __attribute__((ext_vector_type(2))) float f32x2;
typedef __attribute__((ext_vector_type(8))) unsigned u32x8;
typedef __attribute__((ext_vector_type(4))) unsigned u32x4;
typedef __attribute__((ext_vector_type(2))) unsigned u32x2;
typedef __attribute__((ext_vector_type(2))) __bf16 bf16x2_t;
#define LAS __attribute__((address_space(3)))
#define GAS __attribute__((address_space(1)))
#define DEVI __device__ __forceinline__

#define PSLEEP 8
#define WD1 400
#define PACE 65
#define XPOS 74
#define YPOS 40
#define XPOSY 1
#define PWS 1
#define WUS 0
#define WMIX 1
#define PACE2 30
#define GDLY 80
#define QDLY 0
#define CALR 3
#define SPG 3
#define DQ 16
#define DV 12
#define BSLP 1
#define SDLY 0
#define UPOS 79
#define RDLY 45
#define NOYST 0
#define NOTOK 0
#define NOYLD 0
#define GPOSY 8
#define OLDMAP 1
#define UPODD 0
#define XLD 10
#define TROT 8
#define KOUT 1
#define RPOS 75
#define RDY 90
#define APOS 6
#define GPOS 74

#define EXP(o) ((((unsigned)(o) >> 13) << 14) | ((unsigned)(o) & 8191u))
enum { C_XN = 0, C_Y = 1, C_X1 = 2, C_XB = 3, C_H = 4, C_X2 = 5, C_BC = 6 };
#define CNT(type, s) ((unsigned)((type) * 16384 + (s) * 128))
constexpr unsigned PR_B = EXP(65536), PR_SZ = EXP(12 * 8192);
constexpr unsigned DUMMY_B = EXP(0);
constexpr int PG_MEMF = 2;
#define YXR 8
constexpr int PG_YX = 3;
constexpr int PG_XNA = PG_YX + 2 * YXR;
constexpr int PG_X1 = PG_XNA + 4, PG_X2 = PG_X1 + 4;
constexpr int PG_H = PG_X2 + 4;
constexpr int PG_YM = PG_H + 8;
constexpr int NPAGES = PG_YM + 2;
static_assert(NPAGES <= 64, "map");
constexpr unsigned CM_TAB = 0, CM_TICKET = 1024, CM_TMO = 4096, CM_BYTES = 8192;
constexpr unsigned SPIN_CAP = 1u << 20;
#define AUX_SC1 16
#define LDS_BYTES (160 * 1024)

struct Params {
    const u16* x; const u16* mem0; const u16* Wq; const u16* Wk; const u16* Wv; const u16* Wo;
    const u16* wg; const u16* wu; const u16* wd; u16* out; char* cm; char* ws;
};
struct Ctx {
    char* ws; char* cm; __amdgpu_buffer_rsrc_t rs; unsigned long long par;
    int c, l, w, role;
    unsigned p0;
    DEVI unsigned pg(int page) const { return ((unsigned)page << 21) | (((unsigned)(par >> page) & 1u) << 13); }
    DEVI unsigned pg0(int page) const { return pg(page) ^ ((unsigned)c << 13); }
    DEVI unsigned seg(int page, int s) const { return pg(page + (s >> 4)) + (unsigned)(s & 15) * 131072u; }
    DEVI unsigned seg0(int page, int s) const { return seg(page, s) ^ ((unsigned)c << 13); }
    DEVI unsigned hseg(int s) const { return pg(PG_H + (s >> 3)) + (unsigned)(s & 7) * 262144u; }
    DEVI unsigned yx(int sg, int wq) const { return pg(PG_YX + (sg % YXR) * 2 + (wq >> 1)) + (unsigned)(wq & 1) * 1048576u; }
    DEVI unsigned ym(int s) const { return pg(PG_YM + ((s >> 1) & 1)) + (unsigned)(s & 1) * EXP(327680); }
    DEVI unsigned memf(int s) const { return pg(PG_MEMF) + (unsigned)s * 32768u; }
    DEVI char* cnt(int type, int s) const { return ws + (p0 + CNT(type, s)); }
    DEVI char* cnt0(int type, int s) const { return ws + ((p0 ^ ((unsigned)c << 13)) + CNT(type, s)); }
};

DEVI unsigned cvtpk(float a, float b) { return __builtin_bit_cast(unsigned, __builtin_convertvector((f32x2){a, b}, bf16x2_t)); }
DEVI unsigned packs(float a, float b) { return min(cvtpk(a, b), 0xFFFFFFFEu); }
DEVI float blo(unsigned x) { return __uint_as_float(x << 16); }
DEVI float bhi(unsigned x) { return __uint_as_float(x & 0xFFFF0000u); }
DEVI float bfr(float f) { return __uint_as_float(cvtpk(0.f, f)); }
DEVI u32x4 ld128(__amdgpu_buffer_rsrc_t rs, unsigned v, unsigned s) { return __builtin_amdgcn_raw_buffer_load_b128(rs, (int)v, (int)s, AUX_SC1); }
DEVI u32x2 ld64(__amdgpu_buffer_rsrc_t rs, unsigned v, unsigned s) { return __builtin_amdgcn_raw_buffer_load_b64(rs, (int)v, (int)s, AUX_SC1); }
DEVI unsigned ld32(__amdgpu_buffer_rsrc_t rs, unsigned v, unsigned s) { return __builtin_amdgcn_raw_buffer_load_b32(rs, (int)v, (int)s, AUX_SC1); }
DEVI void st128(__amdgpu_buffer_rsrc_t rs, unsigned v, unsigned s, u32x4 x) {
    __builtin_amdgcn_raw_buffer_store_b128(x, rs, (int)v, (int)s, AUX_SC1);
    asm volatile("s_nop 1" :: "v"(x) : "memory");
}
DEVI void st64(__amdgpu_buffer_rsrc_t rs, unsigned v, unsigned s, u32x2 x) { __builtin_amdgcn_raw_buffer_store_b64(x, rs, (int)v, (int)s, AUX_SC1); }
DEVI void st32(__amdgpu_buffer_rsrc_t rs, unsigned v, unsigned s, unsigned x) { __builtin_amdgcn_raw_buffer_store_b32(x, rs, (int)v, (int)s, AUX_SC1); }
#define RAWBAR() { __builtin_amdgcn_s_waitcnt(0xc07f); __builtin_amdgcn_s_barrier(); }
#define BARL(n) { asm volatile("" ::: "memory"); __builtin_amdgcn_s_waitcnt(0xc07f | ((n) << 8)); __builtin_amdgcn_s_barrier(); asm volatile("" ::: "memory"); }
#define DRAIN() asm volatile("s_waitcnt vmcnt(0)" ::: "memory")
#define SCHED0() __builtin_amdgcn_sched_barrier(0)
#define OOB 0x80000000u
DEVI int opq(int x) { asm volatile("" : "+v"(x)); return x; }
#define LANE const int l = opq(k.l), g = l >> 4, n = l & 15; (void)g; (void)n;
DEVI unsigned umax3(unsigned a, unsigned b, unsigned c) { return max(max(a, b), c); }
DEVI unsigned umax4(u32x4 v) { return max(umax3(v[0], v[1], v[2]), v[3]); }
DEVI unsigned rclk() { return (unsigned)__builtin_amdgcn_s_memrealtime(); }
DEVI unsigned long long rt_async() { unsigned long long t; asm volatile("s_memrealtime %0" : "=s"(t)); return t; }
DEVI void lgkm0(unsigned long long& a) { asm volatile("s_waitcnt lgkmcnt(0)" : "+s"(a) :: "memory"); }
DEVI unsigned spack(unsigned lo, unsigned hi) { unsigned d; asm volatile("s_pack_ll_b32_b16 %0, %1, %2" : "=s"(d) : "s"(lo), "s"(hi)); return d; }
DEVI int lane() { int l; asm volatile("v_mbcnt_lo_u32_b32 %0, -1, 0\n\tv_mbcnt_hi_u32_b32 %0, -1, %0" : "=v"(l)); __builtin_assume(l >= 0 && l < 64); return l; }
DEVI void sleep_ticks(int d) { unsigned target = rclk() + d; while ((int)(rclk() - target) < 0) __builtin_amdgcn_s_sleep(4); }
DEVI float xor16max(float x) { auto r = __builtin_amdgcn_permlane16_swap(__float_as_uint(x), __float_as_uint(x), false, false); return __builtin_fmaxf(__uint_as_float(r[0]), __uint_as_float(r[1])); }
DEVI float xor32max(float x) { auto r = __builtin_amdgcn_permlane32_swap(__float_as_uint(x), __float_as_uint(x), false, false); return __builtin_fmaxf(__uint_as_float(r[0]), __uint_as_float(r[1])); }
DEVI float xor16add(float x) { auto r = __builtin_amdgcn_permlane16_swap(__float_as_uint(x), __float_as_uint(x), false, false); return __uint_as_float(r[0]) + __uint_as_float(r[1]); }
DEVI float xor32add(float x) { auto r = __builtin_amdgcn_permlane32_swap(__float_as_uint(x), __float_as_uint(x), false, false); return __uint_as_float(r[0]) + __uint_as_float(r[1]); }
DEVI float fold32(float a, float b) { auto r = __builtin_amdgcn_permlane32_swap(__float_as_uint(a), __float_as_uint(b), false, false); return __uint_as_float(r[0]) + __uint_as_float(r[1]); }
#define ROR16(x, n) __builtin_bit_cast(float, __builtin_amdgcn_update_dpp(0, __builtin_bit_cast(int, (x)), 0x120 + (n), 0xf, 0xf, false))
DEVI float row16sum(float x) { x += ROR16(x, 8); x += ROR16(x, 4); x += ROR16(x, 2); x += ROR16(x, 1); return x; }
DEVI unsigned z0() { unsigned z; asm volatile("v_mov_b32 %0, 0" : "=v"(z)); return z; }
DEVI void cnt_add(char* p) { __hip_atomic_fetch_add((GAS unsigned*)(p + z0()), z0() + 1u, __ATOMIC_RELAXED, __HIP_MEMORY_SCOPE_AGENT); }
DEVI unsigned cnt_get(char* p) { return __hip_atomic_load((GAS unsigned*)(p + z0()), __ATOMIC_RELAXED, __HIP_MEMORY_SCOPE_AGENT) + 1u; }
DEVI bool spin_fail(const Ctx& k, unsigned spins) {
    if (spins > SPIN_CAP || ((spins & 255u) == 255u && __builtin_amdgcn_readfirstlane(cnt_get(k.cm + CM_TMO)) != 0)) { cnt_add(k.cm + CM_TMO); return true; }
    return false;
}
DEVI void cnt_wait(const Ctx& k, char* c, unsigned need) {
_Pragma("unroll 1")
    for (unsigned spins = 0; (unsigned)__builtin_amdgcn_readfirstlane(cnt_get(c)) < need; ++spins) {
        __builtin_amdgcn_s_sleep(PSLEEP);
        if (spin_fail(k, spins)) break;
    }
    __builtin_amdgcn_fence(__ATOMIC_ACQUIRE, "wavefront");
}
DEVI unsigned xn_get(const Ctx& k, unsigned l16, int s) { return ld32(k.rs, opq(l16) & (k.c ? 128u : 0u), k.p0 + CNT(C_XN, k.c ? (s & 1) * 2 : s & 63)); }
DEVI bool xn_has(const Ctx& k, unsigned v, int s) { return __all((int)v >= (k.c ? s : 7)); }
DEVI void xn_wait(const Ctx& k, unsigned l16, int s) {
_Pragma("unroll 1")
    for (unsigned spins = 0; !xn_has(k, xn_get(k, l16, s), s); ++spins) {
        asm volatile("" ::: "memory");
        __builtin_amdgcn_s_sleep(2);
        if (spin_fail(k, spins)) break;
    }
    __builtin_amdgcn_fence(__ATOMIC_ACQUIRE, "wavefront");
}
template <int N> DEVI void prog_wait(const Ctx& k, unsigned a, int s) {
_Pragma("unroll 1")
    for (unsigned spins = 0; !__all((int)ld32(k.rs, min(k.l, N - 1) * 128, a) >= s); ++spins) {
        asm volatile("" ::: "memory");
        __builtin_amdgcn_s_sleep(PWS);
        if (spin_fail(k, spins)) break;
    }
    __builtin_amdgcn_fence(__ATOMIC_ACQUIRE, "wavefront");
}
template <int OFF> DEVI void dma1(const void* sbase, unsigned voff, unsigned lds) {
    asm volatile("s_add_i32 m0, %2, %3\n\ts_nop 0\n\tglobal_load_lds_dwordx4 %0, %1" :: "v"(voff), "s"(sbase), "s"(lds), "n"(OFF) : "memory", "scc");
}
DEVI void set_m0(unsigned lds) { asm volatile("s_mov_b32 m0, %0" :: "s"(lds) : "memory"); }
template <int OFF> DEVI void dmab(__amdgpu_buffer_rsrc_t rs, unsigned voff, unsigned soff) {
    asm volatile("buffer_load_dwordx4 %0, %1, %2 offen offset:%3 sc1 lds" :: "v"(voff), "s"(rs), "s"(soff), "n"(OFF) : "memory");
}
#define PIN(x) asm volatile("" : "+v"(x))
#define PINS(x) asm volatile("" : "+s"(x))
#define LEAD()
#define TOUCH(x) asm volatile("" :: "v"(x))
DEVI float csel(float a, float b, unsigned long long m) { float d; asm volatile("v_cndmask_b32_e64 %0, %1, %2, %3" : "=v"(d) : "v"(a), "v"(b), "s"(m)); return d; }
#define LO8(x) __builtin_shufflevector(x, x, 0, 1, 2, 3, 4, 5, 6, 7)
#define HI8(x) __builtin_shufflevector(x, x, 8, 9, 10, 11, 12, 13, 14, 15)
#define CAT8(a, b) __builtin_shufflevector(a, b, 0, 1, 2, 3, 4, 5, 6, 7, 8, 9, 10, 11, 12, 13, 14, 15)
template <bool AG> DEVI void smf(f32x4& c, const bf16x8& a, const bf16x16& w, unsigned ix) {
    if constexpr (AG) asm volatile("v_smfmac_f32_16x16x64_bf16 %0, %1, %2, %3" : "+v"(c) : "v"(a), "a"(w), "v"(ix));
    else asm volatile("v_smfmac_f32_16x16x64_bf16 %0, %1, %2, %3" : "+v"(c) : "v"(a), "v"(w), "v"(ix));
}
#define WTAB(X) X(0, "{a[0:7]}", "{a[0:3]}", "{a[4:7]}") X(1, "{a[8:15]}", "{a[8:11]}", "{a[12:15]}") X(2, "{a[16:23]}", "{a[16:19]}", "{a[20:23]}") X(3, "{a[24:31]}", "{a[24:27]}", "{a[28:31]}") X(4, "{a[32:39]}", "{a[32:35]}", "{a[36:39]}") X(5, "{a[40:47]}", "{a[40:43]}", "{a[44:47]}") X(6, "{a[48:55]}", "{a[48:51]}", "{a[52:55]}") X(7, "{a[56:63]}", "{a[56:59]}", "{a[60:63]}") X(8, "{a[64:71]}", "{a[64:67]}", "{a[68:71]}") X(9, "{a[72:79]}", "{a[72:75]}", "{a[76:79]}") X(10, "{a[80:87]}", "{a[80:83]}", "{a[84:87]}") X(11, "{a[88:95]}", "{a[88:91]}", "{a[92:95]}") X(12, "{a[96:103]}", "{a[96:99]}", "{a[100:103]}") X(13, "{a[104:111]}", "{a[104:107]}", "{a[108:111]}") X(14, "{a[112:119]}", "{a[112:115]}", "{a[116:119]}") X(15, "{a[120:127]}", "{a[120:123]}", "{a[124:127]}") X(16, "{a[128:135]}", "{a[128:131]}", "{a[132:135]}") X(17, "{a[136:143]}", "{a[136:139]}", "{a[140:143]}") X(18, "{a[144:151]}", "{a[144:147]}", "{a[148:151]}") X(19, "{a[152:159]}", "{a[152:155]}", "{a[156:159]}") X(20, "{a[160:167]}", "{a[160:163]}", "{a[164:167]}") X(21, "{a[168:175]}", "{a[168:171]}", "{a[172:175]}") X(22, "{a[176:183]}", "{a[176:179]}", "{a[180:183]}") X(23, "{a[184:191]}", "{a[184:187]}", "{a[188:191]}") X(24, "{a[192:199]}", "{a[192:195]}", "{a[196:199]}") X(25, "{a[200:207]}", "{a[200:203]}", "{a[204:207]}") X(26, "{a[208:215]}", "{a[208:211]}", "{a[212:215]}") X(27, "{a[216:223]}", "{a[216:219]}", "{a[220:223]}") X(28, "{a[224:231]}", "{a[224:227]}", "{a[228:231]}") X(29, "{a[232:239]}", "{a[232:235]}", "{a[236:239]}") X(30, "{a[240:247]}", "{a[240:243]}", "{a[244:247]}") X(31, "{a[248:255]}", "{a[248:251]}", "{a[252:255]}") X(32, "{v[96:103]}", "{v[96:99]}", "{v[100:103]}") X(33, "{v[104:111]}", "{v[104:107]}", "{v[108:111]}") X(34, "{v[112:119]}", "{v[112:115]}", "{v[116:119]}") X(35, "{v[120:127]}", "{v[120:123]}", "{v[124:127]}") X(36, "{v[128:135]}", "{v[128:131]}", "{v[132:135]}") X(37, "{v[136:143]}", "{v[136:139]}", "{v[140:143]}") X(38, "{v[144:151]}", "{v[144:147]}", "{v[148:151]}") X(39, "{v[152:159]}", "{v[152:155]}", "{v[156:159]}") X(40, "{v[160:167]}", "{v[160:163]}", "{v[164:167]}") X(41, "{v[168:175]}", "{v[168:171]}", "{v[172:175]}") X(42, "{v[176:183]}", "{v[176:179]}", "{v[180:183]}") X(43, "{v[184:191]}", "{v[184:187]}", "{v[188:191]}") X(44, "{v[192:199]}", "{v[192:195]}", "{v[196:199]}") X(45, "{v[200:207]}", "{v[200:203]}", "{v[204:207]}") X(46, "{v[208:215]}", "{v[208:211]}", "{v[212:215]}") X(47, "{v[216:223]}", "{v[216:219]}", "{v[220:223]}") X(48, "{v[224:231]}", "{v[224:227]}", "{v[228:231]}") X(49, "{v[232:239]}", "{v[232:235]}", "{v[236:239]}") X(50, "{v[240:247]}", "{v[240:243]}", "{v[244:247]}") X(51, "{v[248:255]}", "{v[248:251]}", "{v[252:255]}")
template <int ID> DEVI void smfw(f32x4& c, const bf16x8& a, const bf16x16& w, unsigned ix) {
#define X(i, P, L, H) if constexpr (ID == i) asm volatile("v_smfmac_f32_16x16x64_bf16 %0, %1, %2, %3" : "+v"(c) : "v"(a), P(w), "v"(ix));
    WTAB(X)
#undef X
}
template <int ID, int HF, bool WB, bool FIRST> DEVI void mfw(f32x4& c, const bf16x16& w, const bf16x8& x) {
#define Y(R, W8) { if constexpr (FIRST && WB) asm volatile("v_mfma_f32_16x16x32_bf16 %0, %2, %1, 0" : "=&v"(c) : R(W8), "v"(x)); \
    else if constexpr (FIRST) asm volatile("v_mfma_f32_16x16x32_bf16 %0, %1, %2, 0" : "=&v"(c) : R(W8), "v"(x)); \
    else if constexpr (WB) asm volatile("v_mfma_f32_16x16x32_bf16 %0, %2, %1, %0" : "+v"(c) : R(W8), "v"(x)); \
    else asm volatile("v_mfma_f32_16x16x32_bf16 %0, %1, %2, %0" : "+v"(c) : R(W8), "v"(x)); }
#define X(i, P, L, H) if constexpr (ID == i) { if constexpr (HF) Y(H, HI8(w)) else Y(L, LO8(w)) }
    WTAB(X)
#undef X
#undef Y
}
template <bool AG, bool FIRST> DEVI void mfa(f32x4& c, const bf16x8& w, const bf16x8& x) {
    if constexpr (AG && FIRST) asm volatile("v_mfma_f32_16x16x32_bf16 %0, %1, %2, 0" : "=&v"(c) : "a"(w), "v"(x));
    else if constexpr (AG) asm volatile("v_mfma_f32_16x16x32_bf16 %0, %1, %2, %0" : "+v"(c) : "a"(w), "v"(x));
    else if constexpr (FIRST) asm volatile("v_mfma_f32_16x16x32_bf16 %0, %1, %2, 0" : "=&v"(c) : "v"(w), "v"(x));
    else asm volatile("v_mfma_f32_16x16x32_bf16 %0, %1, %2, %0" : "+v"(c) : "v"(w), "v"(x));
}
template <bool AG, bool FIRST> DEVI void mfb(f32x4& c, const bf16x8& x, const bf16x8& w) {
    if constexpr (AG && FIRST) asm volatile("v_mfma_f32_16x16x32_bf16 %0, %1, %2, 0" : "=&v"(c) : "v"(x), "a"(w));
    else if constexpr (AG) asm volatile("v_mfma_f32_16x16x32_bf16 %0, %1, %2, %0" : "+v"(c) : "v"(x), "a"(w));
    else if constexpr (FIRST) asm volatile("v_mfma_f32_16x16x32_bf16 %0, %1, %2, 0" : "=&v"(c) : "v"(x), "v"(w));
    else asm volatile("v_mfma_f32_16x16x32_bf16 %0, %1, %2, %0" : "+v"(c) : "v"(x), "v"(w));
}
DEVI f32x4 mf(const bf16x8& a, const bf16x8& b, const f32x4& c) { return __builtin_amdgcn_mfma_f32_16x16x32_bf16(__builtin_bit_cast(hbf8, a), __builtin_bit_cast(hbf8, b), c, 0, 0, 0); }
template <int N, class F> DEVI void sfor(F f) { [&]<int... I>(std::integer_sequence<int, I...>) { (f(std::integral_constant<int, I>{}), ...); }(std::make_integer_sequence<int, N>{}); }
DEVI bf16x8 lds128(const char* p) { return *(const bf16x8*)p; }
DEVI bf16x4 ldstr(const char* p) { return __builtin_amdgcn_ds_read_tr16_b64_v4i16((LAS bf16x4*)p); }
DEVI bf16x8 ldstr2(const char* p, int d) { bf16x4 a = ldstr(p), b = ldstr(p + d); return __builtin_shufflevector(a, b, 0, 1, 2, 3, 4, 5, 6, 7); }

#define STAMP(s, k)
#define QSTAMP(f, i)
#define STOP(s)
#define PFLUSH(s)
#define PROF_DECL
#define RSTAMP(i)
#define PCOUNT(i)
#define PROF_OUT

#define HSTAMP STAMP
#define HFLUSH PFLUSH
#define HTRACE(s)
#define TROUT
#define TRHINT
#define AQ(m, i)
#define AC3(i, j, k) { }
#define AST(i)
#define AAC(i)
#define ACCM 0
#define HSTOP(m) if constexpr ((ACCM != 0) == m) { STOP(0) }
#define PC8(i, x)
#define PROF_ONLY(x)
#define PROF_S(x)
template <int NT, int OFF> DEVI void wp_issue(const u16* W, int ld, unsigned ring, int l) {
    sfor<NT>([&](auto I) { constexpr int i = decltype(I)::value;
        const int id = i * 64 + l, row = id / (2 * NT), cc = id % (2 * NT);
        dma1<OFF + i * 1024>(W, (unsigned)(row * ld + cc * 8) * 2u, ring); });
}
template <int NT, bool STD> DEVI bf16x8 wp_read(const char* slot, int T, int l) {
    const int row = (STD ? 8 : 4) * (l >> 4) + ((l >> 2) & 3);
    return ldstr2(slot + row * (NT * 32) + T * 32 + (l & 3) * 8, (STD ? 4 : 16) * NT * 32);
}
template <int NU, int NI, int D, class ISSUE, class READ> DEVI void wstream(char* ring, ISSUE issue, READ read) {
    const unsigned base = (unsigned)(size_t)(LAS char*)ring;
    sfor<D>([&](auto U) { constexpr int u = decltype(U)::value; if constexpr (u < NU) issue(U, base); });
    sfor<NU>([&](auto U) {
        constexpr int u = decltype(U)::value, left = (NU - u < D ? NU - u : D) - 1;
        asm volatile("s_waitcnt vmcnt(%0)" :: "n"(left * NI) : "memory");
        read(U, ring + (u % D) * NI * 1024);
        __builtin_amdgcn_s_waitcnt(0xc07f);
        asm volatile("" ::: "memory");
        if constexpr (u + D < NU) issue(std::integral_constant<int, u + D>{}, base);
    });
}

constexpr int L_XN = 0;
constexpr int L_K = 40960;
constexpr int L_MEM = L_K + 12288;
constexpr int L_QM = L_MEM + 10240;
constexpr int L_Q = L_QM + 12288;
constexpr int L_W = L_Q + 12288;
#define IC(x) std::integral_constant<int, (x)>{}
#define CV(X) decltype(X)::value
DEVI bf16x8 lr128(unsigned a) { return *(const LAS bf16x8*)(size_t)a; }
DEVI bf16x4 lrtr(unsigned a) { return __builtin_amdgcn_ds_read_tr16_b64_v4i16((LAS bf16x4*)(size_t)a); }
#define CAT4(a, b) __builtin_shufflevector(a, b, 0, 1, 2, 3, 4, 5, 6, 7)
DEVI void lw128(unsigned a, u32x4 v) { *(LAS u32x4*)(size_t)a = v; }
DEVI void lw64(unsigned a, u32x2 v) { *(LAS u32x2*)(size_t)a = v; }
DEVI void lw32(unsigned a, unsigned v) { *(LAS unsigned*)(size_t)a = v; }
template <int N> DEVI void bar_lgkm() { asm volatile("s_waitcnt lgkmcnt(%0)\n\ts_barrier" :: "n"(N) : "memory"); }
DEVI void atom1(__amdgpu_buffer_rsrc_t rs, unsigned voff, unsigned soff) {
    unsigned one; asm volatile("v_mov_b32 %0, 1\n\tbuffer_atomic_add %0, %1, %2, %3 offen" : "=&v"(one) : "v"(voff), "s"(rs), "s"(soff) : "memory");
}
DEVI unsigned vadd(unsigned v, unsigned sc) { unsigned d; asm volatile("v_add_u32 %0, %1, %2" : "=v"(d) : "s"(sc), "v"(v)); return d; }
DEVI float vzero() { float z; asm volatile("v_mov_b32 %0, 0" : "=v"(z)); return z; }
template <int B, int I> DEVI void xn_op(const Ctx& k, unsigned v, unsigned src, unsigned dst) {
    if constexpr (I == 0) set_m0(dst + B * 8192);
    else if constexpr (I == 1) dmab<0>(k.rs, v, src + B * 16384);
    else dmab<1024>(k.rs, v, src + B * 16384);
}
DEVI void xn_all(const Ctx& k, unsigned v, unsigned src, unsigned dst) {
    sfor<5>([&](auto B) { xn_op<CV(B), 0>(k, v, src, dst); asm volatile("s_nop 0"); xn_op<CV(B), 1>(k, v, src, dst); xn_op<CV(B), 2>(k, v, src, dst); });
}
struct Xn {
    unsigned src, dst, fa, fi, fx, sg, need, t, sb, se, l16, c, off;
    unsigned long long flip, m;
    DEVI void init(const Ctx& k) {
        src = k.seg(PG_XNA, 0) + k.w * 2048; dst = L_XN + k.w * 2048; sg = 0; off = ~0u;
        fa = k.p0 + CNT(C_XN, 0); fi = k.c ? 0u : 128u; fx = k.c ? 256u : 0u;
        const unsigned q = (unsigned)(k.par >> PG_XNA), d = q ^ (q >> 1);
        flip = ((unsigned long long)(d & 1) << 16) | ((unsigned long long)(d & 2) << 31) | ((unsigned long long)(d & 4) << 46);
    }
    template <int I> DEVI void next(const Ctx& k, unsigned l16) {
        if constexpr (I == 0) { sg += 1; src += 131072; PINS(sg); PINS(src); }
        if constexpr (I == 1) asm volatile("s_bitcmp1_b64 %1, %2\n\ts_cselect_b32 %0, 0x2000, 0" : "=s"(t) : "s"(flip), "s"(sg) : "scc");
        if constexpr (I == 2) { src ^= t; fa += fi; PINS(src); PINS(fa); }
        if constexpr (I == 3) { fa ^= fx; PINS(fa); asm volatile("v_and_b32 %0, %1, %2" : "=v"(c) : "s"(fx >> 1), "v"(l16)); }
        if constexpr (I == 4) c = ld32(k.rs, c, fa);
        if constexpr (I == 5) { need = k.c ? sg : 7u; PINS(need); }
        if constexpr (I == 6) { need = sg < 64u ? need : 0x7FFFFFFFu; PINS(need); }
        if constexpr (I == 7) asm volatile("v_cmp_gt_i32_e64 %0, %1, %2" : "=s"(m) : "s"(need), "v"(c) : "memory");
        if constexpr (I == 8) asm volatile("s_cmp_lg_u64 %2, 0\n\ts_cselect_b32 %0, -1, 0\n\ts_cselect_b32 %1, 0x200000, %3" : "=&s"(off), "=s"(se) : "s"(m), "s"(src) : "scc");
        if constexpr (I == 10) asm volatile("s_cmp_lg_u32 %1, 64\n\ts_cselect_b32 %0, %0, 0" : "+s"(off) : "s"(sg) : "scc");
    }
    template <int B, int I> DEVI void op(const Ctx& k) {
        if constexpr (I == 0) { set_m0(dst + B * 8192); sb = se + B * 16384; PINS(sb); }
        else dmab<(I - 1) * 1024>(k.rs, l16, sb);
    }
    template <int NB> DEVI void late(const Ctx& k, unsigned l16) {
        xn_wait(k, l16, sg);
        se = src;
        sfor<NB>([&](auto B) { op<CV(B), 0>(k); asm volatile("s_nop 0"); op<CV(B), 1>(k); op<CV(B), 2>(k); });
        DRAIN();
    }
};
template <int OFF> DEVI u32x4 ald128(__amdgpu_buffer_rsrc_t rs, unsigned v, unsigned s) { u32x4 d; asm volatile("buffer_load_dwordx4 %0, %1, %2, %3 offen offset:%4 sc1" : "=v"(d) : "v"(v), "s"(rs), "s"(s), "n"(OFF) : "memory"); return d; }
#define MADR(b) (L_MEM + ((b) < 6 ? ((b) - 1) * 2048 : ((b) - 6) * 2048 + 3072))
struct Gather {
    u32x4 p0, p1, v0, v1;
    unsigned mx, a, aw, b, base, wo, dst;
    unsigned long long bad;
    DEVI void init(const Ctx& k) { base = k.memf(0); wo = k.w * 1024; dst = L_MEM + k.w * 2048; }
    template <int I> DEVI void op(const Ctx& k, unsigned l16, int s) {
        if constexpr (I == 0) { a = ((unsigned)s << 15) & 0x1F8000u; PINS(a); }
        if constexpr (I == 1) { a += base; aw = a + wo; PINS(a); PINS(aw); }
        if constexpr (I == 2) { b = aw + 3072 + (k.w >> 1) * 8192; PINS(b); set_m0(dst); }
        if constexpr (I == 3) v0 = ald128<2048>(k.rs, l16, aw);
        if constexpr (I == 4) v1 = ald128<3072>(k.rs, l16, b);
        if constexpr (I == 5) dmab<2048>(k.rs, l16, aw);
        if constexpr (I == 6) dmab<3072>(k.rs, l16, b);
        if constexpr (I == 7) p0 = ald128<0>(k.rs, l16, a);
        if constexpr (I == 8) p1 = ald128<1024>(k.rs, l16, a);
    }
    template <int N> DEVI void wait() { asm volatile("s_waitcnt vmcnt(%4)" : "+v"(p0), "+v"(p1), "+v"(v0), "+v"(v1) : "n"(N) : "memory"); }
    template <int I> DEVI void fin(unsigned other) {
#define MX3 "v_max3_u32 %0, %0, %3, %4\n\tv_max3_u32 %0, %0, %5, %6"
        if constexpr (I == 0) asm volatile("v_max3_u32 %0, %7, %1, %2\n\t" MX3 : "=&v"(mx) : "v"(v0[0]), "v"(v0[1]), "v"(v0[2]), "v"(v0[3]), "v"(v1[0]), "v"(v1[1]), "s"(other));
        if constexpr (I == 1) asm volatile("v_max3_u32 %0, %0, %1, %2\n\t" MX3 : "+v"(mx) : "v"(v1[2]), "v"(v1[3]), "v"(p0[0]), "v"(p0[1]), "v"(p0[2]), "v"(p0[3]));
        if constexpr (I == 2) asm volatile("v_max3_u32 %1, %1, %2, %3\n\tv_max3_u32 %1, %1, %4, %5\n\tv_cmp_eq_u32_e64 %0, -1, %1" : "=s"(bad) : "v"(mx), "v"(p1[0]), "v"(p1[1]), "v"(p1[2]), "v"(p1[3]));
    }
    DEVI void also(unsigned other) { asm volatile("v_mov_b32 %0, %1" : "=v"(mx) : "s"(other)); }
    DEVI void all() {
        asm volatile("s_waitcnt vmcnt(0)\n\tv_max3_u32 %1, %1, %2, %3\n\tv_max3_u32 %1, %1, %4, %5\n\tv_max3_u32 %1, %1, %6, %7\n\tv_max3_u32 %1, %1, %8, %9\n\tv_max3_u32 %1, %1, %10, %11\n\tv_max3_u32 %1, %1, %12, %13\n\t"
            "v_max3_u32 %1, %1, %14, %15\n\tv_max3_u32 %1, %1, %16, %17\n\tv_cmp_eq_u32_e64 %0, -1, %1" : "=s"(bad) : "v"(mx), "v"(v0[0]), "v"(v0[1]), "v"(v0[2]), "v"(v0[3]), "v"(v1[0]), "v"(v1[1]), "v"(v1[2]), "v"(v1[3]),
            "v"(p0[0]), "v"(p0[1]), "v"(p0[2]), "v"(p0[3]), "v"(p1[0]), "v"(p1[1]), "v"(p1[2]), "v"(p1[3]) : "memory");
    }
    DEVI void check() { also(0); all(); asm volatile("s_nop 3"); }
    unsigned rv, rs; unsigned long long rt;
    template <int I> DEVI void ask(const Ctx& k, unsigned l16) {
        if constexpr (I == 0) asm volatile("v_and_b32 %0, 0xf0, %1\n\tv_lshlrev_b32 %0, 1, %0" : "=v"(rv) : "v"(l16));
        if constexpr (I == 1) asm volatile("buffer_load_dword %0, %0, %1, %2 offen sc1" : "+v"(rv) : "s"(k.rs), "s"(k.p0 + CNT(C_XB, 0)) : "memory");
    }
    template <int I> DEVI void red() {
        if constexpr (I == 0) asm volatile("s_waitcnt vmcnt(4)\n\tv_max_u32_dpp %0, %0, %0 quad_perm:[1,0,3,2] row_mask:0xf bank_mask:0xf" : "+v"(rv));
        if constexpr (I == 1) asm volatile("v_max_u32_dpp %0, %0, %0 quad_perm:[2,3,0,1] row_mask:0xf bank_mask:0xf" : "+v"(rv));
        if constexpr (I == 2) asm volatile("v_max_u32_dpp %0, %0, %0 row_ror:4 row_mask:0xf bank_mask:0xf" : "+v"(rv));
        if constexpr (I == 3) asm volatile("v_max_u32_dpp %0, %0, %0 row_ror:8 row_mask:0xf bank_mask:0xf" : "+v"(rv));
        if constexpr (I == 4) asm volatile("v_readfirstlane_b32 %0, %1" : "=s"(rs) : "v"(rv));
    }
    template <int I> DEVI void hold(unsigned step) {
        if constexpr (I == 0) rt = rt_async();
        if constexpr (I == 3) { PINS(rt); b = spack((unsigned)rt, step); }
        if constexpr (I == 4) asm volatile("s_sub_u32 %0, %1, %0" : "+s"(b) : "s"(rs) : "scc");
        if constexpr (I == 5) if (__builtin_expect(b < 128u, 0)) { while ((int)((rclk() - rs) << 16) < 0) { } }
    }
    template <int I> DEVI void at(const Ctx& k, unsigned l16) {
        if constexpr (I == 1) op<2>(k, l16, 0);
        if constexpr (I >= 2 && I < 15 && (I % 5 == 2 || I % 5 == 4)) op<3 + (I / 5) * 2 + (I % 5) / 4>(k, l16, 0);
    }
    DEVI void again(const Ctx& k, unsigned l16) {
        asm volatile("s_mov_b32 m0, %9\n\tbuffer_load_dwordx4 %2, %4, %5, %7 offen offset:2048 sc1\n\tbuffer_load_dwordx4 %3, %4, %5, %8 offen offset:3072 sc1\n\t"
            "buffer_load_dwordx4 %4, %5, %7 offen offset:2048 sc1 lds\n\tbuffer_load_dwordx4 %4, %5, %8 offen offset:3072 sc1 lds\n\t"
            "buffer_load_dwordx4 %0, %4, %5, %6 offen sc1\n\tbuffer_load_dwordx4 %1, %4, %5, %6 offen offset:1024 sc1\n\ts_waitcnt vmcnt(0)"
            : "+v"(p0), "+v"(p1), "+v"(v0), "+v"(v1) : "v"(l16), "s"(k.rs), "s"(a), "s"(aw), "s"(b), "s"(dst) : "memory");
        check();
    }
};
DEVI void clr2(f32x4& c) { f32x2 a, b; asm volatile("v_mov_b64 %0, 0\n\tv_mov_b64 %1, 0" : "=v"(a), "=v"(b)); c = __builtin_shufflevector(a, b, 0, 1, 2, 3); }
#define NOP7_3(a) asm volatile("s_nop 7" : "+v"(a[0]), "+v"(a[1]), "+v"(a[2]))

struct G3 {
    f32x4 a0[3], a1[3];
    bf16x8 x0[20], x1[20], wl[20];
    unsigned l16, lwv;
    template <int KS> DEVI void r0() { x0[KS] = lr128(l16 + L_XN + KS * 2048); }
    template <int KS> DEVI void r1() { x1[KS] = lr128(l16 + L_XN + KS * 2048 + 1024); }
    template <int KS> DEVI void rw() { if constexpr (KS >= 4) wl[KS] = lr128(lwv + (KS - 4) * 1024); }
    template <bool WL, int KS> DEVI void pre() { if constexpr (WL && KS >= 4) asm volatile("" :: "v"(wl[KS]), "v"(x0[KS]), "v"(x1[KS])); else asm volatile("" :: "v"(x0[KS]), "v"(x1[KS])); }
    template <int ID0, bool WL, int ks, int m> DEVI void mm(const bf16x16 (&W)[52]) {
        constexpr int b = ks >> 1, t = m % 3, id = ID0 + t * 10 + b;
        f32x4& c = m < 3 ? a0[t] : a1[t];
        const bf16x8& x = m < 3 ? x0[ks] : x1[ks];
        if constexpr (WL && t == 2 && b >= 2) { if constexpr (ID0 == 30) mfb<false, false>(c, x, wl[ks]); else mfa<false, false>(c, wl[ks], x); }
        else mfw<id % 52, ks & 1, ID0 == 30, ks == 0>(c, W[id % 52], x);
        SCHED0();
    }
    template <int ID0, bool WL, int K0, int K1, int PF, bool H6 = false, class HOOK> DEVI void run(const bf16x16 (&W)[52], HOOK hook) {
        sfor<K1 - K0>([&](auto KK) {
            constexpr int kk = CV(KK), ks = K0 + kk;
            sfor<6>([&](auto M) {
                constexpr int m = CV(M);
                mm<ID0, WL, ks, m>(W);
                if constexpr (m == 5 && ks + 1 < K1) { pre<WL, (ks + 1) % 20>(); SCHED0(); }
                if constexpr (ks + 2 < PF) {
                    if constexpr (m == 0) r0<(ks + 2) % 20>();
                    if constexpr (m == 1) r1<(ks + 2) % 20>();
                    if constexpr (m == 3 && WL) rw<(ks + 2) % 20>();
                }
                if constexpr (H6) hook(IC(kk * 6 + m));
                else { hook(IC(kk * 7 + (m < 5 ? m : 6))); if constexpr (m == 4) hook(IC(kk * 7 + 5)); }
                SCHED0();
            });
        });
    }
    template <int ID0, bool WL, int F0, int F1, int PF, class HOOK> DEVI void go(const bf16x16 (&W)[52], HOOK hook) {
        sfor<F1 - F0>([&](auto FF) {
            constexpr int f = F0 + CV(FF), ks = f / 6, m = f % 6;
            mm<ID0, WL, ks, m>(W);
            if constexpr (m == 5 && f + 1 < F1) { pre<WL, (ks + 1) % 20>(); SCHED0(); }
            if constexpr (ks + 2 < PF) rd<WL, (f + 12) % 120>();
            hook(IC(f));
            SCHED0();
        });
    }
    template <int P> DEVI void home() {
        if constexpr (P == 0) asm volatile("" : "+{v[12:15]}"(a0[0]), "+{v[16:19]}"(a0[1]), "+{v[20:23]}"(a0[2]), "+{v[24:27]}"(a1[0]), "+{v[28:31]}"(a1[1]), "+{v[32:35]}"(a1[2]));
        else asm volatile("" : "+{v[36:39]}"(a0[0]), "+{v[40:43]}"(a0[1]), "+{v[44:47]}"(a0[2]), "+{v[48:51]}"(a1[0]), "+{v[52:55]}"(a1[1]), "+{v[56:59]}"(a1[2]));
    }
    template <bool WL, int F> DEVI void rd() {
        if constexpr (F % 6 == 0) r0<F / 6>();
        if constexpr (F % 6 == 1) r1<F / 6>();
        if constexpr (F % 6 == 3 && WL) rw<F / 6>();
    }
    u32x2 o;
    template <int I, int OFF> DEVI void out4(unsigned d0) {
        if constexpr (I >= 0 && I < 24) {
            constexpr int t = I / 8, rt = (I / 4) & 1, j = I & 1;
            const f32x4& a = rt ? a1[t] : a0[t];
            if constexpr (I % 4 < 2) { o[j] = cvtpk(a[2 * j], a[2 * j + 1]); PIN(o[j]); }
            else lw32(d0 + OFF + rt * 6144 + t * 1024 + j * 4, o[j]);
        }
    }
    template <int I, int OFF> DEVI void out(unsigned d0, unsigned d1) {
        if constexpr (I >= 0 && I < 18) {
            constexpr int t = I / 6, rt = (I / 3) & 1;
            const f32x4& a = rt ? a1[t] : a0[t];
            if constexpr (I % 3 == 0) { o[0] = cvtpk(a[0], a[1]); PIN(o[0]); }
            else if constexpr (I % 3 == 1) { o[1] = cvtpk(a[2], a[3]); PIN(o[1]); }
            else if constexpr (OFF > 0) lw64(d0 + (OFF - 1) + rt * 6144 + t * 1024, o);
            else lw64((t == 1 ? d1 : d0) + OFF + rt * 6144 + (t == 2 ? 1024 : 0), o);
        }
    }
};
#define SPIX(l) (((l) & 2) ? 0xEEEEu : 0x4444u)
template <int I> DEVI void fold_op(f32x4& a, unsigned& o) {
    if constexpr (I == 0) asm volatile("v_add_f32 %0, %0, %1" : "+v"(a[0]) : "v"(a[2]));
    if constexpr (I == 1) asm volatile("v_add_f32 %0, %0, %1" : "+v"(a[1]) : "v"(a[3]));
    if constexpr (I == 2) asm volatile("v_cvt_pk_bf16_f32 %0, %1, %2" : "=v"(o) : "v"(a[0]), "v"(a[1]));
}

template <int J> DEVI void fold2(f32x4& a, f32x4& b, unsigned (&o)[2]) {
    if constexpr (J < 2) asm volatile("v_add_f32 %0, %0, %2\n\tv_add_f32 %1, %1, %3" : "+v"(a[J]), "+v"(b[J]) : "v"(a[J + 2]), "v"(b[J + 2]));
    else asm volatile("v_cvt_pk_bf16_f32 %0, %2, %3\n\tv_cvt_pk_bf16_f32 %1, %4, %5" : "=&v"(o[0]), "=&v"(o[1]) : "v"(a[0]), "v"(a[1]), "v"(b[0]), "v"(b[1]));
}
DEVI unsigned vshl1(unsigned x, unsigned b) { unsigned d; asm volatile("v_lshl_or_b32 %0, %1, 1, %2" : "=v"(d) : "s"(x), "n"(b)); return d; }

#define ANNOUNCE(s, now, d) (((unsigned)(s) << 16) | (((unsigned)(now) + (d)) & 0xFFFFu))

struct Soft {
    unsigned long long mA, mY, mC[8];
    unsigned v0, v1;
    unsigned sg, hs;
    float a[6], m, M, t, s, h; u32x2 pk;
    template <int I> DEVI void mask(f32x4 (&sc)[3]) {
        if constexpr (I == 0) asm volatile("v_mov_b32 %0, 0xfe800000" : "=v"(t));
        if constexpr (I >= 1 && I < 9) { float d; asm volatile("v_cndmask_b32_e64 %0, %1, 0, %2" : "=v"(d) : "v"(t), "s"(mC[I - 1])); sc[(I - 1) >> 2][(I - 1) & 3] = d; }
    }
    template <int I> DEVI void op(const Ctx& k, f32x4 (&sc)[3], unsigned a0, unsigned a1, unsigned hv) {
#define FMA(x, y) "v_fmamk_f32 " x ", " x ", 0x3dd53b95, " y "\n\t"
#define EXPC(x) "v_exp_f32_e64 " x ", " x " clamp\n\t"
#define OP3(o, d, x, y) o " " d ", " x ", " y "\n\t"
#define SA(txt) asm volatile(txt : "+v"(a[0]), "+v"(a[1]), "+v"(a[2]), "+v"(a[3]), "+v"(m), "+v"(t))
#define SB(txt) asm volatile(txt : "+v"(a[0]), "+v"(a[1]), "+v"(a[2]), "+v"(a[3]), "+v"(m), "+v"(t), "+v"(s), "+v"(a[4]), "+v"(a[5]), "+v"(M), "+v"(h) : "s"(hs), "s"(sg))
#define ADD(d, x, y) OP3("v_add_f32", d, x, y)
#define MUL(d, x, y) OP3("v_mul_f32", d, x, y)
#define CND(d, x, y, c) "v_cndmask_b32_e64 " d ", " x ", " y ", " c "\n\t"
        if constexpr (I == 0) asm volatile("v_max3_f32 %0, %1, %2, %3\n\tv_max3_f32 %0, %0, %4, %5" : "=&v"(m) : "v"(sc[0][0]), "v"(sc[0][1]), "v"(sc[0][2]), "v"(sc[0][3]), "v"(sc[1][0]));
        if constexpr (I == 1) asm volatile("v_max3_f32 %0, %0, %1, %2" : "+v"(m) : "v"(sc[1][1]), "v"(sc[1][2]));
        if constexpr (I == 2) asm volatile(CND("%0", "%2", "%3", "%6") CND("%1", "%4", "%5", "%6") : "=&v"(a[2]), "=&v"(a[3]) : "v"(sc[0][2]), "v"(sc[1][0]), "v"(sc[0][3]), "v"(sc[1][1]), "s"(mY));
        if constexpr (I == 3) asm volatile("v_max_f32 %1, %1, %5\n\t" CND("%0", "%2", "%3", "%4") : "=&v"(a[0]), "+v"(m) : "v"(sc[0][0]), "v"(a[2]), "s"(mA), "v"(sc[1][3]));
        if constexpr (I == 4) asm volatile("v_mul_f32 %0, 0xbdd53b95, %1" : "=v"(t) : "v"(m));
        if constexpr (I == 5) asm volatile(CND("%0", "%2", "%3", "%5") CND("%1", "%1", "%4", "%5") : "=&v"(a[1]), "+v"(a[2]) : "v"(sc[0][1]), "v"(a[3]), "v"(sc[1][2]), "s"(mA));
        if constexpr (I == 6) asm volatile(CND("%0", "%0", "%2", "%3") FMA("%1", "%4") : "+v"(a[3]), "+v"(a[0]) : "v"(sc[1][3]), "s"(mA), "v"(t));
        if constexpr (I == 7) SA(FMA("%1", "%5"));
        if constexpr (I == 8) SA(FMA("%2", "%5") FMA("%3", "%5"));
        if constexpr (I == 9) SA(EXPC("%0"));
        if constexpr (I == 10) { asm volatile("v_max3_f32 %0, %1, %2, %3" : "=v"(M) : "v"(m), "v"(sc[2][0]), "v"(sc[2][1]), "v"(sc[2])); a[4] = sc[2][0]; a[5] = sc[2][1]; }
        if constexpr (I == 11) asm volatile("v_mov_b32 %0, %3\n\tv_mov_b32 %1, %4" : "=&v"(t), "=&v"(h), "=v"(s) : "v"(M), "s"(hv));
        if constexpr (I == 12) SB("v_permlane16_swap_b32 %9, %5");
        if constexpr (I == 13) SB(OP3("v_max_f32", "%9", "%9", "%5"));
        if constexpr (I == 14) SB(EXPC("%1"));
        if constexpr (I == 15) SB("v_mov_b32 %5, %9\n\t" ADD("%6", "%0", "%1"));
        if constexpr (I == 16) SB("v_permlane32_swap_b32 %9, %5");
        if constexpr (I == 17) SB(OP3("v_max_f32", "%9", "%9", "%5") "v_mul_f32 %9, 0xbdd53b95, %9");
        if constexpr (I == 18) SB(FMA("%7", "%9") FMA("%8", "%9"));
        if constexpr (I == 19) SB(FMA("%4", "%9"));
        if constexpr (I == 20) SB(EXPC("%2"));
        if constexpr (I == 21) SB(EXPC("%3"));
        if constexpr (I == 22) SB(ADD("%6", "%6", "%2"));
        if constexpr (I == 23) SB(EXPC("%7"));
        if constexpr (I == 24) SB(EXPC("%8"));
        if constexpr (I == 25) SB(ADD("%6", "%6", "%3"));
        if constexpr (I == 26) SB(EXPC("%4"));
        if constexpr (I == 27) SB(MUL("%6", "%12", "%6") ADD("%5", "%7", "%8"));
        if constexpr (I == 28) SB(MUL("%6", "%6", "%4"));
        if constexpr (I == 29) SB(OP3("v_fmac_f32", "%6", "%11", "%5"));
        if constexpr (I == 30) SB("v_fmac_f32_dpp %6, %6, %10 row_ror:8 row_mask:0xf bank_mask:0xf\n\tv_mov_b32 %5, %6");
        if constexpr (I == 31) SB("v_permlane16_swap_b32 %6, %5");
        if constexpr (I == 32) SB(ADD("%6", "%6", "%5") "v_mov_b32 %5, %6");
        if constexpr (I == 33) SB("v_permlane32_swap_b32 %6, %5");
        if constexpr (I == 34) SB(ADD("%6", "%6", "%5"));
        if constexpr (I == 35) SB("v_rcp_f32 %6, %6");
        if constexpr (I == 36) SB(MUL("%4", "%4", "%6") MUL("%7", "%7", "%6"));
        if constexpr (I == 37) SB(MUL("%0", "%0", "%4"));
        if constexpr (I == 38) SB(MUL("%1", "%1", "%4") MUL("%2", "%2", "%4"));
        if constexpr (I == 39) { unsigned x; asm volatile(MUL("%1", "%1", "%4") "v_cvt_pk_bf16_f32 %0, %2, %3" : "=&v"(x), "+v"(a[3]) : "v"(a[0]), "v"(a[1]), "v"(m)); pk[0] = x; }
        if constexpr (I == 40) { unsigned x; asm volatile("v_cvt_pk_bf16_f32 %0, %1, %2" : "=v"(x) : "v"(a[2]), "v"(a[3])); pk[1] = x; }
        if constexpr (I == 41) { st64(k.rs, v0, a0, pk); SCHED0(); asm volatile("v_mul_f32 %1, %1, %2\n\tv_cvt_pk_bf16_f32 %0, %0, %1" : "+v"(a[4]), "+v"(a[5]) : "v"(s)); }
        if constexpr (I == 42) st32(k.rs, v1, a1, __float_as_uint(a[4]));
    }
};
#define QIDX(t, b) ((t) * 10 + (b))
DEVI void qk_role(const Params& p, const Ctx& k, char* smem, int h) {
    PROF_DECL
    const int w = k.w;
    sleep_ticks(k.c ? WD1 + QDLY : QDLY);
    bf16x16 W[52];
    {
        const int l = k.l;
        bf16x8 lo[6], wt[16];
        const int col = h * 192 + w * 48;
        wstream<20, 6, 6>(smem + w * 36864,
            [&](auto U, unsigned sl) { constexpr int u = CV(U);
                wp_issue<3, (u % 6) * 6144>(p.Wk + (size_t)u * 32 * 6144 + col, 6144, sl, l); wp_issue<3, (u % 6) * 6144 + 3072>(p.Wq + (size_t)u * 32 * 6144 + col, 6144, sl, l); },
            [&](auto U, char* sp) { constexpr int u = CV(U), b = u >> 1;
#pragma unroll
                for (int t = 0; t < 6; ++t) {
                    bf16x8 f = wp_read<3, true>(sp + (t / 3) * 3072, t % 3, l);
                    if (t == 5 && b >= 2) wt[(u - 4) & 15] = f;
                    else if (u & 1) W[QIDX(t, b) % 52] = CAT8(lo[t], f);
                    else lo[t] = f;
                } });
        RAWBAR()
#pragma unroll
        for (int i = 0; i < 16; ++i) *(bf16x8*)(smem + L_W + w * 16384 + i * 1024 + l * 16) = wt[i];
    }
    RSTAMP(2)
    Gather G;
    Soft S;
    const int hw = h >> 3, hh = h & 7;
    unsigned l16, ix, dqm, dq, trp, qp;
    {
        const int l = k.l, g = l >> 4, n = l & 15, r = n & 7, u = w & 1;
        const bool hf = n >= 8, dup = w != 1;
        S.mA = __ballot(dup && hf);
        const int lim = (w == 0 ? 64 : w == 1 ? n : 16 + 8 * u + r) - 4 * g;
#pragma unroll
        for (int i = 0; i < 8; ++i) S.mC[i] = __ballot((i & 3) + (i >> 2) * 16 <= lim);
        const bool okm = !(dup && hf);
        S.mY = __ballot((dup && hf) == (w != 0));
        S.v0 = w == 0 ? hh * 512 + ((g >> 1) * 16 + (r >> 1) * 4 + hf * 2 + (r & 1)) * 16 + (g & 1) * 8 : w == 1 ? (hh >> 1) * 1024 + l * 16 + (hh & 1) * 8 : hh * 1024 + (g * 16 + 8 * u + r) * 16 + hf * 8;
        S.v1 = !okm ? OOB : w == 0 ? 4096 + hh * 128 + r * 16 + g * 4 : (w == 1 ? 4096 + n * 16 : 6144 + (8 * u + r) * 16) + (hh >> 1) * 512 + (g >> 1) * 256 + (hh & 1) * 8 + (g & 1) * 4;
        l16 = l * 16; ix = SPIX(l);
        const int uu = u ^ ((n >> 2) & 1);
        dqm = L_QM + (w >> 1) * 3072 + uu * 512 + (g >> 1) * 128 + n * 8 + (g & 1) * 4;
        dq = L_QM + 6144 + (w >> 1) * 3072 + uu * 512 + g * 128 + ((n * 8) ^ ((g >> 1) * 64));
        trp = L_QM + 6144 + (l & 3) * 128 + (((l >> 2) * 8) ^ ((l & 2) * 32));
        qp = w == 1 ? trp + 6144 : (w == 0 ? L_QM : L_Q + 6144 + u * 256) + (l & 1) * 128 + (((l >> 2) * 8) ^ (w == 3 ? 64 : 0));
        u32x8 rg = {S.v0, S.v1, l16, ix, dqm, dq, trp, qp};
        asm volatile("" : "+{v[0:7]}"(rg));
        S.v0 = rg[0]; S.v1 = rg[1]; l16 = rg[2]; ix = rg[3]; dqm = rg[4]; dq = rg[5]; trp = rg[6]; qp = rg[7];
    }
    const unsigned po0 = k.p0 + PR_B + hw * 49152 + (w == 0 ? 0 : w == 1 ? 32768 : 16384), po1 = k.p0 + PR_B + hw * 49152 + (w == 0 ? 0 : 32768);
    const unsigned pdum = k.pg(1) + DUMMY_B, pbc = w == 0 ? k.p0 + CNT(C_BC, hw * 16 + hh) : pdum + 65536 + h * 512 + w * 128;
    Xn X;
    X.init(k); G.init(k);
    X.l16 = l16; X.se = 0;
    unsigned long long now = 0, done = 0;
    unsigned a0 = pdum + (po0 - k.p0 - PR_B), a1 = pdum + (po1 - k.p0 - PR_B);
    const unsigned hv = w == 1 ? 0u : 0x3F800000u, hw2 = w == 1 ? 0u : 0x00800000u;
    S.sg = 0x3F800000u; S.hs = 0x3F800000u;
    G3 A, B;
    A.l16 = l16; B.l16 = l16; B.lwv = vadd(l16, L_W + w * 16384);
#pragma unroll
    for (int t = 0; t < 3; ++t) { B.a0[t] = (f32x4){0.f, 0.f, 0.f, 0.f}; B.a1[t] = B.a0[t]; }
    f32x4 ac[6];
#pragma unroll
    for (int t = 0; t < 6; ++t) clr2(ac[t]);
    sfor<9>([&](auto I) { G.op<CV(I)>(k, l16, 0); });
    G.bad = 0;
    unsigned an = 0;
    do {
        if (X.off != 0) {
            PC8(1, X.sg)
            X.late<2>(k, l16);
            G.check();
        }
_Pragma("unroll 1")
        for (unsigned spins = 0; G.bad != 0 && !spin_fail(k, spins); ++spins) { PC8(0, X.sg) G.again(k, l16); }
        HSTOP(0) HSTAMP(0, 0)
        clr2(ac[5]);
        asm volatile("s_nop 1" : "+v"(ac[5]));
        smfw<50>(ac[5], __builtin_bit_cast(bf16x8, G.p0), W[50], ix);
    do {
        PROF_S(X.sg - 1)
        f32x4 sc[3];
        {
            bf16x8 m[16], wa[10], wb[10], kf[12];
            bf16x4 q[12], km[12];
            unsigned fo[6];
            m[0] = __builtin_bit_cast(bf16x8, G.p0); m[1] = __builtin_bit_cast(bf16x8, G.p1);
            auto put = [&](auto T) { constexpr int t = CV(T); if constexpr (t < 3) lw32(dq + t * 1024, fo[t]); else lw32(dqm + (t - 3) * 1024, fo[t]); };
            auto rd = [&](auto C) { constexpr int c = CV(C), j = c % 20;
                if constexpr (c < 0) { }
                else if constexpr (c < 20) m[j] = lr128(l16 + MADR(j < 10 ? j : j - 8));
                else if constexpr (c < 40) wa[j] = lr128(B.lwv + (j - 2) * 2048);
                else if constexpr (c < 60) wb[j] = lr128(B.lwv + (j - 2) * 2048 + 1024);
                else if constexpr (c < 80) kf[j] = lr128(l16 + L_K + ((j & 1) * 6 + j / 2) * 1024);
                else if constexpr (c < 100) q[j] = lrtr(qp + j * 512);
                else if constexpr (c < 120) km[j] = lrtr(trp + j * 512);
                else if constexpr (c < 140) A.r0<j>();
                else A.r1<j>(); };
#define PB(i) ((i) < 12 ? (i) / 6 : (i) < 32 ? (i) / 4 - 1 : (i) < 44 ? 7 + ((i) - 32) % 3 : (i) < 54 ? 7 + ((i) - 44) / 2 : 12 + ((i) - 54) % 3)
#define PT(i) ((i) < 12 ? ((i) + 5) % 6 : (i) < 32 ? (3 + (i) % 4) % 6 : (i) < 44 ? (3 + ((i) - 32) / 3) % 6 : (i) < 54 ? 1 + ((i) & 1) : 1 + ((i) - 54) / 3)
            sfor<82>([&](auto I) {
                constexpr int i = CV(I) + 1, b = PB(i), t = PT(i), bn = PB(i + 1);
                constexpr int rt[83] = {-1, 2, -1, -1, -1, 22, 42, 3, -1, 23, 43, 4, -1, 24, 44, 5, -1, 25, 45, 6,
                    -1, 26, 46, 7, -1, 8, 9, 27, 47, 28, 48, 29, 49, -1, -1, 60, -1, 61, 62, 63,
                    -1, 10, 64, -1, 11, 12, -1, 13, 14, -1, -1, 80, 82, 83, 84, 85, 86, 87, 67, 88,
                    89, 69, -1, 91, -1, 71, -1, 121, -1, 100, 102, 104, 106, 108, 110, -1, 140, -1, -1, 141, -1, -1, 122};
                constexpr int r2[83] = {-1, -1, -1, -1, -1, -1, -1, -1, -1, -1, -1, -1, -1, -1, -1, -1, -1, -1, -1, -1,
                    -1, -1, -1, -1, -1, -1, -1, -1, -1, -1, -1, -1, -1, -1, -1, -1, -1, -1, -1, -1,
                    -1, -1, -1, -1, -1, -1, -1, -1, -1, -1, -1, 81, -1, 65, -1, 66, -1, -1, -1, -1,
                    68, 90, -1, 70, -1, 120, -1, -1, -1, 101, 103, 105, 107, 109, 111, -1, -1, -1, -1, -1, -1, -1, -1};
                if constexpr (i < 60) {
                    if constexpr (t == 5 && b >= 2) smf<false>(ac[5], m[b], CAT8(wa[b], wb[b]), ix);
                    else smfw<QIDX(t, b < 10 ? b : b - 8) % 52>(ac[t], m[b], W[QIDX(t, b < 10 ? b : b - 8) % 52], ix);
                } else if constexpr (i < 72) { constexpr int j = i - 60; mfa<false, false>(sc[j & 1], kf[j], CAT4(q[j & ~1], q[j | 1])); }
                else if constexpr (i < 77) A.mm<0, false, (i - 72) / 3, (i - 72) % 3>(W);
                else { constexpr int ks = i - 77; mfa<false, ks == 0>(sc[2], CAT4(km[2 * ks], km[2 * ks + 1]), CAT4(q[2 * ks], q[2 * ks + 1])); }
                SCHED0();
                if constexpr (i == 1) { AQ(3, 3) asm volatile("s_barrier" ::: "memory"); AQ(1, 2) AQ(3, 4) }
                else if constexpr (i == 50) { BARL(3); AQ(1, 3) }
                else if constexpr (i == 68) { BARL(3); AQ(1, 4) QSTAMP(0, 1) }
                else if constexpr (i >= 12 && i < 59) { if constexpr (PT(i + 1) == 5) TOUCH(wb[bn % 10]); TOUCH(m[bn]); }
                else if constexpr (i >= 59 && i < 71) { asm volatile("" :: "v"(kf[(i - 59) % 12]), "v"(q[((i - 59) | 1) % 12])); }
                else if constexpr (i == 71) TOUCH(A.x0[0]);
                else if constexpr (i == 74) TOUCH(A.x0[1]);
                else if constexpr (i >= 76 && i < 82) TOUCH(km[(2 * (i - 76) + 1) % 12]);
                if constexpr (i == 8 || i == 30) asm volatile("" :: "{v[60:63]}"(ac[0]), "{v[28:31]}"(ac[1]), "{v[32:35]}"(ac[2]), "{v[64:67]}"(ac[3]), "{v[68:71]}"(ac[4]), "{v[72:75]}"(ac[5]));
                if constexpr (i == 14) AC3(0, 0, 0)
                if constexpr (i == 16) AC3(1, 6, 1)
                if constexpr (i == 18) AC3(2, 7, 2)
                if constexpr (i == 20) AC3(-1, -1, 3)
                if constexpr (i == 22) AC3(-1, -1, 4)
                if constexpr (i == 24) AC3(-1, -1, 5)
                if constexpr (i == 66) AC3(3, -1, 6)
                if constexpr (i == 2) { now = rt_async(); HSTOP(1) }
                if constexpr (i == APOS) { PINS(now); an += (unsigned)now; PINS(an); }
                if constexpr (i == APOS + 1) an = spack(an, X.sg);
                SCHED0();
                rd(IC(rt[i])); rd(IC(r2[i]));
                if constexpr (i == APOS + 4) st32(k.rs, 0, pbc, fo[1]);
                if constexpr (i >= 40 && i < 50 && i % 3 == 1) put(IC(((i - 31) / 3) % 6));
                if constexpr (i == 62 || i == 64) put(IC(i / 2 - 30));
                SCHED0();
                if constexpr (i < 25 && i % 4 != 1) B.out<((i - 2) / 4) * 3 + (i - 2) % 4, 6145>(dq, dq);
                if constexpr (i == APOS + 3) { fo[1] = an; PIN(fo[1]); }
                if constexpr (i >= 37 && i < 49) { constexpr int tt = ((i - 28) / 3) % 6; fold_op<(i - 37) % 3>(ac[tt], fo[tt]); }
                if constexpr (i >= 59 && i < 62) fold_op<i - 59>(ac[1], fo[1]);
                if constexpr (i == 62) fold_op<0>(ac[2], fo[2]);
                if constexpr (i == 63) asm volatile("v_add_f32 %1, %1, %2\n\tv_cvt_pk_bf16_f32 %0, %3, %1" : "=v"(fo[2]), "+v"(ac[2][1]) : "v"(ac[2][3]), "v"(ac[2][0]));
                if constexpr (i == 33) S.mask<0>(sc);
                if constexpr (i >= 51 && i < 59) S.mask<i - 50>(sc);
                if constexpr (i >= 75) S.op<i - 75>(k, sc, a0, a1, hv);
                if constexpr (i == 12) { AQ(3, 5) }
                if constexpr (i == 43) { AQ(3, 6) }
                if constexpr (i == 59) { AQ(3, 7) }
                SCHED0();
            });
        }
        QSTAMP(0, 2)
        {
            unsigned d0;
            auto rest = [&](auto J) { constexpr int j = CV(J);
                if constexpr (j >= 0 && j < 12 && (j % 6 == 0 || j % 3 == 2)) X.op<3 + j / 6, (j % 6 + 1) / 3>(k); };
            sfor<43>([&](auto F) { constexpr int f = CV(F) + 5;
                constexpr int e = f < 6 ? f + 3 : f < 9 ? f - 3 : f;
                A.mm<0, false, e / 6, e % 6>(W);
                if constexpr (f % 6 == 2 || f == 5) TOUCH(A.x1[f / 6]);
                else if constexpr (f == 47) { TOUCH(B.x0[0]); AQ(1, 6) AQ(2, 1) }
                else if constexpr (f % 6 == 5) TOUCH(A.x0[f / 6 + 1]);
                if constexpr (f == 40) { done = rt_async(); QSTAMP(0, 3) AQ(1, 5) }
                if constexpr (f == 21) set_m0(X.dst + 2 * 8192);
                if constexpr (f == 24) { X.sb = X.se + 2 * 16384; PINS(X.sb); }
                if constexpr (f == 42) AC3(4, -1, 7)
                SCHED0();
                if constexpr (f % 6 == 4 && f < 36) A.r0<f / 6 + 2>();
                if constexpr (f % 6 == 1 && f < 42) A.r1<f / 6 + 1>();
                if constexpr (f >= 39 && f < 43) B.rd<true, ((f - 39) / 2) * 6 + (f - 39) % 2>();
                if constexpr (f == 26 || f == 31) X.op<2, f / 15>(k);
                SCHED0();
                S.op<f + 3>(k, sc, a0, a1, hv);
                rest(IC(f - 43));
                if constexpr (f == 44) B.lwv = vadd(l16, L_W + w * 16384);
                SCHED0(); });
            B.go<30, true, 0, 48, 8>(W, [&](auto F) { constexpr int f = CV(F);
                rest(IC(f + 5));
                if constexpr (f == 9) AC3(5, -1, -1)
                if constexpr (f == 2) { a0 = (X.sg & 3) * PR_SZ; PINS(a0); }
                if constexpr (f == 4) { a1 = a0 + po1; a0 += po0; PINS(a0); PINS(a1); }
                if constexpr (f == 8) { S.sg = (X.sg & 4) ? 0xBF800000u : 0x3F800000u; PINS(S.sg); }
                if constexpr (f == 10) { S.hs = S.sg - hw2; PINS(S.hs); }
                if constexpr (f == 14 || f == 16) G.op<f / 16>(k, l16, X.sg);
                if constexpr (f == 22 || f == 28 || f == 32) X.next<(f - 22) / 5>(k, l16);
                if constexpr (f == 35) asm volatile("s_waitcnt vmcnt(6)\n\ts_barrier" ::: "memory");
                if constexpr (f == 20) AC3(6, 1, -1)
                if constexpr (f == 26) A.home<0>();
                if constexpr (f >= 36) A.rd<false, f + 12>();
                if constexpr (f == 38 || f == 40 || f == 45 || f == 46) X.next<(f == 38 ? 5 : f == 40 ? 6 : f - 42)>(k, l16);
                if constexpr (f == 47) { A.pre<false, 8>(); AQ(1, 7) AQ(2, 2) } });
            A.go<0, false, 48, 120, 20>(W, [&](auto F) { constexpr int f = CV(F), i = f - XPOS;
                if constexpr (f == 56) B.home<1>();
                if constexpr (i == 0 || i == 2 || i == 6) X.next<7 + i / 2>(k, l16);
                if constexpr (i >= 3 && i < 15 && ((i - 3) % 6 == 0 || (i - 3) % 3 == 2)) X.op<(i - 3) / 6, ((i - 3) % 6 + 1) / 3>(k);
                if constexpr (f == 58) asm volatile("s_waitcnt vmcnt(1)\n\ts_barrier" ::: "memory");
                if constexpr (f == 57) AC3(7, 2, -1)
                G.ask<f - RPOS>(k, l16);
                if constexpr (f >= 108) B.rd<true, f - 60>();
                SCHED0();
                G.red<f - 110>();
                if constexpr (f == 116) asm volatile("v_bfe_u32 %0, %1, 8, 1\n\tv_lshl_or_b32 %0, %0, 3, %1" : "=&v"(d0) : "v"(l16));
                if constexpr (f == 118) asm volatile("v_xor_b32 %0, %1, %0" : "+v"(d0) : "s"(L_K + (w >> 1) * 3072 + (w & 1) * 8));
                if constexpr (f == 119) { B.pre<true, 8>(); AQ(2, 3) } });
            B.go<30, true, 48, 120, 20>(W, [&](auto F) { constexpr int f = CV(F), i = f - 48;
                constexpr int ko[26] = {0, 1, 2, 4, 3, 6, 5, 8, 7, 9, 10, 11, 12, 13, 14, 16, 15, 18, 17, 20, 19, 21, 22, 23, -1, -1};
                G.hold<f - GPOS + 5>(X.sg);
                if constexpr (i < 24) { SCHED0(); A.out4<ko[i % 26], 0>(d0); }
                G.at<f - GPOS>(k, l16);
                if constexpr (f == 114) { lgkm0(done); PINS(G.rt); }
                if constexpr (f == 115) { PINS(now); HTRACE(X.sg - 2) an = (unsigned)done - (unsigned)now; PINS(an); }
                if constexpr (f == 116) { an = min(an, 200u); PINS(an); }
                if constexpr (f == 117) { an += DQ; PINS(an); }
                SCHED0();
                if constexpr (i == 28) AC3(-1, 3, -1)
                if constexpr (i == 50) AC3(-1, 4, -1)
                if constexpr (i == 62) AC3(-1, 5, -1)
                if constexpr (i == 23) { AQ(2, 4) }
                if constexpr (i == 47) { AQ(2, 5) }
                if constexpr (i == 65) { AQ(2, 6) }
                if constexpr (f == 119) { AQ(2, 7) }
                if constexpr (f == 112) asm volatile("" :: "{v[12:15]}"(G.v0), "{v[16:19]}"(G.v1), "{v[20:23]}"(G.p0), "{v[24:27]}"(G.p1), "{v9}"(B.lwv) TRHINT);
                if constexpr (f == 119) G.also(X.off);
                if constexpr (f >= 113 && f < 119) clr2(ac[(f + 6 - 113 + 5) % 6]); });
            static_assert(GPOS >= 70 && GPOS <= 98 && XPOS % 6 == 2 && RPOS > XPOS && RPOS + 1 < XPOS + 5, "GPOS");
            QSTAMP(0, 4) QSTAMP(0, 5)
            HFLUSH(s)
        }
        AST(0)
        PROF_ONLY(G.wait<0>();)
        HSTOP(0) HSTAMP(s, 0) AQ(1, 1) AQ(3, 1)
        G.all();
        SCHED0();
        smfw<50>(ac[5], __builtin_bit_cast(bf16x8, G.p0), W[50], ix);
        AQ(3, 2)
        SCHED0();
    } while (G.bad == 0);
    } while (X.sg != 65u);
    PROF_OUT
}

#define OIDX(t, b) (22 + (t) * 3 + (b))
DEVI void vo_role(const Params& p, const Ctx& k, char* smem, int h) {
    PROF_DECL
    const int w = k.w;
    if (k.c) sleep_ticks(WD1);
    bf16x16 W[52];
    {
        const int l = k.l;
        bf16x8 lo[10], wt[16];
        const u16* wv = p.Wv + h * 192 + w * 48;
        const u16* wo = p.Wo + (size_t)h * 192 * 640 + w * 160;
        wstream<22, 6, 6>(smem + w * 36864,
            [&](auto U, unsigned sl) { constexpr int u = CV(U);
                if constexpr (u >= 12) { wp_issue<3, (u % 6) * 6144>(wv + (size_t)(u - 12) * 64 * 6144, 6144, sl, l); wp_issue<3, (u % 6) * 6144 + 3072>(wv + (size_t)((u - 12) * 64 + 32) * 6144, 6144, sl, l); }
                else { wp_issue<5, (u % 6) * 6144>(wo + (u >> 1) * 32 * 640 + (u & 1) * 80, 640, sl, l); dma1<(u % 6) * 6144 + 5120>(wo, l * 16, sl); } },
            [&](auto U, char* sp) { constexpr int u = CV(U);
                if constexpr (u >= 12) {
                    constexpr int b = u - 12;
#pragma unroll
                    for (int t = 0; t < 3; ++t) {
                        bf16x8 x = wp_read<3, true>(sp, t, l), y = wp_read<3, true>(sp + 3072, t, l);
                        if (t == 2 && b >= 2) { wt[(2 * (b - 2)) & 15] = x; wt[(2 * (b - 2) + 1) & 15] = y; } else W[(t * 10 + b) % 52] = CAT8(x, y);
                    }
                } else {
                    constexpr int ks = u >> 1, b = ks >> 1;
#pragma unroll
                    for (int i = 0; i < 5; ++i) {
                        const int t = (u & 1) * 5 + i;
                        bf16x8 f = wp_read<5, false>(sp, i, l);
                        if (ks & 1) W[OIDX(t, b)] = CAT8(lo[t], f); else lo[t] = f;
                    }
                } });
        RAWBAR()
#pragma unroll
        for (int i = 0; i < 16; ++i) *(bf16x8*)(smem + L_W + w * 16384 + i * 1024 + l * 16) = wt[i];
    }
    RSTAMP(2)
    unsigned l16, lwv, ix, dq0, trp, vo;
    {
        const int l = k.l, g = l >> 4, n = l & 15, q = (l >> 2) & 3, pp = l & 3;
        l16 = l * 16; lwv = l16 + L_W + w * 16384; ix = SPIX(l);
        dq0 = L_QM + (24 * w + (n >> 2) * 2 + (n & 1)) * 32 + g * 8 + ((n >> 1) & 1) * 4;
        trp = L_QM + (16 * (g >> 1) + 4 * (g & 1) + 8 * (q >> 1) + (q & 1)) * 32 + pp * 8;
        vo = EXP(h * 512) + (g >> 1) * 256 + n * 16 + (g & 1) * 8;
        u32x8 r = {dq0, trp, vo, ix, l16, lwv, 0u, 0u};
        PIN(r);
        dq0 = r[0]; trp = r[1]; vo = r[2]; ix = r[3]; l16 = r[4]; lwv = r[5];
    }
    const unsigned pdum = k.pg(1) + DUMMY_B, pbc = w == 0 ? k.p0 + CNT(C_BC, (h >> 3) * 16 + 8 + (h & 7)) : pdum + 98304 + h * 512 + w * 128;
    Gather G;
    G3 V;
    V.l16 = l16; V.lwv = lwv;
    unsigned long long now = 0, done = 0;
    f32x4 y[20];
    bf16x8 v[12];
    u32x4 o[10];
    o[7] = (u32x4){0u, 0u, 0u, 0u};
    clr2(y[17]);
#pragma unroll
    for (int i = 0; i < 12; ++i) v[i] = __builtin_bit_cast(bf16x8, o[7]);
    unsigned yb = k.yx(7, w) + EXP(h * 1024);
    G.mx = ~0u;
    Xn X;
    X.init(k); G.init(k);
    X.l16 = l16; X.se = 0;
    G.base -= 32768;
    const unsigned ypar = (unsigned)(k.par >> (PG_YX + (w >> 1))), yo = ((unsigned)(PG_YX + (w >> 1)) << 21) + (w & 1) * 1048576 + EXP(h * 1024);
    const unsigned ym0 = k.ym(0) + w * 163840, ym2 = k.ym(2) + w * 163840;
    unsigned ym = pdum + w * 163840, ys, t, c = ~0u, an = 0;
#define YT(u) ((u) < 10 ? 2 * ((u) % 5) : ((u) - 10) | 1)
#define YR(u) ((u) < 10 ? (u) / 5 : (u) & 1)
    auto yout = [&](auto U, auto J) { constexpr int u = CV(U), j = CV(J), t = YT(u), rt = YR(u);
        if constexpr (j == 0) { o[t][2 * rt] = cvtpk(y[u][0], y[u][1]); o[t][2 * rt + 1] = cvtpk(y[u][2], y[u][3]); TOUCH(o[t][2 * rt]); TOUCH(o[t][2 * rt + 1]); }
        if constexpr (j == 1 && rt) __builtin_amdgcn_raw_buffer_store_b128(o[t], k.rs, (int)l16, (int)yb, AUX_SC1);
        if constexpr (j == 2 && rt) { yb += t == 8 ? -7 * 65536 : 131072; PINS(yb); TOUCH(o[t]); } };
    auto ymm = [&](auto I) {
        constexpr int i = CV(I), ks = i % 6, u = i / 6, id = OIDX(YT(u), ks >> 1);
        mfw<id, ks & 1, true, ks == 0>(y[u], W[id], v[YR(u) * 6 + ks]);
        SCHED0();
        if constexpr (u > 0 && ks >= 2) yout(IC((u + 19) % 20), IC(ks - 2)); };
    do {
        PC8(1, c)
        X.late<5>(k, l16);
    do {
        PROF_S(X.sg - 1)
        f32x4 ac[3];
        asm volatile("s_waitcnt vmcnt(7)\n\ts_barrier" ::: "memory");
        SCHED0();
        sfor<13>([&](auto I) { constexpr int i = CV(I) + 107;
            ymm(IC(i));
            if constexpr (i == 107) { AST(5) st32(k.rs, 0, pbc + 4, G.mx); }
            if constexpr (i == 119) V.pre<true, 0>();
            G.ask<i - 112>(k, l16);
            if constexpr (i == 108) V.r0<0>();
            if constexpr (i == 109) V.r1<0>();
            if constexpr (i == 114) V.r0<1>();
            if constexpr (i == 115) V.r1<1>();
            SCHED0(); });
        HSTAMP(s, 4)
        V.run<0, true, 0, 16, 17, true>(W, [&](auto I) { constexpr int i = CV(I);
            if constexpr (i >= 2) yout(IC(19), IC(i - 2));
            if constexpr (i == 8) { t = (X.sg & 7) * 2; PINS(t); }
            if constexpr (i == 10) { yb = (ypar >> t) & 1; PINS(yb); }
            if constexpr (i == 14) { t = (t << 21) + yo; PINS(t); }
            if constexpr (i == 16) { yb = (yb << 13) + t; PINS(yb); }
            if constexpr (i == 20) { c = X.sg; PINS(c); }
            if constexpr (i == 22) { G.a = max(c, 1u) << 15; PINS(G.a); }
            if constexpr (i == 28) G.op<1>(k, l16, 0);
            if constexpr (i == 40) { an = min(an, 200u) + DV; PINS(an); }
            if constexpr (i == 32 || i == 34 || i == 38) X.next<(i - 31) / 3>(k, l16);
            if constexpr (i == 23 || i == 29) { AAC(4 + i / 29) }
            if constexpr (i == 83 || i == 89) { AAC(6 + i / 89) }
            if constexpr (i == YPOS) { AST(6) }
            if constexpr (i == YPOS + 1) { AST(7) }
            if constexpr (i == YPOS) { asm volatile("s_waitcnt vmcnt(0)\n\ts_barrier" ::: "memory"); }
            if constexpr (i == YPOS - 2) G.mx = vshl1(c, 0);
            if constexpr (i == YPOS + 1) st32(k.rs, 0, pbc + 4, G.mx);
            SCHED0();
            G.red<i - YPOS - 2>();
            G.hold<i - GPOSY * 6 + 5>(c);
            G.at<i - GPOSY * 6>(k, l16);
            if constexpr (i == 90) PINS(G.rt);
            if constexpr (i == 95) V.pre<true, 16>();
            if constexpr (i >= 92 && i < 95) clr2(ac[(i - 92) % 3]); });
        static_assert(GPOSY * 6 + 2 > YPOS + 1 && GPOSY <= 13, "GPOSY");
        HSTAMP(s, 5)
        HFLUSH(s - 1)
        f32x4 acc[10];
        f32x2 z[20];
        auto clr = [&](auto I) { constexpr int i = CV(I); asm volatile("v_mov_b64 %0, 0" : "=v"(z[i])); if constexpr (i & 1) acc[i >> 1] = __builtin_shufflevector(z[i - 1], z[i], 0, 1, 2, 3); };
        bf16x4 al[3], ah[3];
        unsigned d0, d1;
        {
            bf16x8 m[10], wa[10], wb[10];
            auto rd = [&](auto R) { constexpr int r = CV(R), b = 2 + r / 3;
                if constexpr (b < 10) {
                    if constexpr (r % 3 == 0) m[b] = lr128(l16 + MADR(b));
                    if constexpr (r % 3 == 1) wa[b] = lr128(lwv + (b - 2) * 2048);
                    if constexpr (r % 3 == 2) wb[b] = lr128(lwv + (b - 2) * 2048 + 1024);
                } else if constexpr (b < 13) {
                    if constexpr (r % 3 == 0) V.r0<b + 7>();
                    if constexpr (r % 3 == 1) V.rw<b + 7>();
                    if constexpr (r % 3 == 2) V.r1<b + 7>();
                } };
            AST(0)
            G.wait<0>();
            HSTOP(0) HSTAMP(s, 0) AST(1)
            V.run<0, true, 16, 17, 0, true>(W, [&](auto I) { constexpr int i = CV(I);
                G.fin<i>(0);
                if constexpr (i == 3) {
                    if (__builtin_expect(G.bad != 0, 0)) {
                        unsigned spins = 0;
_Pragma("unroll 1")
                        do { PC8(0, c) G.again(k, l16); } while (G.bad != 0 && !spin_fail(k, spins++));
                        HSTOP(0) HSTAMP(s, 0)
                    }
                    asm volatile("s_barrier" ::: "memory");
                    AST(2)
                }
                if constexpr (i == 4) { now = rt_async(); HSTOP(1) SCHED0(); }
                if constexpr (i >= 3) rd(IC(i - 3)); });
            m[0] = __builtin_bit_cast(bf16x8, G.p0); m[1] = __builtin_bit_cast(bf16x8, G.p1);
            sfor<30>([&](auto I) {
                constexpr int i = CV(I), b = i / 3, t = i % 3;
                if constexpr (t == 2 && b >= 2) smf<false>(ac[2], m[b], CAT8(wa[b], wb[b]), ix); else smfw<t * 10 + b>(ac[t], m[b], W[t * 10 + b], ix);
                SCHED0();
                if constexpr (i >= 6 && i < 29 && t != 0) { if constexpr (t == 1) TOUCH(wb[b]); else TOUCH(m[(b + 1) % 10]); }
                if constexpr (i == 29) V.pre<true, 17>();
                if constexpr (i == 2) { PINS(now); an += (unsigned)now; PINS(an); }
                if constexpr (i == 3) an = spack(an, c);
                SCHED0();
                if constexpr (i < 27) rd(IC(3 + i));
                if constexpr (i == 5) st32(k.rs, 0, pbc, d0);
                SCHED0();
                if constexpr (i == 4) { d0 = an; PIN(d0); }
                if constexpr (i == 26) X.next<3>(k, l16);
                if constexpr (i == 28) d0 = vadd(l16, L_K + ((3 * w) >> 1) * 1024 + ((3 * w) & 1) * 8);
                if constexpr (i == 29) d1 = vadd(l16, L_K + ((3 * w + 1) >> 1) * 1024 + ((3 * w + 1) & 1) * 8);
                SCHED0();
            });
            unsigned fo[3];
            V.run<0, true, 17, 19, 0, true>(W, [&](auto I) { constexpr int i = CV(I);
                if constexpr (i < 3) { rd(IC(30 + i)); SCHED0(); fold_op<0>(ac[i], fo[i]); }
                if constexpr (i == 3 || i == 4 || i == 6) { constexpr int t = i / 2 - 1; asm volatile("v_add_f32 %1, %1, %2\n\tv_cvt_pk_bf16_f32 %0, %3, %1" : "=v"(fo[t]), "+v"(ac[t][1]) : "v"(ac[t][3]), "v"(ac[t][0])); }
                if constexpr (i == 5 || i == 7 || i == 8) lw32(dq0 + ((i - 4) / 2) * 256, fo[(i - 4) / 2]);
                if constexpr (i == 9 || i == 10) clr2(acc[i - 9]);
                if constexpr (i == 11) BARL(0) });
        }
        HSTAMP(s, 1) AST(3)
        {
            unsigned fo[2];
            auto fin2 = [&](auto P, auto J) { fold2<CV(J)>(acc[2 * CV(P)], acc[2 * CV(P) + 1], fo); };
            V.run<0, true, 19, 20, 0, true>(W, [&](auto I) { constexpr int i = CV(I);
                if constexpr (i == 0) { al[0] = lrtr(trp); ah[0] = lrtr(trp + 64); }
                if constexpr (i == 1) al[1] = lrtr(trp + 1024);
                if constexpr (i == 2) ah[1] = lrtr(trp + 1088);
                if constexpr (i == 3) al[2] = lrtr(trp + 2048);
                if constexpr (i == 4) ah[2] = lrtr(trp + 2112);
                SCHED0();
                if constexpr (i >= 1) clr(IC(3 + i)); });
            sfor<30>([&](auto I) { constexpr int i = CV(I), b = (i % 6) >> 1, t = 2 * (i / 6) + (i & 1), id = OIDX(t, b);
                smfw<id>(acc[t], CAT4(al[b], ah[b]), W[id], ix);
                SCHED0();
                if constexpr (i == 1 || i == 3) TOUCH(ah[(i + 1) / 2]);
                if constexpr (i == 29) TOUCH(v[0]);
                if constexpr (i == 21) BARL(0)
                SCHED0();
                if constexpr (i == XLD) X.next<4>(k, l16);
                if constexpr (i >= 21 && i < 27) v[i - 21] = lr128(l16 + L_K + (i - 21) * 1024);
                SCHED0();
                V.out<i - 1, 0>(d0, d1);
                SCHED0();
                if constexpr (i % 3 == 0 && i < 18) clr(IC(9 + i / 3));
                if constexpr (i == 16) clr(IC(15));
                if constexpr (i >= 18 && i < 21) clr(IC(i - 2));
                if constexpr (i == 22) clr(IC(19));
                if constexpr (i >= 23 && i < 29) fold_op<(i - 23) / 2>(acc[(i - 23) & 1], fo[(i - 23) & 1]);
                if constexpr (i == 29) { st64(k.rs, vo, ym, (u32x2){fo[0], fo[1]}); SCHED0(); fin2(IC(1), IC(0)); }
                SCHED0(); });
            HSTAMP(s, 2)
            sfor<107>([&](auto I) { constexpr int i = CV(I), q = i < 8 ? i + 4 : i + 3;
                ymm(I);
                if constexpr (i < 5 || (i >= 29 && i < 35)) TOUCH(v[(i < 5 ? i + 1 : i - 23) % 12]);
                if constexpr (i < 13 && i != 8) {
                    if constexpr (q % 3 == 1) { ym += 32768; PINS(ym); }
                    if constexpr (q % 3 == 2 && i >= 5) __builtin_amdgcn_s_setprio(0);
                    SCHED0();
                    if constexpr (q % 3 == 0) { st64(k.rs, vo, ym, (u32x2){fo[0], fo[1]}); SCHED0(); }
                    if constexpr (q < 15) fin2(IC(q / 3), IC(q % 3));
                }
                if constexpr (i == 13) done = rt_async();
                if constexpr (i >= 15 && i < 23 && i != 18 && i != 20) v[6 + i - 15 - (i > 18) - (i > 20)] = lr128(l16 + L_K + (6 + i - 15 - (i > 18) - (i > 20)) * 1024);
                if constexpr (i == 12 || i == 13) X.next<i - 7>(k, l16);
                if constexpr (i == 15) { SCHED0(); X.next<7>(k, l16); }
                if constexpr (i == 18 || i == 23) X.next<8 + 2 * (i / 23)>(k, l16);
                if constexpr (i >= 19 && i < 37 && (i % 6 == 1 || i % 3 == 0)) X.op<(i - 19) / 6, ((i - 19) % 6 + 1) / 3>(k);
                if constexpr (i >= 66 && i < 90 && i % 3 == 0 && i % 12 != 3) X.op<3 + (i - 66) / 12, ((i - 66) / 3) % 4>(k);
                if constexpr (i == 90) { ym = (c & 2) ? ym2 : ym0; PINS(ym); }
                if constexpr (i == 91) { ys = ym + EXP(327680); PINS(ys); }
                if constexpr (i == 96) { ym = (c & 1) ? ys : ym; PINS(ym); }
                if constexpr (i == 60) { HSTAMP(s, 3) }
                if constexpr (i == 42 || i == 43 || i == 48 || i == 49) { AAC((i - 42) / 3 + (i & 1)) }
                if constexpr (i == 102) lgkm0(done);
                if constexpr (i == 103) { PINS(now); HTRACE(c - 1) an = (unsigned)done - (unsigned)now; PINS(an); }
                if constexpr (i == 105) G.mx = vshl1(c, 1);
                SCHED0(); });
            AST(4)
        }
    } while (X.off == 0);
    } while (X.sg != 65u);
    HFLUSH(63)
    PROF_OUT
}

DEVI void ln_store(const Ctx& k, const float (&v)[40], unsigned dst, int row  ) {
    f32x2 s2[4] = {{0.f, 0.f}, {0.f, 0.f}, {0.f, 0.f}, {0.f, 0.f}};
#pragma unroll
    for (int i = 0; i < 40; i += 2) s2[(i >> 1) & 3] += (f32x2){v[i], v[i + 1]};
    const f32x2 st = (s2[0] + s2[1]) + (s2[2] + s2[3]);
    float m = row16sum(st[0] + st[1]) * (1.f / 640.f);
    f32x2 q2[4] = {{0.f, 0.f}, {0.f, 0.f}, {0.f, 0.f}, {0.f, 0.f}};
#pragma unroll
    for (int i = 0; i < 40; i += 2) { const f32x2 d = (f32x2){v[i], v[i + 1]} - (f32x2){m, m}; q2[(i >> 1) & 3] += d * d; }
    const f32x2 qt = (q2[0] + q2[1]) + (q2[2] + q2[3]);
    const float q = row16sum(qt[0] + qt[1]);
    const float var = bfr(q * (1.f / 640.f));
    m = bfr(m);
    const float rden = __builtin_amdgcn_rcpf(bfr(__builtin_amdgcn_sqrtf(bfr(var + 1e-5f))));
    const int n = k.l & 15;
    const unsigned vo = (n >> 2) * 2048 + (row >> 4) * 1024 + ((n & 3) * 16 + (row & 15)) * 16;
#pragma unroll
    for (int c = 0; c < 5; ++c) {
        u32x4 o;
#pragma unroll
        for (int i = 0; i < 4; ++i) { unsigned d = cvtpk(v[c * 8 + 2 * i] - m, v[c * 8 + 2 * i + 1] - m); o[i] = cvtpk(blo(d) * rden, bhi(d) * rden); }
        st128(k.rs, vo, dst + c * 16384, o);
    }
}
DEVI void unpack8(float* v, u32x4 a) {
#pragma unroll
    for (int i = 0; i < 4; ++i) { v[2 * i] = blo(a[i]); v[2 * i + 1] = bhi(a[i]); }
}
DEVI void ln_x_batch(const Params& p, const Ctx& k, int bw) {
    const int row = bw * 4 + (k.l >> 4), sg = bw >> 3;
    float v[40];
#pragma unroll
    for (int c = 0; c < 5; ++c) unpack8(v + c * 8, *(const u32x4*)(p.x + (size_t)row * 640 + (c * 16 + (k.l & 15)) * 8));
    ln_store(k, v, k.seg0(PG_XNA, sg), row & 31);
    DRAIN();
    if (k.l == 0) cnt_add(k.cnt0(C_XN, sg));
}

DEVI void row_role(const Params& p, const Ctx& k, char* smem, int rt) {
    PROF_DECL
    const int l = k.l, n = l & 15;
    const unsigned fo = k.w * 2048 + rt * 1024;
    RSTAMP(2)
    for (int sg = k.c; sg < 64; sg += 2) {
        prog_wait<5>(k, k.pg0(0) + CNT(C_X2, rt * 5), sg);
        STOP(sg) STAMP(sg, 0)
        const unsigned src = k.seg0(PG_X2, sg) + fo, dst = (k.seg0(PG_XNA, sg) ^ 8192u) + fo;
        u32x4 a[5];
_Pragma("unroll 1")
        for (unsigned spins = 0;; ++spins) {
            unsigned mx = 0;
#pragma unroll
            for (int c = 0; c < 5; ++c) a[c] = ld128(k.rs, l * 16, src + c * 16384);
#pragma unroll
            for (int c = 0; c < 5; ++c) mx = max(mx, umax4(a[c]));
            if (__all(mx != 0xFFFFFFFFu) || spin_fail(k, spins)) break;
            PCOUNT(0)
            asm volatile("" ::: "memory");
        }
        STAMP(sg, 1)
        f32x2 v[20], s2[4], q2[4];
#pragma unroll
        for (int i = 0; i < 20; ++i) { v[i] = (f32x2){blo(a[i >> 2][i & 3]), bhi(a[i >> 2][i & 3])}; s2[i & 3] = i < 4 ? v[i] : s2[i & 3] + v[i]; }
        const f32x2 st = (s2[0] + s2[1]) + (s2[2] + s2[3]);
        const float sm = xor32add(xor16add(st[0] + st[1])), mu = sm * (1.f / 160.f);
#pragma unroll
        for (int i = 0; i < 20; ++i) { const f32x2 d = v[i] - mu; q2[i & 3] = i < 4 ? d * d : __builtin_elementwise_fma(d, d, q2[i & 3]); }
        const f32x2 qt = (q2[0] + q2[1]) + (q2[2] + q2[3]);
        *(f32x2*)(smem + n * 32 + k.w * 8) = (f32x2){sm, xor32add(xor16add(qt[0] + qt[1]))};
        RAWBAR()
        const f32x4 e0 = *(const f32x4*)(smem + n * 32), e1 = *(const f32x4*)(smem + n * 32 + 16);
        float m = ((e0[0] + e0[2]) + (e1[0] + e1[2])) * (1.f / 640.f);
        const f32x4 d = (f32x4){e0[0], e0[2], e1[0], e1[2]} - 160.f * m;
        const float var = bfr((((e0[1] + e0[3]) + (e1[1] + e1[3])) + ((d[0] * d[0] + d[1] * d[1]) + (d[2] * d[2] + d[3] * d[3])) * (1.f / 160.f)) * (1.f / 640.f));
        m = bfr(m);
        const float rden = __builtin_amdgcn_rcpf(bfr(__builtin_amdgcn_sqrtf(bfr(var + 1e-5f))));
#pragma unroll
        for (int c = 0; c < 5; ++c) {
            u32x4 o;
#pragma unroll
            for (int i = 0; i < 4; ++i) { const f32x2 x = v[c * 4 + i] - m; o[i] = cvtpk(bfr(x[0]) * rden, bfr(x[1]) * rden); }
            st128(k.rs, l * 16, dst + c * 16384, o);
        }
        STAMP(sg, 2)
        DRAIN();
        __builtin_amdgcn_s_barrier();
        st32(k.rs, (k.w == 0 && l == 0) ? 0u : OOB, (k.pg0(0) ^ 8192u) + CNT(C_XN, k.c * 2 + rt), (unsigned)sg);
        STAMP(sg, 3) STAMP(sg, 4) STAMP(sg, 5)
        PFLUSH(sg) PFLUSH(sg ^ 1)
    }
    PROF_OUT
}

struct Red {
    u32x4 Y[16];
    template <int I0, int I1> DEVI void yload(const Ctx& k, int j, int sg) {
        const unsigned a = k.yx(sg, j / 5) + (j % 5) * 131072 + k.w * 16384;
#pragma unroll
        for (int i = I0; i < I1; ++i) Y[i] = ld128(k.rs, k.l * 16 + (i & 3) * 1024, a + ((i >> 2) & 1) * 4096 + (i >> 3) * 65536);
    }
};
DEVI unsigned y_get(const Ctx& k) { return ld32(k.rs, (k.l & 7) * 128, k.p0 + CNT(C_BC, k.w * 16 + 8) + 4); }
DEVI void y_wait(const Ctx& k, int s) {
_Pragma("unroll 1")
    for (unsigned spins = 0; !__all((int)y_get(k) >= s); ++spins) {
        asm volatile("" ::: "memory");
        __builtin_amdgcn_s_sleep(2);
        if (spin_fail(k, spins)) break;
    }
    __builtin_amdgcn_fence(__ATOMIC_ACQUIRE, "wavefront");
}
DEVI void wait_until(unsigned t) { while ((int)((rclk() - t) << 16) < 0) { if (WUS) __builtin_amdgcn_s_sleep(WUS); } }
#define DPPMAX(x, c) x = max(x, __builtin_amdgcn_update_dpp(0, x, c, 0xf, 0xf, false))
#define TSLP 1
#define TWU 0
DEVI void wait_until2(unsigned t) { while ((int)((rclk() - t) << 16) < 0) { if (TWU) __builtin_amdgcn_s_sleep(TWU); } }
#define WAIT_BC(s, BSLP) { \
    unsigned bc; \
    _Pragma("unroll 1") for (;; ++spins) { \
        bc = ld32(k.rs, (l & 15) * 128, k.p0 + CNT(C_BC, w * 16)); \
        DRAIN(); \
        if (__all(((int)bc >> 16) > s)) break; \
        __builtin_amdgcn_s_sleep(BSLP); \
        if (spin_fail(k, spins)) break; \
    } \
    const unsigned now = rclk(); \
    int d = (int)((bc - now) << 16); \
    d = ((int)bc >> 16) > s + 1 || d > (256 << 16) ? -(64 << 16) : d; \
    DPPMAX(d, 0xB1); DPPMAX(d, 0x4E); DPPMAX(d, 0x141); \
    tq = now + (__builtin_amdgcn_readlane(d, 0) >> 16); tv = now + (__builtin_amdgcn_readlane(d, 8) >> 16); }
DEVI void mred_role(const Params& p, const Ctx& k, char* smem, int j) {
    PROF_DECL
    const int l = k.l, w = k.w, g = l >> 4, n = l & 15;
    const unsigned ix = SPIX(l);
    const unsigned vp = (g * 16 + ((n >> 1) & 1) * 8 + (n >> 2) * 2 + (n & 1)) * 16;
    const unsigned v16 = (g >> 1) * 1024 + (g & 1) * 256 + n * 16, yo = j * 32768 + EXP(w * 4096);
    const u32x4 ones = {0xFFFFFFFFu, 0xFFFFFFFFu, 0xFFFFFFFFu, 0xFFFFFFFFu};
    auto arm = [&](int s) {
#pragma unroll
        for (int i = 0; i < 4; ++i) st128(k.rs, l * 16 + i * 1024, k.ym(s) + yo, ones);
    };
    const unsigned pv = l == 0 && j % 5 == 0 ? (j / 5) * 128 + w * 32 : OOB;
    Red R;
    RSTAMP(2)
    arm(1);
    y_wait(k, 2);
    R.yload<0, 16>(k, j, 0);
    for (int s = 0; s < 63; ++s) {
        STOP(s) HSTAMP(s, 0)
        const unsigned ap = k.p0 + PR_B + (s & 3) * PR_SZ + w * 49152, am = k.ym(s) + yo;
        const unsigned ph = (s & 4) ? 0x80008000u : 0u;
        unsigned spins = 0, tq, tv;
        unsigned fl;
        WAIT_BC(s, BSLP)
        st32(k.rs, pv, k.p0 + CNT(C_XB, 0), spack(((int)((tq - tv) << 16) < 0 ? tv : tq) + RDY, s + 2));
        STAMP(s, 5)
        char* rb = smem + (s & 1) * 4096;
_Pragma("unroll 1")
        for (spins = 0;; ++spins) {
            u32x4 ym[4], pm[5];
            f32x4 acc[2];
            if (spins == 0) wait_until(tv);
            asm volatile("" ::: "memory");
#pragma unroll
            for (int i = 0; i < 4; ++i) ym[i] = ld128(k.rs, v16 + (i >> 1) * 2048 + (i & 1) * 512, am);
            fl = ld32(k.rs, 0, k.p0 + CNT(C_X1, j));
            asm volatile("" ::: "memory");
            if (spins == 0) { wait_until(tq); STAMP(s, 1) }
            asm volatile("" ::: "memory");
#pragma unroll
            for (int i = 0; i < 5; ++i) pm[i] = ld128(k.rs, i < 4 ? l * 16 + i * 1024 : vp, ap + (i >> 2) * 4096);
            acc[0] = (f32x4){0.f, 0.f, 0.f, 0.f}; acc[1] = acc[0];
            const u32x8 y0 = {ym[0][0], ym[1][0], ym[0][2], ym[1][2], ym[2][0], ym[3][0], ym[2][2], ym[3][2]};
            const u32x8 y1 = {ym[0][1], ym[1][1], ym[0][3], ym[1][3], ym[2][1], ym[3][1], ym[2][3], ym[3][3]};
            unsigned mx = 0;
#pragma unroll
            for (int i = 0; i < 4; ++i) mx = max(mx, umax4(ym[i]));
            unsigned tg = mx == 0xFFFFFFFFu ? 0x80000000u : 0u;
            asm volatile("s_nop 1" : "+v"(acc[0]), "+v"(acc[1]), "+v"(tg) : "v"(y0), "v"(y1), "v"(ix));
            SCHED0();
#pragma unroll
            for (int i = 0; i < 5; ++i) {
                const unsigned u0 = __builtin_amdgcn_bitop3_b32(pm[i][0], pm[i][1], ph, 0x7e), u1 = __builtin_amdgcn_bitop3_b32(pm[i][2], pm[i][3], ph, 0x7e);
                SCHED0();
                if (i < 4) smf<false>(acc[0], __builtin_bit_cast(bf16x8, pm[i]), __builtin_bit_cast(bf16x16, __builtin_shufflevector(R.Y[2 * i], R.Y[2 * i + 1], 0, 1, 2, 3, 4, 5, 6, 7)), ix);
                else smf<false>(acc[0], __builtin_bit_cast(bf16x8, pm[4]), __builtin_bit_cast(bf16x16, y0), ix);
                tg = (tg | u0) | u1;
                SCHED0();
                if (i < 4) smf<false>(acc[1], __builtin_bit_cast(bf16x8, pm[i]), __builtin_bit_cast(bf16x16, __builtin_shufflevector(R.Y[8 + 2 * i], R.Y[9 + 2 * i], 0, 1, 2, 3, 4, 5, 6, 7)), ix);
                else smf<false>(acc[1], __builtin_bit_cast(bf16x8, pm[4]), __builtin_bit_cast(bf16x16, y1), ix);
                SCHED0();
            }
            const unsigned long long bad = __ballot((int)tg < 0);
            asm volatile("s_nop 5" : "+v"(acc[0]), "+v"(acc[1]) : "s"(bad));
#pragma unroll
            for (int t = 0; t < 2; ++t) { float* rp = (float*)(rb + t * 2048 + w * 512 + g * 128 + n * 4); rp[0] = acc[t][0] + acc[t][2]; rp[16] = acc[t][1] + acc[t][3]; }
            if (bad == 0) break;
            PCOUNT(0)
            asm volatile("" ::: "memory");
            if (spin_fail(k, spins)) break;
        }
        STAMP(s, 2)
        RAWBAR()
        if (w < 2) {
            const char* rp = rb + w * 2048 + ((l >> 4) * 2 + ((l >> 2) & 1)) * 64 + ((l & 3) * 4 + ((l >> 3) & 1) * 2) * 4;
            const f32x2 a = *(const f32x2*)rp, b = *(const f32x2*)(rp + 512), c = *(const f32x2*)(rp + 1024), d = *(const f32x2*)(rp + 1536);
            const f32x2 r = (a + b) + (c + d);
            st32(k.rs, l * 4, k.memf(s + 1) + EXP((2 * j + w) * 256), min(cvtpk(r[0], r[1]) ^ ph, 0xFFFFFFFEu));
        }
        STAMP(s, 3)
_Pragma("unroll 1")
        for (spins = 0; (int)__builtin_amdgcn_readfirstlane(fl) < s - 2; ++spins) {
            PCOUNT(1)
            __builtin_amdgcn_s_sleep(PSLEEP);
            if (spin_fail(k, spins)) break;
            fl = ld32(k.rs, 0, k.p0 + CNT(C_X1, j));
        }
        __builtin_amdgcn_fence(__ATOMIC_ACQUIRE, "wavefront");
        y_wait(k, 2 * s + 3);
        R.yload<0, 8>(k, j, s + 1);
        arm(s + 2);
        y_wait(k, 2 * s + 4);
        STAMP(s, 4)
        R.yload<8, 16>(k, j, s + 1);
        PFLUSH(s)
    }
    PFLUSH(63)
    PROF_OUT
}
DEVI void tred_role(const Params& p, const Ctx& k, char* smem, int j) {
    PROF_DECL
    const int l = k.l, w = k.w, g = l >> 4, n = l & 15, T = 2 * j + (w >> 1), rt = w & 1;
    const unsigned v16 = (g >> 1) * 1024 + (g & 1) * 256 + n * 16, yo = j * 32768 + EXP(w * 4096);
    const unsigned xo = EXP(j * 2048) + (T & 1) * 512 + rt * 1024, xv = ((g >> 1) * 16 + n) * 16 + (g & 1) * 8;
    const u32x2 ones = {0xFFFFFFFFu, 0xFFFFFFFFu};
    Red R;
    RSTAMP(2)
    st64(k.rs, xv, k.seg(PG_X1, 0) + xo, ones); st64(k.rs, xv, k.seg(PG_X1, 1) + xo, ones);
    y_wait(k, 2);
    R.yload<0, 16>(k, j, 0);
    for (int s = 0; s < 64; ++s) {
        STOP(s) STAMP(s, 0)
        const unsigned ap1 = k.p0 + PR_B + (s & 3) * PR_SZ + w * 49152 + 16384, ap2 = ap1 + 16384, am = k.ym(s) + yo;
        const unsigned ph = (s & 4) ? 0x80008000u : 0u;
        unsigned spins = 0, tq, tv;
        u32x2 res;
        if (k.c) res = ld64(k.rs, xv, k.seg0(PG_X2, s) + xo);
        else res = *(const u32x2*)(p.x + (size_t)(s * 32 + rt * 16 + n) * 640 + T * 16 + g * 4);
        f32x4 acc[2][2];
        WAIT_BC(s, TSLP)
        u32x4 yl[8];
#pragma unroll
        for (int i = 0; i < 8; ++i) { yl[i] = __builtin_shufflevector(R.Y[2 * i], R.Y[2 * i + 1], 0, 1, 4, 5); PIN(yl[i]); }
        wait_until2(tv); wait_until2(tq + RDLY);
        asm volatile("" ::: "memory");
        STAMP(s, 1)
_Pragma("unroll 1")
        for (;; ++spins) {
            u32x4 ym[4], pp[16];
#pragma unroll
            for (int i = 0; i < 4; ++i) ym[i] = ld128(k.rs, v16 + (i >> 1) * 2048 + (i & 1) * 512, am);
#pragma unroll
            for (int i = 0; i < 16; ++i) pp[i ^ 4] = ld128(k.rs, l * 16 + (i & 3) * 1024, (i < 8 ? ap2 : ap1) + (((i ^ 4) >> 2) & 1) * 4096);
            unsigned mx = 0, tg = 0;
#pragma unroll
            for (int t = 0; t < 2; ++t) {
                acc[t][0] = (f32x4){0.f, 0.f, 0.f, 0.f}; acc[t][1] = acc[t][0];
#pragma unroll
                for (int f = 0; f < 2; ++f) {
                    const u32x4 yf = {ym[2 * f][t], ym[2 * f][t + 2], ym[2 * f + 1][t], ym[2 * f + 1][t + 2]};
#pragma unroll
                    for (int r = 0; r < 2; ++r) acc[t][r] = mf(__builtin_bit_cast(bf16x8, yf), __builtin_bit_cast(bf16x8, pp[4 + 2 * r + f]), acc[t][r]);
                }
            }
#pragma unroll
            for (int i = 0; i < 4; ++i)
#pragma unroll
                for (int t = 0; t < 2; ++t) acc[t][0] = mf(__builtin_bit_cast(bf16x8, yl[t * 4 + i]), __builtin_bit_cast(bf16x8, pp[i]), acc[t][0]);
#pragma unroll
            for (int i = 0; i < 8; ++i)
#pragma unroll
                for (int t = 0; t < 2; ++t) acc[t][1] = mf(__builtin_bit_cast(bf16x8, R.Y[t * 8 + (i ^ 4)]), __builtin_bit_cast(bf16x8, pp[8 + (i ^ 4)]), acc[t][1]);
#pragma unroll
            for (int i = 0; i < 4; ++i) mx = max(mx, umax4(ym[i]));
#pragma unroll
            for (int i = 0; i < 16; ++i) tg |= __builtin_amdgcn_bitop3_b32(pp[i ^ 4][0], pp[i ^ 4][1], ph, 0x7e) | __builtin_amdgcn_bitop3_b32(pp[i ^ 4][2], pp[i ^ 4][3], ph, 0x7e);
            if (__all(mx != 0xFFFFFFFFu && (int)tg >= 0)) break;
            PCOUNT(0)
            asm volatile("" ::: "memory");
            if (spin_fail(k, spins)) break;
        }
        STAMP(s, 2)
        const unsigned yc = y_get(k);
        char* rb = smem + (s & 1) * 16384;
#pragma unroll
        for (int i = 0; i < 4; ++i) *(f32x4*)(rb + i * 4096 + w * 1024 + l * 16) = acc[i >> 1][i & 1];
        RAWBAR()
        st32(k.rs, (w == 0 && l == 0) ? 0u : OOB, k.p0 + CNT(C_X1, j), (unsigned)s);
        {
            const char* rp = rb + w * 4096 + l * 16;
            const f32x4 r = (*(const f32x4*)rp + *(const f32x4*)(rp + 1024)) + (*(const f32x4*)(rp + 2048) + *(const f32x4*)(rp + 3072));
            const unsigned d0 = cvtpk(r[0], r[1]) ^ ph, d1 = cvtpk(r[2], r[3]) ^ ph;
            u32x2 o; o[0] = packs(blo(res[0]) + blo(d0), bhi(res[0]) + bhi(d0)); o[1] = packs(blo(res[1]) + blo(d1), bhi(res[1]) + bhi(d1));
            st64(k.rs, xv, k.seg(PG_X1, s) + xo, o);
            st64(k.rs, s < 62 ? xv : OOB, k.seg(PG_X1, (s + 2) & 63) + xo, ones);
        }
        STAMP(s, 3)
        if (s < 63) {
            if (!__all((int)yc >= 2 * s + 4)) { PCOUNT(2) y_wait(k, 2 * s + 4); }
            __builtin_amdgcn_fence(__ATOMIC_ACQUIRE, "wavefront");
            R.yload<0, 16>(k, j, s + 1);
        }
        STAMP(s, 4)
        STAMP(s, 5)
        PFLUSH(s)
    }
    PROF_OUT
}

DEVI void up_role(const Params& p, const Ctx& k, char* smem, int id) {
    PROF_DECL
    const int l = k.l, w = k.w, g = l >> 4, n = l & 15;
    bf16x8 Wt[80];
    const int tile0 = id * 8 + w * 2;
    wstream<20, 4, 8>(smem + w * 32768,
        [&](auto U, unsigned sl) { constexpr int u = CV(U);
            wp_issue<2, (u % 8) * 4096>(p.wg + (size_t)u * 32 * 1536 + tile0 * 16, 1536, sl, l); wp_issue<2, (u % 8) * 4096 + 2048>(p.wu + (size_t)u * 32 * 1536 + tile0 * 16, 1536, sl, l); },
        [&](auto U, char* sp) { constexpr int u = CV(U);
            Wt[u] = wp_read<2, true>(sp, 0, l); Wt[20 + u] = wp_read<2, true>(sp, 1, l); Wt[40 + u] = wp_read<2, true>(sp + 2048, 0, l); Wt[60 + u] = wp_read<2, true>(sp + 2048, 1, l); });
    RAWBAR()
    RSTAMP(2)
    bf16x8 one8 = {0x3F80, 0x3F80, 0x3F80, 0x3F80, 0x3F80, 0x3F80, 0x3F80, 0x3F80};
    asm volatile("s_nop 1" : "+v"(one8));
    f32x4 cs[4];
    sfor<80>([&](auto I) { constexpr int f = CV(I); mfa<(f < 64), f % 20 == 0>(cs[f / 20], Wt[f], one8); });
    asm volatile("s_nop 7" : "+v"(cs[0]), "+v"(cs[1]), "+v"(cs[2]), "+v"(cs[3]));
    const unsigned ho = EXP((tile0 >> 1) * 1024), l16 = l * 16;
    constexpr int L_ST = 81920;
    const u32x4 ones = {0xFFFFFFFFu, 0xFFFFFFFFu, 0xFFFFFFFFu, 0xFFFFFFFFu};
#pragma unroll
    for (int i = 0; i < 4; ++i) st128(k.rs, l16, k.hseg(i >> 1) + (i & 1) * EXP(49152) + ho, ones);
    unsigned fails = 0;
    const unsigned fl = (w == 0 && l == 0) ? 0u : OOB;
    const unsigned long long m1 = __ballot(n & 1), m2 = __ballot(n & 2);
    bool have = false;
    for (int s = 0; s < 64; ) {
        if (!have) prog_wait<20>(k, k.p0 + CNT(C_X1, 0), s);
        STOP(s) STAMP(s, 0)
        xn_all(k, l16, k.seg(PG_X1, s) + w * 2048, (s & 1) * 40960 + w * 2048);
        const unsigned xb = (s & 1) * 40960 + l16, yb = xb + w * 2048, sp = L_ST + (s & 1) * 2048, rp = sp + n * 16;
        const unsigned dp = sp + (g == (n >> 2) ? n * 16 + w * 4 : 4096 + l * 4);
        unsigned nx;
        f32x4 acc[2][4], st[4];
        bf16x8 x[20], y0[5], y1[5];
        unsigned mx = 0;
        auto blk = [&](auto B, auto J) { constexpr int b = CV(B), j = CV(J);
            if constexpr (j == 0) asm volatile("s_waitcnt vmcnt(%0)\n\ts_barrier" :: "n"(8 - 2 * b) : "memory");
            if constexpr (j >= 1 && j < 5) x[4 * b + j - 1] = lr128(xb + (4 * b + j - 1) * 2048);
            if constexpr (j == 5) y0[b] = lr128(yb + b * 8192);
            if constexpr (j == 6) y1[b] = lr128(yb + b * 8192 + 1024); };
        sfor<7>([&](auto J) { blk(IC(0), J); });
        SCHED0();
        STAMP(s, 1)
        sfor<5>([&](auto B) { constexpr int b = CV(B);
            sfor<20>([&](auto I) { constexpr int i = CV(I), ks = 4 * b + ((i >> 2) & 3), nt = i & 3, f = nt * 20 + ks;
                if constexpr (i < 16) mfa<(f < 64), ks == 0>(acc[0][nt], Wt[f], x[ks]);
                else mfa<false, b == 0>(st[nt], (nt & 1) ? (nt & 2 ? y1[b] : y0[b]) : one8, nt & 2 ? y1[b] : y0[b]);
                SCHED0();
                if constexpr (i >= 8 && i < 15 && b < 4) blk(IC((b + 1) % 5), IC(i - 8));
                if constexpr (i >= 16) { const u32x4 v = __builtin_bit_cast(u32x4, (i & 1) ? y1[b] : y0[b]); mx = umax3(mx, v[i & 2], v[(i & 2) + 1]); PIN(mx); }
                if constexpr (b == 4 && i >= 9 && i < 12) x[i - 9] = lr128(xb + 1024 + (i - 9) * 2048);
                SCHED0(); }); });
        STAMP(s, 2)
        float mean[2], rden[2], G[2], U[2];
        f32x4 sa[2], sb[2];
        u32x4 bad, o[2];
        bool redo;
        auto stat = [&](auto I) { constexpr int i = CV(I), rt = i / 8, j = i % 8;
            if constexpr (j == 0) asm volatile("v_add_f32 %0, %2, %3\n\tv_add_f32 %1, %4, %5" : "=&v"(mean[rt]), "=&v"(rden[rt]) : "v"(sa[rt][0]), "v"(sa[rt][1]), "v"(sb[rt][0]), "v"(sb[rt][1]));
            if constexpr (j == 1 || j == 2) asm volatile("v_add_f32 %0, %0, %2\n\tv_add_f32 %1, %1, %3" : "+v"(mean[rt]), "+v"(rden[rt]) : "v"(sa[rt][j + 1]), "v"(sb[rt][j + 1]));
            if constexpr (j == 3) asm volatile("v_mul_f32 %0, 0xbaccccCD, %0\n\tv_mul_f32 %1, 0x3acccccd, %1" : "+v"(mean[rt]), "+v"(rden[rt]));
            if constexpr (j == 4) asm volatile("v_fma_f32 %1, -%0, %0, %1\n\tv_cvt_pk_bf16_f32 %0, 0, %0\n\tv_max_f32 %1, 0, %1" : "+v"(mean[rt]), "+v"(rden[rt]));
            if constexpr (j == 5) asm volatile("v_cvt_pk_bf16_f32 %0, 0, %0\n\tv_add_f32 %0, 0x3727c5ac, %0\n\tv_cvt_pk_bf16_f32 %0, 0, %0" : "+v"(rden[rt]));
            if constexpr (j == 6) asm volatile("v_sqrt_f32 %0, %0" : "+v"(rden[rt]));
            if constexpr (j == 7) asm volatile("v_cvt_pk_bf16_f32 %0, 0, %0\n\tv_rcp_f32 %0, %0" : "+v"(rden[rt])); };
#define GU "+v"(G[0]), "+v"(G[1]), "+v"(U[0]), "+v"(U[1])
        auto ep = [&](auto I) { constexpr int i = CV(I), q = i / 15, j = i % 15, rt = q >> 2, t = (q >> 1) & 1, c = (q & 1) * 2;
            if constexpr (j == 0) asm volatile("v_fma_f32 %0, %2, %3, %4\n\tv_fma_f32 %1, %2, %5, %6" : "=&v"(G[0]), "=&v"(G[1]) : "v"(mean[rt]), "v"(cs[t][c]), "v"(acc[rt][t][c]), "v"(cs[t][c + 1]), "v"(acc[rt][t][c + 1]));
            if constexpr (j == 1) asm volatile("v_mul_f32 %0, %0, %2\n\tv_mul_f32 %1, %1, %2" : "+v"(G[0]), "+v"(G[1]) : "v"(rden[rt]));
            if constexpr (j == 2) asm volatile("v_cvt_pk_bf16_f32 %0, 0, %0\n\tv_cvt_pk_bf16_f32 %1, 0, %1" : "+v"(G[0]), "+v"(G[1]));
            if constexpr (j == 3) asm volatile("v_mul_f32 %0, 0xbfb8aa3b, %0\n\tv_mul_f32 %1, 0xbfb8aa3b, %1" : "+v"(G[0]), "+v"(G[1]));
            if constexpr (j == 4 || j == 5) asm volatile("v_exp_f32 %0, %0" : "+v"(G[j & 1]));
            if constexpr (j == 6) asm volatile("v_fma_f32 %0, %2, %3, %4\n\tv_fma_f32 %1, %2, %5, %6" : "=&v"(U[0]), "=&v"(U[1]) : "v"(mean[rt]), "v"(cs[2 + t][c]), "v"(acc[rt][2 + t][c]), "v"(cs[2 + t][c + 1]), "v"(acc[rt][2 + t][c + 1]));
            if constexpr (j == 7) asm volatile("v_add_f32 %0, 1.0, %0\n\tv_add_f32 %1, 1.0, %1" : "+v"(G[0]), "+v"(G[1]));
            if constexpr (j == 8 || j == 9) asm volatile("v_rcp_f32 %0, %0" : "+v"(G[j & 1]));
            if constexpr (j == 10) asm volatile("v_mul_f32 %0, %0, %2\n\tv_mul_f32 %1, %1, %2" : "+v"(U[0]), "+v"(U[1]) : "v"(rden[rt]));
            if constexpr (j == 11) asm volatile("v_cvt_pk_bf16_f32 %0, 0, %0\n\tv_cvt_pk_bf16_f32 %1, 0, %1" : "+v"(U[0]), "+v"(U[1]));
            if constexpr (j == 12) asm volatile("v_cvt_pk_bf16_f32 %0, 0, %0\n\tv_cvt_pk_bf16_f32 %1, 0, %1" : "+v"(G[0]), "+v"(G[1]));
            if constexpr (j == 13) asm volatile("v_mul_f32 %0, %0, %2\n\tv_mul_f32 %1, %1, %3" : "+v"(G[0]), "+v"(G[1]) : "v"(U[0]), "v"(U[1]));
            if constexpr (j == 14) { unsigned d; asm volatile("v_cvt_pk_bf16_f32 %0, %1, %2\n\tv_min_u32 %0, -2, %0" : "=&v"(d) : "v"(G[0]), "v"(G[1])); o[rt][q & 3] = d; } };
        sfor<80>([&](auto I) {
            constexpr int i = CV(I), ks = i >> 2, nt = i & 3, f = nt * 20 + ks;
            mfa<(f < 64), ks == 0>(acc[1][nt], Wt[f], x[ks]);
            SCHED0();
            if constexpr (nt == 0 && ks + 3 < 20) x[(ks + 3) % 20] = lr128(xb + 1024 + ((ks + 3) % 20) * 2048);
            if constexpr (i == 1 || i == 3) {
                constexpr int rt2 = i == 3;
                asm volatile("" : "+v"(st[2 * rt2]), "+v"(st[2 * rt2 + 1]));
                const f32x4& q = st[2 * rt2 + 1];
                const float d = csel(csel(q[0], q[1], m1), csel(q[2], q[3], m1), m2);
                lw32(dp + rt2 * 512, __float_as_uint(st[2 * rt2][0])); lw32(dp + rt2 * 512 + 256, __float_as_uint(d));
            }
            if constexpr (i == 2) lw32(sp + 1024 + w * 4, __any(mx == 0xFFFFFFFFu) ? 1u : 0u);
            if constexpr (i == 6) bar_lgkm<1>();
            if constexpr (i == 6) sa[0] = __builtin_bit_cast(f32x4, lr128(rp));
            if constexpr (i == 7) sb[0] = __builtin_bit_cast(f32x4, lr128(rp + 256));
            if constexpr (i == 9) sa[1] = __builtin_bit_cast(f32x4, lr128(rp + 512));
            if constexpr (i == 10) sb[1] = __builtin_bit_cast(f32x4, lr128(rp + 768));
            if constexpr (i == 11) bad = __builtin_bit_cast(u32x4, lr128(sp + 1024));
            if constexpr (i >= 12 && i < 20) stat(IC(i - 12));
            if constexpr (i >= 20) ep(IC(i - 20));
            if constexpr (i >= 21 && i < 53 && nt == 1) stat(IC(8 + (i - 21) / 4));
            if constexpr (i == 54) { redo = __builtin_amdgcn_readfirstlane((bad[0] | bad[1]) | (bad[2] | bad[3])) != 0 && fails++ < 4096u; }
            if constexpr (i == 78) nx = ld32(k.rs, min(l, 19) * 128, k.p0 + CNT(C_X1, 0));
            if constexpr (i == UPOS) st32(k.rs, fl, k.p0 + CNT(C_H, id), (unsigned)(redo ? s - 1 : s));
            SCHED0();
        });
        STAMP(s, 3)
        asm volatile("s_nop 7" : "+v"(acc[1][0]), "+v"(acc[1][1]), "+v"(acc[1][2]), "+v"(acc[1][3]));

#pragma unroll
        for (int q = 0; q < 4; ++q) {
            const int t = q >> 1, c = (q & 1) * 2;
            float hv[2];
#pragma unroll
            for (int i = 0; i < 2; ++i) {
                const float gg = bfr(__builtin_fmaf(mean[1], cs[t][c + i], acc[1][t][c + i]) * rden[1]), u = bfr(__builtin_fmaf(mean[1], cs[2 + t][c + i], acc[1][2 + t][c + i]) * rden[1]);
                hv[i] = bfr(__builtin_amdgcn_rcpf(1.f + __builtin_amdgcn_exp2f(gg * -1.44269504f))) * u;
            }
            o[1][q] = packs(hv[0], hv[1]);
        }
        if (redo) { PCOUNT(0) RAWBAR() continue; }
        st128(k.rs, l16, k.hseg(s) + ho, o[0]); st128(k.rs, l16, k.hseg(s) + EXP(49152) + ho, o[1]);
        st128(k.rs, s < 62 ? l16 : OOB, k.hseg((s + 2) & 63) + ho, ones); st128(k.rs, s < 62 ? l16 : OOB, k.hseg((s + 2) & 63) + EXP(49152) + ho, ones);
        PIN(nx);
        have = __all((int)nx > s);
        __builtin_amdgcn_fence(__ATOMIC_ACQUIRE, "wavefront");
        STAMP(s, 4) STAMP(s, 5)
        PFLUSH(s)
        ++s;
    }
    PROF_OUT
}

DEVI void down_role(const Params& p, const Ctx& k, char* smem, int id) {
    PROF_DECL
    const int l = k.l, w = k.w, g = l >> 4, n = l & 15, rt = id / 5, cg = id % 5;
    bf16x8 Wt[96];
    wstream<12, 8, 4>(smem + w * 32768,
        [&](auto U, unsigned sl) { constexpr int u = decltype(U)::value; wp_issue<8, (u % 4) * 8192>(p.wd + (size_t)(w * 12 + u) * 32 * 640 + cg * 128, 640, sl, l); },
        [&](auto U, char* sp) { constexpr int u = decltype(U)::value;
#pragma unroll
            for (int nt = 0; nt < 8; ++nt) Wt[u * 8 + nt] = wp_read<8, false>(sp, (nt + 2 * w) & 7, l); });
    RAWBAR()
    RSTAMP(2)
    const unsigned xo = EXP((cg * 4 + w) * 2048) + rt * 1024, xv = ((g >> 1) * 16 + n) * 16 + (g & 1) * 8;
    const u32x2 ones = {0xFFFFFFFFu, 0xFFFFFFFFu};
    const unsigned x2v = k.c ? OOB : xv;
#pragma unroll
    for (int i = 0; i < 4; ++i) st64(k.rs, x2v, k.seg(PG_X2, i >> 1) + xo + (i & 1) * 512, ones);
    for (int s = 0; s < 64; ++s) {
        prog_wait<12>(k, k.p0 + CNT(C_H, 0), s);
        STOP(s) STAMP(s, 0)
        f32x4 acc[8];
        u32x2 res[2];
        const unsigned rb = (s & 1) * 32768 + l * 16;
_Pragma("unroll 1")
        for (unsigned spins = 0;; ++spins) {
            u32x4 hx[12];
#pragma unroll
            for (int q = 0; q < 3; ++q) {
                const unsigned a = k.hseg(s) + rt * EXP(49152) + EXP((w * 12 + q * 4) * 1024);
#pragma unroll
                for (int i = 0; i < 4; ++i) hx[q * 4 + i] = ld128(k.rs, l * 16 + i * 1024, a);
            }
#pragma unroll
            for (int t = 0; t < 2; ++t) res[t] = ld64(k.rs, xv + t * 512, k.seg(PG_X1, s) + xo);
            unsigned mx = 0;
            sfor<96>([&](auto I) { constexpr int i = CV(I), ks = i < 72 ? i / 6 : (i - 72) / 2, j = i < 72 ? 2 + i % 6 : i & 1, f = ks * 8 + j;
                mfa<(f < 64), ks == 0>(acc[j], Wt[f], __builtin_bit_cast(bf16x8, hx[ks]));
                SCHED0();
                if constexpr (i < 72 && i % 6 < 2) { mx = umax3(mx, hx[ks][(i & 1) * 2], hx[ks][(i & 1) * 2 + 1]); PIN(mx); }
                if constexpr (i >= 75 && i < 93 && i % 3 == 0) { constexpr int jj = 2 + (i - 75) / 3;
                    asm volatile("" : "+v"(acc[jj])); lw128(rb + ((w + jj / 2) & 3) * 6144 + ((jj / 2 - 1) * 2 + (jj & 1)) * 1024, __builtin_bit_cast(u32x4, acc[jj])); }
                SCHED0(); });
            asm volatile("s_nop 7" : "+v"(acc[0]), "+v"(acc[1]), "+v"(res[0]), "+v"(res[1]));
            if (__all(mx != 0xFFFFFFFFu) || spin_fail(k, spins)) break;
            PCOUNT(0)
            asm volatile("" ::: "memory");
        }
        STAMP(s, 1)
        RAWBAR()
        st32(k.rs, (w == 0 && l == 0) ? 0u : OOB, k.p0 + CNT(C_X2, rt * 5 + cg), (unsigned)s);
#pragma unroll
        for (int t = 0; t < 2; ++t) {
            const unsigned rp = rb + w * 6144 + t * 1024;
            const f32x4 r = (acc[t] + __builtin_bit_cast(f32x4, lr128(rp))) + (__builtin_bit_cast(f32x4, lr128(rp + 2048)) + __builtin_bit_cast(f32x4, lr128(rp + 4096)));
            const unsigned d0 = cvtpk(r[0], r[1]), d1 = cvtpk(r[2], r[3]);
            u32x2 o; o[0] = cvtpk(blo(res[t][0]) + blo(d0), bhi(res[t][0]) + bhi(d0)); o[1] = cvtpk(blo(res[t][1]) + blo(d1), bhi(res[t][1]) + bhi(d1));
            if (k.c) *(u32x2*)(p.out + (size_t)(s * 32 + rt * 16 + n) * 640 + (cg * 8 + w * 2 + t) * 16 + g * 4) = o;
            else st64(k.rs, xv + t * 512, k.seg(PG_X2, s) + xo, (u32x2){min(o[0], 0xFFFFFFFEu), min(o[1], 0xFFFFFFFEu)});
        }
        STAMP(s, 2)
#pragma unroll
        for (int t = 0; t < 2; ++t) st64(k.rs, s < 62 ? x2v : OOB, k.seg(PG_X2, (s + 2) & 63) + xo + t * 512, ones);
        STAMP(s, 3) STAMP(s, 4) STAMP(s, 5)
        PFLUSH(s)
    }
    PROF_OUT
}

DEVI void warm(const Params& p, const Ctx& k, int xl, int hw) {
    if (k.c) sleep_ticks(WD1);
    set_m0(k.w * 256);
    const unsigned t0 = rclk();
_Pragma("unroll 1")
    for (int it = hw; it < 880; it += 64) {
        const u16* b; unsigned o;
        while ((int)(rclk() - t0) < (it >> 6) * PACE) __builtin_amdgcn_s_sleep(1);
        if (OLDMAP) { const int jt = (it & 1) ? min(440 + (it >> 1), 871) : it >> 1;
            if (jt < 200) { b = p.Wo; o = ((xl * 8 + (jt / 5) % 8) * 192 + (jt / 40 + 1) * 32) * 1280 + (jt % 5) * 8192 + k.l * 128; }
            else if (jt < 440) { const unsigned ln = (jt - 200) * 64 + k.l; b = p.Wv; o = (ln / 24) * 12288 + xl * 3072 + (ln % 24) * 128; }
            else { const unsigned ln = ((jt - 440) >> 1) * 64 + k.l; b = (jt & 1) ? p.Wq : p.Wk; o = (64 + ln / 24) * 12288 + xl * 3072 + (ln % 24) * 128; } }
        else if (!(xl & 1)) { const unsigned ln = (min(it, 863) >> 1) * 64 + k.l; b = (it & 1) ? p.Wq : p.Wk; o = (64 + ln / 48) * 12288 + xl * 3072 + (ln % 48) * 128; }
        else if (it < 400) { b = p.Wo; o = (((xl >> 1) * 16 + (it / 5) % 16) * 192 + (it / 80 + 1) * 32) * 1280 + (it % 5) * 8192 + k.l * 128; }
        else { const unsigned ln = (it - 400) * 64 + k.l; b = p.Wv; o = (ln / 48) * 12288 + (xl >> 1) * 6144 + (ln % 48) * 128; }
        asm volatile("global_load_lds_dword %0, %1" :: "v"(o), "s"(b) : "memory");
    }
    if (hw >= 20) {
_Pragma("unroll 1")
        for (int it = hw - 20; it < 240; it += 44) {
            const unsigned ln = (it >> 1) * 64 + k.l;
            const bool dn = (xl & 1) != UPODD;
            const u16* b = dn ? p.wd : (it & 1) ? p.wu : p.wg;
            const unsigned o = dn ? ((it & 3) * 60 + (it >> 2)) * 8192 + k.l * 128 : (ln / 12) * 3072 + (xl >> 1) * 1536 + (ln % 12) * 128;
            while ((int)(rclk() - t0) < 14 * PACE + GDLY + (it / 44) * PACE2) __builtin_amdgcn_s_sleep(1);
            asm volatile("global_load_lds_dword %0, %1" :: "v"(o), "s"(b) : "memory");
        }
    }
    DRAIN();
    RAWBAR()
}

DEVI int xcc_id() { return __builtin_amdgcn_s_getreg(20 | (0 << 6) | (3 << 11)) & 7; }

__global__ __launch_bounds__(256) void init_kernel(Params p) {
    const int b = blockIdx.x, tid = threadIdx.x, l = tid & 63;
    const __amdgpu_buffer_rsrc_t rs = __builtin_amdgcn_make_buffer_rsrc((void*)p.ws, 0, 0x7fffffff, 0x00020000);
    const u32x4 ones = {0xFFFFFFFFu, 0xFFFFFFFFu, 0xFFFFFFFFu, 0xFFFFFFFFu};
    if (b < 64 && tid < 64) {
        unsigned bt[2] = {0xFFFFFFFFu, 0xFFFFFFFFu};
        if (b < NPAGES) {
            const unsigned off = ((unsigned)b << 21) + 7 * 16384 + (l & 7) * 16;
#pragma unroll 1
            for (int r = 0; r < CALR; ++r) {
#pragma unroll
                for (int hf = 0; hf < 2; ++hf) {
                    unsigned long long c0, c1;
                    asm volatile("s_memtime %0\n\ts_waitcnt lgkmcnt(0)" : "=s"(c0) :: "memory");
                    st128(rs, off + hf * 8192, 0, ones);
                    asm volatile("s_waitcnt vmcnt(0)\n\ts_memtime %0\n\ts_waitcnt lgkmcnt(0)" : "=s"(c1) :: "memory");
                    bt[hf] = min(bt[hf], (unsigned)(c1 - c0));
                }
            }
        }
        if (l == 0) ((unsigned*)(p.cm + CM_TAB))[b] = (unsigned)(bt[1] < bt[0]) ^ (unsigned)(xcc_id() >> 2);
    }
    constexpr unsigned N0 = PR_B + 4 * PR_SZ, N1 = 40 * 16384, N2 = 64 * 20480;
    for (unsigned i = (b * 256 + tid) * 16; i < N0 + N1 + N2; i += 256 * 256 * 16) {
        unsigned o;
        u32x4 v = ones;
        if (i < N0) o = i;
        else if (i < N0 + N1) o = ((unsigned)PG_YM << 21) + (i - N0);
        else {
            const unsigned j = i - N0 - N1, s = j / 20480, r = j % 20480;
            const unsigned q = r < 16384 ? r : 16384 + ((r - 16384) >> 11) * 8192 + (r & 2047);
            o = (2u << 21) + s * 32768 + q;
            if (s == 0) {
                const int e = (((q >> 14) << 13) | (q & 8191u)) >> 4, T = e >> 4, hf = (e >> 1) & 1, rr = ((e >> 2) & 3) * 2 + (e & 1);
#pragma unroll
                for (int c = 0; c < 4; ++c) v[c] = min(*(const unsigned*)(p.mem0 + rr * 640 + T * 16 + 4 * c + 2 * hf), 0xFFFFFFFEu);
            }
        }
        *(u32x4*)(p.ws + o) = v;
    }
    if (b == 255) for (unsigned o = CM_TICKET + tid * 16; o < CM_BYTES; o += 4096) *(u32x4*)(p.cm + o) = ones;
}

__global__ __launch_bounds__(256, 1) void mega_kernel(Params p) {
    extern __shared__ __attribute__((aligned(16))) char smem[];
    Ctx k;
    k.ws = p.ws; k.cm = p.cm;
    k.rs = __builtin_amdgcn_make_buffer_rsrc((void*)p.ws, 0, 0x7fffffff, 0x00020000);
    k.l = threadIdx.x & 63; k.w = __builtin_amdgcn_readfirstlane(threadIdx.x >> 6);
    RSTAMP(0)
    k.par = __ballot(((const unsigned*)(p.cm + CM_TAB))[k.l] != 0u);
    if (threadIdx.x == 0) {
        int x = xcc_id(), role = 0;
#pragma unroll 1
        for (int i = 0; i < 8; ++i) {
            const unsigned t = __hip_atomic_fetch_add((GAS unsigned*)(p.cm + CM_TICKET + x * 128), 1u, __ATOMIC_RELAXED, __HIP_MEMORY_SCOPE_AGENT) + 1u;
            if (t < 32u) { role = x * 32 + (int)t; break; }
            x = (x + 1) & 7;
        }
        *(volatile LAS int*)smem = role;
    }
    RAWBAR()
    k.role = __builtin_amdgcn_readfirstlane(*(volatile LAS int*)smem);
    RAWBAR()
    const int x = k.role >> 5, i = k.role & 31, xl = x & 3;
    k.c = x >> 2;
    if (k.c) k.par = ~k.par;
    k.p0 = k.pg(0);
    RSTAMP(1)
    if (k.c) ln_x_batch(p, k, (i * 4 + xl) * 4 + k.w);
    if (OLDMAP ? i < 8 : i < 16 && !(xl & 1)) { k.l = lane(); qk_role(p, k, smem, xl * 8 + i); return; }
    if (i < 16) { k.l = lane(); vo_role(p, k, smem, OLDMAP ? xl * 8 + i - 8 : (xl >> 1) * 16 + i); return; }
    k.l = lane();
    warm(p, k, xl, (i - 16) * 4 + k.w);
    if (i < 21) { mred_role(p, k, smem, xl * 5 + i - 16); return; }
    if (i < 26) { tred_role(p, k, smem, xl * 5 + i - 21); return; }
    if ((xl & 1) == UPODD) up_role(p, k, smem, (xl >> 1) * 6 + i - 26);
    else if (i < 31) down_role(p, k, smem, (xl >> 1) * 5 + i - 26);
    else row_role(p, k, smem, xl >> 1);
}

extern "C" void kernel_launch(void* const* d_in, const int* in_sizes, int n_in,
                              void* d_out, int out_size, void* d_ws, size_t ws_size,
                              hipStream_t stream) {
    Params p;
    p.x = (const u16*)d_in[0]; p.mem0 = (const u16*)d_in[1];
    p.Wq = (const u16*)d_in[2]; p.Wk = (const u16*)d_in[3]; p.Wv = (const u16*)d_in[4]; p.Wo = (const u16*)d_in[5];
    p.wg = (const u16*)d_in[8]; p.wu = (const u16*)d_in[9]; p.wd = (const u16*)d_in[10];
    p.out = (u16*)d_out; p.cm = (char*)d_ws;
    p.ws = (char*)(((uintptr_t)d_ws + CM_BYTES + ((1u << 21) - 1)) & ~(uintptr_t)((1u << 21) - 1));
    (void)hipFuncSetAttribute((const void*)mega_kernel, hipFuncAttributeMaxDynamicSharedMemorySize, LDS_BYTES);
    init_kernel<<<256, 256, 0, stream>>>(p);
    mega_kernel<<<256, 256, LDS_BYTES, stream>>>(p);
}
```

```cpp
#define SWAPG
#include <hip/hip_runtime.h>
#include <hip/hip_bf16.h>
#include <stdint.h>
#include <utility>

typedef unsigned short u16;
typedef __attribute__((ext_vector_type(16))) short bf16x16;
typedef __attribute__((ext_vector_type(8))) short bf16x8;
typedef __attribute__((ext_vector_type(4))) short bf16x4;
typedef __attribute__((ext_vector_type(8))) __bf16 hbf8;
typedef __attribute__((ext_vector_type(16))) __bf16 hbf16;
typedef __attribute__((ext_vector_type(4))) float f32x4;
typedef __attribute__((ext_vector_type(2))) float f32x2;
typedef __attribute__((ext_vector_type(8))) unsigned u32x8;
typedef __attribute__((ext_vector_type(4))) unsigned u32x4;
typedef __attribute__((ext_vector_type(2))) unsigned u32x2;
typedef __attribute__((ext_vector_type(2))) __bf16 bf16x2_t;
#define LAS __attribute__((address_space(3)))
#define GAS __attribute__((address_space(1)))
#define DEVI __device__ __forceinline__

#define PSLEEP 8
#define WD1 900
#define GST 250
#define PACE 65
#define XPOS 74
#define YPOS 40
#define XPOSY 1
#define PWS 1
#define WUS 0
#define WMIX 1
#define PACE2 30
#define GDLY 80
#define QDLY 0
#define CALR 1
#define SPG 3
#define DQ 16
#define DV 12
#define BSLP 1
#define SDLY 0
#define UPOS 79
#define RDLY 45
#define NOYST 0
#define NOTOK 0
#define NOYLD 0
#define GPOSY 8
#define OLDMAP 1
#define UPODD 0
#define XLD 10
#define TROT 8
#define KOUT 1
#define RPOS 75
#define RDY 90
#define APOS 6
#define GPOS 74

#define EXP(o) ((((unsigned)(o) >> 13) << 14) | ((unsigned)(o) & 8191u))
enum { C_XN = 0, C_Y = 1, C_X1 = 2, C_XB = 3, C_H = 4, C_X2 = 5, C_BC = 6 };
#define CNT(type, s) ((unsigned)((type) * 16384 + (s) * 128))
constexpr unsigned PR_B = EXP(65536), PR_SZ = EXP(12 * 8192);
constexpr unsigned DUMMY_B = EXP(0);
constexpr int PG_MEMF = 2;
#define YXR 8
constexpr int PG_YX = 3;
constexpr int PG_XNA = PG_YX + 2 * YXR;
constexpr int PG_X1 = PG_XNA + 4, PG_X2 = PG_X1 + 4;
constexpr int PG_H = PG_X2 + 4;
constexpr int PG_YM = PG_H + 8;
constexpr int NPAGES = PG_YM + 2;
static_assert(NPAGES <= 64, "map");
constexpr unsigned CM_TAB = 0, CM_TICKET = 1024, CM_TMO = 4096, CM_IN = 8192, CM_GO = 40960, CM_BYTES = 73728;
constexpr int MASTER = 5 * 32 + 31;
constexpr unsigned SPIN_CAP = 1u << 20;
#define AUX_SC1 16
#define LDS_BYTES (160 * 1024)

struct Params {
    const u16* x; const u16* mem0; const u16* Wq; const u16* Wk; const u16* Wv; const u16* Wo;
    const u16* wg; const u16* wu; const u16* wd; u16* out; char* cm; char* ws;
};
struct Ctx {
    char* ws; char* cm; __amdgpu_buffer_rsrc_t rs; unsigned long long par;
    int c, l, w, role;
    unsigned p0, t0;
    DEVI unsigned pg(int page) const { return ((unsigned)page << 21) | (((unsigned)(par >> page) & 1u) << 13); }
    DEVI unsigned pg0(int page) const { return pg(page) ^ ((unsigned)c << 13); }
    DEVI unsigned seg(int page, int s) const { return pg(page + (s >> 4)) + (unsigned)(s & 15) * 131072u; }
    DEVI unsigned seg0(int page, int s) const { return seg(page, s) ^ ((unsigned)c << 13); }
    DEVI unsigned segx(int page, int s) const { return seg(page, s) ^ 8192u; }
    DEVI unsigned hseg(int s) const { return pg(PG_H + (s >> 3)) + (unsigned)(s & 7) * 262144u; }
    DEVI unsigned yx(int sg, int wq) const { return pg(PG_YX + (sg % YXR) * 2 + (wq >> 1)) + (unsigned)(wq & 1) * 1048576u; }
    DEVI unsigned ym(int s) const { return pg(PG_YM + ((s >> 1) & 1)) + (unsigned)(s & 1) * EXP(327680); }
    DEVI unsigned memf(int s) const { return pg(PG_MEMF) + (unsigned)s * 32768u; }
    DEVI char* cnt(int type, int s) const { return ws + (p0 + CNT(type, s)); }
    DEVI char* cnt0(int type, int s) const { return ws + ((p0 ^ ((unsigned)c << 13)) + CNT(type, s)); }
};

DEVI unsigned cvtpk(float a, float b) { return __builtin_bit_cast(unsigned, __builtin_convertvector((f32x2){a, b}, bf16x2_t)); }
DEVI unsigned packs(float a, float b) { return min(cvtpk(a, b), 0xFFFFFFFEu); }
DEVI float blo(unsigned x) { return __uint_as_float(x << 16); }
DEVI float bhi(unsigned x) { return __uint_as_float(x & 0xFFFF0000u); }
DEVI float bfr(float f) { return __uint_as_float(cvtpk(0.f, f)); }
DEVI u32x4 ld128(__amdgpu_buffer_rsrc_t rs, unsigned v, unsigned s) { return __builtin_amdgcn_raw_buffer_load_b128(rs, (int)v, (int)s, AUX_SC1); }
DEVI u32x2 ld64(__amdgpu_buffer_rsrc_t rs, unsigned v, unsigned s) { return __builtin_amdgcn_raw_buffer_load_b64(rs, (int)v, (int)s, AUX_SC1); }
DEVI unsigned ld32(__amdgpu_buffer_rsrc_t rs, unsigned v, unsigned s) { return __builtin_amdgcn_raw_buffer_load_b32(rs, (int)v, (int)s, AUX_SC1); }
DEVI void st128(__amdgpu_buffer_rsrc_t rs, unsigned v, unsigned s, u32x4 x) {
    __builtin_amdgcn_raw_buffer_store_b128(x, rs, (int)v, (int)s, AUX_SC1);
    asm volatile("s_nop 1" :: "v"(x) : "memory");
}
DEVI void st64(__amdgpu_buffer_rsrc_t rs, unsigned v, unsigned s, u32x2 x) { __builtin_amdgcn_raw_buffer_store_b64(x, rs, (int)v, (int)s, AUX_SC1); }
DEVI void st32(__amdgpu_buffer_rsrc_t rs, unsigned v, unsigned s, unsigned x) { __builtin_amdgcn_raw_buffer_store_b32(x, rs, (int)v, (int)s, AUX_SC1); }
#define RAWBAR() { __builtin_amdgcn_s_waitcnt(0xc07f); __builtin_amdgcn_s_barrier(); }
#define BARL(n) { asm volatile("" ::: "memory"); __builtin_amdgcn_s_waitcnt(0xc07f | ((n) << 8)); __builtin_amdgcn_s_barrier(); asm volatile("" ::: "memory"); }
#define DRAIN() asm volatile("s_waitcnt vmcnt(0)" ::: "memory")
#define SCHED0() __builtin_amdgcn_sched_barrier(0)
#define OOB 0x80000000u
DEVI int opq(int x) { asm volatile("" : "+v"(x)); return x; }
#define LANE const int l = opq(k.l), g = l >> 4, n = l & 15; (void)g; (void)n;
DEVI unsigned umax3(unsigned a, unsigned b, unsigned c) { return max(max(a, b), c); }
DEVI unsigned umax4(u32x4 v) { return max(umax3(v[0], v[1], v[2]), v[3]); }
DEVI unsigned rclk() { return (unsigned)__builtin_amdgcn_s_memrealtime(); }
DEVI unsigned long long rt_async() { unsigned long long t; asm volatile("s_memrealtime %0" : "=s"(t)); return t; }
DEVI void lgkm0(unsigned long long& a) { asm volatile("s_waitcnt lgkmcnt(0)" : "+s"(a) :: "memory"); }
DEVI unsigned spack(unsigned lo, unsigned hi) { unsigned d; asm volatile("s_pack_ll_b32_b16 %0, %1, %2" : "=s"(d) : "s"(lo), "s"(hi)); return d; }
DEVI int lane() { int l; asm volatile("v_mbcnt_lo_u32_b32 %0, -1, 0\n\tv_mbcnt_hi_u32_b32 %0, -1, %0" : "=v"(l)); __builtin_assume(l >= 0 && l < 64); return l; }
DEVI void sleep_until(unsigned target) { while ((int)(rclk() - target) < 0) __builtin_amdgcn_s_sleep(4); }
DEVI float xor16max(float x) { auto r = __builtin_amdgcn_permlane16_swap(__float_as_uint(x), __float_as_uint(x), false, false); return __builtin_fmaxf(__uint_as_float(r[0]), __uint_as_float(r[1])); }
DEVI float xor32max(float x) { auto r = __builtin_amdgcn_permlane32_swap(__float_as_uint(x), __float_as_uint(x), false, false); return __builtin_fmaxf(__uint_as_float(r[0]), __uint_as_float(r[1])); }
DEVI float xor16add(float x) { auto r = __builtin_amdgcn_permlane16_swap(__float_as_uint(x), __float_as_uint(x), false, false); return __uint_as_float(r[0]) + __uint_as_float(r[1]); }
DEVI float xor32add(float x) { auto r = __builtin_amdgcn_permlane32_swap(__float_as_uint(x), __float_as_uint(x), false, false); return __uint_as_float(r[0]) + __uint_as_float(r[1]); }
DEVI float fold32(float a, float b) { auto r = __builtin_amdgcn_permlane32_swap(__float_as_uint(a), __float_as_uint(b), false, false); return __uint_as_float(r[0]) + __uint_as_float(r[1]); }
#define ROR16(x, n) __builtin_bit_cast(float, __builtin_amdgcn_update_dpp(0, __builtin_bit_cast(int, (x)), 0x120 + (n), 0xf, 0xf, false))
DEVI float row16sum(float x) { x += ROR16(x, 8); x += ROR16(x, 4); x += ROR16(x, 2); x += ROR16(x, 1); return x; }
DEVI unsigned z0() { unsigned z; asm volatile("v_mov_b32 %0, 0" : "=v"(z)); return z; }
DEVI void cnt_add(char* p) { __hip_atomic_fetch_add((GAS unsigned*)(p + z0()), z0() + 1u, __ATOMIC_RELAXED, __HIP_MEMORY_SCOPE_AGENT); }
DEVI unsigned cnt_get(char* p) { return __hip_atomic_load((GAS unsigned*)(p + z0()), __ATOMIC_RELAXED, __HIP_MEMORY_SCOPE_AGENT) + 1u; }
DEVI bool spin_fail(const Ctx& k, unsigned spins) {
    if (spins > SPIN_CAP || ((spins & 255u) == 255u && __builtin_amdgcn_readfirstlane(cnt_get(k.cm + CM_TMO)) != 0)) { cnt_add(k.cm + CM_TMO); return true; }
    return false;
}
DEVI void cm_st(const Ctx& k, unsigned off, unsigned lo, unsigned hi) { asm volatile("global_store_dwordx2 %0, %1, %2 sc1" :: "v"(off), "v"((u32x2){lo, hi}), "s"(k.cm) : "memory"); }
DEVI u32x2 cm_ld(const Ctx& k, unsigned off) { return ld64(__builtin_amdgcn_make_buffer_rsrc((void*)k.cm, 0, CM_BYTES, 0x00020000), off, 0); }
DEVI void hello(const Ctx& k, unsigned vote) { if (k.w == 0) cm_st(k, CM_IN + k.role * 128, vote, OOB); }
DEVI void go_wait(Ctx& k, unsigned vote) {
    u32x2 v;
_Pragma("unroll 1")
    for (unsigned spins = 0; spins < SPIN_CAP; ++spins) {
        v = cm_ld(k, CM_GO + k.role * 128);
        if (__builtin_amdgcn_readfirstlane(v[1]) < 0) break;
        hello(k, vote);
        __builtin_amdgcn_s_sleep(4);
    }
    k.par = ((unsigned long long)(unsigned)__builtin_amdgcn_readfirstlane(v[1]) << 32 | (unsigned)__builtin_amdgcn_readfirstlane(v[0])) ^ (k.c ? ~0ull : 0ull);
    k.p0 = k.pg(0);
    __builtin_amdgcn_fence(__ATOMIC_ACQUIRE, "wavefront");
}
DEVI void cnt_wait(const Ctx& k, char* c, unsigned need) {
_Pragma("unroll 1")
    for (unsigned spins = 0; (unsigned)__builtin_amdgcn_readfirstlane(cnt_get(c)) < need; ++spins) {
        __builtin_amdgcn_s_sleep(PSLEEP);
        if (spin_fail(k, spins)) break;
    }
    __builtin_amdgcn_fence(__ATOMIC_ACQUIRE, "wavefront");
}
DEVI unsigned xn_get(const Ctx& k, unsigned l16, int s) { return ld32(k.rs, opq(l16) & 128u, k.p0 + CNT(C_XN, 0)); }
DEVI bool xn_has(const Ctx& k, unsigned v, int s) { return __all((int)v >= s - (k.c ? 0 : 8)); }
DEVI void xn_wait(const Ctx& k, unsigned l16, int s) {
_Pragma("unroll 1")
    for (unsigned spins = 0; !xn_has(k, xn_get(k, l16, s), s); ++spins) {
        asm volatile("" ::: "memory");
        __builtin_amdgcn_s_sleep(2);
        if (spin_fail(k, spins)) break;
    }
    __builtin_amdgcn_fence(__ATOMIC_ACQUIRE, "wavefront");
}
template <int N> DEVI void prog_wait(const Ctx& k, unsigned a, int s) {
_Pragma("unroll 1")
    for (unsigned spins = 0; !__all((int)ld32(k.rs, min(k.l, N - 1) * 128, a) >= s); ++spins) {
        asm volatile("" ::: "memory");
        __builtin_amdgcn_s_sleep(PWS);
        if (spin_fail(k, spins)) break;
    }
    __builtin_amdgcn_fence(__ATOMIC_ACQUIRE, "wavefront");
}
template <int OFF> DEVI void dma1(const void* sbase, unsigned voff, unsigned lds) {
    asm volatile("s_add_i32 m0, %2, %3\n\ts_nop 0\n\tglobal_load_lds_dwordx4 %0, %1" :: "v"(voff), "s"(sbase), "s"(lds), "n"(OFF) : "memory", "scc");
}
DEVI void set_m0(unsigned lds) { asm volatile("s_mov_b32 m0, %0" :: "s"(lds) : "memory"); }
template <int OFF> DEVI void dmab(__amdgpu_buffer_rsrc_t rs, unsigned voff, unsigned soff) {
    asm volatile("buffer_load_dwordx4 %0, %1, %2 offen offset:%3 sc1 lds" :: "v"(voff), "s"(rs), "s"(soff), "n"(OFF) : "memory");
}
#define PIN(x) asm volatile("" : "+v"(x))
#define PINS(x) asm volatile("" : "+s"(x))
#define LEAD()
#define TOUCH(x) asm volatile("" :: "v"(x))
DEVI float csel(float a, float b, unsigned long long m) { float d; asm volatile("v_cndmask_b32_e64 %0, %1, %2, %3" : "=v"(d) : "v"(a), "v"(b), "s"(m)); return d; }
#define LO8(x) __builtin_shufflevector(x, x, 0, 1, 2, 3, 4, 5, 6, 7)
#define HI8(x) __builtin_shufflevector(x, x, 8, 9, 10, 11, 12, 13, 14, 15)
#define CAT8(a, b) __builtin_shufflevector(a, b, 0, 1, 2, 3, 4, 5, 6, 7, 8, 9, 10, 11, 12, 13, 14, 15)
template <bool AG> DEVI void smf(f32x4& c, const bf16x8& a, const bf16x16& w, unsigned ix) {
    if constexpr (AG) asm volatile("v_smfmac_f32_16x16x64_bf16 %0, %1, %2, %3" : "+v"(c) : "v"(a), "a"(w), "v"(ix));
    else asm volatile("v_smfmac_f32_16x16x64_bf16 %0, %1, %2, %3" : "+v"(c) : "v"(a), "v"(w), "v"(ix));
}
#define WTAB(X) X(0, "{a[0:7]}", "{a[0:3]}", "{a[4:7]}") X(1, "{a[8:15]}", "{a[8:11]}", "{a[12:15]}") X(2, "{a[16:23]}", "{a[16:19]}", "{a[20:23]}") X(3, "{a[24:31]}", "{a[24:27]}", "{a[28:31]}") X(4, "{a[32:39]}", "{a[32:35]}", "{a[36:39]}") X(5, "{a[40:47]}", "{a[40:43]}", "{a[44:47]}") X(6, "{a[48:55]}", "{a[48:51]}", "{a[52:55]}") X(7, "{a[56:63]}", "{a[56:59]}", "{a[60:63]}") X(8, "{a[64:71]}", "{a[64:67]}", "{a[68:71]}") X(9, "{a[72:79]}", "{a[72:75]}", "{a[76:79]}") X(10, "{a[80:87]}", "{a[80:83]}", "{a[84:87]}") X(11, "{a[88:95]}", "{a[88:91]}", "{a[92:95]}") X(12, "{a[96:103]}", "{a[96:99]}", "{a[100:103]}") X(13, "{a[104:111]}", "{a[104:107]}", "{a[108:111]}") X(14, "{a[112:119]}", "{a[112:115]}", "{a[116:119]}") X(15, "{a[120:127]}", "{a[120:123]}", "{a[124:127]}") X(16, "{a[128:135]}", "{a[128:131]}", "{a[132:135]}") X(17, "{a[136:143]}", "{a[136:139]}", "{a[140:143]}") X(18, "{a[144:151]}", "{a[144:147]}", "{a[148:151]}") X(19, "{a[152:159]}", "{a[152:155]}", "{a[156:159]}") X(20, "{a[160:167]}", "{a[160:163]}", "{a[164:167]}") X(21, "{a[168:175]}", "{a[168:171]}", "{a[172:175]}") X(22, "{a[176:183]}", "{a[176:179]}", "{a[180:183]}") X(23, "{a[184:191]}", "{a[184:187]}", "{a[188:191]}") X(24, "{a[192:199]}", "{a[192:195]}", "{a[196:199]}") X(25, "{a[200:207]}", "{a[200:203]}", "{a[204:207]}") X(26, "{a[208:215]}", "{a[208:211]}", "{a[212:215]}") X(27, "{a[216:223]}", "{a[216:219]}", "{a[220:223]}") X(28, "{a[224:231]}", "{a[224:227]}", "{a[228:231]}") X(29, "{a[232:239]}", "{a[232:235]}", "{a[236:239]}") X(30, "{a[240:247]}", "{a[240:243]}", "{a[244:247]}") X(31, "{a[248:255]}", "{a[248:251]}", "{a[252:255]}") X(32, "{v[96:103]}", "{v[96:99]}", "{v[100:103]}") X(33, "{v[104:111]}", "{v[104:107]}", "{v[108:111]}") X(34, "{v[112:119]}", "{v[112:115]}", "{v[116:119]}") X(35, "{v[120:127]}", "{v[120:123]}", "{v[124:127]}") X(36, "{v[128:135]}", "{v[128:131]}", "{v[132:135]}") X(37, "{v[136:143]}", "{v[136:139]}", "{v[140:143]}") X(38, "{v[144:151]}", "{v[144:147]}", "{v[148:151]}") X(39, "{v[152:159]}", "{v[152:155]}", "{v[156:159]}") X(40, "{v[160:167]}", "{v[160:163]}", "{v[164:167]}") X(41, "{v[168:175]}", "{v[168:171]}", "{v[172:175]}") X(42, "{v[176:183]}", "{v[176:179]}", "{v[180:183]}") X(43, "{v[184:191]}", "{v[184:187]}", "{v[188:191]}") X(44, "{v[192:199]}", "{v[192:195]}", "{v[196:199]}") X(45, "{v[200:207]}", "{v[200:203]}", "{v[204:207]}") X(46, "{v[208:215]}", "{v[208:211]}", "{v[212:215]}") X(47, "{v[216:223]}", "{v[216:219]}", "{v[220:223]}") X(48, "{v[224:231]}", "{v[224:227]}", "{v[228:231]}") X(49, "{v[232:239]}", "{v[232:235]}", "{v[236:239]}") X(50, "{v[240:247]}", "{v[240:243]}", "{v[244:247]}") X(51, "{v[248:255]}", "{v[248:251]}", "{v[252:255]}")
template <int ID> DEVI void smfw(f32x4& c, const bf16x8& a, const bf16x16& w, unsigned ix) {
#define X(i, P, L, H) if constexpr (ID == i) asm volatile("v_smfmac_f32_16x16x64_bf16 %0, %1, %2, %3" : "+v"(c) : "v"(a), P(w), "v"(ix));
    WTAB(X)
#undef X
}
template <int ID, int HF, bool WB, bool FIRST> DEVI void mfw(f32x4& c, const bf16x16& w, const bf16x8& x) {
#define Y(R, W8) { if constexpr (FIRST && WB) asm volatile("v_mfma_f32_16x16x32_bf16 %0, %2, %1, 0" : "=&v"(c) : R(W8), "v"(x)); \
    else if constexpr (FIRST) asm volatile("v_mfma_f32_16x16x32_bf16 %0, %1, %2, 0" : "=&v"(c) : R(W8), "v"(x)); \
    else if constexpr (WB) asm volatile("v_mfma_f32_16x16x32_bf16 %0, %2, %1, %0" : "+v"(c) : R(W8), "v"(x)); \
    else asm volatile("v_mfma_f32_16x16x32_bf16 %0, %1, %2, %0" : "+v"(c) : R(W8), "v"(x)); }
#define X(i, P, L, H) if constexpr (ID == i) { if constexpr (HF) Y(H, HI8(w)) else Y(L, LO8(w)) }
    WTAB(X)
#undef X
#undef Y
}
template <bool AG, bool FIRST> DEVI void mfa(f32x4& c, const bf16x8& w, const bf16x8& x) {
    if constexpr (AG && FIRST) asm volatile("v_mfma_f32_16x16x32_bf16 %0, %1, %2, 0" : "=&v"(c) : "a"(w), "v"(x));
    else if constexpr (AG) asm volatile("v_mfma_f32_16x16x32_bf16 %0, %1, %2, %0" : "+v"(c) : "a"(w), "v"(x));
    else if constexpr (FIRST) asm volatile("v_mfma_f32_16x16x32_bf16 %0, %1, %2, 0" : "=&v"(c) : "v"(w), "v"(x));
    else asm volatile("v_mfma_f32_16x16x32_bf16 %0, %1, %2, %0" : "+v"(c) : "v"(w), "v"(x));
}
template <bool AG, bool FIRST> DEVI void mfb(f32x4& c, const bf16x8& x, const bf16x8& w) {
    if constexpr (AG && FIRST) asm volatile("v_mfma_f32_16x16x32_bf16 %0, %1, %2, 0" : "=&v"(c) : "v"(x), "a"(w));
    else if constexpr (AG) asm volatile("v_mfma_f32_16x16x32_bf16 %0, %1, %2, %0" : "+v"(c) : "v"(x), "a"(w));
    else if constexpr (FIRST) asm volatile("v_mfma_f32_16x16x32_bf16 %0, %1, %2, 0" : "=&v"(c) : "v"(x), "v"(w));
    else asm volatile("v_mfma_f32_16x16x32_bf16 %0, %1, %2, %0" : "+v"(c) : "v"(x), "v"(w));
}
DEVI f32x4 mf(const bf16x8& a, const bf16x8& b, const f32x4& c) { return __builtin_amdgcn_mfma_f32_16x16x32_bf16(__builtin_bit_cast(hbf8, a), __builtin_bit_cast(hbf8, b), c, 0, 0, 0); }
template <int N, class F> DEVI void sfor(F f) { [&]<int... I>(std::integer_sequence<int, I...>) { (f(std::integral_constant<int, I>{}), ...); }(std::make_integer_sequence<int, N>{}); }
DEVI bf16x8 lds128(const char* p) { return *(const bf16x8*)p; }
DEVI bf16x4 ldstr(const char* p) { return __builtin_amdgcn_ds_read_tr16_b64_v4i16((LAS bf16x4*)p); }
DEVI bf16x8 ldstr2(const char* p, int d) { bf16x4 a = ldstr(p), b = ldstr(p + d); return __builtin_shufflevector(a, b, 0, 1, 2, 3, 4, 5, 6, 7); }

#define STAMP(s, k)
#define QSTAMP(f, i)
#define STOP(s)
#define PFLUSH(s)
#define PROF_DECL
#define RSTAMP(i)
#define XSTAMP(i)
#define PCOUNT(i)
#define PROF_OUT

#define HSTAMP STAMP
#define HFLUSH PFLUSH
#define HTRACE(s)
#define TROUT
#define TRHINT
#define AQ(m, i)
#define AC3(i, j, k) { }
#define AST(i)
#define AAC(i)
#define ACCM 0
#define HSTOP(m) if constexpr ((ACCM != 0) == m) { STOP(0) }
#define PC8(i, x)
#define PROF_ONLY(x)
#define PROF_S(x)
template <int NT, int OFF> DEVI void wp_issue(const u16* W, int ld, unsigned ring, int l) {
    sfor<NT>([&](auto I) { constexpr int i = decltype(I)::value;
        const int id = i * 64 + l, row = id / (2 * NT), cc = id % (2 * NT);
        dma1<OFF + i * 1024>(W, (unsigned)(row * ld + cc * 8) * 2u, ring); });
}
template <int NT, bool STD> DEVI bf16x8 wp_read(const char* slot, int T, int l) {
    const int row = (STD ? 8 : 4) * (l >> 4) + ((l >> 2) & 3);
    return ldstr2(slot + row * (NT * 32) + T * 32 + (l & 3) * 8, (STD ? 4 : 16) * NT * 32);
}
template <int NU, int NI, int D, class ISSUE, class READ> DEVI void wstream(char* ring, ISSUE issue, READ read) {
    const unsigned base = (unsigned)(size_t)(LAS char*)ring;
    sfor<D>([&](auto U) { constexpr int u = decltype(U)::value; if constexpr (u < NU) issue(U, base); });
    sfor<NU>([&](auto U) {
        constexpr int u = decltype(U)::value, left = (NU - u < D ? NU - u : D) - 1;
        asm volatile("s_waitcnt vmcnt(%0)" :: "n"(left * NI) : "memory");
        read(U, ring + (u % D) * NI * 1024);
        __builtin_amdgcn_s_waitcnt(0xc07f);
        asm volatile("" ::: "memory");
        if constexpr (u + D < NU) issue(std::integral_constant<int, u + D>{}, base);
    });
}

constexpr int L_XN = 0;
constexpr int L_K = 40960;
constexpr int L_MEM = L_K + 12288;
constexpr int L_QM = L_MEM + 10240;
constexpr int L_Q = L_QM + 12288;
constexpr int L_W = L_Q + 12288;
#define IC(x) std::integral_constant<int, (x)>{}
#define CV(X) decltype(X)::value
DEVI bf16x8 lr128(unsigned a) { return *(const LAS bf16x8*)(size_t)a; }
DEVI bf16x4 lrtr(unsigned a) { return __builtin_amdgcn_ds_read_tr16_b64_v4i16((LAS bf16x4*)(size_t)a); }
#define CAT4(a, b) __builtin_shufflevector(a, b, 0, 1, 2, 3, 4, 5, 6, 7)
DEVI void lw128(unsigned a, u32x4 v) { *(LAS u32x4*)(size_t)a = v; }
DEVI void lw64(unsigned a, u32x2 v) { *(LAS u32x2*)(size_t)a = v; }
DEVI void lw32(unsigned a, unsigned v) { *(LAS unsigned*)(size_t)a = v; }
template <int N> DEVI void bar_lgkm() { asm volatile("s_waitcnt lgkmcnt(%0)\n\ts_barrier" :: "n"(N) : "memory"); }
DEVI void atom1(__amdgpu_buffer_rsrc_t rs, unsigned voff, unsigned soff) {
    unsigned one; asm volatile("v_mov_b32 %0, 1\n\tbuffer_atomic_add %0, %1, %2, %3 offen" : "=&v"(one) : "v"(voff), "s"(rs), "s"(soff) : "memory");
}
DEVI unsigned vadd(unsigned v, unsigned sc) { unsigned d; asm volatile("v_add_u32 %0, %1, %2" : "=v"(d) : "s"(sc), "v"(v)); return d; }
DEVI float vzero() { float z; asm volatile("v_mov_b32 %0, 0" : "=v"(z)); return z; }
template <int B, int I> DEVI void xn_op(const Ctx& k, unsigned v, unsigned src, unsigned dst) {
    if constexpr (I == 0) set_m0(dst + B * 8192);
    else if constexpr (I == 1) dmab<0>(k.rs, v, src + B * 16384);
    else dmab<1024>(k.rs, v, src + B * 16384);
}
DEVI void xn_all(const Ctx& k, unsigned v, unsigned src, unsigned dst) {
    sfor<5>([&](auto B) { xn_op<CV(B), 0>(k, v, src, dst); asm volatile("s_nop 0"); xn_op<CV(B), 1>(k, v, src, dst); xn_op<CV(B), 2>(k, v, src, dst); });
}
struct Xn {
    unsigned src, dst, sg, need, t, sb, se, l16, c, off;
    unsigned long long flip, m;
    DEVI void init(const Ctx& k) {
        src = k.seg(PG_XNA, 0) + k.w * 2048; dst = L_XN + k.w * 2048; sg = 0; off = ~0u;
        const unsigned q = (unsigned)(k.par >> PG_XNA), d = q ^ (q >> 1);
        flip = ((unsigned long long)(d & 1) << 16) | ((unsigned long long)(d & 2) << 31) | ((unsigned long long)(d & 4) << 46);
    }
    template <int I> DEVI void next(const Ctx& k, unsigned l16) {
        if constexpr (I == 0) { sg += 1; src += 131072; PINS(sg); PINS(src); }
        if constexpr (I == 1) asm volatile("s_bitcmp1_b64 %1, %2\n\ts_cselect_b32 %0, 0x2000, 0" : "=s"(t) : "s"(flip), "s"(sg) : "scc");
        if constexpr (I == 2) { src ^= t; PINS(src); }
        if constexpr (I == 3) asm volatile("v_and_b32 %0, 0x80, %1" : "=v"(c) : "v"(l16));
        if constexpr (I == 4) c = ld32(k.rs, c, k.p0 + CNT(C_XN, 0));
        if constexpr (I == 5) { need = sg - (k.c ? 0u : 8u); PINS(need); }
        if constexpr (I == 6) { need = sg < 64u ? need : 0x7FFFFFFFu; PINS(need); }
        if constexpr (I == 7) asm volatile("v_cmp_gt_i32_e64 %0, %1, %2" : "=s"(m) : "s"(need), "v"(c) : "memory");
        if constexpr (I == 8) asm volatile("s_cmp_lg_u64 %2, 0\n\ts_cselect_b32 %0, -1, 0\n\ts_cselect_b32 %1, 0x200000, %3" : "=&s"(off), "=s"(se) : "s"(m), "s"(src) : "scc");
        if constexpr (I == 10) asm volatile("s_cmp_lg_u32 %1, 64\n\ts_cselect_b32 %0, %0, 0" : "+s"(off) : "s"(sg) : "scc");
    }
    template <int B, int I> DEVI void op(const Ctx& k) {
        if constexpr (I == 0) { set_m0(dst + B * 8192); sb = se + B * 16384; PINS(sb); }
        else dmab<(I - 1) * 1024>(k.rs, l16, sb);
    }
    template <int NB> DEVI void late(const Ctx& k, unsigned l16) {
        xn_wait(k, l16, sg);
        se = src;
        sfor<NB>([&](auto B) { op<CV(B), 0>(k); asm volatile("s_nop 0"); op<CV(B), 1>(k); op<CV(B), 2>(k); });
        DRAIN();
    }
};
template <int OFF> DEVI u32x4 ald128(__amdgpu_buffer_rsrc_t rs, unsigned v, unsigned s) { u32x4 d; asm volatile("buffer_load_dwordx4 %0, %1, %2, %3 offen offset:%4 sc1" : "=v"(d) : "v"(v), "s"(rs), "s"(s), "n"(OFF) : "memory"); return d; }
#define MADR(b) (L_MEM + ((b) < 6 ? ((b) - 1) * 2048 : ((b) - 6) * 2048 + 3072))
struct Gather {
    u32x4 p0, p1, v0, v1;
    unsigned mx, a, aw, b, base, wo, dst;
    unsigned long long bad;
    DEVI void init(const Ctx& k) { base = k.memf(0); wo = k.w * 1024; dst = L_MEM + k.w * 2048; }
    template <int I> DEVI void op(const Ctx& k, unsigned l16, int s) {
        if constexpr (I == 0) { a = ((unsigned)s << 15) & 0x1F8000u; PINS(a); }
        if constexpr (I == 1) { a += base; aw = a + wo; PINS(a); PINS(aw); }
        if constexpr (I == 2) { b = aw + 3072 + (k.w >> 1) * 8192; PINS(b); set_m0(dst); }
        if constexpr (I == 3) v0 = ald128<2048>(k.rs, l16, aw);
        if constexpr (I == 4) v1 = ald128<3072>(k.rs, l16, b);
        if constexpr (I == 5) dmab<2048>(k.rs, l16, aw);
        if constexpr (I == 6) dmab<3072>(k.rs, l16, b);
        if constexpr (I == 7) p0 = ald128<0>(k.rs, l16, a);
        if constexpr (I == 8) p1 = ald128<1024>(k.rs, l16, a);
    }
    template <int N> DEVI void wait() { asm volatile("s_waitcnt vmcnt(%4)" : "+v"(p0), "+v"(p1), "+v"(v0), "+v"(v1) : "n"(N) : "memory"); }
    template <int I> DEVI void fin(unsigned other) {
#define MX3 "v_max3_u32 %0, %0, %3, %4\n\tv_max3_u32 %0, %0, %5, %6"
        if constexpr (I == 0) asm volatile("v_max3_u32 %0, %7, %1, %2\n\t" MX3 : "=&v"(mx) : "v"(v0[0]), "v"(v0[1]), "v"(v0[2]), "v"(v0[3]), "v"(v1[0]), "v"(v1[1]), "s"(other));
        if constexpr (I == 1) asm volatile("v_max3_u32 %0, %0, %1, %2\n\t" MX3 : "+v"(mx) : "v"(v1[2]), "v"(v1[3]), "v"(p0[0]), "v"(p0[1]), "v"(p0[2]), "v"(p0[3]));
        if constexpr (I == 2) asm volatile("v_max3_u32 %1, %1, %2, %3\n\tv_max3_u32 %1, %1, %4, %5\n\tv_cmp_eq_u32_e64 %0, -1, %1" : "=s"(bad) : "v"(mx), "v"(p1[0]), "v"(p1[1]), "v"(p1[2]), "v"(p1[3]));
    }
    DEVI void also(unsigned other) { asm volatile("v_mov_b32 %0, %1" : "=v"(mx) : "s"(other)); }
    DEVI void all() {
        asm volatile("s_waitcnt vmcnt(0)\n\tv_max3_u32 %1, %1, %2, %3\n\tv_max3_u32 %1, %1, %4, %5\n\tv_max3_u32 %1, %1, %6, %7\n\tv_max3_u32 %1, %1, %8, %9\n\tv_max3_u32 %1, %1, %10, %11\n\tv_max3_u32 %1, %1, %12, %13\n\t"
            "v_max3_u32 %1, %1, %14, %15\n\tv_max3_u32 %1, %1, %16, %17\n\tv_cmp_eq_u32_e64 %0, -1, %1" : "=s"(bad) : "v"(mx), "v"(v0[0]), "v"(v0[1]), "v"(v0[2]), "v"(v0[3]), "v"(v1[0]), "v"(v1[1]), "v"(v1[2]), "v"(v1[3]),
            "v"(p0[0]), "v"(p0[1]), "v"(p0[2]), "v"(p0[3]), "v"(p1[0]), "v"(p1[1]), "v"(p1[2]), "v"(p1[3]) : "memory");
    }
    DEVI void check() { also(0); all(); asm volatile("s_nop 3"); }
    unsigned rv, rs; unsigned long long rt;
    template <int I> DEVI void ask(const Ctx& k, unsigned l16) {
        if constexpr (I == 0) asm volatile("v_and_b32 %0, 0xf0, %1\n\tv_lshlrev_b32 %0, 1, %0" : "=v"(rv) : "v"(l16));
        if constexpr (I == 1) asm volatile("buffer_load_dword %0, %0, %1, %2 offen sc1" : "+v"(rv) : "s"(k.rs), "s"(k.p0 + CNT(C_XB, 0)) : "memory");
    }
    template <int I> DEVI void red() {
        if constexpr (I == 0) asm volatile("s_waitcnt vmcnt(4)\n\tv_max_u32_dpp %0, %0, %0 quad_perm:[1,0,3,2] row_mask:0xf bank_mask:0xf" : "+v"(rv));
        if constexpr (I == 1) asm volatile("v_max_u32_dpp %0, %0, %0 quad_perm:[2,3,0,1] row_mask:0xf bank_mask:0xf" : "+v"(rv));
        if constexpr (I == 2) asm volatile("v_max_u32_dpp %0, %0, %0 row_ror:4 row_mask:0xf bank_mask:0xf" : "+v"(rv));
        if constexpr (I == 3) asm volatile("v_max_u32_dpp %0, %0, %0 row_ror:8 row_mask:0xf bank_mask:0xf" : "+v"(rv));
        if constexpr (I == 4) asm volatile("v_readfirstlane_b32 %0, %1" : "=s"(rs) : "v"(rv));
    }
    template <int I> DEVI void hold(unsigned step) {
        if constexpr (I == 0) rt = rt_async();
        if constexpr (I == 3) { PINS(rt); b = spack((unsigned)rt, step); }
        if constexpr (I == 4) asm volatile("s_sub_u32 %0, %1, %0" : "+s"(b) : "s"(rs) : "scc");
        if constexpr (I == 5) if (__builtin_expect(b < 128u, 0)) { while ((int)((rclk() - rs) << 16) < 0) { } }
    }
    template <int I> DEVI void at(const Ctx& k, unsigned l16) {
        if constexpr (I == 1) op<2>(k, l16, 0);
        if constexpr (I >= 2 && I < 15 && (I % 5 == 2 || I % 5 == 4)) op<3 + (I / 5) * 2 + (I % 5) / 4>(k, l16, 0);
    }
    DEVI void again(const Ctx& k, unsigned l16) {
        asm volatile("s_mov_b32 m0, %9\n\tbuffer_load_dwordx4 %2, %4, %5, %7 offen offset:2048 sc1\n\tbuffer_load_dwordx4 %3, %4, %5, %8 offen offset:3072 sc1\n\t"
            "buffer_load_dwordx4 %4, %5, %7 offen offset:2048 sc1 lds\n\tbuffer_load_dwordx4 %4, %5, %8 offen offset:3072 sc1 lds\n\t"
            "buffer_load_dwordx4 %0, %4, %5, %6 offen sc1\n\tbuffer_load_dwordx4 %1, %4, %5, %6 offen offset:1024 sc1\n\ts_waitcnt vmcnt(0)"
            : "+v"(p0), "+v"(p1), "+v"(v0), "+v"(v1) : "v"(l16), "s"(k.rs), "s"(a), "s"(aw), "s"(b), "s"(dst) : "memory");
        check();
    }
};
DEVI void clr2(f32x4& c) { f32x2 a, b; asm volatile("v_mov_b64 %0, 0\n\tv_mov_b64 %1, 0" : "=v"(a), "=v"(b)); c = __builtin_shufflevector(a, b, 0, 1, 2, 3); }
#define NOP7_3(a) asm volatile("s_nop 7" : "+v"(a[0]), "+v"(a[1]), "+v"(a[2]))

struct G3 {
    f32x4 a0[3], a1[3];
    bf16x8 x0[20], x1[20], wl[20];
    unsigned l16, lwv;
    template <int KS> DEVI void r0() { x0[KS] = lr128(l16 + L_XN + KS * 2048); }
    template <int KS> DEVI void r1() { x1[KS] = lr128(l16 + L_XN + KS * 2048 + 1024); }
    template <int KS> DEVI void rw() { if constexpr (KS >= 4) wl[KS] = lr128(lwv + (KS - 4) * 1024); }
    template <bool WL, int KS> DEVI void pre() { if constexpr (WL && KS >= 4) asm volatile("" :: "v"(wl[KS]), "v"(x0[KS]), "v"(x1[KS])); else asm volatile("" :: "v"(x0[KS]), "v"(x1[KS])); }
    template <int ID0, bool WL, int ks, int m> DEVI void mm(const bf16x16 (&W)[52]) {
        constexpr int b = ks >> 1, t = m % 3, id = ID0 + t * 10 + b;
        f32x4& c = m < 3 ? a0[t] : a1[t];
        const bf16x8& x = m < 3 ? x0[ks] : x1[ks];
        if constexpr (WL && t == 2 && b >= 2) { if constexpr (ID0 == 30) mfb<false, false>(c, x, wl[ks]); else mfa<false, false>(c, wl[ks], x); }
        else mfw<id % 52, ks & 1, ID0 == 30, ks == 0>(c, W[id % 52], x);
        SCHED0();
    }
    template <int ID0, bool WL, int K0, int K1, int PF, bool H6 = false, class HOOK> DEVI void run(const bf16x16 (&W)[52], HOOK hook) {
        sfor<K1 - K0>([&](auto KK) {
            constexpr int kk = CV(KK), ks = K0 + kk;
            sfor<6>([&](auto M) {
                constexpr int m = CV(M);
                mm<ID0, WL, ks, m>(W);
                if constexpr (m == 5 && ks + 1 < K1) { pre<WL, (ks + 1) % 20>(); SCHED0(); }
                if constexpr (ks + 2 < PF) {
                    if constexpr (m == 0) r0<(ks + 2) % 20>();
                    if constexpr (m == 1) r1<(ks + 2) % 20>();
                    if constexpr (m == 3 && WL) rw<(ks + 2) % 20>();
                }
                if constexpr (H6) hook(IC(kk * 6 + m));
                else { hook(IC(kk * 7 + (m < 5 ? m : 6))); if constexpr (m == 4) hook(IC(kk * 7 + 5)); }
                SCHED0();
            });
        });
    }
    template <int ID0, bool WL, int F0, int F1, int PF, class HOOK> DEVI void go(const bf16x16 (&W)[52], HOOK hook) {
        sfor<F1 - F0>([&](auto FF) {
            constexpr int f = F0 + CV(FF), ks = f / 6, m = f % 6;
            mm<ID0, WL, ks, m>(W);
            if constexpr (m == 5 && f + 1 < F1) { pre<WL, (ks + 1) % 20>(); SCHED0(); }
            if constexpr (ks + 2 < PF) rd<WL, (f + 12) % 120>();
            hook(IC(f));
            SCHED0();
        });
    }
    template <int P> DEVI void home() {
        if constexpr (P == 0) asm volatile("" : "+{v[12:15]}"(a0[0]), "+{v[16:19]}"(a0[1]), "+{v[20:23]}"(a0[2]), "+{v[24:27]}"(a1[0]), "+{v[28:31]}"(a1[1]), "+{v[32:35]}"(a1[2]));
        else asm volatile("" : "+{v[36:39]}"(a0[0]), "+{v[40:43]}"(a0[1]), "+{v[44:47]}"(a0[2]), "+{v[48:51]}"(a1[0]), "+{v[52:55]}"(a1[1]), "+{v[56:59]}"(a1[2]));
    }
    template <bool WL, int F> DEVI void rd() {
        if constexpr (F % 6 == 0) r0<F / 6>();
        if constexpr (F % 6 == 1) r1<F / 6>();
        if constexpr (F % 6 == 3 && WL) rw<F / 6>();
    }
    u32x2 o;
    template <int I, int OFF> DEVI void out4(unsigned d0) {
        if constexpr (I >= 0 && I < 24) {
            constexpr int t = I / 8, rt = (I / 4) & 1, j = I & 1;
            const f32x4& a = rt ? a1[t] : a0[t];
            if constexpr (I % 4 < 2) { o[j] = cvtpk(a[2 * j], a[2 * j + 1]); PIN(o[j]); }
            else lw32(d0 + OFF + rt * 6144 + t * 1024 + j * 4, o[j]);
        }
    }
    template <int I, int OFF> DEVI void out(unsigned d0, unsigned d1) {
        if constexpr (I >= 0 && I < 18) {
            constexpr int t = I / 6, rt = (I / 3) & 1;
            const f32x4& a = rt ? a1[t] : a0[t];
            if constexpr (I % 3 == 0) { o[0] = cvtpk(a[0], a[1]); PIN(o[0]); }
            else if constexpr (I % 3 == 1) { o[1] = cvtpk(a[2], a[3]); PIN(o[1]); }
            else if constexpr (OFF > 0) lw64(d0 + (OFF - 1) + rt * 6144 + t * 1024, o);
            else lw64((t == 1 ? d1 : d0) + OFF + rt * 6144 + (t == 2 ? 1024 : 0), o);
        }
    }
};
#define SPIX(l) (((l) & 2) ? 0xEEEEu : 0x4444u)
template <int I> DEVI void fold_op(f32x4& a, unsigned& o) {
    if constexpr (I == 0) asm volatile("v_add_f32 %0, %0, %1" : "+v"(a[0]) : "v"(a[2]));
    if constexpr (I == 1) asm volatile("v_add_f32 %0, %0, %1" : "+v"(a[1]) : "v"(a[3]));
    if constexpr (I == 2) asm volatile("v_cvt_pk_bf16_f32 %0, %1, %2" : "=v"(o) : "v"(a[0]), "v"(a[1]));
}

template <int J> DEVI void fold2(f32x4& a, f32x4& b, unsigned (&o)[2]) {
    if constexpr (J < 2) asm volatile("v_add_f32 %0, %0, %2\n\tv_add_f32 %1, %1, %3" : "+v"(a[J]), "+v"(b[J]) : "v"(a[J + 2]), "v"(b[J + 2]));
    else asm volatile("v_cvt_pk_bf16_f32 %0, %2, %3\n\tv_cvt_pk_bf16_f32 %1, %4, %5" : "=&v"(o[0]), "=&v"(o[1]) : "v"(a[0]), "v"(a[1]), "v"(b[0]), "v"(b[1]));
}
DEVI unsigned vshl1(unsigned x, unsigned b) { unsigned d; asm volatile("v_lshl_or_b32 %0, %1, 1, %2" : "=v"(d) : "s"(x), "n"(b)); return d; }

#define ANNOUNCE(s, now, d) (((unsigned)(s) << 16) | (((unsigned)(now) + (d)) & 0xFFFFu))

struct Soft {
    unsigned long long mA, mY, mC[8];
    unsigned v0, v1;
    unsigned sg, hs;
    float a[6], m, M, t, s, h; u32x2 pk;
    template <int I> DEVI void mask(f32x4 (&sc)[3]) {
        if constexpr (I == 0) asm volatile("v_mov_b32 %0, 0xfe800000" : "=v"(t));
        if constexpr (I >= 1 && I < 9) { float d; asm volatile("v_cndmask_b32_e64 %0, %1, 0, %2" : "=v"(d) : "v"(t), "s"(mC[I - 1])); sc[(I - 1) >> 2][(I - 1) & 3] = d; }
    }
    template <int I> DEVI void op(const Ctx& k, f32x4 (&sc)[3], unsigned a0, unsigned a1, unsigned hv) {
#define FMA(x, y) "v_fmamk_f32 " x ", " x ", 0x3dd53b95, " y "\n\t"
#define EXPC(x) "v_exp_f32_e64 " x ", " x " clamp\n\t"
#define OP3(o, d, x, y) o " " d ", " x ", " y "\n\t"
#define SA(txt) asm volatile(txt : "+v"(a[0]), "+v"(a[1]), "+v"(a[2]), "+v"(a[3]), "+v"(m), "+v"(t))
#define SB(txt) asm volatile(txt : "+v"(a[0]), "+v"(a[1]), "+v"(a[2]), "+v"(a[3]), "+v"(m), "+v"(t), "+v"(s), "+v"(a[4]), "+v"(a[5]), "+v"(M), "+v"(h) : "s"(hs), "s"(sg))
#define ADD(d, x, y) OP3("v_add_f32", d, x, y)
#define MUL(d, x, y) OP3("v_mul_f32", d, x, y)
#define CND(d, x, y, c) "v_cndmask_b32_e64 " d ", " x ", " y ", " c "\n\t"
        if constexpr (I == 0) asm volatile("v_max3_f32 %0, %1, %2, %3\n\tv_max3_f32 %0, %0, %4, %5" : "=&v"(m) : "v"(sc[0][0]), "v"(sc[0][1]), "v"(sc[0][2]), "v"(sc[0][3]), "v"(sc[1][0]));
        if constexpr (I == 1) asm volatile("v_max3_f32 %0, %0, %1, %2" : "+v"(m) : "v"(sc[1][1]), "v"(sc[1][2]));
        if constexpr (I == 2) asm volatile(CND("%0", "%2", "%3", "%6") CND("%1", "%4", "%5", "%6") : "=&v"(a[2]), "=&v"(a[3]) : "v"(sc[0][2]), "v"(sc[1][0]), "v"(sc[0][3]), "v"(sc[1][1]), "s"(mY));
        if constexpr (I == 3) asm volatile("v_max_f32 %1, %1, %5\n\t" CND("%0", "%2", "%3", "%4") : "=&v"(a[0]), "+v"(m) : "v"(sc[0][0]), "v"(a[2]), "s"(mA), "v"(sc[1][3]));
        if constexpr (I == 4) asm volatile("v_mul_f32 %0, 0xbdd53b95, %1" : "=v"(t) : "v"(m));
        if constexpr (I == 5) asm volatile(CND("%0", "%2", "%3", "%5") CND("%1", "%1", "%4", "%5") : "=&v"(a[1]), "+v"(a[2]) : "v"(sc[0][1]), "v"(a[3]), "v"(sc[1][2]), "s"(mA));
        if constexpr (I == 6) asm volatile(CND("%0", "%0", "%2", "%3") FMA("%1", "%4") : "+v"(a[3]), "+v"(a[0]) : "v"(sc[1][3]), "s"(mA), "v"(t));
        if constexpr (I == 7) SA(FMA("%1", "%5"));
        if constexpr (I == 8) SA(FMA("%2", "%5") FMA("%3", "%5"));
        if constexpr (I == 9) SA(EXPC("%0"));
        if constexpr (I == 10) { asm volatile("v_max3_f32 %0, %1, %2, %3" : "=v"(M) : "v"(m), "v"(sc[2][0]), "v"(sc[2][1]), "v"(sc[2])); a[4] = sc[2][0]; a[5] = sc[2][1]; }
        if constexpr (I == 11) asm volatile("v_mov_b32 %0, %3\n\tv_mov_b32 %1, %4" : "=&v"(t), "=&v"(h), "=v"(s) : "v"(M), "s"(hv));
        if constexpr (I == 12) SB("v_permlane16_swap_b32 %9, %5");
        if constexpr (I == 13) SB(OP3("v_max_f32", "%9", "%9", "%5"));
        if constexpr (I == 14) SB(EXPC("%1"));
        if constexpr (I == 15) SB("v_mov_b32 %5, %9\n\t" ADD("%6", "%0", "%1"));
        if constexpr (I == 16) SB("v_permlane32_swap_b32 %9, %5");
        if constexpr (I == 17) SB(OP3("v_max_f32", "%9", "%9", "%5") "v_mul_f32 %9, 0xbdd53b95, %9");
        if constexpr (I == 18) SB(FMA("%7", "%9") FMA("%8", "%9"));
        if constexpr (I == 19) SB(FMA("%4", "%9"));
        if constexpr (I == 20) SB(EXPC("%2"));
        if constexpr (I == 21) SB(EXPC("%3"));
        if constexpr (I == 22) SB(ADD("%6", "%6", "%2"));
        if constexpr (I == 23) SB(EXPC("%7"));
        if constexpr (I == 24) SB(EXPC("%8"));
        if constexpr (I == 25) SB(ADD("%6", "%6", "%3"));
        if constexpr (I == 26) SB(EXPC("%4"));
        if constexpr (I == 27) SB(MUL("%6", "%12", "%6") ADD("%5", "%7", "%8"));
        if constexpr (I == 28) SB(MUL("%6", "%6", "%4"));
        if constexpr (I == 29) SB(OP3("v_fmac_f32", "%6", "%11", "%5"));
        if constexpr (I == 30) SB("v_fmac_f32_dpp %6, %6, %10 row_ror:8 row_mask:0xf bank_mask:0xf\n\tv_mov_b32 %5, %6");
        if constexpr (I == 31) SB("v_permlane16_swap_b32 %6, %5");
        if constexpr (I == 32) SB(ADD("%6", "%6", "%5") "v_mov_b32 %5, %6");
        if constexpr (I == 33) SB("v_permlane32_swap_b32 %6, %5");
        if constexpr (I == 34) SB(ADD("%6", "%6", "%5"));
        if constexpr (I == 35) SB("v_rcp_f32 %6, %6");
        if constexpr (I == 36) SB(MUL("%4", "%4", "%6") MUL("%7", "%7", "%6"));
        if constexpr (I == 37) SB(MUL("%0", "%0", "%4"));
        if constexpr (I == 38) SB(MUL("%1", "%1", "%4") MUL("%2", "%2", "%4"));
        if constexpr (I == 39) { unsigned x; asm volatile(MUL("%1", "%1", "%4") "v_cvt_pk_bf16_f32 %0, %2, %3" : "=&v"(x), "+v"(a[3]) : "v"(a[0]), "v"(a[1]), "v"(m)); pk[0] = x; }
        if constexpr (I == 40) { unsigned x; asm volatile("v_cvt_pk_bf16_f32 %0, %1, %2" : "=v"(x) : "v"(a[2]), "v"(a[3])); pk[1] = x; }
        if constexpr (I == 41) { st64(k.rs, v0, a0, pk); SCHED0(); asm volatile("v_mul_f32 %1, %1, %2\n\tv_cvt_pk_bf16_f32 %0, %0, %1" : "+v"(a[4]), "+v"(a[5]) : "v"(s)); }
        if constexpr (I == 42) st32(k.rs, v1, a1, __float_as_uint(a[4]));
    }
};
#define QIDX(t, b) ((t) * 10 + (b))
DEVI void qk_role(const Params& p, Ctx& k, char* smem, int h) {
    PROF_DECL
    const int w = k.w;
    if (k.c) sleep_until(k.t0 + WD1);
    bf16x16 W[52];
    {
        const int l = k.l;
        bf16x8 lo[6], wt[16];
        const int col = h * 192 + w * 48;
        wstream<20, 6, 6>(smem + w * 36864,
            [&](auto U, unsigned sl) { constexpr int u = CV(U);
                wp_issue<3, (u % 6) * 6144>(p.Wk + (size_t)u * 32 * 6144 + col, 6144, sl, l); wp_issue<3, (u % 6) * 6144 + 3072>(p.Wq + (size_t)u * 32 * 6144 + col, 6144, sl, l); },
            [&](auto U, char* sp) { constexpr int u = CV(U), b = u >> 1;
                if constexpr (u < 8) hello(k, 0);
#pragma unroll
                for (int t = 0; t < 6; ++t) {
                    bf16x8 f = wp_read<3, true>(sp + (t / 3) * 3072, t % 3, l);
                    if (t == 5 && b >= 2) wt[(u - 4) & 15] = f;
                    else if (u & 1) W[QIDX(t, b) % 52] = CAT8(lo[t], f);
                    else lo[t] = f;
                } });
        RAWBAR()
#pragma unroll
        for (int i = 0; i < 16; ++i) *(bf16x8*)(smem + L_W + w * 16384 + i * 1024 + l * 16) = wt[i];
        go_wait(k, 0);
    }
    RSTAMP(2)
    Gather G;
    Soft S;
    const int hw = h >> 3, hh = h & 7;
    unsigned l16, ix, dqm, dq, trp, qp;
    {
        const int l = k.l, g = l >> 4, n = l & 15, r = n & 7, u = w & 1;
        const bool hf = n >= 8, dup = w != 1;
        S.mA = __ballot(dup && hf);
        const int lim = (w == 0 ? 64 : w == 1 ? n : 16 + 8 * u + r) - 4 * g;
#pragma unroll
        for (int i = 0; i < 8; ++i) S.mC[i] = __ballot((i & 3) + (i >> 2) * 16 <= lim);
        const bool okm = !(dup && hf);
        S.mY = __ballot((dup && hf) == (w != 0));
        S.v0 = w == 0 ? hh * 512 + ((g >> 1) * 16 + (r >> 1) * 4 + hf * 2 + (r & 1)) * 16 + (g & 1) * 8 : w == 1 ? (hh >> 1) * 1024 + l * 16 + (hh & 1) * 8 : hh * 1024 + (g * 16 + 8 * u + r) * 16 + hf * 8;
        S.v1 = !okm ? OOB : w == 0 ? 4096 + hh * 128 + r * 16 + g * 4 : (w == 1 ? 4096 + n * 16 : 6144 + (8 * u + r) * 16) + (hh >> 1) * 512 + (g >> 1) * 256 + (hh & 1) * 8 + (g & 1) * 4;
        l16 = l * 16; ix = SPIX(l);
        const int uu = u ^ ((n >> 2) & 1);
        dqm = L_QM + (w >> 1) * 3072 + uu * 512 + (g >> 1) * 128 + n * 8 + (g & 1) * 4;
        dq = L_QM + 6144 + (w >> 1) * 3072 + uu * 512 + g * 128 + ((n * 8) ^ ((g >> 1) * 64));
        trp = L_QM + 6144 + (l & 3) * 128 + (((l >> 2) * 8) ^ ((l & 2) * 32));
        qp = w == 1 ? trp + 6144 : (w == 0 ? L_QM : L_Q + 6144 + u * 256) + (l & 1) * 128 + (((l >> 2) * 8) ^ (w == 3 ? 64 : 0));
        u32x8 rg = {S.v0, S.v1, l16, ix, dqm, dq, trp, qp};
        asm volatile("" : "+{v[0:7]}"(rg));
        S.v0 = rg[0]; S.v1 = rg[1]; l16 = rg[2]; ix = rg[3]; dqm = rg[4]; dq = rg[5]; trp = rg[6]; qp = rg[7];
    }
    const unsigned po0 = k.p0 + PR_B + hw * 49152 + (w == 0 ? 0 : w == 1 ? 32768 : 16384), po1 = k.p0 + PR_B + hw * 49152 + (w == 0 ? 0 : 32768);
    const unsigned pdum = k.pg(1) + DUMMY_B, pbc = w == 0 ? k.p0 + CNT(C_BC, hw * 16 + hh) : pdum + 65536 + h * 512 + w * 128;
    Xn X;
    X.init(k); G.init(k);
    X.l16 = l16; X.se = 0;
    unsigned long long now = 0, done = 0;
    unsigned a0 = pdum + (po0 - k.p0 - PR_B), a1 = pdum + (po1 - k.p0 - PR_B);
    const unsigned hv = w == 1 ? 0u : 0x3F800000u, hw2 = w == 1 ? 0u : 0x00800000u;
    S.sg = 0x3F800000u; S.hs = 0x3F800000u;
    G3 A, B;
    A.l16 = l16; B.l16 = l16; B.lwv = vadd(l16, L_W + w * 16384);
#pragma unroll
    for (int t = 0; t < 3; ++t) { B.a0[t] = (f32x4){0.f, 0.f, 0.f, 0.f}; B.a1[t] = B.a0[t]; }
    f32x4 ac[6];
#pragma unroll
    for (int t = 0; t < 6; ++t) clr2(ac[t]);
    sfor<9>([&](auto I) { G.op<CV(I)>(k, l16, 0); });
    G.bad = 0;
    unsigned an = 0;
    do {
        if (X.off != 0) {
            PC8(1, X.sg)
            X.late<2>(k, l16);
            G.check();
        }
_Pragma("unroll 1")
        for (unsigned spins = 0; G.bad != 0 && !spin_fail(k, spins); ++spins) { PC8(0, X.sg) G.again(k, l16); }
        HSTOP(0) HSTAMP(0, 0)
        clr2(ac[5]);
        asm volatile("s_nop 1" : "+v"(ac[5]));
        smfw<50>(ac[5], __builtin_bit_cast(bf16x8, G.p0), W[50], ix);
    do {
        PROF_S(X.sg - 1)
        f32x4 sc[3];
        {
            bf16x8 m[16], wa[10], wb[10], kf[12];
            bf16x4 q[12], km[12];
            unsigned fo[6];
            m[0] = __builtin_bit_cast(bf16x8, G.p0); m[1] = __builtin_bit_cast(bf16x8, G.p1);
            auto put = [&](auto T) { constexpr int t = CV(T); if constexpr (t < 3) lw32(dq + t * 1024, fo[t]); else lw32(dqm + (t - 3) * 1024, fo[t]); };
            auto rd = [&](auto C) { constexpr int c = CV(C), j = c % 20;
                if constexpr (c < 0) { }
                else if constexpr (c < 20) m[j] = lr128(l16 + MADR(j < 10 ? j : j - 8));
                else if constexpr (c < 40) wa[j] = lr128(B.lwv + (j - 2) * 2048);
                else if constexpr (c < 60) wb[j] = lr128(B.lwv + (j - 2) * 2048 + 1024);
                else if constexpr (c < 80) kf[j] = lr128(l16 + L_K + ((j & 1) * 6 + j / 2) * 1024);
                else if constexpr (c < 100) q[j] = lrtr(qp + j * 512);
                else if constexpr (c < 120) km[j] = lrtr(trp + j * 512);
                else if constexpr (c < 140) A.r0<j>();
                else A.r1<j>(); };
#define PB(i) ((i) < 12 ? (i) / 6 : (i) < 32 ? (i) / 4 - 1 : (i) < 44 ? 7 + ((i) - 32) % 3 : (i) < 54 ? 7 + ((i) - 44) / 2 : 12 + ((i) - 54) % 3)
#define PT(i) ((i) < 12 ? ((i) + 5) % 6 : (i) < 32 ? (3 + (i) % 4) % 6 : (i) < 44 ? (3 + ((i) - 32) / 3) % 6 : (i) < 54 ? 1 + ((i) & 1) : 1 + ((i) - 54) / 3)
            sfor<82>([&](auto I) {
                constexpr int i = CV(I) + 1, b = PB(i), t = PT(i), bn = PB(i + 1);
                constexpr int rt[83] = {-1, 2, -1, -1, -1, 22, 42, 3, -1, 23, 43, 4, -1, 24, 44, 5, -1, 25, 45, 6,
                    -1, 26, 46, 7, -1, 8, 9, 27, 47, 28, 48, 29, 49, -1, -1, 60, -1, 61, 62, 63,
                    -1, 10, 64, -1, 11, 12, -1, 13, 14, -1, -1, 80, 82, 83, 84, 85, 86, 87, 67, 88,
                    89, 69, -1, 91, -1, 71, -1, 121, -1, 100, 102, 104, 106, 108, 110, -1, 140, -1, -1, 141, -1, -1, 122};
                constexpr int r2[83] = {-1, -1, -1, -1, -1, -1, -1, -1, -1, -1, -1, -1, -1, -1, -1, -1, -1, -1, -1, -1,
                    -1, -1, -1, -1, -1, -1, -1, -1, -1, -1, -1, -1, -1, -1, -1, -1, -1, -1, -1, -1,
                    -1, -1, -1, -1, -1, -1, -1, -1, -1, -1, -1, 81, -1, 65, -1, 66, -1, -1, -1, -1,
                    68, 90, -1, 70, -1, 120, -1, -1, -1, 101, 103, 105, 107, 109, 111, -1, -1, -1, -1, -1, -1, -1, -1};
                if constexpr (i < 60) {
                    if constexpr (t == 5 && b >= 2) smf<false>(ac[5], m[b], CAT8(wa[b], wb[b]), ix);
                    else smfw<QIDX(t, b < 10 ? b : b - 8) % 52>(ac[t], m[b], W[QIDX(t, b < 10 ? b : b - 8) % 52], ix);
                } else if constexpr (i < 72) { constexpr int j = i - 60; mfa<false, false>(sc[j & 1], kf[j], CAT4(q[j & ~1], q[j | 1])); }
                else if constexpr (i < 77) A.mm<0, false, (i - 72) / 3, (i - 72) % 3>(W);
                else { constexpr int ks = i - 77; mfa<false, ks == 0>(sc[2], CAT4(km[2 * ks], km[2 * ks + 1]), CAT4(q[2 * ks], q[2 * ks + 1])); }
                SCHED0();
                if constexpr (i == 1) { AQ(3, 3) asm volatile("s_barrier" ::: "memory"); AQ(1, 2) AQ(3, 4) }
                else if constexpr (i == 50) { BARL(3); AQ(1, 3) }
                else if constexpr (i == 68) { BARL(3); AQ(1, 4) QSTAMP(0, 1) }
                else if constexpr (i >= 12 && i < 59) { if constexpr (PT(i + 1) == 5) TOUCH(wb[bn % 10]); TOUCH(m[bn]); }
                else if constexpr (i >= 59 && i < 71) { asm volatile("" :: "v"(kf[(i - 59) % 12]), "v"(q[((i - 59) | 1) % 12])); }
                else if constexpr (i == 71) TOUCH(A.x0[0]);
                else if constexpr (i == 74) TOUCH(A.x0[1]);
                else if constexpr (i >= 76 && i < 82) TOUCH(km[(2 * (i - 76) + 1) % 12]);
                if constexpr (i == 8 || i == 30) asm volatile("" :: "{v[60:63]}"(ac[0]), "{v[28:31]}"(ac[1]), "{v[32:35]}"(ac[2]), "{v[64:67]}"(ac[3]), "{v[68:71]}"(ac[4]), "{v[72:75]}"(ac[5]));
                if constexpr (i == 14) AC3(0, 0, 0)
                if constexpr (i == 16) AC3(1, 6, 1)
                if constexpr (i == 18) AC3(2, 7, 2)
                if constexpr (i == 20) AC3(-1, -1, 3)
                if constexpr (i == 22) AC3(-1, -1, 4)
                if constexpr (i == 24) AC3(-1, -1, 5)
                if constexpr (i == 66) AC3(3, -1, 6)
                if constexpr (i == 2) { now = rt_async(); HSTOP(1) }
                if constexpr (i == APOS) { PINS(now); an += (unsigned)now; PINS(an); }
                if constexpr (i == APOS + 1) an = spack(an, X.sg);
                SCHED0();
                rd(IC(rt[i])); rd(IC(r2[i]));
                if constexpr (i == APOS + 4) st32(k.rs, 0, pbc, fo[1]);
                if constexpr (i >= 40 && i < 50 && i % 3 == 1) put(IC(((i - 31) / 3) % 6));
                if constexpr (i == 62 || i == 64) put(IC(i / 2 - 30));
                SCHED0();
                if constexpr (i < 25 && i % 4 != 1) B.out<((i - 2) / 4) * 3 + (i - 2) % 4, 6145>(dq, dq);
                if constexpr (i == APOS + 3) { fo[1] = an; PIN(fo[1]); }
                if constexpr (i >= 37 && i < 49) { constexpr int tt = ((i - 28) / 3) % 6; fold_op<(i - 37) % 3>(ac[tt], fo[tt]); }
                if constexpr (i >= 59 && i < 62) fold_op<i - 59>(ac[1], fo[1]);
                if constexpr (i == 62) fold_op<0>(ac[2], fo[2]);
                if constexpr (i == 63) asm volatile("v_add_f32 %1, %1, %2\n\tv_cvt_pk_bf16_f32 %0, %3, %1" : "=v"(fo[2]), "+v"(ac[2][1]) : "v"(ac[2][3]), "v"(ac[2][0]));
                if constexpr (i == 33) S.mask<0>(sc);
                if constexpr (i >= 51 && i < 59) S.mask<i - 50>(sc);
                if constexpr (i >= 75) S.op<i - 75>(k, sc, a0, a1, hv);
                if constexpr (i == 12) { AQ(3, 5) }
                if constexpr (i == 43) { AQ(3, 6) }
                if constexpr (i == 59) { AQ(3, 7) }
                SCHED0();
            });
        }
        QSTAMP(0, 2)
        {
            unsigned d0;
            auto rest = [&](auto J) { constexpr int j = CV(J);
                if constexpr (j >= 0 && j < 12 && (j % 6 == 0 || j % 3 == 2)) X.op<3 + j / 6, (j % 6 + 1) / 3>(k); };
            sfor<43>([&](auto F) { constexpr int f = CV(F) + 5;
                constexpr int e = f < 6 ? f + 3 : f < 9 ? f - 3 : f;
                A.mm<0, false, e / 6, e % 6>(W);
                if constexpr (f % 6 == 2 || f == 5) TOUCH(A.x1[f / 6]);
                else if constexpr (f == 47) { TOUCH(B.x0[0]); AQ(1, 6) AQ(2, 1) }
                else if constexpr (f % 6 == 5) TOUCH(A.x0[f / 6 + 1]);
                if constexpr (f == 40) { done = rt_async(); QSTAMP(0, 3) AQ(1, 5) }
                if constexpr (f == 21) set_m0(X.dst + 2 * 8192);
                if constexpr (f == 24) { X.sb = X.se + 2 * 16384; PINS(X.sb); }
                if constexpr (f == 42) AC3(4, -1, 7)
                SCHED0();
                if constexpr (f % 6 == 4 && f < 36) A.r0<f / 6 + 2>();
                if constexpr (f % 6 == 1 && f < 42) A.r1<f / 6 + 1>();
                if constexpr (f >= 39 && f < 43) B.rd<true, ((f - 39) / 2) * 6 + (f - 39) % 2>();
                if constexpr (f == 26 || f == 31) X.op<2, f / 15>(k);
                SCHED0();
                S.op<f + 3>(k, sc, a0, a1, hv);
                rest(IC(f - 43));
                if constexpr (f == 44) B.lwv = vadd(l16, L_W + w * 16384);
                SCHED0(); });
            B.go<30, true, 0, 48, 8>(W, [&](auto F) { constexpr int f = CV(F);
                rest(IC(f + 5));
                if constexpr (f == 9) AC3(5, -1, -1)
                if constexpr (f == 2) { a0 = (X.sg & 3) * PR_SZ; PINS(a0); }
                if constexpr (f == 4) { a1 = a0 + po1; a0 += po0; PINS(a0); PINS(a1); }
                if constexpr (f == 8) { S.sg = (X.sg & 4) ? 0xBF800000u : 0x3F800000u; PINS(S.sg); }
                if constexpr (f == 10) { S.hs = S.sg - hw2; PINS(S.hs); }
                if constexpr (f == 14 || f == 16) G.op<f / 16>(k, l16, X.sg);
                if constexpr (f == 22 || f == 28 || f == 32) X.next<(f - 22) / 5>(k, l16);
                if constexpr (f == 35) asm volatile("s_waitcnt vmcnt(6)\n\ts_barrier" ::: "memory");
                if constexpr (f == 20) AC3(6, 1, -1)
                if constexpr (f == 26) A.home<0>();
                if constexpr (f >= 36) A.rd<false, f + 12>();
                if constexpr (f == 38 || f == 40 || f == 45 || f == 46) X.next<(f == 38 ? 5 : f == 40 ? 6 : f - 42)>(k, l16);
                if constexpr (f == 47) { A.pre<false, 8>(); AQ(1, 7) AQ(2, 2) } });
            A.go<0, false, 48, 120, 20>(W, [&](auto F) { constexpr int f = CV(F), i = f - XPOS;
                if constexpr (f == 56) B.home<1>();
                if constexpr (i == 0 || i == 2 || i == 6) X.next<7 + i / 2>(k, l16);
                if constexpr (i >= 3 && i < 15 && ((i - 3) % 6 == 0 || (i - 3) % 3 == 2)) X.op<(i - 3) / 6, ((i - 3) % 6 + 1) / 3>(k);
                if constexpr (f == 58) asm volatile("s_waitcnt vmcnt(1)\n\ts_barrier" ::: "memory");
                if constexpr (f == 57) AC3(7, 2, -1)
                G.ask<f - RPOS>(k, l16);
                if constexpr (f >= 108) B.rd<true, f - 60>();
                SCHED0();
                G.red<f - 110>();
                if constexpr (f == 116) asm volatile("v_bfe_u32 %0, %1, 8, 1\n\tv_lshl_or_b32 %0, %0, 3, %1" : "=&v"(d0) : "v"(l16));
                if constexpr (f == 118) asm volatile("v_xor_b32 %0, %1, %0" : "+v"(d0) : "s"(L_K + (w >> 1) * 3072 + (w & 1) * 8));
                if constexpr (f == 119) { B.pre<true, 8>(); AQ(2, 3) } });
            B.go<30, true, 48, 120, 20>(W, [&](auto F) { constexpr int f = CV(F), i = f - 48;
                constexpr int ko[26] = {0, 1, 2, 4, 3, 6, 5, 8, 7, 9, 10, 11, 12, 13, 14, 16, 15, 18, 17, 20, 19, 21, 22, 23, -1, -1};
                G.hold<f - GPOS + 5>(X.sg);
                if constexpr (i < 24) { SCHED0(); A.out4<ko[i % 26], 0>(d0); }
                G.at<f - GPOS>(k, l16);
                if constexpr (f == 114) { lgkm0(done); PINS(G.rt); }
                if constexpr (f == 115) { PINS(now); HTRACE(X.sg - 2) an = (unsigned)done - (unsigned)now; PINS(an); }
                if constexpr (f == 116) { an = min(an, 200u); PINS(an); }
                if constexpr (f == 117) { an += DQ; PINS(an); }
                SCHED0();
                if constexpr (i == 28) AC3(-1, 3, -1)
                if constexpr (i == 50) AC3(-1, 4, -1)
                if constexpr (i == 62) AC3(-1, 5, -1)
                if constexpr (i == 23) { AQ(2, 4) }
                if constexpr (i == 47) { AQ(2, 5) }
                if constexpr (i == 65) { AQ(2, 6) }
                if constexpr (f == 119) { AQ(2, 7) }
                if constexpr (f == 112) asm volatile("" :: "{v[12:15]}"(G.v0), "{v[16:19]}"(G.v1), "{v[20:23]}"(G.p0), "{v[24:27]}"(G.p1), "{v9}"(B.lwv) TRHINT);
                if constexpr (f == 119) G.also(X.off);
                if constexpr (f >= 113 && f < 119) clr2(ac[(f + 6 - 113 + 5) % 6]); });
            static_assert(GPOS >= 70 && GPOS <= 98 && XPOS % 6 == 2 && RPOS > XPOS && RPOS + 1 < XPOS + 5, "GPOS");
            QSTAMP(0, 4) QSTAMP(0, 5)
            HFLUSH(s)
        }
        AST(0)
        PROF_ONLY(G.wait<0>();)
        HSTOP(0) HSTAMP(s, 0) AQ(1, 1) AQ(3, 1)
        G.all();
        SCHED0();
        smfw<50>(ac[5], __builtin_bit_cast(bf16x8, G.p0), W[50], ix);
        AQ(3, 2)
        SCHED0();
    } while (G.bad == 0);
    } while (X.sg != 65u);
    PROF_OUT
}

#define OIDX(t, b) (22 + (t) * 3 + (b))
DEVI void vo_role(const Params& p, Ctx& k, char* smem, int h) {
    PROF_DECL
    const int w = k.w;
    if (k.c) sleep_until(k.t0 + WD1);
    bf16x16 W[52];
    {
        const int l = k.l;
        bf16x8 lo[10], wt[16];
        const u16* wv = p.Wv + h * 192 + w * 48;
        const u16* wo = p.Wo + (size_t)h * 192 * 640 + w * 160;
        wstream<22, 6, 6>(smem + w * 36864,
            [&](auto U, unsigned sl) { constexpr int u = CV(U);
                if constexpr (u >= 12) { wp_issue<3, (u % 6) * 6144>(wv + (size_t)(u - 12) * 64 * 6144, 6144, sl, l); wp_issue<3, (u % 6) * 6144 + 3072>(wv + (size_t)((u - 12) * 64 + 32) * 6144, 6144, sl, l); }
                else { wp_issue<5, (u % 6) * 6144>(wo + (u >> 1) * 32 * 640 + (u & 1) * 80, 640, sl, l); dma1<(u % 6) * 6144 + 5120>(wo, l * 16, sl); } },
            [&](auto U, char* sp) { constexpr int u = CV(U);
                if constexpr (u < 8) hello(k, 0);
                if constexpr (u >= 12) {
                    constexpr int b = u - 12;
#pragma unroll
                    for (int t = 0; t < 3; ++t) {
                        bf16x8 x = wp_read<3, true>(sp, t, l), y = wp_read<3, true>(sp + 3072, t, l);
                        if (t == 2 && b >= 2) { wt[(2 * (b - 2)) & 15] = x; wt[(2 * (b - 2) + 1) & 15] = y; } else W[(t * 10 + b) % 52] = CAT8(x, y);
                    }
                } else {
                    constexpr int ks = u >> 1, b = ks >> 1;
#pragma unroll
                    for (int i = 0; i < 5; ++i) {
                        const int t = (u & 1) * 5 + i;
                        bf16x8 f = wp_read<5, false>(sp, i, l);
                        if (ks & 1) W[OIDX(t, b)] = CAT8(lo[t], f); else lo[t] = f;
                    }
                } });
        RAWBAR()
#pragma unroll
        for (int i = 0; i < 16; ++i) *(bf16x8*)(smem + L_W + w * 16384 + i * 1024 + l * 16) = wt[i];
        go_wait(k, 0);
    }
    RSTAMP(2)
    unsigned l16, lwv, ix, dq0, trp, vo;
    {
        const int l = k.l, g = l >> 4, n = l & 15, q = (l >> 2) & 3, pp = l & 3;
        l16 = l * 16; lwv = l16 + L_W + w * 16384; ix = SPIX(l);
        dq0 = L_QM + (24 * w + (n >> 2) * 2 + (n & 1)) * 32 + g * 8 + ((n >> 1) & 1) * 4;
        trp = L_QM + (16 * (g >> 1) + 4 * (g & 1) + 8 * (q >> 1) + (q & 1)) * 32 + pp * 8;
        vo = EXP(h * 512) + (g >> 1) * 256 + n * 16 + (g & 1) * 8;
        u32x8 r = {dq0, trp, vo, ix, l16, lwv, 0u, 0u};
        PIN(r);
        dq0 = r[0]; trp = r[1]; vo = r[2]; ix = r[3]; l16 = r[4]; lwv = r[5];
    }
    const unsigned pdum = k.pg(1) + DUMMY_B, pbc = w == 0 ? k.p0 + CNT(C_BC, (h >> 3) * 16 + 8 + (h & 7)) : pdum + 98304 + h * 512 + w * 128;
    Gather G;
    G3 V;
    V.l16 = l16; V.lwv = lwv;
    unsigned long long now = 0, done = 0;
    f32x4 y[20];
    bf16x8 v[12];
    u32x4 o[10];
    o[7] = (u32x4){0u, 0u, 0u, 0u};
    clr2(y[17]);
#pragma unroll
    for (int i = 0; i < 12; ++i) v[i] = __builtin_bit_cast(bf16x8, o[7]);
    unsigned yb = k.yx(7, w) + EXP(h * 1024);
    G.mx = ~0u;
    Xn X;
    X.init(k); G.init(k);
    X.l16 = l16; X.se = 0;
    G.base -= 32768;
    const unsigned ypar = (unsigned)(k.par >> (PG_YX + (w >> 1))), yo = ((unsigned)(PG_YX + (w >> 1)) << 21) + (w & 1) * 1048576 + EXP(h * 1024);
    const unsigned ym0 = k.ym(0) + w * 163840, ym2 = k.ym(2) + w * 163840;
    unsigned ym = pdum + w * 163840, ys, t, c = ~0u, an = 0;
#define YT(u) ((u) < 10 ? 2 * ((u) % 5) : ((u) - 10) | 1)
#define YR(u) ((u) < 10 ? (u) / 5 : (u) & 1)
    auto yout = [&](auto U, auto J) { constexpr int u = CV(U), j = CV(J), t = YT(u), rt = YR(u);
        if constexpr (j == 0) { o[t][2 * rt] = cvtpk(y[u][0], y[u][1]); o[t][2 * rt + 1] = cvtpk(y[u][2], y[u][3]); TOUCH(o[t][2 * rt]); TOUCH(o[t][2 * rt + 1]); }
        if constexpr (j == 1 && rt) __builtin_amdgcn_raw_buffer_store_b128(o[t], k.rs, (int)l16, (int)yb, AUX_SC1);
        if constexpr (j == 2 && rt) { yb += t == 8 ? -7 * 65536 : 131072; PINS(yb); TOUCH(o[t]); } };
    auto ymm = [&](auto I) {
        constexpr int i = CV(I), ks = i % 6, u = i / 6, id = OIDX(YT(u), ks >> 1);
        mfw<id, ks & 1, true, ks == 0>(y[u], W[id], v[YR(u) * 6 + ks]);
        SCHED0();
        if constexpr (u > 0 && ks >= 2) yout(IC((u + 19) % 20), IC(ks - 2)); };
    do {
        PC8(1, c)
        X.late<5>(k, l16);
    do {
        PROF_S(X.sg - 1)
        f32x4 ac[3];
        asm volatile("s_waitcnt vmcnt(7)\n\ts_barrier" ::: "memory");
        SCHED0();
        sfor<13>([&](auto I) { constexpr int i = CV(I) + 107;
            ymm(IC(i));
            if constexpr (i == 107) { AST(5) st32(k.rs, 0, pbc + 4, G.mx); }
            if constexpr (i == 119) V.pre<true, 0>();
            G.ask<i - 112>(k, l16);
            if constexpr (i == 108) V.r0<0>();
            if constexpr (i == 109) V.r1<0>();
            if constexpr (i == 114) V.r0<1>();
            if constexpr (i == 115) V.r1<1>();
            SCHED0(); });
        HSTAMP(s, 4)
        V.run<0, true, 0, 16, 17, true>(W, [&](auto I) { constexpr int i = CV(I);
            if constexpr (i >= 2) yout(IC(19), IC(i - 2));
            if constexpr (i == 8) { t = (X.sg & 7) * 2; PINS(t); }
            if constexpr (i == 10) { yb = (ypar >> t) & 1; PINS(yb); }
            if constexpr (i == 14) { t = (t << 21) + yo; PINS(t); }
            if constexpr (i == 16) { yb = (yb << 13) + t; PINS(yb); }
            if constexpr (i == 20) { c = X.sg; PINS(c); }
            if constexpr (i == 22) { G.a = max(c, 1u) << 15; PINS(G.a); }
            if constexpr (i == 28) G.op<1>(k, l16, 0);
            if constexpr (i == 40) { an = min(an, 200u) + DV; PINS(an); }
            if constexpr (i == 32 || i == 34 || i == 38) X.next<(i - 31) / 3>(k, l16);
            if constexpr (i == 23 || i == 29) { AAC(4 + i / 29) }
            if constexpr (i == 83 || i == 89) { AAC(6 + i / 89) }
            if constexpr (i == YPOS) { AST(6) }
            if constexpr (i == YPOS + 1) { AST(7) }
            if constexpr (i == YPOS) { asm volatile("s_waitcnt vmcnt(0)\n\ts_barrier" ::: "memory"); }
            if constexpr (i == YPOS - 2) G.mx = vshl1(c, 0);
            if constexpr (i == YPOS + 1) st32(k.rs, 0, pbc + 4, G.mx);
            SCHED0();
            G.red<i - YPOS - 2>();
            G.hold<i - GPOSY * 6 + 5>(c);
            G.at<i - GPOSY * 6>(k, l16);
            if constexpr (i == 90) PINS(G.rt);
            if constexpr (i == 95) V.pre<true, 16>();
            if constexpr (i >= 92 && i < 95) clr2(ac[(i - 92) % 3]); });
        static_assert(GPOSY * 6 + 2 > YPOS + 1 && GPOSY <= 13, "GPOSY");
        HSTAMP(s, 5)
        HFLUSH(s - 1)
        f32x4 acc[10];
        f32x2 z[20];
        auto clr = [&](auto I) { constexpr int i = CV(I); asm volatile("v_mov_b64 %0, 0" : "=v"(z[i])); if constexpr (i & 1) acc[i >> 1] = __builtin_shufflevector(z[i - 1], z[i], 0, 1, 2, 3); };
        bf16x4 al[3], ah[3];
        unsigned d0, d1;
        {
            bf16x8 m[10], wa[10], wb[10];
            auto rd = [&](auto R) { constexpr int r = CV(R), b = 2 + r / 3;
                if constexpr (b < 10) {
                    if constexpr (r % 3 == 0) m[b] = lr128(l16 + MADR(b));
                    if constexpr (r % 3 == 1) wa[b] = lr128(lwv + (b - 2) * 2048);
                    if constexpr (r % 3 == 2) wb[b] = lr128(lwv + (b - 2) * 2048 + 1024);
                } else if constexpr (b < 13) {
                    if constexpr (r % 3 == 0) V.r0<b + 7>();
                    if constexpr (r % 3 == 1) V.rw<b + 7>();
                    if constexpr (r % 3 == 2) V.r1<b + 7>();
                } };
            AST(0)
            G.wait<0>();
            HSTOP(0) HSTAMP(s, 0) AST(1)
            V.run<0, true, 16, 17, 0, true>(W, [&](auto I) { constexpr int i = CV(I);
                G.fin<i>(0);
                if constexpr (i == 3) {
                    if (__builtin_expect(G.bad != 0, 0)) {
                        unsigned spins = 0;
_Pragma("unroll 1")
                        do { PC8(0, c) G.again(k, l16); } while (G.bad != 0 && !spin_fail(k, spins++));
                        HSTOP(0) HSTAMP(s, 0)
                    }
                    asm volatile("s_barrier" ::: "memory");
                    AST(2)
                }
                if constexpr (i == 4) { now = rt_async(); HSTOP(1) SCHED0(); }
                if constexpr (i >= 3) rd(IC(i - 3)); });
            m[0] = __builtin_bit_cast(bf16x8, G.p0); m[1] = __builtin_bit_cast(bf16x8, G.p1);
            sfor<30>([&](auto I) {
                constexpr int i = CV(I), b = i / 3, t = i % 3;
                if constexpr (t == 2 && b >= 2) smf<false>(ac[2], m[b], CAT8(wa[b], wb[b]), ix); else smfw<t * 10 + b>(ac[t], m[b], W[t * 10 + b], ix);
                SCHED0();
                if constexpr (i >= 6 && i < 29 && t != 0) { if constexpr (t == 1) TOUCH(wb[b]); else TOUCH(m[(b + 1) % 10]); }
                if constexpr (i == 29) V.pre<true, 17>();
                if constexpr (i == 2) { PINS(now); an += (unsigned)now; PINS(an); }
                if constexpr (i == 3) an = spack(an, c);
                SCHED0();
                if constexpr (i < 27) rd(IC(3 + i));
                if constexpr (i == 5) st32(k.rs, 0, pbc, d0);
                SCHED0();
                if constexpr (i == 4) { d0 = an; PIN(d0); }
                if constexpr (i == 26) X.next<3>(k, l16);
                if constexpr (i == 28) d0 = vadd(l16, L_K + ((3 * w) >> 1) * 1024 + ((3 * w) & 1) * 8);
                if constexpr (i == 29) d1 = vadd(l16, L_K + ((3 * w + 1) >> 1) * 1024 + ((3 * w + 1) & 1) * 8);
                SCHED0();
            });
            unsigned fo[3];
            V.run<0, true, 17, 19, 0, true>(W, [&](auto I) { constexpr int i = CV(I);
                if constexpr (i < 3) { rd(IC(30 + i)); SCHED0(); fold_op<0>(ac[i], fo[i]); }
                if constexpr (i == 3 || i == 4 || i == 6) { constexpr int t = i / 2 - 1; asm volatile("v_add_f32 %1, %1, %2\n\tv_cvt_pk_bf16_f32 %0, %3, %1" : "=v"(fo[t]), "+v"(ac[t][1]) : "v"(ac[t][3]), "v"(ac[t][0])); }
                if constexpr (i == 5 || i == 7 || i == 8) lw32(dq0 + ((i - 4) / 2) * 256, fo[(i - 4) / 2]);
                if constexpr (i == 9 || i == 10) clr2(acc[i - 9]);
                if constexpr (i == 11) BARL(0) });
        }
        HSTAMP(s, 1) AST(3)
        {
            unsigned fo[2];
            auto fin2 = [&](auto P, auto J) { fold2<CV(J)>(acc[2 * CV(P)], acc[2 * CV(P) + 1], fo); };
            V.run<0, true, 19, 20, 0, true>(W, [&](auto I) { constexpr int i = CV(I);
                if constexpr (i == 0) { al[0] = lrtr(trp); ah[0] = lrtr(trp + 64); }
                if constexpr (i == 1) al[1] = lrtr(trp + 1024);
                if constexpr (i == 2) ah[1] = lrtr(trp + 1088);
                if constexpr (i == 3) al[2] = lrtr(trp + 2048);
                if constexpr (i == 4) ah[2] = lrtr(trp + 2112);
                SCHED0();
                if constexpr (i >= 1) clr(IC(3 + i)); });
            sfor<30>([&](auto I) { constexpr int i = CV(I), b = (i % 6) >> 1, t = 2 * (i / 6) + (i & 1), id = OIDX(t, b);
                smfw<id>(acc[t], CAT4(al[b], ah[b]), W[id], ix);
                SCHED0();
                if constexpr (i == 1 || i == 3) TOUCH(ah[(i + 1) / 2]);
                if constexpr (i == 29) TOUCH(v[0]);
                if constexpr (i == 21) BARL(0)
                SCHED0();
                if constexpr (i == XLD) X.next<4>(k, l16);
                if constexpr (i >= 21 && i < 27) v[i - 21] = lr128(l16 + L_K + (i - 21) * 1024);
                SCHED0();
                V.out<i - 1, 0>(d0, d1);
                SCHED0();
                if constexpr (i % 3 == 0 && i < 18) clr(IC(9 + i / 3));
                if constexpr (i == 16) clr(IC(15));
                if constexpr (i >= 18 && i < 21) clr(IC(i - 2));
                if constexpr (i == 22) clr(IC(19));
                if constexpr (i >= 23 && i < 29) fold_op<(i - 23) / 2>(acc[(i - 23) & 1], fo[(i - 23) & 1]);
                if constexpr (i == 29) { st64(k.rs, vo, ym, (u32x2){fo[0], fo[1]}); SCHED0(); fin2(IC(1), IC(0)); }
                SCHED0(); });
            HSTAMP(s, 2)
            sfor<107>([&](auto I) { constexpr int i = CV(I), q = i < 8 ? i + 4 : i + 3;
                ymm(I);
                if constexpr (i < 5 || (i >= 29 && i < 35)) TOUCH(v[(i < 5 ? i + 1 : i - 23) % 12]);
                if constexpr (i < 13 && i != 8) {
                    if constexpr (q % 3 == 1) { ym += 32768; PINS(ym); }
                    if constexpr (q % 3 == 2 && i >= 5) __builtin_amdgcn_s_setprio(0);
                    SCHED0();
                    if constexpr (q % 3 == 0) { st64(k.rs, vo, ym, (u32x2){fo[0], fo[1]}); SCHED0(); }
                    if constexpr (q < 15) fin2(IC(q / 3), IC(q % 3));
                }
                if constexpr (i == 13) done = rt_async();
                if constexpr (i >= 15 && i < 23 && i != 18 && i != 20) v[6 + i - 15 - (i > 18) - (i > 20)] = lr128(l16 + L_K + (6 + i - 15 - (i > 18) - (i > 20)) * 1024);
                if constexpr (i == 12 || i == 13) X.next<i - 7>(k, l16);
                if constexpr (i == 15) { SCHED0(); X.next<7>(k, l16); }
                if constexpr (i == 18 || i == 23) X.next<8 + 2 * (i / 23)>(k, l16);
                if constexpr (i >= 19 && i < 37 && (i % 6 == 1 || i % 3 == 0)) X.op<(i - 19) / 6, ((i - 19) % 6 + 1) / 3>(k);
                if constexpr (i >= 66 && i < 90 && i % 3 == 0 && i % 12 != 3) X.op<3 + (i - 66) / 12, ((i - 66) / 3) % 4>(k);
                if constexpr (i == 90) { ym = (c & 2) ? ym2 : ym0; PINS(ym); }
                if constexpr (i == 91) { ys = ym + EXP(327680); PINS(ys); }
                if constexpr (i == 96) { ym = (c & 1) ? ys : ym; PINS(ym); }
                if constexpr (i == 60) { HSTAMP(s, 3) }
                if constexpr (i == 42 || i == 43 || i == 48 || i == 49) { AAC((i - 42) / 3 + (i & 1)) }
                if constexpr (i == 102) lgkm0(done);
                if constexpr (i == 103) { PINS(now); HTRACE(c - 1) an = (unsigned)done - (unsigned)now; PINS(an); }
                if constexpr (i == 105) G.mx = vshl1(c, 1);
                SCHED0(); });
            AST(4)
        }
    } while (X.off == 0);
    } while (X.sg != 65u);
    HFLUSH(63)
    PROF_OUT
}

DEVI void ln_store(const Ctx& k, const float (&v)[40], unsigned dst, int row  ) {
    f32x2 s2[4] = {{0.f, 0.f}, {0.f, 0.f}, {0.f, 0.f}, {0.f, 0.f}};
#pragma unroll
    for (int i = 0; i < 40; i += 2) s2[(i >> 1) & 3] += (f32x2){v[i], v[i + 1]};
    const f32x2 st = (s2[0] + s2[1]) + (s2[2] + s2[3]);
    float m = row16sum(st[0] + st[1]) * (1.f / 640.f);
    f32x2 q2[4] = {{0.f, 0.f}, {0.f, 0.f}, {0.f, 0.f}, {0.f, 0.f}};
#pragma unroll
    for (int i = 0; i < 40; i += 2) { const f32x2 d = (f32x2){v[i], v[i + 1]} - (f32x2){m, m}; q2[(i >> 1) & 3] += d * d; }
    const f32x2 qt = (q2[0] + q2[1]) + (q2[2] + q2[3]);
    const float q = row16sum(qt[0] + qt[1]);
    const float var = bfr(q * (1.f / 640.f));
    m = bfr(m);
    const float rden = __builtin_amdgcn_rcpf(bfr(__builtin_amdgcn_sqrtf(bfr(var + 1e-5f))));
    const int n = k.l & 15;
    const unsigned vo = (n >> 2) * 2048 + (row >> 4) * 1024 + ((n & 3) * 16 + (row & 15)) * 16;
#pragma unroll
    for (int c = 0; c < 5; ++c) {
        u32x4 o;
#pragma unroll
        for (int i = 0; i < 4; ++i) { unsigned d = cvtpk(v[c * 8 + 2 * i] - m, v[c * 8 + 2 * i + 1] - m); o[i] = cvtpk(blo(d) * rden, bhi(d) * rden); }
        st128(k.rs, vo, dst + c * 16384, o); st128(k.rs, vo, dst + c * 16384 + 8192, o);
    }
}
DEVI void unpack8(float* v, u32x4 a) {
#pragma unroll
    for (int i = 0; i < 4; ++i) { v[2 * i] = blo(a[i]); v[2 * i + 1] = bhi(a[i]); }
}
DEVI void row_role(const Params& p, const Ctx& k, char* smem, int rt) {
    PROF_DECL
    const int l = k.l, n = l & 15;
    const unsigned fo = k.w * 2048 + rt * 1024;
    RSTAMP(2)
    const __amdgpu_buffer_rsrc_t rs = __builtin_amdgcn_make_buffer_rsrc(k.c ? (void*)k.ws : (void*)p.x, 0, 0x7fffffff, 0x00020000);
    const unsigned vs = k.c ? l * 16 : n * 1280 + (l >> 4) * 16, cs = k.c ? 16384 : 256, lim = k.c ? 0xFFFFFFFEu : 0xFFFFFFFFu;
    for (int sg = k.c ? 0 : 8; sg < 64; ++sg) {
        if (k.c) prog_wait<5>(k, k.p0 + CNT(C_X2, rt * 5), sg);
        STOP(sg) STAMP(sg, 0)
        const unsigned src = k.c ? k.seg(PG_X2, sg) + fo : (sg * 32 + rt * 16) * 1280 + k.w * 64, dst = k.seg(PG_XNA, sg) + fo;
        u32x4 a[5];
_Pragma("unroll 1")
        for (unsigned spins = 0;; ++spins) {
            unsigned mx = 0;
#pragma unroll
            for (int c = 0; c < 5; ++c) a[c] = ld128(rs, vs, src + c * cs);
#pragma unroll
            for (int c = 0; c < 5; ++c) mx = max(mx, umax4(a[c]));
            if (__all(mx <= lim) || spin_fail(k, spins)) break;
            PCOUNT(0)
            asm volatile("" ::: "memory");
        }
        STAMP(sg, 1)
        f32x2 v[20], s2[4], q2[4];
#pragma unroll
        for (int i = 0; i < 20; ++i) { v[i] = (f32x2){blo(a[i >> 2][i & 3]), bhi(a[i >> 2][i & 3])}; s2[i & 3] = i < 4 ? v[i] : s2[i & 3] + v[i]; }
        const f32x2 st = (s2[0] + s2[1]) + (s2[2] + s2[3]);
        const float sm = xor32add(xor16add(st[0] + st[1])), mu = sm * (1.f / 160.f);
#pragma unroll
        for (int i = 0; i < 20; ++i) { const f32x2 d = v[i] - mu; q2[i & 3] = i < 4 ? d * d : __builtin_elementwise_fma(d, d, q2[i & 3]); }
        const f32x2 qt = (q2[0] + q2[1]) + (q2[2] + q2[3]);
        *(f32x2*)(smem + n * 32 + k.w * 8) = (f32x2){sm, xor32add(xor16add(qt[0] + qt[1]))};
        RAWBAR()
        const f32x4 e0 = *(const f32x4*)(smem + n * 32), e1 = *(const f32x4*)(smem + n * 32 + 16);
        float m = ((e0[0] + e0[2]) + (e1[0] + e1[2])) * (1.f / 640.f);
        const f32x4 d = (f32x4){e0[0], e0[2], e1[0], e1[2]} - 160.f * m;
        const float var = bfr((((e0[1] + e0[3]) + (e1[1] + e1[3])) + ((d[0] * d[0] + d[1] * d[1]) + (d[2] * d[2] + d[3] * d[3])) * (1.f / 160.f)) * (1.f / 640.f));
        m = bfr(m);
        const float rden = __builtin_amdgcn_rcpf(bfr(__builtin_amdgcn_sqrtf(bfr(var + 1e-5f))));
#pragma unroll
        for (int c = 0; c < 5; ++c) {
            u32x4 o;
#pragma unroll
            for (int i = 0; i < 4; ++i) { const f32x2 x = v[c * 4 + i] - m; o[i] = cvtpk(bfr(x[0]) * rden, bfr(x[1]) * rden); }
            st128(k.rs, l * 16, dst + c * 16384, o);
        }
        STAMP(sg, 2)
        DRAIN();
        __builtin_amdgcn_s_barrier();
        st32(k.rs, (k.w == 0 && l == 0) ? 0u : OOB, k.p0 + CNT(C_XN, rt), (unsigned)(sg - (k.c ? 0 : 8)));
        STAMP(sg, 3) STAMP(sg, 4) STAMP(sg, 5)
        PFLUSH(sg)
    }
    PROF_OUT
}

struct Red {
    u32x4 Y[16];
    template <int I0, int I1> DEVI void yload(const Ctx& k, int j, int sg) {
        const unsigned a = k.yx(sg, j / 5) + (j % 5) * 131072 + k.w * 16384;
#pragma unroll
        for (int i = I0; i < I1; ++i) Y[i] = ld128(k.rs, k.l * 16 + (i & 3) * 1024, a + ((i >> 2) & 1) * 4096 + (i >> 3) * 65536);
    }
};
DEVI unsigned y_get(const Ctx& k) { return ld32(k.rs, (k.l & 7) * 128, k.p0 + CNT(C_BC, k.w * 16 + 8) + 4); }
DEVI void y_wait(const Ctx& k, int s) {
_Pragma("unroll 1")
    for (unsigned spins = 0; !__all((int)y_get(k) >= s); ++spins) {
        asm volatile("" ::: "memory");
        __builtin_amdgcn_s_sleep(2);
        if (spin_fail(k, spins)) break;
    }
    __builtin_amdgcn_fence(__ATOMIC_ACQUIRE, "wavefront");
}
DEVI void wait_until(unsigned t) { while ((int)((rclk() - t) << 16) < 0) { if (WUS) __builtin_amdgcn_s_sleep(WUS); } }
#define DPPMAX(x, c) x = max(x, __builtin_amdgcn_update_dpp(0, x, c, 0xf, 0xf, false))
#define TSLP 1
#define TWU 0
DEVI void wait_until2(unsigned t) { while ((int)((rclk() - t) << 16) < 0) { if (TWU) __builtin_amdgcn_s_sleep(TWU); } }
#define WAIT_BC(s, BSLP) { \
    unsigned bc; \
    _Pragma("unroll 1") for (;; ++spins) { \
        bc = ld32(k.rs, (l & 15) * 128, k.p0 + CNT(C_BC, w * 16)); \
        DRAIN(); \
        if (__all(((int)bc >> 16) > s)) break; \
        __builtin_amdgcn_s_sleep(BSLP); \
        if (spin_fail(k, spins)) break; \
    } \
    const unsigned now = rclk(); \
    int d = (int)((bc - now) << 16); \
    d = ((int)bc >> 16) > s + 1 || d > (256 << 16) ? -(64 << 16) : d; \
    DPPMAX(d, 0xB1); DPPMAX(d, 0x4E); DPPMAX(d, 0x141); \
    tq = now + (__builtin_amdgcn_readlane(d, 0) >> 16); tv = now + (__builtin_amdgcn_readlane(d, 8) >> 16); }
DEVI void mred_role(const Params& p, const Ctx& k, char* smem, int j) {
    PROF_DECL
    const int l = k.l, w = k.w, g = l >> 4, n = l & 15;
    const unsigned ix = SPIX(l);
    const unsigned vp = (g * 16 + ((n >> 1) & 1) * 8 + (n >> 2) * 2 + (n & 1)) * 16;
    const unsigned v16 = (g >> 1) * 1024 + (g & 1) * 256 + n * 16, yo = j * 32768 + EXP(w * 4096);
    const u32x4 ones = {0xFFFFFFFFu, 0xFFFFFFFFu, 0xFFFFFFFFu, 0xFFFFFFFFu};
    auto arm = [&](int s) {
#pragma unroll
        for (int i = 0; i < 4; ++i) st128(k.rs, l * 16 + i * 1024, k.ym(s) + yo, ones);
    };
    const unsigned pv = l == 0 && j % 5 == 0 ? (j / 5) * 128 + w * 32 : OOB;
    Red R;
    RSTAMP(2)
    arm(1);
#pragma unroll
    for (int i = 0; i < 8; ++i) st128(k.rs, (i || w > 1 || g > 1) ? g * 32768 + n * 16 : OOB, k.memf(i * 8 + (w >> 1) * 4) + EXP((2 * j + (w & 1)) * 256), ones);
#pragma unroll
    for (int i = 0; i < 4; ++i) { const unsigned q = min(i * 80 + j * 4 + w, 287); st128(k.rs, l * 16, k.p0 + PR_B + PR_SZ + EXP(q * 1024), ones); }
    y_wait(k, 2);
    R.yload<0, 16>(k, j, 0);
    for (int s = 0; s < 63; ++s) {
        STOP(s) HSTAMP(s, 0)
        const unsigned ap = k.p0 + PR_B + (s & 3) * PR_SZ + w * 49152, am = k.ym(s) + yo;
        const unsigned ph = (s & 4) ? 0x80008000u : 0u;
        unsigned spins = 0, tq, tv;
        unsigned fl;
        WAIT_BC(s, BSLP)
        st32(k.rs, pv, k.p0 + CNT(C_XB, 0), spack(((int)((tq - tv) << 16) < 0 ? tv : tq) + RDY, s + 2));
        STAMP(s, 5)
        char* rb = smem + (s & 1) * 4096;
_Pragma("unroll 1")
        for (spins = 0;; ++spins) {
            u32x4 ym[4], pm[5];
            f32x4 acc[2];
            if (spins == 0) wait_until(tv);
            asm volatile("" ::: "memory");
#pragma unroll
            for (int i = 0; i < 4; ++i) ym[i] = ld128(k.rs, v16 + (i >> 1) * 2048 + (i & 1) * 512, am);
            fl = ld32(k.rs, 0, (k.p0 ^ 8192u) + CNT(C_X1, j));
            asm volatile("" ::: "memory");
            if (spins == 0) { wait_until(tq); STAMP(s, 1) }
            asm volatile("" ::: "memory");
#pragma unroll
            for (int i = 0; i < 5; ++i) pm[i] = ld128(k.rs, i < 4 ? l * 16 + i * 1024 : vp, ap + (i >> 2) * 4096);
            acc[0] = (f32x4){0.f, 0.f, 0.f, 0.f}; acc[1] = acc[0];
            const u32x8 y0 = {ym[0][0], ym[1][0], ym[0][2], ym[1][2], ym[2][0], ym[3][0], ym[2][2], ym[3][2]};
            const u32x8 y1 = {ym[0][1], ym[1][1], ym[0][3], ym[1][3], ym[2][1], ym[3][1], ym[2][3], ym[3][3]};
            unsigned mx = 0;
#pragma unroll
            for (int i = 0; i < 4; ++i) mx = max(mx, umax4(ym[i]));
            unsigned tg = mx == 0xFFFFFFFFu ? 0x80000000u : 0u;
            asm volatile("s_nop 1" : "+v"(acc[0]), "+v"(acc[1]), "+v"(tg) : "v"(y0), "v"(y1), "v"(ix));
            SCHED0();
#pragma unroll
            for (int i = 0; i < 5; ++i) {
                const unsigned u0 = __builtin_amdgcn_bitop3_b32(pm[i][0], pm[i][1], ph, 0x7e), u1 = __builtin_amdgcn_bitop3_b32(pm[i][2], pm[i][3], ph, 0x7e);
                SCHED0();
                if (i < 4) smf<false>(acc[0], __builtin_bit_cast(bf16x8, pm[i]), __builtin_bit_cast(bf16x16, __builtin_shufflevector(R.Y[2 * i], R.Y[2 * i + 1], 0, 1, 2, 3, 4, 5, 6, 7)), ix);
                else smf<false>(acc[0], __builtin_bit_cast(bf16x8, pm[4]), __builtin_bit_cast(bf16x16, y0), ix);
                tg = (tg | u0) | u1;
                SCHED0();
                if (i < 4) smf<false>(acc[1], __builtin_bit_cast(bf16x8, pm[i]), __builtin_bit_cast(bf16x16, __builtin_shufflevector(R.Y[8 + 2 * i], R.Y[9 + 2 * i], 0, 1, 2, 3, 4, 5, 6, 7)), ix);
                else smf<false>(acc[1], __builtin_bit_cast(bf16x8, pm[4]), __builtin_bit_cast(bf16x16, y1), ix);
                SCHED0();
            }
            const unsigned long long bad = __ballot((int)tg < 0);
            asm volatile("s_nop 5" : "+v"(acc[0]), "+v"(acc[1]) : "s"(bad));
#pragma unroll
            for (int t = 0; t < 2; ++t) { float* rp = (float*)(rb + t * 2048 + w * 512 + g * 128 + n * 4); rp[0] = acc[t][0] + acc[t][2]; rp[16] = acc[t][1] + acc[t][3]; }
            if (bad == 0) break;
            PCOUNT(0)
            asm volatile("" ::: "memory");
            if (spin_fail(k, spins)) break;
        }
        STAMP(s, 2)
        RAWBAR()
        if (w < 2) {
            const char* rp = rb + w * 2048 + ((l >> 4) * 2 + ((l >> 2) & 1)) * 64 + ((l & 3) * 4 + ((l >> 3) & 1) * 2) * 4;
            const f32x2 a = *(const f32x2*)rp, b = *(const f32x2*)(rp + 512), c = *(const f32x2*)(rp + 1024), d = *(const f32x2*)(rp + 1536);
            const f32x2 r = (a + b) + (c + d);
            st32(k.rs, l * 4, k.memf(s + 1) + EXP((2 * j + w) * 256), min(cvtpk(r[0], r[1]) ^ ph, 0xFFFFFFFEu));
        }
        STAMP(s, 3)
_Pragma("unroll 1")
        for (spins = 0; (int)__builtin_amdgcn_readfirstlane(fl) < s - 2; ++spins) {
            PCOUNT(1)
            __builtin_amdgcn_s_sleep(PSLEEP);
            if (spin_fail(k, spins)) break;
            fl = ld32(k.rs, 0, (k.p0 ^ 8192u) + CNT(C_X1, j));
        }
        __builtin_amdgcn_fence(__ATOMIC_ACQUIRE, "wavefront");
        y_wait(k, 2 * s + 3);
        R.yload<0, 8>(k, j, s + 1);
        arm(s + 2);
        y_wait(k, 2 * s + 4);
        STAMP(s, 4)
        R.yload<8, 16>(k, j, s + 1);
        PFLUSH(s)
    }
    PFLUSH(63)
    PROF_OUT
}
DEVI void tred_role(const Params& p, const Ctx& k, char* smem, int j) {
    PROF_DECL
    const int l = k.l, w = k.w, g = l >> 4, n = l & 15, T = 2 * j + (w >> 1), rt = w & 1;
    const unsigned v16 = (g >> 1) * 1024 + (g & 1) * 256 + n * 16, yo = j * 32768 + EXP(w * 4096);
    const unsigned xo = EXP(j * 2048) + (T & 1) * 512 + rt * 1024, xv = ((g >> 1) * 16 + n) * 16 + (g & 1) * 8;
    const u32x2 ones = {0xFFFFFFFFu, 0xFFFFFFFFu};
    Red R;
    RSTAMP(2)
    st64(k.rs, xv, k.segx(PG_X1, 0) + xo, ones); st64(k.rs, xv, k.segx(PG_X1, 1) + xo, ones);
    y_wait(k, 2);
    R.yload<0, 16>(k, j, 0);
    for (int s = 0; s < 64; ++s) {
        STOP(s) STAMP(s, 0)
        const unsigned ap1 = k.p0 + PR_B + (s & 3) * PR_SZ + w * 49152 + 16384, ap2 = ap1 + 16384, am = k.ym(s) + yo;
        const unsigned ph = (s & 4) ? 0x80008000u : 0u;
        unsigned spins = 0, tq, tv;
        u32x2 res;
        if (k.c) res = ld64(k.rs, xv, k.seg(PG_X2, s) + xo);
        else res = *(const u32x2*)(p.x + (size_t)(s * 32 + rt * 16 + n) * 640 + T * 16 + g * 4);
        f32x4 acc[2][2];
        WAIT_BC(s, TSLP)
        u32x4 yl[8];
#pragma unroll
        for (int i = 0; i < 8; ++i) { yl[i] = __builtin_shufflevector(R.Y[2 * i], R.Y[2 * i + 1], 0, 1, 4, 5); PIN(yl[i]); }
        wait_until2(tv); wait_until2(tq + RDLY);
        asm volatile("" ::: "memory");
        STAMP(s, 1)
_Pragma("unroll 1")
        for (;; ++spins) {
            u32x4 ym[4], pp[16];
#pragma unroll
            for (int i = 0; i < 4; ++i) ym[i] = ld128(k.rs, v16 + (i >> 1) * 2048 + (i & 1) * 512, am);
#pragma unroll
            for (int i = 0; i < 16; ++i) pp[i ^ 4] = ld128(k.rs, l * 16 + (i & 3) * 1024, (i < 8 ? ap2 : ap1) + (((i ^ 4) >> 2) & 1) * 4096);
            unsigned mx = 0, tg = 0;
#pragma unroll
            for (int t = 0; t < 2; ++t) {
                acc[t][0] = (f32x4){0.f, 0.f, 0.f, 0.f}; acc[t][1] = acc[t][0];
#pragma unroll
                for (int f = 0; f < 2; ++f) {
                    const u32x4 yf = {ym[2 * f][t], ym[2 * f][t + 2], ym[2 * f + 1][t], ym[2 * f + 1][t + 2]};
#pragma unroll
                    for (int r = 0; r < 2; ++r) acc[t][r] = mf(__builtin_bit_cast(bf16x8, yf), __builtin_bit_cast(bf16x8, pp[4 + 2 * r + f]), acc[t][r]);
                }
            }
#pragma unroll
            for (int i = 0; i < 4; ++i)
#pragma unroll
                for (int t = 0; t < 2; ++t) acc[t][0] = mf(__builtin_bit_cast(bf16x8, yl[t * 4 + i]), __builtin_bit_cast(bf16x8, pp[i]), acc[t][0]);
#pragma unroll
            for (int i = 0; i < 8; ++i)
#pragma unroll
                for (int t = 0; t < 2; ++t) acc[t][1] = mf(__builtin_bit_cast(bf16x8, R.Y[t * 8 + (i ^ 4)]), __builtin_bit_cast(bf16x8, pp[8 + (i ^ 4)]), acc[t][1]);
#pragma unroll
            for (int i = 0; i < 4; ++i) mx = max(mx, umax4(ym[i]));
#pragma unroll
            for (int i = 0; i < 16; ++i) tg |= __builtin_amdgcn_bitop3_b32(pp[i ^ 4][0], pp[i ^ 4][1], ph, 0x7e) | __builtin_amdgcn_bitop3_b32(pp[i ^ 4][2], pp[i ^ 4][3], ph, 0x7e);
            if (__all(mx != 0xFFFFFFFFu && (int)tg >= 0)) break;
            PCOUNT(0)
            asm volatile("" ::: "memory");
            if (spin_fail(k, spins)) break;
        }
        STAMP(s, 2)
        const unsigned yc = y_get(k);
        char* rb = smem + (s & 1) * 16384;
#pragma unroll
        for (int i = 0; i < 4; ++i) *(f32x4*)(rb + i * 4096 + w * 1024 + l * 16) = acc[i >> 1][i & 1];
        RAWBAR()
        st32(k.rs, (w == 0 && l == 0) ? 0u : OOB, (k.p0 ^ 8192u) + CNT(C_X1, j), (unsigned)s);
        {
            const char* rp = rb + w * 4096 + l * 16;
            const f32x4 r = (*(const f32x4*)rp + *(const f32x4*)(rp + 1024)) + (*(const f32x4*)(rp + 2048) + *(const f32x4*)(rp + 3072));
            const unsigned d0 = cvtpk(r[0], r[1]) ^ ph, d1 = cvtpk(r[2], r[3]) ^ ph;
            u32x2 o; o[0] = packs(blo(res[0]) + blo(d0), bhi(res[0]) + bhi(d0)); o[1] = packs(blo(res[1]) + blo(d1), bhi(res[1]) + bhi(d1));
            st64(k.rs, xv, k.segx(PG_X1, s) + xo, o);
            st64(k.rs, s < 62 ? xv : OOB, k.segx(PG_X1, (s + 2) & 63) + xo, ones);
        }
        STAMP(s, 3)
        if (s < 63) {
            if (!__all((int)yc >= 2 * s + 4)) { PCOUNT(2) y_wait(k, 2 * s + 4); }
            __builtin_amdgcn_fence(__ATOMIC_ACQUIRE, "wavefront");
            R.yload<0, 16>(k, j, s + 1);
        }
        STAMP(s, 4)
        STAMP(s, 5)
        PFLUSH(s)
    }
    PROF_OUT
}

DEVI void up_role(const Params& p, Ctx& k, char* smem, int id) {
    PROF_DECL
    const int l = k.l, w = k.w, g = l >> 4, n = l & 15;
    bf16x8 Wt[80];
    const int tile0 = id * 8 + w * 2;
    wstream<20, 4, 8>(smem + w * 32768,
        [&](auto U, unsigned sl) { constexpr int u = CV(U);
            wp_issue<2, (u % 8) * 4096>(p.wg + (size_t)u * 32 * 1536 + tile0 * 16, 1536, sl, l); wp_issue<2, (u % 8) * 4096 + 2048>(p.wu + (size_t)u * 32 * 1536 + tile0 * 16, 1536, sl, l); },
        [&](auto U, char* sp) { constexpr int u = CV(U);
            Wt[u] = wp_read<2, true>(sp, 0, l); Wt[20 + u] = wp_read<2, true>(sp, 1, l); Wt[40 + u] = wp_read<2, true>(sp + 2048, 0, l); Wt[60 + u] = wp_read<2, true>(sp + 2048, 1, l); });
    go_wait(k, 0);
    RAWBAR()
    RSTAMP(2)
    bf16x8 one8 = {0x3F80, 0x3F80, 0x3F80, 0x3F80, 0x3F80, 0x3F80, 0x3F80, 0x3F80};
    asm volatile("s_nop 1" : "+v"(one8));
    f32x4 cs[4];
    sfor<80>([&](auto I) { constexpr int f = CV(I); mfa<(f < 64), f % 20 == 0>(cs[f / 20], Wt[f], one8); });
    asm volatile("s_nop 7" : "+v"(cs[0]), "+v"(cs[1]), "+v"(cs[2]), "+v"(cs[3]));
    const unsigned ho = EXP((tile0 >> 1) * 1024), l16 = l * 16;
    constexpr int L_ST = 81920;
    const u32x4 ones = {0xFFFFFFFFu, 0xFFFFFFFFu, 0xFFFFFFFFu, 0xFFFFFFFFu};
#pragma unroll
    for (int i = 0; i < 4; ++i) st128(k.rs, l16, k.hseg(i >> 1) + (i & 1) * EXP(49152) + ho, ones);
    unsigned fails = 0;
    const unsigned fl = (w == 0 && l == 0) ? 0u : OOB;
    const unsigned long long m1 = __ballot(n & 1), m2 = __ballot(n & 2);
    bool have = false;
    for (int s = 0; s < 64; ) {
        if (!have) prog_wait<20>(k, k.p0 + CNT(C_X1, 0), s);
        STOP(s) STAMP(s, 0)
        xn_all(k, l16, k.seg(PG_X1, s) + w * 2048, (s & 1) * 40960 + w * 2048);
        const unsigned xb = (s & 1) * 40960 + l16, yb = xb + w * 2048, sp = L_ST + (s & 1) * 2048, rp = sp + n * 16;
        const unsigned dp = sp + (g == (n >> 2) ? n * 16 + w * 4 : 4096 + l * 4);
        unsigned nx;
        f32x4 acc[2][4], st[4];
        bf16x8 x[20], y0[5], y1[5];
        unsigned mx = 0;
        auto blk = [&](auto B, auto J) { constexpr int b = CV(B), j = CV(J);
            if constexpr (j == 0) asm volatile("s_waitcnt vmcnt(%0)\n\ts_barrier" :: "n"(8 - 2 * b) : "memory");
            if constexpr (j >= 1 && j < 5) x[4 * b + j - 1] = lr128(xb + (4 * b + j - 1) * 2048);
            if constexpr (j == 5) y0[b] = lr128(yb + b * 8192);
            if constexpr (j == 6) y1[b] = lr128(yb + b * 8192 + 1024); };
        sfor<7>([&](auto J) { blk(IC(0), J); });
        SCHED0();
        STAMP(s, 1)
        sfor<5>([&](auto B) { constexpr int b = CV(B);
            sfor<20>([&](auto I) { constexpr int i = CV(I), ks = 4 * b + ((i >> 2) & 3), nt = i & 3, f = nt * 20 + ks;
                if constexpr (i < 16) mfa<(f < 64), ks == 0>(acc[0][nt], Wt[f], x[ks]);
                else mfa<false, b == 0>(st[nt], (nt & 1) ? (nt & 2 ? y1[b] : y0[b]) : one8, nt & 2 ? y1[b] : y0[b]);
                SCHED0();
                if constexpr (i >= 8 && i < 15 && b < 4) blk(IC((b + 1) % 5), IC(i - 8));
                if constexpr (i >= 16) { const u32x4 v = __builtin_bit_cast(u32x4, (i & 1) ? y1[b] : y0[b]); mx = umax3(mx, v[i & 2], v[(i & 2) + 1]); PIN(mx); }
                if constexpr (b == 4 && i >= 9 && i < 12) x[i - 9] = lr128(xb + 1024 + (i - 9) * 2048);
                SCHED0(); }); });
        STAMP(s, 2)
        float mean[2], rden[2], G[2], U[2];
        f32x4 sa[2], sb[2];
        u32x4 bad, o[2];
        bool redo;
        auto stat = [&](auto I) { constexpr int i = CV(I), rt = i / 8, j = i % 8;
            if constexpr (j == 0) asm volatile("v_add_f32 %0, %2, %3\n\tv_add_f32 %1, %4, %5" : "=&v"(mean[rt]), "=&v"(rden[rt]) : "v"(sa[rt][0]), "v"(sa[rt][1]), "v"(sb[rt][0]), "v"(sb[rt][1]));
            if constexpr (j == 1 || j == 2) asm volatile("v_add_f32 %0, %0, %2\n\tv_add_f32 %1, %1, %3" : "+v"(mean[rt]), "+v"(rden[rt]) : "v"(sa[rt][j + 1]), "v"(sb[rt][j + 1]));
            if constexpr (j == 3) asm volatile("v_mul_f32 %0, 0xbaccccCD, %0\n\tv_mul_f32 %1, 0x3acccccd, %1" : "+v"(mean[rt]), "+v"(rden[rt]));
            if constexpr (j == 4) asm volatile("v_fma_f32 %1, -%0, %0, %1\n\tv_cvt_pk_bf16_f32 %0, 0, %0\n\tv_max_f32 %1, 0, %1" : "+v"(mean[rt]), "+v"(rden[rt]));
            if constexpr (j == 5) asm volatile("v_cvt_pk_bf16_f32 %0, 0, %0\n\tv_add_f32 %0, 0x3727c5ac, %0\n\tv_cvt_pk_bf16_f32 %0, 0, %0" : "+v"(rden[rt]));
            if constexpr (j == 6) asm volatile("v_sqrt_f32 %0, %0" : "+v"(rden[rt]));
            if constexpr (j == 7) asm volatile("v_cvt_pk_bf16_f32 %0, 0, %0\n\tv_rcp_f32 %0, %0" : "+v"(rden[rt])); };
#define GU "+v"(G[0]), "+v"(G[1]), "+v"(U[0]), "+v"(U[1])
        auto ep = [&](auto I) { constexpr int i = CV(I), q = i / 15, j = i % 15, rt = q >> 2, t = (q >> 1) & 1, c = (q & 1) * 2;
            if constexpr (j == 0) asm volatile("v_fma_f32 %0, %2, %3, %4\n\tv_fma_f32 %1, %2, %5, %6" : "=&v"(G[0]), "=&v"(G[1]) : "v"(mean[rt]), "v"(cs[t][c]), "v"(acc[rt][t][c]), "v"(cs[t][c + 1]), "v"(acc[rt][t][c + 1]));
            if constexpr (j == 1) asm volatile("v_mul_f32 %0, %0, %2\n\tv_mul_f32 %1, %1, %2" : "+v"(G[0]), "+v"(G[1]) : "v"(rden[rt]));
            if constexpr (j == 2) asm volatile("v_cvt_pk_bf16_f32 %0, 0, %0\n\tv_cvt_pk_bf16_f32 %1, 0, %1" : "+v"(G[0]), "+v"(G[1]));
            if constexpr (j == 3) asm volatile("v_mul_f32 %0, 0xbfb8aa3b, %0\n\tv_mul_f32 %1, 0xbfb8aa3b, %1" : "+v"(G[0]), "+v"(G[1]));
            if constexpr (j == 4 || j == 5) asm volatile("v_exp_f32 %0, %0" : "+v"(G[j & 1]));
            if constexpr (j == 6) asm volatile("v_fma_f32 %0, %2, %3, %4\n\tv_fma_f32 %1, %2, %5, %6" : "=&v"(U[0]), "=&v"(U[1]) : "v"(mean[rt]), "v"(cs[2 + t][c]), "v"(acc[rt][2 + t][c]), "v"(cs[2 + t][c + 1]), "v"(acc[rt][2 + t][c + 1]));
            if constexpr (j == 7) asm volatile("v_add_f32 %0, 1.0, %0\n\tv_add_f32 %1, 1.0, %1" : "+v"(G[0]), "+v"(G[1]));
            if constexpr (j == 8 || j == 9) asm volatile("v_rcp_f32 %0, %0" : "+v"(G[j & 1]));
            if constexpr (j == 10) asm volatile("v_mul_f32 %0, %0, %2\n\tv_mul_f32 %1, %1, %2" : "+v"(U[0]), "+v"(U[1]) : "v"(rden[rt]));
            if constexpr (j == 11) asm volatile("v_cvt_pk_bf16_f32 %0, 0, %0\n\tv_cvt_pk_bf16_f32 %1, 0, %1" : "+v"(U[0]), "+v"(U[1]));
            if constexpr (j == 12) asm volatile("v_cvt_pk_bf16_f32 %0, 0, %0\n\tv_cvt_pk_bf16_f32 %1, 0, %1" : "+v"(G[0]), "+v"(G[1]));
            if constexpr (j == 13) asm volatile("v_mul_f32 %0, %0, %2\n\tv_mul_f32 %1, %1, %3" : "+v"(G[0]), "+v"(G[1]) : "v"(U[0]), "v"(U[1]));
            if constexpr (j == 14) { unsigned d; asm volatile("v_cvt_pk_bf16_f32 %0, %1, %2\n\tv_min_u32 %0, -2, %0" : "=&v"(d) : "v"(G[0]), "v"(G[1])); o[rt][q & 3] = d; } };
        sfor<80>([&](auto I) {
            constexpr int i = CV(I), ks = i >> 2, nt = i & 3, f = nt * 20 + ks;
            mfa<(f < 64), ks == 0>(acc[1][nt], Wt[f], x[ks]);
            SCHED0();
            if constexpr (nt == 0 && ks + 3 < 20) x[(ks + 3) % 20] = lr128(xb + 1024 + ((ks + 3) % 20) * 2048);
            if constexpr (i == 1 || i == 3) {
                constexpr int rt2 = i == 3;
                asm volatile("" : "+v"(st[2 * rt2]), "+v"(st[2 * rt2 + 1]));
                const f32x4& q = st[2 * rt2 + 1];
                const float d = csel(csel(q[0], q[1], m1), csel(q[2], q[3], m1), m2);
                lw32(dp + rt2 * 512, __float_as_uint(st[2 * rt2][0])); lw32(dp + rt2 * 512 + 256, __float_as_uint(d));
            }
            if constexpr (i == 2) lw32(sp + 1024 + w * 4, __any(mx == 0xFFFFFFFFu) ? 1u : 0u);
            if constexpr (i == 6) bar_lgkm<1>();
            if constexpr (i == 6) sa[0] = __builtin_bit_cast(f32x4, lr128(rp));
            if constexpr (i == 7) sb[0] = __builtin_bit_cast(f32x4, lr128(rp + 256));
            if constexpr (i == 9) sa[1] = __builtin_bit_cast(f32x4, lr128(rp + 512));
            if constexpr (i == 10) sb[1] = __builtin_bit_cast(f32x4, lr128(rp + 768));
            if constexpr (i == 11) bad = __builtin_bit_cast(u32x4, lr128(sp + 1024));
            if constexpr (i >= 12 && i < 20) stat(IC(i - 12));
            if constexpr (i >= 20) ep(IC(i - 20));
            if constexpr (i >= 21 && i < 53 && nt == 1) stat(IC(8 + (i - 21) / 4));
            if constexpr (i == 54) { redo = __builtin_amdgcn_readfirstlane((bad[0] | bad[1]) | (bad[2] | bad[3])) != 0 && fails++ < 4096u; }
            if constexpr (i == 78) nx = ld32(k.rs, min(l, 19) * 128, k.p0 + CNT(C_X1, 0));
            if constexpr (i == UPOS) st32(k.rs, fl, k.p0 + CNT(C_H, id), (unsigned)(redo ? s - 1 : s));
            SCHED0();
        });
        STAMP(s, 3)
        asm volatile("s_nop 7" : "+v"(acc[1][0]), "+v"(acc[1][1]), "+v"(acc[1][2]), "+v"(acc[1][3]));

#pragma unroll
        for (int q = 0; q < 4; ++q) {
            const int t = q >> 1, c = (q & 1) * 2;
            float hv[2];
#pragma unroll
            for (int i = 0; i < 2; ++i) {
                const float gg = bfr(__builtin_fmaf(mean[1], cs[t][c + i], acc[1][t][c + i]) * rden[1]), u = bfr(__builtin_fmaf(mean[1], cs[2 + t][c + i], acc[1][2 + t][c + i]) * rden[1]);
                hv[i] = bfr(__builtin_amdgcn_rcpf(1.f + __builtin_amdgcn_exp2f(gg * -1.44269504f))) * u;
            }
            o[1][q] = packs(hv[0], hv[1]);
        }
        if (redo) { PCOUNT(0) RAWBAR() continue; }
        st128(k.rs, l16, k.hseg(s) + ho, o[0]); st128(k.rs, l16, k.hseg(s) + EXP(49152) + ho, o[1]);
        st128(k.rs, s < 62 ? l16 : OOB, k.hseg((s + 2) & 63) + ho, ones); st128(k.rs, s < 62 ? l16 : OOB, k.hseg((s + 2) & 63) + EXP(49152) + ho, ones);
        PIN(nx);
        have = __all((int)nx > s);
        __builtin_amdgcn_fence(__ATOMIC_ACQUIRE, "wavefront");
        STAMP(s, 4) STAMP(s, 5)
        PFLUSH(s)
        ++s;
    }
    PROF_OUT
}

DEVI void down_role(const Params& p, Ctx& k, char* smem, int id) {
    PROF_DECL
    const int l = k.l, w = k.w, g = l >> 4, n = l & 15, rt = id / 5, cg = id % 5;
    bf16x8 Wt[96];
    wstream<12, 8, 4>(smem + w * 32768,
        [&](auto U, unsigned sl) { constexpr int u = decltype(U)::value; wp_issue<8, (u % 4) * 8192>(p.wd + (size_t)(w * 12 + u) * 32 * 640 + cg * 128, 640, sl, l); },
        [&](auto U, char* sp) { constexpr int u = decltype(U)::value;
#pragma unroll
            for (int nt = 0; nt < 8; ++nt) Wt[u * 8 + nt] = wp_read<8, false>(sp, (nt + 2 * w) & 7, l); });
    go_wait(k, 0);
    RAWBAR()
    RSTAMP(2)
    const unsigned xo = EXP((cg * 4 + w) * 2048) + rt * 1024, xv = ((g >> 1) * 16 + n) * 16 + (g & 1) * 8;
    const u32x2 ones = {0xFFFFFFFFu, 0xFFFFFFFFu};
    const unsigned x2v = k.c ? xv : OOB;
#pragma unroll
    for (int i = 0; i < 4; ++i) st64(k.rs, x2v, k.seg(PG_X2, i >> 1) + xo + (i & 1) * 512, ones);
    for (int s = 0; s < 64; ++s) {
        prog_wait<12>(k, k.p0 + CNT(C_H, 0), s);
        STOP(s) STAMP(s, 0)
        f32x4 acc[8];
        u32x2 res[2];
        const unsigned rb = (s & 1) * 32768 + l * 16;
_Pragma("unroll 1")
        for (unsigned spins = 0;; ++spins) {
            u32x4 hx[12];
#pragma unroll
            for (int q = 0; q < 3; ++q) {
                const unsigned a = k.hseg(s) + rt * EXP(49152) + EXP((w * 12 + q * 4) * 1024);
#pragma unroll
                for (int i = 0; i < 4; ++i) hx[q * 4 + i] = ld128(k.rs, l * 16 + i * 1024, a);
            }
#pragma unroll
            for (int t = 0; t < 2; ++t) res[t] = ld64(k.rs, xv + t * 512, k.seg(PG_X1, s) + xo);
            unsigned mx = 0;
            sfor<96>([&](auto I) { constexpr int i = CV(I), ks = i < 72 ? i / 6 : (i - 72) / 2, j = i < 72 ? 2 + i % 6 : i & 1, f = ks * 8 + j;
                mfa<(f < 64), ks == 0>(acc[j], Wt[f], __builtin_bit_cast(bf16x8, hx[ks]));
                SCHED0();
                if constexpr (i < 72 && i % 6 < 2) { mx = umax3(mx, hx[ks][(i & 1) * 2], hx[ks][(i & 1) * 2 + 1]); PIN(mx); }
                if constexpr (i >= 75 && i < 93 && i % 3 == 0) { constexpr int jj = 2 + (i - 75) / 3;
                    asm volatile("" : "+v"(acc[jj])); lw128(rb + ((w + jj / 2) & 3) * 6144 + ((jj / 2 - 1) * 2 + (jj & 1)) * 1024, __builtin_bit_cast(u32x4, acc[jj])); }
                SCHED0(); });
            asm volatile("s_nop 7" : "+v"(acc[0]), "+v"(acc[1]), "+v"(res[0]), "+v"(res[1]));
            if (__all(mx != 0xFFFFFFFFu) || spin_fail(k, spins)) break;
            PCOUNT(0)
            asm volatile("" ::: "memory");
        }
        STAMP(s, 1)
        RAWBAR()
        st32(k.rs, (w == 0 && l == 0) ? 0u : OOB, k.p0 + CNT(C_X2, rt * 5 + cg), (unsigned)s);
#pragma unroll
        for (int t = 0; t < 2; ++t) {
            const unsigned rp = rb + w * 6144 + t * 1024;
            const f32x4 r = (acc[t] + __builtin_bit_cast(f32x4, lr128(rp))) + (__builtin_bit_cast(f32x4, lr128(rp + 2048)) + __builtin_bit_cast(f32x4, lr128(rp + 4096)));
            const unsigned d0 = cvtpk(r[0], r[1]), d1 = cvtpk(r[2], r[3]);
            u32x2 o; o[0] = cvtpk(blo(res[t][0]) + blo(d0), bhi(res[t][0]) + bhi(d0)); o[1] = cvtpk(blo(res[t][1]) + blo(d1), bhi(res[t][1]) + bhi(d1));
            if (!k.c) *(u32x2*)(p.out + (size_t)(s * 32 + rt * 16 + n) * 640 + (cg * 8 + w * 2 + t) * 16 + g * 4) = o;
            else st64(k.rs, xv + t * 512, k.seg(PG_X2, s) + xo, (u32x2){min(o[0], 0xFFFFFFFEu), min(o[1], 0xFFFFFFFEu)});
        }
        STAMP(s, 2)
#pragma unroll
        for (int t = 0; t < 2; ++t) st64(k.rs, s < 62 ? x2v : OOB, k.seg(PG_X2, (s + 2) & 63) + xo + t * 512, ones);
        STAMP(s, 3) STAMP(s, 4) STAMP(s, 5)
        PFLUSH(s)
    }
    PROF_OUT
}

DEVI void warm(const Params& p, const Ctx& k, int xl, int hw) {
    if (k.c) sleep_until(k.t0 + WD1);
    set_m0(k.w * 256);
    const unsigned t0 = rclk();
_Pragma("unroll 1")
    for (int it = hw; it < 880; it += k.c ? 40 : 64) {
        const u16* b; unsigned o;
        while ((int)(rclk() - t0) < (it >> 6) * PACE) __builtin_amdgcn_s_sleep(1);
        if (OLDMAP) { const int jt = (it & 1) ? min(440 + (it >> 1), 871) : it >> 1;
            if (jt < 200) { b = p.Wo; o = ((xl * 8 + (jt / 5) % 8) * 192 + (jt / 40 + 1) * 32) * 1280 + (jt % 5) * 8192 + k.l * 128; }
            else if (jt < 440) { const unsigned ln = (jt - 200) * 64 + k.l; b = p.Wv; o = (ln / 24) * 12288 + xl * 3072 + (ln % 24) * 128; }
            else { const unsigned ln = ((jt - 440) >> 1) * 64 + k.l; b = (jt & 1) ? p.Wq : p.Wk; o = (64 + ln / 24) * 12288 + xl * 3072 + (ln % 24) * 128; } }
        else if (!(xl & 1)) { const unsigned ln = (min(it, 863) >> 1) * 64 + k.l; b = (it & 1) ? p.Wq : p.Wk; o = (64 + ln / 48) * 12288 + xl * 3072 + (ln % 48) * 128; }
        else if (it < 400) { b = p.Wo; o = (((xl >> 1) * 16 + (it / 5) % 16) * 192 + (it / 80 + 1) * 32) * 1280 + (it % 5) * 8192 + k.l * 128; }
        else { const unsigned ln = (it - 400) * 64 + k.l; b = p.Wv; o = (ln / 48) * 12288 + (xl >> 1) * 6144 + (ln % 48) * 128; }
        asm volatile("global_load_lds_dword %0, %1" :: "v"(o), "s"(b) : "memory");
        if (it < 512) hello(k, 0);
    }
    DRAIN();
    RAWBAR()
}

DEVI int xcc_id() { return __builtin_amdgcn_s_getreg(20 | (0 << 6) | (3 << 11)) & 7; }

DEVI unsigned prepare(const Params& p, Ctx& k, char* smem, int xl, int i) {
    const int l = k.l, j = xl * 22 + i - 4;
    const u32x4 ones = {0xFFFFFFFFu, 0xFFFFFFFFu, 0xFFFFFFFFu, 0xFFFFFFFFu};
    unsigned vote = 0;
    if (i < 4) {
        const int row = ((i * 4 + xl) * 4 + k.w) * 4 + (l >> 4);
        float v[40];
#pragma unroll
        for (int c = 0; c < 5; ++c) unpack8(v + c * 8, *(const u32x4*)(p.x + (size_t)row * 640 + (c * 16 + (l & 15)) * 8));
        ln_store(k, v, k.seg(PG_XNA, i * 2 + (xl >> 1)), row & 31);
    } else if (k.w == 3) {
        const unsigned cal = ((unsigned)(j % 44) << 21) + 7 * 16384 + (xl >> 1) * 4096 + (l & 7) * 16;
        int d = 0;
#pragma unroll 1
        for (int r = 0; r <= 2 * CALR; ++r) {
            unsigned long long c0, c1;
            st128(k.rs, cal, (r & 1) * 8192, ones);
            st128(k.rs, cal, (~r & 1) * 8192, ones);
            asm volatile("s_waitcnt vmcnt(1)\n\ts_memtime %0\n\ts_waitcnt vmcnt(0)\n\ts_memtime %1\n\ts_waitcnt lgkmcnt(0)" : "=&s"(c0), "=s"(c1) :: "memory");
            d = r ? min((int)(c1 - c0), 400) - d : 0;
        }
        vote = (unsigned)(d < 0) ^ (unsigned)(xcc_id() >> 2) ^ (unsigned)(i & 1);
    } else {
        if (k.w == 2 && j < 20) {
            const int e = j * 32 + (l >> 1), T = e >> 4, hf = (e >> 1) & 1, rr = ((e >> 2) & 3) * 2 + (e & 1);
            u32x4 v;
#pragma unroll
            for (int c = 0; c < 4; ++c) v[c] = min(*(const unsigned*)(p.mem0 + rr * 640 + T * 16 + 4 * c + 2 * hf), 0xFFFFFFFEu);
            st128(k.rs, (2u << 21) + EXP(e * 16) + (l & 1) * 8192, 0, v);
        }
        constexpr unsigned N0 = PR_B + PR_SZ, N1 = 40 * 16384, N2 = 32768;
        static_assert(N0 + N1 + N2 <= 4 * 88 * 3072 && NPAGES <= 44, "fill");
#pragma unroll
        for (int r = 0; r < 4; ++r) {
            const unsigned o = ((r * 88 + j) * 3 + k.w) * 1024;
            st128(k.rs, o < N0 + N1 + N2 ? l * 16 : OOB, o < N0 ? o : o < N0 + N1 ? ((unsigned)PG_YM << 21) + (o - N0) : (2u << 21) + 32768 + (o - N0 - N1), ones);
        }
    }
    if (k.w == 3) *(volatile LAS unsigned*)smem = vote;
    DRAIN();
    XSTAMP(0)
    RAWBAR()
    return *(volatile LAS unsigned*)smem;
}
DEVI void master(const Params& p, const Ctx& k) {
    const int l = k.l;
    u32x2 v[4] = {{0u, 0u}, {0u, 0u}, {0u, 0u}, {0u, 0u}};
_Pragma("unroll 1")
    for (unsigned spins = 0; spins < SPIN_CAP; ++spins) {
#pragma unroll
        for (int q = 0; q < 4; ++q) v[q] |= cm_ld(k, (CM_IN + (q * 64 + l) * 128) | v[q][1]);
        if (__all((v[0][1] & v[1][1] & v[2][1] & v[3][1]) != 0)) break;
        asm volatile("" ::: "memory");
    }
    const unsigned long long a = __ballot(v[2][0] != 0), b = __ballot(v[3][0] != 0);
    const int n = __popcll(a) + __popcll(b);
    const unsigned long long g = n > 44 ? ~0ull : 0ull, u = (a & b) | ((a ^ b) & g);
    const unsigned long long t = (__popcll(a ^ b) > 2 || n > 80 || n < 8 ? g : ((u >> 4) & 0x3FFFFF) | ((u >> 36) << 22)) ^ 0xAAAAAAAAAAAAAAAAull;
#pragma unroll
    for (int q = 0; q < 4; ++q) cm_st(k, CM_GO + (q * 64 + l) * 128, (unsigned)t, (unsigned)(t >> 32) | 0x80000000u);
}

__global__ __launch_bounds__(256, 1) void mega_kernel(Params p) {
    extern __shared__ __attribute__((aligned(16))) char smem[];
    Ctx k;
    k.ws = p.ws; k.cm = p.cm;
    k.rs = __builtin_amdgcn_make_buffer_rsrc((void*)p.ws, 0, 0x7fffffff, 0x00020000);
    k.l = threadIdx.x & 63; k.w = __builtin_amdgcn_readfirstlane(threadIdx.x >> 6);
    RSTAMP(0)
    const int x = blockIdx.x & 7, i = blockIdx.x >> 3, xl = x & 3;
    k.role = x * 32 + i; k.c = x >> 2; k.par = 0; k.p0 = 0; k.t0 = rclk();
    if (k.w == 0) {
        if (k.role == MASTER) {
#pragma unroll
            for (int q = 0; q < 4; ++q) cm_st(k, CM_IN + (q * 64 + k.l) * 128, 0, 0);
            cm_st(k, CM_TMO, ~0u, ~0u);
        }
        cm_st(k, CM_GO + k.role * 128, 0, 0);
    }
    if (k.c && i < 26) { const unsigned v = prepare(p, k, smem, xl, i); RSTAMP(1) hello(k, v); go_wait(k, v); XSTAMP(1) RAWBAR() }
    else { DRAIN(); RSTAMP(1) hello(k, 0); }
    if (k.role == MASTER && k.w == 0) master(p, k);
    if (OLDMAP ? i < 8 : i < 16 && !(xl & 1)) { k.l = lane(); qk_role(p, k, smem, xl * 8 + i); return; }
    if (i < 16) { k.l = lane(); vo_role(p, k, smem, OLDMAP ? xl * 8 + i - 8 : (xl >> 1) * 16 + i); return; }
    k.l = lane();
    if (!k.c || i < 26) warm(p, k, xl, (i - 16) * 4 + k.w);
    else { sleep_until(k.t0 + GST); hello(k, 0); RAWBAR() }
    if (i < 26 || (i == 31 && (xl & 1) != UPODD)) go_wait(k, 0);
    if (i < 21) { mred_role(p, k, smem, xl * 5 + i - 16); return; }
    if (i < 26) { tred_role(p, k, smem, xl * 5 + i - 21); return; }
    if ((xl & 1) == UPODD) up_role(p, k, smem, (xl >> 1) * 6 + i - 26);
    else if (i < 31) down_role(p, k, smem, (xl >> 1) * 5 + i - 26);
    else row_role(p, k, smem, xl >> 1);
}

extern "C" void kernel_launch(void* const* d_in, const int* in_sizes, int n_in,
                              void* d_out, int out_size, void* d_ws, size_t ws_size,
                              hipStream_t stream) {
    Params p;
    p.x = (const u16*)d_in[0]; p.mem0 = (const u16*)d_in[1];
    p.Wq = (const u16*)d_in[2]; p.Wk = (const u16*)d_in[3]; p.Wv = (const u16*)d_in[4]; p.Wo = (const u16*)d_in[5];
    p.wg = (const u16*)d_in[8]; p.wu = (const u16*)d_in[9]; p.wd = (const u16*)d_in[10];
    p.out = (u16*)d_out; p.cm = (char*)d_ws;
    p.ws = (char*)(((uintptr_t)d_ws + CM_BYTES + ((1u << 21) - 1)) & ~(uintptr_t)((1u << 21) - 1));
    (void)hipFuncSetAttribute((const void*)mega_kernel, hipFuncAttributeMaxDynamicSharedMemorySize, LDS_BYTES);
    mega_kernel<<<256, 256, LDS_BYTES, stream>>>(p);
}
```

```cpp
#define SWAPG
#include <hip/hip_runtime.h>
#include <hip/hip_bf16.h>
#include <stdint.h>
#include <utility>

typedef unsigned short u16;
typedef __attribute__((ext_vector_type(16))) short bf16x16;
typedef __attribute__((ext_vector_type(8))) short bf16x8;
typedef __attribute__((ext_vector_type(4))) short bf16x4;
typedef __attribute__((ext_vector_type(8))) __bf16 hbf8;
typedef __attribute__((ext_vector_type(16))) __bf16 hbf16;
typedef __attribute__((ext_vector_type(4))) float f32x4;
typedef __attribute__((ext_vector_type(2))) float f32x2;
typedef __attribute__((ext_vector_type(8))) unsigned u32x8;
typedef __attribute__((ext_vector_type(4))) unsigned u32x4;
typedef __attribute__((ext_vector_type(2))) unsigned u32x2;
typedef __attribute__((ext_vector_type(2))) __bf16 bf16x2_t;
#define LAS __attribute__((address_space(3)))
#define GAS __attribute__((address_space(1)))
#define DEVI __device__ __forceinline__

#define PSLEEP 8
#define WD1 1150
#define GST 550
#define NTA " nt"
#define NTB ""
#define PQ 0x077A0
#define PV 0x4AAE4
#define GSA 1600
#define QGS 8
#define VGS 8
#define MN 480
#define MD 250
#define MP 100
#define RSL 1000
#define GSD 250
#define PACE 65
#define XPOS 74
#define YPOS 40
#define XPOSY 1
#define PWS 1
#define WUS 0
#define WMIX 1
#define PACE2 30
#define GDLY 80
#define QDLY 0
#define CALR 1
#define SPG 3
#define DQ 16
#define DV 12
#define BSLP 1
#define SDLY 0
#define UPOS 79
#define RDLY 45
#define NOYST 0
#define NOTOK 0
#define NOYLD 0
#define GPOSY 8
#define OLDMAP 1
#define UPODD 0
#define XLD 10
#define TROT 8
#define KOUT 1
#define RPOS 75
#define RDY 90
#define APOS 6
#define GPOS 74

#define EXP(o) ((((unsigned)(o) >> 13) << 14) | ((unsigned)(o) & 8191u))
enum { C_XN = 0, C_Y = 1, C_X1 = 2, C_XB = 3, C_H = 4, C_X2 = 5, C_BC = 6 };
#define CNT(type, s) ((unsigned)((type) * 16384 + (s) * 128))
constexpr unsigned PR_B = EXP(65536), PR_SZ = EXP(12 * 8192);
constexpr unsigned DUMMY_B = EXP(0);
constexpr int PG_MEMF = 2;
#define YXR 8
constexpr int PG_YX = 3;
constexpr int PG_XNA = PG_YX + 2 * YXR;
constexpr int PG_X1 = PG_XNA + 4, PG_X2 = PG_X1 + 4;
constexpr int PG_H = PG_X2 + 4;
constexpr int PG_YM = PG_H + 8;
constexpr int NPAGES = PG_YM + 2;
static_assert(NPAGES <= 64, "map");
constexpr unsigned CM_TAB = 0, CM_TICKET = 1024, CM_TMO = 4096, CM_IN = 8192, CM_GO = 40960, CM_BYTES = 73728;
constexpr int MASTER = 5 * 32 + 31;
constexpr unsigned SPIN_CAP = 1u << 20;
#define AUX_SC1 16
#define LDS_BYTES (160 * 1024)

struct Params {
    const u16* x; const u16* mem0; const u16* Wq; const u16* Wk; const u16* Wv; const u16* Wo;
    const u16* wg; const u16* wu; const u16* wd; u16* out; char* cm; char* ws;
};
struct Ctx {
    char* ws; char* cm; __amdgpu_buffer_rsrc_t rs; unsigned long long par;
    int c, l, w, role;
    unsigned p0, t0;
    DEVI unsigned pg(int page) const { return ((unsigned)page << 21) | (((unsigned)(par >> page) & 1u) << 13); }
    DEVI unsigned pg0(int page) const { return pg(page) ^ ((unsigned)c << 13); }
    DEVI unsigned seg(int page, int s) const { return pg(page + (s >> 4)) + (unsigned)(s & 15) * 131072u; }
    DEVI unsigned seg0(int page, int s) const { return seg(page, s) ^ ((unsigned)c << 13); }
    DEVI unsigned segx(int page, int s) const { return seg(page, s) ^ 8192u; }
    DEVI unsigned hseg(int s) const { return pg(PG_H + (s >> 3)) + (unsigned)(s & 7) * 262144u; }
    DEVI unsigned yx(int sg, int wq) const { return pg(PG_YX + (sg % YXR) * 2 + (wq >> 1)) + (unsigned)(wq & 1) * 1048576u; }
    DEVI unsigned ym(int s) const { return pg(PG_YM + ((s >> 1) & 1)) + (unsigned)(s & 1) * EXP(327680); }
    DEVI unsigned memf(int s) const { return pg(PG_MEMF) + (unsigned)s * 32768u; }
    DEVI char* cnt(int type, int s) const { return ws + (p0 + CNT(type, s)); }
    DEVI char* cnt0(int type, int s) const { return ws + ((p0 ^ ((unsigned)c << 13)) + CNT(type, s)); }
};

DEVI unsigned cvtpk(float a, float b) { return __builtin_bit_cast(unsigned, __builtin_convertvector((f32x2){a, b}, bf16x2_t)); }
DEVI unsigned packs(float a, float b) { return min(cvtpk(a, b), 0xFFFFFFFEu); }
DEVI float blo(unsigned x) { return __uint_as_float(x << 16); }
DEVI float bhi(unsigned x) { return __uint_as_float(x & 0xFFFF0000u); }
DEVI float bfr(float f) { return __uint_as_float(cvtpk(0.f, f)); }
DEVI u32x4 ld128(__amdgpu_buffer_rsrc_t rs, unsigned v, unsigned s) { return __builtin_amdgcn_raw_buffer_load_b128(rs, (int)v, (int)s, AUX_SC1); }
DEVI u32x2 ld64(__amdgpu_buffer_rsrc_t rs, unsigned v, unsigned s) { return __builtin_amdgcn_raw_buffer_load_b64(rs, (int)v, (int)s, AUX_SC1); }
DEVI unsigned ld32(__amdgpu_buffer_rsrc_t rs, unsigned v, unsigned s) { return __builtin_amdgcn_raw_buffer_load_b32(rs, (int)v, (int)s, AUX_SC1); }
DEVI void st128(__amdgpu_buffer_rsrc_t rs, unsigned v, unsigned s, u32x4 x) {
    __builtin_amdgcn_raw_buffer_store_b128(x, rs, (int)v, (int)s, AUX_SC1);
    asm volatile("s_nop 1" :: "v"(x) : "memory");
}
DEVI void st64(__amdgpu_buffer_rsrc_t rs, unsigned v, unsigned s, u32x2 x) { __builtin_amdgcn_raw_buffer_store_b64(x, rs, (int)v, (int)s, AUX_SC1); }
DEVI void st32(__amdgpu_buffer_rsrc_t rs, unsigned v, unsigned s, unsigned x) { __builtin_amdgcn_raw_buffer_store_b32(x, rs, (int)v, (int)s, AUX_SC1); }
#define RAWBAR() { __builtin_amdgcn_s_waitcnt(0xc07f); __builtin_amdgcn_s_barrier(); }
#define BARL(n) { asm volatile("" ::: "memory"); __builtin_amdgcn_s_waitcnt(0xc07f | ((n) << 8)); __builtin_amdgcn_s_barrier(); asm volatile("" ::: "memory"); }
#define DRAIN() asm volatile("s_waitcnt vmcnt(0)" ::: "memory")
#define SCHED0() __builtin_amdgcn_sched_barrier(0)
#define OOB 0x80000000u
DEVI int opq(int x) { asm volatile("" : "+v"(x)); return x; }
#define LANE const int l = opq(k.l), g = l >> 4, n = l & 15; (void)g; (void)n;
DEVI unsigned umax3(unsigned a, unsigned b, unsigned c) { return max(max(a, b), c); }
DEVI unsigned umax4(u32x4 v) { return max(umax3(v[0], v[1], v[2]), v[3]); }
DEVI unsigned rclk() { return (unsigned)__builtin_amdgcn_s_memrealtime(); }
DEVI unsigned long long rt_async() { unsigned long long t; asm volatile("s_memrealtime %0" : "=s"(t)); return t; }
DEVI void lgkm0(unsigned long long& a) { asm volatile("s_waitcnt lgkmcnt(0)" : "+s"(a) :: "memory"); }
DEVI unsigned spack(unsigned lo, unsigned hi) { unsigned d; asm volatile("s_pack_ll_b32_b16 %0, %1, %2" : "=s"(d) : "s"(lo), "s"(hi)); return d; }
DEVI int lane() { int l; asm volatile("v_mbcnt_lo_u32_b32 %0, -1, 0\n\tv_mbcnt_hi_u32_b32 %0, -1, %0" : "=v"(l)); __builtin_assume(l >= 0 && l < 64); return l; }
DEVI void sleep_until(unsigned target) { while ((int)(rclk() - target) < 0) __builtin_amdgcn_s_sleep(4); }
DEVI float xor16max(float x) { auto r = __builtin_amdgcn_permlane16_swap(__float_as_uint(x), __float_as_uint(x), false, false); return __builtin_fmaxf(__uint_as_float(r[0]), __uint_as_float(r[1])); }
DEVI float xor32max(float x) { auto r = __builtin_amdgcn_permlane32_swap(__float_as_uint(x), __float_as_uint(x), false, false); return __builtin_fmaxf(__uint_as_float(r[0]), __uint_as_float(r[1])); }
DEVI float xor16add(float x) { auto r = __builtin_amdgcn_permlane16_swap(__float_as_uint(x), __float_as_uint(x), false, false); return __uint_as_float(r[0]) + __uint_as_float(r[1]); }
DEVI float xor32add(float x) { auto r = __builtin_amdgcn_permlane32_swap(__float_as_uint(x), __float_as_uint(x), false, false); return __uint_as_float(r[0]) + __uint_as_float(r[1]); }
DEVI float fold32(float a, float b) { auto r = __builtin_amdgcn_permlane32_swap(__float_as_uint(a), __float_as_uint(b), false, false); return __uint_as_float(r[0]) + __uint_as_float(r[1]); }
#define ROR16(x, n) __builtin_bit_cast(float, __builtin_amdgcn_update_dpp(0, __builtin_bit_cast(int, (x)), 0x120 + (n), 0xf, 0xf, false))
DEVI float row16sum(float x) { x += ROR16(x, 8); x += ROR16(x, 4); x += ROR16(x, 2); x += ROR16(x, 1); return x; }
DEVI unsigned z0() { unsigned z; asm volatile("v_mov_b32 %0, 0" : "=v"(z)); return z; }
DEVI void cnt_add(char* p) { __hip_atomic_fetch_add((GAS unsigned*)(p + z0()), z0() + 1u, __ATOMIC_RELAXED, __HIP_MEMORY_SCOPE_AGENT); }
DEVI unsigned cnt_get(char* p) { return __hip_atomic_load((GAS unsigned*)(p + z0()), __ATOMIC_RELAXED, __HIP_MEMORY_SCOPE_AGENT) + 1u; }
DEVI bool spin_fail(const Ctx& k, unsigned spins) {
    if (spins > SPIN_CAP || ((spins & 255u) == 255u && __builtin_amdgcn_readfirstlane(cnt_get(k.cm + CM_TMO)) != 0)) { cnt_add(k.cm + CM_TMO); return true; }
    return false;
}
DEVI __amdgpu_buffer_rsrc_t cm_rs(const Ctx& k) { return __builtin_amdgcn_make_buffer_rsrc((void*)k.cm, 0, CM_BYTES, 0x00020000); }
DEVI void cm_st(const Ctx& k, unsigned v, unsigned off, unsigned lo, unsigned hi) { asm volatile("buffer_store_dwordx2 %0, %1, %2, %3 offen sc1" :: "v"((u32x2){lo, hi}), "v"(v), "s"(cm_rs(k)), "s"(off) : "memory"); }
DEVI u32x2 cm_ld(const Ctx& k, unsigned off) { return ld64(cm_rs(k), off, 0); }
DEVI void line_st(const Ctx& k, unsigned off, unsigned lo, unsigned hi) { cm_st(k, (lane() & 15) * 8 + (k.w ? OOB : 0u), off + k.role * 128, lo, hi); }
DEVI void hello(const Ctx& k, unsigned vote) { line_st(k, CM_IN, vote, OOB); }
DEVI u32x2 go_ask(const Ctx& k) { u32x2 d; asm volatile("buffer_load_dwordx2 %0, off, %1, %2 sc1" : "=v"(d) : "s"(cm_rs(k)), "s"(CM_GO + k.role * 128) : "memory"); return d; }
DEVI void go_wait(Ctx& k, unsigned vote, u32x2 v = {0u, 0u}) {
_Pragma("unroll 1")
    for (unsigned spins = 0; __builtin_amdgcn_readfirstlane(v[1]) >= 0 && spins < SPIN_CAP; ++spins) {
        if (spins) { hello(k, vote); __builtin_amdgcn_s_sleep(4); }
        asm volatile("buffer_load_dwordx2 %0, off, %1, %2 sc1\n\ts_waitcnt vmcnt(0)" : "=v"(v) : "s"(cm_rs(k)), "s"(CM_GO + k.role * 128) : "memory");
    }
    k.par = ((unsigned long long)(unsigned)__builtin_amdgcn_readfirstlane(v[1]) << 32 | (unsigned)__builtin_amdgcn_readfirstlane(v[0])) ^ (k.c ? ~0ull : 0ull);
    k.p0 = k.pg(0);
    __builtin_amdgcn_fence(__ATOMIC_ACQUIRE, "wavefront");
}
DEVI void cnt_wait(const Ctx& k, char* c, unsigned need) {
_Pragma("unroll 1")
    for (unsigned spins = 0; (unsigned)__builtin_amdgcn_readfirstlane(cnt_get(c)) < need; ++spins) {
        __builtin_amdgcn_s_sleep(PSLEEP);
        if (spin_fail(k, spins)) break;
    }
    __builtin_amdgcn_fence(__ATOMIC_ACQUIRE, "wavefront");
}
DEVI unsigned xn_get(const Ctx& k, unsigned l16, int s) { return ld32(k.rs, opq(l16) & 128u, k.p0 + CNT(C_XN, 0)); }
DEVI bool xn_has(const Ctx& k, unsigned v, int s) { return __all((int)v >= s - (k.c ? 0 : 8)); }
DEVI void xn_wait(const Ctx& k, unsigned l16, int s) {
_Pragma("unroll 1")
    for (unsigned spins = 0; !xn_has(k, xn_get(k, l16, s), s); ++spins) {
        asm volatile("" ::: "memory");
        __builtin_amdgcn_s_sleep(2);
        if (spin_fail(k, spins)) break;
    }
    __builtin_amdgcn_fence(__ATOMIC_ACQUIRE, "wavefront");
}
template <int N> DEVI void prog_wait(const Ctx& k, unsigned a, int s) {
_Pragma("unroll 1")
    for (unsigned spins = 0; !__all((int)ld32(k.rs, min(k.l, N - 1) * 128, a) >= s); ++spins) {
        asm volatile("" ::: "memory");
        __builtin_amdgcn_s_sleep(PWS);
        if (spin_fail(k, spins)) break;
    }
    __builtin_amdgcn_fence(__ATOMIC_ACQUIRE, "wavefront");
}
template <int OFF> DEVI void dma1(const void* sbase, unsigned voff, unsigned lds) {
    asm volatile("s_add_i32 m0, %2, %3\n\ts_nop 0\n\tglobal_load_lds_dwordx4 %0, %1" NTA :: "v"(voff), "s"(sbase), "s"(lds), "n"(OFF) : "memory", "scc");
}
DEVI void set_m0(unsigned lds) { asm volatile("s_mov_b32 m0, %0" :: "s"(lds) : "memory"); }
template <int OFF> DEVI void dmab(__amdgpu_buffer_rsrc_t rs, unsigned voff, unsigned soff) {
    asm volatile("buffer_load_dwordx4 %0, %1, %2 offen offset:%3 sc1 lds" :: "v"(voff), "s"(rs), "s"(soff), "n"(OFF) : "memory");
}
#define PIN(x) asm volatile("" : "+v"(x))
#define PINS(x) asm volatile("" : "+s"(x))
#define LEAD()
#define TOUCH(x) asm volatile("" :: "v"(x))
DEVI float csel(float a, float b, unsigned long long m) { float d; asm volatile("v_cndmask_b32_e64 %0, %1, %2, %3" : "=v"(d) : "v"(a), "v"(b), "s"(m)); return d; }
#define LO8(x) __builtin_shufflevector(x, x, 0, 1, 2, 3, 4, 5, 6, 7)
#define HI8(x) __builtin_shufflevector(x, x, 8, 9, 10, 11, 12, 13, 14, 15)
#define CAT8(a, b) __builtin_shufflevector(a, b, 0, 1, 2, 3, 4, 5, 6, 7, 8, 9, 10, 11, 12, 13, 14, 15)
template <bool AG> DEVI void smf(f32x4& c, const bf16x8& a, const bf16x16& w, unsigned ix) {
    if constexpr (AG) asm volatile("v_smfmac_f32_16x16x64_bf16 %0, %1, %2, %3" : "+v"(c) : "v"(a), "a"(w), "v"(ix));
    else asm volatile("v_smfmac_f32_16x16x64_bf16 %0, %1, %2, %3" : "+v"(c) : "v"(a), "v"(w), "v"(ix));
}
#define WTAB(X) X(0, "{a[0:7]}", "{a[0:3]}", "{a[4:7]}") X(1, "{a[8:15]}", "{a[8:11]}", "{a[12:15]}") X(2, "{a[16:23]}", "{a[16:19]}", "{a[20:23]}") X(3, "{a[24:31]}", "{a[24:27]}", "{a[28:31]}") X(4, "{a[32:39]}", "{a[32:35]}", "{a[36:39]}") X(5, "{a[40:47]}", "{a[40:43]}", "{a[44:47]}") X(6, "{a[48:55]}", "{a[48:51]}", "{a[52:55]}") X(7, "{a[56:63]}", "{a[56:59]}", "{a[60:63]}") X(8, "{a[64:71]}", "{a[64:67]}", "{a[68:71]}") X(9, "{a[72:79]}", "{a[72:75]}", "{a[76:79]}") X(10, "{a[80:87]}", "{a[80:83]}", "{a[84:87]}") X(11, "{a[88:95]}", "{a[88:91]}", "{a[92:95]}") X(12, "{a[96:103]}", "{a[96:99]}", "{a[100:103]}") X(13, "{a[104:111]}", "{a[104:107]}", "{a[108:111]}") X(14, "{a[112:119]}", "{a[112:115]}", "{a[116:119]}") X(15, "{a[120:127]}", "{a[120:123]}", "{a[124:127]}") X(16, "{a[128:135]}", "{a[128:131]}", "{a[132:135]}") X(17, "{a[136:143]}", "{a[136:139]}", "{a[140:143]}") X(18, "{a[144:151]}", "{a[144:147]}", "{a[148:151]}") X(19, "{a[152:159]}", "{a[152:155]}", "{a[156:159]}") X(20, "{a[160:167]}", "{a[160:163]}", "{a[164:167]}") X(21, "{a[168:175]}", "{a[168:171]}", "{a[172:175]}") X(22, "{a[176:183]}", "{a[176:179]}", "{a[180:183]}") X(23, "{a[184:191]}", "{a[184:187]}", "{a[188:191]}") X(24, "{a[192:199]}", "{a[192:195]}", "{a[196:199]}") X(25, "{a[200:207]}", "{a[200:203]}", "{a[204:207]}") X(26, "{a[208:215]}", "{a[208:211]}", "{a[212:215]}") X(27, "{a[216:223]}", "{a[216:219]}", "{a[220:223]}") X(28, "{a[224:231]}", "{a[224:227]}", "{a[228:231]}") X(29, "{a[232:239]}", "{a[232:235]}", "{a[236:239]}") X(30, "{a[240:247]}", "{a[240:243]}", "{a[244:247]}") X(31, "{a[248:255]}", "{a[248:251]}", "{a[252:255]}") X(32, "{v[96:103]}", "{v[96:99]}", "{v[100:103]}") X(33, "{v[104:111]}", "{v[104:107]}", "{v[108:111]}") X(34, "{v[112:119]}", "{v[112:115]}", "{v[116:119]}") X(35, "{v[120:127]}", "{v[120:123]}", "{v[124:127]}") X(36, "{v[128:135]}", "{v[128:131]}", "{v[132:135]}") X(37, "{v[136:143]}", "{v[136:139]}", "{v[140:143]}") X(38, "{v[144:151]}", "{v[144:147]}", "{v[148:151]}") X(39, "{v[152:159]}", "{v[152:155]}", "{v[156:159]}") X(40, "{v[160:167]}", "{v[160:163]}", "{v[164:167]}") X(41, "{v[168:175]}", "{v[168:171]}", "{v[172:175]}") X(42, "{v[176:183]}", "{v[176:179]}", "{v[180:183]}") X(43, "{v[184:191]}", "{v[184:187]}", "{v[188:191]}") X(44, "{v[192:199]}", "{v[192:195]}", "{v[196:199]}") X(45, "{v[200:207]}", "{v[200:203]}", "{v[204:207]}") X(46, "{v[208:215]}", "{v[208:211]}", "{v[212:215]}") X(47, "{v[216:223]}", "{v[216:219]}", "{v[220:223]}") X(48, "{v[224:231]}", "{v[224:227]}", "{v[228:231]}") X(49, "{v[232:239]}", "{v[232:235]}", "{v[236:239]}") X(50, "{v[240:247]}", "{v[240:243]}", "{v[244:247]}") X(51, "{v[248:255]}", "{v[248:251]}", "{v[252:255]}")
template <int ID> DEVI void smfw(f32x4& c, const bf16x8& a, const bf16x16& w, unsigned ix) {
#define X(i, P, L, H) if constexpr (ID == i) asm volatile("v_smfmac_f32_16x16x64_bf16 %0, %1, %2, %3" : "+v"(c) : "v"(a), P(w), "v"(ix));
    WTAB(X)
#undef X
}
template <int ID, int HF, bool WB, bool FIRST> DEVI void mfw(f32x4& c, const bf16x16& w, const bf16x8& x) {
#define Y(R, W8) { if constexpr (FIRST && WB) asm volatile("v_mfma_f32_16x16x32_bf16 %0, %2, %1, 0" : "=&v"(c) : R(W8), "v"(x)); \
    else if constexpr (FIRST) asm volatile("v_mfma_f32_16x16x32_bf16 %0, %1, %2, 0" : "=&v"(c) : R(W8), "v"(x)); \
    else if constexpr (WB) asm volatile("v_mfma_f32_16x16x32_bf16 %0, %2, %1, %0" : "+v"(c) : R(W8), "v"(x)); \
    else asm volatile("v_mfma_f32_16x16x32_bf16 %0, %1, %2, %0" : "+v"(c) : R(W8), "v"(x)); }
#define X(i, P, L, H) if constexpr (ID == i) { if constexpr (HF) Y(H, HI8(w)) else Y(L, LO8(w)) }
    WTAB(X)
#undef X
#undef Y
}
template <bool AG, bool FIRST> DEVI void mfa(f32x4& c, const bf16x8& w, const bf16x8& x) {
    if constexpr (AG && FIRST) asm volatile("v_mfma_f32_16x16x32_bf16 %0, %1, %2, 0" : "=&v"(c) : "a"(w), "v"(x));
    else if constexpr (AG) asm volatile("v_mfma_f32_16x16x32_bf16 %0, %1, %2, %0" : "+v"(c) : "a"(w), "v"(x));
    else if constexpr (FIRST) asm volatile("v_mfma_f32_16x16x32_bf16 %0, %1, %2, 0" : "=&v"(c) : "v"(w), "v"(x));
    else asm volatile("v_mfma_f32_16x16x32_bf16 %0, %1, %2, %0" : "+v"(c) : "v"(w), "v"(x));
}
template <bool AG, bool FIRST> DEVI void mfb(f32x4& c, const bf16x8& x, const bf16x8& w) {
    if constexpr (AG && FIRST) asm volatile("v_mfma_f32_16x16x32_bf16 %0, %1, %2, 0" : "=&v"(c) : "v"(x), "a"(w));
    else if constexpr (AG) asm volatile("v_mfma_f32_16x16x32_bf16 %0, %1, %2, %0" : "+v"(c) : "v"(x), "a"(w));
    else if constexpr (FIRST) asm volatile("v_mfma_f32_16x16x32_bf16 %0, %1, %2, 0" : "=&v"(c) : "v"(x), "v"(w));
    else asm volatile("v_mfma_f32_16x16x32_bf16 %0, %1, %2, %0" : "+v"(c) : "v"(x), "v"(w));
}
DEVI f32x4 mf(const bf16x8& a, const bf16x8& b, const f32x4& c) { return __builtin_amdgcn_mfma_f32_16x16x32_bf16(__builtin_bit_cast(hbf8, a), __builtin_bit_cast(hbf8, b), c, 0, 0, 0); }
template <int N, class F> DEVI void sfor(F f) { [&]<int... I>(std::integer_sequence<int, I...>) { (f(std::integral_constant<int, I>{}), ...); }(std::make_integer_sequence<int, N>{}); }
DEVI bf16x8 lds128(const char* p) { return *(const bf16x8*)p; }
DEVI bf16x4 ldstr(const char* p) { return __builtin_amdgcn_ds_read_tr16_b64_v4i16((LAS bf16x4*)p); }
DEVI bf16x8 ldstr2(const char* p, int d) { bf16x4 a = ldstr(p), b = ldstr(p + d); return __builtin_shufflevector(a, b, 0, 1, 2, 3, 4, 5, 6, 7); }

#define STAMP(s, k)
#define QSTAMP(f, i)
#define STOP(s)
#define PFLUSH(s)
#define PROF_DECL
#define RSTAMP(i)
#define XSTAMP(i)
#define PCOUNT(i)
#define PROF_OUT

#define HSTAMP STAMP
#define HFLUSH PFLUSH
#define HTRACE(s)
#define TROUT
#define TRHINT
#define AQ(m, i)
#define AC3(i, j, k) { }
#define AST(i)
#define AAC(i)
#define ACCM 0
#define HSTOP(m) if constexpr ((ACCM != 0) == m) { STOP(0) }
#define PC8(i, x)
#define PROF_ONLY(x)
#define PROF_S(x)
template <int RB, bool STD> DEVI int swz(int row) { return RB == 384 ? ((row >> 1) & 1) | ((row >> 2) & 2) : STD ? (row & 3) | ((row >> 1) & 4) : row & 7; }
template <int RB, bool STD> DEVI unsigned wsrc(unsigned id, int ld) { const unsigned row = id / (RB / 16), c = id % (RB / 16); return row * ld + ((c ^ (swz<RB, STD>(row) << 1)) << 4); }
template <int RB, bool STD> DEVI int wp_adr(int T, int l) { const int row = (STD ? 8 : 4) * (l >> 4) + ((l >> 2) & 3); return row * RB + ((T ^ swz<RB, STD>(row)) << 5) + (l & 3) * 8; }
template <int RB, bool STD> DEVI bf16x8 wp_read(const char* blk, int T, int l) { return ldstr2(blk + wp_adr<RB, STD>(T, l), (STD ? 4 : 16) * RB); }
template <int NU, class CNT, class STEP> DEVI void cstream(CNT cnt, STEP step) {
    sfor<NU>([&](auto V) { constexpr int v = decltype(V)::value;
        asm volatile("s_waitcnt vmcnt(%0) lgkmcnt(0)\n\ts_barrier" :: "n"(decltype(cnt(V))::value * (v ? 1 : 2)) : "memory");
        step(V);
    });
}
template <int NU, int NI, int D, class ISSUE, class READ> DEVI void wstream(char* ring, ISSUE issue, READ read) {
    const unsigned base = (unsigned)(size_t)(LAS char*)ring;
    sfor<D>([&](auto U) { constexpr int u = decltype(U)::value; if constexpr (u < NU) issue(U, base); });
    sfor<NU>([&](auto U) {
        constexpr int u = decltype(U)::value, left = (NU - u < D ? NU - u : D) - 1;
        asm volatile("s_waitcnt vmcnt(%0)" :: "n"(left * NI) : "memory");
        read(U, ring + (u % D) * NI * 1024);
        __builtin_amdgcn_s_waitcnt(0xc07f);
        asm volatile("" ::: "memory");
        if constexpr (u + D < NU) issue(std::integral_constant<int, u + D>{}, base);
    });
}

constexpr int L_XN = 0;
constexpr int L_K = 40960;
constexpr int L_MEM = L_K + 12288;
constexpr int L_QM = L_MEM + 10240;
constexpr int L_Q = L_QM + 12288;
constexpr int L_W = 94208;
#define SLOT(i) ((i) % 3 == 0 ? 45056 : (i) % 3 == 1 ? 69632 : L_W + 40960)
#define CMIN(a, b) ((a) < (b) ? (a) : (b))
#define IC(x) std::integral_constant<int, (x)>{}
#define CV(X) decltype(X)::value
DEVI bf16x8 lr128(unsigned a) { return *(const LAS bf16x8*)(size_t)a; }
DEVI bf16x4 lrtr(unsigned a) { return __builtin_amdgcn_ds_read_tr16_b64_v4i16((LAS bf16x4*)(size_t)a); }
#define CAT4(a, b) __builtin_shufflevector(a, b, 0, 1, 2, 3, 4, 5, 6, 7)
DEVI void lw128(unsigned a, u32x4 v) { *(LAS u32x4*)(size_t)a = v; }
DEVI void lw64(unsigned a, u32x2 v) { *(LAS u32x2*)(size_t)a = v; }
DEVI void lw32(unsigned a, unsigned v) { *(LAS unsigned*)(size_t)a = v; }
template <int N> DEVI void bar_lgkm() { asm volatile("s_waitcnt lgkmcnt(%0)\n\ts_barrier" :: "n"(N) : "memory"); }
DEVI void atom1(__amdgpu_buffer_rsrc_t rs, unsigned voff, unsigned soff) {
    unsigned one; asm volatile("v_mov_b32 %0, 1\n\tbuffer_atomic_add %0, %1, %2, %3 offen" : "=&v"(one) : "v"(voff), "s"(rs), "s"(soff) : "memory");
}
DEVI unsigned vadd(unsigned v, unsigned sc) { unsigned d; asm volatile("v_add_u32 %0, %1, %2" : "=v"(d) : "s"(sc), "v"(v)); return d; }
DEVI float vzero() { float z; asm volatile("v_mov_b32 %0, 0" : "=v"(z)); return z; }
template <int S> DEVI unsigned slot_adr(unsigned a) { if constexpr (S % 3 == 2) return vadd(a, SLOT(2) - SLOT(0)); else return a + (SLOT(S) - SLOT(0)); }
template <int B, int I> DEVI void xn_op(const Ctx& k, unsigned v, unsigned src, unsigned dst) {
    if constexpr (I == 0) set_m0(dst + B * 8192);
    else if constexpr (I == 1) dmab<0>(k.rs, v, src + B * 16384);
    else dmab<1024>(k.rs, v, src + B * 16384);
}
DEVI void xn_all(const Ctx& k, unsigned v, unsigned src, unsigned dst) {
    sfor<5>([&](auto B) { xn_op<CV(B), 0>(k, v, src, dst); asm volatile("s_nop 0"); xn_op<CV(B), 1>(k, v, src, dst); xn_op<CV(B), 2>(k, v, src, dst); });
}
struct Xn {
    unsigned src, dst, sg, need, t, sb, se, l16, c, off;
    unsigned long long flip, m;
    DEVI void init(const Ctx& k) {
        src = k.seg(PG_XNA, 0) + k.w * 2048; dst = L_XN + k.w * 2048; sg = 0; off = ~0u;
        const unsigned q = (unsigned)(k.par >> PG_XNA), d = q ^ (q >> 1);
        flip = ((unsigned long long)(d & 1) << 16) | ((unsigned long long)(d & 2) << 31) | ((unsigned long long)(d & 4) << 46);
    }
    template <int I> DEVI void next(const Ctx& k, unsigned l16) {
        if constexpr (I == 0) { sg += 1; src += 131072; PINS(sg); PINS(src); }
        if constexpr (I == 1) asm volatile("s_bitcmp1_b64 %1, %2\n\ts_cselect_b32 %0, 0x2000, 0" : "=s"(t) : "s"(flip), "s"(sg) : "scc");
        if constexpr (I == 2) { src ^= t; PINS(src); }
        if constexpr (I == 3) asm volatile("v_and_b32 %0, 0x80, %1" : "=v"(c) : "v"(l16));
        if constexpr (I == 4) c = ld32(k.rs, c, k.p0 + CNT(C_XN, 0));
        if constexpr (I == 5) { need = sg - (k.c ? 0u : 8u); PINS(need); }
        if constexpr (I == 6) { need = sg < 64u ? need : 0x7FFFFFFFu; PINS(need); }
        if constexpr (I == 7) asm volatile("v_cmp_gt_i32_e64 %0, %1, %2" : "=s"(m) : "s"(need), "v"(c) : "memory");
        if constexpr (I == 8) asm volatile("s_cmp_lg_u64 %2, 0\n\ts_cselect_b32 %0, -1, 0\n\ts_cselect_b32 %1, 0x200000, %3" : "=&s"(off), "=s"(se) : "s"(m), "s"(src) : "scc");
        if constexpr (I == 10) asm volatile("s_cmp_lg_u32 %1, 64\n\ts_cselect_b32 %0, %0, 0" : "+s"(off) : "s"(sg) : "scc");
    }
    template <int B, int I> DEVI void op(const Ctx& k) {
        if constexpr (I == 0) { set_m0(dst + B * 8192); sb = se + B * 16384; PINS(sb); }
        else dmab<(I - 1) * 1024>(k.rs, l16, sb);
    }
    template <int NB> DEVI void late(const Ctx& k, unsigned l16) {
        xn_wait(k, l16, sg);
        se = src;
        sfor<NB>([&](auto B) { op<CV(B), 0>(k); asm volatile("s_nop 0"); op<CV(B), 1>(k); op<CV(B), 2>(k); });
        DRAIN();
    }
};
template <int OFF> DEVI u32x4 ald128(__amdgpu_buffer_rsrc_t rs, unsigned v, unsigned s) { u32x4 d; asm volatile("buffer_load_dwordx4 %0, %1, %2, %3 offen offset:%4 sc1" : "=v"(d) : "v"(v), "s"(rs), "s"(s), "n"(OFF) : "memory"); return d; }
#define MADR(b) (L_MEM + ((b) < 6 ? ((b) - 1) * 2048 : ((b) - 6) * 2048 + 3072))
struct Gather {
    u32x4 p0, p1, v0, v1;
    unsigned mx, a, aw, b, base, wo, dst;
    unsigned long long bad;
    DEVI void init(const Ctx& k) { base = k.memf(0); wo = k.w * 1024; dst = L_MEM + k.w * 2048; }
    template <int I> DEVI void op(const Ctx& k, unsigned l16, int s) {
        if constexpr (I == 0) { a = ((unsigned)s << 15) & 0x1F8000u; PINS(a); }
        if constexpr (I == 1) { a += base; aw = a + wo; PINS(a); PINS(aw); }
        if constexpr (I == 2) { b = aw + 3072 + (k.w >> 1) * 8192; PINS(b); set_m0(dst); }
        if constexpr (I == 3) v0 = ald128<2048>(k.rs, l16, aw);
        if constexpr (I == 4) v1 = ald128<3072>(k.rs, l16, b);
        if constexpr (I == 5) dmab<2048>(k.rs, l16, aw);
        if constexpr (I == 6) dmab<3072>(k.rs, l16, b);
        if constexpr (I == 7) p0 = ald128<0>(k.rs, l16, a);
        if constexpr (I == 8) p1 = ald128<1024>(k.rs, l16, a);
    }
    template <int N> DEVI void wait() { asm volatile("s_waitcnt vmcnt(%4)" : "+v"(p0), "+v"(p1), "+v"(v0), "+v"(v1) : "n"(N) : "memory"); }
    template <int I> DEVI void fin(unsigned other) {
#define MX3 "v_max3_u32 %0, %0, %3, %4\n\tv_max3_u32 %0, %0, %5, %6"
        if constexpr (I == 0) asm volatile("v_max3_u32 %0, %7, %1, %2\n\t" MX3 : "=&v"(mx) : "v"(v0[0]), "v"(v0[1]), "v"(v0[2]), "v"(v0[3]), "v"(v1[0]), "v"(v1[1]), "s"(other));
        if constexpr (I == 1) asm volatile("v_max3_u32 %0, %0, %1, %2\n\t" MX3 : "+v"(mx) : "v"(v1[2]), "v"(v1[3]), "v"(p0[0]), "v"(p0[1]), "v"(p0[2]), "v"(p0[3]));
        if constexpr (I == 2) asm volatile("v_max3_u32 %1, %1, %2, %3\n\tv_max3_u32 %1, %1, %4, %5\n\tv_cmp_eq_u32_e64 %0, -1, %1" : "=s"(bad) : "v"(mx), "v"(p1[0]), "v"(p1[1]), "v"(p1[2]), "v"(p1[3]));
    }
    DEVI void also(unsigned other) { asm volatile("v_mov_b32 %0, %1" : "=v"(mx) : "s"(other)); }
    DEVI void all() {
        asm volatile("s_waitcnt vmcnt(0)\n\tv_max3_u32 %1, %1, %2, %3\n\tv_max3_u32 %1, %1, %4, %5\n\tv_max3_u32 %1, %1, %6, %7\n\tv_max3_u32 %1, %1, %8, %9\n\tv_max3_u32 %1, %1, %10, %11\n\tv_max3_u32 %1, %1, %12, %13\n\t"
            "v_max3_u32 %1, %1, %14, %15\n\tv_max3_u32 %1, %1, %16, %17\n\tv_cmp_eq_u32_e64 %0, -1, %1" : "=s"(bad) : "v"(mx), "v"(v0[0]), "v"(v0[1]), "v"(v0[2]), "v"(v0[3]), "v"(v1[0]), "v"(v1[1]), "v"(v1[2]), "v"(v1[3]),
            "v"(p0[0]), "v"(p0[1]), "v"(p0[2]), "v"(p0[3]), "v"(p1[0]), "v"(p1[1]), "v"(p1[2]), "v"(p1[3]) : "memory");
    }
    DEVI void check() { also(0); all(); asm volatile("s_nop 3"); }
    unsigned rv, rs; unsigned long long rt;
    template <int I> DEVI void ask(const Ctx& k, unsigned l16) {
        if constexpr (I == 0) asm volatile("v_and_b32 %0, 0xf0, %1\n\tv_lshlrev_b32 %0, 1, %0" : "=v"(rv) : "v"(l16));
        if constexpr (I == 1) asm volatile("buffer_load_dword %0, %0, %1, %2 offen sc1" : "+v"(rv) : "s"(k.rs), "s"(k.p0 + CNT(C_XB, 0)) : "memory");
    }
    template <int I> DEVI void red() {
        if constexpr (I == 0) asm volatile("s_waitcnt vmcnt(4)\n\tv_max_u32_dpp %0, %0, %0 quad_perm:[1,0,3,2] row_mask:0xf bank_mask:0xf" : "+v"(rv));
        if constexpr (I == 1) asm volatile("v_max_u32_dpp %0, %0, %0 quad_perm:[2,3,0,1] row_mask:0xf bank_mask:0xf" : "+v"(rv));
        if constexpr (I == 2) asm volatile("v_max_u32_dpp %0, %0, %0 row_ror:4 row_mask:0xf bank_mask:0xf" : "+v"(rv));
        if constexpr (I == 3) asm volatile("v_max_u32_dpp %0, %0, %0 row_ror:8 row_mask:0xf bank_mask:0xf" : "+v"(rv));
        if constexpr (I == 4) asm volatile("v_readfirstlane_b32 %0, %1" : "=s"(rs) : "v"(rv));
    }
    template <int I> DEVI void hold(unsigned step) {
        if constexpr (I == 0) rt = rt_async();
        if constexpr (I == 3) { PINS(rt); b = spack((unsigned)rt, step); }
        if constexpr (I == 4) asm volatile("s_sub_u32 %0, %1, %0" : "+s"(b) : "s"(rs) : "scc");
        if constexpr (I == 5) if (__builtin_expect(b < 128u, 0)) { while ((int)((rclk() - rs) << 16) < 0) { } }
    }
    template <int I> DEVI void at(const Ctx& k, unsigned l16) {
        if constexpr (I == 1) op<2>(k, l16, 0);
        if constexpr (I >= 2 && I < 15 && (I % 5 == 2 || I % 5 == 4)) op<3 + (I / 5) * 2 + (I % 5) / 4>(k, l16, 0);
    }
    DEVI void again(const Ctx& k, unsigned l16) {
        asm volatile("s_mov_b32 m0, %9\n\tbuffer_load_dwordx4 %2, %4, %5, %7 offen offset:2048 sc1\n\tbuffer_load_dwordx4 %3, %4, %5, %8 offen offset:3072 sc1\n\t"
            "buffer_load_dwordx4 %4, %5, %7 offen offset:2048 sc1 lds\n\tbuffer_load_dwordx4 %4, %5, %8 offen offset:3072 sc1 lds\n\t"
            "buffer_load_dwordx4 %0, %4, %5, %6 offen sc1\n\tbuffer_load_dwordx4 %1, %4, %5, %6 offen offset:1024 sc1\n\ts_waitcnt vmcnt(0)"
            : "+v"(p0), "+v"(p1), "+v"(v0), "+v"(v1) : "v"(l16), "s"(k.rs), "s"(a), "s"(aw), "s"(b), "s"(dst) : "memory");
        check();
    }
};
DEVI void clr2(f32x4& c) { f32x2 a, b; asm volatile("v_mov_b64 %0, 0\n\tv_mov_b64 %1, 0" : "=v"(a), "=v"(b)); c = __builtin_shufflevector(a, b, 0, 1, 2, 3); }
#define NOP7_3(a) asm volatile("s_nop 7" : "+v"(a[0]), "+v"(a[1]), "+v"(a[2]))

struct G3 {
    f32x4 a0[3], a1[3];
    bf16x8 x0[20], x1[20], wl[20];
    unsigned l16, lwv;
    template <int KS> DEVI void r0() { x0[KS] = lr128(l16 + L_XN + KS * 2048); }
    template <int KS> DEVI void r1() { x1[KS] = lr128(l16 + L_XN + KS * 2048 + 1024); }
    template <int KS> DEVI void rw() { if constexpr (KS >= 4) wl[KS] = lr128(lwv + (KS - 4) * 4096); }
    template <bool WL, int KS> DEVI void pre() { if constexpr (WL && KS >= 4) asm volatile("" :: "v"(wl[KS]), "v"(x0[KS]), "v"(x1[KS])); else asm volatile("" :: "v"(x0[KS]), "v"(x1[KS])); }
    template <int ID0, bool WL, int ks, int m> DEVI void mm(const bf16x16 (&W)[52]) {
        constexpr int b = ks >> 1, t = m % 3, id = ID0 + t * 10 + b;
        f32x4& c = m < 3 ? a0[t] : a1[t];
        const bf16x8& x = m < 3 ? x0[ks] : x1[ks];
        if constexpr (WL && t == 2 && b >= 2) { if constexpr (ID0 == 30) mfb<false, false>(c, x, wl[ks]); else mfa<false, false>(c, wl[ks], x); }
        else mfw<id % 52, ks & 1, ID0 == 30, ks == 0>(c, W[id % 52], x);
        SCHED0();
    }
    template <int ID0, bool WL, int K0, int K1, int PF, bool H6 = false, class HOOK> DEVI void run(const bf16x16 (&W)[52], HOOK hook) {
        sfor<K1 - K0>([&](auto KK) {
            constexpr int kk = CV(KK), ks = K0 + kk;
            sfor<6>([&](auto M) {
                constexpr int m = CV(M);
                mm<ID0, WL, ks, m>(W);
                if constexpr (m == 5 && ks + 1 < K1) { pre<WL, (ks + 1) % 20>(); SCHED0(); }
                if constexpr (ks + 2 < PF) {
                    if constexpr (m == 0) r0<(ks + 2) % 20>();
                    if constexpr (m == 1) r1<(ks + 2) % 20>();
                    if constexpr (m == 3 && WL) rw<(ks + 2) % 20>();
                }
                if constexpr (H6) hook(IC(kk * 6 + m));
                else { hook(IC(kk * 7 + (m < 5 ? m : 6))); if constexpr (m == 4) hook(IC(kk * 7 + 5)); }
                SCHED0();
            });
        });
    }
    template <int ID0, bool WL, int F0, int F1, int PF, class HOOK> DEVI void go(const bf16x16 (&W)[52], HOOK hook) {
        sfor<F1 - F0>([&](auto FF) {
            constexpr int f = F0 + CV(FF), ks = f / 6, m = f % 6;
            mm<ID0, WL, ks, m>(W);
            if constexpr (m == 5 && f + 1 < F1) { pre<WL, (ks + 1) % 20>(); SCHED0(); }
            if constexpr (ks + 2 < PF) rd<WL, (f + 12) % 120>();
            hook(IC(f));
            SCHED0();
        });
    }
    template <int P> DEVI void home() {
        if constexpr (P == 0) asm volatile("" : "+{v[12:15]}"(a0[0]), "+{v[16:19]}"(a0[1]), "+{v[20:23]}"(a0[2]), "+{v[24:27]}"(a1[0]), "+{v[28:31]}"(a1[1]), "+{v[32:35]}"(a1[2]));
        else asm volatile("" : "+{v[36:39]}"(a0[0]), "+{v[40:43]}"(a0[1]), "+{v[44:47]}"(a0[2]), "+{v[48:51]}"(a1[0]), "+{v[52:55]}"(a1[1]), "+{v[56:59]}"(a1[2]));
    }
    template <bool WL, int F> DEVI void rd() {
        if constexpr (F % 6 == 0) r0<F / 6>();
        if constexpr (F % 6 == 1) r1<F / 6>();
        if constexpr (F % 6 == 3 && WL) rw<F / 6>();
    }
    u32x2 o;
    template <int I, int OFF> DEVI void out4(unsigned d0) {
        if constexpr (I >= 0 && I < 24) {
            constexpr int t = I / 8, rt = (I / 4) & 1, j = I & 1;
            const f32x4& a = rt ? a1[t] : a0[t];
            if constexpr (I % 4 < 2) { o[j] = cvtpk(a[2 * j], a[2 * j + 1]); PIN(o[j]); }
            else lw32(d0 + OFF + rt * 6144 + t * 1024 + j * 4, o[j]);
        }
    }
    template <int I, int OFF> DEVI void out(unsigned d0, unsigned d1) {
        if constexpr (I >= 0 && I < 18) {
            constexpr int t = I / 6, rt = (I / 3) & 1;
            const f32x4& a = rt ? a1[t] : a0[t];
            if constexpr (I % 3 == 0) { o[0] = cvtpk(a[0], a[1]); PIN(o[0]); }
            else if constexpr (I % 3 == 1) { o[1] = cvtpk(a[2], a[3]); PIN(o[1]); }
            else if constexpr (OFF > 0) lw64(d0 + (OFF - 1) + rt * 6144 + t * 1024, o);
            else lw64((t == 1 ? d1 : d0) + OFF + rt * 6144 + (t == 2 ? 1024 : 0), o);
        }
    }
};
#define SPIX(l) (((l) & 2) ? 0xEEEEu : 0x4444u)
template <int I> DEVI void fold_op(f32x4& a, unsigned& o) {
    if constexpr (I == 0) asm volatile("v_add_f32 %0, %0, %1" : "+v"(a[0]) : "v"(a[2]));
    if constexpr (I == 1) asm volatile("v_add_f32 %0, %0, %1" : "+v"(a[1]) : "v"(a[3]));
    if constexpr (I == 2) asm volatile("v_cvt_pk_bf16_f32 %0, %1, %2" : "=v"(o) : "v"(a[0]), "v"(a[1]));
}

template <int J> DEVI void fold2(f32x4& a, f32x4& b, unsigned (&o)[2]) {
    if constexpr (J < 2) asm volatile("v_add_f32 %0, %0, %2\n\tv_add_f32 %1, %1, %3" : "+v"(a[J]), "+v"(b[J]) : "v"(a[J + 2]), "v"(b[J + 2]));
    else asm volatile("v_cvt_pk_bf16_f32 %0, %2, %3\n\tv_cvt_pk_bf16_f32 %1, %4, %5" : "=&v"(o[0]), "=&v"(o[1]) : "v"(a[0]), "v"(a[1]), "v"(b[0]), "v"(b[1]));
}
DEVI unsigned vshl1(unsigned x, unsigned b) { unsigned d; asm volatile("v_lshl_or_b32 %0, %1, 1, %2" : "=v"(d) : "s"(x), "n"(b)); return d; }

#define ANNOUNCE(s, now, d) (((unsigned)(s) << 16) | (((unsigned)(now) + (d)) & 0xFFFFu))

template <class PUT> DEVI void ln_rows(const u32x4 (&a)[5], int n, int row  , PUT put) {
    float v[40];
#pragma unroll
    for (int i = 0; i < 20; ++i) { v[2 * i] = blo(a[i >> 2][i & 3]); v[2 * i + 1] = bhi(a[i >> 2][i & 3]); }
    f32x2 s2[4] = {{0.f, 0.f}, {0.f, 0.f}, {0.f, 0.f}, {0.f, 0.f}};
#pragma unroll
    for (int i = 0; i < 40; i += 2) s2[(i >> 1) & 3] += (f32x2){v[i], v[i + 1]};
    const f32x2 st = (s2[0] + s2[1]) + (s2[2] + s2[3]);
    float m = row16sum(st[0] + st[1]) * (1.f / 640.f);
    f32x2 q2[4] = {{0.f, 0.f}, {0.f, 0.f}, {0.f, 0.f}, {0.f, 0.f}};
#pragma unroll
    for (int i = 0; i < 40; i += 2) { const f32x2 d = (f32x2){v[i], v[i + 1]} - (f32x2){m, m}; q2[(i >> 1) & 3] += d * d; }
    const f32x2 qt = (q2[0] + q2[1]) + (q2[2] + q2[3]);
    const float q = row16sum(qt[0] + qt[1]);
    const float var = bfr(q * (1.f / 640.f));
    m = bfr(m);
    const float rden = __builtin_amdgcn_rcpf(bfr(__builtin_amdgcn_sqrtf(bfr(var + 1e-5f))));
    const unsigned vo = (n >> 2) * 2048 + (row >> 4) * 1024 + ((n & 3) * 16 + (row & 15)) * 16;
#pragma unroll
    for (int c = 0; c < 5; ++c) {
        u32x4 o;
#pragma unroll
        for (int i = 0; i < 4; ++i) { unsigned d = cvtpk(v[c * 8 + 2 * i] - m, v[c * 8 + 2 * i + 1] - m); o[i] = cvtpk(blo(d) * rden, bhi(d) * rden); }
        put(vo, c, o);
    }
}

struct Soft {
    unsigned long long mA, mY, mC[8];
    unsigned v0, v1;
    unsigned sg, hs;
    float a[6], m, M, t, s, h; u32x2 pk;
    template <int I> DEVI void mask(f32x4 (&sc)[3]) {
        if constexpr (I == 0) asm volatile("v_mov_b32 %0, 0xfe800000" : "=v"(t));
        if constexpr (I >= 1 && I < 9) { float d; asm volatile("v_cndmask_b32_e64 %0, %1, 0, %2" : "=v"(d) : "v"(t), "s"(mC[I - 1])); sc[(I - 1) >> 2][(I - 1) & 3] = d; }
    }
    template <int I> DEVI void op(const Ctx& k, f32x4 (&sc)[3], unsigned a0, unsigned a1, unsigned hv) {
#define FMA(x, y) "v_fmamk_f32 " x ", " x ", 0x3dd53b95, " y "\n\t"
#define EXPC(x) "v_exp_f32_e64 " x ", " x " clamp\n\t"
#define OP3(o, d, x, y) o " " d ", " x ", " y "\n\t"
#define SA(txt) asm volatile(txt : "+v"(a[0]), "+v"(a[1]), "+v"(a[2]), "+v"(a[3]), "+v"(m), "+v"(t))
#define SB(txt) asm volatile(txt : "+v"(a[0]), "+v"(a[1]), "+v"(a[2]), "+v"(a[3]), "+v"(m), "+v"(t), "+v"(s), "+v"(a[4]), "+v"(a[5]), "+v"(M), "+v"(h) : "s"(hs), "s"(sg))
#define ADD(d, x, y) OP3("v_add_f32", d, x, y)
#define MUL(d, x, y) OP3("v_mul_f32", d, x, y)
#define CND(d, x, y, c) "v_cndmask_b32_e64 " d ", " x ", " y ", " c "\n\t"
        if constexpr (I == 0) asm volatile("v_max3_f32 %0, %1, %2, %3\n\tv_max3_f32 %0, %0, %4, %5" : "=&v"(m) : "v"(sc[0][0]), "v"(sc[0][1]), "v"(sc[0][2]), "v"(sc[0][3]), "v"(sc[1][0]));
        if constexpr (I == 1) asm volatile("v_max3_f32 %0, %0, %1, %2" : "+v"(m) : "v"(sc[1][1]), "v"(sc[1][2]));
        if constexpr (I == 2) asm volatile(CND("%0", "%2", "%3", "%6") CND("%1", "%4", "%5", "%6") : "=&v"(a[2]), "=&v"(a[3]) : "v"(sc[0][2]), "v"(sc[1][0]), "v"(sc[0][3]), "v"(sc[1][1]), "s"(mY));
        if constexpr (I == 3) asm volatile("v_max_f32 %1, %1, %5\n\t" CND("%0", "%2", "%3", "%4") : "=&v"(a[0]), "+v"(m) : "v"(sc[0][0]), "v"(a[2]), "s"(mA), "v"(sc[1][3]));
        if constexpr (I == 4) asm volatile("v_mul_f32 %0, 0xbdd53b95, %1" : "=v"(t) : "v"(m));
        if constexpr (I == 5) asm volatile(CND("%0", "%2", "%3", "%5") CND("%1", "%1", "%4", "%5") : "=&v"(a[1]), "+v"(a[2]) : "v"(sc[0][1]), "v"(a[3]), "v"(sc[1][2]), "s"(mA));
        if constexpr (I == 6) asm volatile(CND("%0", "%0", "%2", "%3") FMA("%1", "%4") : "+v"(a[3]), "+v"(a[0]) : "v"(sc[1][3]), "s"(mA), "v"(t));
        if constexpr (I == 7) SA(FMA("%1", "%5"));
        if constexpr (I == 8) SA(FMA("%2", "%5") FMA("%3", "%5"));
        if constexpr (I == 9) SA(EXPC("%0"));
        if constexpr (I == 10) { asm volatile("v_max3_f32 %0, %1, %2, %3" : "=v"(M) : "v"(m), "v"(sc[2][0]), "v"(sc[2][1]), "v"(sc[2])); a[4] = sc[2][0]; a[5] = sc[2][1]; }
        if constexpr (I == 11) asm volatile("v_mov_b32 %0, %3\n\tv_mov_b32 %1, %4" : "=&v"(t), "=&v"(h), "=v"(s) : "v"(M), "s"(hv));
        if constexpr (I == 12) SB("v_permlane16_swap_b32 %9, %5");
        if constexpr (I == 13) SB(OP3("v_max_f32", "%9", "%9", "%5"));
        if constexpr (I == 14) SB(EXPC("%1"));
        if constexpr (I == 15) SB("v_mov_b32 %5, %9\n\t" ADD("%6", "%0", "%1"));
        if constexpr (I == 16) SB("v_permlane32_swap_b32 %9, %5");
        if constexpr (I == 17) SB(OP3("v_max_f32", "%9", "%9", "%5") "v_mul_f32 %9, 0xbdd53b95, %9");
        if constexpr (I == 18) SB(FMA("%7", "%9") FMA("%8", "%9"));
        if constexpr (I == 19) SB(FMA("%4", "%9"));
        if constexpr (I == 20) SB(EXPC("%2"));
        if constexpr (I == 21) SB(EXPC("%3"));
        if constexpr (I == 22) SB(ADD("%6", "%6", "%2"));
        if constexpr (I == 23) SB(EXPC("%7"));
        if constexpr (I == 24) SB(EXPC("%8"));
        if constexpr (I == 25) SB(ADD("%6", "%6", "%3"));
        if constexpr (I == 26) SB(EXPC("%4"));
        if constexpr (I == 27) SB(MUL("%6", "%12", "%6") ADD("%5", "%7", "%8"));
        if constexpr (I == 28) SB(MUL("%6", "%6", "%4"));
        if constexpr (I == 29) SB(OP3("v_fmac_f32", "%6", "%11", "%5"));
        if constexpr (I == 30) SB("v_fmac_f32_dpp %6, %6, %10 row_ror:8 row_mask:0xf bank_mask:0xf\n\tv_mov_b32 %5, %6");
        if constexpr (I == 31) SB("v_permlane16_swap_b32 %6, %5");
        if constexpr (I == 32) SB(ADD("%6", "%6", "%5") "v_mov_b32 %5, %6");
        if constexpr (I == 33) SB("v_permlane32_swap_b32 %6, %5");
        if constexpr (I == 34) SB(ADD("%6", "%6", "%5"));
        if constexpr (I == 35) SB("v_rcp_f32 %6, %6");
        if constexpr (I == 36) SB(MUL("%4", "%4", "%6") MUL("%7", "%7", "%6"));
        if constexpr (I == 37) SB(MUL("%0", "%0", "%4"));
        if constexpr (I == 38) SB(MUL("%1", "%1", "%4") MUL("%2", "%2", "%4"));
        if constexpr (I == 39) { unsigned x; asm volatile(MUL("%1", "%1", "%4") "v_cvt_pk_bf16_f32 %0, %2, %3" : "=&v"(x), "+v"(a[3]) : "v"(a[0]), "v"(a[1]), "v"(m)); pk[0] = x; }
        if constexpr (I == 40) { unsigned x; asm volatile("v_cvt_pk_bf16_f32 %0, %1, %2" : "=v"(x) : "v"(a[2]), "v"(a[3])); pk[1] = x; }
        if constexpr (I == 41) { st64(k.rs, v0, a0, pk); SCHED0(); asm volatile("v_mul_f32 %1, %1, %2\n\tv_cvt_pk_bf16_f32 %0, %0, %1" : "+v"(a[4]), "+v"(a[5]) : "v"(s)); }
        if constexpr (I == 42) st32(k.rs, v1, a1, __float_as_uint(a[4]));
    }
};
#define QIDX(t, b) ((t) * 10 + (b))
DEVI void qk_role(const Params& p, Ctx& k, char* smem, int h) {
    PROF_DECL
    const int w = k.w;
    bf16x16 W[52];
    Gather G;
    Soft S;
    const int hw = h >> 3, hh = h & 7;
    unsigned l16, ix, dqm, dq, trp, qp;
    {
        const int l = k.l, g = l >> 4, n = l & 15, r = n & 7, u = w & 1;
        const bool hf = n >= 8, dup = w != 1;
        S.mA = __ballot(dup && hf);
        const int lim = (w == 0 ? 64 : w == 1 ? n : 16 + 8 * u + r) - 4 * g;
#pragma unroll
        for (int i = 0; i < 8; ++i) S.mC[i] = __ballot((i & 3) + (i >> 2) * 16 <= lim);
        const bool okm = !(dup && hf);
        S.mY = __ballot((dup && hf) == (w != 0));
        S.v0 = w == 0 ? hh * 512 + ((g >> 1) * 16 + (r >> 1) * 4 + hf * 2 + (r & 1)) * 16 + (g & 1) * 8 : w == 1 ? (hh >> 1) * 1024 + l * 16 + (hh & 1) * 8 : hh * 1024 + (g * 16 + 8 * u + r) * 16 + hf * 8;
        S.v1 = !okm ? OOB : w == 0 ? 4096 + hh * 128 + r * 16 + g * 4 : (w == 1 ? 4096 + n * 16 : 6144 + (8 * u + r) * 16) + (hh >> 1) * 512 + (g >> 1) * 256 + (hh & 1) * 8 + (g & 1) * 4;
        l16 = l * 16; ix = SPIX(l);
        const int uu = u ^ ((n >> 2) & 1);
        dqm = L_QM + (w >> 1) * 3072 + uu * 512 + (g >> 1) * 128 + n * 8 + (g & 1) * 4;
        dq = L_QM + 6144 + (w >> 1) * 3072 + uu * 512 + g * 128 + ((n * 8) ^ ((g >> 1) * 64));
        trp = L_QM + 6144 + (l & 3) * 128 + (((l >> 2) * 8) ^ ((l & 2) * 32));
        qp = w == 1 ? trp + 6144 : (w == 0 ? L_QM : L_Q + 6144 + u * 256) + (l & 1) * 128 + (((l >> 2) * 8) ^ (w == 3 ? 64 : 0));
        u32x8 rg = {S.v0, S.v1, l16, ix, dqm, dq, trp, qp};
        asm volatile("" : "+{v[0:7]}"(rg));
        S.v0 = rg[0]; S.v1 = rg[1]; l16 = rg[2]; ix = rg[3]; dqm = rg[4]; dq = rg[5]; trp = rg[6]; qp = rg[7];
    }
    Xn X;
    G3 A, B;
    X.l16 = l16; A.l16 = l16; B.l16 = l16; B.lwv = vadd(l16, L_W + w * 1024);
    {
        const int l = k.l;
        bf16x8 wt[16];
        u32x2 gv;
        unsigned ra[3];
#pragma unroll
        for (int t = 0; t < 3; ++t) ra[t] = opq(wp_adr<384, true>(w * 3 + t, l) + SLOT(0));
#define QEX(v) ((v) < 0 ? 0 : ((v) < 6) + ((v) == QGS) + 10 * ((v) == QGS + 2) + 8 * ((v) == QGS + 7))
#define QKE(v) ((v) < QGS + 4 ? 0 : (v) == 19 ? 20 : CMIN(v, 3 * ((v) - QGS - 3)))
        auto rdx = [&](auto K) { constexpr int ks = CV(K); A.r0<ks>(); A.r1<ks>(); B.x0[ks] = A.x0[ks]; B.x1[ks] = A.x1[ks]; if constexpr (ks < 14 && QKE(ks + 1) <= ks) B.rw<ks>(); };
        cstream<20>([](auto V) { constexpr int v = CV(V); return IC(QEX(v - 1) + (v < 19 ? 6 : 0)); },
            [&](auto U) { constexpr int u = CV(U), b = u >> 1, k0 = QKE(u - 1), n = (QKE(u) - k0) * 12, nn = n ? n : 12;
                if constexpr (u < 6) hello(k, 0);
                if constexpr (u == QGS) gv = go_ask(k);
                if constexpr (u == QGS + 2) { PIN(gv); go_wait(k, 0, gv); X.init(k); G.init(k); X.l16 = k.c ? OOB : l16; xn_all(k, X.l16, X.src, X.dst); }
                if constexpr (u == QGS + 4) rdx(IC(0));
                if constexpr (u == QGS + 7) { sfor<3>([&](auto I) { X.next<CV(I)>(k, l16); G.op<CV(I)>(k, l16, 0); }); X.se = X.src; }
                if constexpr (u == 18) {
                    asm volatile("" :: "{v[152:155]}"(wt[10]), "{v[156:159]}"(wt[11]), "{v[232:235]}"(wt[12]));
#pragma unroll
                    for (int i = 10; i < 13; ++i) lw128(B.lwv + i * 4096, __builtin_bit_cast(u32x4, wt[i]));
                }
                if constexpr (u == 19) { G.wait<0>(); lw128(l16 + G.dst + 2048, G.v0); lw128(l16 + G.dst + 3072, G.v1); }
                XSTAMP(u)
                const unsigned rb[3] = {slot_adr<u>(ra[0]), slot_adr<u>(ra[1]), slot_adr<u>(ra[2])};
                bf16x4 hf[12];
                SCHED0();
                sfor<nn>([&](auto I) { constexpr int i = CV(I), ks = k0 + i / 12, m = i % 12, t = i >> 1, e = u == QGS + 7 ? 8 : 0, j = ((i - e) * 6) / (nn - e);
                    if constexpr (n > 0) { if constexpr (m < 6) A.mm<0, false, ks, m>(W); else B.mm<30, true, ks, m - 6>(W); }
                    if constexpr (i < e) { if constexpr (i % 4 == 0) X.op<i / 4, 0>(k); if constexpr (i & 1) X.op<i / 4, 1 + (i / 2) % 2>(k); else G.op<3 + i / 4 + (i & 2) * 2>(k, l16, 0); }
                    else if constexpr (u > 0 && u < 18 && (i == e || ((i - e - 1) * 6) / (nn - e) != j))
                        dma1<SLOT(u + 2) + (j / 3) * 12288 + (j % 3) * 1024>((j < 3 ? p.Wk : p.Wq) + (size_t)(u + 2) * 32 * 6144 + h * 192, wsrc<384, true>(w * 192 + (j % 3) * 64 + l, 12288), w * 3072);
                    if constexpr (i == 0 && u > 4 && (u < 15 || u >= 18)) lw128(B.lwv + (u - 5) * 4096, __builtin_bit_cast(u32x4, wt[(u - 5) & 15]));
                    if constexpr (i < 12) {
                        hf[i] = lrtr(rb[t % 3] + (t / 3) * 12288 + (i & 1) * 1536);
                        if constexpr (i & 1) {
                            if constexpr (t == 5 && b >= 2) B.wl[u] = wt[(u - 4) & 15] = CAT4(hf[i - 1], hf[i]);
                            else if constexpr (u & 1) W[QIDX(t, b) % 52] = CAT8(LO8(W[QIDX(t, b) % 52]), CAT4(hf[i - 1], hf[i]));
                            else W[QIDX(t, b) % 52] = __builtin_shufflevector(hf[i - 1], hf[i], 0, 1, 2, 3, 4, 5, 6, 7, -1, -1, -1, -1, -1, -1, -1, -1);
                        }
                    }
                    if constexpr (n > 0 && m == 3 && ks < 19) rdx(IC((ks + 1) % 20));
                    SCHED0(); }); });
        lw128(B.lwv + 15 * 4096, __builtin_bit_cast(u32x4, wt[15]));
        static_assert(QKE(17) == 17, "QGS");
        RAWBAR()
        XSTAMP(20)
        const unsigned d0 = (l16 | ((l16 >> 5) & 8)) ^ (L_K + (w >> 1) * 3072 + (w & 1) * 8);
        sfor<24>([&](auto I) { A.out4<CV(I), 0>(d0); });
        G.v0 = G.p0; G.v1 = G.p0; G.bad = 0;
        X.off = 0; X.l16 = l16;
        if (k.c) X.init(k);
    }
    RSTAMP(2)
    const unsigned po0 = k.p0 + PR_B + hw * 49152 + (w == 0 ? 0 : w == 1 ? 32768 : 16384), po1 = k.p0 + PR_B + hw * 49152 + (w == 0 ? 0 : 32768);
    const unsigned pdum = k.pg(1) + DUMMY_B, pbc = w == 0 ? k.p0 + CNT(C_BC, hw * 16 + hh) : pdum + 65536 + h * 512 + w * 128;
    unsigned long long now = 0, done = 0;
    unsigned a0 = po0 + (k.c ? pdum - k.p0 - PR_B : 0u), a1 = po1 + (k.c ? pdum - k.p0 - PR_B : 0u);
    const unsigned hv = w == 1 ? 0u : 0x3F800000u, hw2 = w == 1 ? 0u : 0x00800000u;
    S.sg = 0x3F800000u; S.hs = 0x3F800000u - hw2;
    f32x4 ac[6];
#pragma unroll
    for (int t = 0; t < 6; ++t) clr2(ac[t]);
    unsigned an = 110;
    __builtin_amdgcn_s_waitcnt(0xc07f);
    do {
        if (X.off != 0) {
            PC8(1, X.sg)
            X.late<2>(k, l16);
            G.check();
        }
_Pragma("unroll 1")
        for (unsigned spins = 0; G.bad != 0 && !spin_fail(k, spins); ++spins) { PC8(0, X.sg) G.again(k, l16); }
        HSTOP(0) HSTAMP(0, 0)
        clr2(ac[5]);
        asm volatile("s_nop 1" : "+v"(ac[5]));
        smfw<50>(ac[5], __builtin_bit_cast(bf16x8, G.p0), W[50], ix);
    do {
        PROF_S(X.sg - 1)
        f32x4 sc[3];
        {
            bf16x8 m[16], wa[10], wb[10], kf[12];
            bf16x4 q[12], km[12];
            unsigned fo[6];
            m[0] = __builtin_bit_cast(bf16x8, G.p0); m[1] = __builtin_bit_cast(bf16x8, G.p1);
            auto put = [&](auto T) { constexpr int t = CV(T); if constexpr (t < 3) lw32(dq + t * 1024, fo[t]); else lw32(dqm + (t - 3) * 1024, fo[t]); };
            auto rd = [&](auto C) { constexpr int c = CV(C), j = c % 20;
                if constexpr (c < 0) { }
                else if constexpr (c < 20) m[j] = lr128(l16 + MADR(j < 10 ? j : j - 8));
                else if constexpr (c < 40) wa[j] = lr128(B.lwv + (j - 2) * 8192);
                else if constexpr (c < 60) wb[j] = lr128(B.lwv + (j - 2) * 8192 + 4096);
                else if constexpr (c < 80) kf[j] = lr128(l16 + L_K + ((j & 1) * 6 + j / 2) * 1024);
                else if constexpr (c < 100) q[j] = lrtr(qp + j * 512);
                else if constexpr (c < 120) km[j] = lrtr(trp + j * 512);
                else if constexpr (c < 140) A.r0<j>();
                else A.r1<j>(); };
#define PB(i) ((i) < 12 ? (i) / 6 : (i) < 32 ? (i) / 4 - 1 : (i) < 44 ? 7 + ((i) - 32) % 3 : (i) < 54 ? 7 + ((i) - 44) / 2 : 12 + ((i) - 54) % 3)
#define PT(i) ((i) < 12 ? ((i) + 5) % 6 : (i) < 32 ? (3 + (i) % 4) % 6 : (i) < 44 ? (3 + ((i) - 32) / 3) % 6 : (i) < 54 ? 1 + ((i) & 1) : 1 + ((i) - 54) / 3)
            sfor<82>([&](auto I) {
                constexpr int i = CV(I) + 1, b = PB(i), t = PT(i), bn = PB(i + 1);
                constexpr int rt[83] = {-1, 2, -1, -1, -1, 22, 42, 3, -1, 23, 43, 4, -1, 24, 44, 5, -1, 25, 45, 6,
                    -1, 26, 46, 7, -1, 8, 9, 27, 47, 28, 48, 29, 49, -1, -1, 60, -1, 61, 62, 63,
                    -1, 10, 64, -1, 11, 12, -1, 13, 14, -1, -1, 80, 82, 83, 84, 85, 86, 87, 67, 88,
                    89, 69, -1, 91, -1, 71, -1, 121, -1, 100, 102, 104, 106, 108, 110, -1, 140, -1, -1, 141, -1, -1, 122};
                constexpr int r2[83] = {-1, -1, -1, -1, -1, -1, -1, -1, -1, -1, -1, -1, -1, -1, -1, -1, -1, -1, -1, -1,
                    -1, -1, -1, -1, -1, -1, -1, -1, -1, -1, -1, -1, -1, -1, -1, -1, -1, -1, -1, -1,
                    -1, -1, -1, -1, -1, -1, -1, -1, -1, -1, -1, 81, -1, 65, -1, 66, -1, -1, -1, -1,
                    68, 90, -1, 70, -1, 120, -1, -1, -1, 101, 103, 105, 107, 109, 111, -1, -1, -1, -1, -1, -1, -1, -1};
                if constexpr (i < 60) {
                    if constexpr (t == 5 && b >= 2) smf<false>(ac[5], m[b], CAT8(wa[b], wb[b]), ix);
                    else smfw<QIDX(t, b < 10 ? b : b - 8) % 52>(ac[t], m[b], W[QIDX(t, b < 10 ? b : b - 8) % 52], ix);
                } else if constexpr (i < 72) { constexpr int j = i - 60; mfa<false, false>(sc[j & 1], kf[j], CAT4(q[j & ~1], q[j | 1])); }
                else if constexpr (i < 77) A.mm<0, false, (i - 72) / 3, (i - 72) % 3>(W);
                else { constexpr int ks = i - 77; mfa<false, ks == 0>(sc[2], CAT4(km[2 * ks], km[2 * ks + 1]), CAT4(q[2 * ks], q[2 * ks + 1])); }
                SCHED0();
                if constexpr (i == 1) { AQ(3, 3) asm volatile("s_barrier" ::: "memory"); AQ(1, 2) AQ(3, 4) }
                else if constexpr (i == 50) { BARL(3); AQ(1, 3) }
                else if constexpr (i == 68) { BARL(3); AQ(1, 4) QSTAMP(0, 1) }
                else if constexpr (i >= 12 && i < 59) { if constexpr (PT(i + 1) == 5) TOUCH(wb[bn % 10]); TOUCH(m[bn]); }
                else if constexpr (i >= 59 && i < 71) { asm volatile("" :: "v"(kf[(i - 59) % 12]), "v"(q[((i - 59) | 1) % 12])); }
                else if constexpr (i == 71) TOUCH(A.x0[0]);
                else if constexpr (i == 74) TOUCH(A.x0[1]);
                else if constexpr (i >= 76 && i < 82) TOUCH(km[(2 * (i - 76) + 1) % 12]);
                if constexpr (i == 8 || i == 30) asm volatile("" :: "{v[60:63]}"(ac[0]), "{v[28:31]}"(ac[1]), "{v[32:35]}"(ac[2]), "{v[64:67]}"(ac[3]), "{v[68:71]}"(ac[4]), "{v[72:75]}"(ac[5]));
                if constexpr (i == 14) AC3(0, 0, 0)
                if constexpr (i == 16) AC3(1, 6, 1)
                if constexpr (i == 18) AC3(2, 7, 2)
                if constexpr (i == 20) AC3(-1, -1, 3)
                if constexpr (i == 22) AC3(-1, -1, 4)
                if constexpr (i == 24) AC3(-1, -1, 5)
                if constexpr (i == 66) AC3(3, -1, 6)
                if constexpr (i == 2) { now = rt_async(); HSTOP(1) }
                if constexpr (i == APOS) { PINS(now); an += (unsigned)now; PINS(an); }
                if constexpr (i == APOS + 1) an = spack(an, X.sg);
                SCHED0();
                rd(IC(rt[i])); rd(IC(r2[i]));
                if constexpr (i == APOS + 4) st32(k.rs, 0, pbc, fo[1]);
                if constexpr (i >= 40 && i < 50 && i % 3 == 1) put(IC(((i - 31) / 3) % 6));
                if constexpr (i == 62 || i == 64) put(IC(i / 2 - 30));
                SCHED0();
                if constexpr (i < 25 && i % 4 != 1) B.out<((i - 2) / 4) * 3 + (i - 2) % 4, 6145>(dq, dq);
                if constexpr (i == APOS + 3) { fo[1] = an; PIN(fo[1]); }
                if constexpr (i >= 37 && i < 49) { constexpr int tt = ((i - 28) / 3) % 6; fold_op<(i - 37) % 3>(ac[tt], fo[tt]); }
                if constexpr (i >= 59 && i < 62) fold_op<i - 59>(ac[1], fo[1]);
                if constexpr (i == 62) fold_op<0>(ac[2], fo[2]);
                if constexpr (i == 63) asm volatile("v_add_f32 %1, %1, %2\n\tv_cvt_pk_bf16_f32 %0, %3, %1" : "=v"(fo[2]), "+v"(ac[2][1]) : "v"(ac[2][3]), "v"(ac[2][0]));
                if constexpr (i == 33) S.mask<0>(sc);
                if constexpr (i >= 51 && i < 59) S.mask<i - 50>(sc);
                if constexpr (i >= 75) S.op<i - 75>(k, sc, a0, a1, hv);
                if constexpr (i == 12) { AQ(3, 5) }
                if constexpr (i == 43) { AQ(3, 6) }
                if constexpr (i == 59) { AQ(3, 7) }
                SCHED0();
            });
        }
        QSTAMP(0, 2)
        {
            unsigned d0;
            auto rest = [&](auto J) { constexpr int j = CV(J);
                if constexpr (j >= 0 && j < 12 && (j % 6 == 0 || j % 3 == 2)) X.op<3 + j / 6, (j % 6 + 1) / 3>(k); };
            sfor<43>([&](auto F) { constexpr int f = CV(F) + 5;
                constexpr int e = f < 6 ? f + 3 : f < 9 ? f - 3 : f;
                A.mm<0, false, e / 6, e % 6>(W);
                if constexpr (f % 6 == 2 || f == 5) TOUCH(A.x1[f / 6]);
                else if constexpr (f == 47) { TOUCH(B.x0[0]); AQ(1, 6) AQ(2, 1) }
                else if constexpr (f % 6 == 5) TOUCH(A.x0[f / 6 + 1]);
                if constexpr (f == 40) { done = rt_async(); QSTAMP(0, 3) AQ(1, 5) }
                if constexpr (f == 21) set_m0(X.dst + 2 * 8192);
                if constexpr (f == 24) { X.sb = X.se + 2 * 16384; PINS(X.sb); }
                if constexpr (f == 42) AC3(4, -1, 7)
                SCHED0();
                if constexpr (f % 6 == 4 && f < 36) A.r0<f / 6 + 2>();
                if constexpr (f % 6 == 1 && f < 42) A.r1<f / 6 + 1>();
                if constexpr (f >= 39 && f < 43) B.rd<true, ((f - 39) / 2) * 6 + (f - 39) % 2>();
                if constexpr (f == 26 || f == 31) X.op<2, f / 15>(k);
                SCHED0();
                S.op<f + 3>(k, sc, a0, a1, hv);
                rest(IC(f - 43));
                if constexpr (f == 44) B.lwv = vadd(l16, L_W + w * 1024);
                SCHED0(); });
            B.go<30, true, 0, 48, 8>(W, [&](auto F) { constexpr int f = CV(F);
                rest(IC(f + 5));
                if constexpr (f == 9) AC3(5, -1, -1)
                if constexpr (f == 2) { a0 = (X.sg & 3) * PR_SZ; PINS(a0); }
                if constexpr (f == 4) { a1 = a0 + po1; a0 += po0; PINS(a0); PINS(a1); }
                if constexpr (f == 8) { S.sg = (X.sg & 4) ? 0xBF800000u : 0x3F800000u; PINS(S.sg); }
                if constexpr (f == 10) { S.hs = S.sg - hw2; PINS(S.hs); }
                if constexpr (f == 14 || f == 16) G.op<f / 16>(k, l16, X.sg);
                if constexpr (f == 22 || f == 28 || f == 32) X.next<(f - 22) / 5>(k, l16);
                if constexpr (f == 35) asm volatile("s_waitcnt vmcnt(6)\n\ts_barrier" ::: "memory");
                if constexpr (f == 20) AC3(6, 1, -1)
                if constexpr (f == 26) A.home<0>();
                if constexpr (f >= 36) A.rd<false, f + 12>();
                if constexpr (f == 38 || f == 40 || f == 45 || f == 46) X.next<(f == 38 ? 5 : f == 40 ? 6 : f - 42)>(k, l16);
                if constexpr (f == 47) { A.pre<false, 8>(); AQ(1, 7) AQ(2, 2) } });
            A.go<0, false, 48, 120, 20>(W, [&](auto F) { constexpr int f = CV(F), i = f - XPOS;
                if constexpr (f == 56) B.home<1>();
                if constexpr (i == 0 || i == 2 || i == 6) X.next<7 + i / 2>(k, l16);
                if constexpr (i >= 3 && i < 15 && ((i - 3) % 6 == 0 || (i - 3) % 3 == 2)) X.op<(i - 3) / 6, ((i - 3) % 6 + 1) / 3>(k);
                if constexpr (f == 58) asm volatile("s_waitcnt vmcnt(1)\n\ts_barrier" ::: "memory");
                if constexpr (f == 57) AC3(7, 2, -1)
                G.ask<f - RPOS>(k, l16);
                if constexpr (f >= 108) B.rd<true, f - 60>();
                SCHED0();
                G.red<f - 110>();
                if constexpr (f == 116) asm volatile("v_bfe_u32 %0, %1, 8, 1\n\tv_lshl_or_b32 %0, %0, 3, %1" : "=&v"(d0) : "v"(l16));
                if constexpr (f == 118) asm volatile("v_xor_b32 %0, %1, %0" : "+v"(d0) : "s"(L_K + (w >> 1) * 3072 + (w & 1) * 8));
                if constexpr (f == 119) { B.pre<true, 8>(); AQ(2, 3) } });
            B.go<30, true, 48, 120, 20>(W, [&](auto F) { constexpr int f = CV(F), i = f - 48;
                constexpr int ko[26] = {0, 1, 2, 4, 3, 6, 5, 8, 7, 9, 10, 11, 12, 13, 14, 16, 15, 18, 17, 20, 19, 21, 22, 23, -1, -1};
                G.hold<f - GPOS + 5>(X.sg);
                if constexpr (i < 24) { SCHED0(); A.out4<ko[i % 26], 0>(d0); }
                G.at<f - GPOS>(k, l16);
                if constexpr (f == 114) { lgkm0(done); PINS(G.rt); }
                if constexpr (f == 115) { PINS(now); HTRACE(X.sg - 2) an = (unsigned)done - (unsigned)now; PINS(an); }
                if constexpr (f == 116) { an = min(an, 200u); PINS(an); }
                if constexpr (f == 117) { an += DQ; PINS(an); }
                SCHED0();
                if constexpr (i == 28) AC3(-1, 3, -1)
                if constexpr (i == 50) AC3(-1, 4, -1)
                if constexpr (i == 62) AC3(-1, 5, -1)
                if constexpr (i == 23) { AQ(2, 4) }
                if constexpr (i == 47) { AQ(2, 5) }
                if constexpr (i == 65) { AQ(2, 6) }
                if constexpr (f == 119) { AQ(2, 7) }
                if constexpr (f == 112) asm volatile("" :: "{v[12:15]}"(G.v0), "{v[16:19]}"(G.v1), "{v[20:23]}"(G.p0), "{v[24:27]}"(G.p1), "{v9}"(B.lwv) TRHINT);
                if constexpr (f == 119) G.also(X.off);
                if constexpr (f >= 113 && f < 119) clr2(ac[(f + 6 - 113 + 5) % 6]); });
            static_assert(GPOS >= 70 && GPOS <= 98 && XPOS % 6 == 2 && RPOS > XPOS && RPOS + 1 < XPOS + 5, "GPOS");
            QSTAMP(0, 4) QSTAMP(0, 5)
            HFLUSH(s)
        }
        AST(0)
        PROF_ONLY(G.wait<0>();)
        HSTOP(0) HSTAMP(s, 0) AQ(1, 1) AQ(3, 1)
        G.all();
        SCHED0();
        smfw<50>(ac[5], __builtin_bit_cast(bf16x8, G.p0), W[50], ix);
        AQ(3, 2)
        SCHED0();
    } while (G.bad == 0);
    } while (X.sg != 65u);
    PROF_OUT
}

#define OIDX(t, b) (22 + (t) * 3 + (b))
DEVI void vo_role(const Params& p, Ctx& k, char* smem, int h) {
    PROF_DECL
    const int w = k.w;
    bf16x16 W[52];
    unsigned l16, lwv, ix, dq0, trp, vo;
    {
        const int l = k.l, g = l >> 4, n = l & 15, q = (l >> 2) & 3, pp = l & 3;
        l16 = l * 16; lwv = l16 + L_W + w * 1024; ix = SPIX(l);
        dq0 = L_QM + (24 * w + (n >> 2) * 2 + (n & 1)) * 32 + g * 8 + ((n >> 1) & 1) * 4;
        trp = L_QM + (16 * (g >> 1) + 4 * (g & 1) + 8 * (q >> 1) + (q & 1)) * 32 + pp * 8;
        vo = EXP(h * 512) + (g >> 1) * 256 + n * 16 + (g & 1) * 8;
        u32x8 r = {dq0, trp, vo, ix, l16, lwv, 0u, 0u};
        PIN(r);
        dq0 = r[0]; trp = r[1]; vo = r[2]; ix = r[3]; l16 = r[4]; lwv = r[5];
    }
    Gather G;
    G3 V;
    Xn X;
    V.l16 = l16; V.lwv = lwv; X.l16 = l16;
    f32x4 y[20];
    bf16x8 v[12];
    u32x4 o[10];
    unsigned yb;
    {
        const int l = k.l;
        bf16x8 wt[16];
        bf16x4 q[10][4];
        f32x4 z[9][2];
        u32x2 gv;
        unsigned ra[6];
        const u16* wo = p.Wo + (size_t)h * 192 * 640;
        constexpr int L_V = L_XN + 24576;
#define VEX(v) ((v) < 0 ? 0 : ((v) < 6) + ((v) == VGS) + 10 * ((v) == VGS + 2) + 6 * ((v) == VGS + 6 || (v) == 18))
#define VKE(v) ((v) < VGS + 4 ? 0 : CMIN(20, 7 * ((v) - VGS - 3)))
#define YKE(v) ((v) < 16 ? 0 : 3 * (v) - 45)
        auto put = [&](int t) { st128(k.rs, X.l16, yb + t * 65536, (u32x4){cvtpk(z[t][0][0], z[t][0][1]), cvtpk(z[t][0][2], z[t][0][3]), cvtpk(z[t][1][0], z[t][1][1]), cvtpk(z[t][1][2], z[t][1][3])}); };
        auto rdx = [&](auto K) { constexpr int ks = CV(K); if constexpr (ks < 20) { V.r0<ks % 20>(); V.r1<ks % 20>(); if constexpr (ks < 14) V.rw<ks>(); } };
        auto rdv = [&](auto J) { constexpr int j = CV(J) % 6; v[j] = lr128(l16 + L_V + j * 1024); v[6 + j] = lr128(l16 + L_V + (6 + j) * 1024); };
        cstream<20>([](auto V) { constexpr int v = CV(V); return IC(VEX(v - 1) + (v < 19 ? 6 : 0)); },
            [&](auto U) { constexpr int u = CV(U), k0 = VKE(u - 1), y0 = YKE(u - 1), n = u < 16 ? (VKE(u) - k0) * 6 : u < 18 ? 36 : 18, nn = n ? n : 12, nr = u < 10 ? 3 : u < 16 ? 6 : 4;
                if constexpr (u < 6) hello(k, 0);
                if constexpr (u == VGS) gv = go_ask(k);
                if constexpr (u == VGS + 2) { PIN(gv); go_wait(k, 0, gv); X.init(k); X.l16 = k.c ? OOB : l16; xn_all(k, X.l16, X.src, X.dst); }
                if constexpr (u == VGS + 4) { rdx(IC(0)); rdx(IC(1)); }
                if constexpr (u == VGS + 6) { sfor<3>([&](auto I) { X.next<CV(I)>(k, l16); }); X.se = X.src; yb = k.yx(0, w) + EXP(h * 1024); }
                if constexpr (u == 16) rdv(IC(0));
                XSTAMP(u)
                if constexpr (u == 0 || u == 10 || u == 16) {
#pragma unroll
                    for (int t = 0; t < nr; ++t) ra[t] = opq((u == 0 ? wp_adr<384, true>(w * 3 + t, l) : u == 10 ? wp_adr<768, false>(w * 6 + t, l) : wp_adr<512, false>(w * 4 + t, l)) + SLOT(0));
                }
                unsigned rb[6];
#pragma unroll
                for (int t = 0; t < nr; ++t) rb[t] = slot_adr<u>(ra[t]);
                SCHED0();
                sfor<nn>([&](auto I) { constexpr int i = CV(I), e = u == VGS + 6 ? 9 : 0, j = ((i - e) * 6) / (nn - e), d = u + 2, r0 = u == 18 ? 6 : 0;
                    if constexpr (u < 16 && n > 0) { V.mm<0, true, k0 + i / 6, i % 6>(W); if constexpr (i % 6 == 3) rdx(IC(k0 + i / 6 + 2)); }
                    if constexpr (u >= 16) { constexpr int m = n / 3, ks = (y0 + i / m) % 6, t = (u / 18) * 6 + (i % m) / 2, rt = i & 1;
                        if constexpr (!(ks == 5 && t == 7 && rt)) { mfw<OIDX(t, ks >> 1), ks & 1, true, ks == 0>(z[t][rt], W[OIDX(t, ks >> 1)], v[rt * 6 + ks]); SCHED0(); }
                        if constexpr (i % m == 1 && u < 19) rdv(IC(ks + 1));
                        if constexpr (u == 19 && i < 2) rdv(IC(4 + i));
                        if constexpr (u == 18 && i >= 3 && i < 15 && (i & 1)) put((i - 3) / 2);
                        if constexpr (u == 18 && i < 6) lw128(lwv + (10 + i) * 4096, __builtin_bit_cast(u32x4, wt[10 + i])); }
                    if constexpr (i < e) X.op<i / 3, i % 3>(k);
                    else if constexpr (u > 0 && u < 18 && (i == e || ((i - e - 1) * 6) / (nn - e) != j)) {
                        if constexpr (d < 10) dma1<SLOT(d) + (j / 3) * 12288 + (j % 3) * 1024>(p.Wv + (size_t)(2 * d + j / 3) * 32 * 6144 + h * 192, wsrc<384, true>(w * 192 + (j % 3) * 64 + l, 12288), w * 3072);
                        else if constexpr (d < 16) dma1<SLOT(d) + j * 1024>(wo + (d - 10) * 32 * 640, wsrc<768, false>(w * 384 + j * 64 + l, 1280), w * 6144);
                        else dma1<SLOT(d) + j * 1024>(wo + (d - 16) * 48 * 640 + 384, wsrc<512, false>(w * 384 + j * 64 + l, 1280), w * 6144);
                    }
                    if constexpr (i < 2 && u >= 3 && u < 8) lw128(lwv + (2 * u - 6 + i) * 4096, __builtin_bit_cast(u32x4, wt[(2 * u - 6 + i) & 15]));
                    sfor<2>([&](auto C) { constexpr int c = u == 19 ? 2 * i + CV(C) : i - r0 + CV(C) * 99;
                        if constexpr (c >= 0 && c < 12) {
                            constexpr int t = u < 10 ? c >> 2 : u < 16 ? c >> 1 : 6 + c % 4, r = u < 10 ? c & 3 : u < 16 ? (u - 10) * 2 + (c & 1) : (u - 16) * 3 + c / 4, id = u < 10 ? (t * 10 + u) % 52 : OIDX(t, r >> 2);
                            q[t][r & 3] = lrtr(u < 10 ? rb[t] + (r >> 1) * 12288 + (r & 1) * 1536 : u < 16 ? rb[t] + (c & 1) * 12288 : rb[(t - 6) & 3] + (c / 4) * 8192);
                            if constexpr (u < 10 && u >= 2 && t == 2) { if constexpr (r & 1) V.wl[2 * u + r / 2] = wt[(2 * u - 4 + r / 2) & 15] = CAT4(q[t][r - 1], q[t][r]); }
                            else if constexpr ((r & 3) == 1) W[id] = __builtin_shufflevector(q[t][0], q[t][1], 0, 1, 2, 3, 4, 5, 6, 7, -1, -1, -1, -1, -1, -1, -1, -1);
                            else if constexpr ((r & 3) == 3) W[id] = CAT8(LO8(W[id]), CAT4(q[t][2], q[t][3]));
                        } });
                    SCHED0(); });
                if constexpr (u == VGS + 7) sfor<18>([&](auto I) { V.out<CV(I), 0>(l16 + L_V + ((3 * w) >> 1) * 1024 + ((3 * w) & 1) * 8, l16 + L_V + ((3 * w + 1) >> 1) * 1024 + ((3 * w + 1) & 1) * 8); }); });
        static_assert(VGS >= 6 && VGS < 9, "VGS");
        RAWBAR()
        XSTAMP(20)
        sfor<2>([&](auto B) { X.op<CV(B) + 3, 0>(k); asm volatile("s_nop 0"); X.op<CV(B) + 3, 1>(k); X.op<CV(B) + 3, 2>(k); });
        asm volatile("" : "+v"(z[8][0]), "+v"(z[8][1]), "+v"(z[7][0]), "+v"(z[6][1]));
        put(6); put(8);
        o[7] = (u32x4){cvtpk(z[7][0][0], z[7][0][1]), cvtpk(z[7][0][2], z[7][0][3]), 0u, 0u};
        y[17] = z[7][1];
        yb += 7 * 65536;
        X.off = 0; X.l16 = l16;
        if (k.c) X.init(k);
    }
    RSTAMP(2)
    const unsigned pdum = k.pg(1) + DUMMY_B, pbc = w == 0 ? k.p0 + CNT(C_BC, (h >> 3) * 16 + 8 + (h & 7)) : pdum + 98304 + h * 512 + w * 128;
    unsigned long long now = 0, done = 0;
    G.mx = ~0u;
    G.init(k);
    G.base -= 32768;
    const unsigned ypar = (unsigned)(k.par >> (PG_YX + (w >> 1))), yo = ((unsigned)(PG_YX + (w >> 1)) << 21) + (w & 1) * 1048576 + EXP(h * 1024);
    const unsigned ym0 = k.ym(0) + w * 163840, ym2 = k.ym(2) + w * 163840;
    unsigned ym = k.c ? pdum + w * 163840 : ym0, ys, t, c = ~0u, an = 80;
#define YT(u) ((u) < 10 ? 2 * ((u) % 5) : ((u) - 10) | 1)
#define YR(u) ((u) < 10 ? (u) / 5 : (u) & 1)
    auto yout = [&](auto U, auto J) { constexpr int u = CV(U), j = CV(J), t = YT(u), rt = YR(u);
        if constexpr (j == 0) { o[t][2 * rt] = cvtpk(y[u][0], y[u][1]); o[t][2 * rt + 1] = cvtpk(y[u][2], y[u][3]); TOUCH(o[t][2 * rt]); TOUCH(o[t][2 * rt + 1]); }
        if constexpr (j == 1 && rt) __builtin_amdgcn_raw_buffer_store_b128(o[t], k.rs, (int)l16, (int)yb, AUX_SC1);
        if constexpr (j == 2 && rt) { yb += t == 8 ? -7 * 65536 : 131072; PINS(yb); TOUCH(o[t]); } };
    auto ymm = [&](auto I) {
        constexpr int i = CV(I), ks = i % 6, u = i / 6, id = OIDX(YT(u), ks >> 1);
        mfw<id, ks & 1, true, ks == 0>(y[u], W[id], v[YR(u) * 6 + ks]);
        SCHED0();
        if constexpr (u > 0 && ks >= 2) yout(IC((u + 19) % 20), IC(ks - 2)); };
    do {
        if (X.off != 0) { PC8(1, c) X.late<5>(k, l16); }
    do {
        PROF_S(X.sg - 1)
        f32x4 ac[3];
        asm volatile("s_waitcnt vmcnt(7)\n\ts_barrier" ::: "memory");
        SCHED0();
        sfor<13>([&](auto I) { constexpr int i = CV(I) + 107;
            ymm(IC(i));
            if constexpr (i == 107) { AST(5) st32(k.rs, 0, pbc + 4, G.mx); }
            if constexpr (i == 119) V.pre<true, 0>();
            G.ask<i - 112>(k, l16);
            if constexpr (i == 108) V.r0<0>();
            if constexpr (i == 109) V.r1<0>();
            if constexpr (i == 114) V.r0<1>();
            if constexpr (i == 115) V.r1<1>();
            SCHED0(); });
        HSTAMP(s, 4)
        V.run<0, true, 0, 16, 17, true>(W, [&](auto I) { constexpr int i = CV(I);
            if constexpr (i >= 2) yout(IC(19), IC(i - 2));
            if constexpr (i == 8) { t = (X.sg & 7) * 2; PINS(t); }
            if constexpr (i == 10) { yb = (ypar >> t) & 1; PINS(yb); }
            if constexpr (i == 14) { t = (t << 21) + yo; PINS(t); }
            if constexpr (i == 16) { yb = (yb << 13) + t; PINS(yb); }
            if constexpr (i == 20) { c = X.sg; PINS(c); }
            if constexpr (i == 22) { G.a = max(c, 1u) << 15; PINS(G.a); }
            if constexpr (i == 28) G.op<1>(k, l16, 0);
            if constexpr (i == 40) { an = min(an, 200u) + DV; PINS(an); }
            if constexpr (i == 32 || i == 34 || i == 38) X.next<(i - 31) / 3>(k, l16);
            if constexpr (i == 23 || i == 29) { AAC(4 + i / 29) }
            if constexpr (i == 83 || i == 89) { AAC(6 + i / 89) }
            if constexpr (i == YPOS) { AST(6) }
            if constexpr (i == YPOS + 1) { AST(7) }
            if constexpr (i == YPOS) { asm volatile("s_waitcnt vmcnt(0)\n\ts_barrier" ::: "memory"); }
            if constexpr (i == YPOS - 2) G.mx = vshl1(c, 0);
            if constexpr (i == YPOS + 1) st32(k.rs, 0, pbc + 4, G.mx);
            SCHED0();
            G.red<i - YPOS - 2>();
            G.hold<i - GPOSY * 6 + 5>(c);
            G.at<i - GPOSY * 6>(k, l16);
            if constexpr (i == 90) PINS(G.rt);
            if constexpr (i == 95) V.pre<true, 16>();
            if constexpr (i >= 92 && i < 95) clr2(ac[(i - 92) % 3]); });
        static_assert(GPOSY * 6 + 2 > YPOS + 1 && GPOSY <= 13, "GPOSY");
        HSTAMP(s, 5)
        HFLUSH(s - 1)
        f32x4 acc[10];
        f32x2 z[20];
        auto clr = [&](auto I) { constexpr int i = CV(I); asm volatile("v_mov_b64 %0, 0" : "=v"(z[i])); if constexpr (i & 1) acc[i >> 1] = __builtin_shufflevector(z[i - 1], z[i], 0, 1, 2, 3); };
        bf16x4 al[3], ah[3];
        unsigned d0, d1;
        {
            bf16x8 m[10], wa[10], wb[10];
            auto rd = [&](auto R) { constexpr int r = CV(R), b = 2 + r / 3;
                if constexpr (b < 10) {
                    if constexpr (r % 3 == 0) m[b] = lr128(l16 + MADR(b));
                    if constexpr (r % 3 == 1) wa[b] = lr128(lwv + (b - 2) * 8192);
                    if constexpr (r % 3 == 2) wb[b] = lr128(lwv + (b - 2) * 8192 + 4096);
                } else if constexpr (b < 13) {
                    if constexpr (r % 3 == 0) V.r0<b + 7>();
                    if constexpr (r % 3 == 1) V.rw<b + 7>();
                    if constexpr (r % 3 == 2) V.r1<b + 7>();
                } };
            AST(0)
            G.wait<0>();
            HSTOP(0) HSTAMP(s, 0) AST(1)
            V.run<0, true, 16, 17, 0, true>(W, [&](auto I) { constexpr int i = CV(I);
                G.fin<i>(0);
                if constexpr (i == 3) {
                    if (__builtin_expect(G.bad != 0, 0)) {
                        unsigned spins = 0;
_Pragma("unroll 1")
                        do { PC8(0, c) G.again(k, l16); } while (G.bad != 0 && !spin_fail(k, spins++));
                        HSTOP(0) HSTAMP(s, 0)
                    }
                    asm volatile("s_barrier" ::: "memory");
                    AST(2)
                }
                if constexpr (i == 4) { now = rt_async(); HSTOP(1) SCHED0(); }
                if constexpr (i >= 3) rd(IC(i - 3)); });
            m[0] = __builtin_bit_cast(bf16x8, G.p0); m[1] = __builtin_bit_cast(bf16x8, G.p1);
            sfor<30>([&](auto I) {
                constexpr int i = CV(I), b = i / 3, t = i % 3;
                if constexpr (t == 2 && b >= 2) smf<false>(ac[2], m[b], CAT8(wa[b], wb[b]), ix); else smfw<t * 10 + b>(ac[t], m[b], W[t * 10 + b], ix);
                SCHED0();
                if constexpr (i >= 6 && i < 29 && t != 0) { if constexpr (t == 1) TOUCH(wb[b]); else TOUCH(m[(b + 1) % 10]); }
                if constexpr (i == 29) V.pre<true, 17>();
                if constexpr (i == 2) { PINS(now); an += (unsigned)now; PINS(an); }
                if constexpr (i == 3) an = spack(an, c);
                SCHED0();
                if constexpr (i < 27) rd(IC(3 + i));
                if constexpr (i == 5) st32(k.rs, 0, pbc, d0);
                SCHED0();
                if constexpr (i == 4) { d0 = an; PIN(d0); }
                if constexpr (i == 26) X.next<3>(k, l16);
                if constexpr (i == 28) d0 = vadd(l16, L_K + ((3 * w) >> 1) * 1024 + ((3 * w) & 1) * 8);
                if constexpr (i == 29) d1 = vadd(l16, L_K + ((3 * w + 1) >> 1) * 1024 + ((3 * w + 1) & 1) * 8);
                SCHED0();
            });
            unsigned fo[3];
            V.run<0, true, 17, 19, 0, true>(W, [&](auto I) { constexpr int i = CV(I);
                if constexpr (i < 3) { rd(IC(30 + i)); SCHED0(); fold_op<0>(ac[i], fo[i]); }
                if constexpr (i == 3 || i == 4 || i == 6) { constexpr int t = i / 2 - 1; asm volatile("v_add_f32 %1, %1, %2\n\tv_cvt_pk_bf16_f32 %0, %3, %1" : "=v"(fo[t]), "+v"(ac[t][1]) : "v"(ac[t][3]), "v"(ac[t][0])); }
                if constexpr (i == 5 || i == 7 || i == 8) lw32(dq0 + ((i - 4) / 2) * 256, fo[(i - 4) / 2]);
                if constexpr (i == 9 || i == 10) clr2(acc[i - 9]);
                if constexpr (i == 11) BARL(0) });
        }
        HSTAMP(s, 1) AST(3)
        {
            unsigned fo[2];
            auto fin2 = [&](auto P, auto J) { fold2<CV(J)>(acc[2 * CV(P)], acc[2 * CV(P) + 1], fo); };
            V.run<0, true, 19, 20, 0, true>(W, [&](auto I) { constexpr int i = CV(I);
                if constexpr (i == 0) { al[0] = lrtr(trp); ah[0] = lrtr(trp + 64); }
                if constexpr (i == 1) al[1] = lrtr(trp + 1024);
                if constexpr (i == 2) ah[1] = lrtr(trp + 1088);
                if constexpr (i == 3) al[2] = lrtr(trp + 2048);
                if constexpr (i == 4) ah[2] = lrtr(trp + 2112);
                SCHED0();
                if constexpr (i >= 1) clr(IC(3 + i)); });
            sfor<30>([&](auto I) { constexpr int i = CV(I), b = (i % 6) >> 1, t = 2 * (i / 6) + (i & 1), id = OIDX(t, b);
                smfw<id>(acc[t], CAT4(al[b], ah[b]), W[id], ix);
                SCHED0();
                if constexpr (i == 1 || i == 3) TOUCH(ah[(i + 1) / 2]);
                if constexpr (i == 29) TOUCH(v[0]);
                if constexpr (i == 21) BARL(0)
                SCHED0();
                if constexpr (i == XLD) X.next<4>(k, l16);
                if constexpr (i >= 21 && i < 27) v[i - 21] = lr128(l16 + L_K + (i - 21) * 1024);
                SCHED0();
                V.out<i - 1, 0>(d0, d1);
                SCHED0();
                if constexpr (i % 3 == 0 && i < 18) clr(IC(9 + i / 3));
                if constexpr (i == 16) clr(IC(15));
                if constexpr (i >= 18 && i < 21) clr(IC(i - 2));
                if constexpr (i == 22) clr(IC(19));
                if constexpr (i >= 23 && i < 29) fold_op<(i - 23) / 2>(acc[(i - 23) & 1], fo[(i - 23) & 1]);
                if constexpr (i == 29) { st64(k.rs, vo, ym, (u32x2){fo[0], fo[1]}); SCHED0(); fin2(IC(1), IC(0)); }
                SCHED0(); });
            HSTAMP(s, 2)
            sfor<107>([&](auto I) { constexpr int i = CV(I), q = i < 8 ? i + 4 : i + 3;
                ymm(I);
                if constexpr (i < 5 || (i >= 29 && i < 35)) TOUCH(v[(i < 5 ? i + 1 : i - 23) % 12]);
                if constexpr (i < 13 && i != 8) {
                    if constexpr (q % 3 == 1) { ym += 32768; PINS(ym); }
                    if constexpr (q % 3 == 2 && i >= 5) __builtin_amdgcn_s_setprio(0);
                    SCHED0();
                    if constexpr (q % 3 == 0) { st64(k.rs, vo, ym, (u32x2){fo[0], fo[1]}); SCHED0(); }
                    if constexpr (q < 15) fin2(IC(q / 3), IC(q % 3));
                }
                if constexpr (i == 13) done = rt_async();
                if constexpr (i >= 15 && i < 23 && i != 18 && i != 20) v[6 + i - 15 - (i > 18) - (i > 20)] = lr128(l16 + L_K + (6 + i - 15 - (i > 18) - (i > 20)) * 1024);
                if constexpr (i == 12 || i == 13) X.next<i - 7>(k, l16);
                if constexpr (i == 15) { SCHED0(); X.next<7>(k, l16); }
                if constexpr (i == 18 || i == 23) X.next<8 + 2 * (i / 23)>(k, l16);
                if constexpr (i >= 19 && i < 37 && (i % 6 == 1 || i % 3 == 0)) X.op<(i - 19) / 6, ((i - 19) % 6 + 1) / 3>(k);
                if constexpr (i >= 66 && i < 90 && i % 3 == 0 && i % 12 != 3) X.op<3 + (i - 66) / 12, ((i - 66) / 3) % 4>(k);
                if constexpr (i == 90) { ym = (c & 2) ? ym2 : ym0; PINS(ym); }
                if constexpr (i == 91) { ys = ym + EXP(327680); PINS(ys); }
                if constexpr (i == 96) { ym = (c & 1) ? ys : ym; PINS(ym); }
                if constexpr (i == 60) { HSTAMP(s, 3) }
                if constexpr (i == 42 || i == 43 || i == 48 || i == 49) { AAC((i - 42) / 3 + (i & 1)) }
                if constexpr (i == 102) lgkm0(done);
                if constexpr (i == 103) { PINS(now); HTRACE(c - 1) an = (unsigned)done - (unsigned)now; PINS(an); }
                if constexpr (i == 105) G.mx = vshl1(c, 1);
                SCHED0(); });
            AST(4)
        }
    } while (X.off == 0);
    } while (X.sg != 65u);
    HFLUSH(63)
    PROF_OUT
}

DEVI void row_role(const Params& p, const Ctx& k, char* smem, int rt) {
    PROF_DECL
    const int l = k.l, n = l & 15;
    const unsigned fo = k.w * 2048 + rt * 1024;
    RSTAMP(2)
    const __amdgpu_buffer_rsrc_t rs = __builtin_amdgcn_make_buffer_rsrc(k.c ? (void*)k.ws : (void*)p.x, 0, 0x7fffffff, 0x00020000);
    const unsigned vs = k.c ? l * 16 : n * 1280 + (l >> 4) * 16, cs = k.c ? 16384 : 256, lim = k.c ? 0xFFFFFFFEu : 0xFFFFFFFFu;
    for (int sg = k.c ? 0 : 8; sg < 64; ++sg) {
        if (k.c) prog_wait<5>(k, k.p0 + CNT(C_X2, rt * 5), sg);
        STOP(sg) STAMP(sg, 0)
        const unsigned src = k.c ? k.seg(PG_X2, sg) + fo : (sg * 32 + rt * 16) * 1280 + k.w * 64, dst = k.seg(PG_XNA, sg) + fo;
        u32x4 a[5];
_Pragma("unroll 1")
        for (unsigned spins = 0;; ++spins) {
            unsigned mx = 0;
#pragma unroll
            for (int c = 0; c < 5; ++c) a[c] = ld128(rs, vs, src + c * cs);
#pragma unroll
            for (int c = 0; c < 5; ++c) mx = max(mx, umax4(a[c]));
            if (__all(mx <= lim) || spin_fail(k, spins)) break;
            PCOUNT(0)
            asm volatile("" ::: "memory");
        }
        STAMP(sg, 1)
        f32x2 v[20], s2[4], q2[4];
#pragma unroll
        for (int i = 0; i < 20; ++i) { v[i] = (f32x2){blo(a[i >> 2][i & 3]), bhi(a[i >> 2][i & 3])}; s2[i & 3] = i < 4 ? v[i] : s2[i & 3] + v[i]; }
        const f32x2 st = (s2[0] + s2[1]) + (s2[2] + s2[3]);
        const float sm = xor32add(xor16add(st[0] + st[1])), mu = sm * (1.f / 160.f);
#pragma unroll
        for (int i = 0; i < 20; ++i) { const f32x2 d = v[i] - mu; q2[i & 3] = i < 4 ? d * d : __builtin_elementwise_fma(d, d, q2[i & 3]); }
        const f32x2 qt = (q2[0] + q2[1]) + (q2[2] + q2[3]);
        *(f32x2*)(smem + n * 32 + k.w * 8) = (f32x2){sm, xor32add(xor16add(qt[0] + qt[1]))};
        RAWBAR()
        const f32x4 e0 = *(const f32x4*)(smem + n * 32), e1 = *(const f32x4*)(smem + n * 32 + 16);
        float m = ((e0[0] + e0[2]) + (e1[0] + e1[2])) * (1.f / 640.f);
        const f32x4 d = (f32x4){e0[0], e0[2], e1[0], e1[2]} - 160.f * m;
        const float var = bfr((((e0[1] + e0[3]) + (e1[1] + e1[3])) + ((d[0] * d[0] + d[1] * d[1]) + (d[2] * d[2] + d[3] * d[3])) * (1.f / 160.f)) * (1.f / 640.f));
        m = bfr(m);
        const float rden = __builtin_amdgcn_rcpf(bfr(__builtin_amdgcn_sqrtf(bfr(var + 1e-5f))));
#pragma unroll
        for (int c = 0; c < 5; ++c) {
            u32x4 o;
#pragma unroll
            for (int i = 0; i < 4; ++i) { const f32x2 x = v[c * 4 + i] - m; o[i] = cvtpk(bfr(x[0]) * rden, bfr(x[1]) * rden); }
            st128(k.rs, l * 16, dst + c * 16384, o);
        }
        STAMP(sg, 2)
        DRAIN();
        __builtin_amdgcn_s_barrier();
        st32(k.rs, (k.w == 0 && l == 0) ? 0u : OOB, k.p0 + CNT(C_XN, rt), (unsigned)(sg - (k.c ? 0 : 8)));
        STAMP(sg, 3) STAMP(sg, 4) STAMP(sg, 5)
        PFLUSH(sg)
    }
    PROF_OUT
}

DEVI int red_col(int j) { const int w = j / 5, q = j % 5; return q < 3 ? 3 * w + q : 9 + 2 * w + q; }
struct Red {
    u32x4 Y[16];
    template <int I0, int I1> DEVI void yload(const Ctx& k, int j, int sg) {
        const unsigned a = k.yx(sg, j / 5) + (j % 5) * 131072 + k.w * 16384;
#pragma unroll
        for (int i = I0; i < I1; ++i) Y[i] = ld128(k.rs, k.l * 16 + (i & 3) * 1024, a + ((i >> 2) & 1) * 4096 + (i >> 3) * 65536);
    }
};
DEVI unsigned y_get(const Ctx& k) { return ld32(k.rs, (k.l & 7) * 128, k.p0 + CNT(C_BC, k.w * 16 + 8) + 4); }
DEVI void y_wait(const Ctx& k, int s) {
_Pragma("unroll 1")
    for (unsigned spins = 0; !__all((int)y_get(k) >= s); ++spins) {
        asm volatile("" ::: "memory");
        __builtin_amdgcn_s_sleep(2);
        if (spin_fail(k, spins)) break;
    }
    __builtin_amdgcn_fence(__ATOMIC_ACQUIRE, "wavefront");
}
DEVI void wait_until(unsigned t) { while ((int)((rclk() - t) << 16) < 0) { if (WUS) __builtin_amdgcn_s_sleep(WUS); } }
#define DPPMAX(x, c) x = max(x, __builtin_amdgcn_update_dpp(0, x, c, 0xf, 0xf, false))
#define TSLP 1
#define TWU 0
DEVI void wait_until2(unsigned t) { while ((int)((rclk() - t) << 16) < 0) { if (TWU) __builtin_amdgcn_s_sleep(TWU); } }
#define WAIT_BC(s, BSLP) { \
    unsigned bc; \
    _Pragma("unroll 1") for (;; ++spins) { \
        bc = ld32(k.rs, (l & 15) * 128, k.p0 + CNT(C_BC, w * 16)); \
        DRAIN(); \
        if (__all(((int)bc >> 16) > s)) break; \
        __builtin_amdgcn_s_sleep(BSLP); \
        if (spin_fail(k, spins)) break; \
    } \
    const unsigned now = rclk(); \
    int d = (int)((bc - now) << 16); \
    d = ((int)bc >> 16) > s + 1 || d > (256 << 16) ? -(64 << 16) : d; \
    DPPMAX(d, 0xB1); DPPMAX(d, 0x4E); DPPMAX(d, 0x141); \
    tq = now + (__builtin_amdgcn_readlane(d, 0) >> 16); tv = now + (__builtin_amdgcn_readlane(d, 8) >> 16); }
DEVI void mred_role(const Params& p, const Ctx& k, char* smem, int j) {
    PROF_DECL
    const int l = k.l, w = k.w, g = l >> 4, n = l & 15;
    const unsigned ix = SPIX(l);
    const unsigned vp = (g * 16 + ((n >> 1) & 1) * 8 + (n >> 2) * 2 + (n & 1)) * 16;
    const unsigned v16 = (g >> 1) * 1024 + (g & 1) * 256 + n * 16, yo = j * 32768 + EXP(w * 4096);
    const u32x4 ones = {0xFFFFFFFFu, 0xFFFFFFFFu, 0xFFFFFFFFu, 0xFFFFFFFFu};
    auto arm = [&](int s) {
#pragma unroll
        for (int i = 0; i < 4; ++i) st128(k.rs, l * 16 + i * 1024, k.ym(s) + yo, ones);
    };
    const unsigned pv = l == 0 && j % 5 == 0 ? (j / 5) * 128 + w * 32 : OOB;
    Red R;
    RSTAMP(2)
    arm(1);
#pragma unroll
    for (int i = 0; i < 8; ++i) st128(k.rs, (i || w > 1 || g > 1) ? g * 32768 + n * 16 : OOB, k.memf(i * 8 + (w >> 1) * 4) + EXP((2 * j + (w & 1)) * 256), ones);
#pragma unroll
    for (int i = 0; i < 4; ++i) { const unsigned q = min(i * 80 + j * 4 + w, 287); st128(k.rs, l * 16, k.p0 + PR_B + PR_SZ + EXP(q * 1024), ones); }
    sleep_until(k.t0 + RSL);
    y_wait(k, 2);
    R.yload<0, 16>(k, j, 0);
    for (int s = 0; s < 63; ++s) {
        STOP(s) HSTAMP(s, 0)
        const unsigned ap = k.p0 + PR_B + (s & 3) * PR_SZ + w * 49152, am = k.ym(s) + yo;
        const unsigned ph = (s & 4) ? 0x80008000u : 0u;
        unsigned spins = 0, tq, tv;
        unsigned fl;
        WAIT_BC(s, BSLP)
        st32(k.rs, pv, k.p0 + CNT(C_XB, 0), spack(((int)((tq - tv) << 16) < 0 ? tv : tq) + RDY, s + 2));
        STAMP(s, 5)
        char* rb = smem + (s & 1) * 4096;
_Pragma("unroll 1")
        for (spins = 0;; ++spins) {
            u32x4 ym[4], pm[5];
            f32x4 acc[2];
            if (spins == 0) wait_until(tv);
            asm volatile("" ::: "memory");
#pragma unroll
            for (int i = 0; i < 4; ++i) ym[i] = ld128(k.rs, v16 + (i >> 1) * 2048 + (i & 1) * 512, am);
            fl = ld32(k.rs, 0, (k.p0 ^ 8192u) + CNT(C_X1, j));
            asm volatile("" ::: "memory");
            if (spins == 0) { wait_until(tq); STAMP(s, 1) }
            asm volatile("" ::: "memory");
#pragma unroll
            for (int i = 0; i < 5; ++i) pm[i] = ld128(k.rs, i < 4 ? l * 16 + i * 1024 : vp, ap + (i >> 2) * 4096);
            acc[0] = (f32x4){0.f, 0.f, 0.f, 0.f}; acc[1] = acc[0];
            const u32x8 y0 = {ym[0][0], ym[1][0], ym[0][2], ym[1][2], ym[2][0], ym[3][0], ym[2][2], ym[3][2]};
            const u32x8 y1 = {ym[0][1], ym[1][1], ym[0][3], ym[1][3], ym[2][1], ym[3][1], ym[2][3], ym[3][3]};
            unsigned mx = 0;
#pragma unroll
            for (int i = 0; i < 4; ++i) mx = max(mx, umax4(ym[i]));
            unsigned tg = mx == 0xFFFFFFFFu ? 0x80000000u : 0u;
            asm volatile("s_nop 1" : "+v"(acc[0]), "+v"(acc[1]), "+v"(tg) : "v"(y0), "v"(y1), "v"(ix));
            SCHED0();
#pragma unroll
            for (int i = 0; i < 5; ++i) {
                const unsigned u0 = __builtin_amdgcn_bitop3_b32(pm[i][0], pm[i][1], ph, 0x7e), u1 = __builtin_amdgcn_bitop3_b32(pm[i][2], pm[i][3], ph, 0x7e);
                SCHED0();
                if (i < 4) smf<false>(acc[0], __builtin_bit_cast(bf16x8, pm[i]), __builtin_bit_cast(bf16x16, __builtin_shufflevector(R.Y[2 * i], R.Y[2 * i + 1], 0, 1, 2, 3, 4, 5, 6, 7)), ix);
                else smf<false>(acc[0], __builtin_bit_cast(bf16x8, pm[4]), __builtin_bit_cast(bf16x16, y0), ix);
                tg = (tg | u0) | u1;
                SCHED0();
                if (i < 4) smf<false>(acc[1], __builtin_bit_cast(bf16x8, pm[i]), __builtin_bit_cast(bf16x16, __builtin_shufflevector(R.Y[8 + 2 * i], R.Y[9 + 2 * i], 0, 1, 2, 3, 4, 5, 6, 7)), ix);
                else smf<false>(acc[1], __builtin_bit_cast(bf16x8, pm[4]), __builtin_bit_cast(bf16x16, y1), ix);
                SCHED0();
            }
            const unsigned long long bad = __ballot((int)tg < 0);
            asm volatile("s_nop 5" : "+v"(acc[0]), "+v"(acc[1]) : "s"(bad));
#pragma unroll
            for (int t = 0; t < 2; ++t) { float* rp = (float*)(rb + t * 2048 + w * 512 + g * 128 + n * 4); rp[0] = acc[t][0] + acc[t][2]; rp[16] = acc[t][1] + acc[t][3]; }
            if (bad == 0) break;
            PCOUNT(0)
            asm volatile("" ::: "memory");
            if (spin_fail(k, spins)) break;
        }
        STAMP(s, 2)
        RAWBAR()
        if (w < 2) {
            const char* rp = rb + w * 2048 + ((l >> 4) * 2 + ((l >> 2) & 1)) * 64 + ((l & 3) * 4 + ((l >> 3) & 1) * 2) * 4;
            const f32x2 a = *(const f32x2*)rp, b = *(const f32x2*)(rp + 512), c = *(const f32x2*)(rp + 1024), d = *(const f32x2*)(rp + 1536);
            const f32x2 r = (a + b) + (c + d);
            st32(k.rs, l * 4, k.memf(s + 1) + EXP((2 * red_col(j) + w) * 256), min(cvtpk(r[0], r[1]) ^ ph, 0xFFFFFFFEu));
        }
        STAMP(s, 3)
_Pragma("unroll 1")
        for (spins = 0; (int)__builtin_amdgcn_readfirstlane(fl) < s - 2; ++spins) {
            PCOUNT(1)
            __builtin_amdgcn_s_sleep(PSLEEP);
            if (spin_fail(k, spins)) break;
            fl = ld32(k.rs, 0, (k.p0 ^ 8192u) + CNT(C_X1, j));
        }
        __builtin_amdgcn_fence(__ATOMIC_ACQUIRE, "wavefront");
        y_wait(k, 2 * s + 3);
        R.yload<0, 8>(k, j, s + 1);
        arm(s + 2);
        y_wait(k, 2 * s + 4);
        STAMP(s, 4)
        R.yload<8, 16>(k, j, s + 1);
        PFLUSH(s)
    }
    PFLUSH(63)
    PROF_OUT
}
DEVI void tred_role(const Params& p, const Ctx& k, char* smem, int j) {
    PROF_DECL
    const int l = k.l, w = k.w, g = l >> 4, n = l & 15, T = 2 * red_col(j) + (w >> 1), rt = w & 1;
    const unsigned v16 = (g >> 1) * 1024 + (g & 1) * 256 + n * 16, yo = j * 32768 + EXP(w * 4096);
    const unsigned xo = EXP((T >> 1) * 2048) + (T & 1) * 512 + rt * 1024, xv = ((g >> 1) * 16 + n) * 16 + (g & 1) * 8;
    const u32x2 ones = {0xFFFFFFFFu, 0xFFFFFFFFu};
    Red R;
    RSTAMP(2)
    st64(k.rs, xv, k.segx(PG_X1, 0) + xo, ones); st64(k.rs, xv, k.segx(PG_X1, 1) + xo, ones);
    sleep_until(k.t0 + RSL);
    y_wait(k, 2);
    R.yload<0, 16>(k, j, 0);
    for (int s = 0; s < 64; ++s) {
        STOP(s) STAMP(s, 0)
        const unsigned ap1 = k.p0 + PR_B + (s & 3) * PR_SZ + w * 49152 + 16384, ap2 = ap1 + 16384, am = k.ym(s) + yo;
        const unsigned ph = (s & 4) ? 0x80008000u : 0u;
        unsigned spins = 0, tq, tv;
        u32x2 res;
        if (k.c) res = ld64(k.rs, xv, k.seg(PG_X2, s) + xo);
        else res = *(const u32x2*)(p.x + (size_t)(s * 32 + rt * 16 + n) * 640 + T * 16 + g * 4);
        f32x4 acc[2][2];
        WAIT_BC(s, TSLP)
        u32x4 yl[8];
#pragma unroll
        for (int i = 0; i < 8; ++i) { yl[i] = __builtin_shufflevector(R.Y[2 * i], R.Y[2 * i + 1], 0, 1, 4, 5); PIN(yl[i]); }
        wait_until2(tv); wait_until2(tq + RDLY);
        asm volatile("" ::: "memory");
        STAMP(s, 1)
_Pragma("unroll 1")
        for (;; ++spins) {
            u32x4 ym[4], pp[16];
#pragma unroll
            for (int i = 0; i < 4; ++i) ym[i] = ld128(k.rs, v16 + (i >> 1) * 2048 + (i & 1) * 512, am);
#pragma unroll
            for (int i = 0; i < 16; ++i) pp[i ^ 4] = ld128(k.rs, l * 16 + (i & 3) * 1024, (i < 8 ? ap2 : ap1) + (((i ^ 4) >> 2) & 1) * 4096);
            unsigned mx = 0, tg = 0;
#pragma unroll
            for (int t = 0; t < 2; ++t) {
                acc[t][0] = (f32x4){0.f, 0.f, 0.f, 0.f}; acc[t][1] = acc[t][0];
#pragma unroll
                for (int f = 0; f < 2; ++f) {
                    const u32x4 yf = {ym[2 * f][t], ym[2 * f][t + 2], ym[2 * f + 1][t], ym[2 * f + 1][t + 2]};
#pragma unroll
                    for (int r = 0; r < 2; ++r) acc[t][r] = mf(__builtin_bit_cast(bf16x8, yf), __builtin_bit_cast(bf16x8, pp[4 + 2 * r + f]), acc[t][r]);
                }
            }
#pragma unroll
            for (int i = 0; i < 4; ++i)
#pragma unroll
                for (int t = 0; t < 2; ++t) acc[t][0] = mf(__builtin_bit_cast(bf16x8, yl[t * 4 + i]), __builtin_bit_cast(bf16x8, pp[i]), acc[t][0]);
#pragma unroll
            for (int i = 0; i < 8; ++i)
#pragma unroll
                for (int t = 0; t < 2; ++t) acc[t][1] = mf(__builtin_bit_cast(bf16x8, R.Y[t * 8 + (i ^ 4)]), __builtin_bit_cast(bf16x8, pp[8 + (i ^ 4)]), acc[t][1]);
#pragma unroll
            for (int i = 0; i < 4; ++i) mx = max(mx, umax4(ym[i]));
#pragma unroll
            for (int i = 0; i < 16; ++i) tg |= __builtin_amdgcn_bitop3_b32(pp[i ^ 4][0], pp[i ^ 4][1], ph, 0x7e) | __builtin_amdgcn_bitop3_b32(pp[i ^ 4][2], pp[i ^ 4][3], ph, 0x7e);
            if (__all(mx != 0xFFFFFFFFu && (int)tg >= 0)) break;
            PCOUNT(0)
            asm volatile("" ::: "memory");
            if (spin_fail(k, spins)) break;
        }
        STAMP(s, 2)
        const unsigned yc = y_get(k);
        char* rb = smem + (s & 1) * 16384;
#pragma unroll
        for (int i = 0; i < 4; ++i) *(f32x4*)(rb + i * 4096 + w * 1024 + l * 16) = acc[i >> 1][i & 1];
        RAWBAR()
        st32(k.rs, (w == 0 && l == 0) ? 0u : OOB, (k.p0 ^ 8192u) + CNT(C_X1, j), (unsigned)s);
        {
            const char* rp = rb + w * 4096 + l * 16;
            const f32x4 r = (*(const f32x4*)rp + *(const f32x4*)(rp + 1024)) + (*(const f32x4*)(rp + 2048) + *(const f32x4*)(rp + 3072));
            const unsigned d0 = cvtpk(r[0], r[1]) ^ ph, d1 = cvtpk(r[2], r[3]) ^ ph;
            u32x2 o; o[0] = packs(blo(res[0]) + blo(d0), bhi(res[0]) + bhi(d0)); o[1] = packs(blo(res[1]) + blo(d1), bhi(res[1]) + bhi(d1));
            st64(k.rs, xv, k.segx(PG_X1, s) + xo, o);
            st64(k.rs, s < 62 ? xv : OOB, k.segx(PG_X1, (s + 2) & 63) + xo, ones);
        }
        STAMP(s, 3)
        if (s < 63) {
            if (!__all((int)yc >= 2 * s + 4)) { PCOUNT(2) y_wait(k, 2 * s + 4); }
            __builtin_amdgcn_fence(__ATOMIC_ACQUIRE, "wavefront");
            R.yload<0, 16>(k, j, s + 1);
        }
        STAMP(s, 4)
        STAMP(s, 5)
        PFLUSH(s)
    }
    PROF_OUT
}

DEVI void up_role(const Params& p, const Ctx& k, char* smem, int id) {
    PROF_DECL
    const int l = k.l, w = k.w, g = l >> 4, n = l & 15;
    bf16x8 Wt[80];
    const int tile0 = id * 8 + w * 2;
    auto issue = [&](auto U) { constexpr int u = CV(U);
        sfor<8>([&](auto J) { constexpr int j = CV(J); dma1<(u % 3) * 32768 + j * 1024>(((w & 1) ? p.wu : p.wg) + (size_t)(2 * u + (w >> 1)) * 32 * 1536 + id * 128, wsrc<256, true>(j * 64 + l, 3072), w * 8192); }); };
    sfor<3>(issue);
    cstream<10>([](auto V) { return IC(CV(V) < 9 ? 8 : 0); },
        [&](auto U) { constexpr int u = CV(U);
            if constexpr (u > 0 && u < 8) issue(IC(u + 2));
#pragma unroll
            for (int i = 0; i < 8; ++i) Wt[(i & 3) * 20 + 2 * u + (i >> 2)] = wp_read<256, true>(smem + (u % 3) * 32768 + (i >> 1) * 8192, w * 2 + (i & 1), l); });
    RAWBAR()
    RSTAMP(2)
    bf16x8 one8 = {0x3F80, 0x3F80, 0x3F80, 0x3F80, 0x3F80, 0x3F80, 0x3F80, 0x3F80};
    asm volatile("s_nop 1" : "+v"(one8));
    f32x4 cs[4];
    sfor<80>([&](auto I) { constexpr int f = CV(I); mfa<(f < 64), f % 20 == 0>(cs[f / 20], Wt[f], one8); });
    asm volatile("s_nop 7" : "+v"(cs[0]), "+v"(cs[1]), "+v"(cs[2]), "+v"(cs[3]));
    const unsigned ho = EXP((tile0 >> 1) * 1024), l16 = l * 16;
    constexpr int L_ST = 81920;
    const u32x4 ones = {0xFFFFFFFFu, 0xFFFFFFFFu, 0xFFFFFFFFu, 0xFFFFFFFFu};
#pragma unroll
    for (int i = 0; i < 4; ++i) st128(k.rs, l16, k.hseg(i >> 1) + (i & 1) * EXP(49152) + ho, ones);
    unsigned fails = 0;
    const unsigned fl = (w == 0 && l == 0) ? 0u : OOB;
    const unsigned long long m1 = __ballot(n & 1), m2 = __ballot(n & 2);
    bool have = false;
    for (int s = 0; s < 64; ) {
        if (!have) prog_wait<20>(k, k.p0 + CNT(C_X1, 0), s);
        STOP(s) STAMP(s, 0)
        xn_all(k, l16, k.seg(PG_X1, s) + w * 2048, (s & 1) * 40960 + w * 2048);
        const unsigned xb = (s & 1) * 40960 + l16, yb = xb + w * 2048, sp = L_ST + (s & 1) * 2048, rp = sp + n * 16;
        const unsigned dp = sp + (g == (n >> 2) ? n * 16 + w * 4 : 4096 + l * 4);
        unsigned nx;
        f32x4 acc[2][4], st[4];
        bf16x8 x[20], y0[5], y1[5];
        unsigned mx = 0;
        auto blk = [&](auto B, auto J) { constexpr int b = CV(B), j = CV(J);
            if constexpr (j == 0) asm volatile("s_waitcnt vmcnt(%0)\n\ts_barrier" :: "n"(8 - 2 * b) : "memory");
            if constexpr (j >= 1 && j < 5) x[4 * b + j - 1] = lr128(xb + (4 * b + j - 1) * 2048);
            if constexpr (j == 5) y0[b] = lr128(yb + b * 8192);
            if constexpr (j == 6) y1[b] = lr128(yb + b * 8192 + 1024); };
        sfor<7>([&](auto J) { blk(IC(0), J); });
        SCHED0();
        STAMP(s, 1)
        sfor<5>([&](auto B) { constexpr int b = CV(B);
            sfor<20>([&](auto I) { constexpr int i = CV(I), ks = 4 * b + ((i >> 2) & 3), nt = i & 3, f = nt * 20 + ks;
                if constexpr (i < 16) mfa<(f < 64), ks == 0>(acc[0][nt], Wt[f], x[ks]);
                else mfa<false, b == 0>(st[nt], (nt & 1) ? (nt & 2 ? y1[b] : y0[b]) : one8, nt & 2 ? y1[b] : y0[b]);
                SCHED0();
                if constexpr (i >= 8 && i < 15 && b < 4) blk(IC((b + 1) % 5), IC(i - 8));
                if constexpr (i >= 16) { const u32x4 v = __builtin_bit_cast(u32x4, (i & 1) ? y1[b] : y0[b]); mx = umax3(mx, v[i & 2], v[(i & 2) + 1]); PIN(mx); }
                if constexpr (b == 4 && i >= 9 && i < 12) x[i - 9] = lr128(xb + 1024 + (i - 9) * 2048);
                SCHED0(); }); });
        STAMP(s, 2)
        float mean[2], rden[2], G[2], U[2];
        f32x4 sa[2], sb[2];
        u32x4 bad, o[2];
        bool redo;
        auto stat = [&](auto I) { constexpr int i = CV(I), rt = i / 8, j = i % 8;
            if constexpr (j == 0) asm volatile("v_add_f32 %0, %2, %3\n\tv_add_f32 %1, %4, %5" : "=&v"(mean[rt]), "=&v"(rden[rt]) : "v"(sa[rt][0]), "v"(sa[rt][1]), "v"(sb[rt][0]), "v"(sb[rt][1]));
            if constexpr (j == 1 || j == 2) asm volatile("v_add_f32 %0, %0, %2\n\tv_add_f32 %1, %1, %3" : "+v"(mean[rt]), "+v"(rden[rt]) : "v"(sa[rt][j + 1]), "v"(sb[rt][j + 1]));
            if constexpr (j == 3) asm volatile("v_mul_f32 %0, 0xbaccccCD, %0\n\tv_mul_f32 %1, 0x3acccccd, %1" : "+v"(mean[rt]), "+v"(rden[rt]));
            if constexpr (j == 4) asm volatile("v_fma_f32 %1, -%0, %0, %1\n\tv_cvt_pk_bf16_f32 %0, 0, %0\n\tv_max_f32 %1, 0, %1" : "+v"(mean[rt]), "+v"(rden[rt]));
            if constexpr (j == 5) asm volatile("v_cvt_pk_bf16_f32 %0, 0, %0\n\tv_add_f32 %0, 0x3727c5ac, %0\n\tv_cvt_pk_bf16_f32 %0, 0, %0" : "+v"(rden[rt]));
            if constexpr (j == 6) asm volatile("v_sqrt_f32 %0, %0" : "+v"(rden[rt]));
            if constexpr (j == 7) asm volatile("v_cvt_pk_bf16_f32 %0, 0, %0\n\tv_rcp_f32 %0, %0" : "+v"(rden[rt])); };
#define GU "+v"(G[0]), "+v"(G[1]), "+v"(U[0]), "+v"(U[1])
        auto ep = [&](auto I) { constexpr int i = CV(I), q = i / 15, j = i % 15, rt = q >> 2, t = (q >> 1) & 1, c = (q & 1) * 2;
            if constexpr (j == 0) asm volatile("v_fma_f32 %0, %2, %3, %4\n\tv_fma_f32 %1, %2, %5, %6" : "=&v"(G[0]), "=&v"(G[1]) : "v"(mean[rt]), "v"(cs[t][c]), "v"(acc[rt][t][c]), "v"(cs[t][c + 1]), "v"(acc[rt][t][c + 1]));
            if constexpr (j == 1) asm volatile("v_mul_f32 %0, %0, %2\n\tv_mul_f32 %1, %1, %2" : "+v"(G[0]), "+v"(G[1]) : "v"(rden[rt]));
            if constexpr (j == 2) asm volatile("v_cvt_pk_bf16_f32 %0, 0, %0\n\tv_cvt_pk_bf16_f32 %1, 0, %1" : "+v"(G[0]), "+v"(G[1]));
            if constexpr (j == 3) asm volatile("v_mul_f32 %0, 0xbfb8aa3b, %0\n\tv_mul_f32 %1, 0xbfb8aa3b, %1" : "+v"(G[0]), "+v"(G[1]));
            if constexpr (j == 4 || j == 5) asm volatile("v_exp_f32 %0, %0" : "+v"(G[j & 1]));
            if constexpr (j == 6) asm volatile("v_fma_f32 %0, %2, %3, %4\n\tv_fma_f32 %1, %2, %5, %6" : "=&v"(U[0]), "=&v"(U[1]) : "v"(mean[rt]), "v"(cs[2 + t][c]), "v"(acc[rt][2 + t][c]), "v"(cs[2 + t][c + 1]), "v"(acc[rt][2 + t][c + 1]));
            if constexpr (j == 7) asm volatile("v_add_f32 %0, 1.0, %0\n\tv_add_f32 %1, 1.0, %1" : "+v"(G[0]), "+v"(G[1]));
            if constexpr (j == 8 || j == 9) asm volatile("v_rcp_f32 %0, %0" : "+v"(G[j & 1]));
            if constexpr (j == 10) asm volatile("v_mul_f32 %0, %0, %2\n\tv_mul_f32 %1, %1, %2" : "+v"(U[0]), "+v"(U[1]) : "v"(rden[rt]));
            if constexpr (j == 11) asm volatile("v_cvt_pk_bf16_f32 %0, 0, %0\n\tv_cvt_pk_bf16_f32 %1, 0, %1" : "+v"(U[0]), "+v"(U[1]));
            if constexpr (j == 12) asm volatile("v_cvt_pk_bf16_f32 %0, 0, %0\n\tv_cvt_pk_bf16_f32 %1, 0, %1" : "+v"(G[0]), "+v"(G[1]));
            if constexpr (j == 13) asm volatile("v_mul_f32 %0, %0, %2\n\tv_mul_f32 %1, %1, %3" : "+v"(G[0]), "+v"(G[1]) : "v"(U[0]), "v"(U[1]));
            if constexpr (j == 14) { unsigned d; asm volatile("v_cvt_pk_bf16_f32 %0, %1, %2\n\tv_min_u32 %0, -2, %0" : "=&v"(d) : "v"(G[0]), "v"(G[1])); o[rt][q & 3] = d; } };
        sfor<80>([&](auto I) {
            constexpr int i = CV(I), ks = i >> 2, nt = i & 3, f = nt * 20 + ks;
            mfa<(f < 64), ks == 0>(acc[1][nt], Wt[f], x[ks]);
            SCHED0();
            if constexpr (nt == 0 && ks + 3 < 20) x[(ks + 3) % 20] = lr128(xb + 1024 + ((ks + 3) % 20) * 2048);
            if constexpr (i == 1 || i == 3) {
                constexpr int rt2 = i == 3;
                asm volatile("" : "+v"(st[2 * rt2]), "+v"(st[2 * rt2 + 1]));
                const f32x4& q = st[2 * rt2 + 1];
                const float d = csel(csel(q[0], q[1], m1), csel(q[2], q[3], m1), m2);
                lw32(dp + rt2 * 512, __float_as_uint(st[2 * rt2][0])); lw32(dp + rt2 * 512 + 256, __float_as_uint(d));
            }
            if constexpr (i == 2) lw32(sp + 1024 + w * 4, __any(mx == 0xFFFFFFFFu) ? 1u : 0u);
            if constexpr (i == 6) bar_lgkm<1>();
            if constexpr (i == 6) sa[0] = __builtin_bit_cast(f32x4, lr128(rp));
            if constexpr (i == 7) sb[0] = __builtin_bit_cast(f32x4, lr128(rp + 256));
            if constexpr (i == 9) sa[1] = __builtin_bit_cast(f32x4, lr128(rp + 512));
            if constexpr (i == 10) sb[1] = __builtin_bit_cast(f32x4, lr128(rp + 768));
            if constexpr (i == 11) bad = __builtin_bit_cast(u32x4, lr128(sp + 1024));
            if constexpr (i >= 12 && i < 20) stat(IC(i - 12));
            if constexpr (i >= 20) ep(IC(i - 20));
            if constexpr (i >= 21 && i < 53 && nt == 1) stat(IC(8 + (i - 21) / 4));
            if constexpr (i == 54) { redo = __builtin_amdgcn_readfirstlane((bad[0] | bad[1]) | (bad[2] | bad[3])) != 0 && fails++ < 4096u; }
            if constexpr (i == 78) nx = ld32(k.rs, min(l, 19) * 128, k.p0 + CNT(C_X1, 0));
            if constexpr (i == UPOS) st32(k.rs, fl, k.p0 + CNT(C_H, id), (unsigned)(redo ? s - 1 : s));
            SCHED0();
        });
        STAMP(s, 3)
        asm volatile("s_nop 7" : "+v"(acc[1][0]), "+v"(acc[1][1]), "+v"(acc[1][2]), "+v"(acc[1][3]));

#pragma unroll
        for (int q = 0; q < 4; ++q) {
            const int t = q >> 1, c = (q & 1) * 2;
            float hv[2];
#pragma unroll
            for (int i = 0; i < 2; ++i) {
                const float gg = bfr(__builtin_fmaf(mean[1], cs[t][c + i], acc[1][t][c + i]) * rden[1]), u = bfr(__builtin_fmaf(mean[1], cs[2 + t][c + i], acc[1][2 + t][c + i]) * rden[1]);
                hv[i] = bfr(__builtin_amdgcn_rcpf(1.f + __builtin_amdgcn_exp2f(gg * -1.44269504f))) * u;
            }
            o[1][q] = packs(hv[0], hv[1]);
        }
        if (redo) { PCOUNT(0) RAWBAR() continue; }
        st128(k.rs, l16, k.hseg(s) + ho, o[0]); st128(k.rs, l16, k.hseg(s) + EXP(49152) + ho, o[1]);
        st128(k.rs, s < 62 ? l16 : OOB, k.hseg((s + 2) & 63) + ho, ones); st128(k.rs, s < 62 ? l16 : OOB, k.hseg((s + 2) & 63) + EXP(49152) + ho, ones);
        PIN(nx);
        have = __all((int)nx > s);
        __builtin_amdgcn_fence(__ATOMIC_ACQUIRE, "wavefront");
        STAMP(s, 4) STAMP(s, 5)
        PFLUSH(s)
        ++s;
    }
    PROF_OUT
}

DEVI void down_role(const Params& p, const Ctx& k, char* smem, int id) {
    PROF_DECL
    const int l = k.l, w = k.w, g = l >> 4, n = l & 15, rt = id / 5, cg = id % 5;
    bf16x8 Wt[96];
    wstream<12, 8, 4>(smem + w * 32768,
        [&](auto U, unsigned sl) { constexpr int u = decltype(U)::value; sfor<8>([&](auto J) { constexpr int j = CV(J); dma1<(u % 4) * 8192 + j * 1024>(p.wd + (size_t)(w * 12 + u) * 32 * 640 + cg * 128, wsrc<256, false>(j * 64 + l, 1280), sl); }); },
        [&](auto U, char* sp) { constexpr int u = decltype(U)::value;
#pragma unroll
            for (int nt = 0; nt < 8; ++nt) Wt[u * 8 + nt] = wp_read<256, false>(sp, (nt + 2 * w) & 7, l); });
    RAWBAR()
    RSTAMP(2)
    const unsigned xo = EXP((cg * 4 + w) * 2048) + rt * 1024, xv = ((g >> 1) * 16 + n) * 16 + (g & 1) * 8;
    const u32x2 ones = {0xFFFFFFFFu, 0xFFFFFFFFu};
    const unsigned x2v = k.c ? xv : OOB;
#pragma unroll
    for (int i = 0; i < 4; ++i) st64(k.rs, x2v, k.seg(PG_X2, i >> 1) + xo + (i & 1) * 512, ones);
    for (int s = 0; s < 64; ++s) {
        prog_wait<12>(k, k.p0 + CNT(C_H, 0), s);
        STOP(s) STAMP(s, 0)
        f32x4 acc[8];
        u32x2 res[2];
        const unsigned rb = (s & 1) * 32768 + l * 16;
_Pragma("unroll 1")
        for (unsigned spins = 0;; ++spins) {
            u32x4 hx[12];
#pragma unroll
            for (int q = 0; q < 3; ++q) {
                const unsigned a = k.hseg(s) + rt * EXP(49152) + EXP((w * 12 + q * 4) * 1024);
#pragma unroll
                for (int i = 0; i < 4; ++i) hx[q * 4 + i] = ld128(k.rs, l * 16 + i * 1024, a);
            }
#pragma unroll
            for (int t = 0; t < 2; ++t) res[t] = ld64(k.rs, xv + t * 512, k.seg(PG_X1, s) + xo);
            unsigned mx = 0;
            sfor<96>([&](auto I) { constexpr int i = CV(I), ks = i < 72 ? i / 6 : (i - 72) / 2, j = i < 72 ? 2 + i % 6 : i & 1, f = ks * 8 + j;
                mfa<(f < 64), ks == 0>(acc[j], Wt[f], __builtin_bit_cast(bf16x8, hx[ks]));
                SCHED0();
                if constexpr (i < 72 && i % 6 < 2) { mx = umax3(mx, hx[ks][(i & 1) * 2], hx[ks][(i & 1) * 2 + 1]); PIN(mx); }
                if constexpr (i >= 75 && i < 93 && i % 3 == 0) { constexpr int jj = 2 + (i - 75) / 3;
                    asm volatile("" : "+v"(acc[jj])); lw128(rb + ((w + jj / 2) & 3) * 6144 + ((jj / 2 - 1) * 2 + (jj & 1)) * 1024, __builtin_bit_cast(u32x4, acc[jj])); }
                SCHED0(); });
            asm volatile("s_nop 7" : "+v"(acc[0]), "+v"(acc[1]), "+v"(res[0]), "+v"(res[1]));
            if (__all(mx != 0xFFFFFFFFu) || spin_fail(k, spins)) break;
            PCOUNT(0)
            asm volatile("" ::: "memory");
        }
        STAMP(s, 1)
        RAWBAR()
        st32(k.rs, (w == 0 && l == 0) ? 0u : OOB, k.p0 + CNT(C_X2, rt * 5 + cg), (unsigned)s);
#pragma unroll
        for (int t = 0; t < 2; ++t) {
            const unsigned rp = rb + w * 6144 + t * 1024;
            const f32x4 r = (acc[t] + __builtin_bit_cast(f32x4, lr128(rp))) + (__builtin_bit_cast(f32x4, lr128(rp + 2048)) + __builtin_bit_cast(f32x4, lr128(rp + 4096)));
            const unsigned d0 = cvtpk(r[0], r[1]), d1 = cvtpk(r[2], r[3]);
            u32x2 o; o[0] = cvtpk(blo(res[t][0]) + blo(d0), bhi(res[t][0]) + bhi(d0)); o[1] = cvtpk(blo(res[t][1]) + blo(d1), bhi(res[t][1]) + bhi(d1));
            if (!k.c) *(u32x2*)(p.out + (size_t)(s * 32 + rt * 16 + n) * 640 + (cg * 8 + w * 2 + t) * 16 + g * 4) = o;
            else st64(k.rs, xv + t * 512, k.seg(PG_X2, s) + xo, (u32x2){min(o[0], 0xFFFFFFFEu), min(o[1], 0xFFFFFFFEu)});
        }
        STAMP(s, 2)
#pragma unroll
        for (int t = 0; t < 2; ++t) st64(k.rs, s < 62 ? x2v : OOB, k.seg(PG_X2, (s + 2) & 63) + xo + t * 512, ones);
        STAMP(s, 3) STAMP(s, 4) STAMP(s, 5)
        PFLUSH(s)
    }
    PROF_OUT
}

DEVI void warm(const Params& p, const Ctx& k, int xl, int hw) {
    if (k.c) sleep_until(k.t0 + WD1);
    set_m0(k.w * 256);
_Pragma("unroll 1")
    for (int it = hw; it < 24 * 15; it += k.c ? 40 : 64) {
        const int q = it % 24, t = it / 24;
        const u16* b = (q < 8 ? p.Wv : q < 16 ? p.Wk : p.Wq) + (xl * 8 + (q & 7)) * 192;
        const unsigned ln = t * 64 + k.l, cl = min(((unsigned)(size_t)b >> 7) & 7u, 4u);
        if (t < (((q < 8 ? PV : PQ) >> (cl * 4)) & 15)) asm volatile("global_load_lds_dword %0, %1" :: "v"((639 - ln / 3) * 12288 + (ln % 3) * 128), "s"(b) : "memory");
    }
    DRAIN();
_Pragma("unroll 1")
    for (int i = 3; i < 8; ++i) { sleep_until(k.t0 + i * 50); hello(k, 0); }
    DRAIN();
    RAWBAR()
}

DEVI int xcc_id() { return __builtin_amdgcn_s_getreg(20 | (0 << 6) | (3 << 11)) & 7; }

DEVI unsigned prepare(const Params& p, Ctx& k, char* smem, int xl, int i) {
    const int l = k.l, j = xl * 22 + i - 4;
    const u32x4 ones = {0xFFFFFFFFu, 0xFFFFFFFFu, 0xFFFFFFFFu, 0xFFFFFFFFu};
    unsigned vote = 0;
    if (i < 4) {
        const int row = ((i * 4 + xl) * 4 + k.w) * 4 + (l >> 4);
        const unsigned dst = k.seg(PG_XNA, i * 2 + (xl >> 1));
        u32x4 a[5];
#pragma unroll
        for (int c = 0; c < 5; ++c) a[c] = *(const u32x4*)(p.x + (size_t)row * 640 + (c * 16 + (l & 15)) * 8);
        ln_rows(a, l & 15, row & 31, [&](unsigned vo, int c, u32x4 o) { st128(k.rs, vo, dst + c * 16384, o); st128(k.rs, vo, dst + c * 16384 + 8192, o); });
    } else if (k.w == 3) {
        const unsigned cal = ((unsigned)(j % 44) << 21) + 7 * 16384 + (xl >> 1) * 4096 + (l & 7) * 16;
        int d = 0;
#pragma unroll 1
        for (int r = 0; r <= 2 * CALR; ++r) {
            unsigned long long c0, c1;
            st128(k.rs, cal, (r & 1) * 8192, ones);
            st128(k.rs, cal, (~r & 1) * 8192, ones);
            asm volatile("s_waitcnt vmcnt(1)\n\ts_memtime %0\n\ts_waitcnt vmcnt(0)\n\ts_memtime %1\n\ts_waitcnt lgkmcnt(0)" : "=&s"(c0), "=s"(c1) :: "memory");
            d = r ? min((int)(c1 - c0), 400) - d : 0;
        }
        vote = (unsigned)(d < 0) ^ (unsigned)(xcc_id() >> 2) ^ (unsigned)(i & 1);
    } else {
        if (k.w == 2 && j < 20) {
            const int e = j * 32 + (l >> 1), T = e >> 4, hf = (e >> 1) & 1, rr = ((e >> 2) & 3) * 2 + (e & 1);
            u32x4 v;
#pragma unroll
            for (int c = 0; c < 4; ++c) v[c] = min(*(const unsigned*)(p.mem0 + rr * 640 + T * 16 + 4 * c + 2 * hf), 0xFFFFFFFEu);
            st128(k.rs, (2u << 21) + EXP(e * 16) + (l & 1) * 8192, 0, v);
        }
        constexpr unsigned N0 = PR_B + PR_SZ, N1 = 40 * 16384, N2 = 32768;
        static_assert(N0 + N1 + N2 <= 4 * 88 * 3072 && NPAGES <= 44, "fill");
#pragma unroll
        for (int r = 0; r < 4; ++r) {
            const unsigned o = ((r * 88 + j) * 3 + k.w) * 1024;
            st128(k.rs, o < N0 + N1 + N2 ? l * 16 : OOB, o < N0 ? o : o < N0 + N1 ? ((unsigned)PG_YM << 21) + (o - N0) : (2u << 21) + 32768 + (o - N0 - N1), ones);
        }
    }
    if (k.w == 3) *(volatile LAS unsigned*)smem = vote;
    DRAIN();
    __builtin_amdgcn_s_waitcnt(0x0f70);
    XSTAMP(0)
    RAWBAR()
    return *(volatile LAS unsigned*)smem;
}
DEVI void master(const Params& p, const Ctx& k) {
    const int l = k.l;
    u32x2 v[4] = {{0u, 0u}, {0u, 0u}, {0u, 0u}, {0u, 0u}};
_Pragma("unroll 1")
    for (unsigned spins = 0; spins < SPIN_CAP; ++spins) {
#pragma unroll
        for (int q = 0; q < 4; ++q) v[q] |= cm_ld(k, (CM_IN + (q * 64 + l) * 128) | v[q][1]);
        if (__all((v[0][1] & v[1][1] & v[2][1] & v[3][1]) != 0)) break;
        asm volatile("" ::: "memory");
    }
    const unsigned long long a = __ballot(v[2][0] != 0), b = __ballot(v[3][0] != 0);
    const int n = __popcll(a) + __popcll(b);
    const unsigned long long g = n > 44 ? ~0ull : 0ull, u = (a & b) | ((a ^ b) & g);
    const unsigned long long t = (__popcll(a ^ b) > 2 || n > 80 || n < 8 ? g : ((u >> 4) & 0x3FFFFF) | ((u >> 36) << 22)) ^ 0xAAAAAAAAAAAAAAAAull;
#pragma unroll
    for (int q = 0; q < 4; ++q) cm_st(k, l * 128, CM_GO + q * 8192, (unsigned)t, (unsigned)(t >> 32) | 0x80000000u);
}

#define FIN(n) { __builtin_amdgcn_s_waitcnt(0x0f70); asm volatile("s_nop " #n); return; }
__global__ __launch_bounds__(256, 1) void mega_kernel(Params p) {
    extern __shared__ __attribute__((aligned(16))) char smem[];
    Ctx k;
    const u16 *wq = p.Wq, *wk = p.Wk, *wv = p.Wv;
    asm volatile("" : "+s"(wq), "+s"(wk), "+s"(wv) : "s"(p.cm));
    k.ws = p.ws; k.cm = p.cm;
    k.rs = __builtin_amdgcn_make_buffer_rsrc((void*)p.ws, 0, 0x7fffffff, 0x00020000);
    k.l = threadIdx.x & 63; k.w = __builtin_amdgcn_readfirstlane(threadIdx.x >> 6);
    RSTAMP(0)
    const int x = blockIdx.x & 7, i = blockIdx.x >> 3, xl = x & 3;
    k.role = x * 32 + i; k.c = x >> 2; k.par = 0; k.p0 = 0; k.t0 = rclk();
    line_st(k, CM_GO, 0, 0);
    auto start = [&]() {
        const u16* a = (i < 8 ? wk : wv) + (xl * 8 + (i & 7)) * 192, * b = (i < 8 ? wq : wv + 32 * 6144) + (xl * 8 + (i & 7)) * 192;
        unsigned o[3];
        sfor<18>([&](auto J) { constexpr int j = CV(J);
            if constexpr (j < 3) o[j] = wsrc<384, true>(k.w * 192 + j * 64 + k.l, 12288);
            dma1<SLOT(j / 6) + ((j / 3) & 1) * 12288 + (j % 3) * 1024>((((j / 3) & 1) ? b : a) + (j / 6) * (i < 8 ? 32 * 6144 : 64 * 6144), o[j % 3], k.w * 3072); }); };
    if (!k.c && i < 16) start();
    if (__builtin_expect(k.role == MASTER, 0)) {
#pragma unroll
        for (int q = 0; q < 16; ++q) cm_st(k, k.l * 8, CM_IN + k.w * 8192 + q * 512, 0, 0);
        cm_st(k, 0, CM_TMO, ~0u, ~0u);
        DRAIN();
        RAWBAR()
    }
    if (__builtin_expect(k.c && i < 26, 0)) { const unsigned v = prepare(p, k, smem, xl, i); RSTAMP(1) hello(k, v); go_wait(k, v); XSTAMP(1) RAWBAR() }
    else { RSTAMP(1) if (k.c) { DRAIN(); hello(k, 0); } }
    if (__builtin_expect(k.role == MASTER && k.w == 0, 0)) { master(p, k); __builtin_amdgcn_s_waitcnt(0x0f70); }
    if (k.c && i < 16) { sleep_until(k.t0 + WD1); start(); }
    if (i < 8) { k.l = lane(); qk_role(p, k, smem, xl * 8 + i); FIN(0) }
    if (i < 16) { k.l = lane(); vo_role(p, k, smem, xl * 8 + i - 8); FIN(1) }
    k.l = lane();
    if (!k.c || i < 26) warm(p, k, xl, (i - 16) * 4 + k.w);
    else RAWBAR()
    go_wait(k, 0);
    if (i >= 26) sleep_until(k.t0 + (k.c ? GST : GSA) + ((xl & 1) == UPODD ? 0 : i < 31 ? GSD : -GSA));
    if (i < 21) { mred_role(p, k, smem, xl * 5 + i - 16); FIN(2) }
    if (i < 26) { tred_role(p, k, smem, xl * 5 + i - 21); FIN(3) }
    if ((xl & 1) == UPODD) { up_role(p, k, smem, (xl >> 1) * 6 + i - 26); FIN(4) }
    if (i < 31) { down_role(p, k, smem, (xl >> 1) * 5 + i - 26); FIN(5) }
    row_role(p, k, smem, xl >> 1);
    FIN(6)
}

extern "C" void kernel_launch(void* const* d_in, const int* in_sizes, int n_in,
                              void* d_out, int out_size, void* d_ws, size_t ws_size,
                              hipStream_t stream) {
    Params p;
    p.x = (const u16*)d_in[0]; p.mem0 = (const u16*)d_in[1];
    p.Wq = (const u16*)d_in[2]; p.Wk = (const u16*)d_in[3]; p.Wv = (const u16*)d_in[4]; p.Wo = (const u16*)d_in[5];
    p.wg = (const u16*)d_in[8]; p.wu = (const u16*)d_in[9]; p.wd = (const u16*)d_in[10];
    p.out = (u16*)d_out; p.cm = (char*)d_ws;
    p.ws = (char*)(((uintptr_t)d_ws + CM_BYTES + ((1u << 21) - 1)) & ~(uintptr_t)((1u << 21) - 1));
    (void)hipFuncSetAttribute((const void*)mega_kernel, hipFuncAttributeMaxDynamicSharedMemorySize, LDS_BYTES);
    mega_kernel<<<256, 256, LDS_BYTES, stream>>>(p);
}
```

```cpp
#define SWAPG
#include <hip/hip_runtime.h>
#include <hip/hip_bf16.h>
#include <stdint.h>
#include <utility>

typedef unsigned short u16;
typedef __attribute__((ext_vector_type(16))) short bf16x16;
typedef __attribute__((ext_vector_type(8))) short bf16x8;
typedef __attribute__((ext_vector_type(4))) short bf16x4;
typedef __attribute__((ext_vector_type(8))) __bf16 hbf8;
typedef __attribute__((ext_vector_type(16))) __bf16 hbf16;
typedef __attribute__((ext_vector_type(4))) float f32x4;
typedef __attribute__((ext_vector_type(2))) float f32x2;
typedef __attribute__((ext_vector_type(8))) unsigned u32x8;
typedef __attribute__((ext_vector_type(4))) unsigned u32x4;
typedef __attribute__((ext_vector_type(2))) unsigned u32x2;
typedef __attribute__((ext_vector_type(2))) __bf16 bf16x2_t;
#define LAS __attribute__((address_space(3)))
#define GAS __attribute__((address_space(1)))
#define DEVI __device__ __forceinline__

#define PSLEEP 8
#define WD1 1000
#define GST 550
#define NTA " nt"
#define NTB ""
#define PQ 0x077A0
#define PV 0x4AAE4
#define GSA 1600
#define QGS 8
#define VGS 8
#define MN 480
#define MD 250
#define MP 100
#define RSL 1000
#define GSD 250
#define PO 0
#define PDLY 0
#define WP 600
#define PP 100
#define STG 0
#define UPOS0 88
#define DPOS 88
#define XPF 1400
#define TPOS 33
#define XS0 40
#define XP 29
#define XB 66
#define TB 107
#define PACE 65
#define XPOS 74
#define YPOS 40
#define XPOSY 1
#define PWS 1
#define WUS 0
#define WMIX 1
#define PACE2 30
#define GDLY 80
#define QDLY 0
#define CALR 1
#define SPG 3
#define DQ 16
#define DV 12
#define BSLP 1
#define SDLY 0
#define UPOS 150
#define RDLY 45
#define NOYST 0
#define NOTOK 0
#define NOYLD 0
#define GPOSY 8
#define OLDMAP 1
#define UPODD 0
#define XLD 10
#define TROT 8
#define KOUT 1
#define RPOS 75
#define RDY 90
#define APOS 6
#define GPOS 74

#define EXP(o) ((((unsigned)(o) >> 13) << 14) | ((unsigned)(o) & 8191u))
enum { C_XN = 0, C_Y = 1, C_X1 = 2, C_XB = 3, C_H = 4, C_X2 = 5, C_BC = 6 };
#define CNT(type, s) ((unsigned)((type) * 16384 + (s) * 128))
constexpr unsigned PR_B = EXP(65536), PR_SZ = EXP(12 * 8192);
constexpr unsigned DUMMY_B = EXP(0);
constexpr int PG_MEMF = 2;
#define YXR 8
constexpr int PG_YX = 3;
constexpr int PG_XNA = PG_YX + 2 * YXR;
constexpr int PG_X1 = PG_XNA + 4, PG_X2 = PG_X1 + 4;
constexpr int PG_H = PG_X2 + 4;
constexpr int PG_YM = PG_H + 8;
constexpr int NPAGES = PG_YM + 2;
static_assert(NPAGES <= 64, "map");
constexpr unsigned CM_TAB = 0, CM_TICKET = 1024, CM_TMO = 4096, CM_IN = 8192, CM_GO = 40960, CM_BYTES = 73728;
constexpr int MASTER = 5 * 32 + 31;
constexpr unsigned SPIN_CAP = 1u << 20;
#define AUX_SC1 16
#define LDS_BYTES (160 * 1024)

struct Params {
    const u16* x; const u16* mem0; const u16* Wq; const u16* Wk; const u16* Wv; const u16* Wo;
    const u16* wg; const u16* wu; const u16* wd; u16* out; char* cm; char* ws;
};
struct Ctx {
    char* ws; char* cm; __amdgpu_buffer_rsrc_t rs; unsigned long long par;
    int c, l, w, role;
    unsigned p0, t0;
    DEVI unsigned pg(int page) const { return ((unsigned)page << 21) | (((unsigned)(par >> page) & 1u) << 13); }
    DEVI unsigned pg0(int page) const { return pg(page) ^ ((unsigned)c << 13); }
    DEVI unsigned seg(int page, int s) const { return pg(page + (s >> 4)) + (unsigned)(s & 15) * 131072u; }
    DEVI unsigned seg0(int page, int s) const { return seg(page, s) ^ ((unsigned)c << 13); }
    DEVI unsigned segx(int page, int s) const { return seg(page, s) ^ 8192u; }
    DEVI unsigned hseg(int s) const { return pg(PG_H + (s >> 3)) + (unsigned)(s & 7) * 262144u; }
    DEVI unsigned yx(int sg, int wq) const { return pg(PG_YX + (sg % YXR) * 2 + (wq >> 1)) + (unsigned)(wq & 1) * 1048576u; }
    DEVI unsigned ym(int s) const { return pg(PG_YM + ((s >> 1) & 1)) + (unsigned)(s & 1) * EXP(327680); }
    DEVI unsigned memf(int s) const { return pg(PG_MEMF) + (unsigned)s * 32768u; }
    DEVI char* cnt(int type, int s) const { return ws + (p0 + CNT(type, s)); }
    DEVI char* cnt0(int type, int s) const { return ws + ((p0 ^ ((unsigned)c << 13)) + CNT(type, s)); }
};

DEVI unsigned cvtpk(float a, float b) { return __builtin_bit_cast(unsigned, __builtin_convertvector((f32x2){a, b}, bf16x2_t)); }
DEVI unsigned packs(float a, float b) { return min(cvtpk(a, b), 0xFFFFFFFEu); }
DEVI float blo(unsigned x) { return __uint_as_float(x << 16); }
DEVI float bhi(unsigned x) { return __uint_as_float(x & 0xFFFF0000u); }
DEVI float bfr(float f) { return __uint_as_float(cvtpk(0.f, f)); }
DEVI u32x4 ld128(__amdgpu_buffer_rsrc_t rs, unsigned v, unsigned s) { return __builtin_amdgcn_raw_buffer_load_b128(rs, (int)v, (int)s, AUX_SC1); }
DEVI u32x2 ld64(__amdgpu_buffer_rsrc_t rs, unsigned v, unsigned s) { return __builtin_amdgcn_raw_buffer_load_b64(rs, (int)v, (int)s, AUX_SC1); }
DEVI unsigned ld32(__amdgpu_buffer_rsrc_t rs, unsigned v, unsigned s) { return __builtin_amdgcn_raw_buffer_load_b32(rs, (int)v, (int)s, AUX_SC1); }
DEVI void st128(__amdgpu_buffer_rsrc_t rs, unsigned v, unsigned s, u32x4 x) {
    __builtin_amdgcn_raw_buffer_store_b128(x, rs, (int)v, (int)s, AUX_SC1);
    asm volatile("s_nop 1" :: "v"(x) : "memory");
}
DEVI void st64(__amdgpu_buffer_rsrc_t rs, unsigned v, unsigned s, u32x2 x) { __builtin_amdgcn_raw_buffer_store_b64(x, rs, (int)v, (int)s, AUX_SC1); }
DEVI void st32(__amdgpu_buffer_rsrc_t rs, unsigned v, unsigned s, unsigned x) { __builtin_amdgcn_raw_buffer_store_b32(x, rs, (int)v, (int)s, AUX_SC1); }
#define RAWBAR() { __builtin_amdgcn_s_waitcnt(0xc07f); __builtin_amdgcn_s_barrier(); }
#define BARL(n) { asm volatile("" ::: "memory"); __builtin_amdgcn_s_waitcnt(0xc07f | ((n) << 8)); __builtin_amdgcn_s_barrier(); asm volatile("" ::: "memory"); }
#define DRAIN() asm volatile("s_waitcnt vmcnt(0)" ::: "memory")
#define SCHED0() __builtin_amdgcn_sched_barrier(0)
#define OOB 0x80000000u
DEVI int opq(int x) { asm volatile("" : "+v"(x)); return x; }
#define LANE const int l = opq(k.l), g = l >> 4, n = l & 15; (void)g; (void)n;
DEVI unsigned umax3(unsigned a, unsigned b, unsigned c) { return max(max(a, b), c); }
DEVI unsigned umax4(u32x4 v) { return max(umax3(v[0], v[1], v[2]), v[3]); }
DEVI unsigned rclk() { return (unsigned)__builtin_amdgcn_s_memrealtime(); }
DEVI unsigned long long rt_async() { unsigned long long t; asm volatile("s_memrealtime %0" : "=s"(t)); return t; }
DEVI void lgkm0(unsigned long long& a) { asm volatile("s_waitcnt lgkmcnt(0)" : "+s"(a) :: "memory"); }
DEVI unsigned spack(unsigned lo, unsigned hi) { unsigned d; asm volatile("s_pack_ll_b32_b16 %0, %1, %2" : "=s"(d) : "s"(lo), "s"(hi)); return d; }
DEVI int lane() { int l; asm volatile("v_mbcnt_lo_u32_b32 %0, -1, 0\n\tv_mbcnt_hi_u32_b32 %0, -1, %0" : "=v"(l)); __builtin_assume(l >= 0 && l < 64); return l; }
DEVI void sleep_until(unsigned target) { while ((int)(rclk() - target) < 0) __builtin_amdgcn_s_sleep(4); }
DEVI float xor16max(float x) { auto r = __builtin_amdgcn_permlane16_swap(__float_as_uint(x), __float_as_uint(x), false, false); return __builtin_fmaxf(__uint_as_float(r[0]), __uint_as_float(r[1])); }
DEVI float xor32max(float x) { auto r = __builtin_amdgcn_permlane32_swap(__float_as_uint(x), __float_as_uint(x), false, false); return __builtin_fmaxf(__uint_as_float(r[0]), __uint_as_float(r[1])); }
DEVI float xor16add(float x) { auto r = __builtin_amdgcn_permlane16_swap(__float_as_uint(x), __float_as_uint(x), false, false); return __uint_as_float(r[0]) + __uint_as_float(r[1]); }
DEVI float xor32add(float x) { auto r = __builtin_amdgcn_permlane32_swap(__float_as_uint(x), __float_as_uint(x), false, false); return __uint_as_float(r[0]) + __uint_as_float(r[1]); }
DEVI float fold32(float a, float b) { auto r = __builtin_amdgcn_permlane32_swap(__float_as_uint(a), __float_as_uint(b), false, false); return __uint_as_float(r[0]) + __uint_as_float(r[1]); }
#define ROR16(x, n) __builtin_bit_cast(float, __builtin_amdgcn_update_dpp(0, __builtin_bit_cast(int, (x)), 0x120 + (n), 0xf, 0xf, false))
DEVI float row16sum(float x) { x += ROR16(x, 8); x += ROR16(x, 4); x += ROR16(x, 2); x += ROR16(x, 1); return x; }
DEVI unsigned z0() { unsigned z; asm volatile("v_mov_b32 %0, 0" : "=v"(z)); return z; }
DEVI void cnt_add(char* p) { __hip_atomic_fetch_add((GAS unsigned*)(p + z0()), z0() + 1u, __ATOMIC_RELAXED, __HIP_MEMORY_SCOPE_AGENT); }
DEVI unsigned cnt_get(char* p) { return __hip_atomic_load((GAS unsigned*)(p + z0()), __ATOMIC_RELAXED, __HIP_MEMORY_SCOPE_AGENT) + 1u; }
DEVI bool spin_fail(const Ctx& k, unsigned spins) {
    if (spins > SPIN_CAP || ((spins & 255u) == 255u && __builtin_amdgcn_readfirstlane(cnt_get(k.cm + CM_TMO)) != 0)) { cnt_add(k.cm + CM_TMO); return true; }
    return false;
}
DEVI __amdgpu_buffer_rsrc_t cm_rs(const Ctx& k) { return __builtin_amdgcn_make_buffer_rsrc((void*)k.cm, 0, CM_BYTES, 0x00020000); }
DEVI void cm_st(const Ctx& k, unsigned v, unsigned off, unsigned lo, unsigned hi) { asm volatile("buffer_store_dwordx2 %0, %1, %2, %3 offen sc1" :: "v"((u32x2){lo, hi}), "v"(v), "s"(cm_rs(k)), "s"(off) : "memory"); }
DEVI u32x2 cm_ld(const Ctx& k, unsigned off) { return ld64(cm_rs(k), off, 0); }
DEVI void line_st(const Ctx& k, unsigned off, unsigned lo, unsigned hi) { cm_st(k, (lane() & 15) * 8 + (k.w ? OOB : 0u), off + k.role * 128, lo, hi); }
DEVI void hello(const Ctx& k, unsigned vote) { line_st(k, CM_IN, vote, OOB); }
DEVI u32x2 go_ask(const Ctx& k) { u32x2 d; asm volatile("buffer_load_dwordx2 %0, off, %1, %2 sc1" : "=v"(d) : "s"(cm_rs(k)), "s"(CM_GO + k.role * 128) : "memory"); return d; }
DEVI void go_wait(Ctx& k, unsigned vote, u32x2 v = {0u, 0u}) {
_Pragma("unroll 1")
    for (unsigned spins = 0; __builtin_amdgcn_readfirstlane(v[1]) >= 0 && spins < SPIN_CAP; ++spins) {
        if (spins) { hello(k, vote); __builtin_amdgcn_s_sleep(4); }
        asm volatile("buffer_load_dwordx2 %0, off, %1, %2 sc1\n\ts_waitcnt vmcnt(0)" : "=v"(v) : "s"(cm_rs(k)), "s"(CM_GO + k.role * 128) : "memory");
    }
    k.par = ((unsigned long long)(unsigned)__builtin_amdgcn_readfirstlane(v[1]) << 32 | (unsigned)__builtin_amdgcn_readfirstlane(v[0])) ^ (k.c ? ~0ull : 0ull);
    k.p0 = k.pg(0);
    __builtin_amdgcn_fence(__ATOMIC_ACQUIRE, "wavefront");
}
DEVI void cnt_wait(const Ctx& k, char* c, unsigned need) {
_Pragma("unroll 1")
    for (unsigned spins = 0; (unsigned)__builtin_amdgcn_readfirstlane(cnt_get(c)) < need; ++spins) {
        __builtin_amdgcn_s_sleep(PSLEEP);
        if (spin_fail(k, spins)) break;
    }
    __builtin_amdgcn_fence(__ATOMIC_ACQUIRE, "wavefront");
}
DEVI unsigned xn_get(const Ctx& k, unsigned l16, int s) { return ld32(k.rs, opq(l16) & 0x380u, k.p0 + CNT(C_XN, 0)); }
DEVI bool xn_has(const Ctx& k, unsigned v, int s) { return __all((int)v >= s - (k.c ? 0 : 8)); }
DEVI void xn_wait(const Ctx& k, unsigned l16, int s) {
_Pragma("unroll 1")
    for (unsigned spins = 0; !xn_has(k, xn_get(k, l16, s), s); ++spins) {
        asm volatile("" ::: "memory");
        __builtin_amdgcn_s_sleep(2);
        if (spin_fail(k, spins)) break;
    }
    __builtin_amdgcn_fence(__ATOMIC_ACQUIRE, "wavefront");
}
constexpr unsigned L_PW = 150 * 1024;
DEVI void prog_init(const Ctx& k) { *(volatile LAS unsigned*)(size_t)(L_PW + k.l * 4) = ~0u; RAWBAR() }
template <int N> DEVI void prog_wait(const Ctx& k, unsigned a, int s) {
    unsigned n, t;
    asm volatile("s_mov_b32 m0, %8\n\ts_mov_b32 %0, 0\n\ts_branch 2f\n"
        "1:\n\ts_cmp_lg_u32 %6, 0\n\ts_cbranch_scc1 3f\n\tbuffer_load_dword %2, %3, %4 offen sc1 lds\n"
        "3:\n\ts_sleep %9\n\ts_add_u32 %0, %0, 1\n\ts_and_b32 vcc_lo, %0, 0x7ff\n\ts_cbranch_scc1 2f\n\t"
        "v_mov_b32 %1, 0\n\ts_bitcmp1_b32 %0, 23\n\ts_cbranch_scc0 5f\n\tglobal_store_dword %1, %1, %10 sc1\n"
        "5:\n\tglobal_load_dword %1, %1, %10 sc1\n\ts_waitcnt vmcnt(0)\n\tv_cmp_ne_u32 vcc, -1, %1\n\ts_cbranch_vccnz 4f\n"
        "2:\n\tds_read_b32 %1, %7\n\ts_waitcnt lgkmcnt(0)\n\tv_cmp_gt_i32 vcc, %5, %1\n\ts_cbranch_vccnz 1b\n4:"
        : "=&s"(n), "=&v"(t) : "v"(min(k.l, N - 1) * 128), "s"(k.rs), "s"(a), "s"(s), "s"(k.w), "v"(L_PW + k.l * 4), "n"(L_PW), "n"(PWS), "s"(k.cm + CM_TMO) : "memory", "vcc", "scc");
}
template <int OFF> DEVI void dma1(const void* sbase, unsigned voff, unsigned lds) {
    asm volatile("s_add_i32 m0, %2, %3\n\ts_nop 0\n\tglobal_load_lds_dwordx4 %0, %1" NTA :: "v"(voff), "s"(sbase), "s"(lds), "n"(OFF) : "memory", "scc");
}
DEVI void set_m0(unsigned lds) { asm volatile("s_mov_b32 m0, %0" :: "s"(lds) : "memory"); }
template <int OFF> DEVI void dmab(__amdgpu_buffer_rsrc_t rs, unsigned voff, unsigned soff) {
    asm volatile("buffer_load_dwordx4 %0, %1, %2 offen offset:%3 sc1 lds" :: "v"(voff), "s"(rs), "s"(soff), "n"(OFF) : "memory");
}
#define PIN(x) asm volatile("" : "+v"(x))
#define PINS(x) asm volatile("" : "+s"(x))
#define LEAD()
#define TOUCH(x) asm volatile("" :: "v"(x))
DEVI float csel(float a, float b, unsigned long long m) { float d; asm volatile("v_cndmask_b32_e64 %0, %1, %2, %3" : "=v"(d) : "v"(a), "v"(b), "s"(m)); return d; }
#define LO8(x) __builtin_shufflevector(x, x, 0, 1, 2, 3, 4, 5, 6, 7)
#define HI8(x) __builtin_shufflevector(x, x, 8, 9, 10, 11, 12, 13, 14, 15)
#define CAT8(a, b) __builtin_shufflevector(a, b, 0, 1, 2, 3, 4, 5, 6, 7, 8, 9, 10, 11, 12, 13, 14, 15)
template <bool AG> DEVI void smf(f32x4& c, const bf16x8& a, const bf16x16& w, unsigned ix) {
    if constexpr (AG) asm volatile("v_smfmac_f32_16x16x64_bf16 %0, %1, %2, %3" : "+v"(c) : "v"(a), "a"(w), "v"(ix));
    else asm volatile("v_smfmac_f32_16x16x64_bf16 %0, %1, %2, %3" : "+v"(c) : "v"(a), "v"(w), "v"(ix));
}
#define WTAB(X) X(0, "{a[0:7]}", "{a[0:3]}", "{a[4:7]}") X(1, "{a[8:15]}", "{a[8:11]}", "{a[12:15]}") X(2, "{a[16:23]}", "{a[16:19]}", "{a[20:23]}") X(3, "{a[24:31]}", "{a[24:27]}", "{a[28:31]}") X(4, "{a[32:39]}", "{a[32:35]}", "{a[36:39]}") X(5, "{a[40:47]}", "{a[40:43]}", "{a[44:47]}") X(6, "{a[48:55]}", "{a[48:51]}", "{a[52:55]}") X(7, "{a[56:63]}", "{a[56:59]}", "{a[60:63]}") X(8, "{a[64:71]}", "{a[64:67]}", "{a[68:71]}") X(9, "{a[72:79]}", "{a[72:75]}", "{a[76:79]}") X(10, "{a[80:87]}", "{a[80:83]}", "{a[84:87]}") X(11, "{a[88:95]}", "{a[88:91]}", "{a[92:95]}") X(12, "{a[96:103]}", "{a[96:99]}", "{a[100:103]}") X(13, "{a[104:111]}", "{a[104:107]}", "{a[108:111]}") X(14, "{a[112:119]}", "{a[112:115]}", "{a[116:119]}") X(15, "{a[120:127]}", "{a[120:123]}", "{a[124:127]}") X(16, "{a[128:135]}", "{a[128:131]}", "{a[132:135]}") X(17, "{a[136:143]}", "{a[136:139]}", "{a[140:143]}") X(18, "{a[144:151]}", "{a[144:147]}", "{a[148:151]}") X(19, "{a[152:159]}", "{a[152:155]}", "{a[156:159]}") X(20, "{a[160:167]}", "{a[160:163]}", "{a[164:167]}") X(21, "{a[168:175]}", "{a[168:171]}", "{a[172:175]}") X(22, "{a[176:183]}", "{a[176:179]}", "{a[180:183]}") X(23, "{a[184:191]}", "{a[184:187]}", "{a[188:191]}") X(24, "{a[192:199]}", "{a[192:195]}", "{a[196:199]}") X(25, "{a[200:207]}", "{a[200:203]}", "{a[204:207]}") X(26, "{a[208:215]}", "{a[208:211]}", "{a[212:215]}") X(27, "{a[216:223]}", "{a[216:219]}", "{a[220:223]}") X(28, "{a[224:231]}", "{a[224:227]}", "{a[228:231]}") X(29, "{a[232:239]}", "{a[232:235]}", "{a[236:239]}") X(30, "{a[240:247]}", "{a[240:243]}", "{a[244:247]}") X(31, "{a[248:255]}", "{a[248:251]}", "{a[252:255]}") X(32, "{v[96:103]}", "{v[96:99]}", "{v[100:103]}") X(33, "{v[104:111]}", "{v[104:107]}", "{v[108:111]}") X(34, "{v[112:119]}", "{v[112:115]}", "{v[116:119]}") X(35, "{v[120:127]}", "{v[120:123]}", "{v[124:127]}") X(36, "{v[128:135]}", "{v[128:131]}", "{v[132:135]}") X(37, "{v[136:143]}", "{v[136:139]}", "{v[140:143]}") X(38, "{v[144:151]}", "{v[144:147]}", "{v[148:151]}") X(39, "{v[152:159]}", "{v[152:155]}", "{v[156:159]}") X(40, "{v[160:167]}", "{v[160:163]}", "{v[164:167]}") X(41, "{v[168:175]}", "{v[168:171]}", "{v[172:175]}") X(42, "{v[176:183]}", "{v[176:179]}", "{v[180:183]}") X(43, "{v[184:191]}", "{v[184:187]}", "{v[188:191]}") X(44, "{v[192:199]}", "{v[192:195]}", "{v[196:199]}") X(45, "{v[200:207]}", "{v[200:203]}", "{v[204:207]}") X(46, "{v[208:215]}", "{v[208:211]}", "{v[212:215]}") X(47, "{v[216:223]}", "{v[216:219]}", "{v[220:223]}") X(48, "{v[224:231]}", "{v[224:227]}", "{v[228:231]}") X(49, "{v[232:239]}", "{v[232:235]}", "{v[236:239]}") X(50, "{v[240:247]}", "{v[240:243]}", "{v[244:247]}") X(51, "{v[248:255]}", "{v[248:251]}", "{v[252:255]}")
template <int ID> DEVI void smfw(f32x4& c, const bf16x8& a, const bf16x16& w, unsigned ix) {
#define X(i, P, L, H) if constexpr (ID == i) asm volatile("v_smfmac_f32_16x16x64_bf16 %0, %1, %2, %3" : "+v"(c) : "v"(a), P(w), "v"(ix));
    WTAB(X)
#undef X
}
template <int ID, int HF, bool WB, bool FIRST> DEVI void mfw(f32x4& c, const bf16x16& w, const bf16x8& x) {
#define Y(R, W8) { if constexpr (FIRST && WB) asm volatile("v_mfma_f32_16x16x32_bf16 %0, %2, %1, 0" : "=&v"(c) : R(W8), "v"(x)); \
    else if constexpr (FIRST) asm volatile("v_mfma_f32_16x16x32_bf16 %0, %1, %2, 0" : "=&v"(c) : R(W8), "v"(x)); \
    else if constexpr (WB) asm volatile("v_mfma_f32_16x16x32_bf16 %0, %2, %1, %0" : "+v"(c) : R(W8), "v"(x)); \
    else asm volatile("v_mfma_f32_16x16x32_bf16 %0, %1, %2, %0" : "+v"(c) : R(W8), "v"(x)); }
#define X(i, P, L, H) if constexpr (ID == i) { if constexpr (HF) Y(H, HI8(w)) else Y(L, LO8(w)) }
    WTAB(X)
#undef X
#undef Y
}
template <bool AG, bool FIRST> DEVI void mfa(f32x4& c, const bf16x8& w, const bf16x8& x) {
    if constexpr (AG && FIRST) asm volatile("v_mfma_f32_16x16x32_bf16 %0, %1, %2, 0" : "=&v"(c) : "a"(w), "v"(x));
    else if constexpr (AG) asm volatile("v_mfma_f32_16x16x32_bf16 %0, %1, %2, %0" : "+v"(c) : "a"(w), "v"(x));
    else if constexpr (FIRST) asm volatile("v_mfma_f32_16x16x32_bf16 %0, %1, %2, 0" : "=&v"(c) : "v"(w), "v"(x));
    else asm volatile("v_mfma_f32_16x16x32_bf16 %0, %1, %2, %0" : "+v"(c) : "v"(w), "v"(x));
}
template <bool AG, bool FIRST> DEVI void mfb(f32x4& c, const bf16x8& x, const bf16x8& w) {
    if constexpr (AG && FIRST) asm volatile("v_mfma_f32_16x16x32_bf16 %0, %1, %2, 0" : "=&v"(c) : "v"(x), "a"(w));
    else if constexpr (AG) asm volatile("v_mfma_f32_16x16x32_bf16 %0, %1, %2, %0" : "+v"(c) : "v"(x), "a"(w));
    else if constexpr (FIRST) asm volatile("v_mfma_f32_16x16x32_bf16 %0, %1, %2, 0" : "=&v"(c) : "v"(x), "v"(w));
    else asm volatile("v_mfma_f32_16x16x32_bf16 %0, %1, %2, %0" : "+v"(c) : "v"(x), "v"(w));
}
DEVI f32x4 mf(const bf16x8& a, const bf16x8& b, const f32x4& c) { return __builtin_amdgcn_mfma_f32_16x16x32_bf16(__builtin_bit_cast(hbf8, a), __builtin_bit_cast(hbf8, b), c, 0, 0, 0); }
template <int N, class F> DEVI void sfor(F f) { [&]<int... I>(std::integer_sequence<int, I...>) { (f(std::integral_constant<int, I>{}), ...); }(std::make_integer_sequence<int, N>{}); }
DEVI bf16x8 lds128(const char* p) { return *(const bf16x8*)p; }
DEVI bf16x4 ldstr(const char* p) { return __builtin_amdgcn_ds_read_tr16_b64_v4i16((LAS bf16x4*)p); }
DEVI bf16x8 ldstr2(const char* p, int d) { bf16x4 a = ldstr(p), b = ldstr(p + d); return __builtin_shufflevector(a, b, 0, 1, 2, 3, 4, 5, 6, 7); }

#define STAMP(s, k)
#define QSTAMP(f, i)
#define STOP(s)
#define PFLUSH(s)
#define PROF_DECL
#define RSTAMP(i)
#define XSTAMP(i)
#define PCOUNT(i)
#define PROF_OUT

#define HSTAMP STAMP
#define HFLUSH PFLUSH
#define HTRACE(s)
#define TROUT
#define TRHINT
#define AQ(m, i)
#define AC3(i, j, k) { }
#define AST(i)
#define AAC(i)
#define ACCM 0
#define HSTOP(m) if constexpr ((ACCM != 0) == m) { STOP(0) }
#define PC8(i, x)
#define PROF_ONLY(x)
#define PROF_S(x)
template <int RB, bool STD> DEVI int swz(int row) { return RB == 384 ? ((row >> 1) & 1) | ((row >> 2) & 2) : RB == 128 ? (row >> 1) & 3 : STD ? (row & 3) | ((row >> 1) & 4) : row & 7; }
template <int RB, bool STD> DEVI unsigned wsrc(unsigned id, int ld) { const unsigned row = id / (RB / 16), c = id % (RB / 16); return row * ld + ((c ^ (swz<RB, STD>(row) << 1)) << 4); }
template <int RB, bool STD> DEVI int wp_adr(int T, int l) { const int row = (STD ? 8 : 4) * (l >> 4) + ((l >> 2) & 3); return row * RB + ((T ^ swz<RB, STD>(row)) << 5) + (l & 3) * 8; }
template <int RB, bool STD> DEVI bf16x8 wp_read(const char* blk, int T, int l) { return ldstr2(blk + wp_adr<RB, STD>(T, l), (STD ? 4 : 16) * RB); }
template <int NU, class CNT, class STEP> DEVI void cstream(CNT cnt, STEP step) {
    sfor<NU>([&](auto V) { constexpr int v = decltype(V)::value;
        asm volatile("s_waitcnt vmcnt(%0) lgkmcnt(0)\n\ts_barrier" :: "n"(decltype(cnt(V))::value * (v ? 1 : 2)) : "memory");
        step(V);
    });
}
template <int NU, int NI, int D, class ISSUE, class READ> DEVI void wstream(char* ring, ISSUE issue, READ read) {
    const unsigned base = (unsigned)(size_t)(LAS char*)ring;
    sfor<D>([&](auto U) { constexpr int u = decltype(U)::value; if constexpr (u < NU) issue(U, base); });
    sfor<NU>([&](auto U) {
        constexpr int u = decltype(U)::value, left = (NU - u < D ? NU - u : D) - 1;
        asm volatile("s_waitcnt vmcnt(%0)" :: "n"(left * NI) : "memory");
        read(U, ring + (u % D) * NI * 1024);
        __builtin_amdgcn_s_waitcnt(0xc07f);
        asm volatile("" ::: "memory");
        if constexpr (u + D < NU) issue(std::integral_constant<int, u + D>{}, base);
    });
}

constexpr int L_XN = 0;
constexpr int L_K = 40960;
constexpr int L_MEM = L_K + 12288;
constexpr int L_QM = L_MEM + 10240;
constexpr int L_Q = L_QM + 12288;
constexpr int L_W = 94208;
#define SLOT(i) ((i) % 3 == 0 ? 45056 : (i) % 3 == 1 ? 69632 : L_W + 40960)
#define CMIN(a, b) ((a) < (b) ? (a) : (b))
#define IC(x) std::integral_constant<int, (x)>{}
#define CV(X) decltype(X)::value
DEVI bf16x8 lr128(unsigned a) { return *(const LAS bf16x8*)(size_t)a; }
DEVI bf16x4 lrtr(unsigned a) { return __builtin_amdgcn_ds_read_tr16_b64_v4i16((LAS bf16x4*)(size_t)a); }
#define CAT4(a, b) __builtin_shufflevector(a, b, 0, 1, 2, 3, 4, 5, 6, 7)
DEVI void lw128(unsigned a, u32x4 v) { *(LAS u32x4*)(size_t)a = v; }
DEVI void lw64(unsigned a, u32x2 v) { *(LAS u32x2*)(size_t)a = v; }
DEVI void lw32(unsigned a, unsigned v) { *(LAS unsigned*)(size_t)a = v; }
template <int N> DEVI void bar_lgkm() { asm volatile("s_waitcnt lgkmcnt(%0)\n\ts_barrier" :: "n"(N) : "memory"); }
DEVI void atom1(__amdgpu_buffer_rsrc_t rs, unsigned voff, unsigned soff) {
    unsigned one; asm volatile("v_mov_b32 %0, 1\n\tbuffer_atomic_add %0, %1, %2, %3 offen" : "=&v"(one) : "v"(voff), "s"(rs), "s"(soff) : "memory");
}
DEVI unsigned vadd(unsigned v, unsigned sc) { unsigned d; asm volatile("v_add_u32 %0, %1, %2" : "=v"(d) : "s"(sc), "v"(v)); return d; }
DEVI float vzero() { float z; asm volatile("v_mov_b32 %0, 0" : "=v"(z)); return z; }
template <int S> DEVI unsigned slot_adr(unsigned a) { if constexpr (S % 3 == 2) return vadd(a, SLOT(2) - SLOT(0)); else return a + (SLOT(S) - SLOT(0)); }
template <int B, int I> DEVI void xn_op(const Ctx& k, unsigned v, unsigned src, unsigned dst) {
    if constexpr (I == 0) set_m0(dst + B * 8192);
    else if constexpr (I == 1) dmab<0>(k.rs, v, src + B * 16384);
    else dmab<1024>(k.rs, v, src + B * 16384);
}
DEVI void xn_all(const Ctx& k, unsigned v, unsigned src, unsigned dst) {
    sfor<5>([&](auto B) { xn_op<CV(B), 0>(k, v, src, dst); asm volatile("s_nop 0"); xn_op<CV(B), 1>(k, v, src, dst); xn_op<CV(B), 2>(k, v, src, dst); });
}
struct Xn {
    unsigned src, dst, sg, need, t, sb, se, l16, c, off;
    unsigned long long flip, m;
    DEVI void init(const Ctx& k) {
        src = k.seg(PG_XNA, 0) + k.w * 2048; dst = L_XN + k.w * 2048; sg = 0; off = ~0u;
        const unsigned q = (unsigned)(k.par >> PG_XNA), d = q ^ (q >> 1);
        flip = ((unsigned long long)(d & 1) << 16) | ((unsigned long long)(d & 2) << 31) | ((unsigned long long)(d & 4) << 46);
    }
    template <int I> DEVI void next(const Ctx& k, unsigned l16) {
        if constexpr (I == 0) { sg += 1; src += 131072; PINS(sg); PINS(src); }
        if constexpr (I == 1) asm volatile("s_bitcmp1_b64 %1, %2\n\ts_cselect_b32 %0, 0x2000, 0" : "=s"(t) : "s"(flip), "s"(sg) : "scc");
        if constexpr (I == 2) { src ^= t; PINS(src); }
        if constexpr (I == 3) asm volatile("v_and_b32 %0, 0x380, %1" : "=v"(c) : "v"(l16));
        if constexpr (I == 4) c = ld32(k.rs, c, k.p0 + CNT(C_XN, 0));
        if constexpr (I == 5) { need = sg - (k.c ? 0u : 8u); PINS(need); }
        if constexpr (I == 6) { need = sg < 64u ? need : 0x7FFFFFFFu; PINS(need); }
        if constexpr (I == 7) asm volatile("v_cmp_gt_i32_e64 %0, %1, %2" : "=s"(m) : "s"(need), "v"(c) : "memory");
        if constexpr (I == 8) asm volatile("s_cmp_lg_u64 %2, 0\n\ts_cselect_b32 %0, -1, 0\n\ts_cselect_b32 %1, 0x200000, %3" : "=&s"(off), "=s"(se) : "s"(m), "s"(src) : "scc");
        if constexpr (I == 13) set_m0(L_MEM);
        if constexpr (I == 14) dmab<0>(k.rs, c, k.p0 + CNT(C_XN, 0));
        if constexpr (I == 15) c = *(volatile LAS unsigned*)(size_t)(l16 + L_MEM);
        if constexpr (I == 11) { need = sg < 64u ? need : 0x80000000u; PINS(need); }
        if constexpr (I == 12) {
            if (__builtin_expect(m != 0, 0)) {
_Pragma("unroll 1")
                for (unsigned spins = 0; __any((int)*(volatile LAS unsigned*)(size_t)(l16 + L_MEM) < (int)need) && !spin_fail(k, spins >> 2); ++spins) { next<3>(k, l16); __builtin_amdgcn_s_sleep(1); next<14>(k, l16); }
            }
            se = src; }
        if constexpr (I == 10) asm volatile("s_cmp_lg_u32 %1, 64\n\ts_cselect_b32 %0, %0, 0" : "+s"(off) : "s"(sg) : "scc");
    }
    template <int B, int I> DEVI void op(const Ctx& k) {
        if constexpr (I == 0) { set_m0(dst + B * 8192); sb = se + B * 16384; PINS(sb); }
        else dmab<(I - 1) * 1024>(k.rs, l16, sb);
    }
    template <int NB> DEVI void late(const Ctx& k, unsigned l16) {
        xn_wait(k, l16, sg);
        se = src;
        sfor<NB>([&](auto B) { op<CV(B), 0>(k); asm volatile("s_nop 0"); op<CV(B), 1>(k); op<CV(B), 2>(k); });
        DRAIN();
    }
};
template <int OFF> DEVI u32x4 ald128(__amdgpu_buffer_rsrc_t rs, unsigned v, unsigned s) { u32x4 d; asm volatile("buffer_load_dwordx4 %0, %1, %2, %3 offen offset:%4 sc1" : "=v"(d) : "v"(v), "s"(rs), "s"(s), "n"(OFF) : "memory"); return d; }
#define MADR(b) (L_MEM + ((b) < 6 ? ((b) - 1) * 2048 : ((b) - 6) * 2048 + 3072))
struct Gather {
    u32x4 p0, p1, v0, v1;
    unsigned mx, a, aw, b, base, wo, dst;
    unsigned long long bad;
    DEVI void init(const Ctx& k) { base = k.memf(0); wo = k.w * 1024; dst = L_MEM + k.w * 2048; }
    template <int I> DEVI void op(const Ctx& k, unsigned l16, int s) {
        if constexpr (I == 0) { a = ((unsigned)s << 15) & 0x1F8000u; PINS(a); }
        if constexpr (I == 1) { a += base; aw = a + wo; PINS(a); PINS(aw); }
        if constexpr (I == 2) { b = aw + 3072 + (k.w >> 1) * 8192; PINS(b); set_m0(dst); }
        if constexpr (I == 3) v0 = ald128<2048>(k.rs, l16, aw);
        if constexpr (I == 4) v1 = ald128<3072>(k.rs, l16, b);
        if constexpr (I == 5) dmab<2048>(k.rs, l16, aw);
        if constexpr (I == 6) dmab<3072>(k.rs, l16, b);
        if constexpr (I == 7) p0 = ald128<0>(k.rs, l16, a);
        if constexpr (I == 8) p1 = ald128<1024>(k.rs, l16, a);
    }
    template <int N> DEVI void wait() { asm volatile("s_waitcnt vmcnt(%4)" : "+v"(p0), "+v"(p1), "+v"(v0), "+v"(v1) : "n"(N) : "memory"); }
    template <int I> DEVI void fin(unsigned other) {
#define MX3 "v_max3_u32 %0, %0, %3, %4\n\tv_max3_u32 %0, %0, %5, %6"
        if constexpr (I == 0) asm volatile("v_max3_u32 %0, %7, %1, %2\n\t" MX3 : "=&v"(mx) : "v"(v0[0]), "v"(v0[1]), "v"(v0[2]), "v"(v0[3]), "v"(v1[0]), "v"(v1[1]), "s"(other));
        if constexpr (I == 1) asm volatile("v_max3_u32 %0, %0, %1, %2\n\t" MX3 : "+v"(mx) : "v"(v1[2]), "v"(v1[3]), "v"(p0[0]), "v"(p0[1]), "v"(p0[2]), "v"(p0[3]));
        if constexpr (I == 2) asm volatile("v_max3_u32 %1, %1, %2, %3\n\tv_max3_u32 %1, %1, %4, %5\n\tv_cmp_eq_u32_e64 %0, -1, %1" : "=s"(bad) : "v"(mx), "v"(p1[0]), "v"(p1[1]), "v"(p1[2]), "v"(p1[3]));
    }
    DEVI void also(unsigned other) { asm volatile("v_mov_b32 %0, %1" : "=v"(mx) : "s"(other)); }
    DEVI void all() {
        asm volatile("s_waitcnt vmcnt(0)\n\tv_max3_u32 %1, %1, %2, %3\n\tv_max3_u32 %1, %1, %4, %5\n\tv_max3_u32 %1, %1, %6, %7\n\tv_max3_u32 %1, %1, %8, %9\n\tv_max3_u32 %1, %1, %10, %11\n\tv_max3_u32 %1, %1, %12, %13\n\t"
            "v_max3_u32 %1, %1, %14, %15\n\tv_max3_u32 %1, %1, %16, %17\n\tv_cmp_eq_u32_e64 %0, -1, %1" : "=s"(bad) : "v"(mx), "v"(v0[0]), "v"(v0[1]), "v"(v0[2]), "v"(v0[3]), "v"(v1[0]), "v"(v1[1]), "v"(v1[2]), "v"(v1[3]),
            "v"(p0[0]), "v"(p0[1]), "v"(p0[2]), "v"(p0[3]), "v"(p1[0]), "v"(p1[1]), "v"(p1[2]), "v"(p1[3]) : "memory");
    }
    DEVI void check() { also(0); all(); asm volatile("s_nop 3"); }
    unsigned rv, rs; unsigned long long rt;
    template <int I> DEVI void ask(const Ctx& k, unsigned l16) {
        if constexpr (I == 0) asm volatile("v_and_b32 %0, 0xf0, %1\n\tv_lshlrev_b32 %0, 1, %0" : "=v"(rv) : "v"(l16));
        if constexpr (I == 1) asm volatile("buffer_load_dword %0, %0, %1, %2 offen sc1" : "+v"(rv) : "s"(k.rs), "s"(k.p0 + CNT(C_XB, 0)) : "memory");
    }
    template <int I> DEVI void red() {
        if constexpr (I == 0) asm volatile("s_waitcnt vmcnt(4)\n\tv_max_u32_dpp %0, %0, %0 quad_perm:[1,0,3,2] row_mask:0xf bank_mask:0xf" : "+v"(rv));
        if constexpr (I == 1) asm volatile("v_max_u32_dpp %0, %0, %0 quad_perm:[2,3,0,1] row_mask:0xf bank_mask:0xf" : "+v"(rv));
        if constexpr (I == 2) asm volatile("v_max_u32_dpp %0, %0, %0 row_ror:4 row_mask:0xf bank_mask:0xf" : "+v"(rv));
        if constexpr (I == 3) asm volatile("v_max_u32_dpp %0, %0, %0 row_ror:8 row_mask:0xf bank_mask:0xf" : "+v"(rv));
        if constexpr (I == 4) asm volatile("v_readfirstlane_b32 %0, %1" : "=s"(rs) : "v"(rv));
    }
    template <int I> DEVI void hold(unsigned step) {
        if constexpr (I == 0) rt = rt_async();
        if constexpr (I == 3) { PINS(rt); b = spack((unsigned)rt, step); }
        if constexpr (I == 4) asm volatile("s_sub_u32 %0, %1, %0" : "+s"(b) : "s"(rs) : "scc");
        if constexpr (I == 5) if (__builtin_expect(b < 128u, 0)) { while ((int)((rclk() - rs) << 16) < 0) { } }
    }
    template <int I> DEVI void at(const Ctx& k, unsigned l16) {
        if constexpr (I == 1) op<2>(k, l16, 0);
        if constexpr (I >= 2 && I < 15 && (I % 5 == 2 || I % 5 == 4)) op<3 + (I / 5) * 2 + (I % 5) / 4>(k, l16, 0);
    }
    DEVI void again(const Ctx& k, unsigned l16) {
        asm volatile("s_mov_b32 m0, %9\n\tbuffer_load_dwordx4 %2, %4, %5, %7 offen offset:2048 sc1\n\tbuffer_load_dwordx4 %3, %4, %5, %8 offen offset:3072 sc1\n\t"
            "buffer_load_dwordx4 %4, %5, %7 offen offset:2048 sc1 lds\n\tbuffer_load_dwordx4 %4, %5, %8 offen offset:3072 sc1 lds\n\t"
            "buffer_load_dwordx4 %0, %4, %5, %6 offen sc1\n\tbuffer_load_dwordx4 %1, %4, %5, %6 offen offset:1024 sc1\n\ts_waitcnt vmcnt(0)"
            : "+v"(p0), "+v"(p1), "+v"(v0), "+v"(v1) : "v"(l16), "s"(k.rs), "s"(a), "s"(aw), "s"(b), "s"(dst) : "memory");
        check();
    }
};
DEVI void clr2(f32x4& c) { f32x2 a, b; asm volatile("v_mov_b64 %0, 0\n\tv_mov_b64 %1, 0" : "=v"(a), "=v"(b)); c = __builtin_shufflevector(a, b, 0, 1, 2, 3); }
#define NOP7_3(a) asm volatile("s_nop 7" : "+v"(a[0]), "+v"(a[1]), "+v"(a[2]))

struct G3 {
    f32x4 a0[3], a1[3];
    bf16x8 x0[20], x1[20], wl[20];
    unsigned l16, lwv;
    template <int KS> DEVI void r0() { x0[KS] = lr128(l16 + L_XN + KS * 2048); }
    template <int KS> DEVI void r1() { x1[KS] = lr128(l16 + L_XN + KS * 2048 + 1024); }
    template <int KS> DEVI void rw() { if constexpr (KS >= 4) wl[KS] = lr128(lwv + (KS - 4) * 4096); }
    template <bool WL, int KS> DEVI void pre() { if constexpr (WL && KS >= 4) asm volatile("" :: "v"(wl[KS]), "v"(x0[KS]), "v"(x1[KS])); else asm volatile("" :: "v"(x0[KS]), "v"(x1[KS])); }
    template <int ID0, bool WL, int ks, int m> DEVI void mm(const bf16x16 (&W)[52]) {
        constexpr int b = ks >> 1, t = m % 3, id = ID0 + t * 10 + b;
        f32x4& c = m < 3 ? a0[t] : a1[t];
        const bf16x8& x = m < 3 ? x0[ks] : x1[ks];
        if constexpr (WL && t == 2 && b >= 2) { if constexpr (ID0 == 30) mfb<false, false>(c, x, wl[ks]); else mfa<false, false>(c, wl[ks], x); }
        else mfw<id % 52, ks & 1, ID0 == 30, ks == 0>(c, W[id % 52], x);
        SCHED0();
    }
    template <int ID0, bool WL, int K0, int K1, int PF, bool H6 = false, class HOOK> DEVI void run(const bf16x16 (&W)[52], HOOK hook) {
        sfor<K1 - K0>([&](auto KK) {
            constexpr int kk = CV(KK), ks = K0 + kk;
            sfor<6>([&](auto M) {
                constexpr int m = CV(M);
                mm<ID0, WL, ks, m>(W);
                if constexpr (m == 5 && ks + 1 < K1) { pre<WL, (ks + 1) % 20>(); SCHED0(); }
                if constexpr (ks + 2 < PF) {
                    if constexpr (m == 0) r0<(ks + 2) % 20>();
                    if constexpr (m == 1) r1<(ks + 2) % 20>();
                    if constexpr (m == 3 && WL) rw<(ks + 2) % 20>();
                }
                if constexpr (H6) hook(IC(kk * 6 + m));
                else { hook(IC(kk * 7 + (m < 5 ? m : 6))); if constexpr (m == 4) hook(IC(kk * 7 + 5)); }
                SCHED0();
            });
        });
    }
    template <int ID0, bool WL, int F0, int F1, int PF, class HOOK> DEVI void go(const bf16x16 (&W)[52], HOOK hook) {
        sfor<F1 - F0>([&](auto FF) {
            constexpr int f = F0 + CV(FF), ks = f / 6, m = f % 6;
            mm<ID0, WL, ks, m>(W);
            if constexpr (m == 5 && f + 1 < F1) { pre<WL, (ks + 1) % 20>(); SCHED0(); }
            if constexpr (ks + 2 < PF) rd<WL, (f + 12) % 120>();
            hook(IC(f));
            SCHED0();
        });
    }
    template <int P> DEVI void home() {
        if constexpr (P == 0) asm volatile("" : "+{v[12:15]}"(a0[0]), "+{v[16:19]}"(a0[1]), "+{v[20:23]}"(a0[2]), "+{v[24:27]}"(a1[0]), "+{v[28:31]}"(a1[1]), "+{v[32:35]}"(a1[2]));
        else asm volatile("" : "+{v[36:39]}"(a0[0]), "+{v[40:43]}"(a0[1]), "+{v[44:47]}"(a0[2]), "+{v[48:51]}"(a1[0]), "+{v[52:55]}"(a1[1]), "+{v[56:59]}"(a1[2]));
    }
    template <bool WL, int F> DEVI void rd() {
        if constexpr (F % 6 == 0) r0<F / 6>();
        if constexpr (F % 6 == 1) r1<F / 6>();
        if constexpr (F % 6 == 3 && WL) rw<F / 6>();
    }
    u32x2 o;
    template <int I, int OFF> DEVI void out4(unsigned d0) {
        if constexpr (I >= 0 && I < 24) {
            constexpr int t = I / 8, rt = (I / 4) & 1, j = I & 1;
            const f32x4& a = rt ? a1[t] : a0[t];
            if constexpr (I % 4 < 2) { o[j] = cvtpk(a[2 * j], a[2 * j + 1]); PIN(o[j]); }
            else lw32(d0 + OFF + rt * 6144 + t * 1024 + j * 4, o[j]);
        }
    }
    template <int I, int OFF> DEVI void out(unsigned d0, unsigned d1) {
        if constexpr (I >= 0 && I < 18) {
            constexpr int t = I / 6, rt = (I / 3) & 1;
            const f32x4& a = rt ? a1[t] : a0[t];
            if constexpr (I % 3 == 0) { o[0] = cvtpk(a[0], a[1]); PIN(o[0]); }
            else if constexpr (I % 3 == 1) { o[1] = cvtpk(a[2], a[3]); PIN(o[1]); }
            else if constexpr (OFF > 0) lw64(d0 + (OFF - 1) + rt * 6144 + t * 1024, o);
            else lw64((t == 1 ? d1 : d0) + OFF + rt * 6144 + (t == 2 ? 1024 : 0), o);
        }
    }
};
#define SPIX(l) (((l) & 2) ? 0xEEEEu : 0x4444u)
template <int I> DEVI void fold_op(f32x4& a, unsigned& o) {
    if constexpr (I == 0) asm volatile("v_add_f32 %0, %0, %1" : "+v"(a[0]) : "v"(a[2]));
    if constexpr (I == 1) asm volatile("v_add_f32 %0, %0, %1" : "+v"(a[1]) : "v"(a[3]));
    if constexpr (I == 2) asm volatile("v_cvt_pk_bf16_f32 %0, %1, %2" : "=v"(o) : "v"(a[0]), "v"(a[1]));
}

template <int J> DEVI void fold2(f32x4& a, f32x4& b, unsigned (&o)[2]) {
    if constexpr (J < 2) asm volatile("v_add_f32 %0, %0, %2\n\tv_add_f32 %1, %1, %3" : "+v"(a[J]), "+v"(b[J]) : "v"(a[J + 2]), "v"(b[J + 2]));
    else asm volatile("v_cvt_pk_bf16_f32 %0, %2, %3\n\tv_cvt_pk_bf16_f32 %1, %4, %5" : "=&v"(o[0]), "=&v"(o[1]) : "v"(a[0]), "v"(a[1]), "v"(b[0]), "v"(b[1]));
}
DEVI unsigned vshl1(unsigned x, unsigned b) { unsigned d; asm volatile("v_lshl_or_b32 %0, %1, 1, %2" : "=v"(d) : "s"(x), "n"(b)); return d; }

#define ANNOUNCE(s, now, d) (((unsigned)(s) << 16) | (((unsigned)(now) + (d)) & 0xFFFFu))

template <class PUT> DEVI void ln_rows(const u32x4 (&a)[5], int n, int row  , PUT put) {
    float v[40];
#pragma unroll
    for (int i = 0; i < 20; ++i) { v[2 * i] = blo(a[i >> 2][i & 3]); v[2 * i + 1] = bhi(a[i >> 2][i & 3]); }
    f32x2 s2[4] = {{0.f, 0.f}, {0.f, 0.f}, {0.f, 0.f}, {0.f, 0.f}};
#pragma unroll
    for (int i = 0; i < 40; i += 2) s2[(i >> 1) & 3] += (f32x2){v[i], v[i + 1]};
    const f32x2 st = (s2[0] + s2[1]) + (s2[2] + s2[3]);
    float m = row16sum(st[0] + st[1]) * (1.f / 640.f);
    f32x2 q2[4] = {{0.f, 0.f}, {0.f, 0.f}, {0.f, 0.f}, {0.f, 0.f}};
#pragma unroll
    for (int i = 0; i < 40; i += 2) { const f32x2 d = (f32x2){v[i], v[i + 1]} - (f32x2){m, m}; q2[(i >> 1) & 3] += d * d; }
    const f32x2 qt = (q2[0] + q2[1]) + (q2[2] + q2[3]);
    const float q = row16sum(qt[0] + qt[1]);
    const float var = bfr(q * (1.f / 640.f));
    m = bfr(m);
    const float rden = __builtin_amdgcn_rcpf(bfr(__builtin_amdgcn_sqrtf(bfr(var + 1e-5f))));
    const unsigned vo = (n >> 2) * 2048 + (row >> 4) * 1024 + ((n & 3) * 16 + (row & 15)) * 16;
#pragma unroll
    for (int c = 0; c < 5; ++c) {
        u32x4 o;
#pragma unroll
        for (int i = 0; i < 4; ++i) { unsigned d = cvtpk(v[c * 8 + 2 * i] - m, v[c * 8 + 2 * i + 1] - m); o[i] = cvtpk(blo(d) * rden, bhi(d) * rden); }
        put(vo, c, o);
    }
}

struct Soft {
    unsigned long long mA, mY, mC[8];
    unsigned v0, v1;
    unsigned sg, hs;
    float a[6], m, M, t, s, h; u32x2 pk;
    template <int I> DEVI void mask(f32x4 (&sc)[3]) {
        if constexpr (I == 0) asm volatile("v_mov_b32 %0, 0xfe800000" : "=v"(t));
        if constexpr (I >= 1 && I < 9) { float d; asm volatile("v_cndmask_b32_e64 %0, %1, 0, %2" : "=v"(d) : "v"(t), "s"(mC[I - 1])); sc[(I - 1) >> 2][(I - 1) & 3] = d; }
    }
    template <int I> DEVI void op(const Ctx& k, f32x4 (&sc)[3], unsigned a0, unsigned a1, unsigned hv) {
#define FMA(x, y) "v_fmamk_f32 " x ", " x ", 0x3dd53b95, " y "\n\t"
#define EXPC(x) "v_exp_f32_e64 " x ", " x " clamp\n\t"
#define OP3(o, d, x, y) o " " d ", " x ", " y "\n\t"
#define SA(txt) asm volatile(txt : "+v"(a[0]), "+v"(a[1]), "+v"(a[2]), "+v"(a[3]), "+v"(m), "+v"(t))
#define SB(txt) asm volatile(txt : "+v"(a[0]), "+v"(a[1]), "+v"(a[2]), "+v"(a[3]), "+v"(m), "+v"(t), "+v"(s), "+v"(a[4]), "+v"(a[5]), "+v"(M), "+v"(h) : "s"(hs), "s"(sg))
#define ADD(d, x, y) OP3("v_add_f32", d, x, y)
#define MUL(d, x, y) OP3("v_mul_f32", d, x, y)
#define CND(d, x, y, c) "v_cndmask_b32_e64 " d ", " x ", " y ", " c "\n\t"
        if constexpr (I == 0) asm volatile("v_max3_f32 %0, %1, %2, %3\n\tv_max3_f32 %0, %0, %4, %5" : "=&v"(m) : "v"(sc[0][0]), "v"(sc[0][1]), "v"(sc[0][2]), "v"(sc[0][3]), "v"(sc[1][0]));
        if constexpr (I == 1) asm volatile("v_max3_f32 %0, %0, %1, %2" : "+v"(m) : "v"(sc[1][1]), "v"(sc[1][2]));
        if constexpr (I == 2) asm volatile(CND("%0", "%2", "%3", "%6") CND("%1", "%4", "%5", "%6") : "=&v"(a[2]), "=&v"(a[3]) : "v"(sc[0][2]), "v"(sc[1][0]), "v"(sc[0][3]), "v"(sc[1][1]), "s"(mY));
        if constexpr (I == 3) asm volatile("v_max_f32 %1, %1, %5\n\t" CND("%0", "%2", "%3", "%4") : "=&v"(a[0]), "+v"(m) : "v"(sc[0][0]), "v"(a[2]), "s"(mA), "v"(sc[1][3]));
        if constexpr (I == 4) asm volatile("v_mul_f32 %0, 0xbdd53b95, %1" : "=v"(t) : "v"(m));
        if constexpr (I == 5) asm volatile(CND("%0", "%2", "%3", "%5") CND("%1", "%1", "%4", "%5") : "=&v"(a[1]), "+v"(a[2]) : "v"(sc[0][1]), "v"(a[3]), "v"(sc[1][2]), "s"(mA));
        if constexpr (I == 6) asm volatile(CND("%0", "%0", "%2", "%3") FMA("%1", "%4") : "+v"(a[3]), "+v"(a[0]) : "v"(sc[1][3]), "s"(mA), "v"(t));
        if constexpr (I == 7) SA(FMA("%1", "%5"));
        if constexpr (I == 8) SA(FMA("%2", "%5") FMA("%3", "%5"));
        if constexpr (I == 9) SA(EXPC("%0"));
        if constexpr (I == 10) { asm volatile("v_max3_f32 %0, %1, %2, %3" : "=v"(M) : "v"(m), "v"(sc[2][0]), "v"(sc[2][1]), "v"(sc[2])); a[4] = sc[2][0]; a[5] = sc[2][1]; }
        if constexpr (I == 11) asm volatile("v_mov_b32 %0, %3\n\tv_mov_b32 %1, %4" : "=&v"(t), "=&v"(h), "=v"(s) : "v"(M), "s"(hv));
        if constexpr (I == 12) SB("v_permlane16_swap_b32 %9, %5");
        if constexpr (I == 13) SB(OP3("v_max_f32", "%9", "%9", "%5"));
        if constexpr (I == 14) SB(EXPC("%1"));
        if constexpr (I == 15) SB("v_mov_b32 %5, %9\n\t" ADD("%6", "%0", "%1"));
        if constexpr (I == 16) SB("v_permlane32_swap_b32 %9, %5");
        if constexpr (I == 17) SB(OP3("v_max_f32", "%9", "%9", "%5") "v_mul_f32 %9, 0xbdd53b95, %9");
        if constexpr (I == 18) SB(FMA("%7", "%9") FMA("%8", "%9"));
        if constexpr (I == 19) SB(FMA("%4", "%9"));
        if constexpr (I == 20) SB(EXPC("%2"));
        if constexpr (I == 21) SB(EXPC("%3"));
        if constexpr (I == 22) SB(ADD("%6", "%6", "%2"));
        if constexpr (I == 23) SB(EXPC("%7"));
        if constexpr (I == 24) SB(EXPC("%8"));
        if constexpr (I == 25) SB(ADD("%6", "%6", "%3"));
        if constexpr (I == 26) SB(EXPC("%4"));
        if constexpr (I == 27) SB(MUL("%6", "%12", "%6") ADD("%5", "%7", "%8"));
        if constexpr (I == 28) SB(MUL("%6", "%6", "%4"));
        if constexpr (I == 29) SB(OP3("v_fmac_f32", "%6", "%11", "%5"));
        if constexpr (I == 30) SB("v_fmac_f32_dpp %6, %6, %10 row_ror:8 row_mask:0xf bank_mask:0xf\n\tv_mov_b32 %5, %6");
        if constexpr (I == 31) SB("v_permlane16_swap_b32 %6, %5");
        if constexpr (I == 32) SB(ADD("%6", "%6", "%5") "v_mov_b32 %5, %6");
        if constexpr (I == 33) SB("v_permlane32_swap_b32 %6, %5");
        if constexpr (I == 34) SB(ADD("%6", "%6", "%5"));
        if constexpr (I == 35) SB("v_rcp_f32 %6, %6");
        if constexpr (I == 36) SB(MUL("%4", "%4", "%6") MUL("%7", "%7", "%6"));
        if constexpr (I == 37) SB(MUL("%0", "%0", "%4"));
        if constexpr (I == 38) SB(MUL("%1", "%1", "%4") MUL("%2", "%2", "%4"));
        if constexpr (I == 39) { unsigned x; asm volatile(MUL("%1", "%1", "%4") "v_cvt_pk_bf16_f32 %0, %2, %3" : "=&v"(x), "+v"(a[3]) : "v"(a[0]), "v"(a[1]), "v"(m)); pk[0] = x; }
        if constexpr (I == 40) { unsigned x; asm volatile("v_cvt_pk_bf16_f32 %0, %1, %2" : "=v"(x) : "v"(a[2]), "v"(a[3])); pk[1] = x; }
        if constexpr (I == 41) { st64(k.rs, v0, a0, pk); SCHED0(); asm volatile("v_mul_f32 %1, %1, %2\n\tv_cvt_pk_bf16_f32 %0, %0, %1" : "+v"(a[4]), "+v"(a[5]) : "v"(s)); }
        if constexpr (I == 42) st32(k.rs, v1, a1, __float_as_uint(a[4]));
    }
};
#define QIDX(t, b) ((t) * 10 + (b))
DEVI void qk_role(const Params& p, Ctx& k, char* smem, int h) {
    PROF_DECL
    const int w = k.w;
    bf16x16 W[52];
    Gather G;
    Soft S;
    const int hw = h >> 3, hh = h & 7;
    unsigned l16, ix, dqm, dq, trp, qp;
    {
        const int l = k.l, g = l >> 4, n = l & 15, r = n & 7, u = w & 1;
        const bool hf = n >= 8, dup = w != 1;
        S.mA = __ballot(dup && hf);
        const int lim = (w == 0 ? 64 : w == 1 ? n : 16 + 8 * u + r) - 4 * g;
#pragma unroll
        for (int i = 0; i < 8; ++i) S.mC[i] = __ballot((i & 3) + (i >> 2) * 16 <= lim);
        const bool okm = !(dup && hf);
        S.mY = __ballot((dup && hf) == (w != 0));
        S.v0 = w == 0 ? hh * 512 + ((g >> 1) * 16 + (r >> 1) * 4 + hf * 2 + (r & 1)) * 16 + (g & 1) * 8 : w == 1 ? (hh >> 1) * 1024 + l * 16 + (hh & 1) * 8 : hh * 1024 + (g * 16 + 8 * u + r) * 16 + hf * 8;
        S.v1 = !okm ? OOB : w == 0 ? 4096 + hh * 128 + r * 16 + g * 4 : (w == 1 ? 4096 + n * 16 : 6144 + (8 * u + r) * 16) + (hh >> 1) * 512 + (g >> 1) * 256 + (hh & 1) * 8 + (g & 1) * 4;
        l16 = l * 16; ix = SPIX(l);
        const int uu = u ^ ((n >> 2) & 1);
        dqm = L_QM + (w >> 1) * 3072 + uu * 512 + (g >> 1) * 128 + n * 8 + (g & 1) * 4;
        dq = L_QM + 6144 + (w >> 1) * 3072 + uu * 512 + g * 128 + ((n * 8) ^ ((g >> 1) * 64));
        trp = L_QM + 6144 + (l & 3) * 128 + (((l >> 2) * 8) ^ ((l & 2) * 32));
        qp = w == 1 ? trp + 6144 : (w == 0 ? L_QM : L_Q + 6144 + u * 256) + (l & 1) * 128 + (((l >> 2) * 8) ^ (w == 3 ? 64 : 0));
        u32x8 rg = {S.v0, S.v1, l16, ix, dqm, dq, trp, qp};
        asm volatile("" : "+{v[0:7]}"(rg));
        S.v0 = rg[0]; S.v1 = rg[1]; l16 = rg[2]; ix = rg[3]; dqm = rg[4]; dq = rg[5]; trp = rg[6]; qp = rg[7];
    }
    Xn X;
    G3 A, B;
    X.l16 = l16; A.l16 = l16; B.l16 = l16; B.lwv = vadd(l16, L_W + w * 1024);
    {
        const int l = k.l;
        bf16x8 wt[16];
        u32x2 gv;
        unsigned ra[3];
#pragma unroll
        for (int t = 0; t < 3; ++t) ra[t] = opq(wp_adr<384, true>(w * 3 + t, l) + SLOT(0));
#define QEX(v) ((v) < 0 ? 0 : ((v) < 6) + ((v) == QGS) + 10 * ((v) == QGS + 2) + 8 * ((v) == QGS + 7))
#define QKE(v) ((v) < QGS + 4 ? 0 : (v) == 19 ? 20 : CMIN(v, 3 * ((v) - QGS - 3)))
        auto rdx = [&](auto K) { constexpr int ks = CV(K); A.r0<ks>(); A.r1<ks>(); B.x0[ks] = A.x0[ks]; B.x1[ks] = A.x1[ks]; if constexpr (ks < 14 && QKE(ks + 1) <= ks) B.rw<ks>(); };
        cstream<20>([](auto V) { constexpr int v = CV(V); return IC(QEX(v - 1) + (v < 19 ? 6 : 0)); },
            [&](auto U) { constexpr int u = CV(U), b = u >> 1, k0 = QKE(u - 1), n = (QKE(u) - k0) * 12, nn = n ? n : 12;
                if constexpr (u < 6) hello(k, 0);
                if constexpr (u == QGS) gv = go_ask(k);
                if constexpr (u == QGS + 2) { PIN(gv); go_wait(k, 0, gv); X.init(k); G.init(k); X.l16 = k.c ? OOB : l16; xn_all(k, X.l16, X.src, X.dst); }
                if constexpr (u == QGS + 4) rdx(IC(0));
                if constexpr (u == QGS + 7) { sfor<3>([&](auto I) { X.next<CV(I)>(k, l16); G.op<CV(I)>(k, l16, 0); }); X.se = X.src; }
                if constexpr (u == 18) {
                    asm volatile("" :: "{v[152:155]}"(wt[10]), "{v[156:159]}"(wt[11]), "{v[232:235]}"(wt[12]));
#pragma unroll
                    for (int i = 10; i < 13; ++i) lw128(B.lwv + i * 4096, __builtin_bit_cast(u32x4, wt[i]));
                }
                if constexpr (u == 19) { G.wait<0>(); lw128(l16 + G.dst + 2048, G.v0); lw128(l16 + G.dst + 3072, G.v1); }
                XSTAMP(u)
                const unsigned rb[3] = {slot_adr<u>(ra[0]), slot_adr<u>(ra[1]), slot_adr<u>(ra[2])};
                bf16x4 hf[12];
                SCHED0();
                sfor<nn>([&](auto I) { constexpr int i = CV(I), ks = k0 + i / 12, m = i % 12, t = i >> 1, e = u == QGS + 7 ? 8 : 0, j = ((i - e) * 6) / (nn - e);
                    if constexpr (n > 0) { if constexpr (m < 6) A.mm<0, false, ks, m>(W); else B.mm<30, true, ks, m - 6>(W); }
                    if constexpr (i < e) { if constexpr (i % 4 == 0) X.op<i / 4, 0>(k); if constexpr (i & 1) X.op<i / 4, 1 + (i / 2) % 2>(k); else G.op<3 + i / 4 + (i & 2) * 2>(k, l16, 0); }
                    else if constexpr (u > 0 && u < 18 && (i == e || ((i - e - 1) * 6) / (nn - e) != j))
                        dma1<SLOT(u + 2) + (j / 3) * 12288 + (j % 3) * 1024>((j < 3 ? p.Wk : p.Wq) + (size_t)(u + 2) * 32 * 6144 + h * 192, wsrc<384, true>(w * 192 + (j % 3) * 64 + l, 12288), w * 3072);
                    if constexpr (i == 0 && u > 4 && (u < 15 || u >= 18)) lw128(B.lwv + (u - 5) * 4096, __builtin_bit_cast(u32x4, wt[(u - 5) & 15]));
                    if constexpr (i < 12) {
                        hf[i] = lrtr(rb[t % 3] + (t / 3) * 12288 + (i & 1) * 1536);
                        if constexpr (i & 1) {
                            if constexpr (t == 5 && b >= 2) B.wl[u] = wt[(u - 4) & 15] = CAT4(hf[i - 1], hf[i]);
                            else if constexpr (u & 1) W[QIDX(t, b) % 52] = CAT8(LO8(W[QIDX(t, b) % 52]), CAT4(hf[i - 1], hf[i]));
                            else W[QIDX(t, b) % 52] = __builtin_shufflevector(hf[i - 1], hf[i], 0, 1, 2, 3, 4, 5, 6, 7, -1, -1, -1, -1, -1, -1, -1, -1);
                        }
                    }
                    if constexpr (n > 0 && m == 3 && ks < 19) rdx(IC((ks + 1) % 20));
                    SCHED0(); }); });
        lw128(B.lwv + 15 * 4096, __builtin_bit_cast(u32x4, wt[15]));
        static_assert(QKE(17) == 17, "QGS");
        RAWBAR()
        XSTAMP(20)
        const unsigned d0 = (l16 | ((l16 >> 5) & 8)) ^ (L_K + (w >> 1) * 3072 + (w & 1) * 8);
        sfor<24>([&](auto I) { A.out4<CV(I), 0>(d0); });
        G.v0 = G.p0; G.v1 = G.p0; G.bad = 0;
        X.off = 0; X.l16 = l16;
        if (k.c) X.init(k);
    }
    RSTAMP(2)
    const unsigned po0 = k.p0 + PR_B + hw * 49152 + (w == 0 ? 0 : w == 1 ? 32768 : 16384), po1 = k.p0 + PR_B + hw * 49152 + (w == 0 ? 0 : 32768);
    const unsigned pdum = k.pg(1) + DUMMY_B, pbc = w == 0 ? k.p0 + CNT(C_BC, hw * 16 + hh) : pdum + 65536 + h * 512 + w * 128;
    unsigned long long now = 0, done = 0;
    unsigned a0 = po0 + (k.c ? pdum - k.p0 - PR_B : 0u), a1 = po1 + (k.c ? pdum - k.p0 - PR_B : 0u);
    const unsigned hv = w == 1 ? 0u : 0x3F800000u, hw2 = w == 1 ? 0u : 0x00800000u;
    S.sg = 0x3F800000u; S.hs = 0x3F800000u - hw2;
    f32x4 ac[6];
#pragma unroll
    for (int t = 0; t < 6; ++t) clr2(ac[t]);
    unsigned an = 110;
    __builtin_amdgcn_s_waitcnt(0xc07f);
    do {
        if (X.off != 0) {
            PC8(1, X.sg)
            X.late<2>(k, l16);
            G.check();
        }
_Pragma("unroll 1")
        for (unsigned spins = 0; G.bad != 0 && !spin_fail(k, spins); ++spins) { PC8(0, X.sg) G.again(k, l16); }
        HSTOP(0) HSTAMP(0, 0)
        clr2(ac[5]);
        asm volatile("s_nop 1" : "+v"(ac[5]));
        smfw<50>(ac[5], __builtin_bit_cast(bf16x8, G.p0), W[50], ix);
    do {
        PROF_S(X.sg - 1)
        f32x4 sc[3];
        {
            bf16x8 m[16], wa[10], wb[10], kf[12];
            bf16x4 q[12], km[12];
            unsigned fo[6];
            m[0] = __builtin_bit_cast(bf16x8, G.p0); m[1] = __builtin_bit_cast(bf16x8, G.p1);
            auto put = [&](auto T) { constexpr int t = CV(T); if constexpr (t < 3) lw32(dq + t * 1024, fo[t]); else lw32(dqm + (t - 3) * 1024, fo[t]); };
            auto rd = [&](auto C) { constexpr int c = CV(C), j = c % 20;
                if constexpr (c < 0) { }
                else if constexpr (c < 20) m[j] = lr128(l16 + MADR(j < 10 ? j : j - 8));
                else if constexpr (c < 40) wa[j] = lr128(B.lwv + (j - 2) * 8192);
                else if constexpr (c < 60) wb[j] = lr128(B.lwv + (j - 2) * 8192 + 4096);
                else if constexpr (c < 80) kf[j] = lr128(l16 + L_K + ((j & 1) * 6 + j / 2) * 1024);
                else if constexpr (c < 100) q[j] = lrtr(qp + j * 512);
                else if constexpr (c < 120) km[j] = lrtr(trp + j * 512);
                else if constexpr (c < 140) A.r0<j>();
                else A.r1<j>(); };
#define PB(i) ((i) < 12 ? (i) / 6 : (i) < 32 ? (i) / 4 - 1 : (i) < 44 ? 7 + ((i) - 32) % 3 : (i) < 54 ? 7 + ((i) - 44) / 2 : 12 + ((i) - 54) % 3)
#define PT(i) ((i) < 12 ? ((i) + 5) % 6 : (i) < 32 ? (3 + (i) % 4) % 6 : (i) < 44 ? (3 + ((i) - 32) / 3) % 6 : (i) < 54 ? 1 + ((i) & 1) : 1 + ((i) - 54) / 3)
            sfor<82>([&](auto I) {
                constexpr int i = CV(I) + 1, b = PB(i), t = PT(i), bn = PB(i + 1);
                constexpr int rt[83] = {-1, 2, -1, -1, -1, 22, 42, 3, -1, 23, 43, 4, -1, 24, 44, 5, -1, 25, 45, 6,
                    -1, 26, 46, 7, -1, 8, 9, 27, 47, 28, 48, 29, 49, -1, -1, 60, -1, 61, 62, 63,
                    -1, 10, 64, -1, 11, 12, -1, 13, 14, -1, -1, 80, 82, 83, 84, 85, 86, 87, 67, 88,
                    89, 69, -1, 91, -1, 71, -1, 121, -1, 100, 102, 104, 106, 108, 110, -1, 140, -1, -1, 141, -1, -1, 122};
                constexpr int r2[83] = {-1, -1, -1, -1, -1, -1, -1, -1, -1, -1, -1, -1, -1, -1, -1, -1, -1, -1, -1, -1,
                    -1, -1, -1, -1, -1, -1, -1, -1, -1, -1, -1, -1, -1, -1, -1, -1, -1, -1, -1, -1,
                    -1, -1, -1, -1, -1, -1, -1, -1, -1, -1, -1, 81, -1, 65, -1, 66, -1, -1, -1, -1,
                    68, 90, -1, 70, -1, 120, -1, -1, -1, 101, 103, 105, 107, 109, 111, -1, -1, -1, -1, -1, -1, -1, -1};
                if constexpr (i < 60) {
                    if constexpr (t == 5 && b >= 2) smf<false>(ac[5], m[b], CAT8(wa[b], wb[b]), ix);
                    else smfw<QIDX(t, b < 10 ? b : b - 8) % 52>(ac[t], m[b], W[QIDX(t, b < 10 ? b : b - 8) % 52], ix);
                } else if constexpr (i < 72) { constexpr int j = i - 60; mfa<false, false>(sc[j & 1], kf[j], CAT4(q[j & ~1], q[j | 1])); }
                else if constexpr (i < 77) A.mm<0, false, (i - 72) / 3, (i - 72) % 3>(W);
                else { constexpr int ks = i - 77; mfa<false, ks == 0>(sc[2], CAT4(km[2 * ks], km[2 * ks + 1]), CAT4(q[2 * ks], q[2 * ks + 1])); }
                SCHED0();
                if constexpr (i == 1) { AQ(3, 3) asm volatile("s_barrier" ::: "memory"); AQ(1, 2) AQ(3, 4) }
                else if constexpr (i == 50) { BARL(3); AQ(1, 3) }
                else if constexpr (i == 68) { BARL(3); AQ(1, 4) QSTAMP(0, 1) }
                else if constexpr (i >= 12 && i < 59) { if constexpr (PT(i + 1) == 5) TOUCH(wb[bn % 10]); TOUCH(m[bn]); }
                else if constexpr (i >= 59 && i < 71) { asm volatile("" :: "v"(kf[(i - 59) % 12]), "v"(q[((i - 59) | 1) % 12])); }
                else if constexpr (i == 71) TOUCH(A.x0[0]);
                else if constexpr (i == 74) TOUCH(A.x0[1]);
                else if constexpr (i >= 76 && i < 82) TOUCH(km[(2 * (i - 76) + 1) % 12]);
                if constexpr (i == 8 || i == 30) asm volatile("" :: "{v[60:63]}"(ac[0]), "{v[28:31]}"(ac[1]), "{v[32:35]}"(ac[2]), "{v[64:67]}"(ac[3]), "{v[68:71]}"(ac[4]), "{v[72:75]}"(ac[5]));
                if constexpr (i == 14) AC3(0, 0, 0)
                if constexpr (i == 16) AC3(1, 6, 1)
                if constexpr (i == 18) AC3(2, 7, 2)
                if constexpr (i == 20) AC3(-1, -1, 3)
                if constexpr (i == 22) AC3(-1, -1, 4)
                if constexpr (i == 24) AC3(-1, -1, 5)
                if constexpr (i == 66) AC3(3, -1, 6)
                if constexpr (i == 2) { now = rt_async(); HSTOP(1) }
                if constexpr (i == APOS) { PINS(now); an += (unsigned)now; PINS(an); }
                if constexpr (i == APOS + 1) an = spack(an, X.sg);
                SCHED0();
                rd(IC(rt[i])); rd(IC(r2[i]));
                if constexpr (i == APOS + 4) st32(k.rs, 0, pbc, fo[1]);
                if constexpr (i >= 40 && i < 50 && i % 3 == 1) put(IC(((i - 31) / 3) % 6));
                if constexpr (i == 62 || i == 64) put(IC(i / 2 - 30));
                SCHED0();
                if constexpr (i < 25 && i % 4 != 1) B.out<((i - 2) / 4) * 3 + (i - 2) % 4, 6145>(dq, dq);
                if constexpr (i == APOS + 3) { fo[1] = an; PIN(fo[1]); }
                if constexpr (i >= 37 && i < 49) { constexpr int tt = ((i - 28) / 3) % 6; fold_op<(i - 37) % 3>(ac[tt], fo[tt]); }
                if constexpr (i >= 59 && i < 62) fold_op<i - 59>(ac[1], fo[1]);
                if constexpr (i == 62) fold_op<0>(ac[2], fo[2]);
                if constexpr (i == 63) asm volatile("v_add_f32 %1, %1, %2\n\tv_cvt_pk_bf16_f32 %0, %3, %1" : "=v"(fo[2]), "+v"(ac[2][1]) : "v"(ac[2][3]), "v"(ac[2][0]));
                if constexpr (i == 33) S.mask<0>(sc);
                if constexpr (i >= 51 && i < 59) S.mask<i - 50>(sc);
                if constexpr (i >= 75) S.op<i - 75>(k, sc, a0, a1, hv);
                if constexpr (i == 12) { AQ(3, 5) }
                if constexpr (i == 43) { AQ(3, 6) }
                if constexpr (i == 59) { AQ(3, 7) }
                SCHED0();
            });
        }
        QSTAMP(0, 2)
        {
            unsigned d0;
            auto rest = [&](auto J) { constexpr int j = CV(J);
                if constexpr (j >= 0 && j < 12 && (j % 6 == 0 || j % 3 == 2)) X.op<3 + j / 6, (j % 6 + 1) / 3>(k); };
            sfor<43>([&](auto F) { constexpr int f = CV(F) + 5;
                constexpr int e = f < 6 ? f + 3 : f < 9 ? f - 3 : f;
                A.mm<0, false, e / 6, e % 6>(W);
                if constexpr (f % 6 == 2 || f == 5) TOUCH(A.x1[f / 6]);
                else if constexpr (f == 47) { TOUCH(B.x0[0]); AQ(1, 6) AQ(2, 1) }
                else if constexpr (f % 6 == 5) TOUCH(A.x0[f / 6 + 1]);
                if constexpr (f == 40) { done = rt_async(); QSTAMP(0, 3) AQ(1, 5) }
                if constexpr (f == 21) set_m0(X.dst + 2 * 8192);
                if constexpr (f == 24) { X.sb = X.se + 2 * 16384; PINS(X.sb); }
                if constexpr (f == 42) AC3(4, -1, 7)
                SCHED0();
                if constexpr (f % 6 == 4 && f < 36) A.r0<f / 6 + 2>();
                if constexpr (f % 6 == 1 && f < 42) A.r1<f / 6 + 1>();
                if constexpr (f >= 39 && f < 43) B.rd<true, ((f - 39) / 2) * 6 + (f - 39) % 2>();
                if constexpr (f == 26 || f == 31) X.op<2, f / 15>(k);
                SCHED0();
                S.op<f + 3>(k, sc, a0, a1, hv);
                rest(IC(f - 43));
                if constexpr (f == 44) B.lwv = vadd(l16, L_W + w * 1024);
                SCHED0(); });
            B.go<30, true, 0, 48, 8>(W, [&](auto F) { constexpr int f = CV(F);
                rest(IC(f + 5));
                if constexpr (f == 9) AC3(5, -1, -1)
                if constexpr (f == 2) { a0 = (X.sg & 3) * PR_SZ; PINS(a0); }
                if constexpr (f == 4) { a1 = a0 + po1; a0 += po0; PINS(a0); PINS(a1); }
                if constexpr (f == 8) { S.sg = (X.sg & 4) ? 0xBF800000u : 0x3F800000u; PINS(S.sg); }
                if constexpr (f == 10) { S.hs = S.sg - hw2; PINS(S.hs); }
                if constexpr (f == 14 || f == 16) G.op<f / 16>(k, l16, X.sg);
                if constexpr (f == 22 || f == 28 || f == 32) X.next<(f - 22) / 5>(k, l16);
                if constexpr (f == 35) asm volatile("s_waitcnt vmcnt(6)\n\ts_barrier" ::: "memory");
                if constexpr (f == 20) AC3(6, 1, -1)
                if constexpr (f == 26) A.home<0>();
                if constexpr (f >= 36) A.rd<false, f + 12>();
                if constexpr (f == 38 || f == 40 || f == 45 || f == 46) X.next<(f == 38 ? 5 : f == 40 ? 6 : f - 42)>(k, l16);
                if constexpr (f == 47) { A.pre<false, 8>(); AQ(1, 7) AQ(2, 2) } });
            A.go<0, false, 48, 120, 20>(W, [&](auto F) { constexpr int f = CV(F), i = f - XPOS;
                if constexpr (f == 56) B.home<1>();
                if constexpr (i == 0 || i == 2 || i == 6) X.next<7 + i / 2>(k, l16);
                if constexpr (i >= 3 && i < 15 && ((i - 3) % 6 == 0 || (i - 3) % 3 == 2)) X.op<(i - 3) / 6, ((i - 3) % 6 + 1) / 3>(k);
                if constexpr (f == 58) asm volatile("s_waitcnt vmcnt(1)\n\ts_barrier" ::: "memory");
                if constexpr (f == 57) AC3(7, 2, -1)
                G.ask<f - RPOS>(k, l16);
                if constexpr (f >= 108) B.rd<true, f - 60>();
                SCHED0();
                G.red<f - 110>();
                if constexpr (f == 116) asm volatile("v_bfe_u32 %0, %1, 8, 1\n\tv_lshl_or_b32 %0, %0, 3, %1" : "=&v"(d0) : "v"(l16));
                if constexpr (f == 118) asm volatile("v_xor_b32 %0, %1, %0" : "+v"(d0) : "s"(L_K + (w >> 1) * 3072 + (w & 1) * 8));
                if constexpr (f == 119) { B.pre<true, 8>(); AQ(2, 3) } });
            B.go<30, true, 48, 120, 20>(W, [&](auto F) { constexpr int f = CV(F), i = f - 48;
                constexpr int ko[26] = {0, 1, 2, 4, 3, 6, 5, 8, 7, 9, 10, 11, 12, 13, 14, 16, 15, 18, 17, 20, 19, 21, 22, 23, -1, -1};
                G.hold<f - GPOS + 5>(X.sg);
                if constexpr (i < 24) { SCHED0(); A.out4<ko[i % 26], 0>(d0); }
                G.at<f - GPOS>(k, l16);
                if constexpr (f == 114) { lgkm0(done); PINS(G.rt); }
                if constexpr (f == 115) { PINS(now); HTRACE(X.sg - 2) an = (unsigned)done - (unsigned)now; PINS(an); }
                if constexpr (f == 116) { an = min(an, 200u); PINS(an); }
                if constexpr (f == 117) { an += DQ; PINS(an); }
                SCHED0();
                if constexpr (i == 28) AC3(-1, 3, -1)
                if constexpr (i == 50) AC3(-1, 4, -1)
                if constexpr (i == 62) AC3(-1, 5, -1)
                if constexpr (i == 23) { AQ(2, 4) }
                if constexpr (i == 47) { AQ(2, 5) }
                if constexpr (i == 65) { AQ(2, 6) }
                if constexpr (f == 119) { AQ(2, 7) }
                if constexpr (f == 112) asm volatile("" :: "{v[12:15]}"(G.v0), "{v[16:19]}"(G.v1), "{v[20:23]}"(G.p0), "{v[24:27]}"(G.p1), "{v9}"(B.lwv) TRHINT);
                if constexpr (f == 119) G.also(X.off);
                if constexpr (f >= 113 && f < 119) clr2(ac[(f + 6 - 113 + 5) % 6]); });
            static_assert(GPOS >= 70 && GPOS <= 98 && XPOS % 6 == 2 && RPOS > XPOS && RPOS + 1 < XPOS + 5, "GPOS");
            QSTAMP(0, 4) QSTAMP(0, 5)
            HFLUSH(s)
        }
        AST(0)
        PROF_ONLY(G.wait<0>();)
        HSTOP(0) HSTAMP(s, 0) AQ(1, 1) AQ(3, 1)
        G.all();
        SCHED0();
        smfw<50>(ac[5], __builtin_bit_cast(bf16x8, G.p0), W[50], ix);
        AQ(3, 2)
        SCHED0();
    } while (G.bad == 0);
    } while (X.sg != 65u);
    PROF_OUT
}

#define OIDX(t, b) (22 + (t) * 3 + (b))
DEVI void vo_role(const Params& p, Ctx& k, char* smem, int h) {
    PROF_DECL
    const int w = k.w;
    bf16x16 W[52];
    unsigned l16, lwv, ix, dq0, trp, vo;
    {
        const int l = k.l, g = l >> 4, n = l & 15, q = (l >> 2) & 3, pp = l & 3;
        l16 = l * 16; lwv = l16 + L_W + w * 1024; ix = SPIX(l);
        dq0 = L_QM + (24 * w + (n >> 2) * 2 + (n & 1)) * 32 + g * 8 + ((n >> 1) & 1) * 4;
        trp = L_QM + (16 * (g >> 1) + 4 * (g & 1) + 8 * (q >> 1) + (q & 1)) * 32 + pp * 8;
        vo = EXP(h * 512) + (g >> 1) * 256 + n * 16 + (g & 1) * 8;
        u32x8 r = {dq0, trp, vo, ix, l16, lwv, 0u, 0u};
        PIN(r);
        dq0 = r[0]; trp = r[1]; vo = r[2]; ix = r[3]; l16 = r[4]; lwv = r[5];
    }
    Gather G;
    G3 V;
    Xn X;
    V.l16 = l16; V.lwv = lwv; X.l16 = l16;
    f32x4 y[20];
    bf16x8 v[12];
    u32x4 o[10];
    unsigned yb;
    {
        const int l = k.l;
        bf16x8 wt[16];
        bf16x4 q[10][4];
        f32x4 z[9][2];
        u32x2 gv;
        unsigned ra[6];
        const u16* wo = p.Wo + (size_t)h * 192 * 640;
        constexpr int L_V = L_XN + 24576;
#define VEX(v) ((v) < 0 ? 0 : ((v) < 6) + ((v) == VGS) + 10 * ((v) == VGS + 2) + 6 * ((v) == VGS + 6 || (v) == 18))
#define VKE(v) ((v) < VGS + 4 ? 0 : CMIN(20, 7 * ((v) - VGS - 3)))
#define YKE(v) ((v) < 16 ? 0 : 3 * (v) - 45)
        auto put = [&](int t) { st128(k.rs, X.l16, yb + t * 65536, (u32x4){cvtpk(z[t][0][0], z[t][0][1]), cvtpk(z[t][0][2], z[t][0][3]), cvtpk(z[t][1][0], z[t][1][1]), cvtpk(z[t][1][2], z[t][1][3])}); };
        auto rdx = [&](auto K) { constexpr int ks = CV(K); if constexpr (ks < 20) { V.r0<ks % 20>(); V.r1<ks % 20>(); if constexpr (ks < 14) V.rw<ks>(); } };
        auto rdv = [&](auto J) { constexpr int j = CV(J) % 6; v[j] = lr128(l16 + L_V + j * 1024); v[6 + j] = lr128(l16 + L_V + (6 + j) * 1024); };
        cstream<20>([](auto V) { constexpr int v = CV(V); return IC(VEX(v - 1) + (v < 19 ? 6 : 0)); },
            [&](auto U) { constexpr int u = CV(U), k0 = VKE(u - 1), y0 = YKE(u - 1), n = u < 16 ? (VKE(u) - k0) * 6 : u < 18 ? 36 : 18, nn = n ? n : 12, nr = u < 10 ? 3 : u < 16 ? 6 : 4;
                if constexpr (u < 6) hello(k, 0);
                if constexpr (u == VGS) gv = go_ask(k);
                if constexpr (u == VGS + 2) { PIN(gv); go_wait(k, 0, gv); X.init(k); X.l16 = k.c ? OOB : l16; xn_all(k, X.l16, X.src, X.dst); }
                if constexpr (u == VGS + 4) { rdx(IC(0)); rdx(IC(1)); }
                if constexpr (u == VGS + 6) { sfor<3>([&](auto I) { X.next<CV(I)>(k, l16); }); X.se = X.src; yb = k.yx(0, w) + EXP(h * 1024); }
                if constexpr (u == 16) rdv(IC(0));
                XSTAMP(u)
                if constexpr (u == 0 || u == 10 || u == 16) {
#pragma unroll
                    for (int t = 0; t < nr; ++t) ra[t] = opq((u == 0 ? wp_adr<384, true>(w * 3 + t, l) : u == 10 ? wp_adr<768, false>(w * 6 + t, l) : wp_adr<512, false>(w * 4 + t, l)) + SLOT(0));
                }
                unsigned rb[6];
#pragma unroll
                for (int t = 0; t < nr; ++t) rb[t] = slot_adr<u>(ra[t]);
                SCHED0();
                sfor<nn>([&](auto I) { constexpr int i = CV(I), e = u == VGS + 6 ? 9 : 0, j = ((i - e) * 6) / (nn - e), d = u + 2, r0 = u == 18 ? 6 : 0;
                    if constexpr (u < 16 && n > 0) { V.mm<0, true, k0 + i / 6, i % 6>(W); if constexpr (i % 6 == 3) rdx(IC(k0 + i / 6 + 2)); }
                    if constexpr (u >= 16) { constexpr int m = n / 3, ks = (y0 + i / m) % 6, t = (u / 18) * 6 + (i % m) / 2, rt = i & 1;
                        if constexpr (!(ks == 5 && t == 7 && rt)) { mfw<OIDX(t, ks >> 1), ks & 1, true, ks == 0>(z[t][rt], W[OIDX(t, ks >> 1)], v[rt * 6 + ks]); SCHED0(); }
                        if constexpr (i % m == 1 && u < 19) rdv(IC(ks + 1));
                        if constexpr (u == 19 && i < 2) rdv(IC(4 + i));
                        if constexpr (u == 18 && i >= 3 && i < 15 && (i & 1)) put((i - 3) / 2);
                        if constexpr (u == 18 && i < 6) lw128(lwv + (10 + i) * 4096, __builtin_bit_cast(u32x4, wt[10 + i])); }
                    if constexpr (i < e) X.op<i / 3, i % 3>(k);
                    else if constexpr (u > 0 && u < 18 && (i == e || ((i - e - 1) * 6) / (nn - e) != j)) {
                        if constexpr (d < 10) dma1<SLOT(d) + (j / 3) * 12288 + (j % 3) * 1024>(p.Wv + (size_t)(2 * d + j / 3) * 32 * 6144 + h * 192, wsrc<384, true>(w * 192 + (j % 3) * 64 + l, 12288), w * 3072);
                        else if constexpr (d < 16) dma1<SLOT(d) + j * 1024>(wo + (d - 10) * 32 * 640, wsrc<768, false>(w * 384 + j * 64 + l, 1280), w * 6144);
                        else dma1<SLOT(d) + j * 1024>(wo + (d - 16) * 48 * 640 + 384, wsrc<512, false>(w * 384 + j * 64 + l, 1280), w * 6144);
                    }
                    if constexpr (i < 2 && u >= 3 && u < 8) lw128(lwv + (2 * u - 6 + i) * 4096, __builtin_bit_cast(u32x4, wt[(2 * u - 6 + i) & 15]));
                    sfor<2>([&](auto C) { constexpr int c = u == 19 ? 2 * i + CV(C) : i - r0 + CV(C) * 99;
                        if constexpr (c >= 0 && c < 12) {
                            constexpr int t = u < 10 ? c >> 2 : u < 16 ? c >> 1 : 6 + c % 4, r = u < 10 ? c & 3 : u < 16 ? (u - 10) * 2 + (c & 1) : (u - 16) * 3 + c / 4, id = u < 10 ? (t * 10 + u) % 52 : OIDX(t, r >> 2);
                            q[t][r & 3] = lrtr(u < 10 ? rb[t] + (r >> 1) * 12288 + (r & 1) * 1536 : u < 16 ? rb[t] + (c & 1) * 12288 : rb[(t - 6) & 3] + (c / 4) * 8192);
                            if constexpr (u < 10 && u >= 2 && t == 2) { if constexpr (r & 1) V.wl[2 * u + r / 2] = wt[(2 * u - 4 + r / 2) & 15] = CAT4(q[t][r - 1], q[t][r]); }
                            else if constexpr ((r & 3) == 1) W[id] = __builtin_shufflevector(q[t][0], q[t][1], 0, 1, 2, 3, 4, 5, 6, 7, -1, -1, -1, -1, -1, -1, -1, -1);
                            else if constexpr ((r & 3) == 3) W[id] = CAT8(LO8(W[id]), CAT4(q[t][2], q[t][3]));
                        } });
                    SCHED0(); });
                if constexpr (u == VGS + 7) sfor<18>([&](auto I) { V.out<CV(I), 0>(l16 + L_V + ((3 * w) >> 1) * 1024 + ((3 * w) & 1) * 8, l16 + L_V + ((3 * w + 1) >> 1) * 1024 + ((3 * w + 1) & 1) * 8); }); });
        static_assert(VGS >= 6 && VGS < 9, "VGS");
        RAWBAR()
        XSTAMP(20)
        sfor<2>([&](auto B) { X.op<CV(B) + 3, 0>(k); asm volatile("s_nop 0"); X.op<CV(B) + 3, 1>(k); X.op<CV(B) + 3, 2>(k); });
        asm volatile("" : "+v"(z[8][0]), "+v"(z[8][1]), "+v"(z[7][0]), "+v"(z[6][1]));
        put(6); put(8);
        lw32(l16 + L_MEM, ~0u);
        o[7] = (u32x4){cvtpk(z[7][0][0], z[7][0][1]), cvtpk(z[7][0][2], z[7][0][3]), 0u, 0u};
        y[17] = z[7][1];
        yb += 7 * 65536;
        X.off = 0; X.l16 = l16;
        if (k.c) X.init(k);
    }
    RSTAMP(2)
    const unsigned pdum = k.pg(1) + DUMMY_B, pbc = w == 0 ? k.p0 + CNT(C_BC, (h >> 3) * 16 + 8 + (h & 7)) : pdum + 98304 + h * 512 + w * 128;
    unsigned long long now = 0, done = 0;
    G.mx = ~0u;
    G.init(k);
    G.base -= 32768;
    const unsigned ypar = (unsigned)(k.par >> (PG_YX + (w >> 1))), yo = ((unsigned)(PG_YX + (w >> 1)) << 21) + (w & 1) * 1048576 + EXP(h * 1024);
    const unsigned ym0 = k.ym(0) + w * 163840, ym2 = k.ym(2) + w * 163840;
    unsigned ym = k.c ? pdum + w * 163840 : ym0, ys, t, c = ~0u, an = 80;
#define YT(u) ((u) < 10 ? 2 * ((u) % 5) : ((u) - 10) | 1)
#define YR(u) ((u) < 10 ? (u) / 5 : (u) & 1)
    auto yout = [&](auto U, auto J) { constexpr int u = CV(U), j = CV(J), t = YT(u), rt = YR(u);
        if constexpr (j == 0) { o[t][2 * rt] = cvtpk(y[u][0], y[u][1]); o[t][2 * rt + 1] = cvtpk(y[u][2], y[u][3]); TOUCH(o[t][2 * rt]); TOUCH(o[t][2 * rt + 1]); }
        if constexpr (j == 1 && rt) __builtin_amdgcn_raw_buffer_store_b128(o[t], k.rs, (int)l16, (int)yb, AUX_SC1);
        if constexpr (j == 2 && rt) { yb += t == 8 ? -7 * 65536 : 131072; PINS(yb); TOUCH(o[t]); } };
    auto ymm = [&](auto I) {
        constexpr int i = CV(I), ks = i % 6, u = i / 6, id = OIDX(YT(u), ks >> 1);
        mfw<id, ks & 1, true, ks == 0>(y[u], W[id], v[YR(u) * 6 + ks]);
        SCHED0();
        if constexpr (u > 0 && ks >= 2) yout(IC((u + 19) % 20), IC(ks - 2)); };
    if (k.c) X.late<5>(k, l16);
    do {
        PROF_S(X.sg - 1)
        f32x4 ac[3];
        sfor<13>([&](auto I) { constexpr int i = CV(I) + 107;
            ymm(IC(i));
            if constexpr (i == TB) { asm volatile("s_waitcnt vmcnt(%0)\n\ts_barrier" :: "n"(TB < 111 ? 7 : 8) : "memory"); AST(5) }
            if constexpr (i == TB + 1) st32(k.rs, 0, pbc + 4, G.mx);
            if constexpr (i == 119) V.pre<true, 0>();
            if constexpr (i == 113) G.ask<0>(k, l16);
            if constexpr (i == 119) G.ask<1>(k, l16);
            if constexpr (i == 114) V.r0<0>();
            if constexpr (i == 115) V.r1<0>();
            if constexpr (i == 117) V.r0<1>();
            if constexpr (i == 118) V.r1<1>();
            SCHED0(); });
        HSTAMP(s, 4)
        V.run<0, true, 0, 16, 17, true>(W, [&](auto I) { constexpr int i = CV(I);
            if constexpr (i >= 2) yout(IC(19), IC(i - 2));
            if constexpr (i == 8) { t = (X.sg & 7) * 2; PINS(t); }
            if constexpr (i == 10) { yb = (ypar >> t) & 1; PINS(yb); }
            if constexpr (i == 14) { t = (t << 21) + yo; PINS(t); }
            if constexpr (i == 16) { yb = (yb << 13) + t; PINS(yb); }
            if constexpr (i == 20) { c = X.sg; PINS(c); }
            if constexpr (i == 22) { G.a = max(c, 1u) << 15; PINS(G.a); }
            if constexpr (i == 28) G.op<1>(k, l16, 0);
            if constexpr (i == 40) { an = min(an, 200u) + DV; PINS(an); }
            if constexpr (i == 32 || i == 34 || i == 38) X.next<(i - 31) / 3>(k, l16);
            if constexpr (i == 23 || i == 29) { AAC(4 + i / 29) }
            if constexpr (i == 83 || i == 89) { AAC(6 + i / 89) }
            if constexpr (i == YPOS) { AST(6) }
            if constexpr (i == YPOS + 1) { AST(7) }
            if constexpr (i == YPOS) { asm volatile("s_waitcnt vmcnt(0)\n\ts_barrier" ::: "memory"); }
            if constexpr (i == YPOS - 2) G.mx = vshl1(c, 0);
            if constexpr (i == YPOS + 1) st32(k.rs, 0, pbc + 4, G.mx);
            SCHED0();
            G.red<i - YPOS - 2>();
            G.hold<i - GPOSY * 6 + 5>(c);
            G.at<i - GPOSY * 6>(k, l16);
            if constexpr (i == 90) PINS(G.rt);
            if constexpr (i == 95) V.pre<true, 16>();
            if constexpr (i >= 92 && i < 95) clr2(ac[(i - 92) % 3]); });
        static_assert(GPOSY * 6 + 2 > YPOS + 1 && GPOSY <= 13, "GPOSY");
        HSTAMP(s, 5)
        HFLUSH(s - 1)
        f32x4 acc[10];
        f32x2 z[20];
        auto clr = [&](auto I) { constexpr int i = CV(I); asm volatile("v_mov_b64 %0, 0" : "=v"(z[i])); if constexpr (i & 1) acc[i >> 1] = __builtin_shufflevector(z[i - 1], z[i], 0, 1, 2, 3); };
        bf16x4 al[3], ah[3];
        unsigned d0, d1;
        {
            bf16x8 m[10], wa[10], wb[10];
            auto rd = [&](auto R) { constexpr int r = CV(R), b = 2 + r / 3;
                if constexpr (b < 10) {
                    if constexpr (r % 3 == 0) m[b] = lr128(l16 + MADR(b));
                    if constexpr (r % 3 == 1) wa[b] = lr128(lwv + (b - 2) * 8192);
                    if constexpr (r % 3 == 2) wb[b] = lr128(lwv + (b - 2) * 8192 + 4096);
                } else if constexpr (b < 13) {
                    if constexpr (r % 3 == 0) V.r0<b + 7>();
                    if constexpr (r % 3 == 1) V.rw<b + 7>();
                    if constexpr (r % 3 == 2) V.r1<b + 7>();
                } };
            AST(0)
            G.wait<0>();
            HSTOP(0) HSTAMP(s, 0) AST(1)
            V.run<0, true, 16, 17, 0, true>(W, [&](auto I) { constexpr int i = CV(I);
                G.fin<i>(0);
                if constexpr (i == 3) {
                    if (__builtin_expect(G.bad != 0, 0)) {
                        unsigned spins = 0;
_Pragma("unroll 1")
                        do { PC8(0, c) G.again(k, l16); } while (G.bad != 0 && !spin_fail(k, spins++));
                        HSTOP(0) HSTAMP(s, 0)
                    }
                    asm volatile("s_barrier" ::: "memory");
                    AST(2)
                }
                if constexpr (i == 4) { now = rt_async(); HSTOP(1) SCHED0(); }
                if constexpr (i >= 3) rd(IC(i - 3)); });
            m[0] = __builtin_bit_cast(bf16x8, G.p0); m[1] = __builtin_bit_cast(bf16x8, G.p1);
            sfor<30>([&](auto I) {
                constexpr int i = CV(I), b = i / 3, t = i % 3;
                if constexpr (t == 2 && b >= 2) smf<false>(ac[2], m[b], CAT8(wa[b], wb[b]), ix); else smfw<t * 10 + b>(ac[t], m[b], W[t * 10 + b], ix);
                SCHED0();
                if constexpr (i >= 6 && i < 29 && t != 0) { if constexpr (t == 1) TOUCH(wb[b]); else TOUCH(m[(b + 1) % 10]); }
                if constexpr (i == 29) V.pre<true, 17>();
                if constexpr (i == 2) { PINS(now); an += (unsigned)now; PINS(an); }
                if constexpr (i == 3) an = spack(an, c);
                SCHED0();
                if constexpr (i < 27) rd(IC(3 + i));
                if constexpr (i == 5) st32(k.rs, 0, pbc, d0);
                SCHED0();
                if constexpr (i == 4) { d0 = an; PIN(d0); }
                if constexpr (i == 28) d0 = vadd(l16, L_K + ((3 * w) >> 1) * 1024 + ((3 * w) & 1) * 8);
                if constexpr (i == 29) d1 = vadd(l16, L_K + ((3 * w + 1) >> 1) * 1024 + ((3 * w + 1) & 1) * 8);
                SCHED0();
            });
            unsigned fo[3];
            V.run<0, true, 17, 19, 0, true>(W, [&](auto I) { constexpr int i = CV(I);
                if constexpr (i < 3) { rd(IC(30 + i)); SCHED0(); fold_op<0>(ac[i], fo[i]); }
                if constexpr (i == 3 || i == 4 || i == 6) { constexpr int t = i / 2 - 1; asm volatile("v_add_f32 %1, %1, %2\n\tv_cvt_pk_bf16_f32 %0, %3, %1" : "=v"(fo[t]), "+v"(ac[t][1]) : "v"(ac[t][3]), "v"(ac[t][0])); }
                if constexpr (i == 5 || i == 7 || i == 8) lw32(dq0 + ((i - 4) / 2) * 256, fo[(i - 4) / 2]);
                if constexpr (i == 9 || i == 10) clr2(acc[i - 9]);
                if constexpr (i == 11) BARL(0) });
        }
        HSTAMP(s, 1) AST(3)
        {
            unsigned fo[2];
            auto xq = [&](auto I) { constexpr int i = CV(I) - XP + XS0;
                if constexpr (i == -4) X.next<3>(k, l16);
                if constexpr (i == -1 || i == 0) X.next<14 + i>(k, l16); };
            static_assert(XP % 6 == 5 && XP <= 53 && XP >= 17 && XB > XP + 18 && XB > 63, "XP");
            auto fin2 = [&](auto P, auto J) { fold2<CV(J)>(acc[2 * CV(P)], acc[2 * CV(P) + 1], fo); };
            V.run<0, true, 19, 20, 0, true>(W, [&](auto I) { constexpr int i = CV(I);
                if constexpr (i == 0) { al[0] = lrtr(trp); ah[0] = lrtr(trp + 64); }
                if constexpr (i == 1) al[1] = lrtr(trp + 1024);
                if constexpr (i == 2) ah[1] = lrtr(trp + 1088);
                if constexpr (i == 3) al[2] = lrtr(trp + 2048);
                if constexpr (i == 4) ah[2] = lrtr(trp + 2112);
                SCHED0();
                if constexpr (i >= 1) clr(IC(3 + i)); });
            sfor<30>([&](auto I) { constexpr int i = CV(I), b = (i % 6) >> 1, t = 2 * (i / 6) + (i & 1), id = OIDX(t, b);
                smfw<id>(acc[t], CAT4(al[b], ah[b]), W[id], ix);
                SCHED0();
                if constexpr (i == 1 || i == 3) TOUCH(ah[(i + 1) / 2]);
                if constexpr (i == 29) TOUCH(v[0]);
                if constexpr (i == 21) BARL(0)
                SCHED0();
                if constexpr (i == 4 || i == 7) X.next<(i == 4 ? 5 : 11)>(k, l16);
                xq(IC(i - 30));
                if constexpr (i >= 21 && i < 27) v[i - 21] = lr128(l16 + L_K + (i - 21) * 1024);
                SCHED0();
                V.out<i - 1, 0>(d0, d1);
                SCHED0();
                if constexpr (i % 3 == 0 && i < 18) clr(IC(9 + i / 3));
                if constexpr (i == 16) clr(IC(15));
                if constexpr (i >= 18 && i < 21) clr(IC(i - 2));
                if constexpr (i == 22) clr(IC(19));
                if constexpr (i >= 23 && i < 29) fold_op<(i - 23) / 2>(acc[(i - 23) & 1], fo[(i - 23) & 1]);
                if constexpr (i == 29) { st64(k.rs, vo, ym, (u32x2){fo[0], fo[1]}); SCHED0(); fin2(IC(1), IC(0)); }
                SCHED0(); });
            HSTAMP(s, 2)
            sfor<107>([&](auto I) { constexpr int i = CV(I), q = i < 8 ? i + 4 : i + 3;
                ymm(I);
                if constexpr (i < 5 || (i >= 29 && i < 35)) TOUCH(v[(i < 5 ? i + 1 : i - 23) % 12]);
                if constexpr (i < 13 && i != 8) {
                    if constexpr (q % 3 == 1) { ym += 32768; PINS(ym); }
                    if constexpr (q % 3 == 2 && i >= 5) __builtin_amdgcn_s_setprio(0);
                    SCHED0();
                    if constexpr (q % 3 == 0) { st64(k.rs, vo, ym, (u32x2){fo[0], fo[1]}); SCHED0(); }
                    if constexpr (q < 15) fin2(IC(q / 3), IC(q % 3));
                }
                if constexpr (i == 13) done = rt_async();
                if constexpr (i >= 15 && i < 23 && i != 18 && i != 20) v[6 + i - 15 - (i > 18) - (i > 20)] = lr128(l16 + L_K + (6 + i - 15 - (i > 18) - (i > 20)) * 1024);
                xq(IC(i));
                if constexpr (i == XP - 6) X.next<15>(k, l16);
                if constexpr (i == XP - 1) { SCHED0(); X.next<7>(k, l16); }
                if constexpr (i == XP) X.next<12>(k, l16);
                if constexpr (i > XP && i <= XP + 18 && (i % 6 == 0 || i % 3 == 1)) X.op<(i - XP - 1) / 6, (i % 6 + 2) / 3>(k);
                { constexpr int xp[6] = {XB, XB + 1, XB + 6, XB + 11, XB + 12, XB + 18};
                  sfor<6>([&](auto Q) { if constexpr (xp[CV(Q)] == i) X.op<3 + CV(Q) / 3, CV(Q) % 3>(k); }); }
                if constexpr (i == 90) { ym = (c & 2) ? ym2 : ym0; PINS(ym); }
                if constexpr (i == 91) { ys = ym + EXP(327680); PINS(ys); }
                if constexpr (i == 96) { ym = (c & 1) ? ys : ym; PINS(ym); }
                if constexpr (i == 60) { HSTAMP(s, 3) }
                if constexpr (i == 42 || i == 43 || i == 48 || i == 49) { AAC((i - 42) / 3 + (i & 1)) }
                if constexpr (i == 102) lgkm0(done);
                if constexpr (i == 103) { PINS(now); HTRACE(c - 1) an = (unsigned)done - (unsigned)now; PINS(an); }
                if constexpr (i == 105) G.mx = vshl1(c, 1);
                SCHED0(); });
            AST(4)
        }
    } while (X.sg != 65u);
    HFLUSH(63)
    PROF_OUT
}

DEVI void row_role(const Params& p, const Ctx& k, char* smem, int rt) {
    PROF_DECL
    const int l = k.l, n = l & 15;
    const unsigned fo = k.w * 2048 + rt * 1024;
    RSTAMP(2)
    const __amdgpu_buffer_rsrc_t rs = __builtin_amdgcn_make_buffer_rsrc(k.c ? (void*)k.ws : (void*)p.x, 0, 0x7fffffff, 0x00020000);
    const unsigned vs = k.c ? l * 16 : n * 1280 + (l >> 4) * 16, cs = k.c ? 16384 : 256;
    unsigned ep = n * 32, dp = (l >> 4) == (n >> 2) ? n * 32 + k.w * 8 : 1024 + l * 8;
    const unsigned long long m1 = __ballot(n & 1), m2 = __ballot(n & 2);
    bf16x8 one8 = {0x3F80, 0x3F80, 0x3F80, 0x3F80, 0x3F80, 0x3F80, 0x3F80, 0x3F80};
    PIN(one8);
    unsigned fails = k.c ? 0u : 65536u;
    prog_init(k);
    for (int sg = k.c ? 0 : 8; sg < 64; ++sg) {
        unsigned src[5], dst = k.seg(PG_XNA, sg) + fo;
#pragma unroll
        for (int c = 0; c < 5; ++c) { src[c] = (k.c ? k.seg(PG_X2, sg) + fo : (sg * 32 + rt * 16) * 1280 + k.w * 64) + c * cs; PINS(src[c]); }
        PINS(dst);
        prog_wait<5>(k, k.p0 + CNT(C_X2, rt * 5), k.c ? sg : -1);
        STOP(sg) STAMP(sg, 0)
        u32x4 a[5];
        f32x4 sm, sq;
        f32x2 v[20];
        unsigned long long bad;
        do {
            asm volatile("" ::: "memory");
#pragma unroll
            for (int c = 0; c < 5; ++c) { a[c] = ld128(rs, vs, src[c]); SCHED0(); }
            unsigned mx = 0, lim = ~1u | (fails >> 16);
            ++fails; PINS(fails); PINS(lim);
            sfor<5>([&](auto C) { constexpr int c = CV(C);
                mfa<false, c == 0>(sq, __builtin_bit_cast(bf16x8, a[c]), __builtin_bit_cast(bf16x8, a[c])); mfa<false, c == 0>(sm, one8, __builtin_bit_cast(bf16x8, a[c]));
                mx = umax3(umax3(mx, a[c][0], a[c][1]), a[c][2], a[c][3]); PIN(mx);
                SCHED0(); });
            bad = __ballot(mx > lim);
        } while (__builtin_expect(bad != 0, 0));
        STAMP(sg, 1)
        auto unp = [&](int i0, int i1) {
#pragma unroll
            for (int i = i0; i < i1; ++i) { v[i] = (f32x2){blo(a[i >> 2][i & 3]), bhi(a[i >> 2][i & 3])}; PIN(v[i]); }
            SCHED0(); };
        unp(0, 3);
        asm volatile("" : "+v"(sq), "+v"(sm));
        lw64(dp, (u32x2){__float_as_uint(sm[0]), __float_as_uint(csel(csel(sq[0], sq[1], m1), csel(sq[2], sq[3], m1), m2))});
        SCHED0();
        unp(3, 10);
        RAWBAR()
        const f32x4 e0 = __builtin_bit_cast(f32x4, lr128(ep)), e1 = __builtin_bit_cast(f32x4, lr128(ep + 16));
        SCHED0();
        unp(10, 20);
        const f32x4 e = e0 + e1;
        float m = (e[0] + e[2]) * (1.f / 640.f);
        const float var = bfr(__builtin_fmaxf(__builtin_fmaf(-m, m, (e[1] + e[3]) * (1.f / 640.f)), 0.f));
        m = bfr(m);
        const float rden = __builtin_amdgcn_rcpf(bfr(__builtin_amdgcn_sqrtf(bfr(var + 1e-5f))));
#pragma unroll
        for (int c = 0; c < 5; ++c) {
            u32x4 o;
#pragma unroll
            for (int i = 0; i < 4; ++i) { const f32x2 x = v[c * 4 + i] - m; o[i] = cvtpk(bfr(x[0]) * rden, bfr(x[1]) * rden); }
            st128(k.rs, l * 16, dst + c * 16384, o);
        }
        STAMP(sg, 2)
        unsigned fv = sg - (k.c ? 0 : 8);
        PIN(fv);
        DRAIN();
        st32(k.rs, l == 0 ? 0u : OOB, k.p0 + CNT(C_XN, rt * 4 + k.w), fv);
        dp ^= 2048; ep ^= 2048;
        STAMP(sg, 3) STAMP(sg, 4) STAMP(sg, 5)
        PFLUSH(sg)
    }
    PROF_OUT
}

DEVI int red_col(int j) { const int w = j / 5, q = j % 5; return q < 3 ? 3 * w + q : 9 + 2 * w + q; }
struct Red {
    u32x4 Y[16];
    template <int I0, int I1> DEVI void yload(const Ctx& k, int j, int sg) {
        const unsigned a = k.yx(sg, j / 5) + (j % 5) * 131072 + k.w * 16384;
#pragma unroll
        for (int i = I0; i < I1; ++i) Y[i] = ld128(k.rs, k.l * 16 + (i & 3) * 1024, a + ((i >> 2) & 1) * 4096 + (i >> 3) * 65536);
    }
};
DEVI unsigned y_get(const Ctx& k) { return ld32(k.rs, (k.l & 7) * 128, k.p0 + CNT(C_BC, k.w * 16 + 8) + 4); }
DEVI void y_wait(const Ctx& k, int s) {
_Pragma("unroll 1")
    for (unsigned spins = 0; !__all((int)y_get(k) >= s); ++spins) {
        asm volatile("" ::: "memory");
        __builtin_amdgcn_s_sleep(2);
        if (spin_fail(k, spins)) break;
    }
    __builtin_amdgcn_fence(__ATOMIC_ACQUIRE, "wavefront");
}
DEVI void wait_until(unsigned t) { while ((int)((rclk() - t) << 16) < 0) { if (WUS) __builtin_amdgcn_s_sleep(WUS); } }
#define DPPMAX(x, c) x = max(x, __builtin_amdgcn_update_dpp(0, x, c, 0xf, 0xf, false))
#define TSLP 1
#define TWU 0
DEVI void wait_until2(unsigned t) { while ((int)((rclk() - t) << 16) < 0) { if (TWU) __builtin_amdgcn_s_sleep(TWU); } }
#define WAIT_BC(s, BSLP) { \
    unsigned bc; \
    _Pragma("unroll 1") for (;; ++spins) { \
        bc = ld32(k.rs, (l & 15) * 128, k.p0 + CNT(C_BC, w * 16)); \
        DRAIN(); \
        if (__all(((int)bc >> 16) > s)) break; \
        __builtin_amdgcn_s_sleep(BSLP); \
        if (spin_fail(k, spins)) break; \
    } \
    const unsigned now = rclk(); \
    int d = (int)((bc - now) << 16); \
    d = ((int)bc >> 16) > s + 1 || d > (256 << 16) ? -(64 << 16) : d; \
    DPPMAX(d, 0xB1); DPPMAX(d, 0x4E); DPPMAX(d, 0x141); \
    tq = now + (__builtin_amdgcn_readlane(d, 0) >> 16); tv = now + (__builtin_amdgcn_readlane(d, 8) >> 16); }
DEVI void mred_role(const Params& p, const Ctx& k, char* smem, int j) {
    PROF_DECL
    const int l = k.l, w = k.w, g = l >> 4, n = l & 15;
    const unsigned ix = SPIX(l);
    const unsigned vp = (g * 16 + ((n >> 1) & 1) * 8 + (n >> 2) * 2 + (n & 1)) * 16;
    const unsigned v16 = (g >> 1) * 1024 + (g & 1) * 256 + n * 16, yo = j * 32768 + EXP(w * 4096);
    const u32x4 ones = {0xFFFFFFFFu, 0xFFFFFFFFu, 0xFFFFFFFFu, 0xFFFFFFFFu};
    auto arm = [&](int s) {
#pragma unroll
        for (int i = 0; i < 4; ++i) st128(k.rs, l * 16 + i * 1024, k.ym(s) + yo, ones);
    };
    const unsigned pv = l == 0 && j % 5 == 0 ? (j / 5) * 128 + w * 32 : OOB;
    Red R;
    RSTAMP(2)
    arm(1);
#pragma unroll
    for (int i = 0; i < 8; ++i) st128(k.rs, (i || w > 1 || g > 1) ? g * 32768 + n * 16 : OOB, k.memf(i * 8 + (w >> 1) * 4) + EXP((2 * j + (w & 1)) * 256), ones);
#pragma unroll
    for (int i = 0; i < 4; ++i) { const unsigned q = min(i * 80 + j * 4 + w, 287); st128(k.rs, l * 16, k.p0 + PR_B + PR_SZ + EXP(q * 1024), ones); }
    sleep_until(k.t0 + RSL);
    y_wait(k, 2);
    R.yload<0, 16>(k, j, 0);
    for (int s = 0; s < 63; ++s) {
        STOP(s) HSTAMP(s, 0)
        const unsigned ap = k.p0 + PR_B + (s & 3) * PR_SZ + w * 49152, am = k.ym(s) + yo;
        const unsigned ph = (s & 4) ? 0x80008000u : 0u;
        unsigned spins = 0, tq, tv;
        unsigned fl;
        WAIT_BC(s, BSLP)
        st32(k.rs, pv, k.p0 + CNT(C_XB, 0), spack(((int)((tq - tv) << 16) < 0 ? tv : tq) + RDY, s + 2));
        STAMP(s, 5)
        char* rb = smem + (s & 1) * 4096;
_Pragma("unroll 1")
        for (spins = 0;; ++spins) {
            u32x4 ym[4], pm[5];
            f32x4 acc[2];
            if (spins == 0) wait_until(tv);
            asm volatile("" ::: "memory");
#pragma unroll
            for (int i = 0; i < 4; ++i) ym[i] = ld128(k.rs, v16 + (i >> 1) * 2048 + (i & 1) * 512, am);
            fl = ld32(k.rs, 0, (k.p0 ^ 8192u) + CNT(C_X1, j));
            asm volatile("" ::: "memory");
            if (spins == 0) { wait_until(tq); STAMP(s, 1) }
            asm volatile("" ::: "memory");
#pragma unroll
            for (int i = 0; i < 5; ++i) pm[i] = ld128(k.rs, i < 4 ? l * 16 + i * 1024 : vp, ap + (i >> 2) * 4096);
            acc[0] = (f32x4){0.f, 0.f, 0.f, 0.f}; acc[1] = acc[0];
            const u32x8 y0 = {ym[0][0], ym[1][0], ym[0][2], ym[1][2], ym[2][0], ym[3][0], ym[2][2], ym[3][2]};
            const u32x8 y1 = {ym[0][1], ym[1][1], ym[0][3], ym[1][3], ym[2][1], ym[3][1], ym[2][3], ym[3][3]};
            unsigned mx = 0;
#pragma unroll
            for (int i = 0; i < 4; ++i) mx = max(mx, umax4(ym[i]));
            unsigned tg = mx == 0xFFFFFFFFu ? 0x80000000u : 0u;
            asm volatile("s_nop 1" : "+v"(acc[0]), "+v"(acc[1]), "+v"(tg) : "v"(y0), "v"(y1), "v"(ix));
            SCHED0();
#pragma unroll
            for (int i = 0; i < 5; ++i) {
                const unsigned u0 = __builtin_amdgcn_bitop3_b32(pm[i][0], pm[i][1], ph, 0x7e), u1 = __builtin_amdgcn_bitop3_b32(pm[i][2], pm[i][3], ph, 0x7e);
                SCHED0();
                if (i < 4) smf<false>(acc[0], __builtin_bit_cast(bf16x8, pm[i]), __builtin_bit_cast(bf16x16, __builtin_shufflevector(R.Y[2 * i], R.Y[2 * i + 1], 0, 1, 2, 3, 4, 5, 6, 7)), ix);
                else smf<false>(acc[0], __builtin_bit_cast(bf16x8, pm[4]), __builtin_bit_cast(bf16x16, y0), ix);
                tg = (tg | u0) | u1;
                SCHED0();
                if (i < 4) smf<false>(acc[1], __builtin_bit_cast(bf16x8, pm[i]), __builtin_bit_cast(bf16x16, __builtin_shufflevector(R.Y[8 + 2 * i], R.Y[9 + 2 * i], 0, 1, 2, 3, 4, 5, 6, 7)), ix);
                else smf<false>(acc[1], __builtin_bit_cast(bf16x8, pm[4]), __builtin_bit_cast(bf16x16, y1), ix);
                SCHED0();
            }
            const unsigned long long bad = __ballot((int)tg < 0);
            asm volatile("s_nop 5" : "+v"(acc[0]), "+v"(acc[1]) : "s"(bad));
#pragma unroll
            for (int t = 0; t < 2; ++t) { float* rp = (float*)(rb + t * 2048 + w * 512 + g * 128 + n * 4); rp[0] = acc[t][0] + acc[t][2]; rp[16] = acc[t][1] + acc[t][3]; }
            if (bad == 0) break;
            PCOUNT(0)
            asm volatile("" ::: "memory");
            if (spin_fail(k, spins)) break;
        }
        STAMP(s, 2)
        RAWBAR()
        if (w < 2) {
            const char* rp = rb + w * 2048 + ((l >> 4) * 2 + ((l >> 2) & 1)) * 64 + ((l & 3) * 4 + ((l >> 3) & 1) * 2) * 4;
            const f32x2 a = *(const f32x2*)rp, b = *(const f32x2*)(rp + 512), c = *(const f32x2*)(rp + 1024), d = *(const f32x2*)(rp + 1536);
            const f32x2 r = (a + b) + (c + d);
            st32(k.rs, l * 4, k.memf(s + 1) + EXP((2 * red_col(j) + w) * 256), min(cvtpk(r[0], r[1]) ^ ph, 0xFFFFFFFEu));
        }
        STAMP(s, 3)
_Pragma("unroll 1")
        for (spins = 0; (int)__builtin_amdgcn_readfirstlane(fl) < s - 1; ++spins) {
            PCOUNT(1)
            __builtin_amdgcn_s_sleep(PSLEEP);
            if (spin_fail(k, spins)) break;
            fl = ld32(k.rs, 0, (k.p0 ^ 8192u) + CNT(C_X1, j));
        }
        __builtin_amdgcn_fence(__ATOMIC_ACQUIRE, "wavefront");
        y_wait(k, 2 * s + 3);
        R.yload<0, 8>(k, j, s + 1);
        arm(s + 2);
        y_wait(k, 2 * s + 4);
        STAMP(s, 4)
        R.yload<8, 16>(k, j, s + 1);
        PFLUSH(s)
    }
    PFLUSH(63)
    PROF_OUT
}
DEVI void tred_role(const Params& p, const Ctx& k, char* smem, int j) {
    PROF_DECL
    const int l = k.l, w = k.w, g = l >> 4, n = l & 15, T = 2 * red_col(j) + (w >> 1), rt = w & 1;
    const unsigned v16 = (g >> 1) * 1024 + (g & 1) * 256 + n * 16, yo = j * 32768 + EXP(w * 4096);
    const unsigned xo = EXP((T >> 1) * 2048) + (T & 1) * 512 + rt * 1024, xv = ((g >> 1) * 16 + n) * 16 + (g & 1) * 8;
    const u32x2 ones = {0xFFFFFFFFu, 0xFFFFFFFFu};
    const unsigned fl = (w == 0 && l == 0) ? 0u : OOB;
    unsigned fails = 0;
    Red R;
    RSTAMP(2)
    st64(k.rs, xv, k.segx(PG_X1, 0) + xo, ones); st64(k.rs, xv, k.segx(PG_X1, 1) + xo, ones);
    sleep_until(k.t0 + RSL);
    y_wait(k, 2);
    R.yload<0, 16>(k, j, 0);
    for (int s = 0; s < 64; ++s) {
        STOP(s) STAMP(s, 0)
        unsigned ap[4], am = k.ym(s) + yo, xa = k.segx(PG_X1, s) + xo, rb = (s & 1) * 16384 + l * 16, wb = rb + w * 1024, fv = s;
#pragma unroll
        for (int i = 0; i < 4; ++i) { ap[i] = k.p0 + PR_B + (s & 3) * PR_SZ + w * 49152 + (i < 2 ? 32768 : 16384) + (~i & 1) * 4096; PINS(ap[i]); }
        rb += w * 4096;
        PINS(am); PINS(xa); PIN(rb); PIN(wb); PIN(fv);
        const unsigned ph = (s & 4) ? 0x80008000u : 0u;
        unsigned spins = 0, tq, tv;
        u32x2 res;
        if (k.c) res = ld64(k.rs, xv, k.seg(PG_X2, s) + xo);
        else res = *(const u32x2*)(p.x + (size_t)(s * 32 + rt * 16 + n) * 640 + T * 16 + g * 4);
        f32x4 acc[2][2];
        WAIT_BC(s, TSLP)
        u32x4 yl[8];
#pragma unroll
        for (int i = 0; i < 8; ++i) { yl[i] = __builtin_shufflevector(R.Y[2 * i], R.Y[2 * i + 1], 0, 1, 4, 5); PIN(yl[i]); }
        f32x4 x = {blo(res[0] ^ ph), bhi(res[0] ^ ph), blo(res[1] ^ ph), bhi(res[1] ^ ph)};
        PIN(x);
        wait_until2(tv); wait_until2(tq + (s < 63 ? RDLY : 0));
        STAMP(s, 1)
        unsigned long long bad;
        do {
            asm volatile("" ::: "memory");
            u32x4 ym[4], pp[16];
#pragma unroll
            for (int i = 0; i < 4; ++i) { ym[i] = ld128(k.rs, v16 + (i >> 1) * 2048 + (i & 1) * 512, am); SCHED0(); }
#pragma unroll
            for (int i = 0; i < 16; ++i) { pp[i ^ 4] = ld128(k.rs, l * 16 + (i & 3) * 1024, ap[i >> 2]); SCHED0(); }
            unsigned mx = 0, tg, lim = 0x7FFFFFFFu | (fails << 15);
            ++fails; PINS(fails); PINS(lim);
#pragma unroll
            for (int i = 0; i < 4; ++i) mx = max(mx, umax4(ym[i]));
            tg = mx == 0xFFFFFFFFu ? 0x80000000u : 0u;
            u32x4 yf[4];
#pragma unroll
            for (int i = 0; i < 4; ++i) { const int t = i & 1, f = i >> 1; yf[i] = (u32x4){ym[2 * f][t], ym[2 * f][t + 2], ym[2 * f + 1][t], ym[2 * f + 1][t + 2]}; }
            asm volatile("s_nop 1" : "+v"(yf[0]), "+v"(yf[1]), "+v"(yf[2]), "+v"(yf[3]), "+v"(tg));
            SCHED0();
            sfor<32>([&](auto I) { constexpr int i = CV(I), t = i & 1, q = (i >> 1) ^ 4, r = q < 4 ? 0 : q < 8 ? (q >> 1) & 1 : 1;
                mfa<false, q == 4 || q == 6>(acc[t][r], __builtin_bit_cast(bf16x8, q < 4 ? yl[t * 4 + q] : q < 8 ? yf[(q & 1) * 2 + t] : R.Y[t * 8 + (q & 7)]), __builtin_bit_cast(bf16x8, pp[q]));
                SCHED0();
                tg |= __builtin_amdgcn_bitop3_b32(pp[q][2 * t], pp[q][2 * t + 1], ph, 0x7e); PIN(tg);
                if constexpr (i == 20 || i == 22) { asm volatile("" : "+v"(acc[(i - 20) / 2][0])); lw128(wb + (i - 20) * 4096, __builtin_bit_cast(u32x4, acc[(i - 20) / 2][0])); }
                if constexpr (i == TPOS) st32(k.rs, fl, (k.p0 ^ 8192u) + CNT(C_X1, j), fv);
                SCHED0(); });
            bad = __ballot(tg > lim);
        } while (__builtin_expect(bad != 0, 0));
        STAMP(s, 2)
        const unsigned yc = y_get(k);
        SCHED0();
        asm volatile("s_nop 2" : "+v"(acc[0][1]), "+v"(acc[1][1]));
        lw128(wb + 4096, __builtin_bit_cast(u32x4, acc[0][1])); lw128(wb + 12288, __builtin_bit_cast(u32x4, acc[1][1]));
        RAWBAR()
        {
            const f32x4 r0 = __builtin_bit_cast(f32x4, lr128(rb)), r1 = __builtin_bit_cast(f32x4, lr128(rb + 1024)), r2 = __builtin_bit_cast(f32x4, lr128(rb + 2048)), r3 = __builtin_bit_cast(f32x4, lr128(rb + 3072));
            SCHED0();
            if constexpr (TPOS == 32) st32(k.rs, fl, (k.p0 ^ 8192u) + CNT(C_X1, j), fv);
            SCHED0();
            const f32x4 r = (r0 + r1) + (r2 + r3);
            st64(k.rs, xv, xa, (u32x2){min(cvtpk(x[0] + bfr(r[0]), x[1] + bfr(r[1])) ^ ph, 0xFFFFFFFEu), min(cvtpk(x[2] + bfr(r[2]), x[3] + bfr(r[3])) ^ ph, 0xFFFFFFFEu)});
            SCHED0();
            if constexpr (TPOS == 33) st32(k.rs, fl, (k.p0 ^ 8192u) + CNT(C_X1, j), fv);
        }
        st64(k.rs, s < 62 ? xv : OOB, k.segx(PG_X1, (s + 2) & 63) + xo, ones);
        STAMP(s, 3)
        if (s < 63) {
            if (!__all((int)yc >= 2 * s + 4)) { PCOUNT(2) y_wait(k, 2 * s + 4); }
            __builtin_amdgcn_fence(__ATOMIC_ACQUIRE, "wavefront");
            R.yload<0, 16>(k, j, s + 1);
        }
        STAMP(s, 4)
        STAMP(s, 5)
        PFLUSH(s)
    }
    PROF_OUT
}

#undef TZ
#define TZ z0()
DEVI void up_role(const Params& p, const Ctx& k, char* smem, int id) {
    PROF_DECL
    const int l = k.l, w = k.w, g = l >> 4, n = l & 15;
    bf16x8 Wt[80];
    const int tile0 = id * 8 + w * 2;
    auto issue = [&](auto U) { constexpr int u = CV(U);
        sfor<8>([&](auto J) { constexpr int j = CV(J); dma1<(u % 3) * 32768 + j * 1024>(((w & 1) ? p.wu : p.wg) + (size_t)(2 * u + (w >> 1)) * 32 * 1536 + id * 128, wsrc<256, true>(j * 64 + l, 3072), w * 8192); }); };
    sfor<3>(issue);
    cstream<10>([](auto V) { return IC(CV(V) < 9 ? 8 : 0); },
        [&](auto U) { constexpr int u = CV(U);
            if constexpr (u > 0 && u < 8) issue(IC(u + 2));
#pragma unroll
            for (int i = 0; i < 8; ++i) Wt[(i & 3) * 20 + 2 * u + (i >> 2)] = wp_read<256, true>(smem + (u % 3) * 32768 + (i >> 1) * 8192, w * 2 + (i & 1), l); });
    RAWBAR()
    RSTAMP(2)
    bf16x8 one8 = {0x3F80, 0x3F80, 0x3F80, 0x3F80, 0x3F80, 0x3F80, 0x3F80, 0x3F80};
    asm volatile("s_nop 1" : "+v"(one8));
    f32x4 cs[4];
    sfor<80>([&](auto I) { constexpr int f = CV(I); mfa<(f < 64), f % 20 == 0>(cs[f / 20], Wt[f], one8); });
    asm volatile("s_nop 7" : "+v"(cs[0]), "+v"(cs[1]), "+v"(cs[2]), "+v"(cs[3]));
    const unsigned ho = EXP((tile0 >> 1) * 1024), l16 = l * 16;
    constexpr int L_ST = 81920;
    const u32x4 ones = {0xFFFFFFFFu, 0xFFFFFFFFu, 0xFFFFFFFFu, 0xFFFFFFFFu};
#pragma unroll
    for (int i = 0; i < 4; ++i) st128(k.rs, l16, k.hseg(i >> 1) + (i & 1) * EXP(49152) + ho, ones);
    unsigned fails = 0;
    lw32(L_ST + 1024, 0u);
    prog_init(k);
    const unsigned fl = (w == 0 && l == 0) ? 0u : OOB;
    const unsigned long long m1 = __ballot(n & 1), m2 = __ballot(n & 2);
    for (int s = 0; s < 64; ) {
        unsigned xs = k.seg(PG_X1, s) + w * 2048, xd = (s & 1) * 40960 + w * 2048;
        PINS(xs); PINS(xd);
        prog_wait<20>(k, k.p0 + CNT(C_X1, 0), s);
        STOP(s) STAMP(s, 0)
        xn_all(k, l16, xs, xd);
        const unsigned xb = (s & 1) * 40960 + l16, yb = xb + w * 2048, sp = L_ST + (s & 1) * 2048, rp = sp + n * 16;
        const unsigned dp = sp + (g == (n >> 2) ? n * 16 + w * 4 : 4096 + l * 4);
        f32x4 acc[2][4], st[4];
        bf16x8 x[20], x1[20], y0[5], y1[5];
        unsigned mx = 0;
        auto blk = [&](auto B, auto J) { constexpr int b = CV(B), j = CV(J);
            if constexpr (j == 0) asm volatile("s_waitcnt vmcnt(%0)\n\ts_barrier" :: "n"(8 - 2 * b) : "memory");
            if constexpr (j >= 1 && j < 5) x[4 * b + j - 1] = lr128(xb + (4 * b + j - 1) * 2048);
            if constexpr (j == 5) y0[b] = lr128(yb + b * 8192);
            if constexpr (j == 6) y1[b] = lr128(yb + b * 8192 + 1024); };
        sfor<7>([&](auto J) { blk(IC(0), J); });
        SCHED0();
        STAMP(s, 1)
        float mean[2], rden[2], e[4][4];
        f32x4 sa[2], sb[2];
        u32x4 bad, o[2];
        unsigned sv, fv, r, f0, f1, ha = k.hseg(s) + ho;
        PINS(ha);
        auto stat = [&](auto I) { constexpr int i = CV(I), rt = i / 8, j = i % 8;
            if constexpr (j == 0) asm volatile("v_add_f32 %0, %2, %3\n\tv_add_f32 %1, %4, %5" : "=&v"(mean[rt]), "=&v"(rden[rt]) : "v"(sa[rt][0]), "v"(sa[rt][1]), "v"(sb[rt][0]), "v"(sb[rt][1]));
            if constexpr (j == 1 || j == 2) asm volatile("v_add_f32 %0, %0, %2\n\tv_add_f32 %1, %1, %3" : "+v"(mean[rt]), "+v"(rden[rt]) : "v"(sa[rt][j + 1]), "v"(sb[rt][j + 1]));
            if constexpr (j == 3) asm volatile("v_mul_f32 %0, 0xbaccccCD, %0\n\tv_mul_f32 %1, 0x3acccccd, %1" : "+v"(mean[rt]), "+v"(rden[rt]));
            if constexpr (j == 4) asm volatile("v_fma_f32 %1, -%0, %0, %1\n\tv_cvt_pk_bf16_f32 %0, 0, %0\n\tv_max_f32 %1, 0, %1" : "+v"(mean[rt]), "+v"(rden[rt]));
            if constexpr (j == 5) asm volatile("v_cvt_pk_bf16_f32 %0, 0, %0\n\tv_add_f32 %0, 0x3727c5ac, %0\n\tv_cvt_pk_bf16_f32 %0, 0, %0" : "+v"(rden[rt]));
            if constexpr (j == 6) asm volatile("v_sqrt_f32 %0, %0" : "+v"(rden[rt]));
            if constexpr (j == 7) asm volatile("v_cvt_pk_bf16_f32 %0, 0, %0\n\tv_rcp_f32 %0, %0" : "+v"(rden[rt])); };
        auto ep = [&](auto RT, auto T, auto J) { constexpr int rt = CV(RT), t = CV(T), j = CV(J), c = (j & 1) * 2, u = j >= 20, q = u ? j - 20 : j;
            float (&a)[4] = e[t + 2 * u];
#define A2 "+v"(a[c]), "+v"(a[c + 1])
            if constexpr (j < 0 || j >= 30) { }
            else if constexpr (q < 2) asm volatile("v_fma_f32 %0, %2, %3, %5\n\tv_fma_f32 %1, %2, %4, %6" : "=&v"(a[c]), "=&v"(a[c + 1]) : "v"(mean[rt]), "v"(cs[t + 2 * u][c]), "v"(cs[t + 2 * u][c + 1]), "v"(acc[rt][t + 2 * u][c]), "v"(acc[rt][t + 2 * u][c + 1]));
            else if constexpr (q < 4) asm volatile("v_mul_f32 %0, %0, %2\n\tv_mul_f32 %1, %1, %2" : A2 : "v"(rden[rt]));
            else if constexpr (q < 6 || j == 18 || j == 19) asm volatile("v_cvt_pk_bf16_f32 %0, 0, %0\n\tv_cvt_pk_bf16_f32 %1, 0, %1" : A2);
            else if constexpr (j < 8) asm volatile("v_mul_f32 %0, 0xbfb8aa3b, %0\n\tv_mul_f32 %1, 0xbfb8aa3b, %1" : A2);
            else if constexpr (j < 12) asm volatile("v_exp_f32 %0, %0" : "+v"(a[j & 3]));
            else if constexpr (j < 14) asm volatile("v_add_f32 %0, 1.0, %0\n\tv_add_f32 %1, 1.0, %1" : A2);
            else if constexpr (j < 18) asm volatile("v_rcp_f32 %0, %0" : "+v"(a[(j - 14) & 3]));
            else if constexpr (q < 8) asm volatile("v_mul_f32 %0, %0, %2\n\tv_mul_f32 %1, %1, %3" : A2 : "v"(e[t][c]), "v"(e[t][c + 1]));
            else if constexpr (q == 8) asm volatile("v_cvt_pk_bf16_f32 %0, %0, %2\n\tv_cvt_pk_bf16_f32 %1, %1, %3" : "+v"(a[0]), "+v"(a[2]) : "v"(a[1]), "v"(a[3]));
            else { unsigned d0, d1; asm volatile("v_min_u32 %0, -2, %2\n\tv_min_u32 %1, -2, %3" : "=&v"(d0), "=&v"(d1) : "v"(a[0]), "v"(a[2])); o[rt][2 * t] = d0; o[rt][2 * t + 1] = d1; } };
        sfor<180>([&](auto I) {
            constexpr int a = CV(I), b = a / 8, j = (a % 8) ^ (b == 4 ? 4 : 0), ch = a / 20 - 1, ks = a < 40 ? 4 * b + (j & 3) : a % 20, rt = ch >= 4;
            constexpr int nt = a < 40 ? 0 : rt ? ch - 4 : (ch & 1) * 2 + (ch >> 1), f = nt * 20 + ks, i = a - 36;
            if constexpr (a >= 40 || j < 4) mfa<(f < 64), ks == 0>(acc[rt][nt], Wt[f], rt ? x1[ks] : x[ks]);
            else mfa<false, b == 0>(st[j & 3], (j & 1) ? (j & 2 ? y1[b] : y0[b]) : one8, j & 2 ? y1[b] : y0[b]);
            SCHED0();
            if constexpr (a >= 103 && a < 119) TOUCH(x1[(a - 99) % 20]);
            else if constexpr (a == 60) { r = __builtin_amdgcn_readfirstlane(bad[0]); PINS(r); }
            else if constexpr (a == 61) { r = (fails - r) >> 31; PINS(r); }
            else if constexpr (a == 62) { fails += r; PINS(fails); }
            else if constexpr (a == 63) { f0 = 2 * s - r; PINS(f0); }
            else if constexpr (a == 64) { f1 = f0 | 1; PINS(f1); }
            else if constexpr (a == 65) { r <<= 31; PINS(r); }
            else if constexpr (a >= 56 && (a < 92 || a >= 119)) __builtin_amdgcn_s_setprio(0);
            if constexpr (a < 32 && a % 8 == 7) asm volatile("" :: "v"(x[4 * b + 4]), "v"(x[4 * b + 5]), "v"(x[4 * b + 6]), "v"(x[4 * b + 7]), "v"(y0[b + 1]));
            if constexpr (a < 32 && a % 8 == 4) TOUCH(y1[b]);
            SCHED0();
            if constexpr (a == 15) lw32((rp ^ 2048) + 1024, z0());
            if constexpr (a < 32 && a % 8 < 7) blk(IC((b + 1) % 5), IC(a % 8));
            if constexpr (i == 0 || i == 4 || i == 5 || i == 8) x1[i / 2 - (i == 4 || i == 8)] = lr128(xb + 1024 + (i / 2 - (i == 4 || i == 8)) * 2048);
            if constexpr (a >= 92 && a < 108) x1[a - 88] = lr128(xb + 1024 + (a - 88) * 2048);
            SCHED0();
            if constexpr (a < 40 && j >= 4) { const u32x4 v = __builtin_bit_cast(u32x4, (j & 1) ? y1[b] : y0[b]); mx = umax3(mx, v[j & 2], v[(j & 2) + 1]); PIN(mx); }
            if constexpr (i == 1 || i == 3) {
                constexpr int rt2 = i == 3;
                asm volatile("" : "+v"(st[2 * rt2]), "+v"(st[2 * rt2 + 1]));
                const f32x4& q = st[2 * rt2 + 1];
                const float d = csel(csel(q[0], q[1], m1), csel(q[2], q[3], m1), m2);
                lw32(dp + rt2 * 512, __float_as_uint(st[2 * rt2][0])); lw32(dp + rt2 * 512 + 256, __float_as_uint(d));
            }
            if constexpr (i == 2) lw32(mx == 0xFFFFFFFFu ? sp + 1024 : dp + 4096, 4096u);
            if constexpr (i == 6) bar_lgkm<2>();
            if constexpr (i == 6) sa[0] = __builtin_bit_cast(f32x4, lr128(rp));
            if constexpr (i == 7) sb[0] = __builtin_bit_cast(f32x4, lr128(rp + 256));
            if constexpr (i == 9) sa[1] = __builtin_bit_cast(f32x4, lr128(rp + 512));
            if constexpr (i == 10) sb[1] = __builtin_bit_cast(f32x4, lr128(rp + 768));
            if constexpr (i == 11) bad = __builtin_bit_cast(u32x4, lr128(rp + 1024));
            if constexpr (i >= 12 && i < 20) stat(IC(i - 12));
            if constexpr (a == 40) { STAMP(s, 2) }
            if constexpr (a == UPOS0 || a == UPOS) st32(k.rs, fl, k.p0 + CNT(C_H, id), fv);
            if constexpr (a == 117) __builtin_amdgcn_raw_buffer_store_b128(o[0], k.rs, (int)sv, (int)ha, AUX_SC1);
            if constexpr (a == 118) TOUCH(o[0]);
            SCHED0();
            ep(IC(0), IC(0), IC(a - 56)); ep(IC(0), IC(1), IC(a - 86));
            if constexpr (a >= 116 && a < 124) stat(IC(a - 108));
            if constexpr (a == UPOS0 - 2 || a == UPOS - 2) { fv = a == UPOS - 2 ? f1 : f0; PIN(fv); }
            if constexpr (a == 80) { sv = l16 | r; PIN(sv); }
            if constexpr (a < 144) ep(IC(1), IC(0), IC(a - 124));
            else if constexpr (a < 164) ep(IC(1), IC(1), IC(a - 144));
            else ep(IC(1), IC(0), IC(a - 144));
            SCHED0();
        });
        STAMP(s, 3)
        asm volatile("s_nop 7" : "+v"(acc[1][3]));
        {
            const f32x4 u = (acc[1][3] + mean[1] * cs[3]) * rden[1];
            o[1][2] = packs(bfr(u[0]) * e[1][0], bfr(u[1]) * e[1][1]); o[1][3] = packs(bfr(u[2]) * e[1][2], bfr(u[3]) * e[1][3]);
        }
        st128(k.rs, sv, ha + EXP(49152), o[1]);
        if (r) { PCOUNT(0) RAWBAR() lw32(rp + 1024, 0u); RAWBAR() }
        st128(k.rs, s < 62 ? l16 : OOB, k.hseg((s + 2) & 63) + ho, ones); st128(k.rs, s < 62 ? l16 : OOB, k.hseg((s + 2) & 63) + EXP(49152) + ho, ones);
        STAMP(s, 4) STAMP(s, 5)
        PFLUSH(s)
        s = (f1 + 1) >> 1;
    }
    PROF_OUT
}

DEVI void down_role(const Params& p, const Ctx& k, char* smem, int id) {
    PROF_DECL
    const int l = k.l, w = k.w, g = l >> 4, n = l & 15, rt = id / 5, cg = id % 5;
    bf16x8 Wt[96];
    wstream<12, 8, 4>(smem + w * 32768,
        [&](auto U, unsigned sl) { constexpr int u = decltype(U)::value; sfor<8>([&](auto J) { constexpr int j = CV(J); dma1<(u % 4) * 8192 + j * 1024>(p.wd + (size_t)(w * 12 + u) * 32 * 640 + cg * 128, wsrc<256, false>(j * 64 + l, 1280), sl); }); },
        [&](auto U, char* sp) { constexpr int u = decltype(U)::value;
#pragma unroll
            for (int nt = 0; nt < 8; ++nt) Wt[u * 8 + nt] = wp_read<256, false>(sp, (nt + 2 * w) & 7, l); });
    RAWBAR()
    RSTAMP(2)
    const unsigned xo = EXP((cg * 4 + w) * 2048) + rt * 1024, xv = ((g >> 1) * 16 + n) * 16 + (g & 1) * 8;
    const u32x2 ones = {0xFFFFFFFFu, 0xFFFFFFFFu};
    const unsigned x2v = k.c ? xv : OOB;
    prog_init(k);
#pragma unroll
    for (int i = 0; i < 4; ++i) st64(k.rs, x2v, k.seg(PG_X2, i >> 1) + xo + (i & 1) * 512, ones);
    const __amdgpu_buffer_rsrc_t ro = __builtin_amdgcn_make_buffer_rsrc((void*)p.out, 0, 2048 * 1280, 0x00020000);
    const unsigned ov = k.c ? OOB : n * 1280 + g * 8, lim = k.c ? 0xFFFFFFFEu : 0xFFFFFFFFu, fl = (w == 0 && l == 0) ? 0u : OOB;
    unsigned fails = 0;
    for (int s = 0; s < 64; ++s) {
        unsigned la[3], ra = k.seg(PG_X1, s) + xo, oa = k.c ? k.seg(PG_X2, s) + xo : (s * 32 + rt * 16) * 1280 + (cg * 8 + w * 2) * 32, rb = (s & 1) * 32768 + l * 16, wb[3], fv = s;
#pragma unroll
        for (int q = 0; q < 3; ++q) { la[q] = k.hseg(s) + rt * EXP(49152) + EXP((w * 12 + q * 4) * 1024); wb[q] = rb + ((w + q + 1) & 3) * 6144 + q * 2048; PINS(la[q]); PIN(wb[q]); }
        rb += w * 6144;
        PINS(ra); PINS(oa); PIN(rb); PIN(fv);
        prog_wait<12>(k, k.p0 + CNT(C_H, 0), 2 * s + rt);
        STOP(s) STAMP(s, 0)
        f32x4 acc[8], r[6];
        u32x2 res[2];
        u32x4 hx[12];
        unsigned long long bad;
        do {
            asm volatile("" ::: "memory");
#pragma unroll
            for (int i = 0; i < 12; ++i) { hx[i] = ld128(k.rs, l * 16 + (i & 3) * 1024, la[i >> 2]); SCHED0(); }
#pragma unroll
            for (int t = 0; t < 2; ++t) res[t] = ld64(k.rs, xv + t * 512, ra);
            SCHED0();
            unsigned mx = 0;
            sfor<72>([&](auto I) { constexpr int i = CV(I), ks = i / 6, j = 2 + i % 6, f = ks * 8 + j;
                mfa<(f < 64), ks == 0>(acc[j], Wt[f], __builtin_bit_cast(bf16x8, hx[ks]));
                SCHED0();
                if constexpr (i % 6 < 2) { mx = umax3(mx, hx[ks][(i & 1) * 2], hx[ks][(i & 1) * 2 + 1]); PIN(mx); }
                if constexpr (i == 68) { bad = __ballot(mx == 0xFFFFFFFFu); PINS(bad); }
                if constexpr (i == 69) { bad = fails < 65536u ? bad : 0; PINS(bad); }
                if constexpr (i == 70) { ++fails; PINS(fails); }
                if constexpr (i == DPOS) st32(k.rs, fl, k.p0 + CNT(C_X2, rt * 5 + cg), fv);
                SCHED0(); });
        } while (__builtin_expect(bad != 0, 0));
        STAMP(s, 1)
        float x[8];
        sfor<24>([&](auto I) { constexpr int i = CV(I), ks = i / 2, j = i & 1, f = ks * 8 + j;
            mfa<(f < 64), ks == 0>(acc[j], Wt[f], __builtin_bit_cast(bf16x8, hx[ks]));
            SCHED0();
            if constexpr (i < 12 && !(i & 1)) { constexpr int jj = 2 + i / 2;
                asm volatile("" : "+v"(acc[jj])); lw128(wb[jj / 2 - 1] + (jj & 1) * 1024, __builtin_bit_cast(u32x4, acc[jj])); }
            if constexpr (i < 8 && (i & 1)) { x[i - 1] = blo(res[i >> 2][(i >> 1) & 1]); x[i] = bhi(res[i >> 2][(i >> 1) & 1]); PIN(x[i - 1]); PIN(x[i]); }
            if constexpr (i == 13) bar_lgkm<0>();
            if constexpr (i >= 13 && i < 19) r[i - 13] = __builtin_bit_cast(f32x4, lr128(rb + (i - 13) * 1024));
            if constexpr (i + 72 == DPOS) st32(k.rs, fl, k.p0 + CNT(C_X2, rt * 5 + cg), fv);
            SCHED0(); });
        r[0] += r[2] + r[4]; r[1] += r[3] + r[5];
        asm volatile("s_nop 3" : "+v"(acc[0]), "+v"(acc[1]), "+v"(r[0]), "+v"(r[1]));
#pragma unroll
        for (int t = 0; t < 2; ++t) {
            const f32x4 d = acc[t] + r[t];
            const u32x2 o = {min(cvtpk(x[4 * t] + bfr(d[0]), x[4 * t + 1] + bfr(d[1])), lim), min(cvtpk(x[4 * t + 2] + bfr(d[2]), x[4 * t + 3] + bfr(d[3])), lim)};
            st64(k.rs, x2v + t * 512, oa, o);
            __builtin_amdgcn_raw_buffer_store_b64(o, ro, (int)(ov + t * 32), (int)oa, 0);
        }
        STAMP(s, 2)
#pragma unroll
        for (int t = 0; t < 2; ++t) st64(k.rs, s < 62 ? x2v : OOB, k.seg(PG_X2, (s + 2) & 63) + xo + t * 512, ones);
        STAMP(s, 3) STAMP(s, 4) STAMP(s, 5)
        PFLUSH(s)
    }
    PROF_OUT
}

DEVI void warm(const Params& p, const Ctx& k, int xl, int hw) {
    set_m0(k.w * 256);
_Pragma("unroll 1")
    for (int it = hw; it < 32 * 15; it += k.c ? 40 : 64) {
        const int q = it < 120 ? it & 7 : 8 + (it - 120) % 24, t = it < 120 ? it >> 3 : (it - 120) / 24;
        const u16* b = q < 24 ? (q < 8 ? p.Wv : q < 16 ? p.Wk : p.Wq) + (xl * 8 + (q & 7)) * 192 : p.Wo + (xl * 8 + (q & 7)) * 122880;
        const unsigned ln = t * 64 + k.l, cl = min(((unsigned)(size_t)b >> 7) & 7u, 4u);
        if (k.c) sleep_until(k.t0 + WP + it * PP / 128);
        if (t < (q >= 24 ? PO : ((q < 8 ? PV : PQ) >> (cl * 4)) & 15))
            asm volatile("global_load_lds_dword %0, %1" :: "v"(q < 24 ? (639 - ln / 3) * 12288 + (ln % 3) * 128 : (191 - (ln >> 2)) * 1280 + 768 + (ln & 3) * 128), "s"(b) : "memory");
    }
    DRAIN();
_Pragma("unroll 1")
    for (int i = 3; i < 8; ++i) { sleep_until(k.t0 + i * 50); hello(k, 0); }
    DRAIN();
    RAWBAR()
}

DEVI int xcc_id() { return __builtin_amdgcn_s_getreg(20 | (0 << 6) | (3 << 11)) & 7; }

DEVI unsigned prepare(const Params& p, Ctx& k, char* smem, int xl, int i) {
    const int l = k.l, j = xl * 22 + i - 4;
    const u32x4 ones = {0xFFFFFFFFu, 0xFFFFFFFFu, 0xFFFFFFFFu, 0xFFFFFFFFu};
    unsigned vote = 0;
    if (i < 4) {
        const int row = ((i * 4 + xl) * 4 + k.w) * 4 + (l >> 4);
        const unsigned dst = k.seg(PG_XNA, i * 2 + (xl >> 1));
        u32x4 a[5];
#pragma unroll
        for (int c = 0; c < 5; ++c) a[c] = *(const u32x4*)(p.x + (size_t)row * 640 + (c * 16 + (l & 15)) * 8);
        ln_rows(a, l & 15, row & 31, [&](unsigned vo, int c, u32x4 o) { st128(k.rs, vo, dst + c * 16384, o); st128(k.rs, vo, dst + c * 16384 + 8192, o); });
    } else if (k.w == 3) {
        const unsigned cal = ((unsigned)(j % 44) << 21) + 7 * 16384 + (xl >> 1) * 4096 + (l & 7) * 16;
        int d = 0;
#pragma unroll 1
        for (int r = 0; r <= 2 * CALR; ++r) {
            unsigned long long c0, c1;
            st128(k.rs, cal, (r & 1) * 8192, ones);
            st128(k.rs, cal, (~r & 1) * 8192, ones);
            asm volatile("s_waitcnt vmcnt(1)\n\ts_memtime %0\n\ts_waitcnt vmcnt(0)\n\ts_memtime %1\n\ts_waitcnt lgkmcnt(0)" : "=&s"(c0), "=s"(c1) :: "memory");
            d = r ? min((int)(c1 - c0), 400) - d : 0;
        }
        vote = (unsigned)(d < 0) ^ (unsigned)(xcc_id() >> 2) ^ (unsigned)(i & 1);
    } else {
        if (k.w == 2 && j < 20) {
            const int e = j * 32 + (l >> 1), T = e >> 4, hf = (e >> 1) & 1, rr = ((e >> 2) & 3) * 2 + (e & 1);
            u32x4 v;
#pragma unroll
            for (int c = 0; c < 4; ++c) v[c] = min(*(const unsigned*)(p.mem0 + rr * 640 + T * 16 + 4 * c + 2 * hf), 0xFFFFFFFEu);
            st128(k.rs, (2u << 21) + EXP(e * 16) + (l & 1) * 8192, 0, v);
        }
        constexpr unsigned N0 = PR_B + PR_SZ, N1 = 40 * 16384, N2 = 32768;
        static_assert(N0 + N1 + N2 <= 4 * 88 * 3072 && NPAGES <= 44, "fill");
#pragma unroll
        for (int r = 0; r < 4; ++r) {
            const unsigned o = ((r * 88 + j) * 3 + k.w) * 1024;
            st128(k.rs, o < N0 + N1 + N2 ? l * 16 : OOB, o < N0 ? o : o < N0 + N1 ? ((unsigned)PG_YM << 21) + (o - N0) : (2u << 21) + 32768 + (o - N0 - N1), ones);
        }
    }
    if (k.w == 3) *(volatile LAS unsigned*)smem = vote;
    DRAIN();
    __builtin_amdgcn_s_waitcnt(0x0f70);
    XSTAMP(0)
    RAWBAR()
    return *(volatile LAS unsigned*)smem;
}
DEVI void master(const Params& p, const Ctx& k) {
    const int l = k.l;
    u32x2 v[4] = {{0u, 0u}, {0u, 0u}, {0u, 0u}, {0u, 0u}};
_Pragma("unroll 1")
    for (unsigned spins = 0; spins < SPIN_CAP; ++spins) {
#pragma unroll
        for (int q = 0; q < 4; ++q) v[q] |= cm_ld(k, (CM_IN + (q * 64 + l) * 128) | v[q][1]);
        if (__all((v[0][1] & v[1][1] & v[2][1] & v[3][1]) != 0)) break;
        asm volatile("" ::: "memory");
    }
    const unsigned long long a = __ballot(v[2][0] != 0), b = __ballot(v[3][0] != 0);
    const int n = __popcll(a) + __popcll(b);
    const unsigned long long g = n > 44 ? ~0ull : 0ull, u = (a & b) | ((a ^ b) & g);
    const unsigned long long t = (__popcll(a ^ b) > 2 || n > 80 || n < 8 ? g : ((u >> 4) & 0x3FFFFF) | ((u >> 36) << 22)) ^ 0xAAAAAAAAAAAAAAAAull;
#pragma unroll
    for (int q = 0; q < 4; ++q) cm_st(k, l * 128, CM_GO + q * 8192, (unsigned)t, (unsigned)(t >> 32) | 0x80000000u);
}

#define FIN(n) { __builtin_amdgcn_s_waitcnt(0x0f70); asm volatile("s_nop " #n); return; }
__global__ __launch_bounds__(256, 1) void mega_kernel(Params p) {
    extern __shared__ __attribute__((aligned(16))) char smem[];
    Ctx k;
    const u16 *wq = p.Wq, *wk = p.Wk, *wv = p.Wv;
    asm volatile("" : "+s"(wq), "+s"(wk), "+s"(wv) : "s"(p.cm));
    k.ws = p.ws; k.cm = p.cm;
    k.rs = __builtin_amdgcn_make_buffer_rsrc((void*)p.ws, 0, 0x7fffffff, 0x00020000);
    k.l = threadIdx.x & 63; k.w = __builtin_amdgcn_readfirstlane(threadIdx.x >> 6);
    RSTAMP(0)
    const int x = blockIdx.x & 7, i = blockIdx.x >> 3, xl = x & 3;
    k.role = x * 32 + i; k.c = x >> 2; k.par = 0; k.p0 = 0; k.t0 = rclk();
    line_st(k, CM_GO, 0, 0);
    auto start = [&]() {
        const u16* a = (i < 8 ? wk : wv) + (xl * 8 + (i & 7)) * 192, * b = (i < 8 ? wq : wv + 32 * 6144) + (xl * 8 + (i & 7)) * 192;
        unsigned o[3];
        sfor<18>([&](auto J) { constexpr int j = CV(J);
            if constexpr (j < 3) o[j] = wsrc<384, true>(k.w * 192 + j * 64 + k.l, 12288);
            dma1<SLOT(j / 6) + ((j / 3) & 1) * 12288 + (j % 3) * 1024>((((j / 3) & 1) ? b : a) + (j / 6) * (i < 8 ? 32 * 6144 : 64 * 6144), o[j % 3], k.w * 3072); }); };
    if (!k.c && i < 16) start();
    if (__builtin_expect(k.role == MASTER, 0)) {
#pragma unroll
        for (int q = 0; q < 16; ++q) cm_st(k, k.l * 8, CM_IN + k.w * 8192 + q * 512, 0, 0);
        cm_st(k, 0, CM_TMO, ~0u, ~0u);
        DRAIN();
        RAWBAR()
    }
    if (__builtin_expect(k.c && i < 26, 0)) { const unsigned v = prepare(p, k, smem, xl, i); RSTAMP(1) hello(k, v); go_wait(k, v); XSTAMP(1) RAWBAR() }
    else { RSTAMP(1) if (k.c) { DRAIN(); hello(k, 0); } }
    if (__builtin_expect(k.role == MASTER && k.w == 0, 0)) { master(p, k); __builtin_amdgcn_s_waitcnt(0x0f70); }
    if (k.c && i < 16) { sleep_until(k.t0 + WD1 + (i ^ 8) * STG); start(); }
    if (i < 8) { k.l = lane(); qk_role(p, k, smem, xl * 8 + i); FIN(0) }
    if (i < 16) { k.l = lane(); vo_role(p, k, smem, xl * 8 + i - 8); FIN(1) }
    k.l = lane();
    if (!k.c || i < 26) warm(p, k, xl, (i - 16) * 4 + k.w);
    else RAWBAR()
    go_wait(k, 0);
    if (i >= 26 && !k.c) {
        sleep_until(k.t0 + XPF);
#pragma unroll
        for (int r = 0; r < 3; ++r) asm volatile("global_load_lds_dword %0, %1" :: "v"(min(40 + r * 96 + ((i - 26) * 4 + xl) * 4 + k.w, 319) * 8192 + k.l * 128), "s"(p.x) : "memory");
        DRAIN();
        RAWBAR()
    }
    if (i >= 26) sleep_until(k.t0 + (k.c ? GST : GSA) + ((xl & 1) == UPODD ? 0 : i < 31 ? GSD : -GSA));
    if (i < 21) { mred_role(p, k, smem, xl * 5 + i - 16); FIN(2) }
    if (i < 26) { tred_role(p, k, smem, xl * 5 + i - 21); FIN(3) }
    if ((xl & 1) == UPODD) { up_role(p, k, smem, (xl >> 1) * 6 + i - 26); FIN(4) }
    if (i < 31) { down_role(p, k, smem, (xl >> 1) * 5 + i - 26); FIN(5) }
    row_role(p, k, smem, xl >> 1);
    FIN(6)
}

extern "C" void kernel_launch(void* const* d_in, const int* in_sizes, int n_in,
                              void* d_out, int out_size, void* d_ws, size_t ws_size,
                              hipStream_t stream) {
    Params p;
    p.x = (const u16*)d_in[0]; p.mem0 = (const u16*)d_in[1];
    p.Wq = (const u16*)d_in[2]; p.Wk = (const u16*)d_in[3]; p.Wv = (const u16*)d_in[4]; p.Wo = (const u16*)d_in[5];
    p.wg = (const u16*)d_in[8]; p.wu = (const u16*)d_in[9]; p.wd = (const u16*)d_in[10];
    p.out = (u16*)d_out; p.cm = (char*)d_ws;
    p.ws = (char*)(((uintptr_t)d_ws + CM_BYTES + ((1u << 21) - 1)) & ~(uintptr_t)((1u << 21) - 1));
    (void)hipFuncSetAttribute((const void*)mega_kernel, hipFuncAttributeMaxDynamicSharedMemorySize, LDS_BYTES);
    mega_kernel<<<256, 256, LDS_BYTES, stream>>>(p);
}
```

```cpp
#define EVC 1
#define EVS -5
#define SWAPG
#include <hip/hip_runtime.h>
#include <hip/hip_bf16.h>
#include <stdint.h>
#include <utility>

typedef unsigned short u16;
typedef __attribute__((ext_vector_type(16))) short bf16x16;
typedef __attribute__((ext_vector_type(8))) short bf16x8;
typedef __attribute__((ext_vector_type(4))) short bf16x4;
typedef __attribute__((ext_vector_type(8))) __bf16 hbf8;
typedef __attribute__((ext_vector_type(16))) __bf16 hbf16;
typedef __attribute__((ext_vector_type(4))) float f32x4;
typedef __attribute__((ext_vector_type(2))) float f32x2;
typedef __attribute__((ext_vector_type(8))) unsigned u32x8;
typedef __attribute__((ext_vector_type(4))) unsigned u32x4;
typedef __attribute__((ext_vector_type(2))) unsigned u32x2;
typedef __attribute__((ext_vector_type(2))) __bf16 bf16x2_t;
#define LAS __attribute__((address_space(3)))
#define GAS __attribute__((address_space(1)))
#define DEVI __device__ __forceinline__

#define PSLEEP 8
#define WD1 1000
#define GST 550
#define NTA " nt"
#define NTB ""
#define PQ 0x077A0
#define PV 0x4AAE4
#define GSA 1600
#define QGS 8
#define VGS 8
#define MN 480
#define MD 250
#define MP 100
#define RSL 1000
#define GSD 250
#define PO 0
#define PDLY 0
#define WP 600
#define PP 100
#define STG 0
#define UPOS0 88
#define DPOS 88
#define XPF 1400
#define TPOS 33
#define XS0 40
#define XP 29
#define XB 66
#define TB 107
#define YSLP 2
#define RMSK 0x380u
#define OLDP 3
#define ARMP 0
#define QD 0
#define PACE 65
#define XPOS 74
#define YPOS 40
#define XPOSY 1
#define PWS 1
#define WUS 0
#define WMIX 1
#define PACE2 30
#define GDLY 80
#define QDLY 0
#define CALR 1
#define SPG 3
#define DQ 16
#define DV 12
#define BSLP 2
#define SDLY 0
#define UPOS 150
#define RDLY 45
#define NOYST 0
#define NOTOK 0
#define NOYLD 0
#define GPOSY 8
#define OLDMAP 1
#define UPODD 0
#define XLD 10
#define TROT 8
#define KOUT 1
#define RPOS 75
#define RDY 90
#define APOS 6
#define GPOS 74

#define EXP(o) ((((unsigned)(o) >> 13) << 14) | ((unsigned)(o) & 8191u))
enum { C_XN = 0, C_Y = 1, C_X1 = 2, C_XB = 3, C_H = 4, C_X2 = 5, C_BC = 8 };
#define CNT(type, s) ((unsigned)((type) * 16384 + (s) * 128))
constexpr unsigned PR_B = EXP(131072), PR_SZ = EXP(12 * 8192);
constexpr unsigned DUMMY_B = EXP(0);
constexpr int PG_MEMF = 2;
#define YXR 8
constexpr int PG_YX = 3;
constexpr int PG_XNA = PG_YX + 2 * YXR;
constexpr int PG_X1 = PG_XNA + 4, PG_X2 = PG_X1 + 4;
constexpr int PG_H = PG_X2 + 4;
constexpr int PG_YM = PG_H + 8;
constexpr int NPAGES = PG_YM + 2;
static_assert(NPAGES <= 64, "map");
constexpr unsigned CM_TAB = 0, CM_TICKET = 1024, CM_TMO = 4096, CM_IN = 8192, CM_GO = 40960, CM_BYTES = 73728;
constexpr int MASTER = 5 * 32 + 31;
constexpr unsigned SPIN_CAP = 1u << 20;
#define AUX_SC1 16
#define LDS_BYTES (160 * 1024)

struct Params {
    const u16* x; const u16* mem0; const u16* Wq; const u16* Wk; const u16* Wv; const u16* Wo;
    const u16* wg; const u16* wu; const u16* wd; u16* out; char* cm; char* ws;
};
struct Ctx {
    char* ws; char* cm; __amdgpu_buffer_rsrc_t rs; unsigned long long par;
    int c, l, w, role;
    unsigned p0, t0;
    DEVI unsigned pg(int page) const { return ((unsigned)page << 21) | (((unsigned)(par >> page) & 1u) << 13); }
    DEVI unsigned pg0(int page) const { return pg(page) ^ ((unsigned)c << 13); }
    DEVI unsigned seg(int page, int s) const { return pg(page + (s >> 4)) + (unsigned)(s & 15) * 131072u; }
    DEVI unsigned seg0(int page, int s) const { return seg(page, s) ^ ((unsigned)c << 13); }
    DEVI unsigned segx(int page, int s) const { return seg(page, s) ^ 8192u; }
    DEVI unsigned hseg(int s) const { return pg(PG_H + (s >> 3)) + (unsigned)(s & 7) * 262144u; }
    DEVI unsigned yx(int sg, int wq) const { return pg(PG_YX + (sg % YXR) * 2 + (wq >> 1)) + (unsigned)(wq & 1) * 1048576u; }
    DEVI unsigned ym(int s) const { return pg(PG_YM + ((s >> 1) & 1)) + (unsigned)(s & 1) * EXP(327680); }
    DEVI unsigned memf(int s) const { return pg(PG_MEMF) + (unsigned)s * 32768u; }
    DEVI char* cnt(int type, int s) const { return ws + (p0 + CNT(type, s)); }
    DEVI char* cnt0(int type, int s) const { return ws + ((p0 ^ ((unsigned)c << 13)) + CNT(type, s)); }
};

DEVI unsigned cvtpk(float a, float b) { return __builtin_bit_cast(unsigned, __builtin_convertvector((f32x2){a, b}, bf16x2_t)); }
DEVI unsigned packs(float a, float b) { return min(cvtpk(a, b), 0xFFFFFFFEu); }
DEVI float blo(unsigned x) { return __uint_as_float(x << 16); }
DEVI float bhi(unsigned x) { return __uint_as_float(x & 0xFFFF0000u); }
DEVI float bfr(float f) { return __uint_as_float(cvtpk(0.f, f)); }
DEVI u32x4 ld128(__amdgpu_buffer_rsrc_t rs, unsigned v, unsigned s) { return __builtin_amdgcn_raw_buffer_load_b128(rs, (int)v, (int)s, AUX_SC1); }
DEVI u32x2 ld64(__amdgpu_buffer_rsrc_t rs, unsigned v, unsigned s) { return __builtin_amdgcn_raw_buffer_load_b64(rs, (int)v, (int)s, AUX_SC1); }
DEVI unsigned ld32(__amdgpu_buffer_rsrc_t rs, unsigned v, unsigned s) { return __builtin_amdgcn_raw_buffer_load_b32(rs, (int)v, (int)s, AUX_SC1); }
DEVI void st128(__amdgpu_buffer_rsrc_t rs, unsigned v, unsigned s, u32x4 x) {
    __builtin_amdgcn_raw_buffer_store_b128(x, rs, (int)v, (int)s, AUX_SC1);
    asm volatile("s_nop 1" :: "v"(x) : "memory");
}
DEVI void st64(__amdgpu_buffer_rsrc_t rs, unsigned v, unsigned s, u32x2 x) { __builtin_amdgcn_raw_buffer_store_b64(x, rs, (int)v, (int)s, AUX_SC1); }
DEVI void st32(__amdgpu_buffer_rsrc_t rs, unsigned v, unsigned s, unsigned x) { __builtin_amdgcn_raw_buffer_store_b32(x, rs, (int)v, (int)s, AUX_SC1); }
#define RAWBAR() { __builtin_amdgcn_s_waitcnt(0xc07f); __builtin_amdgcn_s_barrier(); }
#define BARL(n) { asm volatile("" ::: "memory"); __builtin_amdgcn_s_waitcnt(0xc07f | ((n) << 8)); __builtin_amdgcn_s_barrier(); asm volatile("" ::: "memory"); }
#define DRAIN() asm volatile("s_waitcnt vmcnt(0)" ::: "memory")
#define SCHED0() __builtin_amdgcn_sched_barrier(0)
#define OOB 0x80000000u
DEVI int opq(int x) { asm volatile("" : "+v"(x)); return x; }
#define LANE const int l = opq(k.l), g = l >> 4, n = l & 15; (void)g; (void)n;
DEVI unsigned umax3(unsigned a, unsigned b, unsigned c) { return max(max(a, b), c); }
DEVI unsigned umax4(u32x4 v) { return max(umax3(v[0], v[1], v[2]), v[3]); }
DEVI unsigned rclk() { return (unsigned)__builtin_amdgcn_s_memrealtime(); }
DEVI unsigned long long rt_async() { unsigned long long t; asm volatile("s_memrealtime %0" : "=s"(t)); return t; }
DEVI void lgkm0(unsigned long long& a) { asm volatile("s_waitcnt lgkmcnt(0)" : "+s"(a) :: "memory"); }
DEVI unsigned spack(unsigned lo, unsigned hi) { unsigned d; asm volatile("s_pack_ll_b32_b16 %0, %1, %2" : "=s"(d) : "s"(lo), "s"(hi)); return d; }
DEVI int lane() { int l; asm volatile("v_mbcnt_lo_u32_b32 %0, -1, 0\n\tv_mbcnt_hi_u32_b32 %0, -1, %0" : "=v"(l)); __builtin_assume(l >= 0 && l < 64); return l; }
DEVI void sleep_until(unsigned target) { while ((int)(rclk() - target) < 0) __builtin_amdgcn_s_sleep(4); }
DEVI float xor16max(float x) { auto r = __builtin_amdgcn_permlane16_swap(__float_as_uint(x), __float_as_uint(x), false, false); return __builtin_fmaxf(__uint_as_float(r[0]), __uint_as_float(r[1])); }
DEVI float xor32max(float x) { auto r = __builtin_amdgcn_permlane32_swap(__float_as_uint(x), __float_as_uint(x), false, false); return __builtin_fmaxf(__uint_as_float(r[0]), __uint_as_float(r[1])); }
DEVI float xor16add(float x) { auto r = __builtin_amdgcn_permlane16_swap(__float_as_uint(x), __float_as_uint(x), false, false); return __uint_as_float(r[0]) + __uint_as_float(r[1]); }
DEVI float xor32add(float x) { auto r = __builtin_amdgcn_permlane32_swap(__float_as_uint(x), __float_as_uint(x), false, false); return __uint_as_float(r[0]) + __uint_as_float(r[1]); }
DEVI float fold32(float a, float b) { auto r = __builtin_amdgcn_permlane32_swap(__float_as_uint(a), __float_as_uint(b), false, false); return __uint_as_float(r[0]) + __uint_as_float(r[1]); }
#define ROR16(x, n) __builtin_bit_cast(float, __builtin_amdgcn_update_dpp(0, __builtin_bit_cast(int, (x)), 0x120 + (n), 0xf, 0xf, false))
DEVI float row16sum(float x) { x += ROR16(x, 8); x += ROR16(x, 4); x += ROR16(x, 2); x += ROR16(x, 1); return x; }
DEVI unsigned z0() { unsigned z; asm volatile("v_mov_b32 %0, 0" : "=v"(z)); return z; }
DEVI void cnt_add(char* p) { __hip_atomic_fetch_add((GAS unsigned*)(p + z0()), z0() + 1u, __ATOMIC_RELAXED, __HIP_MEMORY_SCOPE_AGENT); }
DEVI unsigned cnt_get(char* p) { return __hip_atomic_load((GAS unsigned*)(p + z0()), __ATOMIC_RELAXED, __HIP_MEMORY_SCOPE_AGENT) + 1u; }
DEVI bool spin_fail(const Ctx& k, unsigned spins) {
    if (spins > SPIN_CAP || ((spins & 255u) == 255u && __builtin_amdgcn_readfirstlane(cnt_get(k.cm + CM_TMO)) != 0)) { cnt_add(k.cm + CM_TMO); return true; }
    return false;
}
DEVI __amdgpu_buffer_rsrc_t cm_rs(const Ctx& k) { return __builtin_amdgcn_make_buffer_rsrc((void*)k.cm, 0, CM_BYTES, 0x00020000); }
DEVI void cm_st(const Ctx& k, unsigned v, unsigned off, unsigned lo, unsigned hi) { asm volatile("buffer_store_dwordx2 %0, %1, %2, %3 offen sc1" :: "v"((u32x2){lo, hi}), "v"(v), "s"(cm_rs(k)), "s"(off) : "memory"); }
DEVI u32x2 cm_ld(const Ctx& k, unsigned off) { return ld64(cm_rs(k), off, 0); }
DEVI void line_st(const Ctx& k, unsigned off, unsigned lo, unsigned hi) { cm_st(k, (lane() & 15) * 8 + (k.w ? OOB : 0u), off + k.role * 128, lo, hi); }
DEVI void hello(const Ctx& k, unsigned vote) { line_st(k, CM_IN, vote, OOB); }
DEVI u32x2 go_ask(const Ctx& k) { u32x2 d; asm volatile("buffer_load_dwordx2 %0, off, %1, %2 sc1" : "=v"(d) : "s"(cm_rs(k)), "s"(CM_GO + k.role * 128) : "memory"); return d; }
DEVI void go_wait(Ctx& k, unsigned vote, u32x2 v = {0u, 0u}) {
_Pragma("unroll 1")
    for (unsigned spins = 0; __builtin_amdgcn_readfirstlane(v[1]) >= 0 && spins < SPIN_CAP; ++spins) {
        if (spins) { hello(k, vote); __builtin_amdgcn_s_sleep(4); }
        asm volatile("buffer_load_dwordx2 %0, off, %1, %2 sc1\n\ts_waitcnt vmcnt(0)" : "=v"(v) : "s"(cm_rs(k)), "s"(CM_GO + k.role * 128) : "memory");
    }
    k.par = ((unsigned long long)(unsigned)__builtin_amdgcn_readfirstlane(v[1]) << 32 | (unsigned)__builtin_amdgcn_readfirstlane(v[0])) ^ (k.c ? ~0ull : 0ull);
    k.p0 = k.pg(0);
    __builtin_amdgcn_fence(__ATOMIC_ACQUIRE, "wavefront");
}
DEVI void cnt_wait(const Ctx& k, char* c, unsigned need) {
_Pragma("unroll 1")
    for (unsigned spins = 0; (unsigned)__builtin_amdgcn_readfirstlane(cnt_get(c)) < need; ++spins) {
        __builtin_amdgcn_s_sleep(PSLEEP);
        if (spin_fail(k, spins)) break;
    }
    __builtin_amdgcn_fence(__ATOMIC_ACQUIRE, "wavefront");
}
DEVI unsigned xn_get(const Ctx& k, unsigned l16, int s) { return ld32(k.rs, opq(l16) & 0x380u, k.p0 + CNT(C_XN, 0)); }
DEVI bool xn_has(const Ctx& k, unsigned v, int s) { return __all((int)v >= s - (k.c ? 0 : 8)); }
DEVI void xn_wait(const Ctx& k, unsigned l16, int s) {
_Pragma("unroll 1")
    for (unsigned spins = 0; !xn_has(k, xn_get(k, l16, s), s); ++spins) {
        asm volatile("" ::: "memory");
        __builtin_amdgcn_s_sleep(2);
        if (spin_fail(k, spins)) break;
    }
    __builtin_amdgcn_fence(__ATOMIC_ACQUIRE, "wavefront");
}
constexpr unsigned L_PW = 150 * 1024;
DEVI void prog_init(const Ctx& k) { *(volatile LAS u32x2*)(size_t)(L_PW + k.w * 512 + k.l * 8) = (u32x2){~0u, ~0u}; RAWBAR() }
DEVI void ask(const Ctx& k, unsigned va, unsigned a, unsigned m) { asm volatile("s_mov_b32 m0, %3\n\ts_nop 0\n\tbuffer_load_dword %0, %1, %2 offen sc1 lds" :: "v"(va), "s"(k.rs), "s"(a), "s"(m) : "memory"); }
template <int SLP> DEVI unsigned watch(const Ctx& k, unsigned va, unsigned a, unsigned m, unsigned ra, int s, int mute) {
    unsigned n, t;
    asm volatile("s_mov_b32 m0, %8\n\ts_mov_b32 %0, 0\n\ts_branch 2f\n"
        "1:\n\ts_cmp_lg_u32 %6, 0\n\ts_cbranch_scc1 3f\n\tbuffer_load_dword %2, %3, %4 offen sc1 lds\n"
        "3:\n\ts_sleep %9\n\ts_add_u32 %0, %0, 1\n\ts_and_b32 vcc_lo, %0, 0x7ff\n\ts_cbranch_scc1 2f\n\t"
        "v_mov_b32 %1, 0\n\ts_bitcmp1_b32 %0, 23\n\ts_cbranch_scc0 5f\n\tglobal_store_dword %1, %1, %10 sc1\n"
        "5:\n\tglobal_load_dword %1, %1, %10 sc1\n\ts_waitcnt vmcnt(0)\n\tv_cmp_ne_u32 vcc, -1, %1\n\ts_cbranch_vccnz 4f\n"
        "2:\n\tds_read_b32 %1, %7\n\ts_waitcnt lgkmcnt(0)\n\tv_cmp_gt_i32 vcc, %5, %1\n\ts_cbranch_vccnz 1b\n4:"
        : "=&s"(n), "=&v"(t) : "v"(va), "s"(k.rs), "s"(a), "s"(s), "s"(mute), "v"(ra), "s"(m), "n"(SLP), "s"(k.cm + CM_TMO) : "memory", "vcc", "scc");
    return t;
}
template <int N> DEVI void prog_wait(const Ctx& k, unsigned a, int s) { watch<PWS>(k, min(k.l, N - 1) * 128, a, L_PW, L_PW + k.l * 4, s, k.w); }
template <int OFF> DEVI void dma1(const void* sbase, unsigned voff, unsigned lds) {
    asm volatile("s_add_i32 m0, %2, %3\n\ts_nop 0\n\tglobal_load_lds_dwordx4 %0, %1" NTA :: "v"(voff), "s"(sbase), "s"(lds), "n"(OFF) : "memory", "scc");
}
DEVI void set_m0(unsigned lds) { asm volatile("s_mov_b32 m0, %0" :: "s"(lds) : "memory"); }
template <int OFF> DEVI void dmab(__amdgpu_buffer_rsrc_t rs, unsigned voff, unsigned soff) {
    asm volatile("buffer_load_dwordx4 %0, %1, %2 offen offset:%3 sc1 lds" :: "v"(voff), "s"(rs), "s"(soff), "n"(OFF) : "memory");
}
#define PIN(x) asm volatile("" : "+v"(x))
#define PINS(x) asm volatile("" : "+s"(x))
#define LEAD()
#define TOUCH(x) asm volatile("" :: "v"(x))
DEVI float csel(float a, float b, unsigned long long m) { float d; asm volatile("v_cndmask_b32_e64 %0, %1, %2, %3" : "=v"(d) : "v"(a), "v"(b), "s"(m)); return d; }
#define LO8(x) __builtin_shufflevector(x, x, 0, 1, 2, 3, 4, 5, 6, 7)
#define HI8(x) __builtin_shufflevector(x, x, 8, 9, 10, 11, 12, 13, 14, 15)
#define CAT8(a, b) __builtin_shufflevector(a, b, 0, 1, 2, 3, 4, 5, 6, 7, 8, 9, 10, 11, 12, 13, 14, 15)
template <bool AG> DEVI void smf(f32x4& c, const bf16x8& a, const bf16x16& w, unsigned ix) {
    if constexpr (AG) asm volatile("v_smfmac_f32_16x16x64_bf16 %0, %1, %2, %3" : "+v"(c) : "v"(a), "a"(w), "v"(ix));
    else asm volatile("v_smfmac_f32_16x16x64_bf16 %0, %1, %2, %3" : "+v"(c) : "v"(a), "v"(w), "v"(ix));
}
#define WTAB(X) X(0, "{a[0:7]}", "{a[0:3]}", "{a[4:7]}") X(1, "{a[8:15]}", "{a[8:11]}", "{a[12:15]}") X(2, "{a[16:23]}", "{a[16:19]}", "{a[20:23]}") X(3, "{a[24:31]}", "{a[24:27]}", "{a[28:31]}") X(4, "{a[32:39]}", "{a[32:35]}", "{a[36:39]}") X(5, "{a[40:47]}", "{a[40:43]}", "{a[44:47]}") X(6, "{a[48:55]}", "{a[48:51]}", "{a[52:55]}") X(7, "{a[56:63]}", "{a[56:59]}", "{a[60:63]}") X(8, "{a[64:71]}", "{a[64:67]}", "{a[68:71]}") X(9, "{a[72:79]}", "{a[72:75]}", "{a[76:79]}") X(10, "{a[80:87]}", "{a[80:83]}", "{a[84:87]}") X(11, "{a[88:95]}", "{a[88:91]}", "{a[92:95]}") X(12, "{a[96:103]}", "{a[96:99]}", "{a[100:103]}") X(13, "{a[104:111]}", "{a[104:107]}", "{a[108:111]}") X(14, "{a[112:119]}", "{a[112:115]}", "{a[116:119]}") X(15, "{a[120:127]}", "{a[120:123]}", "{a[124:127]}") X(16, "{a[128:135]}", "{a[128:131]}", "{a[132:135]}") X(17, "{a[136:143]}", "{a[136:139]}", "{a[140:143]}") X(18, "{a[144:151]}", "{a[144:147]}", "{a[148:151]}") X(19, "{a[152:159]}", "{a[152:155]}", "{a[156:159]}") X(20, "{a[160:167]}", "{a[160:163]}", "{a[164:167]}") X(21, "{a[168:175]}", "{a[168:171]}", "{a[172:175]}") X(22, "{a[176:183]}", "{a[176:179]}", "{a[180:183]}") X(23, "{a[184:191]}", "{a[184:187]}", "{a[188:191]}") X(24, "{a[192:199]}", "{a[192:195]}", "{a[196:199]}") X(25, "{a[200:207]}", "{a[200:203]}", "{a[204:207]}") X(26, "{a[208:215]}", "{a[208:211]}", "{a[212:215]}") X(27, "{a[216:223]}", "{a[216:219]}", "{a[220:223]}") X(28, "{a[224:231]}", "{a[224:227]}", "{a[228:231]}") X(29, "{a[232:239]}", "{a[232:235]}", "{a[236:239]}") X(30, "{a[240:247]}", "{a[240:243]}", "{a[244:247]}") X(31, "{a[248:255]}", "{a[248:251]}", "{a[252:255]}") X(32, "{v[96:103]}", "{v[96:99]}", "{v[100:103]}") X(33, "{v[104:111]}", "{v[104:107]}", "{v[108:111]}") X(34, "{v[112:119]}", "{v[112:115]}", "{v[116:119]}") X(35, "{v[120:127]}", "{v[120:123]}", "{v[124:127]}") X(36, "{v[128:135]}", "{v[128:131]}", "{v[132:135]}") X(37, "{v[136:143]}", "{v[136:139]}", "{v[140:143]}") X(38, "{v[144:151]}", "{v[144:147]}", "{v[148:151]}") X(39, "{v[152:159]}", "{v[152:155]}", "{v[156:159]}") X(40, "{v[160:167]}", "{v[160:163]}", "{v[164:167]}") X(41, "{v[168:175]}", "{v[168:171]}", "{v[172:175]}") X(42, "{v[176:183]}", "{v[176:179]}", "{v[180:183]}") X(43, "{v[184:191]}", "{v[184:187]}", "{v[188:191]}") X(44, "{v[192:199]}", "{v[192:195]}", "{v[196:199]}") X(45, "{v[200:207]}", "{v[200:203]}", "{v[204:207]}") X(46, "{v[208:215]}", "{v[208:211]}", "{v[212:215]}") X(47, "{v[216:223]}", "{v[216:219]}", "{v[220:223]}") X(48, "{v[224:231]}", "{v[224:227]}", "{v[228:231]}") X(49, "{v[232:239]}", "{v[232:235]}", "{v[236:239]}") X(50, "{v[240:247]}", "{v[240:243]}", "{v[244:247]}") X(51, "{v[248:255]}", "{v[248:251]}", "{v[252:255]}")
template <int ID> DEVI void smfw(f32x4& c, const bf16x8& a, const bf16x16& w, unsigned ix) {
#define X(i, P, L, H) if constexpr (ID == i) asm volatile("v_smfmac_f32_16x16x64_bf16 %0, %1, %2, %3" : "+v"(c) : "v"(a), P(w), "v"(ix));
    WTAB(X)
#undef X
}
template <int ID, int HF, bool WB, bool FIRST> DEVI void mfw(f32x4& c, const bf16x16& w, const bf16x8& x) {
#define Y(R, W8) { if constexpr (FIRST && WB) asm volatile("v_mfma_f32_16x16x32_bf16 %0, %2, %1, 0" : "=&v"(c) : R(W8), "v"(x)); \
    else if constexpr (FIRST) asm volatile("v_mfma_f32_16x16x32_bf16 %0, %1, %2, 0" : "=&v"(c) : R(W8), "v"(x)); \
    else if constexpr (WB) asm volatile("v_mfma_f32_16x16x32_bf16 %0, %2, %1, %0" : "+v"(c) : R(W8), "v"(x)); \
    else asm volatile("v_mfma_f32_16x16x32_bf16 %0, %1, %2, %0" : "+v"(c) : R(W8), "v"(x)); }
#define X(i, P, L, H) if constexpr (ID == i) { if constexpr (HF) Y(H, HI8(w)) else Y(L, LO8(w)) }
    WTAB(X)
#undef X
#undef Y
}
template <bool AG, bool FIRST> DEVI void mfa(f32x4& c, const bf16x8& w, const bf16x8& x) {
    if constexpr (AG && FIRST) asm volatile("v_mfma_f32_16x16x32_bf16 %0, %1, %2, 0" : "=&v"(c) : "a"(w), "v"(x));
    else if constexpr (AG) asm volatile("v_mfma_f32_16x16x32_bf16 %0, %1, %2, %0" : "+v"(c) : "a"(w), "v"(x));
    else if constexpr (FIRST) asm volatile("v_mfma_f32_16x16x32_bf16 %0, %1, %2, 0" : "=&v"(c) : "v"(w), "v"(x));
    else asm volatile("v_mfma_f32_16x16x32_bf16 %0, %1, %2, %0" : "+v"(c) : "v"(w), "v"(x));
}
template <bool AG, bool FIRST> DEVI void mfb(f32x4& c, const bf16x8& x, const bf16x8& w) {
    if constexpr (AG && FIRST) asm volatile("v_mfma_f32_16x16x32_bf16 %0, %1, %2, 0" : "=&v"(c) : "v"(x), "a"(w));
    else if constexpr (AG) asm volatile("v_mfma_f32_16x16x32_bf16 %0, %1, %2, %0" : "+v"(c) : "v"(x), "a"(w));
    else if constexpr (FIRST) asm volatile("v_mfma_f32_16x16x32_bf16 %0, %1, %2, 0" : "=&v"(c) : "v"(x), "v"(w));
    else asm volatile("v_mfma_f32_16x16x32_bf16 %0, %1, %2, %0" : "+v"(c) : "v"(x), "v"(w));
}
DEVI f32x4 mf(const bf16x8& a, const bf16x8& b, const f32x4& c) { return __builtin_amdgcn_mfma_f32_16x16x32_bf16(__builtin_bit_cast(hbf8, a), __builtin_bit_cast(hbf8, b), c, 0, 0, 0); }
template <int N, class F> DEVI void sfor(F f) { [&]<int... I>(std::integer_sequence<int, I...>) { (f(std::integral_constant<int, I>{}), ...); }(std::make_integer_sequence<int, N>{}); }
DEVI bf16x8 lds128(const char* p) { return *(const bf16x8*)p; }
DEVI bf16x4 ldstr(const char* p) { return __builtin_amdgcn_ds_read_tr16_b64_v4i16((LAS bf16x4*)p); }
DEVI bf16x8 ldstr2(const char* p, int d) { bf16x4 a = ldstr(p), b = ldstr(p + d); return __builtin_shufflevector(a, b, 0, 1, 2, 3, 4, 5, 6, 7); }

#define STAMP(s, k)
#define QSTAMP(f, i)
#define STOP(s)
#define PFLUSH(s)
#define PROF_DECL
#define RSTAMP(i)
#define XSTAMP(i)
#define PCOUNT(i)
#define PROF_OUT

#define HSTAMP STAMP
#define HFLUSH PFLUSH
#define HTRACE(s)
#define TROUT
#define TRHINT
#define AQ(m, i)
#define AC3(i, j, k) { }
#define AST(i)
#define AAC(i)
#define ACCM 0
#define HSTOP(m) if constexpr ((ACCM != 0) == m) { STOP(0) }
#define PC8(i, x)
#define PROF_ONLY(x)
#define PROF_S(x)
template <int RB, bool STD> DEVI int swz(int row) { return RB == 384 ? ((row >> 1) & 1) | ((row >> 2) & 2) : RB == 128 ? (row >> 1) & 3 : STD ? (row & 3) | ((row >> 1) & 4) : row & 7; }
template <int RB, bool STD> DEVI unsigned wsrc(unsigned id, int ld) { const unsigned row = id / (RB / 16), c = id % (RB / 16); return row * ld + ((c ^ (swz<RB, STD>(row) << 1)) << 4); }
template <int RB, bool STD> DEVI int wp_adr(int T, int l) { const int row = (STD ? 8 : 4) * (l >> 4) + ((l >> 2) & 3); return row * RB + ((T ^ swz<RB, STD>(row)) << 5) + (l & 3) * 8; }
template <int RB, bool STD> DEVI bf16x8 wp_read(const char* blk, int T, int l) { return ldstr2(blk + wp_adr<RB, STD>(T, l), (STD ? 4 : 16) * RB); }
template <int NU, class CNT, class STEP> DEVI void cstream(CNT cnt, STEP step) {
    sfor<NU>([&](auto V) { constexpr int v = decltype(V)::value;
        asm volatile("s_waitcnt vmcnt(%0) lgkmcnt(0)\n\ts_barrier" :: "n"(decltype(cnt(V))::value * (v ? 1 : 2)) : "memory");
        step(V);
    });
}
template <int NU, int NI, int D, class ISSUE, class READ> DEVI void wstream(char* ring, ISSUE issue, READ read) {
    const unsigned base = (unsigned)(size_t)(LAS char*)ring;
    sfor<D>([&](auto U) { constexpr int u = decltype(U)::value; if constexpr (u < NU) issue(U, base); });
    sfor<NU>([&](auto U) {
        constexpr int u = decltype(U)::value, left = (NU - u < D ? NU - u : D) - 1;
        asm volatile("s_waitcnt vmcnt(%0)" :: "n"(left * NI) : "memory");
        read(U, ring + (u % D) * NI * 1024);
        __builtin_amdgcn_s_waitcnt(0xc07f);
        asm volatile("" ::: "memory");
        if constexpr (u + D < NU) issue(std::integral_constant<int, u + D>{}, base);
    });
}

constexpr int L_XN = 0;
constexpr int L_K = 40960;
constexpr int L_MEM = L_K + 12288;
constexpr int L_QM = L_MEM + 10240;
constexpr int L_Q = L_QM + 12288;
constexpr int L_W = 94208;
#define SLOT(i) ((i) % 3 == 0 ? 45056 : (i) % 3 == 1 ? 69632 : L_W + 40960)
#define CMIN(a, b) ((a) < (b) ? (a) : (b))
#define IC(x) std::integral_constant<int, (x)>{}
#define CV(X) decltype(X)::value
DEVI bf16x8 lr128(unsigned a) { return *(const LAS bf16x8*)(size_t)a; }
DEVI bf16x4 lrtr(unsigned a) { return __builtin_amdgcn_ds_read_tr16_b64_v4i16((LAS bf16x4*)(size_t)a); }
#define CAT4(a, b) __builtin_shufflevector(a, b, 0, 1, 2, 3, 4, 5, 6, 7)
DEVI void lw128(unsigned a, u32x4 v) { *(LAS u32x4*)(size_t)a = v; }
DEVI void lw64(unsigned a, u32x2 v) { *(LAS u32x2*)(size_t)a = v; }
DEVI void lw32(unsigned a, unsigned v) { *(LAS unsigned*)(size_t)a = v; }
template <int N> DEVI void bar_lgkm() { asm volatile("s_waitcnt lgkmcnt(%0)\n\ts_barrier" :: "n"(N) : "memory"); }
DEVI void atom1(__amdgpu_buffer_rsrc_t rs, unsigned voff, unsigned soff) {
    unsigned one; asm volatile("v_mov_b32 %0, 1\n\tbuffer_atomic_add %0, %1, %2, %3 offen" : "=&v"(one) : "v"(voff), "s"(rs), "s"(soff) : "memory");
}
DEVI unsigned vadd(unsigned v, unsigned sc) { unsigned d; asm volatile("v_add_u32 %0, %1, %2" : "=v"(d) : "s"(sc), "v"(v)); return d; }
DEVI float vzero() { float z; asm volatile("v_mov_b32 %0, 0" : "=v"(z)); return z; }
template <int S> DEVI unsigned slot_adr(unsigned a) { if constexpr (S % 3 == 2) return vadd(a, SLOT(2) - SLOT(0)); else return a + (SLOT(S) - SLOT(0)); }
template <int B, int I> DEVI void xn_op(const Ctx& k, unsigned v, unsigned src, unsigned dst) {
    if constexpr (I == 0) set_m0(dst + B * 8192);
    else if constexpr (I == 1) dmab<0>(k.rs, v, src + B * 16384);
    else dmab<1024>(k.rs, v, src + B * 16384);
}
DEVI void xn_all(const Ctx& k, unsigned v, unsigned src, unsigned dst) {
    sfor<5>([&](auto B) { xn_op<CV(B), 0>(k, v, src, dst); asm volatile("s_nop 0"); xn_op<CV(B), 1>(k, v, src, dst); xn_op<CV(B), 2>(k, v, src, dst); });
}
struct Xn {
    unsigned src, dst, sg, need, t, sb, se, l16, c, off;
    unsigned long long flip, m;
    DEVI void init(const Ctx& k) {
        src = k.seg(PG_XNA, 0) + k.w * 2048; dst = L_XN + k.w * 2048; sg = 0; off = ~0u;
        const unsigned q = (unsigned)(k.par >> PG_XNA), d = q ^ (q >> 1);
        flip = ((unsigned long long)(d & 1) << 16) | ((unsigned long long)(d & 2) << 31) | ((unsigned long long)(d & 4) << 46);
    }
    template <int I> DEVI void next(const Ctx& k, unsigned l16) {
        if constexpr (I == 0) { sg += 1; src += 131072; PINS(sg); PINS(src); }
        if constexpr (I == 1) asm volatile("s_bitcmp1_b64 %1, %2\n\ts_cselect_b32 %0, 0x2000, 0" : "=s"(t) : "s"(flip), "s"(sg) : "scc");
        if constexpr (I == 2) { src ^= t; PINS(src); }
        if constexpr (I == 3) asm volatile("v_and_b32 %0, 0x380, %1" : "=v"(c) : "v"(l16));
        if constexpr (I == 4) c = ld32(k.rs, c, k.p0 + CNT(C_XN, 0));
        if constexpr (I == 5) { need = sg - (k.c ? 0u : 8u); PINS(need); }
        if constexpr (I == 6) { need = sg < 64u ? need : 0x7FFFFFFFu; PINS(need); }
        if constexpr (I == 7) asm volatile("v_cmp_gt_i32_e64 %0, %1, %2" : "=s"(m) : "s"(need), "v"(c) : "memory");
        if constexpr (I == 8) asm volatile("s_cmp_lg_u64 %2, 0\n\ts_cselect_b32 %0, -1, 0\n\ts_cselect_b32 %1, 0x200000, %3" : "=&s"(off), "=s"(se) : "s"(m), "s"(src) : "scc");
        if constexpr (I == 13) set_m0(L_MEM);
        if constexpr (I == 14) dmab<0>(k.rs, c, k.p0 + CNT(C_XN, 0));
        if constexpr (I == 15) c = *(volatile LAS unsigned*)(size_t)(l16 + L_MEM);
        if constexpr (I == 11) { need = sg < 64u ? need : 0x80000000u; PINS(need); }
        if constexpr (I == 12) {
            if (__builtin_expect(m != 0, 0)) {
_Pragma("unroll 1")
                for (unsigned spins = 0; __any((int)*(volatile LAS unsigned*)(size_t)(l16 + L_MEM) < (int)need) && !spin_fail(k, spins >> 2); ++spins) { next<3>(k, l16); __builtin_amdgcn_s_sleep(1); next<14>(k, l16); }
            }
            se = src; }
        if constexpr (I == 10) asm volatile("s_cmp_lg_u32 %1, 64\n\ts_cselect_b32 %0, %0, 0" : "+s"(off) : "s"(sg) : "scc");
    }
    template <int B, int I> DEVI void op(const Ctx& k) {
        if constexpr (I == 0) { set_m0(dst + B * 8192); sb = se + B * 16384; PINS(sb); }
        else dmab<(I - 1) * 1024>(k.rs, l16, sb);
    }
    template <int NB> DEVI void late(const Ctx& k, unsigned l16) {
        xn_wait(k, l16, sg);
        se = src;
        sfor<NB>([&](auto B) { op<CV(B), 0>(k); asm volatile("s_nop 0"); op<CV(B), 1>(k); op<CV(B), 2>(k); });
        DRAIN();
    }
};
template <int OFF> DEVI u32x4 ald128(__amdgpu_buffer_rsrc_t rs, unsigned v, unsigned s) { u32x4 d; asm volatile("buffer_load_dwordx4 %0, %1, %2, %3 offen offset:%4 sc1" : "=v"(d) : "v"(v), "s"(rs), "s"(s), "n"(OFF) : "memory"); return d; }
#define MADR(b) (L_MEM + ((b) < 6 ? ((b) - 1) * 2048 : ((b) - 6) * 2048 + 3072))
struct Gather {
    u32x4 p0, p1, v0, v1;
    unsigned mx, a, aw, b, base, wo, dst;
    unsigned long long bad;
    DEVI void init(const Ctx& k) { base = k.memf(0); wo = k.w * 1024; dst = L_MEM + k.w * 2048; }
    template <int I> DEVI void op(const Ctx& k, unsigned l16, int s) {
        if constexpr (I == 0) { a = ((unsigned)s << 15) & 0x1F8000u; PINS(a); }
        if constexpr (I == 1) { a += base; aw = a + wo; PINS(a); PINS(aw); }
        if constexpr (I == 2) { b = aw + 3072 + (k.w >> 1) * 8192; PINS(b); set_m0(dst); }
        if constexpr (I == 3) v0 = ald128<2048>(k.rs, l16, aw);
        if constexpr (I == 4) v1 = ald128<3072>(k.rs, l16, b);
        if constexpr (I == 5) dmab<2048>(k.rs, l16, aw);
        if constexpr (I == 6) dmab<3072>(k.rs, l16, b);
        if constexpr (I == 7) p0 = ald128<0>(k.rs, l16, a);
        if constexpr (I == 8) p1 = ald128<1024>(k.rs, l16, a);
    }
    template <int N> DEVI void wait() { asm volatile("s_waitcnt vmcnt(%4)" : "+v"(p0), "+v"(p1), "+v"(v0), "+v"(v1) : "n"(N) : "memory"); }
    template <int I> DEVI void fin(unsigned other) {
#define MX3 "v_max3_u32 %0, %0, %3, %4\n\tv_max3_u32 %0, %0, %5, %6"
        if constexpr (I == 0) asm volatile("v_max3_u32 %0, %7, %1, %2\n\t" MX3 : "=&v"(mx) : "v"(v0[0]), "v"(v0[1]), "v"(v0[2]), "v"(v0[3]), "v"(v1[0]), "v"(v1[1]), "s"(other));
        if constexpr (I == 1) asm volatile("v_max3_u32 %0, %0, %1, %2\n\t" MX3 : "+v"(mx) : "v"(v1[2]), "v"(v1[3]), "v"(p0[0]), "v"(p0[1]), "v"(p0[2]), "v"(p0[3]));
        if constexpr (I == 2) asm volatile("v_max3_u32 %1, %1, %2, %3\n\tv_max3_u32 %1, %1, %4, %5\n\tv_cmp_eq_u32_e64 %0, -1, %1" : "=s"(bad) : "v"(mx), "v"(p1[0]), "v"(p1[1]), "v"(p1[2]), "v"(p1[3]));
    }
    DEVI void also(unsigned other) { asm volatile("v_mov_b32 %0, %1" : "=v"(mx) : "s"(other)); }
    DEVI void all() {
        asm volatile("s_waitcnt vmcnt(0)\n\tv_max3_u32 %1, %1, %2, %3\n\tv_max3_u32 %1, %1, %4, %5\n\tv_max3_u32 %1, %1, %6, %7\n\tv_max3_u32 %1, %1, %8, %9\n\tv_max3_u32 %1, %1, %10, %11\n\tv_max3_u32 %1, %1, %12, %13\n\t"
            "v_max3_u32 %1, %1, %14, %15\n\tv_max3_u32 %1, %1, %16, %17\n\tv_cmp_eq_u32_e64 %0, -1, %1" : "=s"(bad) : "v"(mx), "v"(v0[0]), "v"(v0[1]), "v"(v0[2]), "v"(v0[3]), "v"(v1[0]), "v"(v1[1]), "v"(v1[2]), "v"(v1[3]),
            "v"(p0[0]), "v"(p0[1]), "v"(p0[2]), "v"(p0[3]), "v"(p1[0]), "v"(p1[1]), "v"(p1[2]), "v"(p1[3]) : "memory");
    }
    DEVI void check() { also(0); all(); asm volatile("s_nop 3"); }
    unsigned rv, rs; unsigned long long rt;
    template <int I> DEVI void ask(const Ctx& k, unsigned l16) {
        if constexpr (I == 0) asm volatile("v_and_b32 %0, 0xf0, %1\n\tv_lshlrev_b32 %0, 1, %0" : "=v"(rv) : "v"(l16));
        if constexpr (I == 1) asm volatile("buffer_load_dword %0, %0, %1, %2 offen sc1" : "+v"(rv) : "s"(k.rs), "s"(k.p0 + CNT(C_XB, 0)) : "memory");
    }
    template <int I> DEVI void red() {
        if constexpr (I == 0) asm volatile("s_waitcnt vmcnt(4)\n\tv_max_u32_dpp %0, %0, %0 quad_perm:[1,0,3,2] row_mask:0xf bank_mask:0xf" : "+v"(rv));
        if constexpr (I == 1) asm volatile("v_max_u32_dpp %0, %0, %0 quad_perm:[2,3,0,1] row_mask:0xf bank_mask:0xf" : "+v"(rv));
        if constexpr (I == 2) asm volatile("v_max_u32_dpp %0, %0, %0 row_ror:4 row_mask:0xf bank_mask:0xf" : "+v"(rv));
        if constexpr (I == 3) asm volatile("v_max_u32_dpp %0, %0, %0 row_ror:8 row_mask:0xf bank_mask:0xf" : "+v"(rv));
        if constexpr (I == 4) asm volatile("v_readfirstlane_b32 %0, %1" : "=s"(rs) : "v"(rv));
    }
    template <int I> DEVI void hold(unsigned step) {
        if constexpr (I == 0) rt = rt_async();
        if constexpr (I == 3) { PINS(rt); b = spack((unsigned)rt, step); }
        if constexpr (I == 4) asm volatile("s_sub_u32 %0, %1, %0" : "+s"(b) : "s"(rs) : "scc");
        if constexpr (I == 5) if (__builtin_expect(b < 128u, 0)) { while ((int)((rclk() - rs) << 16) < 0) { } }
    }
    template <int I> DEVI void at(const Ctx& k, unsigned l16) {
        if constexpr (I == 1) op<2>(k, l16, 0);
        if constexpr (I >= 2 && I < 15 && (I % 5 == 2 || I % 5 == 4)) op<3 + (I / 5) * 2 + (I % 5) / 4>(k, l16, 0);
    }
    DEVI void again(const Ctx& k, unsigned l16) {
        asm volatile("s_mov_b32 m0, %9\n\tbuffer_load_dwordx4 %2, %4, %5, %7 offen offset:2048 sc1\n\tbuffer_load_dwordx4 %3, %4, %5, %8 offen offset:3072 sc1\n\t"
            "buffer_load_dwordx4 %4, %5, %7 offen offset:2048 sc1 lds\n\tbuffer_load_dwordx4 %4, %5, %8 offen offset:3072 sc1 lds\n\t"
            "buffer_load_dwordx4 %0, %4, %5, %6 offen sc1\n\tbuffer_load_dwordx4 %1, %4, %5, %6 offen offset:1024 sc1\n\ts_waitcnt vmcnt(0)"
            : "+v"(p0), "+v"(p1), "+v"(v0), "+v"(v1) : "v"(l16), "s"(k.rs), "s"(a), "s"(aw), "s"(b), "s"(dst) : "memory");
        check();
    }
};
DEVI void clr2(f32x4& c) { f32x2 a, b; asm volatile("v_mov_b64 %0, 0\n\tv_mov_b64 %1, 0" : "=v"(a), "=v"(b)); c = __builtin_shufflevector(a, b, 0, 1, 2, 3); }
#define NOP7_3(a) asm volatile("s_nop 7" : "+v"(a[0]), "+v"(a[1]), "+v"(a[2]))

struct G3 {
    f32x4 a0[3], a1[3];
    bf16x8 x0[20], x1[20], wl[20];
    unsigned l16, lwv;
    template <int KS> DEVI void r0() { x0[KS] = lr128(l16 + L_XN + KS * 2048); }
    template <int KS> DEVI void r1() { x1[KS] = lr128(l16 + L_XN + KS * 2048 + 1024); }
    template <int KS> DEVI void rw() { if constexpr (KS >= 4) wl[KS] = lr128(lwv + (KS - 4) * 4096); }
    template <bool WL, int KS> DEVI void pre() { if constexpr (WL && KS >= 4) asm volatile("" :: "v"(wl[KS]), "v"(x0[KS]), "v"(x1[KS])); else asm volatile("" :: "v"(x0[KS]), "v"(x1[KS])); }
    template <int ID0, bool WL, int ks, int m> DEVI void mm(const bf16x16 (&W)[52]) {
        constexpr int b = ks >> 1, t = m % 3, id = ID0 + t * 10 + b;
        f32x4& c = m < 3 ? a0[t] : a1[t];
        const bf16x8& x = m < 3 ? x0[ks] : x1[ks];
        if constexpr (WL && t == 2 && b >= 2) { if constexpr (ID0 == 30) mfb<false, false>(c, x, wl[ks]); else mfa<false, false>(c, wl[ks], x); }
        else mfw<id % 52, ks & 1, ID0 == 30, ks == 0>(c, W[id % 52], x);
        SCHED0();
    }
    template <int ID0, bool WL, int K0, int K1, int PF, bool H6 = false, class HOOK> DEVI void run(const bf16x16 (&W)[52], HOOK hook) {
        sfor<K1 - K0>([&](auto KK) {
            constexpr int kk = CV(KK), ks = K0 + kk;
            sfor<6>([&](auto M) {
                constexpr int m = CV(M);
                mm<ID0, WL, ks, m>(W);
                if constexpr (m == 5 && ks + 1 < K1) { pre<WL, (ks + 1) % 20>(); SCHED0(); }
                if constexpr (ks + 2 < PF) {
                    if constexpr (m == 0) r0<(ks + 2) % 20>();
                    if constexpr (m == 1) r1<(ks + 2) % 20>();
                    if constexpr (m == 3 && WL) rw<(ks + 2) % 20>();
                }
                if constexpr (H6) hook(IC(kk * 6 + m));
                else { hook(IC(kk * 7 + (m < 5 ? m : 6))); if constexpr (m == 4) hook(IC(kk * 7 + 5)); }
                SCHED0();
            });
        });
    }
    template <int ID0, bool WL, int F0, int F1, int PF, class HOOK> DEVI void go(const bf16x16 (&W)[52], HOOK hook) {
        sfor<F1 - F0>([&](auto FF) {
            constexpr int f = F0 + CV(FF), ks = f / 6, m = f % 6;
            mm<ID0, WL, ks, m>(W);
            if constexpr (m == 5 && f + 1 < F1) { pre<WL, (ks + 1) % 20>(); SCHED0(); }
            if constexpr (ks + 2 < PF) rd<WL, (f + 12) % 120>();
            hook(IC(f));
            SCHED0();
        });
    }
    template <int P> DEVI void home() {
        if constexpr (P == 0) asm volatile("" : "+{v[12:15]}"(a0[0]), "+{v[16:19]}"(a0[1]), "+{v[20:23]}"(a0[2]), "+{v[24:27]}"(a1[0]), "+{v[28:31]}"(a1[1]), "+{v[32:35]}"(a1[2]));
        else asm volatile("" : "+{v[36:39]}"(a0[0]), "+{v[40:43]}"(a0[1]), "+{v[44:47]}"(a0[2]), "+{v[48:51]}"(a1[0]), "+{v[52:55]}"(a1[1]), "+{v[56:59]}"(a1[2]));
    }
    template <bool WL, int F> DEVI void rd() {
        if constexpr (F % 6 == 0) r0<F / 6>();
        if constexpr (F % 6 == 1) r1<F / 6>();
        if constexpr (F % 6 == 3 && WL) rw<F / 6>();
    }
    u32x2 o;
    template <int I, int OFF> DEVI void out4(unsigned d0) {
        if constexpr (I >= 0 && I < 24) {
            constexpr int t = I / 8, rt = (I / 4) & 1, j = I & 1;
            const f32x4& a = rt ? a1[t] : a0[t];
            if constexpr (I % 4 < 2) { o[j] = cvtpk(a[2 * j], a[2 * j + 1]); PIN(o[j]); }
            else lw32(d0 + OFF + rt * 6144 + t * 1024 + j * 4, o[j]);
        }
    }
    template <int I, int OFF> DEVI void out(unsigned d0, unsigned d1) {
        if constexpr (I >= 0 && I < 18) {
            constexpr int t = I / 6, rt = (I / 3) & 1;
            const f32x4& a = rt ? a1[t] : a0[t];
            if constexpr (I % 3 == 0) { o[0] = cvtpk(a[0], a[1]); PIN(o[0]); }
            else if constexpr (I % 3 == 1) { o[1] = cvtpk(a[2], a[3]); PIN(o[1]); }
            else if constexpr (OFF > 0) lw64(d0 + (OFF - 1) + rt * 6144 + t * 1024, o);
            else lw64((t == 1 ? d1 : d0) + OFF + rt * 6144 + (t == 2 ? 1024 : 0), o);
        }
    }
};
#define SPIX(l) (((l) & 2) ? 0xEEEEu : 0x4444u)
template <int I> DEVI void fold_op(f32x4& a, unsigned& o) {
    if constexpr (I == 0) asm volatile("v_add_f32 %0, %0, %1" : "+v"(a[0]) : "v"(a[2]));
    if constexpr (I == 1) asm volatile("v_add_f32 %0, %0, %1" : "+v"(a[1]) : "v"(a[3]));
    if constexpr (I == 2) asm volatile("v_cvt_pk_bf16_f32 %0, %1, %2" : "=v"(o) : "v"(a[0]), "v"(a[1]));
}

template <int J> DEVI void fold2(f32x4& a, f32x4& b, unsigned (&o)[2]) {
    if constexpr (J < 2) asm volatile("v_add_f32 %0, %0, %2\n\tv_add_f32 %1, %1, %3" : "+v"(a[J]), "+v"(b[J]) : "v"(a[J + 2]), "v"(b[J + 2]));
    else asm volatile("v_cvt_pk_bf16_f32 %0, %2, %3\n\tv_cvt_pk_bf16_f32 %1, %4, %5" : "=&v"(o[0]), "=&v"(o[1]) : "v"(a[0]), "v"(a[1]), "v"(b[0]), "v"(b[1]));
}
DEVI unsigned vshl1(unsigned x, unsigned b) { unsigned d; asm volatile("v_lshl_or_b32 %0, %1, 1, %2" : "=v"(d) : "s"(x), "n"(b)); return d; }

#define ANNOUNCE(s, now, d) (((unsigned)(s) << 16) | (((unsigned)(now) + (d)) & 0xFFFFu))

template <class PUT> DEVI void ln_rows(const u32x4 (&a)[5], int n, int row  , PUT put) {
    float v[40];
#pragma unroll
    for (int i = 0; i < 20; ++i) { v[2 * i] = blo(a[i >> 2][i & 3]); v[2 * i + 1] = bhi(a[i >> 2][i & 3]); }
    f32x2 s2[4] = {{0.f, 0.f}, {0.f, 0.f}, {0.f, 0.f}, {0.f, 0.f}};
#pragma unroll
    for (int i = 0; i < 40; i += 2) s2[(i >> 1) & 3] += (f32x2){v[i], v[i + 1]};
    const f32x2 st = (s2[0] + s2[1]) + (s2[2] + s2[3]);
    float m = row16sum(st[0] + st[1]) * (1.f / 640.f);
    f32x2 q2[4] = {{0.f, 0.f}, {0.f, 0.f}, {0.f, 0.f}, {0.f, 0.f}};
#pragma unroll
    for (int i = 0; i < 40; i += 2) { const f32x2 d = (f32x2){v[i], v[i + 1]} - (f32x2){m, m}; q2[(i >> 1) & 3] += d * d; }
    const f32x2 qt = (q2[0] + q2[1]) + (q2[2] + q2[3]);
    const float q = row16sum(qt[0] + qt[1]);
    const float var = bfr(q * (1.f / 640.f));
    m = bfr(m);
    const float rden = __builtin_amdgcn_rcpf(bfr(__builtin_amdgcn_sqrtf(bfr(var + 1e-5f))));
    const unsigned vo = (n >> 2) * 2048 + (row >> 4) * 1024 + ((n & 3) * 16 + (row & 15)) * 16;
#pragma unroll
    for (int c = 0; c < 5; ++c) {
        u32x4 o;
#pragma unroll
        for (int i = 0; i < 4; ++i) { unsigned d = cvtpk(v[c * 8 + 2 * i] - m, v[c * 8 + 2 * i + 1] - m); o[i] = cvtpk(blo(d) * rden, bhi(d) * rden); }
        put(vo, c, o);
    }
}

struct Soft {
    unsigned long long mA, mY, mC[8];
    unsigned v0, v1;
    unsigned sg, hs;
    float a[6], m, M, t, s, h; u32x2 pk;
    template <int I> DEVI void mask(f32x4 (&sc)[3]) {
        if constexpr (I == 0) asm volatile("v_mov_b32 %0, 0xfe800000" : "=v"(t));
        if constexpr (I >= 1 && I < 9) { float d; asm volatile("v_cndmask_b32_e64 %0, %1, 0, %2" : "=v"(d) : "v"(t), "s"(mC[I - 1])); sc[(I - 1) >> 2][(I - 1) & 3] = d; }
    }
    template <int I> DEVI void op(const Ctx& k, f32x4 (&sc)[3], unsigned a0, unsigned a1, unsigned hv) {
#define FMA(x, y) "v_fmamk_f32 " x ", " x ", 0x3dd53b95, " y "\n\t"
#define EXPC(x) "v_exp_f32_e64 " x ", " x " clamp\n\t"
#define OP3(o, d, x, y) o " " d ", " x ", " y "\n\t"
#define SA(txt) asm volatile(txt : "+v"(a[0]), "+v"(a[1]), "+v"(a[2]), "+v"(a[3]), "+v"(m), "+v"(t))
#define SB(txt) asm volatile(txt : "+v"(a[0]), "+v"(a[1]), "+v"(a[2]), "+v"(a[3]), "+v"(m), "+v"(t), "+v"(s), "+v"(a[4]), "+v"(a[5]), "+v"(M), "+v"(h) : "s"(hs), "s"(sg))
#define ADD(d, x, y) OP3("v_add_f32", d, x, y)
#define MUL(d, x, y) OP3("v_mul_f32", d, x, y)
#define CND(d, x, y, c) "v_cndmask_b32_e64 " d ", " x ", " y ", " c "\n\t"
        if constexpr (I == 0) asm volatile("v_max3_f32 %0, %1, %2, %3\n\tv_max3_f32 %0, %0, %4, %5" : "=&v"(m) : "v"(sc[0][0]), "v"(sc[0][1]), "v"(sc[0][2]), "v"(sc[0][3]), "v"(sc[1][0]));
        if constexpr (I == 1) asm volatile("v_max3_f32 %0, %0, %1, %2" : "+v"(m) : "v"(sc[1][1]), "v"(sc[1][2]));
        if constexpr (I == 2) asm volatile(CND("%0", "%2", "%3", "%6") CND("%1", "%4", "%5", "%6") : "=&v"(a[2]), "=&v"(a[3]) : "v"(sc[0][2]), "v"(sc[1][0]), "v"(sc[0][3]), "v"(sc[1][1]), "s"(mY));
        if constexpr (I == 3) asm volatile("v_max_f32 %1, %1, %5\n\t" CND("%0", "%2", "%3", "%4") : "=&v"(a[0]), "+v"(m) : "v"(sc[0][0]), "v"(a[2]), "s"(mA), "v"(sc[1][3]));
        if constexpr (I == 4) asm volatile("v_mul_f32 %0, 0xbdd53b95, %1" : "=v"(t) : "v"(m));
        if constexpr (I == 5) asm volatile(CND("%0", "%2", "%3", "%5") CND("%1", "%1", "%4", "%5") : "=&v"(a[1]), "+v"(a[2]) : "v"(sc[0][1]), "v"(a[3]), "v"(sc[1][2]), "s"(mA));
        if constexpr (I == 6) asm volatile(CND("%0", "%0", "%2", "%3") FMA("%1", "%4") : "+v"(a[3]), "+v"(a[0]) : "v"(sc[1][3]), "s"(mA), "v"(t));
        if constexpr (I == 7) SA(FMA("%1", "%5"));
        if constexpr (I == 8) SA(FMA("%2", "%5") FMA("%3", "%5"));
        if constexpr (I == 9) SA(EXPC("%0"));
        if constexpr (I == 10) { asm volatile("v_max3_f32 %0, %1, %2, %3" : "=v"(M) : "v"(m), "v"(sc[2][0]), "v"(sc[2][1]), "v"(sc[2])); a[4] = sc[2][0]; a[5] = sc[2][1]; }
        if constexpr (I == 11) asm volatile("v_mov_b32 %0, %3\n\tv_mov_b32 %1, %4" : "=&v"(t), "=&v"(h), "=v"(s) : "v"(M), "s"(hv));
        if constexpr (I == 12) SB("v_permlane16_swap_b32 %9, %5");
        if constexpr (I == 13) SB(OP3("v_max_f32", "%9", "%9", "%5"));
        if constexpr (I == 14) SB(EXPC("%1"));
        if constexpr (I == 15) SB("v_mov_b32 %5, %9\n\t" ADD("%6", "%0", "%1"));
        if constexpr (I == 16) SB("v_permlane32_swap_b32 %9, %5");
        if constexpr (I == 17) SB(OP3("v_max_f32", "%9", "%9", "%5") "v_mul_f32 %9, 0xbdd53b95, %9");
        if constexpr (I == 18) SB(FMA("%7", "%9") FMA("%8", "%9"));
        if constexpr (I == 19) SB(FMA("%4", "%9"));
        if constexpr (I == 20) SB(EXPC("%2"));
        if constexpr (I == 21) SB(EXPC("%3"));
        if constexpr (I == 22) SB(ADD("%6", "%6", "%2"));
        if constexpr (I == 23) SB(EXPC("%7"));
        if constexpr (I == 24) SB(EXPC("%8"));
        if constexpr (I == 25) SB(ADD("%6", "%6", "%3"));
        if constexpr (I == 26) SB(EXPC("%4"));
        if constexpr (I == 27) SB(MUL("%6", "%12", "%6") ADD("%5", "%7", "%8"));
        if constexpr (I == 28) SB(MUL("%6", "%6", "%4"));
        if constexpr (I == 29) SB(OP3("v_fmac_f32", "%6", "%11", "%5"));
        if constexpr (I == 30) SB("v_fmac_f32_dpp %6, %6, %10 row_ror:8 row_mask:0xf bank_mask:0xf\n\tv_mov_b32 %5, %6");
        if constexpr (I == 31) SB("v_permlane16_swap_b32 %6, %5");
        if constexpr (I == 32) SB(ADD("%6", "%6", "%5") "v_mov_b32 %5, %6");
        if constexpr (I == 33) SB("v_permlane32_swap_b32 %6, %5");
        if constexpr (I == 34) SB(ADD("%6", "%6", "%5"));
        if constexpr (I == 35) SB("v_rcp_f32 %6, %6");
        if constexpr (I == 36) SB(MUL("%4", "%4", "%6") MUL("%7", "%7", "%6"));
        if constexpr (I == 37) SB(MUL("%0", "%0", "%4"));
        if constexpr (I == 38) SB(MUL("%1", "%1", "%4") MUL("%2", "%2", "%4"));
        if constexpr (I == 39) { unsigned x; asm volatile(MUL("%1", "%1", "%4") "v_cvt_pk_bf16_f32 %0, %2, %3" : "=&v"(x), "+v"(a[3]) : "v"(a[0]), "v"(a[1]), "v"(m)); pk[0] = x; }
        if constexpr (I == 40) { unsigned x; asm volatile("v_cvt_pk_bf16_f32 %0, %1, %2" : "=v"(x) : "v"(a[2]), "v"(a[3])); pk[1] = x; }
        if constexpr (I == 41) { st64(k.rs, v0, a0, pk); SCHED0(); asm volatile("v_mul_f32 %1, %1, %2\n\tv_cvt_pk_bf16_f32 %0, %0, %1" : "+v"(a[4]), "+v"(a[5]) : "v"(s)); }
        if constexpr (I == 42) st32(k.rs, v1, a1, __float_as_uint(a[4]));
    }
};
#define QIDX(t, b) ((t) * 10 + (b))
DEVI void qk_role(const Params& p, Ctx& k, char* smem, int h) {
    PROF_DECL
    const int w = k.w;
    bf16x16 W[52];
    Gather G;
    Soft S;
    const int hw = h >> 3, hh = h & 7;
    unsigned l16, ix, dqm, dq, trp, qp;
    {
        const int l = k.l, g = l >> 4, n = l & 15, r = n & 7, u = w & 1;
        const bool hf = n >= 8, dup = w != 1;
        S.mA = __ballot(dup && hf);
        const int lim = (w == 0 ? 64 : w == 1 ? n : 16 + 8 * u + r) - 4 * g;
#pragma unroll
        for (int i = 0; i < 8; ++i) S.mC[i] = __ballot((i & 3) + (i >> 2) * 16 <= lim);
        const bool okm = !(dup && hf);
        S.mY = __ballot((dup && hf) == (w != 0));
        S.v0 = w == 0 ? hh * 512 + ((g >> 1) * 16 + (r >> 1) * 4 + hf * 2 + (r & 1)) * 16 + (g & 1) * 8 : w == 1 ? (hh >> 1) * 1024 + l * 16 + (hh & 1) * 8 : hh * 1024 + (g * 16 + 8 * u + r) * 16 + hf * 8;
        S.v1 = !okm ? OOB : w == 0 ? 4096 + hh * 128 + r * 16 + g * 4 : (w == 1 ? 4096 + n * 16 : 6144 + (8 * u + r) * 16) + (hh >> 1) * 512 + (g >> 1) * 256 + (hh & 1) * 8 + (g & 1) * 4;
        l16 = l * 16; ix = SPIX(l);
        const int uu = u ^ ((n >> 2) & 1);
        dqm = L_QM + (w >> 1) * 3072 + uu * 512 + (g >> 1) * 128 + n * 8 + (g & 1) * 4;
        dq = L_QM + 6144 + (w >> 1) * 3072 + uu * 512 + g * 128 + ((n * 8) ^ ((g >> 1) * 64));
        trp = L_QM + 6144 + (l & 3) * 128 + (((l >> 2) * 8) ^ ((l & 2) * 32));
        qp = w == 1 ? trp + 6144 : (w == 0 ? L_QM : L_Q + 6144 + u * 256) + (l & 1) * 128 + (((l >> 2) * 8) ^ (w == 3 ? 64 : 0));
        u32x8 rg = {S.v0, S.v1, l16, ix, dqm, dq, trp, qp};
        asm volatile("" : "+{v[0:7]}"(rg));
        S.v0 = rg[0]; S.v1 = rg[1]; l16 = rg[2]; ix = rg[3]; dqm = rg[4]; dq = rg[5]; trp = rg[6]; qp = rg[7];
    }
    Xn X;
    G3 A, B;
    X.l16 = l16; A.l16 = l16; B.l16 = l16; B.lwv = vadd(l16, L_W + w * 1024);
    {
        const int l = k.l;
        bf16x8 wt[16];
        u32x2 gv;
        unsigned ra[3];
#pragma unroll
        for (int t = 0; t < 3; ++t) ra[t] = opq(wp_adr<384, true>(w * 3 + t, l) + SLOT(0));
#define QEX(v) ((v) < 0 ? 0 : ((v) < 6) + ((v) == QGS) + 10 * ((v) == QGS + 2) + 8 * ((v) == QGS + 7))
#define QKE(v) ((v) < QGS + 4 ? 0 : (v) == 19 ? 20 : CMIN(v, 3 * ((v) - QGS - 3)))
        auto rdx = [&](auto K) { constexpr int ks = CV(K); A.r0<ks>(); A.r1<ks>(); B.x0[ks] = A.x0[ks]; B.x1[ks] = A.x1[ks]; if constexpr (ks < 14 && QKE(ks + 1) <= ks) B.rw<ks>(); };
        cstream<20>([](auto V) { constexpr int v = CV(V); return IC(QEX(v - 1) + (v < 19 ? 6 : 0)); },
            [&](auto U) { constexpr int u = CV(U), b = u >> 1, k0 = QKE(u - 1), n = (QKE(u) - k0) * 12, nn = n ? n : 12;
                if constexpr (u < 6) hello(k, 0);
                if constexpr (u == QGS) gv = go_ask(k);
                if constexpr (u == QGS + 2) { PIN(gv); go_wait(k, 0, gv); X.init(k); G.init(k); X.l16 = k.c ? OOB : l16; xn_all(k, X.l16, X.src, X.dst); }
                if constexpr (u == QGS + 4) rdx(IC(0));
                if constexpr (u == QGS + 7) { sfor<3>([&](auto I) { X.next<CV(I)>(k, l16); G.op<CV(I)>(k, l16, 0); }); X.se = X.src; }
                if constexpr (u == 18) {
                    asm volatile("" :: "{v[152:155]}"(wt[10]), "{v[156:159]}"(wt[11]), "{v[232:235]}"(wt[12]));
#pragma unroll
                    for (int i = 10; i < 13; ++i) lw128(B.lwv + i * 4096, __builtin_bit_cast(u32x4, wt[i]));
                }
                if constexpr (u == 19) { G.wait<0>(); lw128(l16 + G.dst + 2048, G.v0); lw128(l16 + G.dst + 3072, G.v1); }
                XSTAMP(u)
                const unsigned rb[3] = {slot_adr<u>(ra[0]), slot_adr<u>(ra[1]), slot_adr<u>(ra[2])};
                bf16x4 hf[12];
                SCHED0();
                sfor<nn>([&](auto I) { constexpr int i = CV(I), ks = k0 + i / 12, m = i % 12, t = i >> 1, e = u == QGS + 7 ? 8 : 0, j = ((i - e) * 6) / (nn - e);
                    if constexpr (n > 0) { if constexpr (m < 6) A.mm<0, false, ks, m>(W); else B.mm<30, true, ks, m - 6>(W); }
                    if constexpr (i < e) { if constexpr (i % 4 == 0) X.op<i / 4, 0>(k); if constexpr (i & 1) X.op<i / 4, 1 + (i / 2) % 2>(k); else G.op<3 + i / 4 + (i & 2) * 2>(k, l16, 0); }
                    else if constexpr (u > 0 && u < 18 && (i == e || ((i - e - 1) * 6) / (nn - e) != j))
                        dma1<SLOT(u + 2) + (j / 3) * 12288 + (j % 3) * 1024>((j < 3 ? p.Wk : p.Wq) + (size_t)(u + 2) * 32 * 6144 + h * 192, wsrc<384, true>(w * 192 + (j % 3) * 64 + l, 12288), w * 3072);
                    if constexpr (i == 0 && u > 4 && (u < 15 || u >= 18)) lw128(B.lwv + (u - 5) * 4096, __builtin_bit_cast(u32x4, wt[(u - 5) & 15]));
                    if constexpr (i < 12) {
                        hf[i] = lrtr(rb[t % 3] + (t / 3) * 12288 + (i & 1) * 1536);
                        if constexpr (i & 1) {
                            if constexpr (t == 5 && b >= 2) B.wl[u] = wt[(u - 4) & 15] = CAT4(hf[i - 1], hf[i]);
                            else if constexpr (u & 1) W[QIDX(t, b) % 52] = CAT8(LO8(W[QIDX(t, b) % 52]), CAT4(hf[i - 1], hf[i]));
                            else W[QIDX(t, b) % 52] = __builtin_shufflevector(hf[i - 1], hf[i], 0, 1, 2, 3, 4, 5, 6, 7, -1, -1, -1, -1, -1, -1, -1, -1);
                        }
                    }
                    if constexpr (n > 0 && m == 3 && ks < 19) rdx(IC((ks + 1) % 20));
                    SCHED0(); }); });
        lw128(B.lwv + 15 * 4096, __builtin_bit_cast(u32x4, wt[15]));
        static_assert(QKE(17) == 17, "QGS");
        RAWBAR()
        XSTAMP(20)
        const unsigned d0 = (l16 | ((l16 >> 5) & 8)) ^ (L_K + (w >> 1) * 3072 + (w & 1) * 8);
        sfor<24>([&](auto I) { A.out4<CV(I), 0>(d0); });
        G.v0 = G.p0; G.v1 = G.p0; G.bad = 0;
        X.off = 0; X.l16 = l16;
        if (k.c) X.init(k);
    }
    RSTAMP(2)
    const unsigned po0 = k.p0 + PR_B + hw * 49152 + (w == 0 ? 0 : w == 1 ? 32768 : 16384), po1 = k.p0 + PR_B + hw * 49152 + (w == 0 ? 0 : 32768);
    const unsigned pdum = k.pg(1) + DUMMY_B, pbc = w == 0 ? k.p0 + CNT(C_BC + hw * 2, hh * 8) : pdum + 65536 + h * 512 + w * 128;
    unsigned long long now = 0, done = 0;
    unsigned a0 = po0 + (k.c ? pdum - k.p0 - PR_B : 0u), a1 = po1 + (k.c ? pdum - k.p0 - PR_B : 0u);
    const unsigned hv = w == 1 ? 0u : 0x3F800000u, hw2 = w == 1 ? 0u : 0x00800000u;
    S.sg = 0x3F800000u; S.hs = 0x3F800000u - hw2;
    f32x4 ac[6];
#pragma unroll
    for (int t = 0; t < 6; ++t) clr2(ac[t]);
    unsigned an = 120, al = 110;
    __builtin_amdgcn_s_waitcnt(0xc07f);
    do {
        if (X.off != 0) {
            PC8(1, X.sg)
            X.late<2>(k, l16);
            G.check();
        }
_Pragma("unroll 1")
        for (unsigned spins = 0; G.bad != 0 && !spin_fail(k, spins); ++spins) { PC8(0, X.sg) G.again(k, l16); }
        HSTOP(0) HSTAMP(0, 0)
        clr2(ac[5]);
        asm volatile("s_nop 1" : "+v"(ac[5]));
        smfw<50>(ac[5], __builtin_bit_cast(bf16x8, G.p0), W[50], ix);
    do {
        PROF_S(X.sg - 1)
        f32x4 sc[3];
        {
            bf16x8 m[16], wa[10], wb[10], kf[12];
            bf16x4 q[12], km[12];
            unsigned fo[6];
            m[0] = __builtin_bit_cast(bf16x8, G.p0); m[1] = __builtin_bit_cast(bf16x8, G.p1);
            auto put = [&](auto T) { constexpr int t = CV(T); if constexpr (t < 3) lw32(dq + t * 1024, fo[t]); else lw32(dqm + (t - 3) * 1024, fo[t]); };
            auto rd = [&](auto C) { constexpr int c = CV(C), j = c % 20;
                if constexpr (c < 0) { }
                else if constexpr (c < 20) m[j] = lr128(l16 + MADR(j < 10 ? j : j - 8));
                else if constexpr (c < 40) wa[j] = lr128(B.lwv + (j - 2) * 8192);
                else if constexpr (c < 60) wb[j] = lr128(B.lwv + (j - 2) * 8192 + 4096);
                else if constexpr (c < 80) kf[j] = lr128(l16 + L_K + ((j & 1) * 6 + j / 2) * 1024);
                else if constexpr (c < 100) q[j] = lrtr(qp + j * 512);
                else if constexpr (c < 120) km[j] = lrtr(trp + j * 512);
                else if constexpr (c < 140) A.r0<j>();
                else A.r1<j>(); };
#define PB(i) ((i) < 12 ? (i) / 6 : (i) < 32 ? (i) / 4 - 1 : (i) < 44 ? 7 + ((i) - 32) % 3 : (i) < 54 ? 7 + ((i) - 44) / 2 : 12 + ((i) - 54) % 3)
#define PT(i) ((i) < 12 ? ((i) + 5) % 6 : (i) < 32 ? (3 + (i) % 4) % 6 : (i) < 44 ? (3 + ((i) - 32) / 3) % 6 : (i) < 54 ? 1 + ((i) & 1) : 1 + ((i) - 54) / 3)
            sfor<82>([&](auto I) {
                constexpr int i = CV(I) + 1, b = PB(i), t = PT(i), bn = PB(i + 1);
                constexpr int rt[83] = {-1, 2, -1, -1, -1, 22, 42, 3, -1, 23, 43, 4, -1, 24, 44, 5, -1, 25, 45, 6,
                    -1, 26, 46, 7, -1, 8, 9, 27, 47, 28, 48, 29, 49, -1, -1, 60, -1, 61, 62, 63,
                    -1, 10, 64, -1, 11, 12, -1, 13, 14, -1, -1, 80, 82, 83, 84, 85, 86, 87, 67, 88,
                    89, 69, -1, 91, -1, 71, -1, 121, -1, 100, 102, 104, 106, 108, 110, -1, 140, -1, -1, 141, -1, -1, 122};
                constexpr int r2[83] = {-1, -1, -1, -1, -1, -1, -1, -1, -1, -1, -1, -1, -1, -1, -1, -1, -1, -1, -1, -1,
                    -1, -1, -1, -1, -1, -1, -1, -1, -1, -1, -1, -1, -1, -1, -1, -1, -1, -1, -1, -1,
                    -1, -1, -1, -1, -1, -1, -1, -1, -1, -1, -1, 81, -1, 65, -1, 66, -1, -1, -1, -1,
                    68, 90, -1, 70, -1, 120, -1, -1, -1, 101, 103, 105, 107, 109, 111, -1, -1, -1, -1, -1, -1, -1, -1};
                if constexpr (i < 60) {
                    if constexpr (t == 5 && b >= 2) smf<false>(ac[5], m[b], CAT8(wa[b], wb[b]), ix);
                    else smfw<QIDX(t, b < 10 ? b : b - 8) % 52>(ac[t], m[b], W[QIDX(t, b < 10 ? b : b - 8) % 52], ix);
                } else if constexpr (i < 72) { constexpr int j = i - 60; mfa<false, false>(sc[j & 1], kf[j], CAT4(q[j & ~1], q[j | 1])); }
                else if constexpr (i < 77) A.mm<0, false, (i - 72) / 3, (i - 72) % 3>(W);
                else { constexpr int ks = i - 77; mfa<false, ks == 0>(sc[2], CAT4(km[2 * ks], km[2 * ks + 1]), CAT4(q[2 * ks], q[2 * ks + 1])); }
                SCHED0();
                if constexpr (i == 1) { AQ(3, 3) asm volatile("s_barrier" ::: "memory"); AQ(1, 2) AQ(3, 4) }
                else if constexpr (i == 50) { BARL(3); AQ(1, 3) }
                else if constexpr (i == 68) { BARL(3); AQ(1, 4) QSTAMP(0, 1) }
                else if constexpr (i >= 12 && i < 59) { if constexpr (PT(i + 1) == 5) TOUCH(wb[bn % 10]); TOUCH(m[bn]); }
                else if constexpr (i >= 59 && i < 71) { asm volatile("" :: "v"(kf[(i - 59) % 12]), "v"(q[((i - 59) | 1) % 12])); }
                else if constexpr (i == 71) TOUCH(A.x0[0]);
                else if constexpr (i == 74) TOUCH(A.x0[1]);
                else if constexpr (i >= 76 && i < 82) TOUCH(km[(2 * (i - 76) + 1) % 12]);
                if constexpr (i == 8 || i == 30) asm volatile("" :: "{v[60:63]}"(ac[0]), "{v[28:31]}"(ac[1]), "{v[32:35]}"(ac[2]), "{v[64:67]}"(ac[3]), "{v[68:71]}"(ac[4]), "{v[72:75]}"(ac[5]));
                if constexpr (i == 14) AC3(0, 0, 0)
                if constexpr (i == 16) AC3(1, 6, 1)
                if constexpr (i == 18) AC3(2, 7, 2)
                if constexpr (i == 20) AC3(-1, -1, 3)
                if constexpr (i == 22) AC3(-1, -1, 4)
                if constexpr (i == 24) AC3(-1, -1, 5)
                if constexpr (i == 66) AC3(3, -1, 6)
                if constexpr (i == 2) { now = rt_async(); HSTOP(1) }
                if constexpr (i == APOS) { PINS(now); an += (unsigned)now; PINS(an); }
                if constexpr (i == APOS + 1) an = spack(an, X.sg);
                SCHED0();
                rd(IC(rt[i])); rd(IC(r2[i]));
                if constexpr (i == APOS + 4) st32(k.rs, fo[2], pbc, fo[1]);
                if constexpr (i >= 40 && i < 50 && i % 3 == 1) put(IC(((i - 31) / 3) % 6));
                if constexpr (i == 62 || i == 64) put(IC(i / 2 - 30));
                SCHED0();
                if constexpr (i < 25 && i % 4 != 1) B.out<((i - 2) / 4) * 3 + (i - 2) % 4, 6145>(dq, dq);
                if constexpr (i == APOS + 3) { fo[1] = an; PIN(fo[1]); }
                if constexpr (i == APOS - 1) asm volatile("v_and_b32 %0, %1, %2" : "=v"(fo[2]) : "s"(w ? 0u : RMSK), "v"(l16));
                if constexpr (i >= 37 && i < 49) { constexpr int tt = ((i - 28) / 3) % 6; fold_op<(i - 37) % 3>(ac[tt], fo[tt]); }
                if constexpr (i >= 59 && i < 62) fold_op<i - 59>(ac[1], fo[1]);
                if constexpr (i == 62) fold_op<0>(ac[2], fo[2]);
                if constexpr (i == 63) asm volatile("v_add_f32 %1, %1, %2\n\tv_cvt_pk_bf16_f32 %0, %3, %1" : "=v"(fo[2]), "+v"(ac[2][1]) : "v"(ac[2][3]), "v"(ac[2][0]));
                if constexpr (i == 33) S.mask<0>(sc);
                if constexpr (i >= 51 && i < 59) S.mask<i - 50>(sc);
                if constexpr (i >= 75) S.op<i - 75>(k, sc, a0, a1, hv);
                if constexpr (i == 12) { AQ(3, 5) }
                if constexpr (i == 43) { AQ(3, 6) }
                if constexpr (i == 59) { AQ(3, 7) }
                SCHED0();
            });
        }
        QSTAMP(0, 2)
        {
            unsigned d0;
            auto rest = [&](auto J) { constexpr int j = CV(J);
                if constexpr (j >= 0 && j < 12 && (j % 6 == 0 || j % 3 == 2)) X.op<3 + j / 6, (j % 6 + 1) / 3>(k); };
            sfor<43>([&](auto F) { constexpr int f = CV(F) + 5;
                constexpr int e = f < 6 ? f + 3 : f < 9 ? f - 3 : f;
                A.mm<0, false, e / 6, e % 6>(W);
                if constexpr (f % 6 == 2 || f == 5) TOUCH(A.x1[f / 6]);
                else if constexpr (f == 47) { TOUCH(B.x0[0]); AQ(1, 6) AQ(2, 1) }
                else if constexpr (f % 6 == 5) TOUCH(A.x0[f / 6 + 1]);
                if constexpr (f == 40) { done = rt_async(); QSTAMP(0, 3) AQ(1, 5) }
                if constexpr (f == 21) set_m0(X.dst + 2 * 8192);
                if constexpr (f == 24) { X.sb = X.se + 2 * 16384; PINS(X.sb); }
                if constexpr (f == 42) AC3(4, -1, 7)
                SCHED0();
                if constexpr (f % 6 == 4 && f < 36) A.r0<f / 6 + 2>();
                if constexpr (f % 6 == 1 && f < 42) A.r1<f / 6 + 1>();
                if constexpr (f >= 39 && f < 43) B.rd<true, ((f - 39) / 2) * 6 + (f - 39) % 2>();
                if constexpr (f == 26 || f == 31) X.op<2, f / 15>(k);
                SCHED0();
                S.op<f + 3>(k, sc, a0, a1, hv);
                rest(IC(f - 43));
                if constexpr (f == 44) B.lwv = vadd(l16, L_W + w * 1024);
                SCHED0(); });
            B.go<30, true, 0, 48, 8>(W, [&](auto F) { constexpr int f = CV(F);
                rest(IC(f + 5));
                if constexpr (f == 9) AC3(5, -1, -1)
                if constexpr (f == 2) { a0 = (X.sg & 3) * PR_SZ; PINS(a0); }
                if constexpr (f == 4) { a1 = a0 + po1; a0 += po0; PINS(a0); PINS(a1); }
                if constexpr (f == 8) { S.sg = (X.sg & 4) ? 0xBF800000u : 0x3F800000u; PINS(S.sg); }
                if constexpr (f == 10) { S.hs = S.sg - hw2; PINS(S.hs); }
                if constexpr (f == 14 || f == 16) G.op<f / 16>(k, l16, X.sg);
                if constexpr (f == 22 || f == 28 || f == 32) X.next<(f - 22) / 5>(k, l16);
                if constexpr (f == 35) asm volatile("s_waitcnt vmcnt(6)\n\ts_barrier" ::: "memory");
                if constexpr (f == 20) AC3(6, 1, -1)
                if constexpr (f == 26) A.home<0>();
                if constexpr (f >= 36) A.rd<false, f + 12>();
                if constexpr (f == 38 || f == 40 || f == 45 || f == 46) X.next<(f == 38 ? 5 : f == 40 ? 6 : f - 42)>(k, l16);
                if constexpr (f == 47) { A.pre<false, 8>(); AQ(1, 7) AQ(2, 2) } });
            A.go<0, false, 48, 120, 20>(W, [&](auto F) { constexpr int f = CV(F), i = f - XPOS;
                if constexpr (f == 56) B.home<1>();
                if constexpr (i == 0 || i == 2 || i == 6) X.next<7 + i / 2>(k, l16);
                if constexpr (i >= 3 && i < 15 && ((i - 3) % 6 == 0 || (i - 3) % 3 == 2)) X.op<(i - 3) / 6, ((i - 3) % 6 + 1) / 3>(k);
                if constexpr (f == 58) asm volatile("s_waitcnt vmcnt(1)\n\ts_barrier" ::: "memory");
                if constexpr (f == 57) AC3(7, 2, -1)
                G.ask<f - RPOS>(k, l16);
                if constexpr (f >= 108) B.rd<true, f - 60>();
                SCHED0();
                G.red<f - 110>();
                if constexpr (f == 116) asm volatile("v_bfe_u32 %0, %1, 8, 1\n\tv_lshl_or_b32 %0, %0, 3, %1" : "=&v"(d0) : "v"(l16));
                if constexpr (f == 118) asm volatile("v_xor_b32 %0, %1, %0" : "+v"(d0) : "s"(L_K + (w >> 1) * 3072 + (w & 1) * 8));
                if constexpr (f == 119) { B.pre<true, 8>(); AQ(2, 3) } });
            B.go<30, true, 48, 120, 20>(W, [&](auto F) { constexpr int f = CV(F), i = f - 48;
                constexpr int ko[26] = {0, 1, 2, 4, 3, 6, 5, 8, 7, 9, 10, 11, 12, 13, 14, 16, 15, 18, 17, 20, 19, 21, 22, 23, -1, -1};
                G.hold<f - GPOS + 5>(X.sg);
                if constexpr (i < 24) { SCHED0(); A.out4<ko[i % 26], 0>(d0); }
                G.at<f - GPOS>(k, l16);
                if constexpr (f == 114) { lgkm0(done); PINS(G.rt); }
                if constexpr (f == 115) { PINS(now); HTRACE(X.sg - 2) an = (unsigned)done - (unsigned)now; PINS(an); }
                if constexpr (f == 116) { an = min(an, al); PINS(an); }
                if constexpr (f == 117) { an += DQ; PINS(an); }
                if constexpr (f == 118) { al = an; PINS(al); }
                SCHED0();
                if constexpr (i == 28) AC3(-1, 3, -1)
                if constexpr (i == 50) AC3(-1, 4, -1)
                if constexpr (i == 62) AC3(-1, 5, -1)
                if constexpr (i == 23) { AQ(2, 4) }
                if constexpr (i == 47) { AQ(2, 5) }
                if constexpr (i == 65) { AQ(2, 6) }
                if constexpr (f == 119) { AQ(2, 7) }
                if constexpr (f == 112) asm volatile("" :: "{v[12:15]}"(G.v0), "{v[16:19]}"(G.v1), "{v[20:23]}"(G.p0), "{v[24:27]}"(G.p1), "{v9}"(B.lwv) TRHINT);
                if constexpr (f == 119) G.also(X.off);
                if constexpr (f >= 113 && f < 119) clr2(ac[(f + 6 - 113 + 5) % 6]); });
            static_assert(GPOS >= 70 && GPOS <= 98 && XPOS % 6 == 2 && RPOS > XPOS && RPOS + 1 < XPOS + 5, "GPOS");
            QSTAMP(0, 4) QSTAMP(0, 5)
            HFLUSH(s)
        }
        AST(0)
        PROF_ONLY(G.wait<0>();)
        HSTOP(0) HSTAMP(s, 0) AQ(1, 1) AQ(3, 1)
        G.all();
        SCHED0();
        smfw<50>(ac[5], __builtin_bit_cast(bf16x8, G.p0), W[50], ix);
        AQ(3, 2)
        SCHED0();
    } while (G.bad == 0);
    } while (X.sg != 65u);
    PROF_OUT
}

#define OIDX(t, b) (22 + (t) * 3 + (b))
DEVI void vo_role(const Params& p, Ctx& k, char* smem, int h) {
    PROF_DECL
    const int w = k.w;
    bf16x16 W[52];
    unsigned l16, lwv, ix, dq0, trp, vo, vr;
    {
        const int l = k.l, g = l >> 4, n = l & 15, q = (l >> 2) & 3, pp = l & 3;
        l16 = l * 16; lwv = l16 + L_W + w * 1024; ix = SPIX(l);
        dq0 = L_QM + (24 * w + (n >> 2) * 2 + (n & 1)) * 32 + g * 8 + ((n >> 1) & 1) * 4;
        trp = L_QM + (16 * (g >> 1) + 4 * (g & 1) + 8 * (q >> 1) + (q & 1)) * 32 + pp * 8;
        vo = EXP(h * 512) + (g >> 1) * 256 + n * 16 + (g & 1) * 8;
        u32x8 r = {dq0, trp, vo, ix, l16, lwv, 0u, 0u};
        PIN(r);
        dq0 = r[0]; trp = r[1]; vo = r[2]; ix = r[3]; l16 = r[4]; lwv = r[5];
    }
    Gather G;
    G3 V;
    Xn X;
    V.l16 = l16; V.lwv = lwv; X.l16 = l16;
    f32x4 y[20];
    bf16x8 v[12];
    u32x4 o[10];
    unsigned yb;
    {
        const int l = k.l;
        bf16x8 wt[16];
        bf16x4 q[10][4];
        f32x4 z[9][2];
        u32x2 gv;
        unsigned ra[6];
        const u16* wo = p.Wo + (size_t)h * 192 * 640;
        constexpr int L_V = L_XN + 24576;
#define VEX(v) ((v) < 0 ? 0 : ((v) < 6) + ((v) == VGS) + 10 * ((v) == VGS + 2) + 6 * ((v) == VGS + 6 || (v) == 18))
#define VKE(v) ((v) < VGS + 4 ? 0 : CMIN(20, 7 * ((v) - VGS - 3)))
#define YKE(v) ((v) < 16 ? 0 : 3 * (v) - 45)
        auto put = [&](int t) { st128(k.rs, X.l16, yb + t * 65536, (u32x4){cvtpk(z[t][0][0], z[t][0][1]), cvtpk(z[t][0][2], z[t][0][3]), cvtpk(z[t][1][0], z[t][1][1]), cvtpk(z[t][1][2], z[t][1][3])}); };
        auto rdx = [&](auto K) { constexpr int ks = CV(K); if constexpr (ks < 20) { V.r0<ks % 20>(); V.r1<ks % 20>(); if constexpr (ks < 14) V.rw<ks>(); } };
        auto rdv = [&](auto J) { constexpr int j = CV(J) % 6; v[j] = lr128(l16 + L_V + j * 1024); v[6 + j] = lr128(l16 + L_V + (6 + j) * 1024); };
        cstream<20>([](auto V) { constexpr int v = CV(V); return IC(VEX(v - 1) + (v < 19 ? 6 : 0)); },
            [&](auto U) { constexpr int u = CV(U), k0 = VKE(u - 1), y0 = YKE(u - 1), n = u < 16 ? (VKE(u) - k0) * 6 : u < 18 ? 36 : 18, nn = n ? n : 12, nr = u < 10 ? 3 : u < 16 ? 6 : 4;
                if constexpr (u < 6) hello(k, 0);
                if constexpr (u == VGS) gv = go_ask(k);
                if constexpr (u == VGS + 2) { PIN(gv); go_wait(k, 0, gv); X.init(k); X.l16 = k.c ? OOB : l16; xn_all(k, X.l16, X.src, X.dst); }
                if constexpr (u == VGS + 4) { rdx(IC(0)); rdx(IC(1)); }
                if constexpr (u == VGS + 6) { sfor<3>([&](auto I) { X.next<CV(I)>(k, l16); }); X.se = X.src; yb = k.yx(0, w) + EXP(h * 1024); }
                if constexpr (u == 16) rdv(IC(0));
                XSTAMP(u)
                if constexpr (u == 0 || u == 10 || u == 16) {
#pragma unroll
                    for (int t = 0; t < nr; ++t) ra[t] = opq((u == 0 ? wp_adr<384, true>(w * 3 + t, l) : u == 10 ? wp_adr<768, false>(w * 6 + t, l) : wp_adr<512, false>(w * 4 + t, l)) + SLOT(0));
                }
                unsigned rb[6];
#pragma unroll
                for (int t = 0; t < nr; ++t) rb[t] = slot_adr<u>(ra[t]);
                SCHED0();
                sfor<nn>([&](auto I) { constexpr int i = CV(I), e = u == VGS + 6 ? 9 : 0, j = ((i - e) * 6) / (nn - e), d = u + 2, r0 = u == 18 ? 6 : 0;
                    if constexpr (u < 16 && n > 0) { V.mm<0, true, k0 + i / 6, i % 6>(W); if constexpr (i % 6 == 3) rdx(IC(k0 + i / 6 + 2)); }
                    if constexpr (u >= 16) { constexpr int m = n / 3, ks = (y0 + i / m) % 6, t = (u / 18) * 6 + (i % m) / 2, rt = i & 1;
                        if constexpr (!(ks == 5 && t == 7 && rt)) { mfw<OIDX(t, ks >> 1), ks & 1, true, ks == 0>(z[t][rt], W[OIDX(t, ks >> 1)], v[rt * 6 + ks]); SCHED0(); }
                        if constexpr (i % m == 1 && u < 19) rdv(IC(ks + 1));
                        if constexpr (u == 19 && i < 2) rdv(IC(4 + i));
                        if constexpr (u == 18 && i >= 3 && i < 15 && (i & 1)) put((i - 3) / 2);
                        if constexpr (u == 18 && i < 6) lw128(lwv + (10 + i) * 4096, __builtin_bit_cast(u32x4, wt[10 + i])); }
                    if constexpr (i < e) X.op<i / 3, i % 3>(k);
                    else if constexpr (u > 0 && u < 18 && (i == e || ((i - e - 1) * 6) / (nn - e) != j)) {
                        if constexpr (d < 10) dma1<SLOT(d) + (j / 3) * 12288 + (j % 3) * 1024>(p.Wv + (size_t)(2 * d + j / 3) * 32 * 6144 + h * 192, wsrc<384, true>(w * 192 + (j % 3) * 64 + l, 12288), w * 3072);
                        else if constexpr (d < 16) dma1<SLOT(d) + j * 1024>(wo + (d - 10) * 32 * 640, wsrc<768, false>(w * 384 + j * 64 + l, 1280), w * 6144);
                        else dma1<SLOT(d) + j * 1024>(wo + (d - 16) * 48 * 640 + 384, wsrc<512, false>(w * 384 + j * 64 + l, 1280), w * 6144);
                    }
                    if constexpr (i < 2 && u >= 3 && u < 8) lw128(lwv + (2 * u - 6 + i) * 4096, __builtin_bit_cast(u32x4, wt[(2 * u - 6 + i) & 15]));
                    sfor<2>([&](auto C) { constexpr int c = u == 19 ? 2 * i + CV(C) : i - r0 + CV(C) * 99;
                        if constexpr (c >= 0 && c < 12) {
                            constexpr int t = u < 10 ? c >> 2 : u < 16 ? c >> 1 : 6 + c % 4, r = u < 10 ? c & 3 : u < 16 ? (u - 10) * 2 + (c & 1) : (u - 16) * 3 + c / 4, id = u < 10 ? (t * 10 + u) % 52 : OIDX(t, r >> 2);
                            q[t][r & 3] = lrtr(u < 10 ? rb[t] + (r >> 1) * 12288 + (r & 1) * 1536 : u < 16 ? rb[t] + (c & 1) * 12288 : rb[(t - 6) & 3] + (c / 4) * 8192);
                            if constexpr (u < 10 && u >= 2 && t == 2) { if constexpr (r & 1) V.wl[2 * u + r / 2] = wt[(2 * u - 4 + r / 2) & 15] = CAT4(q[t][r - 1], q[t][r]); }
                            else if constexpr ((r & 3) == 1) W[id] = __builtin_shufflevector(q[t][0], q[t][1], 0, 1, 2, 3, 4, 5, 6, 7, -1, -1, -1, -1, -1, -1, -1, -1);
                            else if constexpr ((r & 3) == 3) W[id] = CAT8(LO8(W[id]), CAT4(q[t][2], q[t][3]));
                        } });
                    SCHED0(); });
                if constexpr (u == VGS + 7) sfor<18>([&](auto I) { V.out<CV(I), 0>(l16 + L_V + ((3 * w) >> 1) * 1024 + ((3 * w) & 1) * 8, l16 + L_V + ((3 * w + 1) >> 1) * 1024 + ((3 * w + 1) & 1) * 8); }); });
        static_assert(VGS >= 6 && VGS < 9, "VGS");
        RAWBAR()
        XSTAMP(20)
        sfor<2>([&](auto B) { X.op<CV(B) + 3, 0>(k); asm volatile("s_nop 0"); X.op<CV(B) + 3, 1>(k); X.op<CV(B) + 3, 2>(k); });
        asm volatile("" : "+v"(z[8][0]), "+v"(z[8][1]), "+v"(z[7][0]), "+v"(z[6][1]));
        put(6); put(8);
        lw32(l16 + L_MEM, ~0u);
        o[7] = (u32x4){cvtpk(z[7][0][0], z[7][0][1]), cvtpk(z[7][0][2], z[7][0][3]), 0u, 0u};
        y[17] = z[7][1];
        yb += 7 * 65536;
        X.off = 0; X.l16 = l16;
        if (k.c) X.init(k);
    }
    RSTAMP(2)
    const unsigned pdum = k.pg(1) + DUMMY_B, pbc = w == 0 ? k.p0 + CNT(C_BC + (h >> 3) * 2 + 1, (h & 7) * 8) : pdum + 98304 + h * 512 + w * 128;
    unsigned long long now = 0, done = 0;
    G.mx = ~0u;
    G.init(k);
    G.base -= 32768;
    const unsigned ypar = (unsigned)(k.par >> (PG_YX + (w >> 1))), yo = ((unsigned)(PG_YX + (w >> 1)) << 21) + (w & 1) * 1048576 + EXP(h * 1024);
    const unsigned ym0 = k.ym(0) + w * 163840, ym2 = k.ym(2) + w * 163840;
    unsigned ym = k.c ? pdum + w * 163840 : ym0, ys, t, c = ~0u, an = 85, al = 90;
#define YT(u) ((u) < 10 ? 2 * ((u) % 5) : ((u) - 10) | 1)
#define YR(u) ((u) < 10 ? (u) / 5 : (u) & 1)
    auto yout = [&](auto U, auto J) { constexpr int u = CV(U), j = CV(J), t = YT(u), rt = YR(u);
        if constexpr (j == 0) { o[t][2 * rt] = cvtpk(y[u][0], y[u][1]); o[t][2 * rt + 1] = cvtpk(y[u][2], y[u][3]); TOUCH(o[t][2 * rt]); TOUCH(o[t][2 * rt + 1]); }
        if constexpr (j == 1 && rt) __builtin_amdgcn_raw_buffer_store_b128(o[t], k.rs, (int)l16, (int)yb, AUX_SC1);
        if constexpr (j == 2 && rt) { yb += t == 8 ? -7 * 65536 : 131072; PINS(yb); TOUCH(o[t]); } };
    auto ymm = [&](auto I) {
        constexpr int i = CV(I), ks = i % 6, u = i / 6, id = OIDX(YT(u), ks >> 1);
        mfw<id, ks & 1, true, ks == 0>(y[u], W[id], v[YR(u) * 6 + ks]);
        SCHED0();
        if constexpr (u > 0 && ks >= 2) yout(IC((u + 19) % 20), IC(ks - 2)); };
    auto mkr = [&]() { asm volatile("v_and_b32 %0, %1, %2" : "=v"(vr) : "s"(w ? 0u : RMSK), "v"(l16)); };
    if (k.c) X.late<5>(k, l16);
    mkr();
    do {
        PROF_S(X.sg - 1)
        f32x4 ac[3];
        sfor<13>([&](auto I) { constexpr int i = CV(I) + 107;
            ymm(IC(i));
            if constexpr (i == TB) { asm volatile("s_waitcnt vmcnt(%0)\n\ts_barrier" :: "n"(TB < 111 ? 7 : 8) : "memory"); AST(5) }
            if constexpr (i == TB + 1) st32(k.rs, vr, pbc + 4, G.mx);
            if constexpr (i == 119) V.pre<true, 0>();
            if constexpr (i == 113) G.ask<0>(k, l16);
            if constexpr (i == 119) G.ask<1>(k, l16);
            if constexpr (i == 114) V.r0<0>();
            if constexpr (i == 115) V.r1<0>();
            if constexpr (i == 117) V.r0<1>();
            if constexpr (i == 118) V.r1<1>();
            SCHED0(); });
        HSTAMP(s, 4)
        V.run<0, true, 0, 16, 17, true>(W, [&](auto I) { constexpr int i = CV(I);
            if constexpr (i >= 2) yout(IC(19), IC(i - 2));
            if constexpr (i == 8) { t = (X.sg & 7) * 2; PINS(t); }
            if constexpr (i == 10) { yb = (ypar >> t) & 1; PINS(yb); }
            if constexpr (i == 14) { t = (t << 21) + yo; PINS(t); }
            if constexpr (i == 16) { yb = (yb << 13) + t; PINS(yb); }
            if constexpr (i == 20) { c = X.sg; PINS(c); }
            if constexpr (i == 22) { G.a = max(c, 1u) << 15; PINS(G.a); }
            if constexpr (i == 28) G.op<1>(k, l16, 0);
            if constexpr (i == 40) { an = min(an, al) + DV; PINS(an); }
            if constexpr (i == 44) { al = an; PINS(al); }
            if constexpr (i == 32 || i == 34 || i == 38) X.next<(i - 31) / 3>(k, l16);
            if constexpr (i == 23 || i == 29) { AAC(4 + i / 29) }
            if constexpr (i == 83 || i == 89) { AAC(6 + i / 89) }
            if constexpr (i == YPOS) { AST(6) }
            if constexpr (i == YPOS + 1) { AST(7) }
            if constexpr (i == YPOS) { asm volatile("s_waitcnt vmcnt(0)\n\ts_barrier" ::: "memory"); }
            if constexpr (i == YPOS - 2) { G.mx = vshl1(c, 0); mkr(); }
            if constexpr (i == YPOS + 1) st32(k.rs, vr, pbc + 4, G.mx);
            SCHED0();
            G.red<i - YPOS - 2>();
            G.hold<i - GPOSY * 6 + 5>(c);
            G.at<i - GPOSY * 6>(k, l16);
            if constexpr (i == 90) PINS(G.rt);
            if constexpr (i == 95) V.pre<true, 16>();
            if constexpr (i >= 92 && i < 95) clr2(ac[(i - 92) % 3]); });
        static_assert(GPOSY * 6 + 2 > YPOS + 1 && GPOSY <= 13, "GPOSY");
        HSTAMP(s, 5)
        HFLUSH(s - 1)
        f32x4 acc[10];
        f32x2 z[20];
        auto clr = [&](auto I) { constexpr int i = CV(I); asm volatile("v_mov_b64 %0, 0" : "=v"(z[i])); if constexpr (i & 1) acc[i >> 1] = __builtin_shufflevector(z[i - 1], z[i], 0, 1, 2, 3); };
        bf16x4 al[3], ah[3];
        unsigned d0, d1;
        {
            bf16x8 m[10], wa[10], wb[10];
            auto rd = [&](auto R) { constexpr int r = CV(R), b = 2 + r / 3;
                if constexpr (b < 10) {
                    if constexpr (r % 3 == 0) m[b] = lr128(l16 + MADR(b));
                    if constexpr (r % 3 == 1) wa[b] = lr128(lwv + (b - 2) * 8192);
                    if constexpr (r % 3 == 2) wb[b] = lr128(lwv + (b - 2) * 8192 + 4096);
                } else if constexpr (b < 13) {
                    if constexpr (r % 3 == 0) V.r0<b + 7>();
                    if constexpr (r % 3 == 1) V.rw<b + 7>();
                    if constexpr (r % 3 == 2) V.r1<b + 7>();
                } };
            AST(0)
            G.wait<0>();
            HSTOP(0) HSTAMP(s, 0) AST(1)
            V.run<0, true, 16, 17, 0, true>(W, [&](auto I) { constexpr int i = CV(I);
                G.fin<i>(0);
                if constexpr (i == 3) {
                    if (__builtin_expect(G.bad != 0, 0)) {
                        unsigned spins = 0;
_Pragma("unroll 1")
                        do { PC8(0, c) G.again(k, l16); } while (G.bad != 0 && !spin_fail(k, spins++));
                        HSTOP(0) HSTAMP(s, 0)
                    }
                    asm volatile("s_barrier" ::: "memory");
                    AST(2)
                }
                if constexpr (i == 4) { now = rt_async(); HSTOP(1) SCHED0(); }
                if constexpr (i >= 3) rd(IC(i - 3)); });
            m[0] = __builtin_bit_cast(bf16x8, G.p0); m[1] = __builtin_bit_cast(bf16x8, G.p1);
            sfor<30>([&](auto I) {
                constexpr int i = CV(I), b = i / 3, t = i % 3;
                if constexpr (t == 2 && b >= 2) smf<false>(ac[2], m[b], CAT8(wa[b], wb[b]), ix); else smfw<t * 10 + b>(ac[t], m[b], W[t * 10 + b], ix);
                SCHED0();
                if constexpr (i >= 6 && i < 29 && t != 0) { if constexpr (t == 1) TOUCH(wb[b]); else TOUCH(m[(b + 1) % 10]); }
                if constexpr (i == 29) V.pre<true, 17>();
                if constexpr (i == 2) { PINS(now); an += (unsigned)now; PINS(an); }
                if constexpr (i == 3) an = spack(an, c);
                SCHED0();
                if constexpr (i < 27) rd(IC(3 + i));
                if constexpr (i == 5) st32(k.rs, vr, pbc, d0);
                SCHED0();
                if constexpr (i == 4) { d0 = an; PIN(d0); }
                if constexpr (i == 3) mkr();
                if constexpr (i == 28) d0 = vadd(l16, L_K + ((3 * w) >> 1) * 1024 + ((3 * w) & 1) * 8);
                if constexpr (i == 29) d1 = vadd(l16, L_K + ((3 * w + 1) >> 1) * 1024 + ((3 * w + 1) & 1) * 8);
                SCHED0();
            });
            unsigned fo[3];
            V.run<0, true, 17, 19, 0, true>(W, [&](auto I) { constexpr int i = CV(I);
                if constexpr (i < 3) { rd(IC(30 + i)); SCHED0(); fold_op<0>(ac[i], fo[i]); }
                if constexpr (i == 3 || i == 4 || i == 6) { constexpr int t = i / 2 - 1; asm volatile("v_add_f32 %1, %1, %2\n\tv_cvt_pk_bf16_f32 %0, %3, %1" : "=v"(fo[t]), "+v"(ac[t][1]) : "v"(ac[t][3]), "v"(ac[t][0])); }
                if constexpr (i == 5 || i == 7 || i == 8) lw32(dq0 + ((i - 4) / 2) * 256, fo[(i - 4) / 2]);
                if constexpr (i == 9 || i == 10) clr2(acc[i - 9]);
                if constexpr (i == 11) BARL(0) });
        }
        HSTAMP(s, 1) AST(3)
        {
            unsigned fo[2];
            auto xq = [&](auto I) { constexpr int i = CV(I) - XP + XS0;
                if constexpr (i == -4) X.next<3>(k, l16);
                if constexpr (i == -1 || i == 0) X.next<14 + i>(k, l16); };
            static_assert(XP % 6 == 5 && XP <= 53 && XP >= 17 && XB > XP + 18 && XB > 63, "XP");
            auto fin2 = [&](auto P, auto J) { fold2<CV(J)>(acc[2 * CV(P)], acc[2 * CV(P) + 1], fo); };
            V.run<0, true, 19, 20, 0, true>(W, [&](auto I) { constexpr int i = CV(I);
                if constexpr (i == 0) { al[0] = lrtr(trp); ah[0] = lrtr(trp + 64); }
                if constexpr (i == 1) al[1] = lrtr(trp + 1024);
                if constexpr (i == 2) ah[1] = lrtr(trp + 1088);
                if constexpr (i == 3) al[2] = lrtr(trp + 2048);
                if constexpr (i == 4) ah[2] = lrtr(trp + 2112);
                SCHED0();
                if constexpr (i >= 1) clr(IC(3 + i)); });
            sfor<30>([&](auto I) { constexpr int i = CV(I), b = (i % 6) >> 1, t = 2 * (i / 6) + (i & 1), id = OIDX(t, b);
                smfw<id>(acc[t], CAT4(al[b], ah[b]), W[id], ix);
                SCHED0();
                if constexpr (i == 1 || i == 3) TOUCH(ah[(i + 1) / 2]);
                if constexpr (i == 29) TOUCH(v[0]);
                if constexpr (i == 21) BARL(0)
                SCHED0();
                if constexpr (i == 4 || i == 7) X.next<(i == 4 ? 5 : 11)>(k, l16);
                xq(IC(i - 30));
                if constexpr (i >= 21 && i < 27) v[i - 21] = lr128(l16 + L_K + (i - 21) * 1024);
                SCHED0();
                V.out<i - 1, 0>(d0, d1);
                SCHED0();
                if constexpr (i % 3 == 0 && i < 18) clr(IC(9 + i / 3));
                if constexpr (i == 16) clr(IC(15));
                if constexpr (i >= 18 && i < 21) clr(IC(i - 2));
                if constexpr (i == 22) clr(IC(19));
                if constexpr (i >= 23 && i < 29) fold_op<(i - 23) / 2>(acc[(i - 23) & 1], fo[(i - 23) & 1]);
                if constexpr (i == 29) { st64(k.rs, vo, ym, (u32x2){fo[0], fo[1]}); SCHED0(); fin2(IC(1), IC(0)); }
                SCHED0(); });
            HSTAMP(s, 2)
            sfor<107>([&](auto I) { constexpr int i = CV(I), q = i < 8 ? i + 4 : i + 3;
                ymm(I);
                if constexpr (i < 5 || (i >= 29 && i < 35)) TOUCH(v[(i < 5 ? i + 1 : i - 23) % 12]);
                if constexpr (i < 13 && i != 8) {
                    if constexpr (q % 3 == 1) { ym += 32768; PINS(ym); }
                    if constexpr (q % 3 == 2 && i >= 5) __builtin_amdgcn_s_setprio(0);
                    SCHED0();
                    if constexpr (q % 3 == 0) { st64(k.rs, vo, ym, (u32x2){fo[0], fo[1]}); SCHED0(); }
                    if constexpr (q < 15) fin2(IC(q / 3), IC(q % 3));
                }
                if constexpr (i == 13) done = rt_async();
                if constexpr (i >= 15 && i < 23 && i != 18 && i != 20) v[6 + i - 15 - (i > 18) - (i > 20)] = lr128(l16 + L_K + (6 + i - 15 - (i > 18) - (i > 20)) * 1024);
                xq(IC(i));
                if constexpr (i == XP - 6) X.next<15>(k, l16);
                if constexpr (i == XP - 1) { SCHED0(); X.next<7>(k, l16); }
                if constexpr (i == XP) X.next<12>(k, l16);
                if constexpr (i > XP && i <= XP + 18 && (i % 6 == 0 || i % 3 == 1)) X.op<(i - XP - 1) / 6, (i % 6 + 2) / 3>(k);
                { constexpr int xp[6] = {XB, XB + 1, XB + 6, XB + 11, XB + 12, XB + 18};
                  sfor<6>([&](auto Q) { if constexpr (xp[CV(Q)] == i) X.op<3 + CV(Q) / 3, CV(Q) % 3>(k); }); }
                if constexpr (i == 90) { ym = (c & 2) ? ym2 : ym0; PINS(ym); }
                if constexpr (i == 91) { ys = ym + EXP(327680); PINS(ys); }
                if constexpr (i == 96) { ym = (c & 1) ? ys : ym; PINS(ym); }
                if constexpr (i == 60) { HSTAMP(s, 3) }
                if constexpr (i == 42 || i == 43 || i == 48 || i == 49) { AAC((i - 42) / 3 + (i & 1)) }
                if constexpr (i == 102) lgkm0(done);
                if constexpr (i == 103) { PINS(now); HTRACE(c - 1) an = (unsigned)done - (unsigned)now; PINS(an); }
                if constexpr (i == 105) { G.mx = vshl1(c, 1); mkr(); }
                SCHED0(); });
            AST(4)
        }
    } while (X.sg != 65u);
    HFLUSH(63)
    PROF_OUT
}

DEVI void row_role(const Params& p, const Ctx& k, char* smem, int rt) {
    PROF_DECL
    const int l = k.l, n = l & 15;
    const unsigned fo = k.w * 2048 + rt * 1024;
    RSTAMP(2)
    const __amdgpu_buffer_rsrc_t rs = __builtin_amdgcn_make_buffer_rsrc(k.c ? (void*)k.ws : (void*)p.x, 0, 0x7fffffff, 0x00020000);
    const unsigned vs = k.c ? l * 16 : n * 1280 + (l >> 4) * 16, cs = k.c ? 16384 : 256;
    unsigned ep = n * 32, dp = (l >> 4) == (n >> 2) ? n * 32 + k.w * 8 : 1024 + l * 8;
    const unsigned long long m1 = __ballot(n & 1), m2 = __ballot(n & 2);
    bf16x8 one8 = {0x3F80, 0x3F80, 0x3F80, 0x3F80, 0x3F80, 0x3F80, 0x3F80, 0x3F80};
    PIN(one8);
    unsigned fails = k.c ? 0u : 65537u;
    prog_init(k);
    for (int sx = k.c ? -1 : 8; sx < 64; ++sx) {
        const int sg = max(sx, 0);
        unsigned src[5], dst = k.seg(PG_XNA, sg) + fo;
#pragma unroll
        for (int c = 0; c < 5; ++c) { src[c] = (k.c ? k.seg(PG_X2, sg) + fo : (sg * 32 + rt * 16) * 1280 + k.w * 64) + c * cs; PINS(src[c]); }
        PINS(dst);
        prog_wait<5>(k, k.p0 + CNT(C_X2, rt * 5), k.c ? sx : -1);
        STOP(sg) STAMP(sg, 0)
        u32x4 a[5];
        f32x4 sm, sq;
        f32x2 v[20];
        unsigned long long bad;
        do {
            asm volatile("" ::: "memory");
#pragma unroll
            for (int c = 0; c < 5; ++c) { a[c] = ld128(rs, vs, src[c]); SCHED0(); }
            unsigned mx = 0, lim = ~1u | ((fails - 1) >> 16);
            ++fails; PINS(fails); PINS(lim);
            sfor<5>([&](auto C) { constexpr int c = CV(C);
                mfa<false, c == 0>(sq, __builtin_bit_cast(bf16x8, a[c]), __builtin_bit_cast(bf16x8, a[c])); mfa<false, c == 0>(sm, one8, __builtin_bit_cast(bf16x8, a[c]));
                mx = umax3(umax3(mx, a[c][0], a[c][1]), a[c][2], a[c][3]); PIN(mx);
                SCHED0(); });
            bad = __ballot(mx > lim);
        } while (__builtin_expect(bad != 0, 0));
        STAMP(sg, 1)
        auto unp = [&](int i0, int i1) {
#pragma unroll
            for (int i = i0; i < i1; ++i) { v[i] = (f32x2){blo(a[i >> 2][i & 3]), bhi(a[i >> 2][i & 3])}; PIN(v[i]); }
            SCHED0(); };
        unp(0, 3);
        asm volatile("" : "+v"(sq), "+v"(sm));
        lw64(dp, (u32x2){__float_as_uint(sm[0]), __float_as_uint(csel(csel(sq[0], sq[1], m1), csel(sq[2], sq[3], m1), m2))});
        SCHED0();
        unp(3, 10);
        RAWBAR()
        const f32x4 e0 = __builtin_bit_cast(f32x4, lr128(ep)), e1 = __builtin_bit_cast(f32x4, lr128(ep + 16));
        SCHED0();
        unp(10, 20);
        const f32x4 e = e0 + e1;
        float m = (e[0] + e[2]) * (1.f / 640.f);
        const float var = bfr(__builtin_fmaxf(__builtin_fmaf(-m, m, (e[1] + e[3]) * (1.f / 640.f)), 0.f));
        m = bfr(m);
        const float rden = __builtin_amdgcn_rcpf(bfr(__builtin_amdgcn_sqrtf(bfr(var + 1e-5f))));
#pragma unroll
        for (int c = 0; c < 5; ++c) {
            u32x4 o;
#pragma unroll
            for (int i = 0; i < 4; ++i) { const f32x2 x = v[c * 4 + i] - m; o[i] = cvtpk(bfr(x[0]) * rden, bfr(x[1]) * rden); }
            st128(k.rs, l * 16, dst + c * 16384, o);
        }
        STAMP(sg, 2)
        unsigned fv = sx - (k.c ? 0 : 8);
        PIN(fv);
        DRAIN();
        st32(k.rs, l == 0 ? 0u : OOB, k.p0 + CNT(C_XN, rt * 4 + k.w), fv);
        dp ^= 2048; ep ^= 2048;
        STAMP(sg, 3) STAMP(sg, 4) STAMP(sg, 5)
        PFLUSH(sg)
    }
    PROF_OUT
}

DEVI int red_col(int j) { const int w = j / 5, q = j % 5; return q < 3 ? 3 * w + q : 9 + 2 * w + q; }
struct Red {
    u32x4 Y[16];
    template <int I0, int I1> DEVI void yload(const Ctx& k, int j, int sg) {
        const unsigned a = k.yx(sg, j / 5) + (j % 5) * 131072 + k.w * 16384;
#pragma unroll
        for (int i = I0; i < I1; ++i) Y[i] = ld128(k.rs, k.l * 16 + (i & 3) * 1024, a + ((i >> 2) & 1) * 4096 + (i >> 3) * 65536);
    }
};
#define BCV (k.l < 8 ? 0 : k.l < 16 ? 16384 : 16388) + (k.l & 7) * 1024, k.p0 + CNT(C_BC + k.w * 2, 0) + ((((k.role >> 5) * 256) | ((k.role & 31) > 20 ? 128 : 0)) & RMSK)
template <int SLP> DEVI unsigned bc_watch(const Ctx& k, bool y, int s) {
    if (OLDP & (y ? 2 : 1)) {
        unsigned v;
_Pragma("unroll 1")
        for (unsigned spins = 0;; ++spins) {
            v = ld32(k.rs, (y ? 16388 : (k.l & 8) * 2048) + (k.l & 7) * 1024, k.p0 + CNT(C_BC + k.w * 2, 0) + ((((k.role >> 5) * 256) | ((k.role & 31) > 20 ? 128 : 0)) & RMSK));
            if (__all((int)v >= s)) break;
            asm volatile("" ::: "memory");
            __builtin_amdgcn_s_sleep(1);
            if (spin_fail(k, spins)) break;
        }
        asm volatile("" ::: "memory");
        return v;
    }
    return watch<SLP>(k, BCV, L_PW + k.w * 512, L_PW + k.w * 512 + (y ? 64 : 0) + (k.l & 15) * 4, s, 0);
}
DEVI void y_wait(const Ctx& k, int s) { bc_watch<YSLP>(k, true, s); }
DEVI void wait_until(unsigned t) { while ((int)((rclk() - t) << 16) < 0) { if (WUS) __builtin_amdgcn_s_sleep(WUS); } }
#define DPPMAX(x, c) x = max(x, __builtin_amdgcn_update_dpp(0, x, c, 0xf, 0xf, false))
#define TSLP 4
#define TWU 0
DEVI void wait_until2(unsigned t) { while ((int)((rclk() - t) << 16) < 0) { if (TWU) __builtin_amdgcn_s_sleep(TWU); } }
#define WAIT_BC(s, BSLP) { \
    const unsigned bc = bc_watch<BSLP>(k, false, (s + 1) << 16); \
    const unsigned now = rclk(); \
    int d = (int)((bc - now) << 16); \
    d = ((int)bc >> 16) > s + 1 || d > (256 << 16) ? -(64 << 16) : d; \
    DPPMAX(d, 0xB1); DPPMAX(d, 0x4E); DPPMAX(d, 0x141); \
    tq = now + (__builtin_amdgcn_readlane(d, 0) >> 16); tv = now + (__builtin_amdgcn_readlane(d, 8) >> 16); }
DEVI void mred_role(const Params& p, const Ctx& k, char* smem, int j) {
    PROF_DECL
    const int l = k.l, w = k.w, g = l >> 4, n = l & 15;
    const unsigned ix = SPIX(l);
    const unsigned vp = 4096 + (g * 16 + ((n >> 1) & 1) * 8 + (n >> 2) * 2 + (n & 1)) * 16;
    const unsigned v16 = (g >> 1) * 1024 + (g & 1) * 256 + n * 16, yo = j * 32768 + EXP(w * 4096);
    const u32x4 ones = {0xFFFFFFFFu, 0xFFFFFFFFu, 0xFFFFFFFFu, 0xFFFFFFFFu};
    auto arm = [&](int s) {
#pragma unroll
        for (int i = 0; i < 4; ++i) st128(k.rs, l * 16 + i * 1024, k.ym(s) + yo, ones);
    };
    const unsigned mv = w < 2 ? l * 4 : OOB;
    unsigned fails = 0, wb = w * 512 + g * 128 + n * 4, rb = (w & 1) * 2048 + (g * 2 + ((l >> 2) & 1)) * 64 + ((l & 3) * 4 + ((l >> 3) & 1) * 2) * 4;
    const unsigned pv = l == 0 && j % 5 == 0 ? (j / 5) * 128 + w * 32 : OOB;
    Red R;
    RSTAMP(2)
    arm(1);
#pragma unroll
    for (int i = 0; i < 8; ++i) st128(k.rs, (i || w > 1 || g > 1) ? g * 32768 + n * 16 : OOB, k.memf(i * 8 + (w >> 1) * 4) + EXP((2 * j + (w & 1)) * 256), ones);
#pragma unroll
    for (int i = 0; i < 4; ++i) { const unsigned q = min(i * 80 + j * 4 + w, 287); st128(k.rs, l * 16, k.p0 + PR_B + PR_SZ + EXP(q * 1024), ones); }
    prog_init(k);
    sleep_until(k.t0 + RSL);
    y_wait(k, 2);
    R.yload<0, 16>(k, j, 0);
    for (int s = 0; s < 63; ++s) {
        STOP(s) HSTAMP(s, 0)
        unsigned ap = k.p0 + PR_B + (s & 3) * PR_SZ + w * 49152, am = k.ym(s) + yo, ma = k.memf(s + 1) + EXP((2 * red_col(j) + (w & 1)) * 256);
        unsigned ph = (s & 4) ? 0x80008000u : 0u;
        PINS(ap); PINS(am); PINS(ma); PINS(ph);
        unsigned tq, tv;
        WAIT_BC(s, BSLP)
        st32(k.rs, pv, k.p0 + CNT(C_XB, 0), spack(((int)((tq - tv) << 16) < 0 ? tv : tq) + RDY, s + 2));
        STAMP(s, 5)
        unsigned long long bad;
        do {
            u32x4 ym[4], pm[5];
            f32x4 acc[2];
            unsigned lim = 0x7FFFFFFFu | (fails << 15);
            ++fails; PINS(fails); PINS(lim);
            clr2(acc[0]); clr2(acc[1]);
            wait_until(tv);
            asm volatile("" ::: "memory");
#pragma unroll
            for (int i = 0; i < 4; ++i) ym[i] = ld128(k.rs, v16 + (i >> 1) * 2048 + (i & 1) * 512, am);
            asm volatile("" ::: "memory");
            wait_until(tq);
            STAMP(s, 1)
            asm volatile("" ::: "memory");
#pragma unroll
            for (int i = 0; i < 5; ++i) { pm[i] = ld128(k.rs, i < 4 ? l * 16 + i * 1024 : vp, ap); SCHED0(); }
            const u32x8 y0 = {ym[0][0], ym[1][0], ym[0][2], ym[1][2], ym[2][0], ym[3][0], ym[2][2], ym[3][2]};
            const u32x8 y1 = {ym[0][1], ym[1][1], ym[0][3], ym[1][3], ym[2][1], ym[3][1], ym[2][3], ym[3][3]};
            unsigned mx = 0;
#pragma unroll
            for (int i = 0; i < 4; ++i) mx = max(mx, umax4(ym[i]));
            unsigned tg = mx == 0xFFFFFFFFu ? 0x80000000u : 0u;
            asm volatile("s_nop 1" : "+v"(acc[0]), "+v"(acc[1]), "+v"(tg) : "v"(y0), "v"(y1), "v"(ix));
            SCHED0();
#pragma unroll
            for (int i = 0; i < 5; ++i) {
                if (i < 4) smf<false>(acc[0], __builtin_bit_cast(bf16x8, pm[i]), __builtin_bit_cast(bf16x16, __builtin_shufflevector(R.Y[2 * i], R.Y[2 * i + 1], 0, 1, 2, 3, 4, 5, 6, 7)), ix);
                else smf<false>(acc[0], __builtin_bit_cast(bf16x8, pm[4]), __builtin_bit_cast(bf16x16, y0), ix);
                if (i < 4) smf<false>(acc[1], __builtin_bit_cast(bf16x8, pm[i]), __builtin_bit_cast(bf16x16, __builtin_shufflevector(R.Y[8 + 2 * i], R.Y[9 + 2 * i], 0, 1, 2, 3, 4, 5, 6, 7)), ix);
                else smf<false>(acc[1], __builtin_bit_cast(bf16x8, pm[4]), __builtin_bit_cast(bf16x16, y1), ix);
                SCHED0();
            }
#pragma unroll
            for (int i = 0; i < 2; ++i) tg |= __builtin_amdgcn_bitop3_b32(pm[i][0], pm[i][1], ph, 0x7e) | __builtin_amdgcn_bitop3_b32(pm[i][2], pm[i][3], ph, 0x7e);
            asm volatile("s_nop 1" : "+v"(acc[0]), "+v"(acc[1]), "+v"(tg));
#pragma unroll
            for (int t = 0; t < 2; ++t) { lw32(wb + t * 2048, __float_as_uint(acc[t][0] + acc[t][2])); lw32(wb + t * 2048 + 64, __float_as_uint(acc[t][1] + acc[t][3])); }
            SCHED0();
#pragma unroll
            for (int i = 2; i < 5; ++i) tg |= __builtin_amdgcn_bitop3_b32(pm[i][0], pm[i][1], ph, 0x7e) | __builtin_amdgcn_bitop3_b32(pm[i][2], pm[i][3], ph, 0x7e);
            bad = __ballot(tg > lim);
        } while (__builtin_expect(bad != 0, 0));
        STAMP(s, 2)
        RAWBAR()
        {
            const LAS f32x2* rp = (const LAS f32x2*)(size_t)rb;
            const f32x2 r = (rp[0] + rp[64]) + (rp[128] + rp[192]);
            st32(k.rs, mv, ma, min(cvtpk(r[0], r[1]) ^ ph, 0xFFFFFFFEu));
        }
        SCHED0();
        STAMP(s, 3)
        wb ^= 4096; rb ^= 4096;
        ask(k, z0(), (k.p0 ^ 8192u) + CNT(C_X1, j), L_PW + w * 512 + 256);
        y_wait(k, 2 * s + 3);
        R.yload<0, 8>(k, j, s + 1);
        if (ARMP == 0) {
        watch<PSLEEP>(k, z0(), (k.p0 ^ 8192u) + CNT(C_X1, j), L_PW + w * 512 + 256, L_PW + w * 512 + 256 + z0(), s - 1, 0);
        arm(s + 2);
        }
        y_wait(k, 2 * s + 4);
        STAMP(s, 4)
        R.yload<8, 16>(k, j, s + 1);
        if (ARMP == 1) {
        watch<PSLEEP>(k, z0(), (k.p0 ^ 8192u) + CNT(C_X1, j), L_PW + w * 512 + 256, L_PW + w * 512 + 256 + z0(), s - 1, 0);
        arm(s + 2);
        }
        PFLUSH(s)
    }
    PFLUSH(63)
    PROF_OUT
}
DEVI void tred_role(const Params& p, const Ctx& k, char* smem, int j) {
    PROF_DECL
    const int l = k.l, w = k.w, g = l >> 4, n = l & 15, T = 2 * red_col(j) + (w >> 1), rt = w & 1;
    const unsigned v16 = (g >> 1) * 1024 + (g & 1) * 256 + n * 16, yo = j * 32768 + EXP(w * 4096);
    const unsigned xo = EXP((T >> 1) * 2048) + (T & 1) * 512 + rt * 1024, xv = ((g >> 1) * 16 + n) * 16 + (g & 1) * 8;
    const u32x2 ones = {0xFFFFFFFFu, 0xFFFFFFFFu};
    const unsigned fl = (w == 0 && l == 0) ? 0u : OOB;
    unsigned fails = 0;
    Red R;
    RSTAMP(2)
    st64(k.rs, xv, k.segx(PG_X1, 0) + xo, ones); st64(k.rs, xv, k.segx(PG_X1, 1) + xo, ones);
    prog_init(k);
    sleep_until(k.t0 + RSL);
    y_wait(k, 2);
    R.yload<0, 16>(k, j, 0);
    for (int s = 0; s < 64; ++s) {
        STOP(s) STAMP(s, 0)
        unsigned ap[4], am = k.ym(s) + yo, xa = k.segx(PG_X1, s) + xo, rb = (s & 1) * 16384 + l * 16, wb = rb + w * 1024, fv = s;
#pragma unroll
        for (int i = 0; i < 4; ++i) { ap[i] = k.p0 + PR_B + (s & 3) * PR_SZ + w * 49152 + (i < 2 ? 32768 : 16384) + (~i & 1) * 4096; PINS(ap[i]); }
        rb += w * 4096;
        PINS(am); PINS(xa); PIN(rb); PIN(wb); PIN(fv);
        const unsigned ph = (s & 4) ? 0x80008000u : 0u;
        unsigned tq, tv;
        u32x2 res;
        if (k.c) res = ld64(k.rs, xv, k.seg(PG_X2, s) + xo);
        else res = *(const u32x2*)(p.x + (size_t)(s * 32 + rt * 16 + n) * 640 + T * 16 + g * 4);
        f32x4 acc[2][2];
        WAIT_BC(s, TSLP)
        u32x4 yl[8];
#pragma unroll
        for (int i = 0; i < 8; ++i) { yl[i] = __builtin_shufflevector(R.Y[2 * i], R.Y[2 * i + 1], 0, 1, 4, 5); PIN(yl[i]); }
        f32x4 x = {blo(res[0] ^ ph), bhi(res[0] ^ ph), blo(res[1] ^ ph), bhi(res[1] ^ ph)};
        PIN(x);
        wait_until2(tv); wait_until2(tq + (s < 63 ? RDLY : 0));
        STAMP(s, 1)
        unsigned long long bad;
        do {
            asm volatile("" ::: "memory");
            u32x4 ym[4], pp[16];
#pragma unroll
            for (int i = 0; i < 4; ++i) { ym[i] = ld128(k.rs, v16 + (i >> 1) * 2048 + (i & 1) * 512, am); SCHED0(); }
#pragma unroll
            for (int i = 0; i < 16; ++i) { pp[i ^ 4] = ld128(k.rs, l * 16 + (i & 3) * 1024, ap[i >> 2]); SCHED0(); }
            unsigned mx = 0, tg, lim = 0x7FFFFFFFu | (fails << 15);
            ++fails; PINS(fails); PINS(lim);
#pragma unroll
            for (int i = 0; i < 4; ++i) mx = max(mx, umax4(ym[i]));
            tg = mx == 0xFFFFFFFFu ? 0x80000000u : 0u;
            u32x4 yf[4];
#pragma unroll
            for (int i = 0; i < 4; ++i) { const int t = i & 1, f = i >> 1; yf[i] = (u32x4){ym[2 * f][t], ym[2 * f][t + 2], ym[2 * f + 1][t], ym[2 * f + 1][t + 2]}; }
            asm volatile("s_nop 1" : "+v"(yf[0]), "+v"(yf[1]), "+v"(yf[2]), "+v"(yf[3]), "+v"(tg));
            SCHED0();
            sfor<32>([&](auto I) { constexpr int i = CV(I), t = i & 1, q = (i >> 1) ^ 4, r = q < 4 ? 0 : q < 8 ? (q >> 1) & 1 : 1;
                mfa<false, q == 4 || q == 6>(acc[t][r], __builtin_bit_cast(bf16x8, q < 4 ? yl[t * 4 + q] : q < 8 ? yf[(q & 1) * 2 + t] : R.Y[t * 8 + (q & 7)]), __builtin_bit_cast(bf16x8, pp[q]));
                SCHED0();
                tg |= __builtin_amdgcn_bitop3_b32(pp[q][2 * t], pp[q][2 * t + 1], ph, 0x7e); PIN(tg);
                if constexpr (i == 20 || i == 22) { asm volatile("" : "+v"(acc[(i - 20) / 2][0])); lw128(wb + (i - 20) * 4096, __builtin_bit_cast(u32x4, acc[(i - 20) / 2][0])); }
                if constexpr (i == TPOS) st32(k.rs, fl, (k.p0 ^ 8192u) + CNT(C_X1, j), fv);
                SCHED0(); });
            bad = __ballot(tg > lim);
        } while (__builtin_expect(bad != 0, 0));
        STAMP(s, 2)
        asm volatile("s_nop 3" : "+v"(acc[0][1]), "+v"(acc[1][1]));
        lw128(wb + 4096, __builtin_bit_cast(u32x4, acc[0][1])); lw128(wb + 12288, __builtin_bit_cast(u32x4, acc[1][1]));
        SCHED0();
        ask(k, BCV, L_PW + w * 512);
        RAWBAR()
        {
            const f32x4 r0 = __builtin_bit_cast(f32x4, lr128(rb)), r1 = __builtin_bit_cast(f32x4, lr128(rb + 1024)), r2 = __builtin_bit_cast(f32x4, lr128(rb + 2048)), r3 = __builtin_bit_cast(f32x4, lr128(rb + 3072));
            SCHED0();
            if constexpr (TPOS == 32) st32(k.rs, fl, (k.p0 ^ 8192u) + CNT(C_X1, j), fv);
            SCHED0();
            const f32x4 r = (r0 + r1) + (r2 + r3);
            st64(k.rs, xv, xa, (u32x2){min(cvtpk(x[0] + bfr(r[0]), x[1] + bfr(r[1])) ^ ph, 0xFFFFFFFEu), min(cvtpk(x[2] + bfr(r[2]), x[3] + bfr(r[3])) ^ ph, 0xFFFFFFFEu)});
            SCHED0();
            if constexpr (TPOS == 33) st32(k.rs, fl, (k.p0 ^ 8192u) + CNT(C_X1, j), fv);
        }
        st64(k.rs, s < 62 ? xv : OOB, k.segx(PG_X1, (s + 2) & 63) + xo, ones);
        STAMP(s, 3)
        if (s < 63) {
            watch<YSLP>(k, BCV, L_PW + k.w * 512, L_PW + k.w * 512 + 64 + (k.l & 15) * 4, 2 * s + 4, 0);
            R.yload<0, 16>(k, j, s + 1);
        }
        STAMP(s, 4)
        STAMP(s, 5)
        PFLUSH(s)
    }
    PROF_OUT
}

#undef TZ
#define TZ z0()
DEVI void up_role(const Params& p, const Ctx& k, char* smem, int id) {
    PROF_DECL
    const int l = k.l, w = k.w, g = l >> 4, n = l & 15;
    bf16x8 Wt[80];
    const int tile0 = id * 8 + w * 2;
    auto issue = [&](auto U) { constexpr int u = CV(U);
        sfor<8>([&](auto J) { constexpr int j = CV(J); dma1<(u % 3) * 32768 + j * 1024>(((w & 1) ? p.wu : p.wg) + (size_t)(2 * u + (w >> 1)) * 32 * 1536 + id * 128, wsrc<256, true>(j * 64 + l, 3072), w * 8192); }); };
    sfor<3>(issue);
    cstream<10>([](auto V) { return IC(CV(V) < 9 ? 8 : 0); },
        [&](auto U) { constexpr int u = CV(U);
            if constexpr (u > 0 && u < 8) issue(IC(u + 2));
#pragma unroll
            for (int i = 0; i < 8; ++i) Wt[(i & 3) * 20 + 2 * u + (i >> 2)] = wp_read<256, true>(smem + (u % 3) * 32768 + (i >> 1) * 8192, w * 2 + (i & 1), l); });
    RAWBAR()
    RSTAMP(2)
    bf16x8 one8 = {0x3F80, 0x3F80, 0x3F80, 0x3F80, 0x3F80, 0x3F80, 0x3F80, 0x3F80};
    asm volatile("s_nop 1" : "+v"(one8));
    f32x4 cs[4];
    sfor<80>([&](auto I) { constexpr int f = CV(I); mfa<(f < 64), f % 20 == 0>(cs[f / 20], Wt[f], one8); });
    asm volatile("s_nop 7" : "+v"(cs[0]), "+v"(cs[1]), "+v"(cs[2]), "+v"(cs[3]));
    const unsigned ho = EXP((tile0 >> 1) * 1024), l16 = l * 16;
    constexpr int L_ST = 81920;
    const u32x4 ones = {0xFFFFFFFFu, 0xFFFFFFFFu, 0xFFFFFFFFu, 0xFFFFFFFFu};
#pragma unroll
    for (int i = 0; i < 4; ++i) st128(k.rs, l16, k.hseg(i >> 1) + (i & 1) * EXP(49152) + ho, ones);
    unsigned fails = 0;
    lw32(L_ST + 1024, 4096u);
    prog_init(k);
    const unsigned fl = (w == 0 && l == 0) ? 0u : OOB;
    const unsigned long long m1 = __ballot(n & 1), m2 = __ballot(n & 2);
    for (int s = 0; s < 64; ) {
        unsigned xs = k.seg(PG_X1, s) + w * 2048, xd = (s & 1) * 40960 + w * 2048;
        PINS(xs); PINS(xd);
        prog_wait<20>(k, k.p0 + CNT(C_X1, 0), fails ? s : -1);
        STOP(s) STAMP(s, 0)
        xn_all(k, l16, xs, xd);
        const unsigned xb = (s & 1) * 40960 + l16, yb = xb + w * 2048, sp = L_ST + (s & 1) * 2048, rp = sp + n * 16;
        const unsigned dp = sp + (g == (n >> 2) ? n * 16 + w * 4 : 4096 + l * 4);
        f32x4 acc[2][4], st[4];
        bf16x8 x[20], x1[20], y0[5], y1[5];
        unsigned mx = 0;
        auto blk = [&](auto B, auto J) { constexpr int b = CV(B), j = CV(J);
            if constexpr (j == 0) asm volatile("s_waitcnt vmcnt(%0)\n\ts_barrier" :: "n"(8 - 2 * b) : "memory");
            if constexpr (j >= 1 && j < 5) x[4 * b + j - 1] = lr128(xb + (4 * b + j - 1) * 2048);
            if constexpr (j == 5) y0[b] = lr128(yb + b * 8192);
            if constexpr (j == 6) y1[b] = lr128(yb + b * 8192 + 1024); };
        sfor<7>([&](auto J) { blk(IC(0), J); });
        SCHED0();
        STAMP(s, 1)
        float mean[2], rden[2], e[4][4];
        f32x4 sa[2], sb[2];
        u32x4 bad, o[2];
        unsigned sv, fv, r, f0, f1, ha = k.hseg(s) + ho;
        PINS(ha);
        auto stat = [&](auto I) { constexpr int i = CV(I), rt = i / 8, j = i % 8;
            if constexpr (j == 0) asm volatile("v_add_f32 %0, %2, %3\n\tv_add_f32 %1, %4, %5" : "=&v"(mean[rt]), "=&v"(rden[rt]) : "v"(sa[rt][0]), "v"(sa[rt][1]), "v"(sb[rt][0]), "v"(sb[rt][1]));
            if constexpr (j == 1 || j == 2) asm volatile("v_add_f32 %0, %0, %2\n\tv_add_f32 %1, %1, %3" : "+v"(mean[rt]), "+v"(rden[rt]) : "v"(sa[rt][j + 1]), "v"(sb[rt][j + 1]));
            if constexpr (j == 3) asm volatile("v_mul_f32 %0, 0xbaccccCD, %0\n\tv_mul_f32 %1, 0x3acccccd, %1" : "+v"(mean[rt]), "+v"(rden[rt]));
            if constexpr (j == 4) asm volatile("v_fma_f32 %1, -%0, %0, %1\n\tv_cvt_pk_bf16_f32 %0, 0, %0\n\tv_max_f32 %1, 0, %1" : "+v"(mean[rt]), "+v"(rden[rt]));
            if constexpr (j == 5) asm volatile("v_cvt_pk_bf16_f32 %0, 0, %0\n\tv_add_f32 %0, 0x3727c5ac, %0\n\tv_cvt_pk_bf16_f32 %0, 0, %0" : "+v"(rden[rt]));
            if constexpr (j == 6) asm volatile("v_sqrt_f32 %0, %0" : "+v"(rden[rt]));
            if constexpr (j == 7) asm volatile("v_cvt_pk_bf16_f32 %0, 0, %0\n\tv_rcp_f32 %0, %0" : "+v"(rden[rt])); };
        auto ep = [&](auto RT, auto T, auto J) { constexpr int rt = CV(RT), t = CV(T), j = CV(J), c = (j & 1) * 2, u = j >= 20, q = u ? j - 20 : j;
            float (&a)[4] = e[t + 2 * u];
#define A2 "+v"(a[c]), "+v"(a[c + 1])
            if constexpr (j < 0 || j >= 30) { }
            else if constexpr (q < 2) asm volatile("v_fma_f32 %0, %2, %3, %5\n\tv_fma_f32 %1, %2, %4, %6" : "=&v"(a[c]), "=&v"(a[c + 1]) : "v"(mean[rt]), "v"(cs[t + 2 * u][c]), "v"(cs[t + 2 * u][c + 1]), "v"(acc[rt][t + 2 * u][c]), "v"(acc[rt][t + 2 * u][c + 1]));
            else if constexpr (q < 4) asm volatile("v_mul_f32 %0, %0, %2\n\tv_mul_f32 %1, %1, %2" : A2 : "v"(rden[rt]));
            else if constexpr (q < 6 || j == 18 || j == 19) asm volatile("v_cvt_pk_bf16_f32 %0, 0, %0\n\tv_cvt_pk_bf16_f32 %1, 0, %1" : A2);
            else if constexpr (j < 8) asm volatile("v_mul_f32 %0, 0xbfb8aa3b, %0\n\tv_mul_f32 %1, 0xbfb8aa3b, %1" : A2);
            else if constexpr (j < 12) asm volatile("v_exp_f32 %0, %0" : "+v"(a[j & 3]));
            else if constexpr (j < 14) asm volatile("v_add_f32 %0, 1.0, %0\n\tv_add_f32 %1, 1.0, %1" : A2);
            else if constexpr (j < 18) asm volatile("v_rcp_f32 %0, %0" : "+v"(a[(j - 14) & 3]));
            else if constexpr (q < 8) asm volatile("v_mul_f32 %0, %0, %2\n\tv_mul_f32 %1, %1, %3" : A2 : "v"(e[t][c]), "v"(e[t][c + 1]));
            else if constexpr (q == 8) asm volatile("v_cvt_pk_bf16_f32 %0, %0, %2\n\tv_cvt_pk_bf16_f32 %1, %1, %3" : "+v"(a[0]), "+v"(a[2]) : "v"(a[1]), "v"(a[3]));
            else { unsigned d0, d1; asm volatile("v_min_u32 %0, -2, %2\n\tv_min_u32 %1, -2, %3" : "=&v"(d0), "=&v"(d1) : "v"(a[0]), "v"(a[2])); o[rt][2 * t] = d0; o[rt][2 * t + 1] = d1; } };
        sfor<180>([&](auto I) {
            constexpr int a = CV(I), b = a / 8, j = (a % 8) ^ (b == 4 ? 4 : 0), ch = a / 20 - 1, ks = a < 40 ? 4 * b + (j & 3) : a % 20, rt = ch >= 4;
            constexpr int nt = a < 40 ? 0 : rt ? ch - 4 : (ch & 1) * 2 + (ch >> 1), f = nt * 20 + ks, i = a - 36;
            if constexpr (a >= 40 || j < 4) mfa<(f < 64), ks == 0>(acc[rt][nt], Wt[f], rt ? x1[ks] : x[ks]);
            else mfa<false, b == 0>(st[j & 3], (j & 1) ? (j & 2 ? y1[b] : y0[b]) : one8, j & 2 ? y1[b] : y0[b]);
            SCHED0();
            if constexpr (a >= 103 && a < 119) TOUCH(x1[(a - 99) % 20]);
            else if constexpr (a == 60) { r = __builtin_amdgcn_readfirstlane(bad[0]); PINS(r); }
            else if constexpr (a == 61) { r = (fails - r) >> 31; PINS(r); }
            else if constexpr (a == 62) { fails += r; PINS(fails); }
            else if constexpr (a == 63) { f0 = 2 * s - r; PINS(f0); }
            else if constexpr (a == 64) { f1 = f0 | 1; PINS(f1); }
            else if constexpr (a == 65) { r <<= 31; PINS(r); }
            else if constexpr (a >= 56 && (a < 92 || a >= 119)) __builtin_amdgcn_s_setprio(0);
            if constexpr (a < 32 && a % 8 == 7) asm volatile("" :: "v"(x[4 * b + 4]), "v"(x[4 * b + 5]), "v"(x[4 * b + 6]), "v"(x[4 * b + 7]), "v"(y0[b + 1]));
            if constexpr (a < 32 && a % 8 == 4) TOUCH(y1[b]);
            SCHED0();
            if constexpr (a == 15) lw32((rp ^ 2048) + 1024, z0());
            if constexpr (a < 32 && a % 8 < 7) blk(IC((b + 1) % 5), IC(a % 8));
            if constexpr (i == 0 || i == 4 || i == 5 || i == 8) x1[i / 2 - (i == 4 || i == 8)] = lr128(xb + 1024 + (i / 2 - (i == 4 || i == 8)) * 2048);
            if constexpr (a >= 92 && a < 108) x1[a - 88] = lr128(xb + 1024 + (a - 88) * 2048);
            SCHED0();
            if constexpr (a < 40 && j >= 4) { const u32x4 v = __builtin_bit_cast(u32x4, (j & 1) ? y1[b] : y0[b]); mx = umax3(mx, v[j & 2], v[(j & 2) + 1]); PIN(mx); }
            if constexpr (i == 1 || i == 3) {
                constexpr int rt2 = i == 3;
                asm volatile("" : "+v"(st[2 * rt2]), "+v"(st[2 * rt2 + 1]));
                const f32x4& q = st[2 * rt2 + 1];
                const float d = csel(csel(q[0], q[1], m1), csel(q[2], q[3], m1), m2);
                lw32(dp + rt2 * 512, __float_as_uint(st[2 * rt2][0])); lw32(dp + rt2 * 512 + 256, __float_as_uint(d));
            }
            if constexpr (i == 2) lw32(mx == 0xFFFFFFFFu ? sp + 1024 : dp + 4096, 4096u);
            if constexpr (i == 6) bar_lgkm<2>();
            if constexpr (i == 6) sa[0] = __builtin_bit_cast(f32x4, lr128(rp));
            if constexpr (i == 7) sb[0] = __builtin_bit_cast(f32x4, lr128(rp + 256));
            if constexpr (i == 9) sa[1] = __builtin_bit_cast(f32x4, lr128(rp + 512));
            if constexpr (i == 10) sb[1] = __builtin_bit_cast(f32x4, lr128(rp + 768));
            if constexpr (i == 11) bad = __builtin_bit_cast(u32x4, lr128(rp + 1024));
            if constexpr (i >= 12 && i < 20) stat(IC(i - 12));
            if constexpr (a == 40) { STAMP(s, 2) }
            if constexpr (a == UPOS0 || a == UPOS) st32(k.rs, fl, k.p0 + CNT(C_H, id), fv);
            if constexpr (a == 117) __builtin_amdgcn_raw_buffer_store_b128(o[0], k.rs, (int)sv, (int)ha, AUX_SC1);
            if constexpr (a == 118) TOUCH(o[0]);
            SCHED0();
            ep(IC(0), IC(0), IC(a - 56)); ep(IC(0), IC(1), IC(a - 86));
            if constexpr (a >= 116 && a < 124) stat(IC(a - 108));
            if constexpr (a == UPOS0 - 2 || a == UPOS - 2) { fv = a == UPOS - 2 ? f1 : f0; PIN(fv); }
            if constexpr (a == 80) { sv = l16 | r; PIN(sv); }
            if constexpr (a < 144) ep(IC(1), IC(0), IC(a - 124));
            else if constexpr (a < 164) ep(IC(1), IC(1), IC(a - 144));
            else ep(IC(1), IC(0), IC(a - 144));
            SCHED0();
        });
        STAMP(s, 3)
        asm volatile("s_nop 7" : "+v"(acc[1][3]));
        {
            const f32x4 u = (acc[1][3] + mean[1] * cs[3]) * rden[1];
            o[1][2] = packs(bfr(u[0]) * e[1][0], bfr(u[1]) * e[1][1]); o[1][3] = packs(bfr(u[2]) * e[1][2], bfr(u[3]) * e[1][3]);
        }
        st128(k.rs, sv, ha + EXP(49152), o[1]);
        if (r) { PCOUNT(0) RAWBAR() lw32(rp + 1024, 0u); RAWBAR() }
        st128(k.rs, s < 62 ? l16 : OOB, k.hseg((s + 2) & 63) + ho, ones); st128(k.rs, s < 62 ? l16 : OOB, k.hseg((s + 2) & 63) + EXP(49152) + ho, ones);
        STAMP(s, 4) STAMP(s, 5)
        PFLUSH(s)
        s = (f1 + 1) >> 1;
    }
    PROF_OUT
}

DEVI void down_role(const Params& p, const Ctx& k, char* smem, int id) {
    PROF_DECL
    const int l = k.l, w = k.w, g = l >> 4, n = l & 15, rt = id / 5, cg = id % 5;
    bf16x8 Wt[96];
    wstream<12, 8, 4>(smem + w * 32768,
        [&](auto U, unsigned sl) { constexpr int u = decltype(U)::value; sfor<8>([&](auto J) { constexpr int j = CV(J); dma1<(u % 4) * 8192 + j * 1024>(p.wd + (size_t)(w * 12 + u) * 32 * 640 + cg * 128, wsrc<256, false>(j * 64 + l, 1280), sl); }); },
        [&](auto U, char* sp) { constexpr int u = decltype(U)::value;
#pragma unroll
            for (int nt = 0; nt < 8; ++nt) Wt[u * 8 + nt] = wp_read<256, false>(sp, (nt + 2 * w) & 7, l); });
    RAWBAR()
    RSTAMP(2)
    const unsigned xo = EXP((cg * 4 + w) * 2048) + rt * 1024, xv = ((g >> 1) * 16 + n) * 16 + (g & 1) * 8;
    const u32x2 ones = {0xFFFFFFFFu, 0xFFFFFFFFu};
    const unsigned x2v = k.c ? xv : OOB;
    prog_init(k);
#pragma unroll
    for (int i = 0; i < 4; ++i) st64(k.rs, x2v, k.seg(PG_X2, i >> 1) + xo + (i & 1) * 512, ones);
    const __amdgpu_buffer_rsrc_t ro = __builtin_amdgcn_make_buffer_rsrc((void*)p.out, 0, 2048 * 1280, 0x00020000);
    const unsigned ov = k.c ? OOB : n * 1280 + g * 8, lim = k.c ? 0xFFFFFFFEu : 0xFFFFFFFFu, fl = (w == 0 && l == 0) ? 0u : OOB;
    unsigned fails = 0;
    unsigned ev = k.t0;
    for (int sx = -1; sx < 64; ++sx) {
        const int s = max(sx, 0);
        unsigned x2w = x2v | (sx & OOB), ow = ov | (sx & OOB);
        PIN(x2w); PIN(ow);
        unsigned la[3], ra = k.seg(PG_X1, s) + xo, oa = k.c ? k.seg(PG_X2, s) + xo : (s * 32 + rt * 16) * 1280 + (cg * 8 + w * 2) * 32, rb = (sx & 1) * 32768 + l * 16, wb[3], fv = sx;
#pragma unroll
        for (int q = 0; q < 3; ++q) { la[q] = k.hseg(s) + rt * EXP(49152) + EXP((w * 12 + q * 4) * 1024); wb[q] = rb + ((w + q + 1) & 3) * 6144 + q * 2048; PINS(la[q]); PIN(wb[q]); }
        rb += w * 6144;
        PINS(ra); PINS(oa); PIN(rb); PIN(fv);
        prog_wait<12>(k, k.p0 + CNT(C_H, 0), 2 * sx + rt);
        if (sx == EVS) ev = rclk();
        STOP(s) STAMP(s, 0)
        f32x4 acc[8], r[6];
        u32x2 res[2];
        u32x4 hx[12];
        unsigned long long bad;
        do {
            asm volatile("" ::: "memory");
#pragma unroll
            for (int i = 0; i < 12; ++i) { hx[i] = ld128(k.rs, l * 16 + (i & 3) * 1024, la[i >> 2]); SCHED0(); }
#pragma unroll
            for (int t = 0; t < 2; ++t) res[t] = ld64(k.rs, xv + t * 512, ra);
            SCHED0();
            unsigned mx = 0;
            sfor<72>([&](auto I) { constexpr int i = CV(I), ks = i / 6, j = 2 + i % 6, f = ks * 8 + j;
                mfa<(f < 64), ks == 0>(acc[j], Wt[f], __builtin_bit_cast(bf16x8, hx[ks]));
                SCHED0();
                if constexpr (i % 6 < 2) { mx = umax3(mx, hx[ks][(i & 1) * 2], hx[ks][(i & 1) * 2 + 1]); PIN(mx); }
                if constexpr (i == 68) { bad = __ballot(mx == 0xFFFFFFFFu); PINS(bad); }
                if constexpr (i == 69) { bad = fails - 1 < 65536u ? bad : 0; PINS(bad); }
                if constexpr (i == 70) { ++fails; PINS(fails); }
                if constexpr (i == DPOS) st32(k.rs, fl, k.p0 + CNT(C_X2, rt * 5 + cg), fv);
                SCHED0(); });
        } while (__builtin_expect(bad != 0, 0));
        STAMP(s, 1)
        float x[8];
        sfor<24>([&](auto I) { constexpr int i = CV(I), ks = i / 2, j = i & 1, f = ks * 8 + j;
            mfa<(f < 64), ks == 0>(acc[j], Wt[f], __builtin_bit_cast(bf16x8, hx[ks]));
            SCHED0();
            if constexpr (i < 12 && !(i & 1)) { constexpr int jj = 2 + i / 2;
                asm volatile("" : "+v"(acc[jj])); lw128(wb[jj / 2 - 1] + (jj & 1) * 1024, __builtin_bit_cast(u32x4, acc[jj])); }
            if constexpr (i < 8 && (i & 1)) { x[i - 1] = blo(res[i >> 2][(i >> 1) & 1]); x[i] = bhi(res[i >> 2][(i >> 1) & 1]); PIN(x[i - 1]); PIN(x[i]); }
            if constexpr (i == 13) bar_lgkm<0>();
            if constexpr (i >= 13 && i < 19) r[i - 13] = __builtin_bit_cast(f32x4, lr128(rb + (i - 13) * 1024));
            if constexpr (i + 72 == DPOS) st32(k.rs, fl, k.p0 + CNT(C_X2, rt * 5 + cg), fv);
            SCHED0(); });
        r[0] += r[2] + r[4]; r[1] += r[3] + r[5];
        asm volatile("s_nop 3" : "+v"(acc[0]), "+v"(acc[1]), "+v"(r[0]), "+v"(r[1]));
#pragma unroll
        for (int t = 0; t < 2; ++t) {
            const f32x4 d = acc[t] + r[t];
            const u32x2 o = {min(cvtpk(x[4 * t] + bfr(d[0]), x[4 * t + 1] + bfr(d[1])), lim), min(cvtpk(x[4 * t + 2] + bfr(d[2]), x[4 * t + 3] + bfr(d[3])), lim)};
            st64(k.rs, x2w + t * 512, oa, o);
            __builtin_amdgcn_raw_buffer_store_b64(o, ro, (int)(ow + t * 32), (int)oa, 0);
        }
        STAMP(s, 2)
#pragma unroll
        for (int t = 0; t < 2; ++t) st64(k.rs, s < 62 ? x2v : OOB, k.seg(PG_X2, (s + 2) & 63) + xo + t * 512, ones);
        STAMP(s, 3) STAMP(s, 4) STAMP(s, 5)
        PFLUSH(s)
    }
    if (id == 0 && k.c == EVC) sleep_until(ev + 30000);
    PROF_OUT
}

DEVI void warm(const Params& p, const Ctx& k, int xl, int hw) {
    set_m0(k.w * 256);
_Pragma("unroll 1")
    for (int it = hw; it < 32 * 15; it += k.c ? 40 : 64) {
        const int q = it < 120 ? it & 7 : 8 + (it - 120) % 24, t = it < 120 ? it >> 3 : (it - 120) / 24;
        const u16* b = q < 24 ? (q < 8 ? p.Wv : q < 16 ? p.Wk : p.Wq) + (xl * 8 + (q & 7)) * 192 : p.Wo + (xl * 8 + (q & 7)) * 122880;
        const unsigned ln = t * 64 + k.l, cl = min(((unsigned)(size_t)b >> 7) & 7u, 4u);
        if (k.c) sleep_until(k.t0 + WP + it * PP / 128);
        if (t < (q >= 24 ? PO : ((q < 8 ? PV : PQ) >> (cl * 4)) & 15))
            asm volatile("global_load_lds_dword %0, %1" :: "v"(q < 24 ? (639 - ln / 3) * 12288 + (ln % 3) * 128 : (191 - (ln >> 2)) * 1280 + 768 + (ln & 3) * 128), "s"(b) : "memory");
    }
    DRAIN();
_Pragma("unroll 1")
    for (int i = 3; i < 8; ++i) { sleep_until(k.t0 + i * 50); hello(k, 0); }
    DRAIN();
    RAWBAR()
}

DEVI int xcc_id() { return __builtin_amdgcn_s_getreg(20 | (0 << 6) | (3 << 11)) & 7; }

DEVI unsigned prepare(const Params& p, Ctx& k, char* smem, int xl, int i) {
    const int l = k.l, j = xl * 22 + i - 4;
    const u32x4 ones = {0xFFFFFFFFu, 0xFFFFFFFFu, 0xFFFFFFFFu, 0xFFFFFFFFu};
    unsigned vote = 0;
    if (i < 4) {
        const int row = ((i * 4 + xl) * 4 + k.w) * 4 + (l >> 4);
        const unsigned dst = k.seg(PG_XNA, i * 2 + (xl >> 1));
        u32x4 a[5];
#pragma unroll
        for (int c = 0; c < 5; ++c) a[c] = *(const u32x4*)(p.x + (size_t)row * 640 + (c * 16 + (l & 15)) * 8);
        ln_rows(a, l & 15, row & 31, [&](unsigned vo, int c, u32x4 o) { st128(k.rs, vo, dst + c * 16384, o); st128(k.rs, vo, dst + c * 16384 + 8192, o); });
    } else if (k.w == 3) {
        const unsigned cal = ((unsigned)(j % 44) << 21) + 7 * 16384 + (xl >> 1) * 4096 + (l & 7) * 16;
        int d = 0;
#pragma unroll 1
        for (int r = 0; r <= 2 * CALR; ++r) {
            unsigned long long c0, c1;
            st128(k.rs, cal, (r & 1) * 8192, ones);
            st128(k.rs, cal, (~r & 1) * 8192, ones);
            asm volatile("s_waitcnt vmcnt(1)\n\ts_memtime %0\n\ts_waitcnt vmcnt(0)\n\ts_memtime %1\n\ts_waitcnt lgkmcnt(0)" : "=&s"(c0), "=s"(c1) :: "memory");
            d = r ? min((int)(c1 - c0), 400) - d : 0;
        }
        vote = (unsigned)(d < 0) ^ (unsigned)(xcc_id() >> 2) ^ (unsigned)(i & 1);
    } else {
        if (k.w == 2 && j < 20) {
            const int e = j * 32 + (l >> 1), T = e >> 4, hf = (e >> 1) & 1, rr = ((e >> 2) & 3) * 2 + (e & 1);
            u32x4 v;
#pragma unroll
            for (int c = 0; c < 4; ++c) v[c] = min(*(const unsigned*)(p.mem0 + rr * 640 + T * 16 + 4 * c + 2 * hf), 0xFFFFFFFEu);
            st128(k.rs, (2u << 21) + EXP(e * 16) + (l & 1) * 8192, 0, v);
        }
        constexpr unsigned N0 = PR_B + PR_SZ, N1 = 40 * 16384, N2 = 32768;
        static_assert(N0 + N1 + N2 <= 5 * 88 * 3072 && NPAGES <= 44, "fill");
#pragma unroll
        for (int r = 0; r < 5; ++r) {
            const unsigned o = ((r * 88 + j) * 3 + k.w) * 1024;
            st128(k.rs, o < N0 + N1 + N2 ? l * 16 : OOB, o < N0 ? o : o < N0 + N1 ? ((unsigned)PG_YM << 21) + (o - N0) : (2u << 21) + 32768 + (o - N0 - N1), ones);
        }
    }
    if (k.w == 3) *(volatile LAS unsigned*)smem = vote;
    DRAIN();
    __builtin_amdgcn_s_waitcnt(0x0f70);
    XSTAMP(0)
    RAWBAR()
    return *(volatile LAS unsigned*)smem;
}
DEVI void master(const Params& p, const Ctx& k) {
    const int l = k.l;
    u32x2 v[4] = {{0u, 0u}, {0u, 0u}, {0u, 0u}, {0u, 0u}};
_Pragma("unroll 1")
    for (unsigned spins = 0; spins < SPIN_CAP; ++spins) {
#pragma unroll
        for (int q = 0; q < 4; ++q) v[q] |= cm_ld(k, (CM_IN + (q * 64 + l) * 128) | v[q][1]);
        if (__all((v[0][1] & v[1][1] & v[2][1] & v[3][1]) != 0)) break;
        asm volatile("" ::: "memory");
    }
    const unsigned long long a = __ballot(v[2][0] != 0), b = __ballot(v[3][0] != 0);
    const int n = __popcll(a) + __popcll(b);
    const unsigned long long g = n > 44 ? ~0ull : 0ull, u = (a & b) | ((a ^ b) & g);
    const unsigned long long t = (__popcll(a ^ b) > 2 || n > 80 || n < 8 ? g : ((u >> 4) & 0x3FFFFF) | ((u >> 36) << 22)) ^ 0xAAAAAAAAAAAAAAAAull;
#pragma unroll
    for (int q = 0; q < 4; ++q) cm_st(k, l * 128, CM_GO + q * 8192, (unsigned)t, (unsigned)(t >> 32) | 0x80000000u);
}

#define FIN(n) { __builtin_amdgcn_s_waitcnt(0x0f70); asm volatile("s_nop " #n); return; }
__global__ __launch_bounds__(256, 1) void mega_kernel(Params p) {
    extern __shared__ __attribute__((aligned(16))) char smem[];
    Ctx k;
    const u16 *wq = p.Wq, *wk = p.Wk, *wv = p.Wv;
    asm volatile("" : "+s"(wq), "+s"(wk), "+s"(wv) : "s"(p.cm));
    k.ws = p.ws; k.cm = p.cm;
    k.rs = __builtin_amdgcn_make_buffer_rsrc((void*)p.ws, 0, 0x7fffffff, 0x00020000);
    k.l = threadIdx.x & 63; k.w = __builtin_amdgcn_readfirstlane(threadIdx.x >> 6);
    RSTAMP(0)
    const int x = blockIdx.x & 7, i = blockIdx.x >> 3, xl = x & 3;
    k.role = x * 32 + i; k.c = x >> 2; k.par = 0; k.p0 = 0; k.t0 = rclk();
    line_st(k, CM_GO, 0, 0);
    auto start = [&]() {
        const u16* a = (i < 8 ? wk : wv) + (xl * 8 + (i & 7)) * 192, * b = (i < 8 ? wq : wv + 32 * 6144) + (xl * 8 + (i & 7)) * 192;
        unsigned o[3];
        sfor<18>([&](auto J) { constexpr int j = CV(J);
            if constexpr (j < 3) o[j] = wsrc<384, true>(k.w * 192 + j * 64 + k.l, 12288);
            dma1<SLOT(j / 6) + ((j / 3) & 1) * 12288 + (j % 3) * 1024>((((j / 3) & 1) ? b : a) + (j / 6) * (i < 8 ? 32 * 6144 : 64 * 6144), o[j % 3], k.w * 3072); }); };
    if (!k.c && i < 16) { if (QD && i < 8) sleep_until(k.t0 + QD); start(); }
    if (__builtin_expect(k.role == MASTER, 0)) {
#pragma unroll
        for (int q = 0; q < 16; ++q) cm_st(k, k.l * 8, CM_IN + k.w * 8192 + q * 512, 0, 0);
        cm_st(k, 0, CM_TMO, ~0u, ~0u);
        DRAIN();
        RAWBAR()
    }
    if (__builtin_expect(k.c && i < 26, 0)) { const unsigned v = prepare(p, k, smem, xl, i); RSTAMP(1) hello(k, v); go_wait(k, v); XSTAMP(1) RAWBAR() }
    else { RSTAMP(1) if (k.c) { DRAIN(); hello(k, 0); } }
    if (__builtin_expect(k.role == MASTER && k.w == 0, 0)) { master(p, k); __builtin_amdgcn_s_waitcnt(0x0f70); }
    if (k.c && i < 16) { sleep_until(k.t0 + WD1 + (i ^ 8) * STG); start(); }
    if (i < 8) { k.l = lane(); qk_role(p, k, smem, xl * 8 + i); FIN(0) }
    if (i < 16) { k.l = lane(); vo_role(p, k, smem, xl * 8 + i - 8); FIN(1) }
    k.l = lane();
    if (!k.c || i < 26) warm(p, k, xl, (i - 16) * 4 + k.w);
    else RAWBAR()
    go_wait(k, 0);
    if (i >= 26 && !k.c) {
        sleep_until(k.t0 + XPF);
#pragma unroll
        for (int r = 0; r < 3; ++r) asm volatile("global_load_lds_dword %0, %1" :: "v"(min(40 + r * 96 + ((i - 26) * 4 + xl) * 4 + k.w, 319) * 8192 + k.l * 128), "s"(p.x) : "memory");
        DRAIN();
        RAWBAR()
    }
    if (i >= 26) sleep_until(k.t0 + (k.c ? GST : GSA) + ((xl & 1) == UPODD ? 0 : i < 31 ? GSD : -GSA));
    if (i < 21) { mred_role(p, k, smem, xl * 5 + i - 16); FIN(2) }
    if (i < 26) { tred_role(p, k, smem, xl * 5 + i - 21); FIN(3) }
    if ((xl & 1) == UPODD) { up_role(p, k, smem, (xl >> 1) * 6 + i - 26); FIN(4) }
    if (i < 31) { down_role(p, k, smem, (xl >> 1) * 5 + i - 26); FIN(5) }
    row_role(p, k, smem, xl >> 1);
    FIN(6)
}

extern "C" void kernel_launch(void* const* d_in, const int* in_sizes, int n_in,
                              void* d_out, int out_size, void* d_ws, size_t ws_size,
                              hipStream_t stream) {
    Params p;
    p.x = (const u16*)d_in[0]; p.mem0 = (const u16*)d_in[1];
    p.Wq = (const u16*)d_in[2]; p.Wk = (const u16*)d_in[3]; p.Wv = (const u16*)d_in[4]; p.Wo = (const u16*)d_in[5];
    p.wg = (const u16*)d_in[8]; p.wu = (const u16*)d_in[9]; p.wd = (const u16*)d_in[10];
    p.out = (u16*)d_out; p.cm = (char*)d_ws;
    p.ws = (char*)(((uintptr_t)d_ws + CM_BYTES + ((1u << 21) - 1)) & ~(uintptr_t)((1u << 21) - 1));
    (void)hipFuncSetAttribute((const void*)mega_kernel, hipFuncAttributeMaxDynamicSharedMemorySize, LDS_BYTES);
    mega_kernel<<<256, 256, LDS_BYTES, stream>>>(p);
}
```

```cpp
#define EVB 0
#define EVT 1
#define OUTAUX 0
#define SWAPG
#include <hip/hip_runtime.h>
#include <hip/hip_bf16.h>
#include <stdint.h>
#include <utility>

typedef unsigned short u16;
typedef __attribute__((ext_vector_type(16))) short bf16x16;
typedef __attribute__((ext_vector_type(8))) short bf16x8;
typedef __attribute__((ext_vector_type(4))) short bf16x4;
typedef __attribute__((ext_vector_type(8))) __bf16 hbf8;
typedef __attribute__((ext_vector_type(16))) __bf16 hbf16;
typedef __attribute__((ext_vector_type(4))) float f32x4;
typedef __attribute__((ext_vector_type(2))) float f32x2;
typedef __attribute__((ext_vector_type(8))) unsigned u32x8;
typedef __attribute__((ext_vector_type(4))) unsigned u32x4;
typedef __attribute__((ext_vector_type(2))) unsigned u32x2;
typedef __attribute__((ext_vector_type(2))) __bf16 bf16x2_t;
#define LAS __attribute__((address_space(3)))
#define GAS __attribute__((address_space(1)))
#define DEVI __device__ __forceinline__

#define PSLEEP 8
#define WD1 1000
#define GST 550
#define NTA " nt"
#define NTB ""
#define PQ 0x077A0
#define PV 0x4AAE4
#define GSA 1600
#define QGS 8
#define VGS 8
#define MN 480
#define MD 250
#define MP 100
#define RSL 1000
#define GSD 250
#define PO 0
#define PDLY 0
#define WP 600
#define PP 100
#define STG 0
#define UPOS0 88
#define DPOS 88
#define XPF 1400
#define TPOS 33
#define XS0 40
#define XP 29
#define XB 66
#define TB 107
#define YSLP 2
#define RMSK 0x380u
#define OLDP 3
#define ARMP 0
#define QD 0
#define PACE 65
#define XPOS 74
#define YPOS 40
#define XPOSY 1
#define PWS 1
#define WUS 0
#define WMIX 1
#define PACE2 30
#define GDLY 80
#define QDLY 0
#define CALR 1
#define SPG 3
#define DQ 16
#define DV 12
#define BSLP 2
#define SDLY 0
#define UPOS 150
#define RDLY 45
#define NOYST 0
#define NOTOK 0
#define NOYLD 0
#define GPOSY 8
#define OLDMAP 1
#define UPODD 0
#define XLD 10
#define TROT 8
#define KOUT 1
#define RPOS 75
#define RDY 90
#define APOS 6
#define GPOS 74

#define EXP(o) ((((unsigned)(o) >> 13) << 14) | ((unsigned)(o) & 8191u))
enum { C_XN = 0, C_Y = 1, C_X1 = 2, C_XB = 3, C_H = 4, C_X2 = 5, C_BC = 8 };
#define CNT(type, s) ((unsigned)((type) * 16384 + (s) * 128))
constexpr unsigned PR_B = EXP(131072), PR_SZ = EXP(12 * 8192);
constexpr unsigned DUMMY_B = EXP(0);
constexpr int PG_MEMF = 2;
#define YXR 8
constexpr int PG_YX = 3;
constexpr int PG_XNA = PG_YX + 2 * YXR;
constexpr int PG_X1 = PG_XNA + 4, PG_X2 = PG_X1 + 4;
constexpr int PG_H = PG_X2 + 4;
constexpr int PG_YM = PG_H + 8;
constexpr int NPAGES = PG_YM + 2;
static_assert(NPAGES <= 64, "map");
constexpr unsigned CM_TAB = 0, CM_TICKET = 1024, CM_TMO = 4096, CM_IN = 8192, CM_GO = 40960, CM_BYTES = 73728;
constexpr int MASTER = 5 * 32 + 31;
constexpr unsigned SPIN_CAP = 1u << 20;
#define AUX_SC1 16
#define LDS_BYTES (160 * 1024)

struct Params {
    const u16* x; const u16* mem0; const u16* Wq; const u16* Wk; const u16* Wv; const u16* Wo;
    const u16* wg; const u16* wu; const u16* wd; u16* out; char* cm; char* ws;
};
struct Ctx {
    char* ws; char* cm; __amdgpu_buffer_rsrc_t rs; unsigned long long par;
    int c, l, w, role;
    unsigned p0, t0;
    DEVI unsigned pg(int page) const { return ((unsigned)page << 21) | (((unsigned)(par >> page) & 1u) << 13); }
    DEVI unsigned pg0(int page) const { return pg(page) ^ ((unsigned)c << 13); }
    DEVI unsigned seg(int page, int s) const { return pg(page + (s >> 4)) + (unsigned)(s & 15) * 131072u; }
    DEVI unsigned seg0(int page, int s) const { return seg(page, s) ^ ((unsigned)c << 13); }
    DEVI unsigned segx(int page, int s) const { return seg(page, s) ^ 8192u; }
    DEVI unsigned hseg(int s) const { return pg(PG_H + (s >> 3)) + (unsigned)(s & 7) * 262144u; }
    DEVI unsigned yx(int sg, int wq) const { return pg(PG_YX + (sg % YXR) * 2 + (wq >> 1)) + (unsigned)(wq & 1) * 1048576u; }
    DEVI unsigned ym(int s) const { return pg(PG_YM + ((s >> 1) & 1)) + (unsigned)(s & 1) * EXP(327680); }
    DEVI unsigned memf(int s) const { return pg(PG_MEMF) + (unsigned)s * 32768u; }
    DEVI char* cnt(int type, int s) const { return ws + (p0 + CNT(type, s)); }
    DEVI char* cnt0(int type, int s) const { return ws + ((p0 ^ ((unsigned)c << 13)) + CNT(type, s)); }
};

DEVI unsigned cvtpk(float a, float b) { return __builtin_bit_cast(unsigned, __builtin_convertvector((f32x2){a, b}, bf16x2_t)); }
DEVI unsigned packs(float a, float b) { return min(cvtpk(a, b), 0xFFFFFFFEu); }
DEVI float blo(unsigned x) { return __uint_as_float(x << 16); }
DEVI float bhi(unsigned x) { return __uint_as_float(x & 0xFFFF0000u); }
DEVI float bfr(float f) { return __uint_as_float(cvtpk(0.f, f)); }
DEVI u32x4 ld128(__amdgpu_buffer_rsrc_t rs, unsigned v, unsigned s) { return __builtin_amdgcn_raw_buffer_load_b128(rs, (int)v, (int)s, AUX_SC1); }
DEVI u32x2 ld64(__amdgpu_buffer_rsrc_t rs, unsigned v, unsigned s) { return __builtin_amdgcn_raw_buffer_load_b64(rs, (int)v, (int)s, AUX_SC1); }
DEVI unsigned ld32(__amdgpu_buffer_rsrc_t rs, unsigned v, unsigned s) { return __builtin_amdgcn_raw_buffer_load_b32(rs, (int)v, (int)s, AUX_SC1); }
DEVI void st128(__amdgpu_buffer_rsrc_t rs, unsigned v, unsigned s, u32x4 x) {
    __builtin_amdgcn_raw_buffer_store_b128(x, rs, (int)v, (int)s, AUX_SC1);
    asm volatile("s_nop 1" :: "v"(x) : "memory");
}
DEVI void st64(__amdgpu_buffer_rsrc_t rs, unsigned v, unsigned s, u32x2 x) { __builtin_amdgcn_raw_buffer_store_b64(x, rs, (int)v, (int)s, AUX_SC1); }
DEVI void st32(__amdgpu_buffer_rsrc_t rs, unsigned v, unsigned s, unsigned x) { __builtin_amdgcn_raw_buffer_store_b32(x, rs, (int)v, (int)s, AUX_SC1); }
#define RAWBAR() { __builtin_amdgcn_s_waitcnt(0xc07f); __builtin_amdgcn_s_barrier(); }
#define BARL(n) { asm volatile("" ::: "memory"); __builtin_amdgcn_s_waitcnt(0xc07f | ((n) << 8)); __builtin_amdgcn_s_barrier(); asm volatile("" ::: "memory"); }
#define DRAIN() asm volatile("s_waitcnt vmcnt(0)" ::: "memory")
#define SCHED0() __builtin_amdgcn_sched_barrier(0)
#define OOB 0x80000000u
DEVI int opq(int x) { asm volatile("" : "+v"(x)); return x; }
#define LANE const int l = opq(k.l), g = l >> 4, n = l & 15; (void)g; (void)n;
DEVI unsigned umax3(unsigned a, unsigned b, unsigned c) { return max(max(a, b), c); }
DEVI unsigned umax4(u32x4 v) { return max(umax3(v[0], v[1], v[2]), v[3]); }
DEVI unsigned rclk() { return (unsigned)__builtin_amdgcn_s_memrealtime(); }
DEVI unsigned long long rt_async() { unsigned long long t; asm volatile("s_memrealtime %0" : "=s"(t)); return t; }
DEVI void lgkm0(unsigned long long& a) { asm volatile("s_waitcnt lgkmcnt(0)" : "+s"(a) :: "memory"); }
DEVI unsigned spack(unsigned lo, unsigned hi) { unsigned d; asm volatile("s_pack_ll_b32_b16 %0, %1, %2" : "=s"(d) : "s"(lo), "s"(hi)); return d; }
DEVI int lane() { int l; asm volatile("v_mbcnt_lo_u32_b32 %0, -1, 0\n\tv_mbcnt_hi_u32_b32 %0, -1, %0" : "=v"(l)); __builtin_assume(l >= 0 && l < 64); return l; }
DEVI void sleep_until(unsigned target) { while ((int)(rclk() - target) < 0) __builtin_amdgcn_s_sleep(4); }
DEVI float xor16max(float x) { auto r = __builtin_amdgcn_permlane16_swap(__float_as_uint(x), __float_as_uint(x), false, false); return __builtin_fmaxf(__uint_as_float(r[0]), __uint_as_float(r[1])); }
DEVI float xor32max(float x) { auto r = __builtin_amdgcn_permlane32_swap(__float_as_uint(x), __float_as_uint(x), false, false); return __builtin_fmaxf(__uint_as_float(r[0]), __uint_as_float(r[1])); }
DEVI float xor16add(float x) { auto r = __builtin_amdgcn_permlane16_swap(__float_as_uint(x), __float_as_uint(x), false, false); return __uint_as_float(r[0]) + __uint_as_float(r[1]); }
DEVI float xor32add(float x) { auto r = __builtin_amdgcn_permlane32_swap(__float_as_uint(x), __float_as_uint(x), false, false); return __uint_as_float(r[0]) + __uint_as_float(r[1]); }
DEVI float fold32(float a, float b) { auto r = __builtin_amdgcn_permlane32_swap(__float_as_uint(a), __float_as_uint(b), false, false); return __uint_as_float(r[0]) + __uint_as_float(r[1]); }
#define ROR16(x, n) __builtin_bit_cast(float, __builtin_amdgcn_update_dpp(0, __builtin_bit_cast(int, (x)), 0x120 + (n), 0xf, 0xf, false))
DEVI float row16sum(float x) { x += ROR16(x, 8); x += ROR16(x, 4); x += ROR16(x, 2); x += ROR16(x, 1); return x; }
DEVI unsigned z0() { unsigned z; asm volatile("v_mov_b32 %0, 0" : "=v"(z)); return z; }
DEVI void cnt_add(char* p) { __hip_atomic_fetch_add((GAS unsigned*)(p + z0()), z0() + 1u, __ATOMIC_RELAXED, __HIP_MEMORY_SCOPE_AGENT); }
DEVI unsigned cnt_get(char* p) { return __hip_atomic_load((GAS unsigned*)(p + z0()), __ATOMIC_RELAXED, __HIP_MEMORY_SCOPE_AGENT) + 1u; }
DEVI bool spin_fail(const Ctx& k, unsigned spins) {
    if (spins > SPIN_CAP || ((spins & 255u) == 255u && __builtin_amdgcn_readfirstlane(cnt_get(k.cm + CM_TMO)) != 0)) { cnt_add(k.cm + CM_TMO); return true; }
    return false;
}
DEVI __amdgpu_buffer_rsrc_t cm_rs(const Ctx& k) { return __builtin_amdgcn_make_buffer_rsrc((void*)k.cm, 0, CM_BYTES, 0x00020000); }
DEVI void cm_st(const Ctx& k, unsigned v, unsigned off, unsigned lo, unsigned hi) { asm volatile("buffer_store_dwordx2 %0, %1, %2, %3 offen sc1" :: "v"((u32x2){lo, hi}), "v"(v), "s"(cm_rs(k)), "s"(off) : "memory"); }
DEVI u32x2 cm_ld(const Ctx& k, unsigned off) { return ld64(cm_rs(k), off, 0); }
DEVI void line_st(const Ctx& k, unsigned off, unsigned lo, unsigned hi) { cm_st(k, (lane() & 15) * 8 + (k.w ? OOB : 0u), off + k.role * 128, lo, hi); }
DEVI void hello(const Ctx& k, unsigned vote) { line_st(k, CM_IN, vote, OOB); }
DEVI u32x2 go_ask(const Ctx& k) { u32x2 d; asm volatile("buffer_load_dwordx2 %0, off, %1, %2 sc1" : "=v"(d) : "s"(cm_rs(k)), "s"(CM_GO + k.role * 128) : "memory"); return d; }
DEVI void go_wait(Ctx& k, unsigned vote, u32x2 v = {0u, 0u}) {
_Pragma("unroll 1")
    for (unsigned spins = 0; __builtin_amdgcn_readfirstlane(v[1]) >= 0 && spins < SPIN_CAP; ++spins) {
        if (spins) { hello(k, vote); __builtin_amdgcn_s_sleep(4); }
        asm volatile("buffer_load_dwordx2 %0, off, %1, %2 sc1\n\ts_waitcnt vmcnt(0)" : "=v"(v) : "s"(cm_rs(k)), "s"(CM_GO + k.role * 128) : "memory");
    }
    k.par = ((unsigned long long)(unsigned)__builtin_amdgcn_readfirstlane(v[1]) << 32 | (unsigned)__builtin_amdgcn_readfirstlane(v[0])) ^ (k.c ? ~0ull : 0ull);
    k.p0 = k.pg(0);
    __builtin_amdgcn_fence(__ATOMIC_ACQUIRE, "wavefront");
}
DEVI void cnt_wait(const Ctx& k, char* c, unsigned need) {
_Pragma("unroll 1")
    for (unsigned spins = 0; (unsigned)__builtin_amdgcn_readfirstlane(cnt_get(c)) < need; ++spins) {
        __builtin_amdgcn_s_sleep(PSLEEP);
        if (spin_fail(k, spins)) break;
    }
    __builtin_amdgcn_fence(__ATOMIC_ACQUIRE, "wavefront");
}
DEVI unsigned xn_get(const Ctx& k, unsigned l16, int s) { return ld32(k.rs, opq(l16) & 0x380u, k.p0 + CNT(C_XN, 0)); }
DEVI bool xn_has(const Ctx& k, unsigned v, int s) { return __all((int)v >= s - (k.c ? 0 : 8)); }
DEVI void xn_wait(const Ctx& k, unsigned l16, int s) {
_Pragma("unroll 1")
    for (unsigned spins = 0; !xn_has(k, xn_get(k, l16, s), s); ++spins) {
        asm volatile("" ::: "memory");
        __builtin_amdgcn_s_sleep(2);
        if (spin_fail(k, spins)) break;
    }
    __builtin_amdgcn_fence(__ATOMIC_ACQUIRE, "wavefront");
}
constexpr unsigned L_PW = 150 * 1024;
DEVI void prog_init(const Ctx& k) { *(volatile LAS u32x2*)(size_t)(L_PW + k.w * 512 + k.l * 8) = (u32x2){~0u, ~0u}; RAWBAR() }
DEVI void ask(const Ctx& k, unsigned va, unsigned a, unsigned m) { asm volatile("s_mov_b32 m0, %3\n\ts_nop 0\n\tbuffer_load_dword %0, %1, %2 offen sc1 lds" :: "v"(va), "s"(k.rs), "s"(a), "s"(m) : "memory"); }
template <int SLP> DEVI unsigned watch(const Ctx& k, unsigned va, unsigned a, unsigned m, unsigned ra, int s, int mute) {
    unsigned n, t;
    asm volatile("s_mov_b32 m0, %8\n\ts_mov_b32 %0, 0\n\ts_branch 2f\n"
        "1:\n\ts_cmp_lg_u32 %6, 0\n\ts_cbranch_scc1 3f\n\tbuffer_load_dword %2, %3, %4 offen sc1 lds\n"
        "3:\n\ts_sleep %9\n\ts_add_u32 %0, %0, 1\n\ts_and_b32 vcc_lo, %0, 0x7ff\n\ts_cbranch_scc1 2f\n\t"
        "v_mov_b32 %1, 0\n\ts_bitcmp1_b32 %0, 23\n\ts_cbranch_scc0 5f\n\tglobal_store_dword %1, %1, %10 sc1\n"
        "5:\n\tglobal_load_dword %1, %1, %10 sc1\n\ts_waitcnt vmcnt(0)\n\tv_cmp_ne_u32 vcc, -1, %1\n\ts_cbranch_vccnz 4f\n"
        "2:\n\tds_read_b32 %1, %7\n\ts_waitcnt lgkmcnt(0)\n\tv_cmp_gt_i32 vcc, %5, %1\n\ts_cbranch_vccnz 1b\n4:"
        : "=&s"(n), "=&v"(t) : "v"(va), "s"(k.rs), "s"(a), "s"(s), "s"(mute), "v"(ra), "s"(m), "n"(SLP), "s"(k.cm + CM_TMO) : "memory", "vcc", "scc");
    return t;
}
template <int N> DEVI void prog_wait(const Ctx& k, unsigned a, int s) { watch<PWS>(k, min(k.l, N - 1) * 128, a, L_PW, L_PW + k.l * 4, s, k.w); }
template <int OFF> DEVI void dma1(const void* sbase, unsigned voff, unsigned lds) {
    asm volatile("s_add_i32 m0, %2, %3\n\ts_nop 0\n\tglobal_load_lds_dwordx4 %0, %1" NTA :: "v"(voff), "s"(sbase), "s"(lds), "n"(OFF) : "memory", "scc");
}
DEVI void set_m0(unsigned lds) { asm volatile("s_mov_b32 m0, %0" :: "s"(lds) : "memory"); }
template <int OFF> DEVI void dmab(__amdgpu_buffer_rsrc_t rs, unsigned voff, unsigned soff) {
    asm volatile("buffer_load_dwordx4 %0, %1, %2 offen offset:%3 sc1 lds" :: "v"(voff), "s"(rs), "s"(soff), "n"(OFF) : "memory");
}
#define PIN(x) asm volatile("" : "+v"(x))
#define PINS(x) asm volatile("" : "+s"(x))
#define LEAD()
#define TOUCH(x) asm volatile("" :: "v"(x))
DEVI float csel(float a, float b, unsigned long long m) { float d; asm volatile("v_cndmask_b32_e64 %0, %1, %2, %3" : "=v"(d) : "v"(a), "v"(b), "s"(m)); return d; }
#define LO8(x) __builtin_shufflevector(x, x, 0, 1, 2, 3, 4, 5, 6, 7)
#define HI8(x) __builtin_shufflevector(x, x, 8, 9, 10, 11, 12, 13, 14, 15)
#define CAT8(a, b) __builtin_shufflevector(a, b, 0, 1, 2, 3, 4, 5, 6, 7, 8, 9, 10, 11, 12, 13, 14, 15)
template <bool AG> DEVI void smf(f32x4& c, const bf16x8& a, const bf16x16& w, unsigned ix) {
    if constexpr (AG) asm volatile("v_smfmac_f32_16x16x64_bf16 %0, %1, %2, %3" : "+v"(c) : "v"(a), "a"(w), "v"(ix));
    else asm volatile("v_smfmac_f32_16x16x64_bf16 %0, %1, %2, %3" : "+v"(c) : "v"(a), "v"(w), "v"(ix));
}
#define WTAB(X) X(0, "{a[0:7]}", "{a[0:3]}", "{a[4:7]}") X(1, "{a[8:15]}", "{a[8:11]}", "{a[12:15]}") X(2, "{a[16:23]}", "{a[16:19]}", "{a[20:23]}") X(3, "{a[24:31]}", "{a[24:27]}", "{a[28:31]}") X(4, "{a[32:39]}", "{a[32:35]}", "{a[36:39]}") X(5, "{a[40:47]}", "{a[40:43]}", "{a[44:47]}") X(6, "{a[48:55]}", "{a[48:51]}", "{a[52:55]}") X(7, "{a[56:63]}", "{a[56:59]}", "{a[60:63]}") X(8, "{a[64:71]}", "{a[64:67]}", "{a[68:71]}") X(9, "{a[72:79]}", "{a[72:75]}", "{a[76:79]}") X(10, "{a[80:87]}", "{a[80:83]}", "{a[84:87]}") X(11, "{a[88:95]}", "{a[88:91]}", "{a[92:95]}") X(12, "{a[96:103]}", "{a[96:99]}", "{a[100:103]}") X(13, "{a[104:111]}", "{a[104:107]}", "{a[108:111]}") X(14, "{a[112:119]}", "{a[112:115]}", "{a[116:119]}") X(15, "{a[120:127]}", "{a[120:123]}", "{a[124:127]}") X(16, "{a[128:135]}", "{a[128:131]}", "{a[132:135]}") X(17, "{a[136:143]}", "{a[136:139]}", "{a[140:143]}") X(18, "{a[144:151]}", "{a[144:147]}", "{a[148:151]}") X(19, "{a[152:159]}", "{a[152:155]}", "{a[156:159]}") X(20, "{a[160:167]}", "{a[160:163]}", "{a[164:167]}") X(21, "{a[168:175]}", "{a[168:171]}", "{a[172:175]}") X(22, "{a[176:183]}", "{a[176:179]}", "{a[180:183]}") X(23, "{a[184:191]}", "{a[184:187]}", "{a[188:191]}") X(24, "{a[192:199]}", "{a[192:195]}", "{a[196:199]}") X(25, "{a[200:207]}", "{a[200:203]}", "{a[204:207]}") X(26, "{a[208:215]}", "{a[208:211]}", "{a[212:215]}") X(27, "{a[216:223]}", "{a[216:219]}", "{a[220:223]}") X(28, "{a[224:231]}", "{a[224:227]}", "{a[228:231]}") X(29, "{a[232:239]}", "{a[232:235]}", "{a[236:239]}") X(30, "{a[240:247]}", "{a[240:243]}", "{a[244:247]}") X(31, "{a[248:255]}", "{a[248:251]}", "{a[252:255]}") X(32, "{v[96:103]}", "{v[96:99]}", "{v[100:103]}") X(33, "{v[104:111]}", "{v[104:107]}", "{v[108:111]}") X(34, "{v[112:119]}", "{v[112:115]}", "{v[116:119]}") X(35, "{v[120:127]}", "{v[120:123]}", "{v[124:127]}") X(36, "{v[128:135]}", "{v[128:131]}", "{v[132:135]}") X(37, "{v[136:143]}", "{v[136:139]}", "{v[140:143]}") X(38, "{v[144:151]}", "{v[144:147]}", "{v[148:151]}") X(39, "{v[152:159]}", "{v[152:155]}", "{v[156:159]}") X(40, "{v[160:167]}", "{v[160:163]}", "{v[164:167]}") X(41, "{v[168:175]}", "{v[168:171]}", "{v[172:175]}") X(42, "{v[176:183]}", "{v[176:179]}", "{v[180:183]}") X(43, "{v[184:191]}", "{v[184:187]}", "{v[188:191]}") X(44, "{v[192:199]}", "{v[192:195]}", "{v[196:199]}") X(45, "{v[200:207]}", "{v[200:203]}", "{v[204:207]}") X(46, "{v[208:215]}", "{v[208:211]}", "{v[212:215]}") X(47, "{v[216:223]}", "{v[216:219]}", "{v[220:223]}") X(48, "{v[224:231]}", "{v[224:227]}", "{v[228:231]}") X(49, "{v[232:239]}", "{v[232:235]}", "{v[236:239]}") X(50, "{v[240:247]}", "{v[240:243]}", "{v[244:247]}") X(51, "{v[248:255]}", "{v[248:251]}", "{v[252:255]}")
template <int ID> DEVI void smfw(f32x4& c, const bf16x8& a, const bf16x16& w, unsigned ix) {
#define X(i, P, L, H) if constexpr (ID == i) asm volatile("v_smfmac_f32_16x16x64_bf16 %0, %1, %2, %3" : "+v"(c) : "v"(a), P(w), "v"(ix));
    WTAB(X)
#undef X
}
template <int ID, int HF, bool WB, bool FIRST> DEVI void mfw(f32x4& c, const bf16x16& w, const bf16x8& x) {
#define Y(R, W8) { if constexpr (FIRST && WB) asm volatile("v_mfma_f32_16x16x32_bf16 %0, %2, %1, 0" : "=&v"(c) : R(W8), "v"(x)); \
    else if constexpr (FIRST) asm volatile("v_mfma_f32_16x16x32_bf16 %0, %1, %2, 0" : "=&v"(c) : R(W8), "v"(x)); \
    else if constexpr (WB) asm volatile("v_mfma_f32_16x16x32_bf16 %0, %2, %1, %0" : "+v"(c) : R(W8), "v"(x)); \
    else asm volatile("v_mfma_f32_16x16x32_bf16 %0, %1, %2, %0" : "+v"(c) : R(W8), "v"(x)); }
#define X(i, P, L, H) if constexpr (ID == i) { if constexpr (HF) Y(H, HI8(w)) else Y(L, LO8(w)) }
    WTAB(X)
#undef X
#undef Y
}
template <bool AG, bool FIRST> DEVI void mfa(f32x4& c, const bf16x8& w, const bf16x8& x) {
    if constexpr (AG && FIRST) asm volatile("v_mfma_f32_16x16x32_bf16 %0, %1, %2, 0" : "=&v"(c) : "a"(w), "v"(x));
    else if constexpr (AG) asm volatile("v_mfma_f32_16x16x32_bf16 %0, %1, %2, %0" : "+v"(c) : "a"(w), "v"(x));
    else if constexpr (FIRST) asm volatile("v_mfma_f32_16x16x32_bf16 %0, %1, %2, 0" : "=&v"(c) : "v"(w), "v"(x));
    else asm volatile("v_mfma_f32_16x16x32_bf16 %0, %1, %2, %0" : "+v"(c) : "v"(w), "v"(x));
}
template <bool AG, bool FIRST> DEVI void mfb(f32x4& c, const bf16x8& x, const bf16x8& w) {
    if constexpr (AG && FIRST) asm volatile("v_mfma_f32_16x16x32_bf16 %0, %1, %2, 0" : "=&v"(c) : "v"(x), "a"(w));
    else if constexpr (AG) asm volatile("v_mfma_f32_16x16x32_bf16 %0, %1, %2, %0" : "+v"(c) : "v"(x), "a"(w));
    else if constexpr (FIRST) asm volatile("v_mfma_f32_16x16x32_bf16 %0, %1, %2, 0" : "=&v"(c) : "v"(x), "v"(w));
    else asm volatile("v_mfma_f32_16x16x32_bf16 %0, %1, %2, %0" : "+v"(c) : "v"(x), "v"(w));
}
DEVI f32x4 mf(const bf16x8& a, const bf16x8& b, const f32x4& c) { return __builtin_amdgcn_mfma_f32_16x16x32_bf16(__builtin_bit_cast(hbf8, a), __builtin_bit_cast(hbf8, b), c, 0, 0, 0); }
template <int N, class F> DEVI void sfor(F f) { [&]<int... I>(std::integer_sequence<int, I...>) { (f(std::integral_constant<int, I>{}), ...); }(std::make_integer_sequence<int, N>{}); }
DEVI bf16x8 lds128(const char* p) { return *(const bf16x8*)p; }
DEVI bf16x4 ldstr(const char* p) { return __builtin_amdgcn_ds_read_tr16_b64_v4i16((LAS bf16x4*)p); }
DEVI bf16x8 ldstr2(const char* p, int d) { bf16x4 a = ldstr(p), b = ldstr(p + d); return __builtin_shufflevector(a, b, 0, 1, 2, 3, 4, 5, 6, 7); }

#define STAMP(s, k)
#define QSTAMP(f, i)
#define STOP(s)
#define PFLUSH(s)
#define PROF_DECL
#define RSTAMP(i)
#define XSTAMP(i)
#define PCOUNT(i)
#define PROF_OUT

#define HSTAMP STAMP
#define HFLUSH PFLUSH
#define HTRACE(s)
#define TROUT
#define TRHINT
#define AQ(m, i)
#define AC3(i, j, k) { }
#define AST(i)
#define AAC(i)
#define ACCM 0
#define HSTOP(m) if constexpr ((ACCM != 0) == m) { STOP(0) }
#define PC8(i, x)
#define PROF_ONLY(x)
#define PROF_S(x)
template <int RB, bool STD> DEVI int swz(int row) { return RB == 384 ? ((row >> 1) & 1) | ((row >> 2) & 2) : RB == 128 ? (row >> 1) & 3 : STD ? (row & 3) | ((row >> 1) & 4) : row & 7; }
template <int RB, bool STD> DEVI unsigned wsrc(unsigned id, int ld) { const unsigned row = id / (RB / 16), c = id % (RB / 16); return row * ld + ((c ^ (swz<RB, STD>(row) << 1)) << 4); }
template <int RB, bool STD> DEVI int wp_adr(int T, int l) { const int row = (STD ? 8 : 4) * (l >> 4) + ((l >> 2) & 3); return row * RB + ((T ^ swz<RB, STD>(row)) << 5) + (l & 3) * 8; }
template <int RB, bool STD> DEVI bf16x8 wp_read(const char* blk, int T, int l) { return ldstr2(blk + wp_adr<RB, STD>(T, l), (STD ? 4 : 16) * RB); }
template <int NU, class CNT, class STEP> DEVI void cstream(CNT cnt, STEP step) {
    sfor<NU>([&](auto V) { constexpr int v = decltype(V)::value;
        asm volatile("s_waitcnt vmcnt(%0) lgkmcnt(0)\n\ts_barrier" :: "n"(decltype(cnt(V))::value * (v ? 1 : 2)) : "memory");
        step(V);
    });
}
template <int NU, int NI, int D, class ISSUE, class READ> DEVI void wstream(char* ring, ISSUE issue, READ read) {
    const unsigned base = (unsigned)(size_t)(LAS char*)ring;
    sfor<D>([&](auto U) { constexpr int u = decltype(U)::value; if constexpr (u < NU) issue(U, base); });
    sfor<NU>([&](auto U) {
        constexpr int u = decltype(U)::value, left = (NU - u < D ? NU - u : D) - 1;
        asm volatile("s_waitcnt vmcnt(%0)" :: "n"(left * NI) : "memory");
        read(U, ring + (u % D) * NI * 1024);
        __builtin_amdgcn_s_waitcnt(0xc07f);
        asm volatile("" ::: "memory");
        if constexpr (u + D < NU) issue(std::integral_constant<int, u + D>{}, base);
    });
}

constexpr int L_XN = 0;
constexpr int L_K = 40960;
constexpr int L_MEM = L_K + 12288;
constexpr int L_QM = L_MEM + 10240;
constexpr int L_Q = L_QM + 12288;
constexpr int L_W = 94208;
#define SLOT(i) ((i) % 3 == 0 ? 45056 : (i) % 3 == 1 ? 69632 : L_W + 40960)
#define CMIN(a, b) ((a) < (b) ? (a) : (b))
#define IC(x) std::integral_constant<int, (x)>{}
#define CV(X) decltype(X)::value
DEVI bf16x8 lr128(unsigned a) { return *(const LAS bf16x8*)(size_t)a; }
DEVI bf16x4 lrtr(unsigned a) { return __builtin_amdgcn_ds_read_tr16_b64_v4i16((LAS bf16x4*)(size_t)a); }
#define CAT4(a, b) __builtin_shufflevector(a, b, 0, 1, 2, 3, 4, 5, 6, 7)
DEVI void lw128(unsigned a, u32x4 v) { *(LAS u32x4*)(size_t)a = v; }
DEVI void lw64(unsigned a, u32x2 v) { *(LAS u32x2*)(size_t)a = v; }
DEVI void lw32(unsigned a, unsigned v) { *(LAS unsigned*)(size_t)a = v; }
template <int N> DEVI void bar_lgkm() { asm volatile("s_waitcnt lgkmcnt(%0)\n\ts_barrier" :: "n"(N) : "memory"); }
DEVI void atom1(__amdgpu_buffer_rsrc_t rs, unsigned voff, unsigned soff) {
    unsigned one; asm volatile("v_mov_b32 %0, 1\n\tbuffer_atomic_add %0, %1, %2, %3 offen" : "=&v"(one) : "v"(voff), "s"(rs), "s"(soff) : "memory");
}
DEVI unsigned vadd(unsigned v, unsigned sc) { unsigned d; asm volatile("v_add_u32 %0, %1, %2" : "=v"(d) : "s"(sc), "v"(v)); return d; }
DEVI float vzero() { float z; asm volatile("v_mov_b32 %0, 0" : "=v"(z)); return z; }
template <int S> DEVI unsigned slot_adr(unsigned a) { if constexpr (S % 3 == 2) return vadd(a, SLOT(2) - SLOT(0)); else return a + (SLOT(S) - SLOT(0)); }
template <int B, int I> DEVI void xn_op(const Ctx& k, unsigned v, unsigned src, unsigned dst) {
    if constexpr (I == 0) set_m0(dst + B * 8192);
    else if constexpr (I == 1) dmab<0>(k.rs, v, src + B * 16384);
    else dmab<1024>(k.rs, v, src + B * 16384);
}
DEVI void xn_all(const Ctx& k, unsigned v, unsigned src, unsigned dst) {
    sfor<5>([&](auto B) { xn_op<CV(B), 0>(k, v, src, dst); asm volatile("s_nop 0"); xn_op<CV(B), 1>(k, v, src, dst); xn_op<CV(B), 2>(k, v, src, dst); });
}
struct Xn {
    unsigned src, dst, sg, need, t, sb, se, l16, c, off;
    unsigned long long flip, m;
    DEVI void init(const Ctx& k) {
        src = k.seg(PG_XNA, 0) + k.w * 2048; dst = L_XN + k.w * 2048; sg = 0; off = ~0u;
        const unsigned q = (unsigned)(k.par >> PG_XNA), d = q ^ (q >> 1);
        flip = ((unsigned long long)(d & 1) << 16) | ((unsigned long long)(d & 2) << 31) | ((unsigned long long)(d & 4) << 46);
    }
    template <int I> DEVI void next(const Ctx& k, unsigned l16) {
        if constexpr (I == 0) { sg += 1; src += 131072; PINS(sg); PINS(src); }
        if constexpr (I == 1) asm volatile("s_bitcmp1_b64 %1, %2\n\ts_cselect_b32 %0, 0x2000, 0" : "=s"(t) : "s"(flip), "s"(sg) : "scc");
        if constexpr (I == 2) { src ^= t; PINS(src); }
        if constexpr (I == 3) asm volatile("v_and_b32 %0, 0x380, %1" : "=v"(c) : "v"(l16));
        if constexpr (I == 4) c = ld32(k.rs, c, k.p0 + CNT(C_XN, 0));
        if constexpr (I == 5) { need = sg - (k.c ? 0u : 8u); PINS(need); }
        if constexpr (I == 6) { need = sg < 64u ? need : 0x7FFFFFFFu; PINS(need); }
        if constexpr (I == 7) asm volatile("v_cmp_gt_i32_e64 %0, %1, %2" : "=s"(m) : "s"(need), "v"(c) : "memory");
        if constexpr (I == 8) asm volatile("s_cmp_lg_u64 %2, 0\n\ts_cselect_b32 %0, -1, 0\n\ts_cselect_b32 %1, 0x200000, %3" : "=&s"(off), "=s"(se) : "s"(m), "s"(src) : "scc");
        if constexpr (I == 13) set_m0(L_MEM);
        if constexpr (I == 14) dmab<0>(k.rs, c, k.p0 + CNT(C_XN, 0));
        if constexpr (I == 15) c = *(volatile LAS unsigned*)(size_t)(l16 + L_MEM);
        if constexpr (I == 11) { need = sg < 64u ? need : 0x80000000u; PINS(need); }
        if constexpr (I == 12) {
            if (__builtin_expect(m != 0, 0)) {
_Pragma("unroll 1")
                for (unsigned spins = 0; __any((int)*(volatile LAS unsigned*)(size_t)(l16 + L_MEM) < (int)need) && !spin_fail(k, spins >> 2); ++spins) { next<3>(k, l16); __builtin_amdgcn_s_sleep(1); next<14>(k, l16); }
            }
            se = src; }
        if constexpr (I == 10) asm volatile("s_cmp_lg_u32 %1, 64\n\ts_cselect_b32 %0, %0, 0" : "+s"(off) : "s"(sg) : "scc");
    }
    template <int B, int I> DEVI void op(const Ctx& k) {
        if constexpr (I == 0) { set_m0(dst + B * 8192); sb = se + B * 16384; PINS(sb); }
        else dmab<(I - 1) * 1024>(k.rs, l16, sb);
    }
    template <int NB> DEVI void late(const Ctx& k, unsigned l16) {
        xn_wait(k, l16, sg);
        se = src;
        sfor<NB>([&](auto B) { op<CV(B), 0>(k); asm volatile("s_nop 0"); op<CV(B), 1>(k); op<CV(B), 2>(k); });
        DRAIN();
    }
};
template <int OFF> DEVI u32x4 ald128(__amdgpu_buffer_rsrc_t rs, unsigned v, unsigned s) { u32x4 d; asm volatile("buffer_load_dwordx4 %0, %1, %2, %3 offen offset:%4 sc1" : "=v"(d) : "v"(v), "s"(rs), "s"(s), "n"(OFF) : "memory"); return d; }
#define MADR(b) (L_MEM + ((b) < 6 ? ((b) - 1) * 2048 : ((b) - 6) * 2048 + 3072))
struct Gather {
    u32x4 p0, p1, v0, v1;
    unsigned mx, a, aw, b, base, wo, dst;
    unsigned long long bad;
    DEVI void init(const Ctx& k) { base = k.memf(0); wo = k.w * 1024; dst = L_MEM + k.w * 2048; }
    template <int I> DEVI void op(const Ctx& k, unsigned l16, int s) {
        if constexpr (I == 0) { a = ((unsigned)s << 15) & 0x1F8000u; PINS(a); }
        if constexpr (I == 1) { a += base; aw = a + wo; PINS(a); PINS(aw); }
        if constexpr (I == 2) { b = aw + 3072 + (k.w >> 1) * 8192; PINS(b); set_m0(dst); }
        if constexpr (I == 3) v0 = ald128<2048>(k.rs, l16, aw);
        if constexpr (I == 4) v1 = ald128<3072>(k.rs, l16, b);
        if constexpr (I == 5) dmab<2048>(k.rs, l16, aw);
        if constexpr (I == 6) dmab<3072>(k.rs, l16, b);
        if constexpr (I == 7) p0 = ald128<0>(k.rs, l16, a);
        if constexpr (I == 8) p1 = ald128<1024>(k.rs, l16, a);
    }
    template <int N> DEVI void wait() { asm volatile("s_waitcnt vmcnt(%4)" : "+v"(p0), "+v"(p1), "+v"(v0), "+v"(v1) : "n"(N) : "memory"); }
    template <int I> DEVI void fin(unsigned other) {
#define MX3 "v_max3_u32 %0, %0, %3, %4\n\tv_max3_u32 %0, %0, %5, %6"
        if constexpr (I == 0) asm volatile("v_max3_u32 %0, %7, %1, %2\n\t" MX3 : "=&v"(mx) : "v"(v0[0]), "v"(v0[1]), "v"(v0[2]), "v"(v0[3]), "v"(v1[0]), "v"(v1[1]), "s"(other));
        if constexpr (I == 1) asm volatile("v_max3_u32 %0, %0, %1, %2\n\t" MX3 : "+v"(mx) : "v"(v1[2]), "v"(v1[3]), "v"(p0[0]), "v"(p0[1]), "v"(p0[2]), "v"(p0[3]));
        if constexpr (I == 2) asm volatile("v_max3_u32 %1, %1, %2, %3\n\tv_max3_u32 %1, %1, %4, %5\n\tv_cmp_eq_u32_e64 %0, -1, %1" : "=s"(bad) : "v"(mx), "v"(p1[0]), "v"(p1[1]), "v"(p1[2]), "v"(p1[3]));
    }
    DEVI void also(unsigned other) { asm volatile("v_mov_b32 %0, %1" : "=v"(mx) : "s"(other)); }
    DEVI void all() {
        asm volatile("s_waitcnt vmcnt(0)\n\tv_max3_u32 %1, %1, %2, %3\n\tv_max3_u32 %1, %1, %4, %5\n\tv_max3_u32 %1, %1, %6, %7\n\tv_max3_u32 %1, %1, %8, %9\n\tv_max3_u32 %1, %1, %10, %11\n\tv_max3_u32 %1, %1, %12, %13\n\t"
            "v_max3_u32 %1, %1, %14, %15\n\tv_max3_u32 %1, %1, %16, %17\n\tv_cmp_eq_u32_e64 %0, -1, %1" : "=s"(bad) : "v"(mx), "v"(v0[0]), "v"(v0[1]), "v"(v0[2]), "v"(v0[3]), "v"(v1[0]), "v"(v1[1]), "v"(v1[2]), "v"(v1[3]),
            "v"(p0[0]), "v"(p0[1]), "v"(p0[2]), "v"(p0[3]), "v"(p1[0]), "v"(p1[1]), "v"(p1[2]), "v"(p1[3]) : "memory");
    }
    DEVI void check() { also(0); all(); asm volatile("s_nop 3"); }
    unsigned rv, rs; unsigned long long rt;
    template <int I> DEVI void ask(const Ctx& k, unsigned l16) {
        if constexpr (I == 0) asm volatile("v_and_b32 %0, 0xf0, %1\n\tv_lshlrev_b32 %0, 1, %0" : "=v"(rv) : "v"(l16));
        if constexpr (I == 1) asm volatile("buffer_load_dword %0, %0, %1, %2 offen sc1" : "+v"(rv) : "s"(k.rs), "s"(k.p0 + CNT(C_XB, 0)) : "memory");
    }
    template <int I> DEVI void red() {
        if constexpr (I == 0) asm volatile("s_waitcnt vmcnt(4)\n\tv_max_u32_dpp %0, %0, %0 quad_perm:[1,0,3,2] row_mask:0xf bank_mask:0xf" : "+v"(rv));
        if constexpr (I == 1) asm volatile("v_max_u32_dpp %0, %0, %0 quad_perm:[2,3,0,1] row_mask:0xf bank_mask:0xf" : "+v"(rv));
        if constexpr (I == 2) asm volatile("v_max_u32_dpp %0, %0, %0 row_ror:4 row_mask:0xf bank_mask:0xf" : "+v"(rv));
        if constexpr (I == 3) asm volatile("v_max_u32_dpp %0, %0, %0 row_ror:8 row_mask:0xf bank_mask:0xf" : "+v"(rv));
        if constexpr (I == 4) asm volatile("v_readfirstlane_b32 %0, %1" : "=s"(rs) : "v"(rv));
    }
    template <int I> DEVI void hold(unsigned step) {
        if constexpr (I == 0) rt = rt_async();
        if constexpr (I == 3) { PINS(rt); b = spack((unsigned)rt, step); }
        if constexpr (I == 4) asm volatile("s_sub_u32 %0, %1, %0" : "+s"(b) : "s"(rs) : "scc");
        if constexpr (I == 5) if (__builtin_expect(b < 128u, 0)) { while ((int)((rclk() - rs) << 16) < 0) { } }
    }
    template <int I> DEVI void at(const Ctx& k, unsigned l16) {
        if constexpr (I == 1) op<2>(k, l16, 0);
        if constexpr (I >= 2 && I < 15 && (I % 5 == 2 || I % 5 == 4)) op<3 + (I / 5) * 2 + (I % 5) / 4>(k, l16, 0);
    }
    DEVI void again(const Ctx& k, unsigned l16) {
        asm volatile("s_mov_b32 m0, %9\n\tbuffer_load_dwordx4 %2, %4, %5, %7 offen offset:2048 sc1\n\tbuffer_load_dwordx4 %3, %4, %5, %8 offen offset:3072 sc1\n\t"
            "buffer_load_dwordx4 %4, %5, %7 offen offset:2048 sc1 lds\n\tbuffer_load_dwordx4 %4, %5, %8 offen offset:3072 sc1 lds\n\t"
            "buffer_load_dwordx4 %0, %4, %5, %6 offen sc1\n\tbuffer_load_dwordx4 %1, %4, %5, %6 offen offset:1024 sc1\n\ts_waitcnt vmcnt(0)"
            : "+v"(p0), "+v"(p1), "+v"(v0), "+v"(v1) : "v"(l16), "s"(k.rs), "s"(a), "s"(aw), "s"(b), "s"(dst) : "memory");
        check();
    }
};
DEVI void clr2(f32x4& c) { f32x2 a, b; asm volatile("v_mov_b64 %0, 0\n\tv_mov_b64 %1, 0" : "=v"(a), "=v"(b)); c = __builtin_shufflevector(a, b, 0, 1, 2, 3); }
#define NOP7_3(a) asm volatile("s_nop 7" : "+v"(a[0]), "+v"(a[1]), "+v"(a[2]))

struct G3 {
    f32x4 a0[3], a1[3];
    bf16x8 x0[20], x1[20], wl[20];
    unsigned l16, lwv;
    template <int KS> DEVI void r0() { x0[KS] = lr128(l16 + L_XN + KS * 2048); }
    template <int KS> DEVI void r1() { x1[KS] = lr128(l16 + L_XN + KS * 2048 + 1024); }
    template <int KS> DEVI void rw() { if constexpr (KS >= 4) wl[KS] = lr128(lwv + (KS - 4) * 4096); }
    template <bool WL, int KS> DEVI void pre() { if constexpr (WL && KS >= 4) asm volatile("" :: "v"(wl[KS]), "v"(x0[KS]), "v"(x1[KS])); else asm volatile("" :: "v"(x0[KS]), "v"(x1[KS])); }
    template <int ID0, bool WL, int ks, int m> DEVI void mm(const bf16x16 (&W)[52]) {
        constexpr int b = ks >> 1, t = m % 3, id = ID0 + t * 10 + b;
        f32x4& c = m < 3 ? a0[t] : a1[t];
        const bf16x8& x = m < 3 ? x0[ks] : x1[ks];
        if constexpr (WL && t == 2 && b >= 2) { if constexpr (ID0 == 30) mfb<false, false>(c, x, wl[ks]); else mfa<false, false>(c, wl[ks], x); }
        else mfw<id % 52, ks & 1, ID0 == 30, ks == 0>(c, W[id % 52], x);
        SCHED0();
    }
    template <int ID0, bool WL, int K0, int K1, int PF, bool H6 = false, class HOOK> DEVI void run(const bf16x16 (&W)[52], HOOK hook) {
        sfor<K1 - K0>([&](auto KK) {
            constexpr int kk = CV(KK), ks = K0 + kk;
            sfor<6>([&](auto M) {
                constexpr int m = CV(M);
                mm<ID0, WL, ks, m>(W);
                if constexpr (m == 5 && ks + 1 < K1) { pre<WL, (ks + 1) % 20>(); SCHED0(); }
                if constexpr (ks + 2 < PF) {
                    if constexpr (m == 0) r0<(ks + 2) % 20>();
                    if constexpr (m == 1) r1<(ks + 2) % 20>();
                    if constexpr (m == 3 && WL) rw<(ks + 2) % 20>();
                }
                if constexpr (H6) hook(IC(kk * 6 + m));
                else { hook(IC(kk * 7 + (m < 5 ? m : 6))); if constexpr (m == 4) hook(IC(kk * 7 + 5)); }
                SCHED0();
            });
        });
    }
    template <int ID0, bool WL, int F0, int F1, int PF, class HOOK> DEVI void go(const bf16x16 (&W)[52], HOOK hook) {
        sfor<F1 - F0>([&](auto FF) {
            constexpr int f = F0 + CV(FF), ks = f / 6, m = f % 6;
            mm<ID0, WL, ks, m>(W);
            if constexpr (m == 5 && f + 1 < F1) { pre<WL, (ks + 1) % 20>(); SCHED0(); }
            if constexpr (ks + 2 < PF) rd<WL, (f + 12) % 120>();
            hook(IC(f));
            SCHED0();
        });
    }
    template <int P> DEVI void home() {
        if constexpr (P == 0) asm volatile("" : "+{v[12:15]}"(a0[0]), "+{v[16:19]}"(a0[1]), "+{v[20:23]}"(a0[2]), "+{v[24:27]}"(a1[0]), "+{v[28:31]}"(a1[1]), "+{v[32:35]}"(a1[2]));
        else asm volatile("" : "+{v[36:39]}"(a0[0]), "+{v[40:43]}"(a0[1]), "+{v[44:47]}"(a0[2]), "+{v[48:51]}"(a1[0]), "+{v[52:55]}"(a1[1]), "+{v[56:59]}"(a1[2]));
    }
    template <bool WL, int F> DEVI void rd() {
        if constexpr (F % 6 == 0) r0<F / 6>();
        if constexpr (F % 6 == 1) r1<F / 6>();
        if constexpr (F % 6 == 3 && WL) rw<F / 6>();
    }
    u32x2 o;
    template <int I, int OFF> DEVI void out4(unsigned d0) {
        if constexpr (I >= 0 && I < 24) {
            constexpr int t = I / 8, rt = (I / 4) & 1, j = I & 1;
            const f32x4& a = rt ? a1[t] : a0[t];
            if constexpr (I % 4 < 2) { o[j] = cvtpk(a[2 * j], a[2 * j + 1]); PIN(o[j]); }
            else lw32(d0 + OFF + rt * 6144 + t * 1024 + j * 4, o[j]);
        }
    }
    template <int I, int OFF> DEVI void out(unsigned d0, unsigned d1) {
        if constexpr (I >= 0 && I < 18) {
            constexpr int t = I / 6, rt = (I / 3) & 1;
            const f32x4& a = rt ? a1[t] : a0[t];
            if constexpr (I % 3 == 0) { o[0] = cvtpk(a[0], a[1]); PIN(o[0]); }
            else if constexpr (I % 3 == 1) { o[1] = cvtpk(a[2], a[3]); PIN(o[1]); }
            else if constexpr (OFF > 0) lw64(d0 + (OFF - 1) + rt * 6144 + t * 1024, o);
            else lw64((t == 1 ? d1 : d0) + OFF + rt * 6144 + (t == 2 ? 1024 : 0), o);
        }
    }
};
#define SPIX(l) (((l) & 2) ? 0xEEEEu : 0x4444u)
template <int I> DEVI void fold_op(f32x4& a, unsigned& o) {
    if constexpr (I == 0) asm volatile("v_add_f32 %0, %0, %1" : "+v"(a[0]) : "v"(a[2]));
    if constexpr (I == 1) asm volatile("v_add_f32 %0, %0, %1" : "+v"(a[1]) : "v"(a[3]));
    if constexpr (I == 2) asm volatile("v_cvt_pk_bf16_f32 %0, %1, %2" : "=v"(o) : "v"(a[0]), "v"(a[1]));
}

template <int J> DEVI void fold2(f32x4& a, f32x4& b, unsigned (&o)[2]) {
    if constexpr (J < 2) asm volatile("v_add_f32 %0, %0, %2\n\tv_add_f32 %1, %1, %3" : "+v"(a[J]), "+v"(b[J]) : "v"(a[J + 2]), "v"(b[J + 2]));
    else asm volatile("v_cvt_pk_bf16_f32 %0, %2, %3\n\tv_cvt_pk_bf16_f32 %1, %4, %5" : "=&v"(o[0]), "=&v"(o[1]) : "v"(a[0]), "v"(a[1]), "v"(b[0]), "v"(b[1]));
}
DEVI unsigned vshl1(unsigned x, unsigned b) { unsigned d; asm volatile("v_lshl_or_b32 %0, %1, 1, %2" : "=v"(d) : "s"(x), "n"(b)); return d; }

#define ANNOUNCE(s, now, d) (((unsigned)(s) << 16) | (((unsigned)(now) + (d)) & 0xFFFFu))

template <class PUT> DEVI void ln_rows(const u32x4 (&a)[5], int n, int row  , PUT put) {
    float v[40];
#pragma unroll
    for (int i = 0; i < 20; ++i) { v[2 * i] = blo(a[i >> 2][i & 3]); v[2 * i + 1] = bhi(a[i >> 2][i & 3]); }
    f32x2 s2[4] = {{0.f, 0.f}, {0.f, 0.f}, {0.f, 0.f}, {0.f, 0.f}};
#pragma unroll
    for (int i = 0; i < 40; i += 2) s2[(i >> 1) & 3] += (f32x2){v[i], v[i + 1]};
    const f32x2 st = (s2[0] + s2[1]) + (s2[2] + s2[3]);
    float m = row16sum(st[0] + st[1]) * (1.f / 640.f);
    f32x2 q2[4] = {{0.f, 0.f}, {0.f, 0.f}, {0.f, 0.f}, {0.f, 0.f}};
#pragma unroll
    for (int i = 0; i < 40; i += 2) { const f32x2 d = (f32x2){v[i], v[i + 1]} - (f32x2){m, m}; q2[(i >> 1) & 3] += d * d; }
    const f32x2 qt = (q2[0] + q2[1]) + (q2[2] + q2[3]);
    const float q = row16sum(qt[0] + qt[1]);
    const float var = bfr(q * (1.f / 640.f));
    m = bfr(m);
    const float rden = __builtin_amdgcn_rcpf(bfr(__builtin_amdgcn_sqrtf(bfr(var + 1e-5f))));
    const unsigned vo = (n >> 2) * 2048 + (row >> 4) * 1024 + ((n & 3) * 16 + (row & 15)) * 16;
#pragma unroll
    for (int c = 0; c < 5; ++c) {
        u32x4 o;
#pragma unroll
        for (int i = 0; i < 4; ++i) { unsigned d = cvtpk(v[c * 8 + 2 * i] - m, v[c * 8 + 2 * i + 1] - m); o[i] = cvtpk(blo(d) * rden, bhi(d) * rden); }
        put(vo, c, o);
    }
}

struct Soft {
    unsigned long long mA, mY, mC[8];
    unsigned v0, v1;
    unsigned sg, hs;
    float a[6], m, M, t, s, h; u32x2 pk;
    template <int I> DEVI void mask(f32x4 (&sc)[3]) {
        if constexpr (I == 0) asm volatile("v_mov_b32 %0, 0xfe800000" : "=v"(t));
        if constexpr (I >= 1 && I < 9) { float d; asm volatile("v_cndmask_b32_e64 %0, %1, 0, %2" : "=v"(d) : "v"(t), "s"(mC[I - 1])); sc[(I - 1) >> 2][(I - 1) & 3] = d; }
    }
    template <int I> DEVI void op(const Ctx& k, f32x4 (&sc)[3], unsigned a0, unsigned a1, unsigned hv) {
#define FMA(x, y) "v_fmamk_f32 " x ", " x ", 0x3dd53b95, " y "\n\t"
#define EXPC(x) "v_exp_f32_e64 " x ", " x " clamp\n\t"
#define OP3(o, d, x, y) o " " d ", " x ", " y "\n\t"
#define SA(txt) asm volatile(txt : "+v"(a[0]), "+v"(a[1]), "+v"(a[2]), "+v"(a[3]), "+v"(m), "+v"(t))
#define SB(txt) asm volatile(txt : "+v"(a[0]), "+v"(a[1]), "+v"(a[2]), "+v"(a[3]), "+v"(m), "+v"(t), "+v"(s), "+v"(a[4]), "+v"(a[5]), "+v"(M), "+v"(h) : "s"(hs), "s"(sg))
#define ADD(d, x, y) OP3("v_add_f32", d, x, y)
#define MUL(d, x, y) OP3("v_mul_f32", d, x, y)
#define CND(d, x, y, c) "v_cndmask_b32_e64 " d ", " x ", " y ", " c "\n\t"
        if constexpr (I == 0) asm volatile("v_max3_f32 %0, %1, %2, %3\n\tv_max3_f32 %0, %0, %4, %5" : "=&v"(m) : "v"(sc[0][0]), "v"(sc[0][1]), "v"(sc[0][2]), "v"(sc[0][3]), "v"(sc[1][0]));
        if constexpr (I == 1) asm volatile("v_max3_f32 %0, %0, %1, %2" : "+v"(m) : "v"(sc[1][1]), "v"(sc[1][2]));
        if constexpr (I == 2) asm volatile(CND("%0", "%2", "%3", "%6") CND("%1", "%4", "%5", "%6") : "=&v"(a[2]), "=&v"(a[3]) : "v"(sc[0][2]), "v"(sc[1][0]), "v"(sc[0][3]), "v"(sc[1][1]), "s"(mY));
        if constexpr (I == 3) asm volatile("v_max_f32 %1, %1, %5\n\t" CND("%0", "%2", "%3", "%4") : "=&v"(a[0]), "+v"(m) : "v"(sc[0][0]), "v"(a[2]), "s"(mA), "v"(sc[1][3]));
        if constexpr (I == 4) asm volatile("v_mul_f32 %0, 0xbdd53b95, %1" : "=v"(t) : "v"(m));
        if constexpr (I == 5) asm volatile(CND("%0", "%2", "%3", "%5") CND("%1", "%1", "%4", "%5") : "=&v"(a[1]), "+v"(a[2]) : "v"(sc[0][1]), "v"(a[3]), "v"(sc[1][2]), "s"(mA));
        if constexpr (I == 6) asm volatile(CND("%0", "%0", "%2", "%3") FMA("%1", "%4") : "+v"(a[3]), "+v"(a[0]) : "v"(sc[1][3]), "s"(mA), "v"(t));
        if constexpr (I == 7) SA(FMA("%1", "%5"));
        if constexpr (I == 8) SA(FMA("%2", "%5") FMA("%3", "%5"));
        if constexpr (I == 9) SA(EXPC("%0"));
        if constexpr (I == 10) { asm volatile("v_max3_f32 %0, %1, %2, %3" : "=v"(M) : "v"(m), "v"(sc[2][0]), "v"(sc[2][1]), "v"(sc[2])); a[4] = sc[2][0]; a[5] = sc[2][1]; }
        if constexpr (I == 11) asm volatile("v_mov_b32 %0, %3\n\tv_mov_b32 %1, %4" : "=&v"(t), "=&v"(h), "=v"(s) : "v"(M), "s"(hv));
        if constexpr (I == 12) SB("v_permlane16_swap_b32 %9, %5");
        if constexpr (I == 13) SB(OP3("v_max_f32", "%9", "%9", "%5"));
        if constexpr (I == 14) SB(EXPC("%1"));
        if constexpr (I == 15) SB("v_mov_b32 %5, %9\n\t" ADD("%6", "%0", "%1"));
        if constexpr (I == 16) SB("v_permlane32_swap_b32 %9, %5");
        if constexpr (I == 17) SB(OP3("v_max_f32", "%9", "%9", "%5") "v_mul_f32 %9, 0xbdd53b95, %9");
        if constexpr (I == 18) SB(FMA("%7", "%9") FMA("%8", "%9"));
        if constexpr (I == 19) SB(FMA("%4", "%9"));
        if constexpr (I == 20) SB(EXPC("%2"));
        if constexpr (I == 21) SB(EXPC("%3"));
        if constexpr (I == 22) SB(ADD("%6", "%6", "%2"));
        if constexpr (I == 23) SB(EXPC("%7"));
        if constexpr (I == 24) SB(EXPC("%8"));
        if constexpr (I == 25) SB(ADD("%6", "%6", "%3"));
        if constexpr (I == 26) SB(EXPC("%4"));
        if constexpr (I == 27) SB(MUL("%6", "%12", "%6") ADD("%5", "%7", "%8"));
        if constexpr (I == 28) SB(MUL("%6", "%6", "%4"));
        if constexpr (I == 29) SB(OP3("v_fmac_f32", "%6", "%11", "%5"));
        if constexpr (I == 30) SB("v_fmac_f32_dpp %6, %6, %10 row_ror:8 row_mask:0xf bank_mask:0xf\n\tv_mov_b32 %5, %6");
        if constexpr (I == 31) SB("v_permlane16_swap_b32 %6, %5");
        if constexpr (I == 32) SB(ADD("%6", "%6", "%5") "v_mov_b32 %5, %6");
        if constexpr (I == 33) SB("v_permlane32_swap_b32 %6, %5");
        if constexpr (I == 34) SB(ADD("%6", "%6", "%5"));
        if constexpr (I == 35) SB("v_rcp_f32 %6, %6");
        if constexpr (I == 36) SB(MUL("%4", "%4", "%6") MUL("%7", "%7", "%6"));
        if constexpr (I == 37) SB(MUL("%0", "%0", "%4"));
        if constexpr (I == 38) SB(MUL("%1", "%1", "%4") MUL("%2", "%2", "%4"));
        if constexpr (I == 39) { unsigned x; asm volatile(MUL("%1", "%1", "%4") "v_cvt_pk_bf16_f32 %0, %2, %3" : "=&v"(x), "+v"(a[3]) : "v"(a[0]), "v"(a[1]), "v"(m)); pk[0] = x; }
        if constexpr (I == 40) { unsigned x; asm volatile("v_cvt_pk_bf16_f32 %0, %1, %2" : "=v"(x) : "v"(a[2]), "v"(a[3])); pk[1] = x; }
        if constexpr (I == 41) { st64(k.rs, v0, a0, pk); SCHED0(); asm volatile("v_mul_f32 %1, %1, %2\n\tv_cvt_pk_bf16_f32 %0, %0, %1" : "+v"(a[4]), "+v"(a[5]) : "v"(s)); }
        if constexpr (I == 42) st32(k.rs, v1, a1, __float_as_uint(a[4]));
    }
};
#define QIDX(t, b) ((t) * 10 + (b))
DEVI void qk_role(const Params& p, Ctx& k, char* smem, int h) {
    PROF_DECL
    const int w = k.w;
    bf16x16 W[52];
    Gather G;
    Soft S;
    const int hw = h >> 3, hh = h & 7;
    unsigned l16, ix, dqm, dq, trp, qp;
    {
        const int l = k.l, g = l >> 4, n = l & 15, r = n & 7, u = w & 1;
        const bool hf = n >= 8, dup = w != 1;
        S.mA = __ballot(dup && hf);
        const int lim = (w == 0 ? 64 : w == 1 ? n : 16 + 8 * u + r) - 4 * g;
#pragma unroll
        for (int i = 0; i < 8; ++i) S.mC[i] = __ballot((i & 3) + (i >> 2) * 16 <= lim);
        const bool okm = !(dup && hf);
        S.mY = __ballot((dup && hf) == (w != 0));
        S.v0 = w == 0 ? hh * 512 + ((g >> 1) * 16 + (r >> 1) * 4 + hf * 2 + (r & 1)) * 16 + (g & 1) * 8 : w == 1 ? (hh >> 1) * 1024 + l * 16 + (hh & 1) * 8 : hh * 1024 + (g * 16 + 8 * u + r) * 16 + hf * 8;
        S.v1 = !okm ? OOB : w == 0 ? 4096 + hh * 128 + r * 16 + g * 4 : (w == 1 ? 4096 + n * 16 : 6144 + (8 * u + r) * 16) + (hh >> 1) * 512 + (g >> 1) * 256 + (hh & 1) * 8 + (g & 1) * 4;
        l16 = l * 16; ix = SPIX(l);
        const int uu = u ^ ((n >> 2) & 1);
        dqm = L_QM + (w >> 1) * 3072 + uu * 512 + (g >> 1) * 128 + n * 8 + (g & 1) * 4;
        dq = L_QM + 6144 + (w >> 1) * 3072 + uu * 512 + g * 128 + ((n * 8) ^ ((g >> 1) * 64));
        trp = L_QM + 6144 + (l & 3) * 128 + (((l >> 2) * 8) ^ ((l & 2) * 32));
        qp = w == 1 ? trp + 6144 : (w == 0 ? L_QM : L_Q + 6144 + u * 256) + (l & 1) * 128 + (((l >> 2) * 8) ^ (w == 3 ? 64 : 0));
        u32x8 rg = {S.v0, S.v1, l16, ix, dqm, dq, trp, qp};
        asm volatile("" : "+{v[0:7]}"(rg));
        S.v0 = rg[0]; S.v1 = rg[1]; l16 = rg[2]; ix = rg[3]; dqm = rg[4]; dq = rg[5]; trp = rg[6]; qp = rg[7];
    }
    Xn X;
    G3 A, B;
    X.l16 = l16; A.l16 = l16; B.l16 = l16; B.lwv = vadd(l16, L_W + w * 1024);
    {
        const int l = k.l;
        bf16x8 wt[16];
        u32x2 gv;
        unsigned ra[3];
#pragma unroll
        for (int t = 0; t < 3; ++t) ra[t] = opq(wp_adr<384, true>(w * 3 + t, l) + SLOT(0));
#define QEX(v) ((v) < 0 ? 0 : ((v) < 6) + ((v) == QGS) + 10 * ((v) == QGS + 2) + 8 * ((v) == QGS + 7))
#define QKE(v) ((v) < QGS + 4 ? 0 : (v) == 19 ? 20 : CMIN(v, 3 * ((v) - QGS - 3)))
        auto rdx = [&](auto K) { constexpr int ks = CV(K); A.r0<ks>(); A.r1<ks>(); B.x0[ks] = A.x0[ks]; B.x1[ks] = A.x1[ks]; if constexpr (ks < 14 && QKE(ks + 1) <= ks) B.rw<ks>(); };
        cstream<20>([](auto V) { constexpr int v = CV(V); return IC(QEX(v - 1) + (v < 19 ? 6 : 0)); },
            [&](auto U) { constexpr int u = CV(U), b = u >> 1, k0 = QKE(u - 1), n = (QKE(u) - k0) * 12, nn = n ? n : 12;
                if constexpr (u < 6) hello(k, 0);
                if constexpr (u == QGS) gv = go_ask(k);
                if constexpr (u == QGS + 2) { PIN(gv); go_wait(k, 0, gv); X.init(k); G.init(k); X.l16 = k.c ? OOB : l16; xn_all(k, X.l16, X.src, X.dst); }
                if constexpr (u == QGS + 4) rdx(IC(0));
                if constexpr (u == QGS + 7) { sfor<3>([&](auto I) { X.next<CV(I)>(k, l16); G.op<CV(I)>(k, l16, 0); }); X.se = X.src; }
                if constexpr (u == 18) {
                    asm volatile("" :: "{v[152:155]}"(wt[10]), "{v[156:159]}"(wt[11]), "{v[232:235]}"(wt[12]));
#pragma unroll
                    for (int i = 10; i < 13; ++i) lw128(B.lwv + i * 4096, __builtin_bit_cast(u32x4, wt[i]));
                }
                if constexpr (u == 19) { G.wait<0>(); lw128(l16 + G.dst + 2048, G.v0); lw128(l16 + G.dst + 3072, G.v1); }
                XSTAMP(u)
                const unsigned rb[3] = {slot_adr<u>(ra[0]), slot_adr<u>(ra[1]), slot_adr<u>(ra[2])};
                bf16x4 hf[12];
                SCHED0();
                sfor<nn>([&](auto I) { constexpr int i = CV(I), ks = k0 + i / 12, m = i % 12, t = i >> 1, e = u == QGS + 7 ? 8 : 0, j = ((i - e) * 6) / (nn - e);
                    if constexpr (n > 0) { if constexpr (m < 6) A.mm<0, false, ks, m>(W); else B.mm<30, true, ks, m - 6>(W); }
                    if constexpr (i < e) { if constexpr (i % 4 == 0) X.op<i / 4, 0>(k); if constexpr (i & 1) X.op<i / 4, 1 + (i / 2) % 2>(k); else G.op<3 + i / 4 + (i & 2) * 2>(k, l16, 0); }
                    else if constexpr (u > 0 && u < 18 && (i == e || ((i - e - 1) * 6) / (nn - e) != j))
                        dma1<SLOT(u + 2) + (j / 3) * 12288 + (j % 3) * 1024>((j < 3 ? p.Wk : p.Wq) + (size_t)(u + 2) * 32 * 6144 + h * 192, wsrc<384, true>(w * 192 + (j % 3) * 64 + l, 12288), w * 3072);
                    if constexpr (i == 0 && u > 4 && (u < 15 || u >= 18)) lw128(B.lwv + (u - 5) * 4096, __builtin_bit_cast(u32x4, wt[(u - 5) & 15]));
                    if constexpr (i < 12) {
                        hf[i] = lrtr(rb[t % 3] + (t / 3) * 12288 + (i & 1) * 1536);
                        if constexpr (i & 1) {
                            if constexpr (t == 5 && b >= 2) B.wl[u] = wt[(u - 4) & 15] = CAT4(hf[i - 1], hf[i]);
                            else if constexpr (u & 1) W[QIDX(t, b) % 52] = CAT8(LO8(W[QIDX(t, b) % 52]), CAT4(hf[i - 1], hf[i]));
                            else W[QIDX(t, b) % 52] = __builtin_shufflevector(hf[i - 1], hf[i], 0, 1, 2, 3, 4, 5, 6, 7, -1, -1, -1, -1, -1, -1, -1, -1);
                        }
                    }
                    if constexpr (n > 0 && m == 3 && ks < 19) rdx(IC((ks + 1) % 20));
                    SCHED0(); }); });
        lw128(B.lwv + 15 * 4096, __builtin_bit_cast(u32x4, wt[15]));
        static_assert(QKE(17) == 17, "QGS");
        RAWBAR()
        XSTAMP(20)
        const unsigned d0 = (l16 | ((l16 >> 5) & 8)) ^ (L_K + (w >> 1) * 3072 + (w & 1) * 8);
        sfor<24>([&](auto I) { A.out4<CV(I), 0>(d0); });
        G.v0 = G.p0; G.v1 = G.p0; G.bad = 0;
        X.off = 0; X.l16 = l16;
        if (k.c) X.init(k);
    }
    RSTAMP(2)
    const unsigned po0 = k.p0 + PR_B + hw * 49152 + (w == 0 ? 0 : w == 1 ? 32768 : 16384), po1 = k.p0 + PR_B + hw * 49152 + (w == 0 ? 0 : 32768);
    const unsigned pdum = k.pg(1) + DUMMY_B, pbc = w == 0 ? k.p0 + CNT(C_BC + hw * 2, hh * 8) : pdum + 65536 + h * 512 + w * 128;
    unsigned long long now = 0, done = 0;
    unsigned a0 = po0 + (k.c ? pdum - k.p0 - PR_B : 0u), a1 = po1 + (k.c ? pdum - k.p0 - PR_B : 0u);
    const unsigned hv = w == 1 ? 0u : 0x3F800000u, hw2 = w == 1 ? 0u : 0x00800000u;
    S.sg = 0x3F800000u; S.hs = 0x3F800000u - hw2;
    f32x4 ac[6];
#pragma unroll
    for (int t = 0; t < 6; ++t) clr2(ac[t]);
    unsigned an = 120, al = 110;
    __builtin_amdgcn_s_waitcnt(0xc07f);
    do {
        if (X.off != 0) {
            PC8(1, X.sg)
            X.late<2>(k, l16);
            G.check();
        }
_Pragma("unroll 1")
        for (unsigned spins = 0; G.bad != 0 && !spin_fail(k, spins); ++spins) { PC8(0, X.sg) G.again(k, l16); }
        HSTOP(0) HSTAMP(0, 0)
        clr2(ac[5]);
        asm volatile("s_nop 1" : "+v"(ac[5]));
        smfw<50>(ac[5], __builtin_bit_cast(bf16x8, G.p0), W[50], ix);
    do {
        PROF_S(X.sg - 1)
        f32x4 sc[3];
        {
            bf16x8 m[16], wa[10], wb[10], kf[12];
            bf16x4 q[12], km[12];
            unsigned fo[6];
            m[0] = __builtin_bit_cast(bf16x8, G.p0); m[1] = __builtin_bit_cast(bf16x8, G.p1);
            auto put = [&](auto T) { constexpr int t = CV(T); if constexpr (t < 3) lw32(dq + t * 1024, fo[t]); else lw32(dqm + (t - 3) * 1024, fo[t]); };
            auto rd = [&](auto C) { constexpr int c = CV(C), j = c % 20;
                if constexpr (c < 0) { }
                else if constexpr (c < 20) m[j] = lr128(l16 + MADR(j < 10 ? j : j - 8));
                else if constexpr (c < 40) wa[j] = lr128(B.lwv + (j - 2) * 8192);
                else if constexpr (c < 60) wb[j] = lr128(B.lwv + (j - 2) * 8192 + 4096);
                else if constexpr (c < 80) kf[j] = lr128(l16 + L_K + ((j & 1) * 6 + j / 2) * 1024);
                else if constexpr (c < 100) q[j] = lrtr(qp + j * 512);
                else if constexpr (c < 120) km[j] = lrtr(trp + j * 512);
                else if constexpr (c < 140) A.r0<j>();
                else A.r1<j>(); };
#define PB(i) ((i) < 12 ? (i) / 6 : (i) < 32 ? (i) / 4 - 1 : (i) < 44 ? 7 + ((i) - 32) % 3 : (i) < 54 ? 7 + ((i) - 44) / 2 : 12 + ((i) - 54) % 3)
#define PT(i) ((i) < 12 ? ((i) + 5) % 6 : (i) < 32 ? (3 + (i) % 4) % 6 : (i) < 44 ? (3 + ((i) - 32) / 3) % 6 : (i) < 54 ? 1 + ((i) & 1) : 1 + ((i) - 54) / 3)
            sfor<82>([&](auto I) {
                constexpr int i = CV(I) + 1, b = PB(i), t = PT(i), bn = PB(i + 1);
                constexpr int rt[83] = {-1, 2, -1, -1, -1, 22, 42, 3, -1, 23, 43, 4, -1, 24, 44, 5, -1, 25, 45, 6,
                    -1, 26, 46, 7, -1, 8, 9, 27, 47, 28, 48, 29, 49, -1, -1, 60, -1, 61, 62, 63,
                    -1, 10, 64, -1, 11, 12, -1, 13, 14, -1, -1, 80, 82, 83, 84, 85, 86, 87, 67, 88,
                    89, 69, -1, 91, -1, 71, -1, 121, -1, 100, 102, 104, 106, 108, 110, -1, 140, -1, -1, 141, -1, -1, 122};
                constexpr int r2[83] = {-1, -1, -1, -1, -1, -1, -1, -1, -1, -1, -1, -1, -1, -1, -1, -1, -1, -1, -1, -1,
                    -1, -1, -1, -1, -1, -1, -1, -1, -1, -1, -1, -1, -1, -1, -1, -1, -1, -1, -1, -1,
                    -1, -1, -1, -1, -1, -1, -1, -1, -1, -1, -1, 81, -1, 65, -1, 66, -1, -1, -1, -1,
                    68, 90, -1, 70, -1, 120, -1, -1, -1, 101, 103, 105, 107, 109, 111, -1, -1, -1, -1, -1, -1, -1, -1};
                if constexpr (i < 60) {
                    if constexpr (t == 5 && b >= 2) smf<false>(ac[5], m[b], CAT8(wa[b], wb[b]), ix);
                    else smfw<QIDX(t, b < 10 ? b : b - 8) % 52>(ac[t], m[b], W[QIDX(t, b < 10 ? b : b - 8) % 52], ix);
                } else if constexpr (i < 72) { constexpr int j = i - 60; mfa<false, false>(sc[j & 1], kf[j], CAT4(q[j & ~1], q[j | 1])); }
                else if constexpr (i < 77) A.mm<0, false, (i - 72) / 3, (i - 72) % 3>(W);
                else { constexpr int ks = i - 77; mfa<false, ks == 0>(sc[2], CAT4(km[2 * ks], km[2 * ks + 1]), CAT4(q[2 * ks], q[2 * ks + 1])); }
                SCHED0();
                if constexpr (i == 1) { AQ(3, 3) asm volatile("s_barrier" ::: "memory"); AQ(1, 2) AQ(3, 4) }
                else if constexpr (i == 50) { BARL(3); AQ(1, 3) }
                else if constexpr (i == 68) { BARL(3); AQ(1, 4) QSTAMP(0, 1) }
                else if constexpr (i >= 12 && i < 59) { if constexpr (PT(i + 1) == 5) TOUCH(wb[bn % 10]); TOUCH(m[bn]); }
                else if constexpr (i >= 59 && i < 71) { asm volatile("" :: "v"(kf[(i - 59) % 12]), "v"(q[((i - 59) | 1) % 12])); }
                else if constexpr (i == 71) TOUCH(A.x0[0]);
                else if constexpr (i == 74) TOUCH(A.x0[1]);
                else if constexpr (i >= 76 && i < 82) TOUCH(km[(2 * (i - 76) + 1) % 12]);
                if constexpr (i == 8 || i == 30) asm volatile("" :: "{v[60:63]}"(ac[0]), "{v[28:31]}"(ac[1]), "{v[32:35]}"(ac[2]), "{v[64:67]}"(ac[3]), "{v[68:71]}"(ac[4]), "{v[72:75]}"(ac[5]));
                if constexpr (i == 14) AC3(0, 0, 0)
                if constexpr (i == 16) AC3(1, 6, 1)
                if constexpr (i == 18) AC3(2, 7, 2)
                if constexpr (i == 20) AC3(-1, -1, 3)
                if constexpr (i == 22) AC3(-1, -1, 4)
                if constexpr (i == 24) AC3(-1, -1, 5)
                if constexpr (i == 66) AC3(3, -1, 6)
                if constexpr (i == 2) { now = rt_async(); HSTOP(1) }
                if constexpr (i == APOS) { PINS(now); an += (unsigned)now; PINS(an); }
                if constexpr (i == APOS + 1) an = spack(an, X.sg);
                SCHED0();
                rd(IC(rt[i])); rd(IC(r2[i]));
                if constexpr (i == APOS + 4) st32(k.rs, fo[2], pbc, fo[1]);
                if constexpr (i >= 40 && i < 50 && i % 3 == 1) put(IC(((i - 31) / 3) % 6));
                if constexpr (i == 62 || i == 64) put(IC(i / 2 - 30));
                SCHED0();
                if constexpr (i < 25 && i % 4 != 1) B.out<((i - 2) / 4) * 3 + (i - 2) % 4, 6145>(dq, dq);
                if constexpr (i == APOS + 3) { fo[1] = an; PIN(fo[1]); }
                if constexpr (i == APOS - 1) asm volatile("v_and_b32 %0, %1, %2" : "=v"(fo[2]) : "s"(w ? 0u : RMSK), "v"(l16));
                if constexpr (i >= 37 && i < 49) { constexpr int tt = ((i - 28) / 3) % 6; fold_op<(i - 37) % 3>(ac[tt], fo[tt]); }
                if constexpr (i >= 59 && i < 62) fold_op<i - 59>(ac[1], fo[1]);
                if constexpr (i == 62) fold_op<0>(ac[2], fo[2]);
                if constexpr (i == 63) asm volatile("v_add_f32 %1, %1, %2\n\tv_cvt_pk_bf16_f32 %0, %3, %1" : "=v"(fo[2]), "+v"(ac[2][1]) : "v"(ac[2][3]), "v"(ac[2][0]));
                if constexpr (i == 33) S.mask<0>(sc);
                if constexpr (i >= 51 && i < 59) S.mask<i - 50>(sc);
                if constexpr (i >= 75) S.op<i - 75>(k, sc, a0, a1, hv);
                if constexpr (i == 12) { AQ(3, 5) }
                if constexpr (i == 43) { AQ(3, 6) }
                if constexpr (i == 59) { AQ(3, 7) }
                SCHED0();
            });
        }
        QSTAMP(0, 2)
        {
            unsigned d0;
            auto rest = [&](auto J) { constexpr int j = CV(J);
                if constexpr (j >= 0 && j < 12 && (j % 6 == 0 || j % 3 == 2)) X.op<3 + j / 6, (j % 6 + 1) / 3>(k); };
            sfor<43>([&](auto F) { constexpr int f = CV(F) + 5;
                constexpr int e = f < 6 ? f + 3 : f < 9 ? f - 3 : f;
                A.mm<0, false, e / 6, e % 6>(W);
                if constexpr (f % 6 == 2 || f == 5) TOUCH(A.x1[f / 6]);
                else if constexpr (f == 47) { TOUCH(B.x0[0]); AQ(1, 6) AQ(2, 1) }
                else if constexpr (f % 6 == 5) TOUCH(A.x0[f / 6 + 1]);
                if constexpr (f == 40) { done = rt_async(); QSTAMP(0, 3) AQ(1, 5) }
                if constexpr (f == 21) set_m0(X.dst + 2 * 8192);
                if constexpr (f == 24) { X.sb = X.se + 2 * 16384; PINS(X.sb); }
                if constexpr (f == 42) AC3(4, -1, 7)
                SCHED0();
                if constexpr (f % 6 == 4 && f < 36) A.r0<f / 6 + 2>();
                if constexpr (f % 6 == 1 && f < 42) A.r1<f / 6 + 1>();
                if constexpr (f >= 39 && f < 43) B.rd<true, ((f - 39) / 2) * 6 + (f - 39) % 2>();
                if constexpr (f == 26 || f == 31) X.op<2, f / 15>(k);
                SCHED0();
                S.op<f + 3>(k, sc, a0, a1, hv);
                rest(IC(f - 43));
                if constexpr (f == 44) B.lwv = vadd(l16, L_W + w * 1024);
                SCHED0(); });
            B.go<30, true, 0, 48, 8>(W, [&](auto F) { constexpr int f = CV(F);
                rest(IC(f + 5));
                if constexpr (f == 9) AC3(5, -1, -1)
                if constexpr (f == 2) { a0 = (X.sg & 3) * PR_SZ; PINS(a0); }
                if constexpr (f == 4) { a1 = a0 + po1; a0 += po0; PINS(a0); PINS(a1); }
                if constexpr (f == 8) { S.sg = (X.sg & 4) ? 0xBF800000u : 0x3F800000u; PINS(S.sg); }
                if constexpr (f == 10) { S.hs = S.sg - hw2; PINS(S.hs); }
                if constexpr (f == 14 || f == 16) G.op<f / 16>(k, l16, X.sg);
                if constexpr (f == 22 || f == 28 || f == 32) X.next<(f - 22) / 5>(k, l16);
                if constexpr (f == 35) asm volatile("s_waitcnt vmcnt(6)\n\ts_barrier" ::: "memory");
                if constexpr (f == 20) AC3(6, 1, -1)
                if constexpr (f == 26) A.home<0>();
                if constexpr (f >= 36) A.rd<false, f + 12>();
                if constexpr (f == 38 || f == 40 || f == 45 || f == 46) X.next<(f == 38 ? 5 : f == 40 ? 6 : f - 42)>(k, l16);
                if constexpr (f == 47) { A.pre<false, 8>(); AQ(1, 7) AQ(2, 2) } });
            A.go<0, false, 48, 120, 20>(W, [&](auto F) { constexpr int f = CV(F), i = f - XPOS;
                if constexpr (f == 56) B.home<1>();
                if constexpr (i == 0 || i == 2 || i == 6) X.next<7 + i / 2>(k, l16);
                if constexpr (i >= 3 && i < 15 && ((i - 3) % 6 == 0 || (i - 3) % 3 == 2)) X.op<(i - 3) / 6, ((i - 3) % 6 + 1) / 3>(k);
                if constexpr (f == 58) asm volatile("s_waitcnt vmcnt(1)\n\ts_barrier" ::: "memory");
                if constexpr (f == 57) AC3(7, 2, -1)
                G.ask<f - RPOS>(k, l16);
                if constexpr (f >= 108) B.rd<true, f - 60>();
                SCHED0();
                G.red<f - 110>();
                if constexpr (f == 116) asm volatile("v_bfe_u32 %0, %1, 8, 1\n\tv_lshl_or_b32 %0, %0, 3, %1" : "=&v"(d0) : "v"(l16));
                if constexpr (f == 118) asm volatile("v_xor_b32 %0, %1, %0" : "+v"(d0) : "s"(L_K + (w >> 1) * 3072 + (w & 1) * 8));
                if constexpr (f == 119) { B.pre<true, 8>(); AQ(2, 3) } });
            B.go<30, true, 48, 120, 20>(W, [&](auto F) { constexpr int f = CV(F), i = f - 48;
                constexpr int ko[26] = {0, 1, 2, 4, 3, 6, 5, 8, 7, 9, 10, 11, 12, 13, 14, 16, 15, 18, 17, 20, 19, 21, 22, 23, -1, -1};
                G.hold<f - GPOS + 5>(X.sg);
                if constexpr (i < 24) { SCHED0(); A.out4<ko[i % 26], 0>(d0); }
                G.at<f - GPOS>(k, l16);
                if constexpr (f == 114) { lgkm0(done); PINS(G.rt); }
                if constexpr (f == 115) { PINS(now); HTRACE(X.sg - 2) an = (unsigned)done - (unsigned)now; PINS(an); }
                if constexpr (f == 116) { an = min(an, al); PINS(an); }
                if constexpr (f == 117) { an += DQ; PINS(an); }
                if constexpr (f == 118) { al = an; PINS(al); }
                SCHED0();
                if constexpr (i == 28) AC3(-1, 3, -1)
                if constexpr (i == 50) AC3(-1, 4, -1)
                if constexpr (i == 62) AC3(-1, 5, -1)
                if constexpr (i == 23) { AQ(2, 4) }
                if constexpr (i == 47) { AQ(2, 5) }
                if constexpr (i == 65) { AQ(2, 6) }
                if constexpr (f == 119) { AQ(2, 7) }
                if constexpr (f == 112) asm volatile("" :: "{v[12:15]}"(G.v0), "{v[16:19]}"(G.v1), "{v[20:23]}"(G.p0), "{v[24:27]}"(G.p1), "{v9}"(B.lwv) TRHINT);
                if constexpr (f == 119) G.also(X.off);
                if constexpr (f >= 113 && f < 119) clr2(ac[(f + 6 - 113 + 5) % 6]); });
            static_assert(GPOS >= 70 && GPOS <= 98 && XPOS % 6 == 2 && RPOS > XPOS && RPOS + 1 < XPOS + 5, "GPOS");
            QSTAMP(0, 4) QSTAMP(0, 5)
            HFLUSH(s)
        }
        AST(0)
        PROF_ONLY(G.wait<0>();)
        HSTOP(0) HSTAMP(s, 0) AQ(1, 1) AQ(3, 1)
        G.all();
        SCHED0();
        smfw<50>(ac[5], __builtin_bit_cast(bf16x8, G.p0), W[50], ix);
        AQ(3, 2)
        SCHED0();
    } while (G.bad == 0);
    } while (X.sg != 65u);
    PROF_OUT
}

#define OIDX(t, b) (22 + (t) * 3 + (b))
DEVI void vo_role(const Params& p, Ctx& k, char* smem, int h) {
    PROF_DECL
    const int w = k.w;
    bf16x16 W[52];
    unsigned l16, lwv, ix, dq0, trp, vo, vr;
    {
        const int l = k.l, g = l >> 4, n = l & 15, q = (l >> 2) & 3, pp = l & 3;
        l16 = l * 16; lwv = l16 + L_W + w * 1024; ix = SPIX(l);
        dq0 = L_QM + (24 * w + (n >> 2) * 2 + (n & 1)) * 32 + g * 8 + ((n >> 1) & 1) * 4;
        trp = L_QM + (16 * (g >> 1) + 4 * (g & 1) + 8 * (q >> 1) + (q & 1)) * 32 + pp * 8;
        vo = EXP(h * 512) + (g >> 1) * 256 + n * 16 + (g & 1) * 8;
        u32x8 r = {dq0, trp, vo, ix, l16, lwv, 0u, 0u};
        PIN(r);
        dq0 = r[0]; trp = r[1]; vo = r[2]; ix = r[3]; l16 = r[4]; lwv = r[5];
    }
    Gather G;
    G3 V;
    Xn X;
    V.l16 = l16; V.lwv = lwv; X.l16 = l16;
    f32x4 y[20];
    bf16x8 v[12];
    u32x4 o[10];
    unsigned yb;
    {
        const int l = k.l;
        bf16x8 wt[16];
        bf16x4 q[10][4];
        f32x4 z[9][2];
        u32x2 gv;
        unsigned ra[6];
        const u16* wo = p.Wo + (size_t)h * 192 * 640;
        constexpr int L_V = L_XN + 24576;
#define VEX(v) ((v) < 0 ? 0 : ((v) < 6) + ((v) == VGS) + 10 * ((v) == VGS + 2) + 6 * ((v) == VGS + 6 || (v) == 18))
#define VKE(v) ((v) < VGS + 4 ? 0 : CMIN(20, 7 * ((v) - VGS - 3)))
#define YKE(v) ((v) < 16 ? 0 : 3 * (v) - 45)
        auto put = [&](int t) { st128(k.rs, X.l16, yb + t * 65536, (u32x4){cvtpk(z[t][0][0], z[t][0][1]), cvtpk(z[t][0][2], z[t][0][3]), cvtpk(z[t][1][0], z[t][1][1]), cvtpk(z[t][1][2], z[t][1][3])}); };
        auto rdx = [&](auto K) { constexpr int ks = CV(K); if constexpr (ks < 20) { V.r0<ks % 20>(); V.r1<ks % 20>(); if constexpr (ks < 14) V.rw<ks>(); } };
        auto rdv = [&](auto J) { constexpr int j = CV(J) % 6; v[j] = lr128(l16 + L_V + j * 1024); v[6 + j] = lr128(l16 + L_V + (6 + j) * 1024); };
        cstream<20>([](auto V) { constexpr int v = CV(V); return IC(VEX(v - 1) + (v < 19 ? 6 : 0)); },
            [&](auto U) { constexpr int u = CV(U), k0 = VKE(u - 1), y0 = YKE(u - 1), n = u < 16 ? (VKE(u) - k0) * 6 : u < 18 ? 36 : 18, nn = n ? n : 12, nr = u < 10 ? 3 : u < 16 ? 6 : 4;
                if constexpr (u < 6) hello(k, 0);
                if constexpr (u == VGS) gv = go_ask(k);
                if constexpr (u == VGS + 2) { PIN(gv); go_wait(k, 0, gv); X.init(k); X.l16 = k.c ? OOB : l16; xn_all(k, X.l16, X.src, X.dst); }
                if constexpr (u == VGS + 4) { rdx(IC(0)); rdx(IC(1)); }
                if constexpr (u == VGS + 6) { sfor<3>([&](auto I) { X.next<CV(I)>(k, l16); }); X.se = X.src; yb = k.yx(0, w) + EXP(h * 1024); }
                if constexpr (u == 16) rdv(IC(0));
                XSTAMP(u)
                if constexpr (u == 0 || u == 10 || u == 16) {
#pragma unroll
                    for (int t = 0; t < nr; ++t) ra[t] = opq((u == 0 ? wp_adr<384, true>(w * 3 + t, l) : u == 10 ? wp_adr<768, false>(w * 6 + t, l) : wp_adr<512, false>(w * 4 + t, l)) + SLOT(0));
                }
                unsigned rb[6];
#pragma unroll
                for (int t = 0; t < nr; ++t) rb[t] = slot_adr<u>(ra[t]);
                SCHED0();
                sfor<nn>([&](auto I) { constexpr int i = CV(I), e = u == VGS + 6 ? 9 : 0, j = ((i - e) * 6) / (nn - e), d = u + 2, r0 = u == 18 ? 6 : 0;
                    if constexpr (u < 16 && n > 0) { V.mm<0, true, k0 + i / 6, i % 6>(W); if constexpr (i % 6 == 3) rdx(IC(k0 + i / 6 + 2)); }
                    if constexpr (u >= 16) { constexpr int m = n / 3, ks = (y0 + i / m) % 6, t = (u / 18) * 6 + (i % m) / 2, rt = i & 1;
                        if constexpr (!(ks == 5 && t == 7 && rt)) { mfw<OIDX(t, ks >> 1), ks & 1, true, ks == 0>(z[t][rt], W[OIDX(t, ks >> 1)], v[rt * 6 + ks]); SCHED0(); }
                        if constexpr (i % m == 1 && u < 19) rdv(IC(ks + 1));
                        if constexpr (u == 19 && i < 2) rdv(IC(4 + i));
                        if constexpr (u == 18 && i >= 3 && i < 15 && (i & 1)) put((i - 3) / 2);
                        if constexpr (u == 18 && i < 6) lw128(lwv + (10 + i) * 4096, __builtin_bit_cast(u32x4, wt[10 + i])); }
                    if constexpr (i < e) X.op<i / 3, i % 3>(k);
                    else if constexpr (u > 0 && u < 18 && (i == e || ((i - e - 1) * 6) / (nn - e) != j)) {
                        if constexpr (d < 10) dma1<SLOT(d) + (j / 3) * 12288 + (j % 3) * 1024>(p.Wv + (size_t)(2 * d + j / 3) * 32 * 6144 + h * 192, wsrc<384, true>(w * 192 + (j % 3) * 64 + l, 12288), w * 3072);
                        else if constexpr (d < 16) dma1<SLOT(d) + j * 1024>(wo + (d - 10) * 32 * 640, wsrc<768, false>(w * 384 + j * 64 + l, 1280), w * 6144);
                        else dma1<SLOT(d) + j * 1024>(wo + (d - 16) * 48 * 640 + 384, wsrc<512, false>(w * 384 + j * 64 + l, 1280), w * 6144);
                    }
                    if constexpr (i < 2 && u >= 3 && u < 8) lw128(lwv + (2 * u - 6 + i) * 4096, __builtin_bit_cast(u32x4, wt[(2 * u - 6 + i) & 15]));
                    sfor<2>([&](auto C) { constexpr int c = u == 19 ? 2 * i + CV(C) : i - r0 + CV(C) * 99;
                        if constexpr (c >= 0 && c < 12) {
                            constexpr int t = u < 10 ? c >> 2 : u < 16 ? c >> 1 : 6 + c % 4, r = u < 10 ? c & 3 : u < 16 ? (u - 10) * 2 + (c & 1) : (u - 16) * 3 + c / 4, id = u < 10 ? (t * 10 + u) % 52 : OIDX(t, r >> 2);
                            q[t][r & 3] = lrtr(u < 10 ? rb[t] + (r >> 1) * 12288 + (r & 1) * 1536 : u < 16 ? rb[t] + (c & 1) * 12288 : rb[(t - 6) & 3] + (c / 4) * 8192);
                            if constexpr (u < 10 && u >= 2 && t == 2) { if constexpr (r & 1) V.wl[2 * u + r / 2] = wt[(2 * u - 4 + r / 2) & 15] = CAT4(q[t][r - 1], q[t][r]); }
                            else if constexpr ((r & 3) == 1) W[id] = __builtin_shufflevector(q[t][0], q[t][1], 0, 1, 2, 3, 4, 5, 6, 7, -1, -1, -1, -1, -1, -1, -1, -1);
                            else if constexpr ((r & 3) == 3) W[id] = CAT8(LO8(W[id]), CAT4(q[t][2], q[t][3]));
                        } });
                    SCHED0(); });
                if constexpr (u == VGS + 7) sfor<18>([&](auto I) { V.out<CV(I), 0>(l16 + L_V + ((3 * w) >> 1) * 1024 + ((3 * w) & 1) * 8, l16 + L_V + ((3 * w + 1) >> 1) * 1024 + ((3 * w + 1) & 1) * 8); }); });
        static_assert(VGS >= 6 && VGS < 9, "VGS");
        RAWBAR()
        XSTAMP(20)
        sfor<2>([&](auto B) { X.op<CV(B) + 3, 0>(k); asm volatile("s_nop 0"); X.op<CV(B) + 3, 1>(k); X.op<CV(B) + 3, 2>(k); });
        asm volatile("" : "+v"(z[8][0]), "+v"(z[8][1]), "+v"(z[7][0]), "+v"(z[6][1]));
        put(6); put(8);
        lw32(l16 + L_MEM, ~0u);
        o[7] = (u32x4){cvtpk(z[7][0][0], z[7][0][1]), cvtpk(z[7][0][2], z[7][0][3]), 0u, 0u};
        y[17] = z[7][1];
        yb += 7 * 65536;
        X.off = 0; X.l16 = l16;
        if (k.c) X.init(k);
    }
    RSTAMP(2)
    const unsigned pdum = k.pg(1) + DUMMY_B, pbc = w == 0 ? k.p0 + CNT(C_BC + (h >> 3) * 2 + 1, (h & 7) * 8) : pdum + 98304 + h * 512 + w * 128;
    unsigned long long now = 0, done = 0;
    G.mx = ~0u;
    G.init(k);
    G.base -= 32768;
    const unsigned ypar = (unsigned)(k.par >> (PG_YX + (w >> 1))), yo = ((unsigned)(PG_YX + (w >> 1)) << 21) + (w & 1) * 1048576 + EXP(h * 1024);
    const unsigned ym0 = k.ym(0) + w * 163840, ym2 = k.ym(2) + w * 163840;
    unsigned ym = k.c ? pdum + w * 163840 : ym0, ys, t, c = ~0u, an = 85, al = 90;
#define YT(u) ((u) < 10 ? 2 * ((u) % 5) : ((u) - 10) | 1)
#define YR(u) ((u) < 10 ? (u) / 5 : (u) & 1)
    auto yout = [&](auto U, auto J) { constexpr int u = CV(U), j = CV(J), t = YT(u), rt = YR(u);
        if constexpr (j == 0) { o[t][2 * rt] = cvtpk(y[u][0], y[u][1]); o[t][2 * rt + 1] = cvtpk(y[u][2], y[u][3]); TOUCH(o[t][2 * rt]); TOUCH(o[t][2 * rt + 1]); }
        if constexpr (j == 1 && rt) __builtin_amdgcn_raw_buffer_store_b128(o[t], k.rs, (int)l16, (int)yb, AUX_SC1);
        if constexpr (j == 2 && rt) { yb += t == 8 ? -7 * 65536 : 131072; PINS(yb); TOUCH(o[t]); } };
    auto ymm = [&](auto I) {
        constexpr int i = CV(I), ks = i % 6, u = i / 6, id = OIDX(YT(u), ks >> 1);
        mfw<id, ks & 1, true, ks == 0>(y[u], W[id], v[YR(u) * 6 + ks]);
        SCHED0();
        if constexpr (u > 0 && ks >= 2) yout(IC((u + 19) % 20), IC(ks - 2)); };
    auto mkr = [&]() { asm volatile("v_and_b32 %0, %1, %2" : "=v"(vr) : "s"(w ? 0u : RMSK), "v"(l16)); };
    if (k.c) X.late<5>(k, l16);
    mkr();
    do {
        PROF_S(X.sg - 1)
        f32x4 ac[3];
        sfor<13>([&](auto I) { constexpr int i = CV(I) + 107;
            ymm(IC(i));
            if constexpr (i == TB) { asm volatile("s_waitcnt vmcnt(%0)\n\ts_barrier" :: "n"(TB < 111 ? 7 : 8) : "memory"); AST(5) }
            if constexpr (i == TB + 1) st32(k.rs, vr, pbc + 4, G.mx);
            if constexpr (i == 119) V.pre<true, 0>();
            if constexpr (i == 113) G.ask<0>(k, l16);
            if constexpr (i == 119) G.ask<1>(k, l16);
            if constexpr (i == 114) V.r0<0>();
            if constexpr (i == 115) V.r1<0>();
            if constexpr (i == 117) V.r0<1>();
            if constexpr (i == 118) V.r1<1>();
            SCHED0(); });
        HSTAMP(s, 4)
        V.run<0, true, 0, 16, 17, true>(W, [&](auto I) { constexpr int i = CV(I);
            if constexpr (i >= 2) yout(IC(19), IC(i - 2));
            if constexpr (i == 8) { t = (X.sg & 7) * 2; PINS(t); }
            if constexpr (i == 10) { yb = (ypar >> t) & 1; PINS(yb); }
            if constexpr (i == 14) { t = (t << 21) + yo; PINS(t); }
            if constexpr (i == 16) { yb = (yb << 13) + t; PINS(yb); }
            if constexpr (i == 20) { c = X.sg; PINS(c); }
            if constexpr (i == 22) { G.a = max(c, 1u) << 15; PINS(G.a); }
            if constexpr (i == 28) G.op<1>(k, l16, 0);
            if constexpr (i == 40) { an = min(an, al) + DV; PINS(an); }
            if constexpr (i == 44) { al = an; PINS(al); }
            if constexpr (i == 32 || i == 34 || i == 38) X.next<(i - 31) / 3>(k, l16);
            if constexpr (i == 23 || i == 29) { AAC(4 + i / 29) }
            if constexpr (i == 83 || i == 89) { AAC(6 + i / 89) }
            if constexpr (i == YPOS) { AST(6) }
            if constexpr (i == YPOS + 1) { AST(7) }
            if constexpr (i == YPOS) { asm volatile("s_waitcnt vmcnt(0)\n\ts_barrier" ::: "memory"); }
            if constexpr (i == YPOS - 2) { G.mx = vshl1(c, 0); mkr(); }
            if constexpr (i == YPOS + 1) st32(k.rs, vr, pbc + 4, G.mx);
            SCHED0();
            G.red<i - YPOS - 2>();
            G.hold<i - GPOSY * 6 + 5>(c);
            G.at<i - GPOSY * 6>(k, l16);
            if constexpr (i == 90) PINS(G.rt);
            if constexpr (i == 95) V.pre<true, 16>();
            if constexpr (i >= 92 && i < 95) clr2(ac[(i - 92) % 3]); });
        static_assert(GPOSY * 6 + 2 > YPOS + 1 && GPOSY <= 13, "GPOSY");
        HSTAMP(s, 5)
        HFLUSH(s - 1)
        f32x4 acc[10];
        f32x2 z[20];
        auto clr = [&](auto I) { constexpr int i = CV(I); asm volatile("v_mov_b64 %0, 0" : "=v"(z[i])); if constexpr (i & 1) acc[i >> 1] = __builtin_shufflevector(z[i - 1], z[i], 0, 1, 2, 3); };
        bf16x4 al[3], ah[3];
        unsigned d0, d1;
        {
            bf16x8 m[10], wa[10], wb[10];
            auto rd = [&](auto R) { constexpr int r = CV(R), b = 2 + r / 3;
                if constexpr (b < 10) {
                    if constexpr (r % 3 == 0) m[b] = lr128(l16 + MADR(b));
                    if constexpr (r % 3 == 1) wa[b] = lr128(lwv + (b - 2) * 8192);
                    if constexpr (r % 3 == 2) wb[b] = lr128(lwv + (b - 2) * 8192 + 4096);
                } else if constexpr (b < 13) {
                    if constexpr (r % 3 == 0) V.r0<b + 7>();
                    if constexpr (r % 3 == 1) V.rw<b + 7>();
                    if constexpr (r % 3 == 2) V.r1<b + 7>();
                } };
            AST(0)
            G.wait<0>();
            HSTOP(0) HSTAMP(s, 0) AST(1)
            V.run<0, true, 16, 17, 0, true>(W, [&](auto I) { constexpr int i = CV(I);
                G.fin<i>(0);
                if constexpr (i == 3) {
                    if (__builtin_expect(G.bad != 0, 0)) {
                        unsigned spins = 0;
_Pragma("unroll 1")
                        do { PC8(0, c) G.again(k, l16); } while (G.bad != 0 && !spin_fail(k, spins++));
                        HSTOP(0) HSTAMP(s, 0)
                    }
                    asm volatile("s_barrier" ::: "memory");
                    AST(2)
                }
                if constexpr (i == 4) { now = rt_async(); HSTOP(1) SCHED0(); }
                if constexpr (i >= 3) rd(IC(i - 3)); });
            m[0] = __builtin_bit_cast(bf16x8, G.p0); m[1] = __builtin_bit_cast(bf16x8, G.p1);
            sfor<30>([&](auto I) {
                constexpr int i = CV(I), b = i / 3, t = i % 3;
                if constexpr (t == 2 && b >= 2) smf<false>(ac[2], m[b], CAT8(wa[b], wb[b]), ix); else smfw<t * 10 + b>(ac[t], m[b], W[t * 10 + b], ix);
                SCHED0();
                if constexpr (i >= 6 && i < 29 && t != 0) { if constexpr (t == 1) TOUCH(wb[b]); else TOUCH(m[(b + 1) % 10]); }
                if constexpr (i == 29) V.pre<true, 17>();
                if constexpr (i == 2) { PINS(now); an += (unsigned)now; PINS(an); }
                if constexpr (i == 3) an = spack(an, c);
                SCHED0();
                if constexpr (i < 27) rd(IC(3 + i));
                if constexpr (i == 5) st32(k.rs, vr, pbc, d0);
                SCHED0();
                if constexpr (i == 4) { d0 = an; PIN(d0); }
                if constexpr (i == 3) mkr();
                if constexpr (i == 28) d0 = vadd(l16, L_K + ((3 * w) >> 1) * 1024 + ((3 * w) & 1) * 8);
                if constexpr (i == 29) d1 = vadd(l16, L_K + ((3 * w + 1) >> 1) * 1024 + ((3 * w + 1) & 1) * 8);
                SCHED0();
            });
            unsigned fo[3];
            V.run<0, true, 17, 19, 0, true>(W, [&](auto I) { constexpr int i = CV(I);
                if constexpr (i < 3) { rd(IC(30 + i)); SCHED0(); fold_op<0>(ac[i], fo[i]); }
                if constexpr (i == 3 || i == 4 || i == 6) { constexpr int t = i / 2 - 1; asm volatile("v_add_f32 %1, %1, %2\n\tv_cvt_pk_bf16_f32 %0, %3, %1" : "=v"(fo[t]), "+v"(ac[t][1]) : "v"(ac[t][3]), "v"(ac[t][0])); }
                if constexpr (i == 5 || i == 7 || i == 8) lw32(dq0 + ((i - 4) / 2) * 256, fo[(i - 4) / 2]);
                if constexpr (i == 9 || i == 10) clr2(acc[i - 9]);
                if constexpr (i == 11) BARL(0) });
        }
        HSTAMP(s, 1) AST(3)
        {
            unsigned fo[2];
            auto xq = [&](auto I) { constexpr int i = CV(I) - XP + XS0;
                if constexpr (i == -4) X.next<3>(k, l16);
                if constexpr (i == -1 || i == 0) X.next<14 + i>(k, l16); };
            static_assert(XP % 6 == 5 && XP <= 53 && XP >= 17 && XB > XP + 18 && XB > 63, "XP");
            auto fin2 = [&](auto P, auto J) { fold2<CV(J)>(acc[2 * CV(P)], acc[2 * CV(P) + 1], fo); };
            V.run<0, true, 19, 20, 0, true>(W, [&](auto I) { constexpr int i = CV(I);
                if constexpr (i == 0) { al[0] = lrtr(trp); ah[0] = lrtr(trp + 64); }
                if constexpr (i == 1) al[1] = lrtr(trp + 1024);
                if constexpr (i == 2) ah[1] = lrtr(trp + 1088);
                if constexpr (i == 3) al[2] = lrtr(trp + 2048);
                if constexpr (i == 4) ah[2] = lrtr(trp + 2112);
                SCHED0();
                if constexpr (i >= 1) clr(IC(3 + i)); });
            sfor<30>([&](auto I) { constexpr int i = CV(I), b = (i % 6) >> 1, t = 2 * (i / 6) + (i & 1), id = OIDX(t, b);
                smfw<id>(acc[t], CAT4(al[b], ah[b]), W[id], ix);
                SCHED0();
                if constexpr (i == 1 || i == 3) TOUCH(ah[(i + 1) / 2]);
                if constexpr (i == 29) TOUCH(v[0]);
                if constexpr (i == 21) BARL(0)
                SCHED0();
                if constexpr (i == 4 || i == 7) X.next<(i == 4 ? 5 : 11)>(k, l16);
                xq(IC(i - 30));
                if constexpr (i >= 21 && i < 27) v[i - 21] = lr128(l16 + L_K + (i - 21) * 1024);
                SCHED0();
                V.out<i - 1, 0>(d0, d1);
                SCHED0();
                if constexpr (i % 3 == 0 && i < 18) clr(IC(9 + i / 3));
                if constexpr (i == 16) clr(IC(15));
                if constexpr (i >= 18 && i < 21) clr(IC(i - 2));
                if constexpr (i == 22) clr(IC(19));
                if constexpr (i >= 23 && i < 29) fold_op<(i - 23) / 2>(acc[(i - 23) & 1], fo[(i - 23) & 1]);
                if constexpr (i == 29) { st64(k.rs, vo, ym, (u32x2){fo[0], fo[1]}); SCHED0(); fin2(IC(1), IC(0)); }
                SCHED0(); });
            HSTAMP(s, 2)
            sfor<107>([&](auto I) { constexpr int i = CV(I), q = i < 8 ? i + 4 : i + 3;
                ymm(I);
                if constexpr (i < 5 || (i >= 29 && i < 35)) TOUCH(v[(i < 5 ? i + 1 : i - 23) % 12]);
                if constexpr (i < 13 && i != 8) {
                    if constexpr (q % 3 == 1) { ym += 32768; PINS(ym); }
                    if constexpr (q % 3 == 2 && i >= 5) __builtin_amdgcn_s_setprio(0);
                    SCHED0();
                    if constexpr (q % 3 == 0) { st64(k.rs, vo, ym, (u32x2){fo[0], fo[1]}); SCHED0(); }
                    if constexpr (q < 15) fin2(IC(q / 3), IC(q % 3));
                }
                if constexpr (i == 13) done = rt_async();
                if constexpr (i >= 15 && i < 23 && i != 18 && i != 20) v[6 + i - 15 - (i > 18) - (i > 20)] = lr128(l16 + L_K + (6 + i - 15 - (i > 18) - (i > 20)) * 1024);
                xq(IC(i));
                if constexpr (i == XP - 6) X.next<15>(k, l16);
                if constexpr (i == XP - 1) { SCHED0(); X.next<7>(k, l16); }
                if constexpr (i == XP) X.next<12>(k, l16);
                if constexpr (i > XP && i <= XP + 18 && (i % 6 == 0 || i % 3 == 1)) X.op<(i - XP - 1) / 6, (i % 6 + 2) / 3>(k);
                { constexpr int xp[6] = {XB, XB + 1, XB + 6, XB + 11, XB + 12, XB + 18};
                  sfor<6>([&](auto Q) { if constexpr (xp[CV(Q)] == i) X.op<3 + CV(Q) / 3, CV(Q) % 3>(k); }); }
                if constexpr (i == 90) { ym = (c & 2) ? ym2 : ym0; PINS(ym); }
                if constexpr (i == 91) { ys = ym + EXP(327680); PINS(ys); }
                if constexpr (i == 96) { ym = (c & 1) ? ys : ym; PINS(ym); }
                if constexpr (i == 60) { HSTAMP(s, 3) }
                if constexpr (i == 42 || i == 43 || i == 48 || i == 49) { AAC((i - 42) / 3 + (i & 1)) }
                if constexpr (i == 102) lgkm0(done);
                if constexpr (i == 103) { PINS(now); HTRACE(c - 1) an = (unsigned)done - (unsigned)now; PINS(an); }
                if constexpr (i == 105) { G.mx = vshl1(c, 1); mkr(); }
                SCHED0(); });
            AST(4)
        }
    } while (X.sg != 65u);
    HFLUSH(63)
    PROF_OUT
}

DEVI void row_role(const Params& p, const Ctx& k, char* smem, int rt) {
    PROF_DECL
    const int l = k.l, n = l & 15;
    const unsigned fo = k.w * 2048 + rt * 1024;
    RSTAMP(2)
    const __amdgpu_buffer_rsrc_t rs = __builtin_amdgcn_make_buffer_rsrc(k.c ? (void*)k.ws : (void*)p.x, 0, 0x7fffffff, 0x00020000);
    const unsigned vs = k.c ? l * 16 : n * 1280 + (l >> 4) * 16, cs = k.c ? 16384 : 256;
    unsigned ep = n * 32, dp = (l >> 4) == (n >> 2) ? n * 32 + k.w * 8 : 1024 + l * 8;
    const unsigned long long m1 = __ballot(n & 1), m2 = __ballot(n & 2);
    bf16x8 one8 = {0x3F80, 0x3F80, 0x3F80, 0x3F80, 0x3F80, 0x3F80, 0x3F80, 0x3F80};
    PIN(one8);
    unsigned fails = k.c ? 0u : 65537u;
    prog_init(k);
    for (int sx = k.c ? -1 : 8; sx < 64; ++sx) {
        const int sg = max(sx, 0);
        unsigned src[5], dst = k.seg(PG_XNA, sg) + fo;
#pragma unroll
        for (int c = 0; c < 5; ++c) { src[c] = (k.c ? k.seg(PG_X2, sg) + fo : (sg * 32 + rt * 16) * 1280 + k.w * 64) + c * cs; PINS(src[c]); }
        PINS(dst);
        prog_wait<5>(k, k.p0 + CNT(C_X2, rt * 5), k.c ? sx : -1);
        STOP(sg) STAMP(sg, 0)
        u32x4 a[5];
        f32x4 sm, sq;
        f32x2 v[20];
        unsigned long long bad;
        do {
            asm volatile("" ::: "memory");
#pragma unroll
            for (int c = 0; c < 5; ++c) { a[c] = ld128(rs, vs, src[c]); SCHED0(); }
            unsigned mx = 0, lim = ~1u | ((fails - 1) >> 16);
            ++fails; PINS(fails); PINS(lim);
            sfor<5>([&](auto C) { constexpr int c = CV(C);
                mfa<false, c == 0>(sq, __builtin_bit_cast(bf16x8, a[c]), __builtin_bit_cast(bf16x8, a[c])); mfa<false, c == 0>(sm, one8, __builtin_bit_cast(bf16x8, a[c]));
                mx = umax3(umax3(mx, a[c][0], a[c][1]), a[c][2], a[c][3]); PIN(mx);
                SCHED0(); });
            bad = __ballot(mx > lim);
        } while (__builtin_expect(bad != 0, 0));
        STAMP(sg, 1)
        auto unp = [&](int i0, int i1) {
#pragma unroll
            for (int i = i0; i < i1; ++i) { v[i] = (f32x2){blo(a[i >> 2][i & 3]), bhi(a[i >> 2][i & 3])}; PIN(v[i]); }
            SCHED0(); };
        unp(0, 3);
        asm volatile("" : "+v"(sq), "+v"(sm));
        lw64(dp, (u32x2){__float_as_uint(sm[0]), __float_as_uint(csel(csel(sq[0], sq[1], m1), csel(sq[2], sq[3], m1), m2))});
        SCHED0();
        unp(3, 10);
        RAWBAR()
        const f32x4 e0 = __builtin_bit_cast(f32x4, lr128(ep)), e1 = __builtin_bit_cast(f32x4, lr128(ep + 16));
        SCHED0();
        unp(10, 20);
        const f32x4 e = e0 + e1;
        float m = (e[0] + e[2]) * (1.f / 640.f);
        const float var = bfr(__builtin_fmaxf(__builtin_fmaf(-m, m, (e[1] + e[3]) * (1.f / 640.f)), 0.f));
        m = bfr(m);
        const float rden = __builtin_amdgcn_rcpf(bfr(__builtin_amdgcn_sqrtf(bfr(var + 1e-5f))));
#pragma unroll
        for (int c = 0; c < 5; ++c) {
            u32x4 o;
#pragma unroll
            for (int i = 0; i < 4; ++i) { const f32x2 x = v[c * 4 + i] - m; o[i] = cvtpk(bfr(x[0]) * rden, bfr(x[1]) * rden); }
            st128(k.rs, l * 16, dst + c * 16384, o);
        }
        STAMP(sg, 2)
        unsigned fv = sx - (k.c ? 0 : 8);
        PIN(fv);
        DRAIN();
        st32(k.rs, l == 0 ? 0u : OOB, k.p0 + CNT(C_XN, rt * 4 + k.w), fv);
        dp ^= 2048; ep ^= 2048;
        STAMP(sg, 3) STAMP(sg, 4) STAMP(sg, 5)
        PFLUSH(sg)
    }
    PROF_OUT
}

DEVI int red_col(int j) { const int w = j / 5, q = j % 5; return q < 3 ? 3 * w + q : 9 + 2 * w + q; }
struct Red {
    u32x4 Y[16];
    template <int I0, int I1> DEVI void yload(const Ctx& k, int j, int sg) {
        const unsigned a = k.yx(sg, j / 5) + (j % 5) * 131072 + k.w * 16384;
#pragma unroll
        for (int i = I0; i < I1; ++i) Y[i] = ld128(k.rs, k.l * 16 + (i & 3) * 1024, a + ((i >> 2) & 1) * 4096 + (i >> 3) * 65536);
    }
};
#define BCV (k.l < 8 ? 0 : k.l < 16 ? 16384 : 16388) + (k.l & 7) * 1024, k.p0 + CNT(C_BC + k.w * 2, 0) + ((((k.role >> 5) * 256) | ((k.role & 31) > 20 ? 128 : 0)) & RMSK)
template <int SLP> DEVI unsigned bc_watch(const Ctx& k, bool y, int s) {
    if (OLDP & (y ? 2 : 1)) {
        unsigned v;
_Pragma("unroll 1")
        for (unsigned spins = 0;; ++spins) {
            v = ld32(k.rs, (y ? 16388 : (k.l & 8) * 2048) + (k.l & 7) * 1024, k.p0 + CNT(C_BC + k.w * 2, 0) + ((((k.role >> 5) * 256) | ((k.role & 31) > 20 ? 128 : 0)) & RMSK));
            if (__all((int)v >= s)) break;
            asm volatile("" ::: "memory");
            __builtin_amdgcn_s_sleep(1);
            if (spin_fail(k, spins)) break;
        }
        asm volatile("" ::: "memory");
        return v;
    }
    return watch<SLP>(k, BCV, L_PW + k.w * 512, L_PW + k.w * 512 + (y ? 64 : 0) + (k.l & 15) * 4, s, 0);
}
DEVI void y_wait(const Ctx& k, int s) { bc_watch<YSLP>(k, true, s); }
DEVI void wait_until(unsigned t) { while ((int)((rclk() - t) << 16) < 0) { if (WUS) __builtin_amdgcn_s_sleep(WUS); } }
#define DPPMAX(x, c) x = max(x, __builtin_amdgcn_update_dpp(0, x, c, 0xf, 0xf, false))
#define TSLP 4
#define TWU 0
DEVI void wait_until2(unsigned t) { while ((int)((rclk() - t) << 16) < 0) { if (TWU) __builtin_amdgcn_s_sleep(TWU); } }
#define WAIT_BC(s, BSLP) { \
    const unsigned bc = bc_watch<BSLP>(k, false, (s + 1) << 16); \
    const unsigned now = rclk(); \
    int d = (int)((bc - now) << 16); \
    d = ((int)bc >> 16) > s + 1 || d > (256 << 16) ? -(64 << 16) : d; \
    DPPMAX(d, 0xB1); DPPMAX(d, 0x4E); DPPMAX(d, 0x141); \
    tq = now + (__builtin_amdgcn_readlane(d, 0) >> 16); tv = now + (__builtin_amdgcn_readlane(d, 8) >> 16); }
DEVI void mred_role(const Params& p, const Ctx& k, char* smem, int j) {
    PROF_DECL
    const int l = k.l, w = k.w, g = l >> 4, n = l & 15;
    const unsigned ix = SPIX(l);
    const unsigned vp = 4096 + (g * 16 + ((n >> 1) & 1) * 8 + (n >> 2) * 2 + (n & 1)) * 16;
    const unsigned v16 = (g >> 1) * 1024 + (g & 1) * 256 + n * 16, yo = j * 32768 + EXP(w * 4096);
    const u32x4 ones = {0xFFFFFFFFu, 0xFFFFFFFFu, 0xFFFFFFFFu, 0xFFFFFFFFu};
    auto arm = [&](int s) {
#pragma unroll
        for (int i = 0; i < 4; ++i) st128(k.rs, l * 16 + i * 1024, k.ym(s) + yo, ones);
    };
    const unsigned mv = w < 2 ? l * 4 : OOB;
    unsigned fails = 0, wb = w * 512 + g * 128 + n * 4, rb = (w & 1) * 2048 + (g * 2 + ((l >> 2) & 1)) * 64 + ((l & 3) * 4 + ((l >> 3) & 1) * 2) * 4;
    const unsigned pv = l == 0 && j % 5 == 0 ? (j / 5) * 128 + w * 32 : OOB;
    Red R;
    RSTAMP(2)
    arm(1);
#pragma unroll
    for (int i = 0; i < 8; ++i) st128(k.rs, (i || w > 1 || g > 1) ? g * 32768 + n * 16 : OOB, k.memf(i * 8 + (w >> 1) * 4) + EXP((2 * j + (w & 1)) * 256), ones);
#pragma unroll
    for (int i = 0; i < 4; ++i) { const unsigned q = min(i * 80 + j * 4 + w, 287); st128(k.rs, l * 16, k.p0 + PR_B + PR_SZ + EXP(q * 1024), ones); }
    prog_init(k);
    sleep_until(k.t0 + RSL);
    y_wait(k, 2);
    R.yload<0, 16>(k, j, 0);
    for (int s = 0; s < 63; ++s) {
        STOP(s) HSTAMP(s, 0)
        unsigned ap = k.p0 + PR_B + (s & 3) * PR_SZ + w * 49152, am = k.ym(s) + yo, ma = k.memf(s + 1) + EXP((2 * red_col(j) + (w & 1)) * 256);
        unsigned ph = (s & 4) ? 0x80008000u : 0u;
        PINS(ap); PINS(am); PINS(ma); PINS(ph);
        unsigned tq, tv;
        WAIT_BC(s, BSLP)
        st32(k.rs, pv, k.p0 + CNT(C_XB, 0), spack(((int)((tq - tv) << 16) < 0 ? tv : tq) + RDY, s + 2));
        STAMP(s, 5)
        unsigned long long bad;
        do {
            u32x4 ym[4], pm[5];
            f32x4 acc[2];
            unsigned lim = 0x7FFFFFFFu | (fails << 15);
            ++fails; PINS(fails); PINS(lim);
            clr2(acc[0]); clr2(acc[1]);
            wait_until(tv);
            asm volatile("" ::: "memory");
#pragma unroll
            for (int i = 0; i < 4; ++i) ym[i] = ld128(k.rs, v16 + (i >> 1) * 2048 + (i & 1) * 512, am);
            asm volatile("" ::: "memory");
            wait_until(tq);
            STAMP(s, 1)
            asm volatile("" ::: "memory");
#pragma unroll
            for (int i = 0; i < 5; ++i) { pm[i] = ld128(k.rs, i < 4 ? l * 16 + i * 1024 : vp, ap); SCHED0(); }
            const u32x8 y0 = {ym[0][0], ym[1][0], ym[0][2], ym[1][2], ym[2][0], ym[3][0], ym[2][2], ym[3][2]};
            const u32x8 y1 = {ym[0][1], ym[1][1], ym[0][3], ym[1][3], ym[2][1], ym[3][1], ym[2][3], ym[3][3]};
            unsigned mx = 0;
#pragma unroll
            for (int i = 0; i < 4; ++i) mx = max(mx, umax4(ym[i]));
            unsigned tg = mx == 0xFFFFFFFFu ? 0x80000000u : 0u;
            asm volatile("s_nop 1" : "+v"(acc[0]), "+v"(acc[1]), "+v"(tg) : "v"(y0), "v"(y1), "v"(ix));
            SCHED0();
#pragma unroll
            for (int i = 0; i < 5; ++i) {
                if (i < 4) smf<false>(acc[0], __builtin_bit_cast(bf16x8, pm[i]), __builtin_bit_cast(bf16x16, __builtin_shufflevector(R.Y[2 * i], R.Y[2 * i + 1], 0, 1, 2, 3, 4, 5, 6, 7)), ix);
                else smf<false>(acc[0], __builtin_bit_cast(bf16x8, pm[4]), __builtin_bit_cast(bf16x16, y0), ix);
                if (i < 4) smf<false>(acc[1], __builtin_bit_cast(bf16x8, pm[i]), __builtin_bit_cast(bf16x16, __builtin_shufflevector(R.Y[8 + 2 * i], R.Y[9 + 2 * i], 0, 1, 2, 3, 4, 5, 6, 7)), ix);
                else smf<false>(acc[1], __builtin_bit_cast(bf16x8, pm[4]), __builtin_bit_cast(bf16x16, y1), ix);
                SCHED0();
            }
#pragma unroll
            for (int i = 0; i < 2; ++i) tg |= __builtin_amdgcn_bitop3_b32(pm[i][0], pm[i][1], ph, 0x7e) | __builtin_amdgcn_bitop3_b32(pm[i][2], pm[i][3], ph, 0x7e);
            asm volatile("s_nop 1" : "+v"(acc[0]), "+v"(acc[1]), "+v"(tg));
#pragma unroll
            for (int t = 0; t < 2; ++t) { lw32(wb + t * 2048, __float_as_uint(acc[t][0] + acc[t][2])); lw32(wb + t * 2048 + 64, __float_as_uint(acc[t][1] + acc[t][3])); }
            SCHED0();
#pragma unroll
            for (int i = 2; i < 5; ++i) tg |= __builtin_amdgcn_bitop3_b32(pm[i][0], pm[i][1], ph, 0x7e) | __builtin_amdgcn_bitop3_b32(pm[i][2], pm[i][3], ph, 0x7e);
            bad = __ballot(tg > lim);
        } while (__builtin_expect(bad != 0, 0));
        STAMP(s, 2)
        RAWBAR()
        {
            const LAS f32x2* rp = (const LAS f32x2*)(size_t)rb;
            const f32x2 r = (rp[0] + rp[64]) + (rp[128] + rp[192]);
            st32(k.rs, mv, ma, min(cvtpk(r[0], r[1]) ^ ph, 0xFFFFFFFEu));
        }
        SCHED0();
        STAMP(s, 3)
        wb ^= 4096; rb ^= 4096;
        ask(k, z0(), (k.p0 ^ 8192u) + CNT(C_X1, j), L_PW + w * 512 + 256);
        y_wait(k, 2 * s + 3);
        R.yload<0, 8>(k, j, s + 1);
        if (ARMP == 0) {
        watch<PSLEEP>(k, z0(), (k.p0 ^ 8192u) + CNT(C_X1, j), L_PW + w * 512 + 256, L_PW + w * 512 + 256 + z0(), s - 1, 0);
        arm(s + 2);
        }
        y_wait(k, 2 * s + 4);
        STAMP(s, 4)
        R.yload<8, 16>(k, j, s + 1);
        if (ARMP == 1) {
        watch<PSLEEP>(k, z0(), (k.p0 ^ 8192u) + CNT(C_X1, j), L_PW + w * 512 + 256, L_PW + w * 512 + 256 + z0(), s - 1, 0);
        arm(s + 2);
        }
        PFLUSH(s)
    }
    PFLUSH(63)
    PROF_OUT
}
DEVI void tred_role(const Params& p, const Ctx& k, char* smem, int j) {
    PROF_DECL
    const int l = k.l, w = k.w, g = l >> 4, n = l & 15, T = 2 * red_col(j) + (w >> 1), rt = w & 1;
    const unsigned v16 = (g >> 1) * 1024 + (g & 1) * 256 + n * 16, yo = j * 32768 + EXP(w * 4096);
    const unsigned xo = EXP((T >> 1) * 2048) + (T & 1) * 512 + rt * 1024, xv = ((g >> 1) * 16 + n) * 16 + (g & 1) * 8;
    const u32x2 ones = {0xFFFFFFFFu, 0xFFFFFFFFu};
    const unsigned fl = (w == 0 && l == 0) ? 0u : OOB;
    unsigned fails = 0;
    Red R;
    RSTAMP(2)
    st64(k.rs, xv, k.segx(PG_X1, 0) + xo, ones); st64(k.rs, xv, k.segx(PG_X1, 1) + xo, ones);
    prog_init(k);
    sleep_until(k.t0 + RSL);
    y_wait(k, 2);
    R.yload<0, 16>(k, j, 0);
    for (int s = 0; s < 64; ++s) {
        STOP(s) STAMP(s, 0)
        unsigned ap[4], am = k.ym(s) + yo, xa = k.segx(PG_X1, s) + xo, rb = (s & 1) * 16384 + l * 16, wb = rb + w * 1024, fv = s;
#pragma unroll
        for (int i = 0; i < 4; ++i) { ap[i] = k.p0 + PR_B + (s & 3) * PR_SZ + w * 49152 + (i < 2 ? 32768 : 16384) + (~i & 1) * 4096; PINS(ap[i]); }
        rb += w * 4096;
        PINS(am); PINS(xa); PIN(rb); PIN(wb); PIN(fv);
        const unsigned ph = (s & 4) ? 0x80008000u : 0u;
        unsigned tq, tv;
        u32x2 res;
        if (k.c) res = ld64(k.rs, xv, k.seg(PG_X2, s) + xo);
        else res = *(const u32x2*)(p.x + (size_t)(s * 32 + rt * 16 + n) * 640 + T * 16 + g * 4);
        f32x4 acc[2][2];
        WAIT_BC(s, TSLP)
        u32x4 yl[8];
#pragma unroll
        for (int i = 0; i < 8; ++i) { yl[i] = __builtin_shufflevector(R.Y[2 * i], R.Y[2 * i + 1], 0, 1, 4, 5); PIN(yl[i]); }
        f32x4 x = {blo(res[0] ^ ph), bhi(res[0] ^ ph), blo(res[1] ^ ph), bhi(res[1] ^ ph)};
        PIN(x);
        wait_until2(tv); wait_until2(tq + (s < 63 ? RDLY : 0));
        STAMP(s, 1)
        unsigned long long bad;
        do {
            asm volatile("" ::: "memory");
            u32x4 ym[4], pp[16];
#pragma unroll
            for (int i = 0; i < 4; ++i) { ym[i] = ld128(k.rs, v16 + (i >> 1) * 2048 + (i & 1) * 512, am); SCHED0(); }
#pragma unroll
            for (int i = 0; i < 16; ++i) { pp[i ^ 4] = ld128(k.rs, l * 16 + (i & 3) * 1024, ap[i >> 2]); SCHED0(); }
            unsigned mx = 0, tg, lim = 0x7FFFFFFFu | (fails << 15);
            ++fails; PINS(fails); PINS(lim);
#pragma unroll
            for (int i = 0; i < 4; ++i) mx = max(mx, umax4(ym[i]));
            tg = mx == 0xFFFFFFFFu ? 0x80000000u : 0u;
            u32x4 yf[4];
#pragma unroll
            for (int i = 0; i < 4; ++i) { const int t = i & 1, f = i >> 1; yf[i] = (u32x4){ym[2 * f][t], ym[2 * f][t + 2], ym[2 * f + 1][t], ym[2 * f + 1][t + 2]}; }
            asm volatile("s_nop 1" : "+v"(yf[0]), "+v"(yf[1]), "+v"(yf[2]), "+v"(yf[3]), "+v"(tg));
            SCHED0();
            sfor<32>([&](auto I) { constexpr int i = CV(I), t = i & 1, q = (i >> 1) ^ 4, r = q < 4 ? 0 : q < 8 ? (q >> 1) & 1 : 1;
                mfa<false, q == 4 || q == 6>(acc[t][r], __builtin_bit_cast(bf16x8, q < 4 ? yl[t * 4 + q] : q < 8 ? yf[(q & 1) * 2 + t] : R.Y[t * 8 + (q & 7)]), __builtin_bit_cast(bf16x8, pp[q]));
                SCHED0();
                tg |= __builtin_amdgcn_bitop3_b32(pp[q][2 * t], pp[q][2 * t + 1], ph, 0x7e); PIN(tg);
                if constexpr (i == 20 || i == 22) { asm volatile("" : "+v"(acc[(i - 20) / 2][0])); lw128(wb + (i - 20) * 4096, __builtin_bit_cast(u32x4, acc[(i - 20) / 2][0])); }
                if constexpr (i == TPOS) st32(k.rs, fl, (k.p0 ^ 8192u) + CNT(C_X1, j), fv);
                SCHED0(); });
            bad = __ballot(tg > lim);
        } while (__builtin_expect(bad != 0, 0));
        STAMP(s, 2)
        asm volatile("s_nop 3" : "+v"(acc[0][1]), "+v"(acc[1][1]));
        lw128(wb + 4096, __builtin_bit_cast(u32x4, acc[0][1])); lw128(wb + 12288, __builtin_bit_cast(u32x4, acc[1][1]));
        SCHED0();
        ask(k, BCV, L_PW + w * 512);
        RAWBAR()
        {
            const f32x4 r0 = __builtin_bit_cast(f32x4, lr128(rb)), r1 = __builtin_bit_cast(f32x4, lr128(rb + 1024)), r2 = __builtin_bit_cast(f32x4, lr128(rb + 2048)), r3 = __builtin_bit_cast(f32x4, lr128(rb + 3072));
            SCHED0();
            if constexpr (TPOS == 32) st32(k.rs, fl, (k.p0 ^ 8192u) + CNT(C_X1, j), fv);
            SCHED0();
            const f32x4 r = (r0 + r1) + (r2 + r3);
            st64(k.rs, xv, xa, (u32x2){min(cvtpk(x[0] + bfr(r[0]), x[1] + bfr(r[1])) ^ ph, 0xFFFFFFFEu), min(cvtpk(x[2] + bfr(r[2]), x[3] + bfr(r[3])) ^ ph, 0xFFFFFFFEu)});
            SCHED0();
            if constexpr (TPOS == 33) st32(k.rs, fl, (k.p0 ^ 8192u) + CNT(C_X1, j), fv);
        }
        st64(k.rs, s < 62 ? xv : OOB, k.segx(PG_X1, (s + 2) & 63) + xo, ones);
        STAMP(s, 3)
        if (s < 63) {
            watch<YSLP>(k, BCV, L_PW + k.w * 512, L_PW + k.w * 512 + 64 + (k.l & 15) * 4, 2 * s + 4, 0);
            R.yload<0, 16>(k, j, s + 1);
        }
        STAMP(s, 4)
        STAMP(s, 5)
        PFLUSH(s)
    }
    PROF_OUT
}

#undef TZ
#define TZ z0()
DEVI void up_role(const Params& p, const Ctx& k, char* smem, int id) {
    PROF_DECL
    const int l = k.l, w = k.w, g = l >> 4, n = l & 15;
    bf16x8 Wt[80];
    const int tile0 = id * 8 + w * 2;
    auto issue = [&](auto U) { constexpr int u = CV(U);
        sfor<8>([&](auto J) { constexpr int j = CV(J); dma1<(u % 3) * 32768 + j * 1024>(((w & 1) ? p.wu : p.wg) + (size_t)(2 * u + (w >> 1)) * 32 * 1536 + id * 128, wsrc<256, true>(j * 64 + l, 3072), w * 8192); }); };
    sfor<3>(issue);
    cstream<10>([](auto V) { return IC(CV(V) < 9 ? 8 : 0); },
        [&](auto U) { constexpr int u = CV(U);
            if constexpr (u > 0 && u < 8) issue(IC(u + 2));
#pragma unroll
            for (int i = 0; i < 8; ++i) Wt[(i & 3) * 20 + 2 * u + (i >> 2)] = wp_read<256, true>(smem + (u % 3) * 32768 + (i >> 1) * 8192, w * 2 + (i & 1), l); });
    RAWBAR()
    RSTAMP(2)
    bf16x8 one8 = {0x3F80, 0x3F80, 0x3F80, 0x3F80, 0x3F80, 0x3F80, 0x3F80, 0x3F80};
    asm volatile("s_nop 1" : "+v"(one8));
    f32x4 cs[4];
    sfor<80>([&](auto I) { constexpr int f = CV(I); mfa<(f < 64), f % 20 == 0>(cs[f / 20], Wt[f], one8); });
    asm volatile("s_nop 7" : "+v"(cs[0]), "+v"(cs[1]), "+v"(cs[2]), "+v"(cs[3]));
    const unsigned ho = EXP((tile0 >> 1) * 1024), l16 = l * 16;
    constexpr int L_ST = 81920;
    const u32x4 ones = {0xFFFFFFFFu, 0xFFFFFFFFu, 0xFFFFFFFFu, 0xFFFFFFFFu};
#pragma unroll
    for (int i = 0; i < 4; ++i) st128(k.rs, l16, k.hseg(i >> 1) + (i & 1) * EXP(49152) + ho, ones);
    unsigned fails = 0;
    lw32(L_ST + 1024, 4096u);
    prog_init(k);
    const unsigned fl = (w == 0 && l == 0) ? 0u : OOB;
    const unsigned long long m1 = __ballot(n & 1), m2 = __ballot(n & 2);
    for (int s = 0; s < 64; ) {
        unsigned xs = k.seg(PG_X1, s) + w * 2048, xd = (s & 1) * 40960 + w * 2048;
        PINS(xs); PINS(xd);
        prog_wait<20>(k, k.p0 + CNT(C_X1, 0), fails ? s : -1);
        STOP(s) STAMP(s, 0)
        xn_all(k, l16, xs, xd);
        const unsigned xb = (s & 1) * 40960 + l16, yb = xb + w * 2048, sp = L_ST + (s & 1) * 2048, rp = sp + n * 16;
        const unsigned dp = sp + (g == (n >> 2) ? n * 16 + w * 4 : 4096 + l * 4);
        f32x4 acc[2][4], st[4];
        bf16x8 x[20], x1[20], y0[5], y1[5];
        unsigned mx = 0;
        auto blk = [&](auto B, auto J) { constexpr int b = CV(B), j = CV(J);
            if constexpr (j == 0) asm volatile("s_waitcnt vmcnt(%0)\n\ts_barrier" :: "n"(8 - 2 * b) : "memory");
            if constexpr (j >= 1 && j < 5) x[4 * b + j - 1] = lr128(xb + (4 * b + j - 1) * 2048);
            if constexpr (j == 5) y0[b] = lr128(yb + b * 8192);
            if constexpr (j == 6) y1[b] = lr128(yb + b * 8192 + 1024); };
        sfor<7>([&](auto J) { blk(IC(0), J); });
        SCHED0();
        STAMP(s, 1)
        float mean[2], rden[2], e[4][4];
        f32x4 sa[2], sb[2];
        u32x4 bad, o[2];
        unsigned sv, fv, r, f0, f1, ha = k.hseg(s) + ho;
        PINS(ha);
        auto stat = [&](auto I) { constexpr int i = CV(I), rt = i / 8, j = i % 8;
            if constexpr (j == 0) asm volatile("v_add_f32 %0, %2, %3\n\tv_add_f32 %1, %4, %5" : "=&v"(mean[rt]), "=&v"(rden[rt]) : "v"(sa[rt][0]), "v"(sa[rt][1]), "v"(sb[rt][0]), "v"(sb[rt][1]));
            if constexpr (j == 1 || j == 2) asm volatile("v_add_f32 %0, %0, %2\n\tv_add_f32 %1, %1, %3" : "+v"(mean[rt]), "+v"(rden[rt]) : "v"(sa[rt][j + 1]), "v"(sb[rt][j + 1]));
            if constexpr (j == 3) asm volatile("v_mul_f32 %0, 0xbaccccCD, %0\n\tv_mul_f32 %1, 0x3acccccd, %1" : "+v"(mean[rt]), "+v"(rden[rt]));
            if constexpr (j == 4) asm volatile("v_fma_f32 %1, -%0, %0, %1\n\tv_cvt_pk_bf16_f32 %0, 0, %0\n\tv_max_f32 %1, 0, %1" : "+v"(mean[rt]), "+v"(rden[rt]));
            if constexpr (j == 5) asm volatile("v_cvt_pk_bf16_f32 %0, 0, %0\n\tv_add_f32 %0, 0x3727c5ac, %0\n\tv_cvt_pk_bf16_f32 %0, 0, %0" : "+v"(rden[rt]));
            if constexpr (j == 6) asm volatile("v_sqrt_f32 %0, %0" : "+v"(rden[rt]));
            if constexpr (j == 7) asm volatile("v_cvt_pk_bf16_f32 %0, 0, %0\n\tv_rcp_f32 %0, %0" : "+v"(rden[rt])); };
        auto ep = [&](auto RT, auto T, auto J) { constexpr int rt = CV(RT), t = CV(T), j = CV(J), c = (j & 1) * 2, u = j >= 20, q = u ? j - 20 : j;
            float (&a)[4] = e[t + 2 * u];
#define A2 "+v"(a[c]), "+v"(a[c + 1])
            if constexpr (j < 0 || j >= 30) { }
            else if constexpr (q < 2) asm volatile("v_fma_f32 %0, %2, %3, %5\n\tv_fma_f32 %1, %2, %4, %6" : "=&v"(a[c]), "=&v"(a[c + 1]) : "v"(mean[rt]), "v"(cs[t + 2 * u][c]), "v"(cs[t + 2 * u][c + 1]), "v"(acc[rt][t + 2 * u][c]), "v"(acc[rt][t + 2 * u][c + 1]));
            else if constexpr (q < 4) asm volatile("v_mul_f32 %0, %0, %2\n\tv_mul_f32 %1, %1, %2" : A2 : "v"(rden[rt]));
            else if constexpr (q < 6 || j == 18 || j == 19) asm volatile("v_cvt_pk_bf16_f32 %0, 0, %0\n\tv_cvt_pk_bf16_f32 %1, 0, %1" : A2);
            else if constexpr (j < 8) asm volatile("v_mul_f32 %0, 0xbfb8aa3b, %0\n\tv_mul_f32 %1, 0xbfb8aa3b, %1" : A2);
            else if constexpr (j < 12) asm volatile("v_exp_f32 %0, %0" : "+v"(a[j & 3]));
            else if constexpr (j < 14) asm volatile("v_add_f32 %0, 1.0, %0\n\tv_add_f32 %1, 1.0, %1" : A2);
            else if constexpr (j < 18) asm volatile("v_rcp_f32 %0, %0" : "+v"(a[(j - 14) & 3]));
            else if constexpr (q < 8) asm volatile("v_mul_f32 %0, %0, %2\n\tv_mul_f32 %1, %1, %3" : A2 : "v"(e[t][c]), "v"(e[t][c + 1]));
            else if constexpr (q == 8) asm volatile("v_cvt_pk_bf16_f32 %0, %0, %2\n\tv_cvt_pk_bf16_f32 %1, %1, %3" : "+v"(a[0]), "+v"(a[2]) : "v"(a[1]), "v"(a[3]));
            else { unsigned d0, d1; asm volatile("v_min_u32 %0, -2, %2\n\tv_min_u32 %1, -2, %3" : "=&v"(d0), "=&v"(d1) : "v"(a[0]), "v"(a[2])); o[rt][2 * t] = d0; o[rt][2 * t + 1] = d1; } };
        sfor<180>([&](auto I) {
            constexpr int a = CV(I), b = a / 8, j = (a % 8) ^ (b == 4 ? 4 : 0), ch = a / 20 - 1, ks = a < 40 ? 4 * b + (j & 3) : a % 20, rt = ch >= 4;
            constexpr int nt = a < 40 ? 0 : rt ? ch - 4 : (ch & 1) * 2 + (ch >> 1), f = nt * 20 + ks, i = a - 36;
            if constexpr (a >= 40 || j < 4) mfa<(f < 64), ks == 0>(acc[rt][nt], Wt[f], rt ? x1[ks] : x[ks]);
            else mfa<false, b == 0>(st[j & 3], (j & 1) ? (j & 2 ? y1[b] : y0[b]) : one8, j & 2 ? y1[b] : y0[b]);
            SCHED0();
            if constexpr (a >= 103 && a < 119) TOUCH(x1[(a - 99) % 20]);
            else if constexpr (a == 60) { r = __builtin_amdgcn_readfirstlane(bad[0]); PINS(r); }
            else if constexpr (a == 61) { r = (fails - r) >> 31; PINS(r); }
            else if constexpr (a == 62) { fails += r; PINS(fails); }
            else if constexpr (a == 63) { f0 = 2 * s - r; PINS(f0); }
            else if constexpr (a == 64) { f1 = f0 | 1; PINS(f1); }
            else if constexpr (a == 65) { r <<= 31; PINS(r); }
            else if constexpr (a >= 56 && (a < 92 || a >= 119)) __builtin_amdgcn_s_setprio(0);
            if constexpr (a < 32 && a % 8 == 7) asm volatile("" :: "v"(x[4 * b + 4]), "v"(x[4 * b + 5]), "v"(x[4 * b + 6]), "v"(x[4 * b + 7]), "v"(y0[b + 1]));
            if constexpr (a < 32 && a % 8 == 4) TOUCH(y1[b]);
            SCHED0();
            if constexpr (a == 15) lw32((rp ^ 2048) + 1024, z0());
            if constexpr (a < 32 && a % 8 < 7) blk(IC((b + 1) % 5), IC(a % 8));
            if constexpr (i == 0 || i == 4 || i == 5 || i == 8) x1[i / 2 - (i == 4 || i == 8)] = lr128(xb + 1024 + (i / 2 - (i == 4 || i == 8)) * 2048);
            if constexpr (a >= 92 && a < 108) x1[a - 88] = lr128(xb + 1024 + (a - 88) * 2048);
            SCHED0();
            if constexpr (a < 40 && j >= 4) { const u32x4 v = __builtin_bit_cast(u32x4, (j & 1) ? y1[b] : y0[b]); mx = umax3(mx, v[j & 2], v[(j & 2) + 1]); PIN(mx); }
            if constexpr (i == 1 || i == 3) {
                constexpr int rt2 = i == 3;
                asm volatile("" : "+v"(st[2 * rt2]), "+v"(st[2 * rt2 + 1]));
                const f32x4& q = st[2 * rt2 + 1];
                const float d = csel(csel(q[0], q[1], m1), csel(q[2], q[3], m1), m2);
                lw32(dp + rt2 * 512, __float_as_uint(st[2 * rt2][0])); lw32(dp + rt2 * 512 + 256, __float_as_uint(d));
            }
            if constexpr (i == 2) lw32(mx == 0xFFFFFFFFu ? sp + 1024 : dp + 4096, 4096u);
            if constexpr (i == 6) bar_lgkm<2>();
            if constexpr (i == 6) sa[0] = __builtin_bit_cast(f32x4, lr128(rp));
            if constexpr (i == 7) sb[0] = __builtin_bit_cast(f32x4, lr128(rp + 256));
            if constexpr (i == 9) sa[1] = __builtin_bit_cast(f32x4, lr128(rp + 512));
            if constexpr (i == 10) sb[1] = __builtin_bit_cast(f32x4, lr128(rp + 768));
            if constexpr (i == 11) bad = __builtin_bit_cast(u32x4, lr128(rp + 1024));
            if constexpr (i >= 12 && i < 20) stat(IC(i - 12));
            if constexpr (a == 40) { STAMP(s, 2) }
            if constexpr (a == UPOS0 || a == UPOS) st32(k.rs, fl, k.p0 + CNT(C_H, id), fv);
            if constexpr (a == 117) __builtin_amdgcn_raw_buffer_store_b128(o[0], k.rs, (int)sv, (int)ha, AUX_SC1);
            if constexpr (a == 118) TOUCH(o[0]);
            SCHED0();
            ep(IC(0), IC(0), IC(a - 56)); ep(IC(0), IC(1), IC(a - 86));
            if constexpr (a >= 116 && a < 124) stat(IC(a - 108));
            if constexpr (a == UPOS0 - 2 || a == UPOS - 2) { fv = a == UPOS - 2 ? f1 : f0; PIN(fv); }
            if constexpr (a == 80) { sv = l16 | r; PIN(sv); }
            if constexpr (a < 144) ep(IC(1), IC(0), IC(a - 124));
            else if constexpr (a < 164) ep(IC(1), IC(1), IC(a - 144));
            else ep(IC(1), IC(0), IC(a - 144));
            SCHED0();
        });
        STAMP(s, 3)
        asm volatile("s_nop 7" : "+v"(acc[1][3]));
        {
            const f32x4 u = (acc[1][3] + mean[1] * cs[3]) * rden[1];
            o[1][2] = packs(bfr(u[0]) * e[1][0], bfr(u[1]) * e[1][1]); o[1][3] = packs(bfr(u[2]) * e[1][2], bfr(u[3]) * e[1][3]);
        }
        st128(k.rs, sv, ha + EXP(49152), o[1]);
        if (r) { PCOUNT(0) RAWBAR() lw32(rp + 1024, 0u); RAWBAR() }
        st128(k.rs, s < 62 ? l16 : OOB, k.hseg((s + 2) & 63) + ho, ones); st128(k.rs, s < 62 ? l16 : OOB, k.hseg((s + 2) & 63) + EXP(49152) + ho, ones);
        STAMP(s, 4) STAMP(s, 5)
        PFLUSH(s)
        s = (f1 + 1) >> 1;
    }
    PROF_OUT
}

DEVI void down_role(const Params& p, const Ctx& k, char* smem, int id) {
    PROF_DECL
    const int l = k.l, w = k.w, g = l >> 4, n = l & 15, rt = id / 5, cg = id % 5;
    bf16x8 Wt[96];
    wstream<12, 8, 4>(smem + w * 32768,
        [&](auto U, unsigned sl) { constexpr int u = decltype(U)::value; sfor<8>([&](auto J) { constexpr int j = CV(J); dma1<(u % 4) * 8192 + j * 1024>(p.wd + (size_t)(w * 12 + u) * 32 * 640 + cg * 128, wsrc<256, false>(j * 64 + l, 1280), sl); }); },
        [&](auto U, char* sp) { constexpr int u = decltype(U)::value;
#pragma unroll
            for (int nt = 0; nt < 8; ++nt) Wt[u * 8 + nt] = wp_read<256, false>(sp, (nt + 2 * w) & 7, l); });
    RAWBAR()
    RSTAMP(2)
    const unsigned xo = EXP((cg * 4 + w) * 2048) + rt * 1024, xv = ((g >> 1) * 16 + n) * 16 + (g & 1) * 8;
    const u32x2 ones = {0xFFFFFFFFu, 0xFFFFFFFFu};
    const unsigned x2v = k.c ? xv : OOB;
    prog_init(k);
#pragma unroll
    for (int i = 0; i < 4; ++i) st64(k.rs, x2v, k.seg(PG_X2, i >> 1) + xo + (i & 1) * 512, ones);
    const __amdgpu_buffer_rsrc_t ro = __builtin_amdgcn_make_buffer_rsrc((void*)p.out, 0, 2048 * 1280, 0x00020000);
    const unsigned ov = k.c ? OOB : n * 1280 + g * 8, lim = k.c ? 0xFFFFFFFEu : 0xFFFFFFFFu, fl = (w == 0 && l == 0) ? 0u : OOB;
    unsigned fails = 0;
    for (int sx = -1; sx < 64; ++sx) {
        const int s = max(sx, 0);
        unsigned x2w = x2v | (sx & OOB), ow = ov | (sx & OOB);
        PIN(x2w); PIN(ow);
        unsigned la[3], ra = k.seg(PG_X1, s) + xo, oa = k.c ? k.seg(PG_X2, s) + xo : (s * 32 + rt * 16) * 1280 + (cg * 8 + w * 2) * 32, rb = (sx & 1) * 32768 + l * 16, wb[3], fv = sx;
#pragma unroll
        for (int q = 0; q < 3; ++q) { la[q] = k.hseg(s) + rt * EXP(49152) + EXP((w * 12 + q * 4) * 1024); wb[q] = rb + ((w + q + 1) & 3) * 6144 + q * 2048; PINS(la[q]); PIN(wb[q]); }
        rb += w * 6144;
        PINS(ra); PINS(oa); PIN(rb); PIN(fv);
        prog_wait<12>(k, k.p0 + CNT(C_H, 0), 2 * sx + rt);
        STOP(s) STAMP(s, 0)
        f32x4 acc[8], r[6];
        u32x2 res[2];
        u32x4 hx[12];
        unsigned long long bad;
        do {
            asm volatile("" ::: "memory");
#pragma unroll
            for (int i = 0; i < 12; ++i) { hx[i] = ld128(k.rs, l * 16 + (i & 3) * 1024, la[i >> 2]); SCHED0(); }
#pragma unroll
            for (int t = 0; t < 2; ++t) res[t] = ld64(k.rs, xv + t * 512, ra);
            SCHED0();
            unsigned mx = 0;
            sfor<72>([&](auto I) { constexpr int i = CV(I), ks = i / 6, j = 2 + i % 6, f = ks * 8 + j;
                mfa<(f < 64), ks == 0>(acc[j], Wt[f], __builtin_bit_cast(bf16x8, hx[ks]));
                SCHED0();
                if constexpr (i % 6 < 2) { mx = umax3(mx, hx[ks][(i & 1) * 2], hx[ks][(i & 1) * 2 + 1]); PIN(mx); }
                if constexpr (i == 68) { bad = __ballot(mx == 0xFFFFFFFFu); PINS(bad); }
                if constexpr (i == 69) { bad = fails - 1 < 65536u ? bad : 0; PINS(bad); }
                if constexpr (i == 70) { ++fails; PINS(fails); }
                if constexpr (i == DPOS) st32(k.rs, fl, k.p0 + CNT(C_X2, rt * 5 + cg), fv);
                SCHED0(); });
        } while (__builtin_expect(bad != 0, 0));
        STAMP(s, 1)
        float x[8];
        sfor<24>([&](auto I) { constexpr int i = CV(I), ks = i / 2, j = i & 1, f = ks * 8 + j;
            mfa<(f < 64), ks == 0>(acc[j], Wt[f], __builtin_bit_cast(bf16x8, hx[ks]));
            SCHED0();
            if constexpr (i < 12 && !(i & 1)) { constexpr int jj = 2 + i / 2;
                asm volatile("" : "+v"(acc[jj])); lw128(wb[jj / 2 - 1] + (jj & 1) * 1024, __builtin_bit_cast(u32x4, acc[jj])); }
            if constexpr (i < 8 && (i & 1)) { x[i - 1] = blo(res[i >> 2][(i >> 1) & 1]); x[i] = bhi(res[i >> 2][(i >> 1) & 1]); PIN(x[i - 1]); PIN(x[i]); }
            if constexpr (i == 13) bar_lgkm<0>();
            if constexpr (i >= 13 && i < 19) r[i - 13] = __builtin_bit_cast(f32x4, lr128(rb + (i - 13) * 1024));
            if constexpr (i + 72 == DPOS) st32(k.rs, fl, k.p0 + CNT(C_X2, rt * 5 + cg), fv);
            SCHED0(); });
        r[0] += r[2] + r[4]; r[1] += r[3] + r[5];
        asm volatile("s_nop 3" : "+v"(acc[0]), "+v"(acc[1]), "+v"(r[0]), "+v"(r[1]));
#pragma unroll
        for (int t = 0; t < 2; ++t) {
            const f32x4 d = acc[t] + r[t];
            const u32x2 o = {min(cvtpk(x[4 * t] + bfr(d[0]), x[4 * t + 1] + bfr(d[1])), lim), min(cvtpk(x[4 * t + 2] + bfr(d[2]), x[4 * t + 3] + bfr(d[3])), lim)};
            st64(k.rs, x2w + t * 512, oa, o);
            __builtin_amdgcn_raw_buffer_store_b64(o, ro, (int)(ow + t * 32), (int)oa, OUTAUX);
        }
        STAMP(s, 2)
#pragma unroll
        for (int t = 0; t < 2; ++t) st64(k.rs, s < 62 ? x2v : OOB, k.seg(PG_X2, (s + 2) & 63) + xo + t * 512, ones);
        STAMP(s, 3) STAMP(s, 4) STAMP(s, 5)
        PFLUSH(s)
    }
    PROF_OUT
}

DEVI void warm(const Params& p, const Ctx& k, int xl, int hw) {
    set_m0(k.w * 256);
_Pragma("unroll 1")
    for (int it = hw; it < 32 * 15; it += k.c ? 40 : 64) {
        const int q = it < 120 ? it & 7 : 8 + (it - 120) % 24, t = it < 120 ? it >> 3 : (it - 120) / 24;
        const u16* b = q < 24 ? (q < 8 ? p.Wv : q < 16 ? p.Wk : p.Wq) + (xl * 8 + (q & 7)) * 192 : p.Wo + (xl * 8 + (q & 7)) * 122880;
        const unsigned ln = t * 64 + k.l, cl = min(((unsigned)(size_t)b >> 7) & 7u, 4u);
        if (k.c) sleep_until(k.t0 + WP + it * PP / 128);
        if (t < (q >= 24 ? PO : ((q < 8 ? PV : PQ) >> (cl * 4)) & 15))
            asm volatile("global_load_lds_dword %0, %1" :: "v"(q < 24 ? (639 - ln / 3) * 12288 + (ln % 3) * 128 : (191 - (ln >> 2)) * 1280 + 768 + (ln & 3) * 128), "s"(b) : "memory");
    }
    DRAIN();
_Pragma("unroll 1")
    for (int i = 3; i < 8; ++i) { sleep_until(k.t0 + i * 50); hello(k, 0); }
    DRAIN();
    RAWBAR()
}

DEVI int xcc_id() { return __builtin_amdgcn_s_getreg(20 | (0 << 6) | (3 << 11)) & 7; }

DEVI unsigned prepare(const Params& p, Ctx& k, char* smem, int xl, int i) {
    const int l = k.l, j = xl * 22 + i - 4;
    const u32x4 ones = {0xFFFFFFFFu, 0xFFFFFFFFu, 0xFFFFFFFFu, 0xFFFFFFFFu};
    unsigned vote = 0;
    if (i < 4) {
        const int row = ((i * 4 + xl) * 4 + k.w) * 4 + (l >> 4);
        const unsigned dst = k.seg(PG_XNA, i * 2 + (xl >> 1));
        u32x4 a[5];
#pragma unroll
        for (int c = 0; c < 5; ++c) a[c] = *(const u32x4*)(p.x + (size_t)row * 640 + (c * 16 + (l & 15)) * 8);
        ln_rows(a, l & 15, row & 31, [&](unsigned vo, int c, u32x4 o) { st128(k.rs, vo, dst + c * 16384, o); st128(k.rs, vo, dst + c * 16384 + 8192, o); });
    } else if (k.w == 3) {
        const unsigned cal = ((unsigned)(j % 44) << 21) + 7 * 16384 + (xl >> 1) * 4096 + (l & 7) * 16;
        int d = 0;
#pragma unroll 1
        for (int r = 0; r <= 2 * CALR; ++r) {
            unsigned long long c0, c1;
            st128(k.rs, cal, (r & 1) * 8192, ones);
            st128(k.rs, cal, (~r & 1) * 8192, ones);
            asm volatile("s_waitcnt vmcnt(1)\n\ts_memtime %0\n\ts_waitcnt vmcnt(0)\n\ts_memtime %1\n\ts_waitcnt lgkmcnt(0)" : "=&s"(c0), "=s"(c1) :: "memory");
            d = r ? min((int)(c1 - c0), 400) - d : 0;
        }
        vote = (unsigned)(d < 0) ^ (unsigned)(xcc_id() >> 2) ^ (unsigned)(i & 1);
    } else {
        if (k.w == 2 && j < 20) {
            const int e = j * 32 + (l >> 1), T = e >> 4, hf = (e >> 1) & 1, rr = ((e >> 2) & 3) * 2 + (e & 1);
            u32x4 v;
#pragma unroll
            for (int c = 0; c < 4; ++c) v[c] = min(*(const unsigned*)(p.mem0 + rr * 640 + T * 16 + 4 * c + 2 * hf), 0xFFFFFFFEu);
            st128(k.rs, (2u << 21) + EXP(e * 16) + (l & 1) * 8192, 0, v);
        }
        constexpr unsigned N0 = PR_B + PR_SZ, N1 = 40 * 16384, N2 = 32768;
        static_assert(N0 + N1 + N2 <= 5 * 88 * 3072 && NPAGES <= 44, "fill");
#pragma unroll
        for (int r = 0; r < 5; ++r) {
            const unsigned o = ((r * 88 + j) * 3 + k.w) * 1024;
            st128(k.rs, o < N0 + N1 + N2 ? l * 16 : OOB, o < N0 ? o : o < N0 + N1 ? ((unsigned)PG_YM << 21) + (o - N0) : (2u << 21) + 32768 + (o - N0 - N1), ones);
        }
    }
    if (k.w == 3) *(volatile LAS unsigned*)smem = vote;
    DRAIN();
    __builtin_amdgcn_s_waitcnt(0x0f70);
    XSTAMP(0)
    RAWBAR()
    return *(volatile LAS unsigned*)smem;
}
DEVI void master(const Params& p, const Ctx& k) {
    const int l = k.l;
    u32x2 v[4] = {{0u, 0u}, {0u, 0u}, {0u, 0u}, {0u, 0u}};
_Pragma("unroll 1")
    for (unsigned spins = 0; spins < SPIN_CAP; ++spins) {
#pragma unroll
        for (int q = 0; q < 4; ++q) v[q] |= cm_ld(k, (CM_IN + (q * 64 + l) * 128) | v[q][1]);
        if (__all((v[0][1] & v[1][1] & v[2][1] & v[3][1]) != 0)) break;
        asm volatile("" ::: "memory");
    }
    const unsigned long long a = __ballot(v[2][0] != 0), b = __ballot(v[3][0] != 0);
    const int n = __popcll(a) + __popcll(b);
    const unsigned long long g = n > 44 ? ~0ull : 0ull, u = (a & b) | ((a ^ b) & g);
    const unsigned long long t = (__popcll(a ^ b) > 2 || n > 80 || n < 8 ? g : ((u >> 4) & 0x3FFFFF) | ((u >> 36) << 22)) ^ 0xAAAAAAAAAAAAAAAAull;
#pragma unroll
    for (int q = 0; q < 4; ++q) cm_st(k, l * 128, CM_GO + q * 8192, (unsigned)t, (unsigned)(t >> 32) | 0x80000000u);
}

#define FIN(n) { if (blockIdx.x == EVB) sleep_until((unsigned)tf + 30000); __builtin_amdgcn_s_waitcnt(0x0f70); asm volatile("s_nop " #n); return; }
__global__ __launch_bounds__(256, 1) void mega_kernel(Params p) {
    extern __shared__ __attribute__((aligned(16))) char smem[];
    Ctx k;
    unsigned long long tf = rt_async();
    SCHED0();
    const u16 *wq = p.Wq, *wk = p.Wk, *wv = p.Wv;
    asm volatile("" : "+s"(wq), "+s"(wk), "+s"(wv) : "s"(p.cm));
    k.ws = p.ws; k.cm = p.cm;
    k.rs = __builtin_amdgcn_make_buffer_rsrc((void*)p.ws, 0, 0x7fffffff, 0x00020000);
    k.l = threadIdx.x & 63; k.w = __builtin_amdgcn_readfirstlane(threadIdx.x >> 6);
    RSTAMP(0)
    const int x = blockIdx.x & 7, i = blockIdx.x >> 3, xl = x & 3;
    k.role = x * 32 + i; k.c = x >> 2; k.par = 0; k.p0 = 0; k.t0 = rclk();
    lgkm0(tf);
    tf = k.t0;
    line_st(k, CM_GO, 0, 0);
    auto start = [&]() {
        const u16* a = (i < 8 ? wk : wv) + (xl * 8 + (i & 7)) * 192, * b = (i < 8 ? wq : wv + 32 * 6144) + (xl * 8 + (i & 7)) * 192;
        unsigned o[3];
        sfor<18>([&](auto J) { constexpr int j = CV(J);
            if constexpr (j < 3) o[j] = wsrc<384, true>(k.w * 192 + j * 64 + k.l, 12288);
            dma1<SLOT(j / 6) + ((j / 3) & 1) * 12288 + (j % 3) * 1024>((((j / 3) & 1) ? b : a) + (j / 6) * (i < 8 ? 32 * 6144 : 64 * 6144), o[j % 3], k.w * 3072); }); };
    if (!k.c && i < 16) { if (QD && i < 8) sleep_until(k.t0 + QD); start(); }
    if (__builtin_expect(k.role == MASTER, 0)) {
#pragma unroll
        for (int q = 0; q < 16; ++q) cm_st(k, k.l * 8, CM_IN + k.w * 8192 + q * 512, 0, 0);
        cm_st(k, 0, CM_TMO, ~0u, ~0u);
        DRAIN();
        RAWBAR()
    }
    if (__builtin_expect(k.c && i < 26, 0)) { const unsigned v = prepare(p, k, smem, xl, i); RSTAMP(1) hello(k, v); go_wait(k, v); XSTAMP(1) RAWBAR() }
    else { RSTAMP(1) if (k.c) { DRAIN(); hello(k, 0); } }
    if (__builtin_expect(k.role == MASTER && k.w == 0, 0)) { master(p, k); __builtin_amdgcn_s_waitcnt(0x0f70); }
    if (k.c && i < 16) { sleep_until(k.t0 + WD1 + (i ^ 8) * STG); start(); }
    if (i < 8) { k.l = lane(); qk_role(p, k, smem, xl * 8 + i); FIN(0) }
    if (i < 16) { k.l = lane(); vo_role(p, k, smem, xl * 8 + i - 8); FIN(1) }
    k.l = lane();
    if (!k.c || i < 26) warm(p, k, xl, (i - 16) * 4 + k.w);
    else RAWBAR()
    go_wait(k, 0);
    if (i >= 26 && !k.c) {
        sleep_until(k.t0 + XPF);
#pragma unroll
        for (int r = 0; r < 3; ++r) asm volatile("global_load_lds_dword %0, %1" :: "v"(min(40 + r * 96 + ((i - 26) * 4 + xl) * 4 + k.w, 319) * 8192 + k.l * 128), "s"(p.x) : "memory");
        DRAIN();
        RAWBAR()
    }
    if (i >= 26) sleep_until(k.t0 + (k.c ? GST : GSA) + ((xl & 1) == UPODD ? 0 : i < 31 ? GSD : -GSA));
    if (i < 21) { mred_role(p, k, smem, xl * 5 + i - 16); FIN(2) }
    if (i < 26) { tred_role(p, k, smem, xl * 5 + i - 21); FIN(3) }
    if ((xl & 1) == UPODD) { up_role(p, k, smem, (xl >> 1) * 6 + i - 26); FIN(4) }
    if (i < 31) { down_role(p, k, smem, (xl >> 1) * 5 + i - 26); FIN(5) }
    row_role(p, k, smem, xl >> 1);
    FIN(6)
}

extern "C" void kernel_launch(void* const* d_in, const int* in_sizes, int n_in,
                              void* d_out, int out_size, void* d_ws, size_t ws_size,
                              hipStream_t stream) {
    Params p;
    p.x = (const u16*)d_in[0]; p.mem0 = (const u16*)d_in[1];
    p.Wq = (const u16*)d_in[2]; p.Wk = (const u16*)d_in[3]; p.Wv = (const u16*)d_in[4]; p.Wo = (const u16*)d_in[5];
    p.wg = (const u16*)d_in[8]; p.wu = (const u16*)d_in[9]; p.wd = (const u16*)d_in[10];
    p.out = (u16*)d_out; p.cm = (char*)d_ws;
    p.ws = (char*)(((uintptr_t)d_ws + CM_BYTES + ((1u << 21) - 1)) & ~(uintptr_t)((1u << 21) - 1));
    (void)hipFuncSetAttribute((const void*)mega_kernel, hipFuncAttributeMaxDynamicSharedMemorySize, LDS_BYTES);
    mega_kernel<<<256, 256, LDS_BYTES, stream>>>(p);
}
```

```cpp
#define SWAPG
#include <hip/hip_runtime.h>
#include <hip/hip_bf16.h>
#include <stdint.h>
#include <utility>

typedef unsigned short u16;
typedef __attribute__((ext_vector_type(16))) short bf16x16;
typedef __attribute__((ext_vector_type(8))) short bf16x8;
typedef __attribute__((ext_vector_type(4))) short bf16x4;
typedef __attribute__((ext_vector_type(8))) __bf16 hbf8;
typedef __attribute__((ext_vector_type(16))) __bf16 hbf16;
typedef __attribute__((ext_vector_type(4))) float f32x4;
typedef __attribute__((ext_vector_type(2))) float f32x2;
typedef __attribute__((ext_vector_type(8))) unsigned u32x8;
typedef __attribute__((ext_vector_type(4))) unsigned u32x4;
typedef __attribute__((ext_vector_type(2))) unsigned u32x2;
typedef __attribute__((ext_vector_type(2))) __bf16 bf16x2_t;
#define LAS __attribute__((address_space(3)))
#define GAS __attribute__((address_space(1)))
#define DEVI __device__ __forceinline__

#define PSLEEP 8
#define CPOS 34
#define WD1 1000
#define GST 550
#define NTA " nt"
#define NTB ""
#define PQ 0x077A0
#define PV 0x4AAE4
#define GSA 1600
#define QGS 8
#define VGS 8
#define MN 480
#define MD 250
#define MP 100
#define RSL 1000
#define GSD 250
#define PO 0
#define PDLY 0
#define WP 600
#define PP 100
#define STG 0
#define UPOS0 88
#define DPOS 88
#define XPF 1400
#define TPOS 33
#define XS0 40
#define XP 29
#define XB 66
#define TB 107
#define PACE 65
#define XPOS 68
#define YPOS 40
#define XPOSY 1
#define PWS 1
#define WUS 0
#define WMIX 1
#define PACE2 30
#define GDLY 80
#define QDLY 0
#define CALR 1
#define SPG 3
#define DQ 16
#define DV 12
#define BSLP 1
#define SDLY 0
#define UPOS 150
#define RDLY 45
#define NOYST 0
#define NOTOK 0
#define NOYLD 0
#define GPOSY 8
#define OLDMAP 1
#define UPODD 0
#define XLD 10
#define TROT 8
#define KOUT 1
#define RPOS 69
#define RDY 90
#define APOS 6
#define GPOS 74

#define EXP(o) ((((unsigned)(o) >> 13) << 14) | ((unsigned)(o) & 8191u))
enum { C_XN = 0, C_Y = 1, C_X1 = 2, C_XB = 3, C_H = 4, C_X2 = 5, C_BC = 6 };
#define CNT(type, s) ((unsigned)((type) * 16384 + (s) * 128))
constexpr unsigned PR_B = EXP(65536), PR_SZ = EXP(12 * 8192);
constexpr unsigned DUMMY_B = EXP(0);
constexpr int PG_MEMF = 2;
#define YXR 8
constexpr int PG_YX = 3;
constexpr int PG_XNA = PG_YX + 2 * YXR;
constexpr int PG_X1 = PG_XNA + 4, PG_X2 = PG_X1 + 4;
constexpr int PG_H = PG_X2 + 4;
constexpr int PG_YM = PG_H + 8;
constexpr int NPAGES = PG_YM + 2;
static_assert(NPAGES <= 64, "map");
constexpr unsigned CM_TAB = 0, CM_TICKET = 1024, CM_TMO = 4096, CM_IN = 8192, CM_GO = 40960, CM_BYTES = 73728;
constexpr int MASTER = 5 * 32 + 31;
constexpr unsigned SPIN_CAP = 1u << 20;
#define AUX_SC1 16
#define LDS_BYTES (160 * 1024)

struct Params {
    const u16* x; const u16* mem0; const u16* Wq; const u16* Wk; const u16* Wv; const u16* Wo;
    const u16* wg; const u16* wu; const u16* wd; u16* out; char* cm; char* ws;
};
struct Ctx {
    char* ws; char* cm; __amdgpu_buffer_rsrc_t rs; unsigned long long par;
    int c, l, w, role;
    unsigned p0, t0;
    DEVI unsigned pg(int page) const { return ((unsigned)page << 21) | (((unsigned)(par >> page) & 1u) << 13); }
    DEVI unsigned pg0(int page) const { return pg(page) ^ ((unsigned)c << 13); }
    DEVI unsigned seg(int page, int s) const { return pg(page + (s >> 4)) + (unsigned)(s & 15) * 131072u; }
    DEVI unsigned seg0(int page, int s) const { return seg(page, s) ^ ((unsigned)c << 13); }
    DEVI unsigned segx(int page, int s) const { return seg(page, s) ^ 8192u; }
    DEVI unsigned hseg(int s) const { return pg(PG_H + (s >> 3)) + (unsigned)(s & 7) * 262144u; }
    DEVI unsigned yx(int sg, int wq) const { return pg(PG_YX + (sg % YXR) * 2 + (wq >> 1)) + (unsigned)(wq & 1) * 1048576u; }
    DEVI unsigned ym(int s) const { return pg(PG_YM + ((s >> 1) & 1)) + (unsigned)(s & 1) * EXP(327680); }
    DEVI unsigned memf(int s) const { return pg(PG_MEMF) + (unsigned)s * 32768u; }
    DEVI char* cnt(int type, int s) const { return ws + (p0 + CNT(type, s)); }
    DEVI char* cnt0(int type, int s) const { return ws + ((p0 ^ ((unsigned)c << 13)) + CNT(type, s)); }
};

DEVI unsigned cvtpk(float a, float b) { return __builtin_bit_cast(unsigned, __builtin_convertvector((f32x2){a, b}, bf16x2_t)); }
DEVI unsigned packs(float a, float b) { return min(cvtpk(a, b), 0xFFFFFFFEu); }
DEVI float blo(unsigned x) { return __uint_as_float(x << 16); }
DEVI float bhi(unsigned x) { return __uint_as_float(x & 0xFFFF0000u); }
DEVI float bfr(float f) { return __uint_as_float(cvtpk(0.f, f)); }
DEVI u32x4 ld128(__amdgpu_buffer_rsrc_t rs, unsigned v, unsigned s) { return __builtin_amdgcn_raw_buffer_load_b128(rs, (int)v, (int)s, AUX_SC1); }
DEVI u32x2 ld64(__amdgpu_buffer_rsrc_t rs, unsigned v, unsigned s) { return __builtin_amdgcn_raw_buffer_load_b64(rs, (int)v, (int)s, AUX_SC1); }
DEVI unsigned ld32(__amdgpu_buffer_rsrc_t rs, unsigned v, unsigned s) { return __builtin_amdgcn_raw_buffer_load_b32(rs, (int)v, (int)s, AUX_SC1); }
DEVI void st128(__amdgpu_buffer_rsrc_t rs, unsigned v, unsigned s, u32x4 x) {
    __builtin_amdgcn_raw_buffer_store_b128(x, rs, (int)v, (int)s, AUX_SC1);
    asm volatile("s_nop 1" :: "v"(x) : "memory");
}
DEVI void st64(__amdgpu_buffer_rsrc_t rs, unsigned v, unsigned s, u32x2 x) { __builtin_amdgcn_raw_buffer_store_b64(x, rs, (int)v, (int)s, AUX_SC1); }
DEVI void st32(__amdgpu_buffer_rsrc_t rs, unsigned v, unsigned s, unsigned x) { __builtin_amdgcn_raw_buffer_store_b32(x, rs, (int)v, (int)s, AUX_SC1); }
#define RAWBAR() { __builtin_amdgcn_s_waitcnt(0xc07f); __builtin_amdgcn_s_barrier(); }
#define BARL(n) { asm volatile("" ::: "memory"); __builtin_amdgcn_s_waitcnt(0xc07f | ((n) << 8)); __builtin_amdgcn_s_barrier(); asm volatile("" ::: "memory"); }
#define DRAIN() asm volatile("s_waitcnt vmcnt(0)" ::: "memory")
#define SCHED0() __builtin_amdgcn_sched_barrier(0)
#define OOB 0x80000000u
DEVI int opq(int x) { asm volatile("" : "+v"(x)); return x; }
#define LANE const int l = opq(k.l), g = l >> 4, n = l & 15; (void)g; (void)n;
DEVI unsigned umax3(unsigned a, unsigned b, unsigned c) { return max(max(a, b), c); }
DEVI unsigned umax4(u32x4 v) { return max(umax3(v[0], v[1], v[2]), v[3]); }
DEVI unsigned rclk() { return (unsigned)__builtin_amdgcn_s_memrealtime(); }
DEVI unsigned long long rt_async() { unsigned long long t; asm volatile("s_memrealtime %0" : "=s"(t)); return t; }
DEVI void lgkm0(unsigned long long& a) { asm volatile("s_waitcnt lgkmcnt(0)" : "+s"(a) :: "memory"); }
DEVI unsigned spack(unsigned lo, unsigned hi) { unsigned d; asm volatile("s_pack_ll_b32_b16 %0, %1, %2" : "=s"(d) : "s"(lo), "s"(hi)); return d; }
DEVI int lane() { int l; asm volatile("v_mbcnt_lo_u32_b32 %0, -1, 0\n\tv_mbcnt_hi_u32_b32 %0, -1, %0" : "=v"(l)); __builtin_assume(l >= 0 && l < 64); return l; }
DEVI void sleep_until(unsigned target) { while ((int)(rclk() - target) < 0) __builtin_amdgcn_s_sleep(4); }
DEVI float xor16max(float x) { auto r = __builtin_amdgcn_permlane16_swap(__float_as_uint(x), __float_as_uint(x), false, false); return __builtin_fmaxf(__uint_as_float(r[0]), __uint_as_float(r[1])); }
DEVI float xor32max(float x) { auto r = __builtin_amdgcn_permlane32_swap(__float_as_uint(x), __float_as_uint(x), false, false); return __builtin_fmaxf(__uint_as_float(r[0]), __uint_as_float(r[1])); }
DEVI float xor16add(float x) { auto r = __builtin_amdgcn_permlane16_swap(__float_as_uint(x), __float_as_uint(x), false, false); return __uint_as_float(r[0]) + __uint_as_float(r[1]); }
DEVI float xor32add(float x) { auto r = __builtin_amdgcn_permlane32_swap(__float_as_uint(x), __float_as_uint(x), false, false); return __uint_as_float(r[0]) + __uint_as_float(r[1]); }
DEVI float fold32(float a, float b) { auto r = __builtin_amdgcn_permlane32_swap(__float_as_uint(a), __float_as_uint(b), false, false); return __uint_as_float(r[0]) + __uint_as_float(r[1]); }
#define ROR16(x, n) __builtin_bit_cast(float, __builtin_amdgcn_update_dpp(0, __builtin_bit_cast(int, (x)), 0x120 + (n), 0xf, 0xf, false))
DEVI float row16sum(float x) { x += ROR16(x, 8); x += ROR16(x, 4); x += ROR16(x, 2); x += ROR16(x, 1); return x; }
DEVI unsigned z0() { unsigned z; asm volatile("v_mov_b32 %0, 0" : "=v"(z)); return z; }
DEVI void cnt_add(char* p) { __hip_atomic_fetch_add((GAS unsigned*)(p + z0()), z0() + 1u, __ATOMIC_RELAXED, __HIP_MEMORY_SCOPE_AGENT); }
DEVI unsigned cnt_get(char* p) { return __hip_atomic_load((GAS unsigned*)(p + z0()), __ATOMIC_RELAXED, __HIP_MEMORY_SCOPE_AGENT) + 1u; }
DEVI bool spin_fail(const Ctx& k, unsigned spins) {
    if (spins > SPIN_CAP || ((spins & 255u) == 255u && __builtin_amdgcn_readfirstlane(cnt_get(k.cm + CM_TMO)) != 0)) { cnt_add(k.cm + CM_TMO); return true; }
    return false;
}
DEVI __amdgpu_buffer_rsrc_t cm_rs(const Ctx& k) { return __builtin_amdgcn_make_buffer_rsrc((void*)k.cm, 0, CM_BYTES, 0x00020000); }
DEVI void cm_st(const Ctx& k, unsigned v, unsigned off, unsigned lo, unsigned hi) { asm volatile("buffer_store_dwordx2 %0, %1, %2, %3 offen sc1" :: "v"((u32x2){lo, hi}), "v"(v), "s"(cm_rs(k)), "s"(off) : "memory"); }
DEVI u32x2 cm_ld(const Ctx& k, unsigned off) { return ld64(cm_rs(k), off, 0); }
DEVI void line_st(const Ctx& k, unsigned off, unsigned lo, unsigned hi) { cm_st(k, (lane() & 15) * 8 + (k.w ? OOB : 0u), off + k.role * 128, lo, hi); }
DEVI void hello(const Ctx& k, unsigned vote) { line_st(k, CM_IN, vote, OOB); }
DEVI u32x2 go_ask(const Ctx& k) { u32x2 d; asm volatile("buffer_load_dwordx2 %0, off, %1, %2 sc1" : "=v"(d) : "s"(cm_rs(k)), "s"(CM_GO + k.role * 128) : "memory"); return d; }
DEVI void go_wait(Ctx& k, unsigned vote, u32x2 v = {0u, 0u}) {
_Pragma("unroll 1")
    for (unsigned spins = 0; __builtin_amdgcn_readfirstlane(v[1]) >= 0 && spins < SPIN_CAP; ++spins) {
        if (spins) { hello(k, vote); __builtin_amdgcn_s_sleep(4); }
        asm volatile("buffer_load_dwordx2 %0, off, %1, %2 sc1\n\ts_waitcnt vmcnt(0)" : "=v"(v) : "s"(cm_rs(k)), "s"(CM_GO + k.role * 128) : "memory");
    }
    k.par = ((unsigned long long)(unsigned)__builtin_amdgcn_readfirstlane(v[1]) << 32 | (unsigned)__builtin_amdgcn_readfirstlane(v[0])) ^ (k.c ? ~0ull : 0ull);
    k.p0 = k.pg(0);
    __builtin_amdgcn_fence(__ATOMIC_ACQUIRE, "wavefront");
}
DEVI void cnt_wait(const Ctx& k, char* c, unsigned need) {
_Pragma("unroll 1")
    for (unsigned spins = 0; (unsigned)__builtin_amdgcn_readfirstlane(cnt_get(c)) < need; ++spins) {
        __builtin_amdgcn_s_sleep(PSLEEP);
        if (spin_fail(k, spins)) break;
    }
    __builtin_amdgcn_fence(__ATOMIC_ACQUIRE, "wavefront");
}
DEVI unsigned xn_get(const Ctx& k, unsigned l16, int s) { return ld32(k.rs, opq(l16) & 0x380u, k.p0 + CNT(C_XN, 0)); }
DEVI bool xn_has(const Ctx& k, unsigned v, int s) { return __all((int)v >= s - (k.c ? 0 : 8)); }
DEVI void xn_wait(const Ctx& k, unsigned l16, int s) {
_Pragma("unroll 1")
    for (unsigned spins = 0; !xn_has(k, xn_get(k, l16, s), s); ++spins) {
        asm volatile("" ::: "memory");
        __builtin_amdgcn_s_sleep(2);
        if (spin_fail(k, spins)) break;
    }
    __builtin_amdgcn_fence(__ATOMIC_ACQUIRE, "wavefront");
}
constexpr unsigned L_PW = 150 * 1024;
DEVI void prog_init(const Ctx& k) { *(volatile LAS unsigned*)(size_t)(L_PW + k.l * 4) = ~0u; RAWBAR() }
template <int N> DEVI void prog_wait(const Ctx& k, unsigned a, int s) {
    unsigned n, t;
    asm volatile("s_mov_b32 m0, %8\n\ts_mov_b32 %0, 0\n\ts_branch 2f\n"
        "1:\n\ts_cmp_lg_u32 %6, 0\n\ts_cbranch_scc1 3f\n\tbuffer_load_dword %2, %3, %4 offen sc1 lds\n"
        "3:\n\ts_sleep %9\n\ts_add_u32 %0, %0, 1\n\ts_and_b32 vcc_lo, %0, 0x7ff\n\ts_cbranch_scc1 2f\n\t"
        "v_mov_b32 %1, 0\n\ts_bitcmp1_b32 %0, 23\n\ts_cbranch_scc0 5f\n\tglobal_store_dword %1, %1, %10 sc1\n"
        "5:\n\tglobal_load_dword %1, %1, %10 sc1\n\ts_waitcnt vmcnt(0)\n\tv_cmp_ne_u32 vcc, -1, %1\n\ts_cbranch_vccnz 4f\n"
        "2:\n\tds_read_b32 %1, %7\n\ts_waitcnt lgkmcnt(0)\n\tv_cmp_gt_i32 vcc, %5, %1\n\ts_cbranch_vccnz 1b\n4:"
        : "=&s"(n), "=&v"(t) : "v"(min(k.l, N - 1) * 128), "s"(k.rs), "s"(a), "s"(s), "s"(k.w), "v"(L_PW + k.l * 4), "n"(L_PW), "n"(PWS), "s"(k.cm + CM_TMO) : "memory", "vcc", "scc");
}
template <int OFF> DEVI void dma1(const void* sbase, unsigned voff, unsigned lds) {
    asm volatile("s_add_i32 m0, %2, %3\n\ts_nop 0\n\tglobal_load_lds_dwordx4 %0, %1" NTA :: "v"(voff), "s"(sbase), "s"(lds), "n"(OFF) : "memory", "scc");
}
DEVI void set_m0(unsigned lds) { asm volatile("s_mov_b32 m0, %0" :: "s"(lds) : "memory"); }
template <int OFF> DEVI void dmab(__amdgpu_buffer_rsrc_t rs, unsigned voff, unsigned soff) {
    asm volatile("buffer_load_dwordx4 %0, %1, %2 offen offset:%3 sc1 lds" :: "v"(voff), "s"(rs), "s"(soff), "n"(OFF) : "memory");
}
#define PIN(x) asm volatile("" : "+v"(x))
#define PINS(x) asm volatile("" : "+s"(x))
#define LEAD()
#define TOUCH(x) asm volatile("" :: "v"(x))
DEVI float csel(float a, float b, unsigned long long m) { float d; asm volatile("v_cndmask_b32_e64 %0, %1, %2, %3" : "=v"(d) : "v"(a), "v"(b), "s"(m)); return d; }
#define LO8(x) __builtin_shufflevector(x, x, 0, 1, 2, 3, 4, 5, 6, 7)
#define HI8(x) __builtin_shufflevector(x, x, 8, 9, 10, 11, 12, 13, 14, 15)
#define CAT8(a, b) __builtin_shufflevector(a, b, 0, 1, 2, 3, 4, 5, 6, 7, 8, 9, 10, 11, 12, 13, 14, 15)
template <bool AG> DEVI void smf(f32x4& c, const bf16x8& a, const bf16x16& w, unsigned ix) {
    if constexpr (AG) asm volatile("v_smfmac_f32_16x16x64_bf16 %0, %1, %2, %3" : "+v"(c) : "v"(a), "a"(w), "v"(ix));
    else asm volatile("v_smfmac_f32_16x16x64_bf16 %0, %1, %2, %3" : "+v"(c) : "v"(a), "v"(w), "v"(ix));
}
#define WTAB(X) X(0, "{a[0:7]}", "{a[0:3]}", "{a[4:7]}") X(1, "{a[8:15]}", "{a[8:11]}", "{a[12:15]}") X(2, "{a[16:23]}", "{a[16:19]}", "{a[20:23]}") X(3, "{a[24:31]}", "{a[24:27]}", "{a[28:31]}") X(4, "{a[32:39]}", "{a[32:35]}", "{a[36:39]}") X(5, "{a[40:47]}", "{a[40:43]}", "{a[44:47]}") X(6, "{a[48:55]}", "{a[48:51]}", "{a[52:55]}") X(7, "{a[56:63]}", "{a[56:59]}", "{a[60:63]}") X(8, "{a[64:71]}", "{a[64:67]}", "{a[68:71]}") X(9, "{a[72:79]}", "{a[72:75]}", "{a[76:79]}") X(10, "{a[80:87]}", "{a[80:83]}", "{a[84:87]}") X(11, "{a[88:95]}", "{a[88:91]}", "{a[92:95]}") X(12, "{a[96:103]}", "{a[96:99]}", "{a[100:103]}") X(13, "{a[104:111]}", "{a[104:107]}", "{a[108:111]}") X(14, "{a[112:119]}", "{a[112:115]}", "{a[116:119]}") X(15, "{a[120:127]}", "{a[120:123]}", "{a[124:127]}") X(16, "{a[128:135]}", "{a[128:131]}", "{a[132:135]}") X(17, "{a[136:143]}", "{a[136:139]}", "{a[140:143]}") X(18, "{a[144:151]}", "{a[144:147]}", "{a[148:151]}") X(19, "{a[152:159]}", "{a[152:155]}", "{a[156:159]}") X(20, "{a[160:167]}", "{a[160:163]}", "{a[164:167]}") X(21, "{a[168:175]}", "{a[168:171]}", "{a[172:175]}") X(22, "{a[176:183]}", "{a[176:179]}", "{a[180:183]}") X(23, "{a[184:191]}", "{a[184:187]}", "{a[188:191]}") X(24, "{a[192:199]}", "{a[192:195]}", "{a[196:199]}") X(25, "{a[200:207]}", "{a[200:203]}", "{a[204:207]}") X(26, "{a[208:215]}", "{a[208:211]}", "{a[212:215]}") X(27, "{a[216:223]}", "{a[216:219]}", "{a[220:223]}") X(28, "{a[224:231]}", "{a[224:227]}", "{a[228:231]}") X(29, "{a[232:239]}", "{a[232:235]}", "{a[236:239]}") X(30, "{a[240:247]}", "{a[240:243]}", "{a[244:247]}") X(31, "{a[248:255]}", "{a[248:251]}", "{a[252:255]}") X(32, "{v[96:103]}", "{v[96:99]}", "{v[100:103]}") X(33, "{v[104:111]}", "{v[104:107]}", "{v[108:111]}") X(34, "{v[112:119]}", "{v[112:115]}", "{v[116:119]}") X(35, "{v[120:127]}", "{v[120:123]}", "{v[124:127]}") X(36, "{v[128:135]}", "{v[128:131]}", "{v[132:135]}") X(37, "{v[136:143]}", "{v[136:139]}", "{v[140:143]}") X(38, "{v[144:151]}", "{v[144:147]}", "{v[148:151]}") X(39, "{v[152:159]}", "{v[152:155]}", "{v[156:159]}") X(40, "{v[160:167]}", "{v[160:163]}", "{v[164:167]}") X(41, "{v[168:175]}", "{v[168:171]}", "{v[172:175]}") X(42, "{v[176:183]}", "{v[176:179]}", "{v[180:183]}") X(43, "{v[184:191]}", "{v[184:187]}", "{v[188:191]}") X(44, "{v[192:199]}", "{v[192:195]}", "{v[196:199]}") X(45, "{v[200:207]}", "{v[200:203]}", "{v[204:207]}") X(46, "{v[208:215]}", "{v[208:211]}", "{v[212:215]}") X(47, "{v[216:223]}", "{v[216:219]}", "{v[220:223]}") X(48, "{v[224:231]}", "{v[224:227]}", "{v[228:231]}") X(49, "{v[232:239]}", "{v[232:235]}", "{v[236:239]}") X(50, "{v[240:247]}", "{v[240:243]}", "{v[244:247]}") X(51, "{v[248:255]}", "{v[248:251]}", "{v[252:255]}")
template <int ID> DEVI void smfw(f32x4& c, const bf16x8& a, const bf16x16& w, unsigned ix) {
#define X(i, P, L, H) if constexpr (ID == i) asm volatile("v_smfmac_f32_16x16x64_bf16 %0, %1, %2, %3" : "+v"(c) : "v"(a), P(w), "v"(ix));
    WTAB(X)
#undef X
}
template <int ID, int HF, bool WB, bool FIRST> DEVI void mfw(f32x4& c, const bf16x16& w, const bf16x8& x) {
#define Y(R, W8) { if constexpr (FIRST && WB) asm volatile("v_mfma_f32_16x16x32_bf16 %0, %2, %1, 0" : "=&v"(c) : R(W8), "v"(x)); \
    else if constexpr (FIRST) asm volatile("v_mfma_f32_16x16x32_bf16 %0, %1, %2, 0" : "=&v"(c) : R(W8), "v"(x)); \
    else if constexpr (WB) asm volatile("v_mfma_f32_16x16x32_bf16 %0, %2, %1, %0" : "+v"(c) : R(W8), "v"(x)); \
    else asm volatile("v_mfma_f32_16x16x32_bf16 %0, %1, %2, %0" : "+v"(c) : R(W8), "v"(x)); }
#define X(i, P, L, H) if constexpr (ID == i) { if constexpr (HF) Y(H, HI8(w)) else Y(L, LO8(w)) }
    WTAB(X)
#undef X
#undef Y
}
template <bool AG, bool FIRST> DEVI void mfa(f32x4& c, const bf16x8& w, const bf16x8& x) {
    if constexpr (AG && FIRST) asm volatile("v_mfma_f32_16x16x32_bf16 %0, %1, %2, 0" : "=&v"(c) : "a"(w), "v"(x));
    else if constexpr (AG) asm volatile("v_mfma_f32_16x16x32_bf16 %0, %1, %2, %0" : "+v"(c) : "a"(w), "v"(x));
    else if constexpr (FIRST) asm volatile("v_mfma_f32_16x16x32_bf16 %0, %1, %2, 0" : "=&v"(c) : "v"(w), "v"(x));
    else asm volatile("v_mfma_f32_16x16x32_bf16 %0, %1, %2, %0" : "+v"(c) : "v"(w), "v"(x));
}
template <bool AG, bool FIRST> DEVI void mfb(f32x4& c, const bf16x8& x, const bf16x8& w) {
    if constexpr (AG && FIRST) asm volatile("v_mfma_f32_16x16x32_bf16 %0, %1, %2, 0" : "=&v"(c) : "v"(x), "a"(w));
    else if constexpr (AG) asm volatile("v_mfma_f32_16x16x32_bf16 %0, %1, %2, %0" : "+v"(c) : "v"(x), "a"(w));
    else if constexpr (FIRST) asm volatile("v_mfma_f32_16x16x32_bf16 %0, %1, %2, 0" : "=&v"(c) : "v"(x), "v"(w));
    else asm volatile("v_mfma_f32_16x16x32_bf16 %0, %1, %2, %0" : "+v"(c) : "v"(x), "v"(w));
}
DEVI f32x4 mf(const bf16x8& a, const bf16x8& b, const f32x4& c) { return __builtin_amdgcn_mfma_f32_16x16x32_bf16(__builtin_bit_cast(hbf8, a), __builtin_bit_cast(hbf8, b), c, 0, 0, 0); }
template <int N, class F> DEVI void sfor(F f) { [&]<int... I>(std::integer_sequence<int, I...>) { (f(std::integral_constant<int, I>{}), ...); }(std::make_integer_sequence<int, N>{}); }
DEVI bf16x8 lds128(const char* p) { return *(const bf16x8*)p; }
DEVI bf16x4 ldstr(const char* p) { return __builtin_amdgcn_ds_read_tr16_b64_v4i16((LAS bf16x4*)p); }
DEVI bf16x8 ldstr2(const char* p, int d) { bf16x4 a = ldstr(p), b = ldstr(p + d); return __builtin_shufflevector(a, b, 0, 1, 2, 3, 4, 5, 6, 7); }

#define STAMP(s, k)
#define QSTAMP(f, i)
#define STOP(s)
#define PFLUSH(s)
#define PROF_DECL
#define RSTAMP(i)
#define XSTAMP(i)
#define PCOUNT(i)
#define PROF_OUT

#define HSTAMP STAMP
#define HFLUSH PFLUSH
#define HTRACE(s)
#define TROUT
#define TRHINT
#define TPQ(n)
#define TPV(n)
#define AQ(m, i)
#define AC3(i, j, k) { }
#define AST(i)
#define AAC(i)
#define ACCM 0
#define HSTOP(m) if constexpr ((ACCM != 0) == m) { STOP(0) }
#define PC8(i, x)
#define PROF_ONLY(x)
#define PROF_S(x)
template <int RB, bool STD> DEVI int swz(int row) { return RB == 384 ? ((row >> 1) & 1) | ((row >> 2) & 2) : RB == 128 ? (row >> 1) & 3 : STD ? (row & 3) | ((row >> 1) & 4) : row & 7; }
template <int RB, bool STD> DEVI unsigned wsrc(unsigned id, int ld) { const unsigned row = id / (RB / 16), c = id % (RB / 16); return row * ld + ((c ^ (swz<RB, STD>(row) << 1)) << 4); }
template <int RB, bool STD> DEVI int wp_adr(int T, int l) { const int row = (STD ? 8 : 4) * (l >> 4) + ((l >> 2) & 3); return row * RB + ((T ^ swz<RB, STD>(row)) << 5) + (l & 3) * 8; }
template <int RB, bool STD> DEVI bf16x8 wp_read(const char* blk, int T, int l) { return ldstr2(blk + wp_adr<RB, STD>(T, l), (STD ? 4 : 16) * RB); }
template <int NU, class CNT, class STEP> DEVI void cstream(CNT cnt, STEP step) {
    sfor<NU>([&](auto V) { constexpr int v = decltype(V)::value;
        asm volatile("s_waitcnt vmcnt(%0) lgkmcnt(0)\n\ts_barrier" :: "n"(decltype(cnt(V))::value * (v ? 1 : 2)) : "memory");
        step(V);
    });
}
template <int NU, int NI, int D, class ISSUE, class READ> DEVI void wstream(char* ring, ISSUE issue, READ read) {
    const unsigned base = (unsigned)(size_t)(LAS char*)ring;
    sfor<D>([&](auto U) { constexpr int u = decltype(U)::value; if constexpr (u < NU) issue(U, base); });
    sfor<NU>([&](auto U) {
        constexpr int u = decltype(U)::value, left = (NU - u < D ? NU - u : D) - 1;
        asm volatile("s_waitcnt vmcnt(%0)" :: "n"(left * NI) : "memory");
        read(U, ring + (u % D) * NI * 1024);
        __builtin_amdgcn_s_waitcnt(0xc07f);
        asm volatile("" ::: "memory");
        if constexpr (u + D < NU) issue(std::integral_constant<int, u + D>{}, base);
    });
}

constexpr int L_XN = 0;
constexpr int L_K = 40960;
constexpr int L_MEM = L_K + 12288;
constexpr int L_QM = L_MEM + 10240;
constexpr int L_Q = L_QM + 12288;
constexpr int L_W = 94208;
#define SLOT(i) ((i) % 3 == 0 ? 45056 : (i) % 3 == 1 ? 69632 : L_W + 40960)
#define CMIN(a, b) ((a) < (b) ? (a) : (b))
#define IC(x) std::integral_constant<int, (x)>{}
#define CV(X) decltype(X)::value
DEVI bf16x8 lr128(unsigned a) { return *(const LAS bf16x8*)(size_t)a; }
DEVI bf16x4 lrtr(unsigned a) { return __builtin_amdgcn_ds_read_tr16_b64_v4i16((LAS bf16x4*)(size_t)a); }
#define CAT4(a, b) __builtin_shufflevector(a, b, 0, 1, 2, 3, 4, 5, 6, 7)
DEVI void lw128(unsigned a, u32x4 v) { *(LAS u32x4*)(size_t)a = v; }
DEVI void lw64(unsigned a, u32x2 v) { *(LAS u32x2*)(size_t)a = v; }
DEVI void lw32(unsigned a, unsigned v) { *(LAS unsigned*)(size_t)a = v; }
template <int N> DEVI void bar_lgkm() { asm volatile("s_waitcnt lgkmcnt(%0)\n\ts_barrier" :: "n"(N) : "memory"); }
DEVI void atom1(__amdgpu_buffer_rsrc_t rs, unsigned voff, unsigned soff) {
    unsigned one; asm volatile("v_mov_b32 %0, 1\n\tbuffer_atomic_add %0, %1, %2, %3 offen" : "=&v"(one) : "v"(voff), "s"(rs), "s"(soff) : "memory");
}
DEVI unsigned vadd(unsigned v, unsigned sc) { unsigned d; asm volatile("v_add_u32 %0, %1, %2" : "=v"(d) : "s"(sc), "v"(v)); return d; }
DEVI float vzero() { float z; asm volatile("v_mov_b32 %0, 0" : "=v"(z)); return z; }
template <int S> DEVI unsigned slot_adr(unsigned a) { if constexpr (S % 3 == 2) return vadd(a, SLOT(2) - SLOT(0)); else return a + (SLOT(S) - SLOT(0)); }
template <int B, int I> DEVI void xn_op(const Ctx& k, unsigned v, unsigned src, unsigned dst) {
    if constexpr (I == 0) set_m0(dst + B * 8192);
    else if constexpr (I == 1) dmab<0>(k.rs, v, src + B * 16384);
    else dmab<1024>(k.rs, v, src + B * 16384);
}
DEVI void xn_all(const Ctx& k, unsigned v, unsigned src, unsigned dst) {
    sfor<5>([&](auto B) { xn_op<CV(B), 0>(k, v, src, dst); asm volatile("s_nop 0"); xn_op<CV(B), 1>(k, v, src, dst); xn_op<CV(B), 2>(k, v, src, dst); });
}
struct Xn {
    unsigned src, dst, sg, need, t, sb, se, l16, c, off;
    unsigned long long flip, m;
    DEVI void init(const Ctx& k) {
        src = k.seg(PG_XNA, 0) + k.w * 2048; dst = L_XN + k.w * 2048; sg = 0; off = ~0u;
        const unsigned q = (unsigned)(k.par >> PG_XNA), d = q ^ (q >> 1);
        flip = ((unsigned long long)(d & 1) << 16) | ((unsigned long long)(d & 2) << 31) | ((unsigned long long)(d & 4) << 46);
    }
    template <int I> DEVI void next(const Ctx& k, unsigned l16) {
        if constexpr (I == 0) { sg += 1; src += 131072; PINS(sg); PINS(src); }
        if constexpr (I == 1) asm volatile("s_bitcmp1_b64 %1, %2\n\ts_cselect_b32 %0, 0x2000, 0" : "=s"(t) : "s"(flip), "s"(sg) : "scc");
        if constexpr (I == 2) { src ^= t; PINS(src); }
        if constexpr (I == 3) asm volatile("v_and_b32 %0, 0x380, %1" : "=v"(c) : "v"(l16));
        if constexpr (I == 4) c = ld32(k.rs, c, k.p0 + CNT(C_XN, 0));
        if constexpr (I == 5) { need = sg - (k.c ? 0u : 8u); PINS(need); }
        if constexpr (I == 6) { need = sg < 64u ? need : 0x7FFFFFFFu; PINS(need); }
        if constexpr (I == 7) asm volatile("v_cmp_gt_i32_e64 %0, %1, %2" : "=s"(m) : "s"(need), "v"(c) : "memory");
        if constexpr (I == 8) asm volatile("s_cmp_lg_u64 %2, 0\n\ts_cselect_b32 %0, -1, 0\n\ts_cselect_b32 %1, 0x200000, %3" : "=&s"(off), "=s"(se) : "s"(m), "s"(src) : "scc");
        if constexpr (I == 13) set_m0(L_MEM);
        if constexpr (I == 14) dmab<0>(k.rs, c, k.p0 + CNT(C_XN, 0));
        if constexpr (I == 15) c = *(volatile LAS unsigned*)(size_t)(l16 + L_MEM);
        if constexpr (I == 11) { need = sg < 64u ? need : 0x80000000u; PINS(need); }
        if constexpr (I == 12) {
            if (__builtin_expect(m != 0, 0)) {
_Pragma("unroll 1")
                for (unsigned spins = 0; __any((int)*(volatile LAS unsigned*)(size_t)(l16 + L_MEM) < (int)need) && !spin_fail(k, spins >> 2); ++spins) { next<3>(k, l16); __builtin_amdgcn_s_sleep(1); next<14>(k, l16); }
            }
            se = src; }
        if constexpr (I == 10) asm volatile("s_cmp_lg_u32 %1, 64\n\ts_cselect_b32 %0, %0, 0" : "+s"(off) : "s"(sg) : "scc");
    }
    template <int B, int I> DEVI void op(const Ctx& k) {
        if constexpr (I == 0) { set_m0(dst + B * 8192); sb = se + B * 16384; PINS(sb); }
        else dmab<(I - 1) * 1024>(k.rs, l16, sb);
    }
    template <int NB> DEVI void late(const Ctx& k, unsigned l16) {
        xn_wait(k, l16, sg);
        se = src;
        sfor<NB>([&](auto B) { op<CV(B), 0>(k); asm volatile("s_nop 0"); op<CV(B), 1>(k); op<CV(B), 2>(k); });
        DRAIN();
    }
};
template <int OFF> DEVI u32x4 ald128(__amdgpu_buffer_rsrc_t rs, unsigned v, unsigned s) { u32x4 d; asm volatile("buffer_load_dwordx4 %0, %1, %2, %3 offen offset:%4 sc1" : "=v"(d) : "v"(v), "s"(rs), "s"(s), "n"(OFF) : "memory"); return d; }
#define MADR(b) (L_MEM + ((b) < 6 ? ((b) - 1) * 2048 : ((b) - 6) * 2048 + 3072))
struct Gather {
    u32x4 p0, p1, v0, v1;
    unsigned mx, a, aw, b, base, wo, dst;
    unsigned long long bad;
    DEVI void init(const Ctx& k) { base = k.memf(0); wo = k.w * 1024; dst = L_MEM + k.w * 2048; }
    template <int I> DEVI void op(const Ctx& k, unsigned l16, int s) {
        if constexpr (I == 0) { a = ((unsigned)s << 15) & 0x1F8000u; PINS(a); }
        if constexpr (I == 1) { a += base; aw = a + wo; PINS(a); PINS(aw); }
        if constexpr (I == 2) { b = aw + 3072 + (k.w >> 1) * 8192; PINS(b); set_m0(dst); }
        if constexpr (I == 3) v0 = ald128<2048>(k.rs, l16, aw);
        if constexpr (I == 4) v1 = ald128<3072>(k.rs, l16, b);
        if constexpr (I == 5) dmab<2048>(k.rs, l16, aw);
        if constexpr (I == 6) dmab<3072>(k.rs, l16, b);
        if constexpr (I == 7) p0 = ald128<0>(k.rs, l16, a);
        if constexpr (I == 8) p1 = ald128<1024>(k.rs, l16, a);
    }
    template <int N> DEVI void wait() { asm volatile("s_waitcnt vmcnt(%4)" : "+v"(p0), "+v"(p1), "+v"(v0), "+v"(v1) : "n"(N) : "memory"); }
    template <int I> DEVI void fin(unsigned other) {
#define MX3 "v_max3_u32 %0, %0, %3, %4\n\tv_max3_u32 %0, %0, %5, %6"
        if constexpr (I == 0) asm volatile("v_max3_u32 %0, %7, %1, %2\n\t" MX3 : "=&v"(mx) : "v"(v0[0]), "v"(v0[1]), "v"(v0[2]), "v"(v0[3]), "v"(v1[0]), "v"(v1[1]), "s"(other));
        if constexpr (I == 1) asm volatile("v_max3_u32 %0, %0, %1, %2\n\t" MX3 : "+v"(mx) : "v"(v1[2]), "v"(v1[3]), "v"(p0[0]), "v"(p0[1]), "v"(p0[2]), "v"(p0[3]));
        if constexpr (I == 2) asm volatile("v_max3_u32 %1, %1, %2, %3\n\tv_max3_u32 %1, %1, %4, %5\n\tv_cmp_eq_u32_e64 %0, -1, %1" : "=s"(bad) : "v"(mx), "v"(p1[0]), "v"(p1[1]), "v"(p1[2]), "v"(p1[3]));
    }
    DEVI void also(unsigned other) { asm volatile("v_mov_b32 %0, %1" : "=v"(mx) : "s"(other)); }
    DEVI void all() {
        asm volatile("s_waitcnt vmcnt(0)\n\tv_max3_u32 %1, %1, %2, %3\n\tv_max3_u32 %1, %1, %4, %5\n\tv_max3_u32 %1, %1, %6, %7\n\tv_max3_u32 %1, %1, %8, %9\n\tv_max3_u32 %1, %1, %10, %11\n\tv_max3_u32 %1, %1, %12, %13\n\t"
            "v_max3_u32 %1, %1, %14, %15\n\tv_max3_u32 %1, %1, %16, %17\n\tv_cmp_eq_u32_e64 %0, -1, %1" : "=s"(bad) : "v"(mx), "v"(v0[0]), "v"(v0[1]), "v"(v0[2]), "v"(v0[3]), "v"(v1[0]), "v"(v1[1]), "v"(v1[2]), "v"(v1[3]),
            "v"(p0[0]), "v"(p0[1]), "v"(p0[2]), "v"(p0[3]), "v"(p1[0]), "v"(p1[1]), "v"(p1[2]), "v"(p1[3]) : "memory");
    }
    DEVI void check() { also(0); all(); asm volatile("s_nop 3"); }
    unsigned rv, rs; unsigned long long rt;
    template <int I> DEVI void ask(const Ctx& k, unsigned l16) {
        if constexpr (I == 0) asm volatile("v_and_b32 %0, 0xf0, %1\n\tv_lshlrev_b32 %0, 1, %0" : "=v"(rv) : "v"(l16));
        if constexpr (I == 1) asm volatile("buffer_load_dword %0, %0, %1, %2 offen sc1" : "+v"(rv) : "s"(k.rs), "s"(k.p0 + CNT(C_XB, 0)) : "memory");
    }
    template <int I, int N = 4> DEVI void red() {
        if constexpr (I == 0) asm volatile("s_waitcnt vmcnt(%1)\n\tv_max_u32_dpp %0, %0, %0 quad_perm:[1,0,3,2] row_mask:0xf bank_mask:0xf" : "+v"(rv) : "n"(N));
        if constexpr (I == 1) asm volatile("v_max_u32_dpp %0, %0, %0 quad_perm:[2,3,0,1] row_mask:0xf bank_mask:0xf" : "+v"(rv));
        if constexpr (I == 2) asm volatile("v_max_u32_dpp %0, %0, %0 row_ror:4 row_mask:0xf bank_mask:0xf" : "+v"(rv));
        if constexpr (I == 3) asm volatile("v_max_u32_dpp %0, %0, %0 row_ror:8 row_mask:0xf bank_mask:0xf" : "+v"(rv));
        if constexpr (I == 4) asm volatile("v_readfirstlane_b32 %0, %1" : "=s"(rs) : "v"(rv));
    }
    template <int I> DEVI void hold(unsigned step) {
        if constexpr (I == 0) rt = rt_async();
        if constexpr (I == 3) { PINS(rt); b = spack((unsigned)rt, step); }
        if constexpr (I == 4) asm volatile("s_sub_u32 %0, %1, %0" : "+s"(b) : "s"(rs) : "scc");
        if constexpr (I == 5) if (__builtin_expect(b < 128u, 0)) { while ((int)((rclk() - rs) << 16) < 0) { } }
    }
    template <int I> DEVI void at(const Ctx& k, unsigned l16) {
        if constexpr (I == 1) op<2>(k, l16, 0);
        if constexpr (I >= 2 && I < 15 && (I % 5 == 2 || I % 5 == 4)) op<3 + (I / 5) * 2 + (I % 5) / 4>(k, l16, 0);
    }
    DEVI void again(const Ctx& k, unsigned l16) {
        asm volatile("s_mov_b32 m0, %9\n\tbuffer_load_dwordx4 %2, %4, %5, %7 offen offset:2048 sc1\n\tbuffer_load_dwordx4 %3, %4, %5, %8 offen offset:3072 sc1\n\t"
            "buffer_load_dwordx4 %4, %5, %7 offen offset:2048 sc1 lds\n\tbuffer_load_dwordx4 %4, %5, %8 offen offset:3072 sc1 lds\n\t"
            "buffer_load_dwordx4 %0, %4, %5, %6 offen sc1\n\tbuffer_load_dwordx4 %1, %4, %5, %6 offen offset:1024 sc1\n\ts_waitcnt vmcnt(0)"
            : "+v"(p0), "+v"(p1), "+v"(v0), "+v"(v1) : "v"(l16), "s"(k.rs), "s"(a), "s"(aw), "s"(b), "s"(dst) : "memory");
        check();
    }
};
DEVI void clr2(f32x4& c) { f32x2 a, b; asm volatile("v_mov_b64 %0, 0\n\tv_mov_b64 %1, 0" : "=v"(a), "=v"(b)); c = __builtin_shufflevector(a, b, 0, 1, 2, 3); }
#define NOP7_3(a) asm volatile("s_nop 7" : "+v"(a[0]), "+v"(a[1]), "+v"(a[2]))

struct G3 {
    f32x4 a0[3], a1[3];
    bf16x8 x0[20], x1[20], wl[20];
    unsigned l16, lwv;
    template <int KS> DEVI void r0() { x0[KS] = lr128(l16 + L_XN + KS * 2048); }
    template <int KS> DEVI void r1() { x1[KS] = lr128(l16 + L_XN + KS * 2048 + 1024); }
    template <int KS> DEVI void rw() { if constexpr (KS >= 4) wl[KS] = lr128(lwv + (KS - 4) * 4096); }
    template <bool WL, int KS> DEVI void pre() { if constexpr (WL && KS >= 4) asm volatile("" :: "v"(wl[KS]), "v"(x0[KS]), "v"(x1[KS])); else asm volatile("" :: "v"(x0[KS]), "v"(x1[KS])); }
    template <int ID0, bool WL, int ks, int m> DEVI void mm(const bf16x16 (&W)[52]) {
        constexpr int b = ks >> 1, t = m % 3, id = ID0 + t * 10 + b;
        f32x4& c = m < 3 ? a0[t] : a1[t];
        const bf16x8& x = m < 3 ? x0[ks] : x1[ks];
        if constexpr (WL && t == 2 && b >= 2) { if constexpr (ID0 == 30) mfb<false, false>(c, x, wl[ks]); else mfa<false, false>(c, wl[ks], x); }
        else mfw<id % 52, ks & 1, ID0 == 30, ks == 0>(c, W[id % 52], x);
        SCHED0();
    }
    template <int ID0, bool WL, int K0, int K1, int PF, bool H6 = false, class HOOK> DEVI void run(const bf16x16 (&W)[52], HOOK hook) {
        sfor<K1 - K0>([&](auto KK) {
            constexpr int kk = CV(KK), ks = K0 + kk;
            sfor<6>([&](auto M) {
                constexpr int m = CV(M);
                mm<ID0, WL, ks, m>(W);
                if constexpr (m == 5 && ks + 1 < K1) { pre<WL, (ks + 1) % 20>(); SCHED0(); }
                if constexpr (ks + 2 < PF) {
                    if constexpr (m == 0) r0<(ks + 2) % 20>();
                    if constexpr (m == 1) r1<(ks + 2) % 20>();
                    if constexpr (m == 3 && WL) rw<(ks + 2) % 20>();
                }
                if constexpr (H6) hook(IC(kk * 6 + m));
                else { hook(IC(kk * 7 + (m < 5 ? m : 6))); if constexpr (m == 4) hook(IC(kk * 7 + 5)); }
                SCHED0();
            });
        });
    }
    template <int ID0, bool WL, int F0, int F1, int PF, class HOOK> DEVI void go(const bf16x16 (&W)[52], HOOK hook) {
        sfor<F1 - F0>([&](auto FF) {
            constexpr int f = F0 + CV(FF), ks = f / 6, m = f % 6;
            mm<ID0, WL, ks, m>(W);
            if constexpr (m == 5 && f + 1 < F1) { pre<WL, (ks + 1) % 20>(); SCHED0(); }
            if constexpr (ks + 2 < PF) rd<WL, (f + 12) % 120>();
            hook(IC(f));
            SCHED0();
        });
    }
    template <int P> DEVI void home() {
        if constexpr (P == 0) asm volatile("" : "+{v[12:15]}"(a0[0]), "+{v[16:19]}"(a0[1]), "+{v[20:23]}"(a0[2]), "+{v[24:27]}"(a1[0]), "+{v[28:31]}"(a1[1]), "+{v[32:35]}"(a1[2]));
        else asm volatile("" : "+{v[36:39]}"(a0[0]), "+{v[40:43]}"(a0[1]), "+{v[44:47]}"(a0[2]), "+{v[48:51]}"(a1[0]), "+{v[52:55]}"(a1[1]), "+{v[56:59]}"(a1[2]));
    }
    template <bool WL, int F> DEVI void rd() {
        if constexpr (F % 6 == 0) r0<F / 6>();
        if constexpr (F % 6 == 1) r1<F / 6>();
        if constexpr (F % 6 == 3 && WL) rw<F / 6>();
    }
    u32x2 o;
    template <int I, int OFF> DEVI void out4(unsigned d0) {
        if constexpr (I >= 0 && I < 24) {
            constexpr int t = I / 8, rt = (I / 4) & 1, j = I & 1;
            const f32x4& a = rt ? a1[t] : a0[t];
            if constexpr (I % 4 < 2) { o[j] = cvtpk(a[2 * j], a[2 * j + 1]); PIN(o[j]); }
            else lw32(d0 + OFF + rt * 6144 + t * 1024 + j * 4, o[j]);
        }
    }
    template <int I, int OFF> DEVI void out(unsigned d0, unsigned d1) {
        if constexpr (I >= 0 && I < 18) {
            constexpr int t = I / 6, rt = (I / 3) & 1;
            const f32x4& a = rt ? a1[t] : a0[t];
            if constexpr (I % 3 == 0) { o[0] = cvtpk(a[0], a[1]); PIN(o[0]); }
            else if constexpr (I % 3 == 1) { o[1] = cvtpk(a[2], a[3]); PIN(o[1]); }
            else if constexpr (OFF > 0) lw64(d0 + (OFF - 1) + rt * 6144 + t * 1024, o);
            else lw64((t == 1 ? d1 : d0) + OFF + rt * 6144 + (t == 2 ? 1024 : 0), o);
        }
    }
};
#define SPIX(l) (((l) & 2) ? 0xEEEEu : 0x4444u)
template <int I> DEVI void fold_op(f32x4& a, unsigned& o) {
    if constexpr (I == 0) asm volatile("v_add_f32 %0, %0, %1" : "+v"(a[0]) : "v"(a[2]));
    if constexpr (I == 1) asm volatile("v_add_f32 %0, %0, %1" : "+v"(a[1]) : "v"(a[3]));
    if constexpr (I == 2) asm volatile("v_cvt_pk_bf16_f32 %0, %1, %2" : "=v"(o) : "v"(a[0]), "v"(a[1]));
}

template <int J> DEVI void fold2(f32x4& a, f32x4& b, unsigned (&o)[2]) {
    if constexpr (J < 2) asm volatile("v_add_f32 %0, %0, %2\n\tv_add_f32 %1, %1, %3" : "+v"(a[J]), "+v"(b[J]) : "v"(a[J + 2]), "v"(b[J + 2]));
    else asm volatile("v_cvt_pk_bf16_f32 %0, %2, %3\n\tv_cvt_pk_bf16_f32 %1, %4, %5" : "=&v"(o[0]), "=&v"(o[1]) : "v"(a[0]), "v"(a[1]), "v"(b[0]), "v"(b[1]));
}
DEVI unsigned vshl1(unsigned x, unsigned b) { unsigned d; asm volatile("v_lshl_or_b32 %0, %1, 1, %2" : "=v"(d) : "s"(x), "n"(b)); return d; }

#define ANNOUNCE(s, now, d) (((unsigned)(s) << 16) | (((unsigned)(now) + (d)) & 0xFFFFu))

template <class PUT> DEVI void ln_rows(const u32x4 (&a)[5], int n, int row  , PUT put) {
    float v[40];
#pragma unroll
    for (int i = 0; i < 20; ++i) { v[2 * i] = blo(a[i >> 2][i & 3]); v[2 * i + 1] = bhi(a[i >> 2][i & 3]); }
    f32x2 s2[4] = {{0.f, 0.f}, {0.f, 0.f}, {0.f, 0.f}, {0.f, 0.f}};
#pragma unroll
    for (int i = 0; i < 40; i += 2) s2[(i >> 1) & 3] += (f32x2){v[i], v[i + 1]};
    const f32x2 st = (s2[0] + s2[1]) + (s2[2] + s2[3]);
    float m = row16sum(st[0] + st[1]) * (1.f / 640.f);
    f32x2 q2[4] = {{0.f, 0.f}, {0.f, 0.f}, {0.f, 0.f}, {0.f, 0.f}};
#pragma unroll
    for (int i = 0; i < 40; i += 2) { const f32x2 d = (f32x2){v[i], v[i + 1]} - (f32x2){m, m}; q2[(i >> 1) & 3] += d * d; }
    const f32x2 qt = (q2[0] + q2[1]) + (q2[2] + q2[3]);
    const float q = row16sum(qt[0] + qt[1]);
    const float var = bfr(q * (1.f / 640.f));
    m = bfr(m);
    const float rden = __builtin_amdgcn_rcpf(bfr(__builtin_amdgcn_sqrtf(bfr(var + 1e-5f))));
    const unsigned vo = (n >> 2) * 2048 + (row >> 4) * 1024 + ((n & 3) * 16 + (row & 15)) * 16;
#pragma unroll
    for (int c = 0; c < 5; ++c) {
        u32x4 o;
#pragma unroll
        for (int i = 0; i < 4; ++i) { unsigned d = cvtpk(v[c * 8 + 2 * i] - m, v[c * 8 + 2 * i + 1] - m); o[i] = cvtpk(blo(d) * rden, bhi(d) * rden); }
        put(vo, c, o);
    }
}

struct Soft {
    unsigned long long mA, mY, mC[8];
    unsigned v0, v1;
    unsigned sg, hs;
    float a[6], m, M, t, s, h; u32x2 pk;
    template <int I> DEVI void mask(f32x4 (&sc)[3]) {
        if constexpr (I == 0) asm volatile("v_mov_b32 %0, 0xfe800000" : "=v"(t));
        if constexpr (I >= 1 && I < 9) { float d; asm volatile("v_cndmask_b32_e64 %0, %1, 0, %2" : "=v"(d) : "v"(t), "s"(mC[I - 1])); sc[(I - 1) >> 2][(I - 1) & 3] = d; }
    }
    template <int I> DEVI void op(const Ctx& k, f32x4 (&sc)[3], unsigned a0, unsigned a1, unsigned hv) {
#define FMA(x, y) "v_fmamk_f32 " x ", " x ", 0x3dd53b95, " y "\n\t"
#define EXPC(x) "v_exp_f32_e64 " x ", " x " clamp\n\t"
#define OP3(o, d, x, y) o " " d ", " x ", " y "\n\t"
#define SA(txt) asm volatile(txt : "+v"(a[0]), "+v"(a[1]), "+v"(a[2]), "+v"(a[3]), "+v"(m), "+v"(t))
#define SB(txt) asm volatile(txt : "+v"(a[0]), "+v"(a[1]), "+v"(a[2]), "+v"(a[3]), "+v"(m), "+v"(t), "+v"(s), "+v"(a[4]), "+v"(a[5]), "+v"(M), "+v"(h) : "s"(hs), "s"(sg))
#define ADD(d, x, y) OP3("v_add_f32", d, x, y)
#define MUL(d, x, y) OP3("v_mul_f32", d, x, y)
#define CND(d, x, y, c) "v_cndmask_b32_e64 " d ", " x ", " y ", " c "\n\t"
        if constexpr (I == 0) asm volatile("v_max3_f32 %0, %1, %2, %3\n\tv_max3_f32 %0, %0, %4, %5" : "=&v"(m) : "v"(sc[0][0]), "v"(sc[0][1]), "v"(sc[0][2]), "v"(sc[0][3]), "v"(sc[1][0]));
        if constexpr (I == 1) asm volatile("v_max3_f32 %0, %0, %1, %2" : "+v"(m) : "v"(sc[1][1]), "v"(sc[1][2]));
        if constexpr (I == 2) asm volatile(CND("%0", "%2", "%3", "%6") CND("%1", "%4", "%5", "%6") : "=&v"(a[2]), "=&v"(a[3]) : "v"(sc[0][2]), "v"(sc[1][0]), "v"(sc[0][3]), "v"(sc[1][1]), "s"(mY));
        if constexpr (I == 3) asm volatile("v_max_f32 %1, %1, %5\n\t" CND("%0", "%2", "%3", "%4") : "=&v"(a[0]), "+v"(m) : "v"(sc[0][0]), "v"(a[2]), "s"(mA), "v"(sc[1][3]));
        if constexpr (I == 4) asm volatile("v_mul_f32 %0, 0xbdd53b95, %1" : "=v"(t) : "v"(m));
        if constexpr (I == 5) asm volatile(CND("%0", "%2", "%3", "%5") CND("%1", "%1", "%4", "%5") : "=&v"(a[1]), "+v"(a[2]) : "v"(sc[0][1]), "v"(a[3]), "v"(sc[1][2]), "s"(mA));
        if constexpr (I == 6) asm volatile(CND("%0", "%0", "%2", "%3") FMA("%1", "%4") : "+v"(a[3]), "+v"(a[0]) : "v"(sc[1][3]), "s"(mA), "v"(t));
        if constexpr (I == 7) SA(FMA("%1", "%5"));
        if constexpr (I == 8) SA(FMA("%2", "%5") FMA("%3", "%5"));
        if constexpr (I == 9) SA(EXPC("%0"));
        if constexpr (I == 10) { asm volatile("v_max3_f32 %0, %1, %2, %3" : "=v"(M) : "v"(m), "v"(sc[2][0]), "v"(sc[2][1]), "v"(sc[2])); a[4] = sc[2][0]; a[5] = sc[2][1]; }
        if constexpr (I == 11) asm volatile("v_mov_b32 %0, %3\n\tv_mov_b32 %1, %4" : "=&v"(t), "=&v"(h), "=v"(s) : "v"(M), "s"(hv));
        if constexpr (I == 12) SB("v_permlane16_swap_b32 %9, %5");
        if constexpr (I == 13) SB(OP3("v_max_f32", "%9", "%9", "%5"));
        if constexpr (I == 14) SB(EXPC("%1"));
        if constexpr (I == 15) SB("v_mov_b32 %5, %9\n\t" ADD("%6", "%0", "%1"));
        if constexpr (I == 16) SB("v_permlane32_swap_b32 %9, %5");
        if constexpr (I == 17) SB(OP3("v_max_f32", "%9", "%9", "%5") "v_mul_f32 %9, 0xbdd53b95, %9");
        if constexpr (I == 18) SB(FMA("%7", "%9") FMA("%8", "%9"));
        if constexpr (I == 19) SB(FMA("%4", "%9"));
        if constexpr (I == 20) SB(EXPC("%2"));
        if constexpr (I == 21) SB(EXPC("%3"));
        if constexpr (I == 22) SB(ADD("%6", "%6", "%2"));
        if constexpr (I == 23) SB(EXPC("%7"));
        if constexpr (I == 24) SB(EXPC("%8"));
        if constexpr (I == 25) SB(ADD("%6", "%6", "%3"));
        if constexpr (I == 26) SB(EXPC("%4"));
        if constexpr (I == 27) SB(MUL("%6", "%12", "%6") ADD("%5", "%7", "%8"));
        if constexpr (I == 28) SB(MUL("%6", "%6", "%4"));
        if constexpr (I == 29) SB(OP3("v_fmac_f32", "%6", "%11", "%5"));
        if constexpr (I == 30) SB("v_fmac_f32_dpp %6, %6, %10 row_ror:8 row_mask:0xf bank_mask:0xf\n\tv_mov_b32 %5, %6");
        if constexpr (I == 31) SB("v_permlane16_swap_b32 %6, %5");
        if constexpr (I == 32) SB(ADD("%6", "%6", "%5") "v_mov_b32 %5, %6");
        if constexpr (I == 33) SB("v_permlane32_swap_b32 %6, %5");
        if constexpr (I == 34) SB(ADD("%6", "%6", "%5"));
        if constexpr (I == 35) SB("v_rcp_f32 %6, %6");
        if constexpr (I == 36) SB(MUL("%4", "%4", "%6") MUL("%7", "%7", "%6"));
        if constexpr (I == 37) SB(MUL("%0", "%0", "%4"));
        if constexpr (I == 38) SB(MUL("%1", "%1", "%4") MUL("%2", "%2", "%4"));
        if constexpr (I == 39) { unsigned x; asm volatile(MUL("%1", "%1", "%4") "v_cvt_pk_bf16_f32 %0, %2, %3" : "=&v"(x), "+v"(a[3]) : "v"(a[0]), "v"(a[1]), "v"(m)); pk[0] = x; }
        if constexpr (I == 40) { unsigned x; asm volatile("v_cvt_pk_bf16_f32 %0, %1, %2" : "=v"(x) : "v"(a[2]), "v"(a[3])); pk[1] = x; }
        if constexpr (I == 41) { st64(k.rs, v0, a0, pk); SCHED0(); asm volatile("v_mul_f32 %1, %1, %2\n\tv_cvt_pk_bf16_f32 %0, %0, %1" : "+v"(a[4]), "+v"(a[5]) : "v"(s)); }
        if constexpr (I == 42) st32(k.rs, v1, a1, __float_as_uint(a[4]));
    }
};
#define QIDX(t, b) ((t) * 10 + (b))
DEVI void qk_role(const Params& p, Ctx& k, char* smem, int h) {
    PROF_DECL
    const int w = k.w;
    bf16x16 W[52];
    Gather G;
    Soft S;
    const int hw = h >> 3, hh = h & 7;
    unsigned l16, ix, dqm, dq, trp, qp;
    {
        const int l = k.l, g = l >> 4, n = l & 15, r = n & 7, u = w & 1;
        const bool hf = n >= 8, dup = w != 1;
        S.mA = __ballot(dup && hf);
        const int lim = (w == 0 ? 64 : w == 1 ? n : 16 + 8 * u + r) - 4 * g;
#pragma unroll
        for (int i = 0; i < 8; ++i) S.mC[i] = __ballot((i & 3) + (i >> 2) * 16 <= lim);
        const bool okm = !(dup && hf);
        S.mY = __ballot((dup && hf) == (w != 0));
        S.v0 = w == 0 ? hh * 512 + ((g >> 1) * 16 + (r >> 1) * 4 + hf * 2 + (r & 1)) * 16 + (g & 1) * 8 : w == 1 ? (hh >> 1) * 1024 + l * 16 + (hh & 1) * 8 : hh * 1024 + (g * 16 + 8 * u + r) * 16 + hf * 8;
        S.v1 = !okm ? OOB : w == 0 ? 4096 + hh * 128 + r * 16 + g * 4 : (w == 1 ? 4096 + n * 16 : 6144 + (8 * u + r) * 16) + (hh >> 1) * 512 + (g >> 1) * 256 + (hh & 1) * 8 + (g & 1) * 4;
        l16 = l * 16; ix = SPIX(l);
        const int uu = u ^ ((n >> 2) & 1);
        dqm = L_QM + (w >> 1) * 3072 + uu * 512 + (g >> 1) * 128 + n * 8 + (g & 1) * 4;
        dq = L_QM + 6144 + (w >> 1) * 3072 + uu * 512 + g * 128 + ((n * 8) ^ ((g >> 1) * 64));
        trp = L_QM + 6144 + (l & 3) * 128 + (((l >> 2) * 8) ^ ((l & 2) * 32));
        qp = w == 1 ? trp + 6144 : (w == 0 ? L_QM : L_Q + 6144 + u * 256) + (l & 1) * 128 + (((l >> 2) * 8) ^ (w == 3 ? 64 : 0));
        u32x8 rg = {S.v0, S.v1, l16, ix, dqm, dq, trp, qp};
        asm volatile("" : "+{v[0:7]}"(rg));
        S.v0 = rg[0]; S.v1 = rg[1]; l16 = rg[2]; ix = rg[3]; dqm = rg[4]; dq = rg[5]; trp = rg[6]; qp = rg[7];
    }
    Xn X;
    G3 A, B;
    X.l16 = l16; A.l16 = l16; B.l16 = l16; B.lwv = vadd(l16, L_W + w * 1024);
    {
        const int l = k.l;
        bf16x8 wt[16];
        u32x2 gv;
        unsigned ra[3];
#pragma unroll
        for (int t = 0; t < 3; ++t) ra[t] = opq(wp_adr<384, true>(w * 3 + t, l) + SLOT(0));
#define QEX(v) ((v) < 0 ? 0 : ((v) < 6) + ((v) == QGS) + 10 * ((v) == QGS + 2) + 8 * ((v) == QGS + 7))
#define QKE(v) ((v) < QGS + 4 ? 0 : (v) == 19 ? 20 : CMIN(v, 3 * ((v) - QGS - 3)))
        auto rdx = [&](auto K) { constexpr int ks = CV(K); A.r0<ks>(); A.r1<ks>(); B.x0[ks] = A.x0[ks]; B.x1[ks] = A.x1[ks]; if constexpr (ks < 14 && QKE(ks + 1) <= ks) B.rw<ks>(); };
        cstream<20>([](auto V) { constexpr int v = CV(V); return IC(QEX(v - 1) + (v < 19 ? 6 : 0)); },
            [&](auto U) { constexpr int u = CV(U), b = u >> 1, k0 = QKE(u - 1), n = (QKE(u) - k0) * 12, nn = n ? n : 12;
                if constexpr (u < 6) hello(k, 0);
                if constexpr (u == QGS) gv = go_ask(k);
                if constexpr (u == QGS + 2) { PIN(gv); go_wait(k, 0, gv); X.init(k); G.init(k); X.l16 = k.c ? OOB : l16; xn_all(k, X.l16, X.src, X.dst); }
                if constexpr (u == QGS + 4) rdx(IC(0));
                if constexpr (u == QGS + 7) { sfor<3>([&](auto I) { X.next<CV(I)>(k, l16); G.op<CV(I)>(k, l16, 0); }); X.se = X.src; }
                if constexpr (u == 18) {
                    asm volatile("" :: "{v[152:155]}"(wt[10]), "{v[156:159]}"(wt[11]), "{v[232:235]}"(wt[12]));
#pragma unroll
                    for (int i = 10; i < 13; ++i) lw128(B.lwv + i * 4096, __builtin_bit_cast(u32x4, wt[i]));
                }
                if constexpr (u == 19) { G.wait<0>(); lw128(l16 + G.dst + 2048, G.v0); lw128(l16 + G.dst + 3072, G.v1); }
                XSTAMP(u)
                const unsigned rb[3] = {slot_adr<u>(ra[0]), slot_adr<u>(ra[1]), slot_adr<u>(ra[2])};
                bf16x4 hf[12];
                SCHED0();
                sfor<nn>([&](auto I) { constexpr int i = CV(I), ks = k0 + i / 12, m = i % 12, t = i >> 1, e = u == QGS + 7 ? 8 : 0, j = ((i - e) * 6) / (nn - e);
                    if constexpr (n > 0) { if constexpr (m < 6) A.mm<0, false, ks, m>(W); else B.mm<30, true, ks, m - 6>(W); }
                    if constexpr (i < e) { if constexpr (i % 4 == 0) X.op<i / 4, 0>(k); if constexpr (i & 1) X.op<i / 4, 1 + (i / 2) % 2>(k); else G.op<3 + i / 4 + (i & 2) * 2>(k, l16, 0); }
                    else if constexpr (u > 0 && u < 18 && (i == e || ((i - e - 1) * 6) / (nn - e) != j))
                        dma1<SLOT(u + 2) + (j / 3) * 12288 + (j % 3) * 1024>((j < 3 ? p.Wk : p.Wq) + (size_t)(u + 2) * 32 * 6144 + h * 192, wsrc<384, true>(w * 192 + (j % 3) * 64 + l, 12288), w * 3072);
                    if constexpr (i == 0 && u > 4 && (u < 15 || u >= 18)) lw128(B.lwv + (u - 5) * 4096, __builtin_bit_cast(u32x4, wt[(u - 5) & 15]));
                    if constexpr (i < 12) {
                        hf[i] = lrtr(rb[t % 3] + (t / 3) * 12288 + (i & 1) * 1536);
                        if constexpr (i & 1) {
                            if constexpr (t == 5 && b >= 2) B.wl[u] = wt[(u - 4) & 15] = CAT4(hf[i - 1], hf[i]);
                            else if constexpr (u & 1) W[QIDX(t, b) % 52] = CAT8(LO8(W[QIDX(t, b) % 52]), CAT4(hf[i - 1], hf[i]));
                            else W[QIDX(t, b) % 52] = __builtin_shufflevector(hf[i - 1], hf[i], 0, 1, 2, 3, 4, 5, 6, 7, -1, -1, -1, -1, -1, -1, -1, -1);
                        }
                    }
                    if constexpr (n > 0 && m == 3 && ks < 19) rdx(IC((ks + 1) % 20));
                    SCHED0(); }); });
        lw128(B.lwv + 15 * 4096, __builtin_bit_cast(u32x4, wt[15]));
        static_assert(QKE(17) == 17, "QGS");
        RAWBAR()
        XSTAMP(20)
        const unsigned d0 = (l16 | ((l16 >> 5) & 8)) ^ (L_K + (w >> 1) * 3072 + (w & 1) * 8);
        sfor<24>([&](auto I) { A.out4<CV(I), 0>(d0); });
        G.v0 = G.p0; G.v1 = G.p0; G.bad = 0;
        X.off = 0; X.l16 = l16;
        if (k.c) X.init(k);
    }
    RSTAMP(2)
    const unsigned po0 = k.p0 + PR_B + hw * 49152 + (w == 0 ? 0 : w == 1 ? 32768 : 16384), po1 = k.p0 + PR_B + hw * 49152 + (w == 0 ? 0 : 32768);
    const unsigned pdum = k.pg(1) + DUMMY_B, pbc = w == 0 ? k.p0 + CNT(C_BC, hw * 16 + hh) : pdum + EXP(32768 + h * 512 + w * 128);
    unsigned long long now = 0, done = 0, tpa = 0, tpb = 0;
    unsigned a0 = po0 + (k.c ? pdum - k.p0 - PR_B : 0u), a1 = po1 + (k.c ? pdum - k.p0 - PR_B : 0u);
    const unsigned hv = w == 1 ? 0u : 0x3F800000u, hw2 = w == 1 ? 0u : 0x00800000u;
    S.sg = 0x3F800000u; S.hs = 0x3F800000u - hw2;
    f32x4 ac[6];
#pragma unroll
    for (int t = 0; t < 6; ++t) clr2(ac[t]);
    unsigned an = 110;
    __builtin_amdgcn_s_waitcnt(0xc07f);
    do {
        if (X.off != 0) {
            PC8(1, X.sg)
            X.late<2>(k, l16);
            G.check();
        }
_Pragma("unroll 1")
        for (unsigned spins = 0; G.bad != 0 && !spin_fail(k, spins); ++spins) { PC8(0, X.sg) G.again(k, l16); }
        HSTOP(0) HSTAMP(0, 0)
        clr2(ac[5]);
        asm volatile("s_nop 1" : "+v"(ac[5]));
        smfw<50>(ac[5], __builtin_bit_cast(bf16x8, G.p0), W[50], ix);
    do {
        PROF_S(X.sg - 1)
        f32x4 sc[3];
        {
            bf16x8 m[16], wa[10], wb[10], kf[12];
            bf16x4 q[12], km[12];
            unsigned fo[6];
            m[0] = __builtin_bit_cast(bf16x8, G.p0); m[1] = __builtin_bit_cast(bf16x8, G.p1);
            auto put = [&](auto T) { constexpr int t = CV(T); if constexpr (t < 3) lw32(dq + t * 1024, fo[t]); else lw32(dqm + (t - 3) * 1024, fo[t]); };
            auto rd = [&](auto C) { constexpr int c = CV(C), j = c % 20;
                if constexpr (c < 0) { }
                else if constexpr (c < 20) m[j] = lr128(l16 + MADR(j < 10 ? j : j - 8));
                else if constexpr (c < 40) wa[j] = lr128(B.lwv + (j - 2) * 8192);
                else if constexpr (c < 60) wb[j] = lr128(B.lwv + (j - 2) * 8192 + 4096);
                else if constexpr (c < 80) kf[j] = lr128(l16 + L_K + ((j & 1) * 6 + j / 2) * 1024);
                else if constexpr (c < 100) q[j] = lrtr(qp + j * 512);
                else if constexpr (c < 120) km[j] = lrtr(trp + j * 512);
                else if constexpr (c < 140) A.r0<j>();
                else A.r1<j>(); };
#define PB(i) ((i) < 12 ? (i) / 6 : (i) < 32 ? (i) / 4 - 1 : (i) < 44 ? 7 + ((i) - 32) % 3 : (i) < 54 ? 7 + ((i) - 44) / 2 : 12 + ((i) - 54) % 3)
#define PT(i) ((i) < 12 ? ((i) + 5) % 6 : (i) < 32 ? (3 + (i) % 4) % 6 : (i) < 44 ? (3 + ((i) - 32) / 3) % 6 : (i) < 54 ? 1 + ((i) & 1) : 1 + ((i) - 54) / 3)
            sfor<82>([&](auto I) {
                constexpr int i = CV(I) + 1, b = PB(i), t = PT(i), bn = PB(i + 1);
                constexpr int rt[83] = {-1, 2, -1, -1, -1, 22, 42, 3, -1, 23, 43, 4, -1, 24, 44, 5, -1, 25, 45, 6,
                    -1, 26, 46, 7, -1, 8, 9, 27, 47, 28, 48, 29, 49, -1, -1, 60, -1, 61, 62, 63,
                    -1, 10, 64, -1, 11, 12, -1, 13, 14, -1, -1, 80, 82, 83, 84, 85, 86, 87, 67, 88,
                    89, 69, -1, 91, -1, 71, -1, 121, -1, 100, 102, 104, 106, 108, 110, -1, 140, -1, -1, 141, -1, -1, 122};
                constexpr int r2[83] = {-1, -1, -1, -1, -1, -1, -1, -1, -1, -1, -1, -1, -1, -1, -1, -1, -1, -1, -1, -1,
                    -1, -1, -1, -1, -1, -1, -1, -1, -1, -1, -1, -1, -1, -1, -1, -1, -1, -1, -1, -1,
                    -1, -1, -1, -1, -1, -1, -1, -1, -1, -1, -1, 81, -1, 65, -1, 66, -1, -1, -1, -1,
                    68, 90, -1, 70, -1, 120, -1, -1, -1, 101, 103, 105, 107, 109, 111, -1, -1, -1, -1, -1, -1, -1, -1};
                if constexpr (i < 60) {
                    if constexpr (t == 5 && b >= 2) smf<false>(ac[5], m[b], CAT8(wa[b], wb[b]), ix);
                    else smfw<QIDX(t, b < 10 ? b : b - 8) % 52>(ac[t], m[b], W[QIDX(t, b < 10 ? b : b - 8) % 52], ix);
                } else if constexpr (i < 72) { constexpr int j = i - 60; mfa<false, false>(sc[j & 1], kf[j], CAT4(q[j & ~1], q[j | 1])); }
                else if constexpr (i < 77) A.mm<0, false, (i - 72) / 3, (i - 72) % 3>(W);
                else { constexpr int ks = i - 77; mfa<false, ks == 0>(sc[2], CAT4(km[2 * ks], km[2 * ks + 1]), CAT4(q[2 * ks], q[2 * ks + 1])); }
                SCHED0();
                if constexpr (i == 1) { AQ(3, 3) asm volatile("s_barrier" ::: "memory"); AQ(1, 2) AQ(3, 4) }
                else if constexpr (i == 50) { BARL(3); AQ(1, 3) }
                else if constexpr (i == 68) { BARL(3); AQ(1, 4) QSTAMP(0, 1) }
                else if constexpr (i >= 12 && i < 59) { if constexpr (PT(i + 1) == 5) TOUCH(wb[bn % 10]); TOUCH(m[bn]); }
                else if constexpr (i >= 59 && i < 71) { asm volatile("" :: "v"(kf[(i - 59) % 12]), "v"(q[((i - 59) | 1) % 12])); }
                else if constexpr (i == 71) TOUCH(A.x0[0]);
                else if constexpr (i == 74) TOUCH(A.x0[1]);
                else if constexpr (i >= 76 && i < 82) TOUCH(km[(2 * (i - 76) + 1) % 12]);
                if constexpr (i == 8 || i == 30) asm volatile("" :: "{v[60:63]}"(ac[0]), "{v[28:31]}"(ac[1]), "{v[32:35]}"(ac[2]), "{v[64:67]}"(ac[3]), "{v[68:71]}"(ac[4]), "{v[72:75]}"(ac[5]));
                TPQ(i)
                if constexpr (i == 14) AC3(0, 0, 0)
                if constexpr (i == 16) AC3(1, 6, 1)
                if constexpr (i == 18) AC3(2, 7, 2)
                if constexpr (i == 20) AC3(-1, -1, 3)
                if constexpr (i == 22) AC3(-1, -1, 4)
                if constexpr (i == 24) AC3(-1, -1, 5)
                if constexpr (i == 66) AC3(3, -1, 6)
                if constexpr (i == 2) { now = rt_async(); HSTOP(1) }
                if constexpr (i == APOS) { PINS(now); an += (unsigned)now; PINS(an); }
                if constexpr (i == APOS + 1) an = spack(an, X.sg);
                SCHED0();
                rd(IC(rt[i])); rd(IC(r2[i]));
                if constexpr (i == APOS + 4) st32(k.rs, 0, pbc, fo[1]);
                if constexpr (i >= 40 && i < 50 && i % 3 == 1) put(IC(((i - 31) / 3) % 6));
                if constexpr (i == 62 || i == 64) put(IC(i / 2 - 30));
                SCHED0();
                if constexpr (i < 25 && i % 4 != 1) B.out<((i - 2) / 4) * 3 + (i - 2) % 4, 6145>(dq, dq);
                if constexpr (i == APOS + 3) { fo[1] = an; PIN(fo[1]); }
                if constexpr (i >= 37 && i < 49) { constexpr int tt = ((i - 28) / 3) % 6; fold_op<(i - 37) % 3>(ac[tt], fo[tt]); }
                if constexpr (i >= 59 && i < 62) fold_op<i - 59>(ac[1], fo[1]);
                if constexpr (i == 62) fold_op<0>(ac[2], fo[2]);
                if constexpr (i == 63) asm volatile("v_add_f32 %1, %1, %2\n\tv_cvt_pk_bf16_f32 %0, %3, %1" : "=v"(fo[2]), "+v"(ac[2][1]) : "v"(ac[2][3]), "v"(ac[2][0]));
                if constexpr (i == 33) S.mask<0>(sc);
                if constexpr (i >= 51 && i < 59) S.mask<i - 50>(sc);
                if constexpr (i >= 75) S.op<i - 75>(k, sc, a0, a1, hv);
                if constexpr (i == 12) { AQ(3, 5) }
                if constexpr (i == 43) { AQ(3, 6) }
                if constexpr (i == 59) { AQ(3, 7) }
                SCHED0();
            });
        }
        QSTAMP(0, 2)
        {
            unsigned d0;
            auto rest = [&](auto J) { constexpr int j = CV(J);
                if constexpr (j >= 0 && j < 12 && (j % 6 == 0 || j % 3 == 2)) X.op<3 + j / 6, (j % 6 + 1) / 3>(k); };
            sfor<43>([&](auto F) { constexpr int f = CV(F) + 5;
                constexpr int e = f < 6 ? f + 3 : f < 9 ? f - 3 : f;
                A.mm<0, false, e / 6, e % 6>(W);
                if constexpr (f % 6 == 2 || f == 5) TOUCH(A.x1[f / 6]);
                else if constexpr (f == 47) { TOUCH(B.x0[0]); AQ(1, 6) AQ(2, 1) }
                else if constexpr (f % 6 == 5) TOUCH(A.x0[f / 6 + 1]);
                if constexpr (f == 40) { done = rt_async(); QSTAMP(0, 3) AQ(1, 5) }
                TPQ(f + 78)
                if constexpr (f == 21) set_m0(X.dst + 2 * 8192);
                if constexpr (f == 24) { X.sb = X.se + 2 * 16384; PINS(X.sb); }
                if constexpr (f == 42) AC3(4, -1, 7)
                SCHED0();
                if constexpr (f % 6 == 4 && f < 36) A.r0<f / 6 + 2>();
                if constexpr (f % 6 == 1 && f < 42) A.r1<f / 6 + 1>();
                if constexpr (f >= 39 && f < 43) B.rd<true, ((f - 39) / 2) * 6 + (f - 39) % 2>();
                if constexpr (f == 26 || f == 31) X.op<2, f / 15>(k);
                SCHED0();
                S.op<f + 3>(k, sc, a0, a1, hv);
                rest(IC(f - 43));
                if constexpr (f == 44) B.lwv = vadd(l16, L_W + w * 1024);
                SCHED0(); });
            B.go<30, true, 0, 48, 8>(W, [&](auto F) { constexpr int f = CV(F);
                TPQ(f + 126)
                rest(IC(f + 5));
                if constexpr (f == 9) AC3(5, -1, -1)
                if constexpr (f == 2) { a0 = (X.sg & 3) * PR_SZ; PINS(a0); }
                if constexpr (f == 4) { a1 = a0 + po1; a0 += po0; PINS(a0); PINS(a1); }
                if constexpr (f == 8) { S.sg = (X.sg & 4) ? 0xBF800000u : 0x3F800000u; PINS(S.sg); }
                if constexpr (f == 10) { S.hs = S.sg - hw2; PINS(S.hs); }
                if constexpr (f == 14 || f == 16) G.op<f / 16>(k, l16, X.sg);
                if constexpr (f == 22 || f == 28 || f == 32) X.next<(f - 22) / 5>(k, l16);
                if constexpr (f == 35) asm volatile("s_waitcnt vmcnt(%0)\n\ts_barrier" :: "n"(CPOS < 35 ? 7 : 6) : "memory");
                if constexpr (f == 20) AC3(6, 1, -1)
                if constexpr (f == 26) A.home<0>();
                if constexpr (f >= 36) A.rd<false, f + 12>();
                if constexpr (f == 38 || f == 40) X.next<(f == 38 ? 5 : 6)>(k, l16);
                if constexpr (f == CPOS - 1 || f == CPOS) X.next<f - CPOS + 4>(k, l16);
                if constexpr (f == 47) { A.pre<false, 8>(); AQ(1, 7) AQ(2, 2) } });
            A.go<0, false, 48, 120, 20>(W, [&](auto F) { constexpr int f = CV(F), i = f - XPOS;
                TPQ(f + 126)
                if constexpr (f == 56) B.home<1>();
                if constexpr (i == 0 || i == 2 || i == 6) X.next<7 + i / 2>(k, l16);
                if constexpr (i >= 3 && i < 15 && ((i - 3) % 6 == 0 || (i - 3) % 3 == 2)) X.op<(i - 3) / 6, ((i - 3) % 6 + 1) / 3>(k);
                if constexpr (f == 58) asm volatile("s_waitcnt vmcnt(1)\n\ts_barrier" ::: "memory");
                if constexpr (f == 57) AC3(7, 2, -1)
                G.ask<f - RPOS>(k, l16);
                if constexpr (f >= 108) B.rd<true, f - 60>();
                SCHED0();
                G.red<f - 110, (RPOS < XPOS + 5 ? 4 : 0)>();
                if constexpr (f == 116) asm volatile("v_bfe_u32 %0, %1, 8, 1\n\tv_lshl_or_b32 %0, %0, 3, %1" : "=&v"(d0) : "v"(l16));
                if constexpr (f == 118) asm volatile("v_xor_b32 %0, %1, %0" : "+v"(d0) : "s"(L_K + (w >> 1) * 3072 + (w & 1) * 8));
                if constexpr (f == 119) { B.pre<true, 8>(); AQ(2, 3) } });
            B.go<30, true, 48, 120, 20>(W, [&](auto F) { constexpr int f = CV(F), i = f - 48;
                constexpr int ko[26] = {0, 1, 2, 4, 3, 6, 5, 8, 7, 9, 10, 11, 12, 13, 14, 16, 15, 18, 17, 20, 19, 21, 22, 23, -1, -1};
                TPQ(f + 198)
                G.hold<f - GPOS + 5>(X.sg);
                if constexpr (i < 24) { SCHED0(); A.out4<ko[i % 26], 0>(d0); }
                G.at<f - GPOS>(k, l16);
                if constexpr (f == 114) { lgkm0(done); PINS(G.rt); }
                if constexpr (f == 115) { PINS(now); HTRACE(X.sg - 2) an = (unsigned)done - (unsigned)now; PINS(an); }
                if constexpr (f == 116) { an = min(an, 200u); PINS(an); }
                if constexpr (f == 117) { an += DQ; PINS(an); }
                SCHED0();
                if constexpr (i == 28) AC3(-1, 3, -1)
                if constexpr (i == 50) AC3(-1, 4, -1)
                if constexpr (i == 62) AC3(-1, 5, -1)
                if constexpr (i == 23) { AQ(2, 4) }
                if constexpr (i == 47) { AQ(2, 5) }
                if constexpr (i == 65) { AQ(2, 6) }
                if constexpr (f == 119) { AQ(2, 7) }
                if constexpr (f == 112) asm volatile("" :: "{v[12:15]}"(G.v0), "{v[16:19]}"(G.v1), "{v[20:23]}"(G.p0), "{v[24:27]}"(G.p1), "{v9}"(B.lwv) TRHINT);
                if constexpr (f == 119) G.also(X.off);
                if constexpr (f >= 113 && f < 119) clr2(ac[(f + 6 - 113 + 5) % 6]); });
            static_assert(GPOS >= 70 && GPOS <= 98 && XPOS % 6 == 2 && RPOS > XPOS && (RPOS + 1 < XPOS + 5 || RPOS >= XPOS + 14) && XPOS > 58 && CPOS > 8 && CPOS != 35, "GPOS");
            QSTAMP(0, 4) QSTAMP(0, 5)
            HFLUSH(s)
        }
        AST(0)
        PROF_ONLY(G.wait<0>();)
        HSTOP(0) HSTAMP(s, 0) AQ(1, 1) AQ(3, 1)
        G.all();
        SCHED0();
        smfw<50>(ac[5], __builtin_bit_cast(bf16x8, G.p0), W[50], ix);
        AQ(3, 2)
        SCHED0();
    } while (G.bad == 0);
    } while (X.sg != 65u);
    PROF_OUT
}

#define OIDX(t, b) (22 + (t) * 3 + (b))
DEVI void vo_role(const Params& p, Ctx& k, char* smem, int h) {
    PROF_DECL
    const int w = k.w;
    bf16x16 W[52];
    unsigned l16, lwv, ix, dq0, trp, vo;
    {
        const int l = k.l, g = l >> 4, n = l & 15, q = (l >> 2) & 3, pp = l & 3;
        l16 = l * 16; lwv = l16 + L_W + w * 1024; ix = SPIX(l);
        dq0 = L_QM + (24 * w + (n >> 2) * 2 + (n & 1)) * 32 + g * 8 + ((n >> 1) & 1) * 4;
        trp = L_QM + (16 * (g >> 1) + 4 * (g & 1) + 8 * (q >> 1) + (q & 1)) * 32 + pp * 8;
        vo = EXP(h * 512) + (g >> 1) * 256 + n * 16 + (g & 1) * 8;
        u32x8 r = {dq0, trp, vo, ix, l16, lwv, 0u, 0u};
        PIN(r);
        dq0 = r[0]; trp = r[1]; vo = r[2]; ix = r[3]; l16 = r[4]; lwv = r[5];
    }
    Gather G;
    G3 V;
    Xn X;
    V.l16 = l16; V.lwv = lwv; X.l16 = l16;
    f32x4 y[20];
    bf16x8 v[12];
    u32x4 o[10];
    unsigned yb;
    {
        const int l = k.l;
        bf16x8 wt[16];
        bf16x4 q[10][4];
        f32x4 z[9][2];
        u32x2 gv;
        unsigned ra[6];
        const u16* wo = p.Wo + (size_t)h * 192 * 640;
        constexpr int L_V = L_XN + 24576;
#define VEX(v) ((v) < 0 ? 0 : ((v) < 6) + ((v) == VGS) + 10 * ((v) == VGS + 2) + 6 * ((v) == VGS + 6 || (v) == 18))
#define VKE(v) ((v) < VGS + 4 ? 0 : CMIN(20, 7 * ((v) - VGS - 3)))
#define YKE(v) ((v) < 16 ? 0 : 3 * (v) - 45)
        auto put = [&](int t) { st128(k.rs, X.l16, yb + t * 65536, (u32x4){cvtpk(z[t][0][0], z[t][0][1]), cvtpk(z[t][0][2], z[t][0][3]), cvtpk(z[t][1][0], z[t][1][1]), cvtpk(z[t][1][2], z[t][1][3])}); };
        auto rdx = [&](auto K) { constexpr int ks = CV(K); if constexpr (ks < 20) { V.r0<ks % 20>(); V.r1<ks % 20>(); if constexpr (ks < 14) V.rw<ks>(); } };
        auto rdv = [&](auto J) { constexpr int j = CV(J) % 6; v[j] = lr128(l16 + L_V + j * 1024); v[6 + j] = lr128(l16 + L_V + (6 + j) * 1024); };
        cstream<20>([](auto V) { constexpr int v = CV(V); return IC(VEX(v - 1) + (v < 19 ? 6 : 0)); },
            [&](auto U) { constexpr int u = CV(U), k0 = VKE(u - 1), y0 = YKE(u - 1), n = u < 16 ? (VKE(u) - k0) * 6 : u < 18 ? 36 : 18, nn = n ? n : 12, nr = u < 10 ? 3 : u < 16 ? 6 : 4;
                if constexpr (u < 6) hello(k, 0);
                if constexpr (u == VGS) gv = go_ask(k);
                if constexpr (u == VGS + 2) { PIN(gv); go_wait(k, 0, gv); X.init(k); X.l16 = k.c ? OOB : l16; xn_all(k, X.l16, X.src, X.dst); }
                if constexpr (u == VGS + 4) { rdx(IC(0)); rdx(IC(1)); }
                if constexpr (u == VGS + 6) { sfor<3>([&](auto I) { X.next<CV(I)>(k, l16); }); X.se = X.src; yb = k.yx(0, w) + EXP(h * 1024); }
                if constexpr (u == 16) rdv(IC(0));
                XSTAMP(u)
                if constexpr (u == 0 || u == 10 || u == 16) {
#pragma unroll
                    for (int t = 0; t < nr; ++t) ra[t] = opq((u == 0 ? wp_adr<384, true>(w * 3 + t, l) : u == 10 ? wp_adr<768, false>(w * 6 + t, l) : wp_adr<512, false>(w * 4 + t, l)) + SLOT(0));
                }
                unsigned rb[6];
#pragma unroll
                for (int t = 0; t < nr; ++t) rb[t] = slot_adr<u>(ra[t]);
                SCHED0();
                sfor<nn>([&](auto I) { constexpr int i = CV(I), e = u == VGS + 6 ? 9 : 0, j = ((i - e) * 6) / (nn - e), d = u + 2, r0 = u == 18 ? 6 : 0;
                    if constexpr (u < 16 && n > 0) { V.mm<0, true, k0 + i / 6, i % 6>(W); if constexpr (i % 6 == 3) rdx(IC(k0 + i / 6 + 2)); }
                    if constexpr (u >= 16) { constexpr int m = n / 3, ks = (y0 + i / m) % 6, t = (u / 18) * 6 + (i % m) / 2, rt = i & 1;
                        if constexpr (!(ks == 5 && t == 7 && rt)) { mfw<OIDX(t, ks >> 1), ks & 1, true, ks == 0>(z[t][rt], W[OIDX(t, ks >> 1)], v[rt * 6 + ks]); SCHED0(); }
                        if constexpr (i % m == 1 && u < 19) rdv(IC(ks + 1));
                        if constexpr (u == 19 && i < 2) rdv(IC(4 + i));
                        if constexpr (u == 18 && i >= 3 && i < 15 && (i & 1)) put((i - 3) / 2);
                        if constexpr (u == 18 && i < 6) lw128(lwv + (10 + i) * 4096, __builtin_bit_cast(u32x4, wt[10 + i])); }
                    if constexpr (i < e) X.op<i / 3, i % 3>(k);
                    else if constexpr (u > 0 && u < 18 && (i == e || ((i - e - 1) * 6) / (nn - e) != j)) {
                        if constexpr (d < 10) dma1<SLOT(d) + (j / 3) * 12288 + (j % 3) * 1024>(p.Wv + (size_t)(2 * d + j / 3) * 32 * 6144 + h * 192, wsrc<384, true>(w * 192 + (j % 3) * 64 + l, 12288), w * 3072);
                        else if constexpr (d < 16) dma1<SLOT(d) + j * 1024>(wo + (d - 10) * 32 * 640, wsrc<768, false>(w * 384 + j * 64 + l, 1280), w * 6144);
                        else dma1<SLOT(d) + j * 1024>(wo + (d - 16) * 48 * 640 + 384, wsrc<512, false>(w * 384 + j * 64 + l, 1280), w * 6144);
                    }
                    if constexpr (i < 2 && u >= 3 && u < 8) lw128(lwv + (2 * u - 6 + i) * 4096, __builtin_bit_cast(u32x4, wt[(2 * u - 6 + i) & 15]));
                    sfor<2>([&](auto C) { constexpr int c = u == 19 ? 2 * i + CV(C) : i - r0 + CV(C) * 99;
                        if constexpr (c >= 0 && c < 12) {
                            constexpr int t = u < 10 ? c >> 2 : u < 16 ? c >> 1 : 6 + c % 4, r = u < 10 ? c & 3 : u < 16 ? (u - 10) * 2 + (c & 1) : (u - 16) * 3 + c / 4, id = u < 10 ? (t * 10 + u) % 52 : OIDX(t, r >> 2);
                            q[t][r & 3] = lrtr(u < 10 ? rb[t] + (r >> 1) * 12288 + (r & 1) * 1536 : u < 16 ? rb[t] + (c & 1) * 12288 : rb[(t - 6) & 3] + (c / 4) * 8192);
                            if constexpr (u < 10 && u >= 2 && t == 2) { if constexpr (r & 1) V.wl[2 * u + r / 2] = wt[(2 * u - 4 + r / 2) & 15] = CAT4(q[t][r - 1], q[t][r]); }
                            else if constexpr ((r & 3) == 1) W[id] = __builtin_shufflevector(q[t][0], q[t][1], 0, 1, 2, 3, 4, 5, 6, 7, -1, -1, -1, -1, -1, -1, -1, -1);
                            else if constexpr ((r & 3) == 3) W[id] = CAT8(LO8(W[id]), CAT4(q[t][2], q[t][3]));
                        } });
                    SCHED0(); });
                if constexpr (u == VGS + 7) sfor<18>([&](auto I) { V.out<CV(I), 0>(l16 + L_V + ((3 * w) >> 1) * 1024 + ((3 * w) & 1) * 8, l16 + L_V + ((3 * w + 1) >> 1) * 1024 + ((3 * w + 1) & 1) * 8); }); });
        static_assert(VGS >= 6 && VGS < 9, "VGS");
        RAWBAR()
        XSTAMP(20)
        sfor<2>([&](auto B) { X.op<CV(B) + 3, 0>(k); asm volatile("s_nop 0"); X.op<CV(B) + 3, 1>(k); X.op<CV(B) + 3, 2>(k); });
        asm volatile("" : "+v"(z[8][0]), "+v"(z[8][1]), "+v"(z[7][0]), "+v"(z[6][1]));
        put(6); put(8);
        lw32(l16 + L_MEM, ~0u);
        o[7] = (u32x4){cvtpk(z[7][0][0], z[7][0][1]), cvtpk(z[7][0][2], z[7][0][3]), 0u, 0u};
        y[17] = z[7][1];
        yb += 7 * 65536;
        X.off = 0; X.l16 = l16;
        if (k.c) X.init(k);
    }
    RSTAMP(2)
    const unsigned pdum = k.pg(1) + DUMMY_B, pbc = w == 0 ? k.p0 + CNT(C_BC, (h >> 3) * 16 + 8 + (h & 7)) : pdum + EXP(49152 + h * 512 + w * 128);
    unsigned long long now = 0, done = 0, tpa = 0, tpb = 0;
    G.mx = ~0u;
    G.init(k);
    G.base -= 32768;
    const unsigned ypar = (unsigned)(k.par >> (PG_YX + (w >> 1))), yo = ((unsigned)(PG_YX + (w >> 1)) << 21) + (w & 1) * 1048576 + EXP(h * 1024);
    const unsigned ym0 = k.ym(0) + w * 163840, ym2 = k.ym(2) + w * 163840;
    unsigned ym = k.c ? pdum + w * 163840 : ym0, ys, t, c = ~0u, an = 80;
#define YT(u) ((u) < 10 ? 2 * ((u) % 5) : ((u) - 10) | 1)
#define YR(u) ((u) < 10 ? (u) / 5 : (u) & 1)
    auto yout = [&](auto U, auto J) { constexpr int u = CV(U), j = CV(J), t = YT(u), rt = YR(u);
        if constexpr (j == 0) { o[t][2 * rt] = cvtpk(y[u][0], y[u][1]); o[t][2 * rt + 1] = cvtpk(y[u][2], y[u][3]); TOUCH(o[t][2 * rt]); TOUCH(o[t][2 * rt + 1]); }
        if constexpr (j == 1 && rt) __builtin_amdgcn_raw_buffer_store_b128(o[t], k.rs, (int)l16, (int)yb, AUX_SC1);
        if constexpr (j == 2 && rt) { yb += t == 8 ? -7 * 65536 : 131072; PINS(yb); TOUCH(o[t]); } };
    auto ymm = [&](auto I) {
        constexpr int i = CV(I), ks = i % 6, u = i / 6, id = OIDX(YT(u), ks >> 1);
        mfw<id, ks & 1, true, ks == 0>(y[u], W[id], v[YR(u) * 6 + ks]);
        SCHED0();
        if constexpr (u > 0 && ks >= 2) yout(IC((u + 19) % 20), IC(ks - 2)); };
    if (k.c) X.late<5>(k, l16);
    do {
        PROF_S(X.sg - 1)
        f32x4 ac[3];
        sfor<13>([&](auto I) { constexpr int i = CV(I) + 107;
            ymm(IC(i));
            TPV(i - 107)
            if constexpr (i == TB) { asm volatile("s_waitcnt vmcnt(%0)\n\ts_barrier" :: "n"(TB < 111 ? 7 : 8) : "memory"); AST(5) }
            if constexpr (i == TB + 1) st32(k.rs, 0, pbc + 4, G.mx);
            if constexpr (i == 119) V.pre<true, 0>();
            if constexpr (i == 113) G.ask<0>(k, l16);
            if constexpr (i == 119) G.ask<1>(k, l16);
            if constexpr (i == 114) V.r0<0>();
            if constexpr (i == 115) V.r1<0>();
            if constexpr (i == 117) V.r0<1>();
            if constexpr (i == 118) V.r1<1>();
            SCHED0(); });
        HSTAMP(s, 4)
        V.run<0, true, 0, 16, 17, true>(W, [&](auto I) { constexpr int i = CV(I);
            TPV(i + 13)
            if constexpr (i >= 2) yout(IC(19), IC(i - 2));
            if constexpr (i == 8) { t = (X.sg & 7) * 2; PINS(t); }
            if constexpr (i == 10) { yb = (ypar >> t) & 1; PINS(yb); }
            if constexpr (i == 14) { t = (t << 21) + yo; PINS(t); }
            if constexpr (i == 16) { yb = (yb << 13) + t; PINS(yb); }
            if constexpr (i == 20) { c = X.sg; PINS(c); }
            if constexpr (i == 22) { G.a = max(c, 1u) << 15; PINS(G.a); }
            if constexpr (i == 28) G.op<1>(k, l16, 0);
            if constexpr (i == 40) { an = min(an, 200u) + DV; PINS(an); }
            if constexpr (i == 32 || i == 34 || i == 38) X.next<(i - 31) / 3>(k, l16);
            if constexpr (i == 23 || i == 29) { AAC(4 + i / 29) }
            if constexpr (i == 83 || i == 89) { AAC(6 + i / 89) }
            if constexpr (i == YPOS) { AST(6) }
            if constexpr (i == YPOS + 1) { AST(7) }
            if constexpr (i == YPOS) { asm volatile("s_waitcnt vmcnt(0)\n\ts_barrier" ::: "memory"); }
            if constexpr (i == YPOS - 2) G.mx = vshl1(c, 0);
            if constexpr (i == YPOS + 1) st32(k.rs, 0, pbc + 4, G.mx);
            SCHED0();
            G.red<i - YPOS - 2>();
            G.hold<i - GPOSY * 6 + 5>(c);
            G.at<i - GPOSY * 6>(k, l16);
            if constexpr (i == 90) PINS(G.rt);
            if constexpr (i == 95) V.pre<true, 16>();
            if constexpr (i >= 92 && i < 95) clr2(ac[(i - 92) % 3]); });
        static_assert(GPOSY * 6 + 2 > YPOS + 1 && GPOSY <= 13, "GPOSY");
        HSTAMP(s, 5)
        HFLUSH(s - 1)
        f32x4 acc[10];
        f32x2 z[20];
        auto clr = [&](auto I) { constexpr int i = CV(I); asm volatile("v_mov_b64 %0, 0" : "=v"(z[i])); if constexpr (i & 1) acc[i >> 1] = __builtin_shufflevector(z[i - 1], z[i], 0, 1, 2, 3); };
        bf16x4 al[3], ah[3];
        unsigned d0, d1;
        {
            bf16x8 m[10], wa[10], wb[10];
            auto rd = [&](auto R) { constexpr int r = CV(R), b = 2 + r / 3;
                if constexpr (b < 10) {
                    if constexpr (r % 3 == 0) m[b] = lr128(l16 + MADR(b));
                    if constexpr (r % 3 == 1) wa[b] = lr128(lwv + (b - 2) * 8192);
                    if constexpr (r % 3 == 2) wb[b] = lr128(lwv + (b - 2) * 8192 + 4096);
                } else if constexpr (b < 13) {
                    if constexpr (r % 3 == 0) V.r0<b + 7>();
                    if constexpr (r % 3 == 1) V.rw<b + 7>();
                    if constexpr (r % 3 == 2) V.r1<b + 7>();
                } };
            AST(0)
            G.wait<0>();
            HSTOP(0) HSTAMP(s, 0) AST(1)
            V.run<0, true, 16, 17, 0, true>(W, [&](auto I) { constexpr int i = CV(I);
                TPV(i + 109)
                G.fin<i>(0);
                if constexpr (i == 3) {
                    if (__builtin_expect(G.bad != 0, 0)) {
                        unsigned spins = 0;
_Pragma("unroll 1")
                        do { PC8(0, c) G.again(k, l16); } while (G.bad != 0 && !spin_fail(k, spins++));
                        HSTOP(0) HSTAMP(s, 0)
                    }
                    asm volatile("s_barrier" ::: "memory");
                    AST(2)
                }
                if constexpr (i == 4) { now = rt_async(); HSTOP(1) SCHED0(); }
                if constexpr (i >= 3) rd(IC(i - 3)); });
            m[0] = __builtin_bit_cast(bf16x8, G.p0); m[1] = __builtin_bit_cast(bf16x8, G.p1);
            sfor<30>([&](auto I) {
                constexpr int i = CV(I), b = i / 3, t = i % 3;
                if constexpr (t == 2 && b >= 2) smf<false>(ac[2], m[b], CAT8(wa[b], wb[b]), ix); else smfw<t * 10 + b>(ac[t], m[b], W[t * 10 + b], ix);
                SCHED0();
                TPV(i + 115)
                if constexpr (i >= 6 && i < 29 && t != 0) { if constexpr (t == 1) TOUCH(wb[b]); else TOUCH(m[(b + 1) % 10]); }
                if constexpr (i == 29) V.pre<true, 17>();
                if constexpr (i == 2) { PINS(now); an += (unsigned)now; PINS(an); }
                if constexpr (i == 3) an = spack(an, c);
                SCHED0();
                if constexpr (i < 27) rd(IC(3 + i));
                if constexpr (i == 5) st32(k.rs, 0, pbc, d0);
                SCHED0();
                if constexpr (i == 4) { d0 = an; PIN(d0); }
                if constexpr (i == 28) d0 = vadd(l16, L_K + ((3 * w) >> 1) * 1024 + ((3 * w) & 1) * 8);
                if constexpr (i == 29) d1 = vadd(l16, L_K + ((3 * w + 1) >> 1) * 1024 + ((3 * w + 1) & 1) * 8);
                SCHED0();
            });
            unsigned fo[3];
            V.run<0, true, 17, 19, 0, true>(W, [&](auto I) { constexpr int i = CV(I);
                TPV(i + 145)
                if constexpr (i < 3) { rd(IC(30 + i)); SCHED0(); fold_op<0>(ac[i], fo[i]); }
                if constexpr (i == 3 || i == 4 || i == 6) { constexpr int t = i / 2 - 1; asm volatile("v_add_f32 %1, %1, %2\n\tv_cvt_pk_bf16_f32 %0, %3, %1" : "=v"(fo[t]), "+v"(ac[t][1]) : "v"(ac[t][3]), "v"(ac[t][0])); }
                if constexpr (i == 5 || i == 7 || i == 8) lw32(dq0 + ((i - 4) / 2) * 256, fo[(i - 4) / 2]);
                if constexpr (i == 9 || i == 10) clr2(acc[i - 9]);
                if constexpr (i == 11) BARL(0) });
        }
        HSTAMP(s, 1) AST(3)
        {
            unsigned fo[2];
            auto xq = [&](auto I) { constexpr int i = CV(I) - XP + XS0;
                if constexpr (i == -4) X.next<3>(k, l16);
                if constexpr (i == -1 || i == 0) X.next<14 + i>(k, l16); };
            static_assert(XP % 6 == 5 && XP <= 53 && XP >= 17 && XB > XP + 18 && XB > 63, "XP");
            auto fin2 = [&](auto P, auto J) { fold2<CV(J)>(acc[2 * CV(P)], acc[2 * CV(P) + 1], fo); };
            V.run<0, true, 19, 20, 0, true>(W, [&](auto I) { constexpr int i = CV(I);
                TPV(i + 157)
                if constexpr (i == 0) { al[0] = lrtr(trp); ah[0] = lrtr(trp + 64); }
                if constexpr (i == 1) al[1] = lrtr(trp + 1024);
                if constexpr (i == 2) ah[1] = lrtr(trp + 1088);
                if constexpr (i == 3) al[2] = lrtr(trp + 2048);
                if constexpr (i == 4) ah[2] = lrtr(trp + 2112);
                SCHED0();
                if constexpr (i >= 1) clr(IC(3 + i)); });
            sfor<30>([&](auto I) { constexpr int i = CV(I), b = (i % 6) >> 1, t = 2 * (i / 6) + (i & 1), id = OIDX(t, b);
                smfw<id>(acc[t], CAT4(al[b], ah[b]), W[id], ix);
                SCHED0();
                TPV(i + 163)
                if constexpr (i == 1 || i == 3) TOUCH(ah[(i + 1) / 2]);
                if constexpr (i == 29) TOUCH(v[0]);
                if constexpr (i == 21) BARL(0)
                SCHED0();
                if constexpr (i == 4 || i == 7) X.next<(i == 4 ? 5 : 11)>(k, l16);
                xq(IC(i - 30));
                if constexpr (i >= 21 && i < 27) v[i - 21] = lr128(l16 + L_K + (i - 21) * 1024);
                SCHED0();
                V.out<i - 1, 0>(d0, d1);
                SCHED0();
                if constexpr (i % 3 == 0 && i < 18) clr(IC(9 + i / 3));
                if constexpr (i == 16) clr(IC(15));
                if constexpr (i >= 18 && i < 21) clr(IC(i - 2));
                if constexpr (i == 22) clr(IC(19));
                if constexpr (i >= 23 && i < 29) fold_op<(i - 23) / 2>(acc[(i - 23) & 1], fo[(i - 23) & 1]);
                if constexpr (i == 29) { st64(k.rs, vo, ym, (u32x2){fo[0], fo[1]}); SCHED0(); fin2(IC(1), IC(0)); }
                SCHED0(); });
            HSTAMP(s, 2)
            sfor<107>([&](auto I) { constexpr int i = CV(I), q = i < 8 ? i + 4 : i + 3;
                ymm(I);
                TPV(i + 193)
                if constexpr (i < 5 || (i >= 29 && i < 35)) TOUCH(v[(i < 5 ? i + 1 : i - 23) % 12]);
                if constexpr (i < 13 && i != 8) {
                    if constexpr (q % 3 == 1) { ym += 32768; PINS(ym); }
                    if constexpr (q % 3 == 2 && i >= 5) __builtin_amdgcn_s_setprio(0);
                    SCHED0();
                    if constexpr (q % 3 == 0) { st64(k.rs, vo, ym, (u32x2){fo[0], fo[1]}); SCHED0(); }
                    if constexpr (q < 15) fin2(IC(q / 3), IC(q % 3));
                }
                if constexpr (i == 13) done = rt_async();
                if constexpr (i >= 15 && i < 23 && i != 18 && i != 20) v[6 + i - 15 - (i > 18) - (i > 20)] = lr128(l16 + L_K + (6 + i - 15 - (i > 18) - (i > 20)) * 1024);
                xq(IC(i));
                if constexpr (i == XP - 6) X.next<15>(k, l16);
                if constexpr (i == XP - 1) { SCHED0(); X.next<7>(k, l16); }
                if constexpr (i == XP) X.next<12>(k, l16);
                if constexpr (i > XP && i <= XP + 18 && (i % 6 == 0 || i % 3 == 1)) X.op<(i - XP - 1) / 6, (i % 6 + 2) / 3>(k);
                { constexpr int xp[6] = {XB, XB + 1, XB + 6, XB + 11, XB + 12, XB + 18};
                  sfor<6>([&](auto Q) { if constexpr (xp[CV(Q)] == i) X.op<3 + CV(Q) / 3, CV(Q) % 3>(k); }); }
                if constexpr (i == 90) { ym = (c & 2) ? ym2 : ym0; PINS(ym); }
                if constexpr (i == 91) { ys = ym + EXP(327680); PINS(ys); }
                if constexpr (i == 96) { ym = (c & 1) ? ys : ym; PINS(ym); }
                if constexpr (i == 60) { HSTAMP(s, 3) }
                if constexpr (i == 42 || i == 43 || i == 48 || i == 49) { AAC((i - 42) / 3 + (i & 1)) }
                if constexpr (i == 102) lgkm0(done);
                if constexpr (i == 103) { PINS(now); HTRACE(c - 1) an = (unsigned)done - (unsigned)now; PINS(an); }
                if constexpr (i == 105) G.mx = vshl1(c, 1);
                SCHED0(); });
            AST(4)
        }
    } while (X.sg != 65u);
    HFLUSH(63)
    PROF_OUT
}

DEVI void row_role(const Params& p, const Ctx& k, char* smem, int rt) {
    PROF_DECL
    const int l = k.l, n = l & 15;
    const unsigned fo = k.w * 2048 + rt * 1024;
    RSTAMP(2)
    const __amdgpu_buffer_rsrc_t rs = __builtin_amdgcn_make_buffer_rsrc(k.c ? (void*)k.ws : (void*)p.x, 0, 0x7fffffff, 0x00020000);
    const unsigned vs = k.c ? l * 16 : n * 1280 + (l >> 4) * 16, cs = k.c ? 16384 : 256;
    unsigned ep = n * 32, dp = (l >> 4) == (n >> 2) ? n * 32 + k.w * 8 : 1024 + l * 8;
    const unsigned long long m1 = __ballot(n & 1), m2 = __ballot(n & 2);
    bf16x8 one8 = {0x3F80, 0x3F80, 0x3F80, 0x3F80, 0x3F80, 0x3F80, 0x3F80, 0x3F80};
    PIN(one8);
    unsigned fails = k.c ? 0u : 65536u;
    prog_init(k);
    for (int sg = k.c ? 0 : 8; sg < 64; ++sg) {
        unsigned src[5], dst = k.seg(PG_XNA, sg) + fo;
#pragma unroll
        for (int c = 0; c < 5; ++c) { src[c] = (k.c ? k.seg(PG_X2, sg) + fo : (sg * 32 + rt * 16) * 1280 + k.w * 64) + c * cs; PINS(src[c]); }
        PINS(dst);
        prog_wait<5>(k, k.p0 + CNT(C_X2, rt * 5), k.c ? sg : -1);
        STOP(sg) STAMP(sg, 0)
        u32x4 a[5];
        f32x4 sm, sq;
        f32x2 v[20];
        unsigned long long bad;
        do {
            asm volatile("" ::: "memory");
#pragma unroll
            for (int c = 0; c < 5; ++c) { a[c] = ld128(rs, vs, src[c]); SCHED0(); }
            unsigned mx = 0, lim = ~1u | (fails >> 16);
            ++fails; PINS(fails); PINS(lim);
            sfor<5>([&](auto C) { constexpr int c = CV(C);
                mfa<false, c == 0>(sq, __builtin_bit_cast(bf16x8, a[c]), __builtin_bit_cast(bf16x8, a[c])); mfa<false, c == 0>(sm, one8, __builtin_bit_cast(bf16x8, a[c]));
                mx = umax3(umax3(mx, a[c][0], a[c][1]), a[c][2], a[c][3]); PIN(mx);
                SCHED0(); });
            bad = __ballot(mx > lim);
        } while (__builtin_expect(bad != 0, 0));
        STAMP(sg, 1)
        auto unp = [&](int i0, int i1) {
#pragma unroll
            for (int i = i0; i < i1; ++i) { v[i] = (f32x2){blo(a[i >> 2][i & 3]), bhi(a[i >> 2][i & 3])}; PIN(v[i]); }
            SCHED0(); };
        unp(0, 3);
        asm volatile("" : "+v"(sq), "+v"(sm));
        lw64(dp, (u32x2){__float_as_uint(sm[0]), __float_as_uint(csel(csel(sq[0], sq[1], m1), csel(sq[2], sq[3], m1), m2))});
        SCHED0();
        unp(3, 10);
        RAWBAR()
        const f32x4 e0 = __builtin_bit_cast(f32x4, lr128(ep)), e1 = __builtin_bit_cast(f32x4, lr128(ep + 16));
        SCHED0();
        unp(10, 20);
        const f32x4 e = e0 + e1;
        float m = (e[0] + e[2]) * (1.f / 640.f);
        const float var = bfr(__builtin_fmaxf(__builtin_fmaf(-m, m, (e[1] + e[3]) * (1.f / 640.f)), 0.f));
        m = bfr(m);
        const float rden = __builtin_amdgcn_rcpf(bfr(__builtin_amdgcn_sqrtf(bfr(var + 1e-5f))));
#pragma unroll
        for (int c = 0; c < 5; ++c) {
            u32x4 o;
#pragma unroll
            for (int i = 0; i < 4; ++i) { const f32x2 x = v[c * 4 + i] - m; o[i] = cvtpk(bfr(x[0]) * rden, bfr(x[1]) * rden); }
            st128(k.rs, l * 16, dst + c * 16384, o);
        }
        STAMP(sg, 2)
        unsigned fv = sg - (k.c ? 0 : 8);
        PIN(fv);
        DRAIN();
        st32(k.rs, l == 0 ? 0u : OOB, k.p0 + CNT(C_XN, rt * 4 + k.w), fv);
        dp ^= 2048; ep ^= 2048;
        STAMP(sg, 3) STAMP(sg, 4) STAMP(sg, 5)
        PFLUSH(sg)
    }
    PROF_OUT
}

DEVI int red_col(int j) { const int w = j / 5, q = j % 5; return q < 3 ? 3 * w + q : 9 + 2 * w + q; }
struct Red {
    u32x4 Y[16];
    template <int I0, int I1> DEVI void yload(const Ctx& k, int j, int sg) {
        const unsigned a = k.yx(sg, j / 5) + (j % 5) * 131072 + k.w * 16384;
#pragma unroll
        for (int i = I0; i < I1; ++i) Y[i] = ld128(k.rs, k.l * 16 + (i & 3) * 1024, a + ((i >> 2) & 1) * 4096 + (i >> 3) * 65536);
    }
};
DEVI unsigned y_get(const Ctx& k) { return ld32(k.rs, (k.l & 7) * 128, k.p0 + CNT(C_BC, k.w * 16 + 8) + 4); }
DEVI void y_wait(const Ctx& k, int s) {
_Pragma("unroll 1")
    for (unsigned spins = 0; !__all((int)y_get(k) >= s); ++spins) {
        asm volatile("" ::: "memory");
        __builtin_amdgcn_s_sleep(2);
        if (spin_fail(k, spins)) break;
    }
    __builtin_amdgcn_fence(__ATOMIC_ACQUIRE, "wavefront");
}
DEVI void wait_until(unsigned t) { while ((int)((rclk() - t) << 16) < 0) { if (WUS) __builtin_amdgcn_s_sleep(WUS); } }
#define DPPMAX(x, c) x = max(x, __builtin_amdgcn_update_dpp(0, x, c, 0xf, 0xf, false))
#define TSLP 1
#define TWU 0
DEVI void wait_until2(unsigned t) { while ((int)((rclk() - t) << 16) < 0) { if (TWU) __builtin_amdgcn_s_sleep(TWU); } }
#define WAIT_BC(s, BSLP) { \
    unsigned bc; \
    _Pragma("unroll 1") for (;; ++spins) { \
        bc = ld32(k.rs, (l & 15) * 128, k.p0 + CNT(C_BC, w * 16)); \
        DRAIN(); \
        if (__all(((int)bc >> 16) > s)) break; \
        __builtin_amdgcn_s_sleep(BSLP); \
        if (spin_fail(k, spins)) break; \
    } \
    const unsigned now = rclk(); \
    int d = (int)((bc - now) << 16); \
    d = ((int)bc >> 16) > s + 1 || d > (256 << 16) ? -(64 << 16) : d; \
    DPPMAX(d, 0xB1); DPPMAX(d, 0x4E); DPPMAX(d, 0x141); \
    tq = now + (__builtin_amdgcn_readlane(d, 0) >> 16); tv = now + (__builtin_amdgcn_readlane(d, 8) >> 16); }
DEVI void mred_role(const Params& p, const Ctx& k, char* smem, int j) {
    PROF_DECL
    const int l = k.l, w = k.w, g = l >> 4, n = l & 15;
    const unsigned ix = SPIX(l);
    const unsigned vp = (g * 16 + ((n >> 1) & 1) * 8 + (n >> 2) * 2 + (n & 1)) * 16;
    const unsigned v16 = (g >> 1) * 1024 + (g & 1) * 256 + n * 16, yo = j * 32768 + EXP(w * 4096);
    const u32x4 ones = {0xFFFFFFFFu, 0xFFFFFFFFu, 0xFFFFFFFFu, 0xFFFFFFFFu};
    auto arm = [&](int s) {
#pragma unroll
        for (int i = 0; i < 4; ++i) st128(k.rs, l * 16 + i * 1024, k.ym(s) + yo, ones);
    };
    const unsigned pv = l == 0 && j % 5 == 0 ? (j / 5) * 128 + w * 32 : OOB;
    Red R;
    RSTAMP(2)
    arm(1);
#pragma unroll
    for (int i = 0; i < 8; ++i) st128(k.rs, (i || w > 1 || g > 1) ? g * 32768 + n * 16 : OOB, k.memf(i * 8 + (w >> 1) * 4) + EXP((2 * j + (w & 1)) * 256), ones);
#pragma unroll
    for (int i = 0; i < 4; ++i) { const unsigned q = min(i * 80 + j * 4 + w, 287); st128(k.rs, l * 16, k.p0 + PR_B + PR_SZ + EXP(q * 1024), ones); }
    sleep_until(k.t0 + RSL);
    y_wait(k, 2);
    R.yload<0, 16>(k, j, 0);
    for (int s = 0; s < 63; ++s) {
        STOP(s) HSTAMP(s, 0)
        const unsigned ap = k.p0 + PR_B + (s & 3) * PR_SZ + w * 49152, am = k.ym(s) + yo;
        const unsigned ph = (s & 4) ? 0x80008000u : 0u;
        unsigned spins = 0, tq, tv;
        unsigned fl;
        WAIT_BC(s, BSLP)
        st32(k.rs, pv, k.p0 + CNT(C_XB, 0), spack(((int)((tq - tv) << 16) < 0 ? tv : tq) + RDY, s + 2));
        STAMP(s, 5)
        char* rb = smem + (s & 1) * 4096;
_Pragma("unroll 1")
        for (spins = 0;; ++spins) {
            u32x4 ym[4], pm[5];
            f32x4 acc[2];
            if (spins == 0) wait_until(tv);
            asm volatile("" ::: "memory");
#pragma unroll
            for (int i = 0; i < 4; ++i) ym[i] = ld128(k.rs, v16 + (i >> 1) * 2048 + (i & 1) * 512, am);
            fl = ld32(k.rs, 0, (k.p0 ^ 8192u) + CNT(C_X1, j));
            asm volatile("" ::: "memory");
            if (spins == 0) { wait_until(tq); STAMP(s, 1) }
            asm volatile("" ::: "memory");
#pragma unroll
            for (int i = 0; i < 5; ++i) pm[i] = ld128(k.rs, i < 4 ? l * 16 + i * 1024 : vp, ap + (i >> 2) * 4096);
            acc[0] = (f32x4){0.f, 0.f, 0.f, 0.f}; acc[1] = acc[0];
            const u32x8 y0 = {ym[0][0], ym[1][0], ym[0][2], ym[1][2], ym[2][0], ym[3][0], ym[2][2], ym[3][2]};
            const u32x8 y1 = {ym[0][1], ym[1][1], ym[0][3], ym[1][3], ym[2][1], ym[3][1], ym[2][3], ym[3][3]};
            unsigned mx = 0;
#pragma unroll
            for (int i = 0; i < 4; ++i) mx = max(mx, umax4(ym[i]));
            unsigned tg = mx == 0xFFFFFFFFu ? 0x80000000u : 0u;
            asm volatile("s_nop 1" : "+v"(acc[0]), "+v"(acc[1]), "+v"(tg) : "v"(y0), "v"(y1), "v"(ix));
            SCHED0();
#pragma unroll
            for (int i = 0; i < 5; ++i) {
                const unsigned u0 = __builtin_amdgcn_bitop3_b32(pm[i][0], pm[i][1], ph, 0x7e), u1 = __builtin_amdgcn_bitop3_b32(pm[i][2], pm[i][3], ph, 0x7e);
                SCHED0();
                if (i < 4) smf<false>(acc[0], __builtin_bit_cast(bf16x8, pm[i]), __builtin_bit_cast(bf16x16, __builtin_shufflevector(R.Y[2 * i], R.Y[2 * i + 1], 0, 1, 2, 3, 4, 5, 6, 7)), ix);
                else smf<false>(acc[0], __builtin_bit_cast(bf16x8, pm[4]), __builtin_bit_cast(bf16x16, y0), ix);
                tg = (tg | u0) | u1;
                SCHED0();
                if (i < 4) smf<false>(acc[1], __builtin_bit_cast(bf16x8, pm[i]), __builtin_bit_cast(bf16x16, __builtin_shufflevector(R.Y[8 + 2 * i], R.Y[9 + 2 * i], 0, 1, 2, 3, 4, 5, 6, 7)), ix);
                else smf<false>(acc[1], __builtin_bit_cast(bf16x8, pm[4]), __builtin_bit_cast(bf16x16, y1), ix);
                SCHED0();
            }
            const unsigned long long bad = __ballot((int)tg < 0);
            asm volatile("s_nop 5" : "+v"(acc[0]), "+v"(acc[1]) : "s"(bad));
#pragma unroll
            for (int t = 0; t < 2; ++t) { float* rp = (float*)(rb + t * 2048 + w * 512 + g * 128 + n * 4); rp[0] = acc[t][0] + acc[t][2]; rp[16] = acc[t][1] + acc[t][3]; }
            if (bad == 0) break;
            PCOUNT(0)
            asm volatile("" ::: "memory");
            if (spin_fail(k, spins)) break;
        }
        STAMP(s, 2)
        RAWBAR()
        if (w < 2) {
            const char* rp = rb + w * 2048 + ((l >> 4) * 2 + ((l >> 2) & 1)) * 64 + ((l & 3) * 4 + ((l >> 3) & 1) * 2) * 4;
            const f32x2 a = *(const f32x2*)rp, b = *(const f32x2*)(rp + 512), c = *(const f32x2*)(rp + 1024), d = *(const f32x2*)(rp + 1536);
            const f32x2 r = (a + b) + (c + d);
            st32(k.rs, l * 4, k.memf(s + 1) + EXP((2 * red_col(j) + w) * 256), min(cvtpk(r[0], r[1]) ^ ph, 0xFFFFFFFEu));
        }
        STAMP(s, 3)
_Pragma("unroll 1")
        for (spins = 0; (int)__builtin_amdgcn_readfirstlane(fl) < s - 1; ++spins) {
            PCOUNT(1)
            __builtin_amdgcn_s_sleep(PSLEEP);
            if (spin_fail(k, spins)) break;
            fl = ld32(k.rs, 0, (k.p0 ^ 8192u) + CNT(C_X1, j));
        }
        __builtin_amdgcn_fence(__ATOMIC_ACQUIRE, "wavefront");
        y_wait(k, 2 * s + 3);
        R.yload<0, 8>(k, j, s + 1);
        arm(s + 2);
        y_wait(k, 2 * s + 4);
        STAMP(s, 4)
        R.yload<8, 16>(k, j, s + 1);
        PFLUSH(s)
    }
    PFLUSH(63)
    PROF_OUT
}
DEVI void tred_role(const Params& p, const Ctx& k, char* smem, int j) {
    PROF_DECL
    const int l = k.l, w = k.w, g = l >> 4, n = l & 15, T = 2 * red_col(j) + (w >> 1), rt = w & 1;
    const unsigned v16 = (g >> 1) * 1024 + (g & 1) * 256 + n * 16, yo = j * 32768 + EXP(w * 4096);
    const unsigned xo = EXP((T >> 1) * 2048) + (T & 1) * 512 + rt * 1024, xv = ((g >> 1) * 16 + n) * 16 + (g & 1) * 8;
    const u32x2 ones = {0xFFFFFFFFu, 0xFFFFFFFFu};
    const unsigned fl = (w == 0 && l == 0) ? 0u : OOB;
    unsigned fails = 0;
    Red R;
    RSTAMP(2)
    st64(k.rs, xv, k.segx(PG_X1, 0) + xo, ones); st64(k.rs, xv, k.segx(PG_X1, 1) + xo, ones);
    sleep_until(k.t0 + RSL);
    y_wait(k, 2);
    R.yload<0, 16>(k, j, 0);
    for (int s = 0; s < 64; ++s) {
        STOP(s) STAMP(s, 0)
        unsigned ap[4], am = k.ym(s) + yo, xa = k.segx(PG_X1, s) + xo, rb = (s & 1) * 16384 + l * 16, wb = rb + w * 1024, fv = s;
#pragma unroll
        for (int i = 0; i < 4; ++i) { ap[i] = k.p0 + PR_B + (s & 3) * PR_SZ + w * 49152 + (i < 2 ? 32768 : 16384) + (~i & 1) * 4096; PINS(ap[i]); }
        rb += w * 4096;
        PINS(am); PINS(xa); PIN(rb); PIN(wb); PIN(fv);
        const unsigned ph = (s & 4) ? 0x80008000u : 0u;
        unsigned spins = 0, tq, tv;
        u32x2 res;
        if (k.c) res = ld64(k.rs, xv, k.seg(PG_X2, s) + xo);
        else res = *(const u32x2*)(p.x + (size_t)(s * 32 + rt * 16 + n) * 640 + T * 16 + g * 4);
        f32x4 acc[2][2];
        WAIT_BC(s, TSLP)
        u32x4 yl[8];
#pragma unroll
        for (int i = 0; i < 8; ++i) { yl[i] = __builtin_shufflevector(R.Y[2 * i], R.Y[2 * i + 1], 0, 1, 4, 5); PIN(yl[i]); }
        f32x4 x = {blo(res[0] ^ ph), bhi(res[0] ^ ph), blo(res[1] ^ ph), bhi(res[1] ^ ph)};
        PIN(x);
        wait_until2(tv); wait_until2(tq + (s < 63 ? RDLY : 0));
        STAMP(s, 1)
        unsigned long long bad;
        do {
            asm volatile("" ::: "memory");
            u32x4 ym[4], pp[16];
#pragma unroll
            for (int i = 0; i < 4; ++i) { ym[i] = ld128(k.rs, v16 + (i >> 1) * 2048 + (i & 1) * 512, am); SCHED0(); }
#pragma unroll
            for (int i = 0; i < 16; ++i) { pp[i ^ 4] = ld128(k.rs, l * 16 + (i & 3) * 1024, ap[i >> 2]); SCHED0(); }
            unsigned mx = 0, tg, lim = 0x7FFFFFFFu | (fails << 15);
            ++fails; PINS(fails); PINS(lim);
#pragma unroll
            for (int i = 0; i < 4; ++i) mx = max(mx, umax4(ym[i]));
            tg = mx == 0xFFFFFFFFu ? 0x80000000u : 0u;
            u32x4 yf[4];
#pragma unroll
            for (int i = 0; i < 4; ++i) { const int t = i & 1, f = i >> 1; yf[i] = (u32x4){ym[2 * f][t], ym[2 * f][t + 2], ym[2 * f + 1][t], ym[2 * f + 1][t + 2]}; }
            asm volatile("s_nop 1" : "+v"(yf[0]), "+v"(yf[1]), "+v"(yf[2]), "+v"(yf[3]), "+v"(tg));
            SCHED0();
            sfor<32>([&](auto I) { constexpr int i = CV(I), t = i & 1, q = (i >> 1) ^ 4, r = q < 4 ? 0 : q < 8 ? (q >> 1) & 1 : 1;
                mfa<false, q == 4 || q == 6>(acc[t][r], __builtin_bit_cast(bf16x8, q < 4 ? yl[t * 4 + q] : q < 8 ? yf[(q & 1) * 2 + t] : R.Y[t * 8 + (q & 7)]), __builtin_bit_cast(bf16x8, pp[q]));
                SCHED0();
                tg |= __builtin_amdgcn_bitop3_b32(pp[q][2 * t], pp[q][2 * t + 1], ph, 0x7e); PIN(tg);
                if constexpr (i == 20 || i == 22) { asm volatile("" : "+v"(acc[(i - 20) / 2][0])); lw128(wb + (i - 20) * 4096, __builtin_bit_cast(u32x4, acc[(i - 20) / 2][0])); }
                if constexpr (i == TPOS) st32(k.rs, fl, (k.p0 ^ 8192u) + CNT(C_X1, j), fv);
                SCHED0(); });
            bad = __ballot(tg > lim);
        } while (__builtin_expect(bad != 0, 0));
        STAMP(s, 2)
        const unsigned yc = y_get(k);
        SCHED0();
        asm volatile("s_nop 2" : "+v"(acc[0][1]), "+v"(acc[1][1]));
        lw128(wb + 4096, __builtin_bit_cast(u32x4, acc[0][1])); lw128(wb + 12288, __builtin_bit_cast(u32x4, acc[1][1]));
        RAWBAR()
        {
            const f32x4 r0 = __builtin_bit_cast(f32x4, lr128(rb)), r1 = __builtin_bit_cast(f32x4, lr128(rb + 1024)), r2 = __builtin_bit_cast(f32x4, lr128(rb + 2048)), r3 = __builtin_bit_cast(f32x4, lr128(rb + 3072));
            SCHED0();
            if constexpr (TPOS == 32) st32(k.rs, fl, (k.p0 ^ 8192u) + CNT(C_X1, j), fv);
            SCHED0();
            const f32x4 r = (r0 + r1) + (r2 + r3);
            st64(k.rs, xv, xa, (u32x2){min(cvtpk(x[0] + bfr(r[0]), x[1] + bfr(r[1])) ^ ph, 0xFFFFFFFEu), min(cvtpk(x[2] + bfr(r[2]), x[3] + bfr(r[3])) ^ ph, 0xFFFFFFFEu)});
            SCHED0();
            if constexpr (TPOS == 33) st32(k.rs, fl, (k.p0 ^ 8192u) + CNT(C_X1, j), fv);
        }
        st64(k.rs, s < 62 ? xv : OOB, k.segx(PG_X1, (s + 2) & 63) + xo, ones);
        STAMP(s, 3)
        if (s < 63) {
            if (!__all((int)yc >= 2 * s + 4)) { PCOUNT(2) y_wait(k, 2 * s + 4); }
            __builtin_amdgcn_fence(__ATOMIC_ACQUIRE, "wavefront");
            R.yload<0, 16>(k, j, s + 1);
        }
        STAMP(s, 4)
        STAMP(s, 5)
        PFLUSH(s)
    }
    PROF_OUT
}

#undef TZ
#define TZ z0()
DEVI void up_role(const Params& p, const Ctx& k, char* smem, int id) {
    PROF_DECL
    const int l = k.l, w = k.w, g = l >> 4, n = l & 15;
    bf16x8 Wt[80];
    const int tile0 = id * 8 + w * 2;
    auto issue = [&](auto U) { constexpr int u = CV(U);
        sfor<8>([&](auto J) { constexpr int j = CV(J); dma1<(u % 3) * 32768 + j * 1024>(((w & 1) ? p.wu : p.wg) + (size_t)(2 * u + (w >> 1)) * 32 * 1536 + id * 128, wsrc<256, true>(j * 64 + l, 3072), w * 8192); }); };
    sfor<3>(issue);
    cstream<10>([](auto V) { return IC(CV(V) < 9 ? 8 : 0); },
        [&](auto U) { constexpr int u = CV(U);
            if constexpr (u > 0 && u < 8) issue(IC(u + 2));
#pragma unroll
            for (int i = 0; i < 8; ++i) Wt[(i & 3) * 20 + 2 * u + (i >> 2)] = wp_read<256, true>(smem + (u % 3) * 32768 + (i >> 1) * 8192, w * 2 + (i & 1), l); });
    RAWBAR()
    RSTAMP(2)
    bf16x8 one8 = {0x3F80, 0x3F80, 0x3F80, 0x3F80, 0x3F80, 0x3F80, 0x3F80, 0x3F80};
    asm volatile("s_nop 1" : "+v"(one8));
    f32x4 cs[4];
    sfor<80>([&](auto I) { constexpr int f = CV(I); mfa<(f < 64), f % 20 == 0>(cs[f / 20], Wt[f], one8); });
    asm volatile("s_nop 7" : "+v"(cs[0]), "+v"(cs[1]), "+v"(cs[2]), "+v"(cs[3]));
    const unsigned ho = EXP((tile0 >> 1) * 1024), l16 = l * 16;
    constexpr int L_ST = 81920;
    const u32x4 ones = {0xFFFFFFFFu, 0xFFFFFFFFu, 0xFFFFFFFFu, 0xFFFFFFFFu};
#pragma unroll
    for (int i = 0; i < 4; ++i) st128(k.rs, l16, k.hseg(i >> 1) + (i & 1) * EXP(49152) + ho, ones);
    unsigned fails = 0;
    lw32(L_ST + 1024, 0u);
    prog_init(k);
    const unsigned fl = (w == 0 && l == 0) ? 0u : OOB;
    const unsigned long long m1 = __ballot(n & 1), m2 = __ballot(n & 2);
    for (int s = 0; s < 64; ) {
        unsigned xs = k.seg(PG_X1, s) + w * 2048, xd = (s & 1) * 40960 + w * 2048;
        PINS(xs); PINS(xd);
        prog_wait<20>(k, k.p0 + CNT(C_X1, 0), s);
        STOP(s) STAMP(s, 0)
        xn_all(k, l16, xs, xd);
        const unsigned xb = (s & 1) * 40960 + l16, yb = xb + w * 2048, sp = L_ST + (s & 1) * 2048, rp = sp + n * 16;
        const unsigned dp = sp + (g == (n >> 2) ? n * 16 + w * 4 : 4096 + l * 4);
        f32x4 acc[2][4], st[4];
        bf16x8 x[20], x1[20], y0[5], y1[5];
        unsigned mx = 0;
        auto blk = [&](auto B, auto J) { constexpr int b = CV(B), j = CV(J);
            if constexpr (j == 0) asm volatile("s_waitcnt vmcnt(%0)\n\ts_barrier" :: "n"(8 - 2 * b) : "memory");
            if constexpr (j >= 1 && j < 5) x[4 * b + j - 1] = lr128(xb + (4 * b + j - 1) * 2048);
            if constexpr (j == 5) y0[b] = lr128(yb + b * 8192);
            if constexpr (j == 6) y1[b] = lr128(yb + b * 8192 + 1024); };
        sfor<7>([&](auto J) { blk(IC(0), J); });
        SCHED0();
        STAMP(s, 1)
        float mean[2], rden[2], e[4][4];
        f32x4 sa[2], sb[2];
        u32x4 bad, o[2];
        unsigned sv, fv, r, f0, f1, ha = k.hseg(s) + ho;
        PINS(ha);
        auto stat = [&](auto I) { constexpr int i = CV(I), rt = i / 8, j = i % 8;
            if constexpr (j == 0) asm volatile("v_add_f32 %0, %2, %3\n\tv_add_f32 %1, %4, %5" : "=&v"(mean[rt]), "=&v"(rden[rt]) : "v"(sa[rt][0]), "v"(sa[rt][1]), "v"(sb[rt][0]), "v"(sb[rt][1]));
            if constexpr (j == 1 || j == 2) asm volatile("v_add_f32 %0, %0, %2\n\tv_add_f32 %1, %1, %3" : "+v"(mean[rt]), "+v"(rden[rt]) : "v"(sa[rt][j + 1]), "v"(sb[rt][j + 1]));
            if constexpr (j == 3) asm volatile("v_mul_f32 %0, 0xbaccccCD, %0\n\tv_mul_f32 %1, 0x3acccccd, %1" : "+v"(mean[rt]), "+v"(rden[rt]));
            if constexpr (j == 4) asm volatile("v_fma_f32 %1, -%0, %0, %1\n\tv_cvt_pk_bf16_f32 %0, 0, %0\n\tv_max_f32 %1, 0, %1" : "+v"(mean[rt]), "+v"(rden[rt]));
            if constexpr (j == 5) asm volatile("v_cvt_pk_bf16_f32 %0, 0, %0\n\tv_add_f32 %0, 0x3727c5ac, %0\n\tv_cvt_pk_bf16_f32 %0, 0, %0" : "+v"(rden[rt]));
            if constexpr (j == 6) asm volatile("v_sqrt_f32 %0, %0" : "+v"(rden[rt]));
            if constexpr (j == 7) asm volatile("v_cvt_pk_bf16_f32 %0, 0, %0\n\tv_rcp_f32 %0, %0" : "+v"(rden[rt])); };
        auto ep = [&](auto RT, auto T, auto J) { constexpr int rt = CV(RT), t = CV(T), j = CV(J), c = (j & 1) * 2, u = j >= 20, q = u ? j - 20 : j;
            float (&a)[4] = e[t + 2 * u];
#define A2 "+v"(a[c]), "+v"(a[c + 1])
            if constexpr (j < 0 || j >= 30) { }
            else if constexpr (q < 2) asm volatile("v_fma_f32 %0, %2, %3, %5\n\tv_fma_f32 %1, %2, %4, %6" : "=&v"(a[c]), "=&v"(a[c + 1]) : "v"(mean[rt]), "v"(cs[t + 2 * u][c]), "v"(cs[t + 2 * u][c + 1]), "v"(acc[rt][t + 2 * u][c]), "v"(acc[rt][t + 2 * u][c + 1]));
            else if constexpr (q < 4) asm volatile("v_mul_f32 %0, %0, %2\n\tv_mul_f32 %1, %1, %2" : A2 : "v"(rden[rt]));
            else if constexpr (q < 6 || j == 18 || j == 19) asm volatile("v_cvt_pk_bf16_f32 %0, 0, %0\n\tv_cvt_pk_bf16_f32 %1, 0, %1" : A2);
            else if constexpr (j < 8) asm volatile("v_mul_f32 %0, 0xbfb8aa3b, %0\n\tv_mul_f32 %1, 0xbfb8aa3b, %1" : A2);
            else if constexpr (j < 12) asm volatile("v_exp_f32 %0, %0" : "+v"(a[j & 3]));
            else if constexpr (j < 14) asm volatile("v_add_f32 %0, 1.0, %0\n\tv_add_f32 %1, 1.0, %1" : A2);
            else if constexpr (j < 18) asm volatile("v_rcp_f32 %0, %0" : "+v"(a[(j - 14) & 3]));
            else if constexpr (q < 8) asm volatile("v_mul_f32 %0, %0, %2\n\tv_mul_f32 %1, %1, %3" : A2 : "v"(e[t][c]), "v"(e[t][c + 1]));
            else if constexpr (q == 8) asm volatile("v_cvt_pk_bf16_f32 %0, %0, %2\n\tv_cvt_pk_bf16_f32 %1, %1, %3" : "+v"(a[0]), "+v"(a[2]) : "v"(a[1]), "v"(a[3]));
            else { unsigned d0, d1; asm volatile("v_min_u32 %0, -2, %2\n\tv_min_u32 %1, -2, %3" : "=&v"(d0), "=&v"(d1) : "v"(a[0]), "v"(a[2])); o[rt][2 * t] = d0; o[rt][2 * t + 1] = d1; } };
        sfor<180>([&](auto I) {
            constexpr int a = CV(I), b = a / 8, j = (a % 8) ^ (b == 4 ? 4 : 0), ch = a / 20 - 1, ks = a < 40 ? 4 * b + (j & 3) : a % 20, rt = ch >= 4;
            constexpr int nt = a < 40 ? 0 : rt ? ch - 4 : (ch & 1) * 2 + (ch >> 1), f = nt * 20 + ks, i = a - 36;
            if constexpr (a >= 40 || j < 4) mfa<(f < 64), ks == 0>(acc[rt][nt], Wt[f], rt ? x1[ks] : x[ks]);
            else mfa<false, b == 0>(st[j & 3], (j & 1) ? (j & 2 ? y1[b] : y0[b]) : one8, j & 2 ? y1[b] : y0[b]);
            SCHED0();
            if constexpr (a >= 103 && a < 119) TOUCH(x1[(a - 99) % 20]);
            else if constexpr (a == 60) { r = __builtin_amdgcn_readfirstlane(bad[0]); PINS(r); }
            else if constexpr (a == 61) { r = (fails - r) >> 31; PINS(r); }
            else if constexpr (a == 62) { fails += r; PINS(fails); }
            else if constexpr (a == 63) { f0 = 2 * s - r; PINS(f0); }
            else if constexpr (a == 64) { f1 = f0 | 1; PINS(f1); }
            else if constexpr (a == 65) { r <<= 31; PINS(r); }
            else if constexpr (a >= 56 && (a < 92 || a >= 119)) __builtin_amdgcn_s_setprio(0);
            if constexpr (a < 32 && a % 8 == 7) asm volatile("" :: "v"(x[4 * b + 4]), "v"(x[4 * b + 5]), "v"(x[4 * b + 6]), "v"(x[4 * b + 7]), "v"(y0[b + 1]));
            if constexpr (a < 32 && a % 8 == 4) TOUCH(y1[b]);
            SCHED0();
            if constexpr (a == 15) lw32((rp ^ 2048) + 1024, z0());
            if constexpr (a < 32 && a % 8 < 7) blk(IC((b + 1) % 5), IC(a % 8));
            if constexpr (i == 0 || i == 4 || i == 5 || i == 8) x1[i / 2 - (i == 4 || i == 8)] = lr128(xb + 1024 + (i / 2 - (i == 4 || i == 8)) * 2048);
            if constexpr (a >= 92 && a < 108) x1[a - 88] = lr128(xb + 1024 + (a - 88) * 2048);
            SCHED0();
            if constexpr (a < 40 && j >= 4) { const u32x4 v = __builtin_bit_cast(u32x4, (j & 1) ? y1[b] : y0[b]); mx = umax3(mx, v[j & 2], v[(j & 2) + 1]); PIN(mx); }
            if constexpr (i == 1 || i == 3) {
                constexpr int rt2 = i == 3;
                asm volatile("" : "+v"(st[2 * rt2]), "+v"(st[2 * rt2 + 1]));
                const f32x4& q = st[2 * rt2 + 1];
                const float d = csel(csel(q[0], q[1], m1), csel(q[2], q[3], m1), m2);
                lw32(dp + rt2 * 512, __float_as_uint(st[2 * rt2][0])); lw32(dp + rt2 * 512 + 256, __float_as_uint(d));
            }
            if constexpr (i == 2) lw32(mx == 0xFFFFFFFFu ? sp + 1024 : dp + 4096, 4096u);
            if constexpr (i == 6) bar_lgkm<2>();
            if constexpr (i == 6) sa[0] = __builtin_bit_cast(f32x4, lr128(rp));
            if constexpr (i == 7) sb[0] = __builtin_bit_cast(f32x4, lr128(rp + 256));
            if constexpr (i == 9) sa[1] = __builtin_bit_cast(f32x4, lr128(rp + 512));
            if constexpr (i == 10) sb[1] = __builtin_bit_cast(f32x4, lr128(rp + 768));
            if constexpr (i == 11) bad = __builtin_bit_cast(u32x4, lr128(rp + 1024));
            if constexpr (i >= 12 && i < 20) stat(IC(i - 12));
            if constexpr (a == 40) { STAMP(s, 2) }
            if constexpr (a == UPOS0 || a == UPOS) st32(k.rs, fl, k.p0 + CNT(C_H, id), fv);
            if constexpr (a == 117) __builtin_amdgcn_raw_buffer_store_b128(o[0], k.rs, (int)sv, (int)ha, AUX_SC1);
            if constexpr (a == 118) TOUCH(o[0]);
            SCHED0();
            ep(IC(0), IC(0), IC(a - 56)); ep(IC(0), IC(1), IC(a - 86));
            if constexpr (a >= 116 && a < 124) stat(IC(a - 108));
            if constexpr (a == UPOS0 - 2 || a == UPOS - 2) { fv = a == UPOS - 2 ? f1 : f0; PIN(fv); }
            if constexpr (a == 80) { sv = l16 | r; PIN(sv); }
            if constexpr (a < 144) ep(IC(1), IC(0), IC(a - 124));
            else if constexpr (a < 164) ep(IC(1), IC(1), IC(a - 144));
            else ep(IC(1), IC(0), IC(a - 144));
            SCHED0();
        });
        STAMP(s, 3)
        asm volatile("s_nop 7" : "+v"(acc[1][3]));
        {
            const f32x4 u = (acc[1][3] + mean[1] * cs[3]) * rden[1];
            o[1][2] = packs(bfr(u[0]) * e[1][0], bfr(u[1]) * e[1][1]); o[1][3] = packs(bfr(u[2]) * e[1][2], bfr(u[3]) * e[1][3]);
        }
        st128(k.rs, sv, ha + EXP(49152), o[1]);
        if (r) { PCOUNT(0) RAWBAR() lw32(rp + 1024, 0u); RAWBAR() }
        st128(k.rs, s < 62 ? l16 : OOB, k.hseg((s + 2) & 63) + ho, ones); st128(k.rs, s < 62 ? l16 : OOB, k.hseg((s + 2) & 63) + EXP(49152) + ho, ones);
        STAMP(s, 4) STAMP(s, 5)
        PFLUSH(s)
        s = (f1 + 1) >> 1;
    }
    PROF_OUT
}

DEVI void down_role(const Params& p, const Ctx& k, char* smem, int id) {
    PROF_DECL
    const int l = k.l, w = k.w, g = l >> 4, n = l & 15, rt = id / 5, cg = id % 5;
    bf16x8 Wt[96];
    wstream<12, 8, 4>(smem + w * 32768,
        [&](auto U, unsigned sl) { constexpr int u = decltype(U)::value; sfor<8>([&](auto J) { constexpr int j = CV(J); dma1<(u % 4) * 8192 + j * 1024>(p.wd + (size_t)(w * 12 + u) * 32 * 640 + cg * 128, wsrc<256, false>(j * 64 + l, 1280), sl); }); },
        [&](auto U, char* sp) { constexpr int u = decltype(U)::value;
#pragma unroll
            for (int nt = 0; nt < 8; ++nt) Wt[u * 8 + nt] = wp_read<256, false>(sp, (nt + 2 * w) & 7, l); });
    RAWBAR()
    RSTAMP(2)
    const unsigned xo = EXP((cg * 4 + w) * 2048) + rt * 1024, xv = ((g >> 1) * 16 + n) * 16 + (g & 1) * 8;
    const u32x2 ones = {0xFFFFFFFFu, 0xFFFFFFFFu};
    const unsigned x2v = k.c ? xv : OOB;
    prog_init(k);
#pragma unroll
    for (int i = 0; i < 4; ++i) st64(k.rs, x2v, k.seg(PG_X2, i >> 1) + xo + (i & 1) * 512, ones);
    const __amdgpu_buffer_rsrc_t ro = __builtin_amdgcn_make_buffer_rsrc((void*)p.out, 0, 2048 * 1280, 0x00020000);
    const unsigned ov = k.c ? OOB : n * 1280 + g * 8, lim = k.c ? 0xFFFFFFFEu : 0xFFFFFFFFu, fl = (w == 0 && l == 0) ? 0u : OOB;
    unsigned fails = 0;
    for (int s = 0; s < 64; ++s) {
        unsigned la[3], ra = k.seg(PG_X1, s) + xo, oa = k.c ? k.seg(PG_X2, s) + xo : (s * 32 + rt * 16) * 1280 + (cg * 8 + w * 2) * 32, rb = (s & 1) * 32768 + l * 16, wb[3], fv = s;
#pragma unroll
        for (int q = 0; q < 3; ++q) { la[q] = k.hseg(s) + rt * EXP(49152) + EXP((w * 12 + q * 4) * 1024); wb[q] = rb + ((w + q + 1) & 3) * 6144 + q * 2048; PINS(la[q]); PIN(wb[q]); }
        rb += w * 6144;
        PINS(ra); PINS(oa); PIN(rb); PIN(fv);
        prog_wait<12>(k, k.p0 + CNT(C_H, 0), 2 * s + rt);
        STOP(s) STAMP(s, 0)
        f32x4 acc[8], r[6];
        u32x2 res[2];
        u32x4 hx[12];
        unsigned long long bad;
        do {
            asm volatile("" ::: "memory");
#pragma unroll
            for (int i = 0; i < 12; ++i) { hx[i] = ld128(k.rs, l * 16 + (i & 3) * 1024, la[i >> 2]); SCHED0(); }
#pragma unroll
            for (int t = 0; t < 2; ++t) res[t] = ld64(k.rs, xv + t * 512, ra);
            SCHED0();
            unsigned mx = 0;
            sfor<72>([&](auto I) { constexpr int i = CV(I), ks = i / 6, j = 2 + i % 6, f = ks * 8 + j;
                mfa<(f < 64), ks == 0>(acc[j], Wt[f], __builtin_bit_cast(bf16x8, hx[ks]));
                SCHED0();
                if constexpr (i % 6 < 2) { mx = umax3(mx, hx[ks][(i & 1) * 2], hx[ks][(i & 1) * 2 + 1]); PIN(mx); }
                if constexpr (i == 68) { bad = __ballot(mx == 0xFFFFFFFFu); PINS(bad); }
                if constexpr (i == 69) { bad = fails < 65536u ? bad : 0; PINS(bad); }
                if constexpr (i == 70) { ++fails; PINS(fails); }
                if constexpr (i == DPOS) st32(k.rs, fl, k.p0 + CNT(C_X2, rt * 5 + cg), fv);
                SCHED0(); });
        } while (__builtin_expect(bad != 0, 0));
        STAMP(s, 1)
        float x[8];
        sfor<24>([&](auto I) { constexpr int i = CV(I), ks = i / 2, j = i & 1, f = ks * 8 + j;
            mfa<(f < 64), ks == 0>(acc[j], Wt[f], __builtin_bit_cast(bf16x8, hx[ks]));
            SCHED0();
            if constexpr (i < 12 && !(i & 1)) { constexpr int jj = 2 + i / 2;
                asm volatile("" : "+v"(acc[jj])); lw128(wb[jj / 2 - 1] + (jj & 1) * 1024, __builtin_bit_cast(u32x4, acc[jj])); }
            if constexpr (i < 8 && (i & 1)) { x[i - 1] = blo(res[i >> 2][(i >> 1) & 1]); x[i] = bhi(res[i >> 2][(i >> 1) & 1]); PIN(x[i - 1]); PIN(x[i]); }
            if constexpr (i == 13) bar_lgkm<0>();
            if constexpr (i >= 13 && i < 19) r[i - 13] = __builtin_bit_cast(f32x4, lr128(rb + (i - 13) * 1024));
            if constexpr (i + 72 == DPOS) st32(k.rs, fl, k.p0 + CNT(C_X2, rt * 5 + cg), fv);
            SCHED0(); });
        r[0] += r[2] + r[4]; r[1] += r[3] + r[5];
        asm volatile("s_nop 3" : "+v"(acc[0]), "+v"(acc[1]), "+v"(r[0]), "+v"(r[1]));
#pragma unroll
        for (int t = 0; t < 2; ++t) {
            const f32x4 d = acc[t] + r[t];
            const u32x2 o = {min(cvtpk(x[4 * t] + bfr(d[0]), x[4 * t + 1] + bfr(d[1])), lim), min(cvtpk(x[4 * t + 2] + bfr(d[2]), x[4 * t + 3] + bfr(d[3])), lim)};
            st64(k.rs, x2v + t * 512, oa, o);
            __builtin_amdgcn_raw_buffer_store_b64(o, ro, (int)(ov + t * 32), (int)oa, 0);
        }
        STAMP(s, 2)
#pragma unroll
        for (int t = 0; t < 2; ++t) st64(k.rs, s < 62 ? x2v : OOB, k.seg(PG_X2, (s + 2) & 63) + xo + t * 512, ones);
        STAMP(s, 3) STAMP(s, 4) STAMP(s, 5)
        PFLUSH(s)
    }
    PROF_OUT
}

DEVI void warm(const Params& p, const Ctx& k, int xl, int hw) {
    set_m0(k.w * 256);
_Pragma("unroll 1")
    for (int it = hw; it < 32 * 15; it += k.c ? 40 : 64) {
        const int q = it < 120 ? it & 7 : 8 + (it - 120) % 24, t = it < 120 ? it >> 3 : (it - 120) / 24;
        const u16* b = q < 24 ? (q < 8 ? p.Wv : q < 16 ? p.Wk : p.Wq) + (xl * 8 + (q & 7)) * 192 : p.Wo + (xl * 8 + (q & 7)) * 122880;
        const unsigned ln = t * 64 + k.l, cl = min(((unsigned)(size_t)b >> 7) & 7u, 4u);
        if (k.c) sleep_until(k.t0 + WP + it * PP / 128);
        if (t < (q >= 24 ? PO : ((q < 8 ? PV : PQ) >> (cl * 4)) & 15))
            asm volatile("global_load_lds_dword %0, %1" :: "v"(q < 24 ? (639 - ln / 3) * 12288 + (ln % 3) * 128 : (191 - (ln >> 2)) * 1280 + 768 + (ln & 3) * 128), "s"(b) : "memory");
    }
    DRAIN();
_Pragma("unroll 1")
    for (int i = 3; i < 8; ++i) { sleep_until(k.t0 + i * 50); hello(k, 0); }
    DRAIN();
    RAWBAR()
}

DEVI int xcc_id() { return __builtin_amdgcn_s_getreg(20 | (0 << 6) | (3 << 11)) & 7; }

DEVI unsigned prepare(const Params& p, Ctx& k, char* smem, int xl, int i) {
    const int l = k.l, j = xl * 22 + i - 4;
    const u32x4 ones = {0xFFFFFFFFu, 0xFFFFFFFFu, 0xFFFFFFFFu, 0xFFFFFFFFu};
    unsigned vote = 0;
    if (i < 4) {
        const int row = ((i * 4 + xl) * 4 + k.w) * 4 + (l >> 4);
        const unsigned dst = k.seg(PG_XNA, i * 2 + (xl >> 1));
        u32x4 a[5];
#pragma unroll
        for (int c = 0; c < 5; ++c) a[c] = *(const u32x4*)(p.x + (size_t)row * 640 + (c * 16 + (l & 15)) * 8);
        ln_rows(a, l & 15, row & 31, [&](unsigned vo, int c, u32x4 o) { st128(k.rs, vo, dst + c * 16384, o); st128(k.rs, vo, dst + c * 16384 + 8192, o); });
    } else if (k.w == 3) {
        const unsigned cal = ((unsigned)(j % 44) << 21) + 7 * 16384 + (xl >> 1) * 4096 + (l & 7) * 16;
        int d = 0;
#pragma unroll 1
        for (int r = 0; r <= 2 * CALR; ++r) {
            unsigned long long c0, c1;
            st128(k.rs, cal, (r & 1) * 8192, ones);
            st128(k.rs, cal, (~r & 1) * 8192, ones);
            asm volatile("s_waitcnt vmcnt(1)\n\ts_memtime %0\n\ts_waitcnt vmcnt(0)\n\ts_memtime %1\n\ts_waitcnt lgkmcnt(0)" : "=&s"(c0), "=s"(c1) :: "memory");
            d = r ? min((int)(c1 - c0), 400) - d : 0;
        }
        vote = (unsigned)(d < 0) ^ (unsigned)(xcc_id() >> 2) ^ (unsigned)(i & 1);
    } else {
        if (k.w == 2 && j < 20) {
            const int e = j * 32 + (l >> 1), T = e >> 4, hf = (e >> 1) & 1, rr = ((e >> 2) & 3) * 2 + (e & 1);
            u32x4 v;
#pragma unroll
            for (int c = 0; c < 4; ++c) v[c] = min(*(const unsigned*)(p.mem0 + rr * 640 + T * 16 + 4 * c + 2 * hf), 0xFFFFFFFEu);
            st128(k.rs, (2u << 21) + EXP(e * 16) + (l & 1) * 8192, 0, v);
        }
        constexpr unsigned N0 = PR_B + PR_SZ, N1 = 40 * 16384, N2 = 32768;
        static_assert(N0 + N1 + N2 <= 4 * 88 * 3072 && NPAGES <= 44, "fill");
#pragma unroll
        for (int r = 0; r < 4; ++r) {
            const unsigned o = ((r * 88 + j) * 3 + k.w) * 1024;
            st128(k.rs, o < N0 + N1 + N2 ? l * 16 : OOB, o < N0 ? o : o < N0 + N1 ? ((unsigned)PG_YM << 21) + (o - N0) : (2u << 21) + 32768 + (o - N0 - N1), ones);
        }
    }
    if (k.w == 3) *(volatile LAS unsigned*)smem = vote;
    DRAIN();
    __builtin_amdgcn_s_waitcnt(0x0f70);
    XSTAMP(0)
    RAWBAR()
    return *(volatile LAS unsigned*)smem;
}
DEVI void master(const Params& p, const Ctx& k) {
    const int l = k.l;
    u32x2 v[4] = {{0u, 0u}, {0u, 0u}, {0u, 0u}, {0u, 0u}};
_Pragma("unroll 1")
    for (unsigned spins = 0; spins < SPIN_CAP; ++spins) {
#pragma unroll
        for (int q = 0; q < 4; ++q) v[q] |= cm_ld(k, (CM_IN + (q * 64 + l) * 128) | v[q][1]);
        if (__all((v[0][1] & v[1][1] & v[2][1] & v[3][1]) != 0)) break;
        asm volatile("" ::: "memory");
    }
    const unsigned long long a = __ballot(v[2][0] != 0), b = __ballot(v[3][0] != 0);
    const int n = __popcll(a) + __popcll(b);
    const unsigned long long g = n > 44 ? ~0ull : 0ull, u = (a & b) | ((a ^ b) & g);
    const unsigned long long t = (__popcll(a ^ b) > 2 || n > 80 || n < 8 ? g : ((u >> 4) & 0x3FFFFF) | ((u >> 36) << 22)) ^ 0xAAAAAAAAAAAAAAAAull;
#pragma unroll
    for (int q = 0; q < 4; ++q) cm_st(k, l * 128, CM_GO + q * 8192, (unsigned)t, (unsigned)(t >> 32) | 0x80000000u);
}

#define FIN(n) { __builtin_amdgcn_s_waitcnt(0x0f70); asm volatile("s_nop " #n); return; }
__global__ __launch_bounds__(256, 1) void mega_kernel(Params p) {
    extern __shared__ __attribute__((aligned(16))) char smem[];
    Ctx k;
    const u16 *wq = p.Wq, *wk = p.Wk, *wv = p.Wv;
    asm volatile("" : "+s"(wq), "+s"(wk), "+s"(wv) : "s"(p.cm));
    k.ws = p.ws; k.cm = p.cm;
    k.rs = __builtin_amdgcn_make_buffer_rsrc((void*)p.ws, 0, 0x7fffffff, 0x00020000);
    k.l = threadIdx.x & 63; k.w = __builtin_amdgcn_readfirstlane(threadIdx.x >> 6);
    RSTAMP(0)
    const int x = blockIdx.x & 7, i = blockIdx.x >> 3, xl = x & 3;
    k.role = x * 32 + i; k.c = x >> 2; k.par = 0; k.p0 = 0; k.t0 = rclk();
    line_st(k, CM_GO, 0, 0);
    auto start = [&]() {
        const u16* a = (i < 8 ? wk : wv) + (xl * 8 + (i & 7)) * 192, * b = (i < 8 ? wq : wv + 32 * 6144) + (xl * 8 + (i & 7)) * 192;
        unsigned o[3];
        sfor<18>([&](auto J) { constexpr int j = CV(J);
            if constexpr (j < 3) o[j] = wsrc<384, true>(k.w * 192 + j * 64 + k.l, 12288);
            dma1<SLOT(j / 6) + ((j / 3) & 1) * 12288 + (j % 3) * 1024>((((j / 3) & 1) ? b : a) + (j / 6) * (i < 8 ? 32 * 6144 : 64 * 6144), o[j % 3], k.w * 3072); }); };
    if (!k.c && i < 16) start();
    if (__builtin_expect(k.role == MASTER, 0)) {
#pragma unroll
        for (int q = 0; q < 16; ++q) cm_st(k, k.l * 8, CM_IN + k.w * 8192 + q * 512, 0, 0);
        cm_st(k, 0, CM_TMO, ~0u, ~0u);
        DRAIN();
        RAWBAR()
    }
    if (__builtin_expect(k.c && i < 26, 0)) { const unsigned v = prepare(p, k, smem, xl, i); RSTAMP(1) hello(k, v); go_wait(k, v); XSTAMP(1) RAWBAR() }
    else { RSTAMP(1) if (k.c) { DRAIN(); hello(k, 0); } }
    if (__builtin_expect(k.role == MASTER && k.w == 0, 0)) { master(p, k); __builtin_amdgcn_s_waitcnt(0x0f70); }
    if (k.c && i < 16) { sleep_until(k.t0 + WD1 + (i ^ 8) * STG); start(); }
    if (i < 8) { k.l = lane(); qk_role(p, k, smem, xl * 8 + i); FIN(0) }
    if (i < 16) { k.l = lane(); vo_role(p, k, smem, xl * 8 + i - 8); FIN(1) }
    k.l = lane();
    if (!k.c || i < 26) warm(p, k, xl, (i - 16) * 4 + k.w);
    else RAWBAR()
    go_wait(k, 0);
    if (i >= 26 && !k.c) {
        sleep_until(k.t0 + XPF);
#pragma unroll
        for (int r = 0; r < 3; ++r) asm volatile("global_load_lds_dword %0, %1" :: "v"(min(40 + r * 96 + ((i - 26) * 4 + xl) * 4 + k.w, 319) * 8192 + k.l * 128), "s"(p.x) : "memory");
        DRAIN();
        RAWBAR()
    }
    if (i >= 26) sleep_until(k.t0 + (k.c ? GST : GSA) + ((xl & 1) == UPODD ? 0 : i < 31 ? GSD : -GSA));
    if (i < 21) { mred_role(p, k, smem, xl * 5 + i - 16); FIN(2) }
    if (i < 26) { tred_role(p, k, smem, xl * 5 + i - 21); FIN(3) }
    if ((xl & 1) == UPODD) { up_role(p, k, smem, (xl >> 1) * 6 + i - 26); FIN(4) }
    if (i < 31) { down_role(p, k, smem, (xl >> 1) * 5 + i - 26); FIN(5) }
    row_role(p, k, smem, xl >> 1);
    FIN(6)
}

extern "C" void kernel_launch(void* const* d_in, const int* in_sizes, int n_in,
                              void* d_out, int out_size, void* d_ws, size_t ws_size,
                              hipStream_t stream) {
    Params p;
    p.x = (const u16*)d_in[0]; p.mem0 = (const u16*)d_in[1];
    p.Wq = (const u16*)d_in[2]; p.Wk = (const u16*)d_in[3]; p.Wv = (const u16*)d_in[4]; p.Wo = (const u16*)d_in[5];
    p.wg = (const u16*)d_in[8]; p.wu = (const u16*)d_in[9]; p.wd = (const u16*)d_in[10];
    p.out = (u16*)d_out; p.cm = (char*)d_ws;
    p.ws = (char*)(((uintptr_t)d_ws + CM_BYTES + ((1u << 21) - 1)) & ~(uintptr_t)((1u << 21) - 1));
    (void)hipFuncSetAttribute((const void*)mega_kernel, hipFuncAttributeMaxDynamicSharedMemorySize, LDS_BYTES);
    mega_kernel<<<256, 256, LDS_BYTES, stream>>>(p);
}
```

```cpp
#define SWAPG
#include <hip/hip_runtime.h>
#include <hip/hip_bf16.h>
#include <stdint.h>
#include <utility>

typedef unsigned short u16;
typedef __attribute__((ext_vector_type(16))) short bf16x16;
typedef __attribute__((ext_vector_type(8))) short bf16x8;
typedef __attribute__((ext_vector_type(4))) short bf16x4;
typedef __attribute__((ext_vector_type(8))) __bf16 hbf8;
typedef __attribute__((ext_vector_type(16))) __bf16 hbf16;
typedef __attribute__((ext_vector_type(4))) float f32x4;
typedef __attribute__((ext_vector_type(2))) float f32x2;
typedef __attribute__((ext_vector_type(8))) unsigned u32x8;
typedef __attribute__((ext_vector_type(4))) unsigned u32x4;
typedef __attribute__((ext_vector_type(2))) unsigned u32x2;
typedef __attribute__((ext_vector_type(2))) __bf16 bf16x2_t;
#define LAS __attribute__((address_space(3)))
#define GAS __attribute__((address_space(1)))
#define DEVI __device__ __forceinline__

#define PSLEEP 8
#define CPOS 34
#define EPOS 110
#define XA -5
#define SOFT2 1
#define YS 2
#define Y1 0
#define Y2 0
#define YB 58
#define ARMP 2
#define WD1 1000
#define GST 550
#define NTA " nt"
#define NTB ""
#define PQ 0x077A0
#define PV 0x4AAE4
#define GSA 1600
#define QGS 8
#define VGS 8
#define MN 480
#define MD 250
#define MP 100
#define RSL 1000
#define GSD 250
#define PO 0
#define PDLY 0
#define WP 600
#define PP 100
#define STG 0
#define UPOS0 88
#define DPOS 88
#define XPF 1400
#define TPOS 33
#define XS0 40
#define XP 23
#define XB 66
#define TB 107
#define PACE 65
#define XPOS 68
#define YPOS 40
#define XPOSY 1
#define PWS 1
#define WUS 0
#define WMIX 1
#define PACE2 30
#define GDLY 80
#define QDLY 0
#define CALR 1
#define SPG 3
#define DQ 16
#define DV 12
#define BSLP 1
#define SDLY 0
#define UPOS 150
#define RDLY 45
#define NOYST 0
#define NOTOK 0
#define NOYLD 0
#define GPOSY 8
#define OLDMAP 1
#define UPODD 0
#define XLD 10
#define TROT 8
#define KOUT 1
#define RPOS 69
#define RDY 90
#define APOS 6
#define GPOS 74

#define EXP(o) ((((unsigned)(o) >> 13) << 14) | ((unsigned)(o) & 8191u))
enum { C_XN = 0, C_Y = 1, C_X1 = 2, C_XB = 3, C_H = 4, C_X2 = 5, C_BC = 6 };
#define CNT(type, s) ((unsigned)((type) * 16384 + (s) * 128))
#define YLINE(w, h) CNT(C_Y + ((w) >> 1) * 6, ((w) & 1) * 32 + (h))
constexpr unsigned PR_B = EXP(65536), PR_SZ = EXP(12 * 8192);
constexpr unsigned DUMMY_B = EXP(0);
constexpr int PG_MEMF = 2;
#define YXR 8
constexpr int PG_YX = 3;
constexpr int PG_XNA = PG_YX + 2 * YXR;
constexpr int PG_X1 = PG_XNA + 4, PG_X2 = PG_X1 + 4;
constexpr int PG_H = PG_X2 + 4;
constexpr int PG_YM = PG_H + 8;
constexpr int NPAGES = PG_YM + 2;
static_assert(NPAGES <= 64, "map");
constexpr unsigned CM_TAB = 0, CM_TICKET = 1024, CM_TMO = 4096, CM_IN = 8192, CM_GO = 40960, CM_BYTES = 73728;
constexpr int MASTER = 5 * 32 + 31;
constexpr unsigned SPIN_CAP = 1u << 20;
#define AUX_SC1 16
#define LDS_BYTES (160 * 1024)

struct Params {
    const u16* x; const u16* mem0; const u16* Wq; const u16* Wk; const u16* Wv; const u16* Wo;
    const u16* wg; const u16* wu; const u16* wd; u16* out; char* cm; char* ws;
};
struct Ctx {
    char* ws; char* cm; __amdgpu_buffer_rsrc_t rs; unsigned long long par;
    int c, l, w, role;
    unsigned p0, t0;
    DEVI unsigned pg(int page) const { return ((unsigned)page << 21) | (((unsigned)(par >> page) & 1u) << 13); }
    DEVI unsigned pg0(int page) const { return pg(page) ^ ((unsigned)c << 13); }
    DEVI unsigned seg(int page, int s) const { return pg(page + (s >> 4)) + (unsigned)(s & 15) * 131072u; }
    DEVI unsigned seg0(int page, int s) const { return seg(page, s) ^ ((unsigned)c << 13); }
    DEVI unsigned segx(int page, int s) const { return seg(page, s) ^ 8192u; }
    DEVI unsigned hseg(int s) const { return pg(PG_H + (s >> 3)) + (unsigned)(s & 7) * 262144u; }
    DEVI unsigned yx(int sg, int wq) const { return pg(PG_YX + (sg % YXR) * 2 + (wq >> 1)) + (unsigned)(wq & 1) * 1048576u; }
    DEVI unsigned ym(int s) const { return pg(PG_YM + ((s >> 1) & 1)) + (unsigned)(s & 1) * EXP(327680); }
    DEVI unsigned memf(int s) const { return pg(PG_MEMF) + (unsigned)s * 32768u; }
    DEVI char* cnt(int type, int s) const { return ws + (p0 + CNT(type, s)); }
    DEVI char* cnt0(int type, int s) const { return ws + ((p0 ^ ((unsigned)c << 13)) + CNT(type, s)); }
};

DEVI unsigned cvtpk(float a, float b) { return __builtin_bit_cast(unsigned, __builtin_convertvector((f32x2){a, b}, bf16x2_t)); }
DEVI unsigned packs(float a, float b) { return min(cvtpk(a, b), 0xFFFFFFFEu); }
DEVI float blo(unsigned x) { return __uint_as_float(x << 16); }
DEVI float bhi(unsigned x) { return __uint_as_float(x & 0xFFFF0000u); }
DEVI float bfr(float f) { return __uint_as_float(cvtpk(0.f, f)); }
DEVI u32x4 ld128(__amdgpu_buffer_rsrc_t rs, unsigned v, unsigned s) { return __builtin_amdgcn_raw_buffer_load_b128(rs, (int)v, (int)s, AUX_SC1); }
DEVI u32x2 ld64(__amdgpu_buffer_rsrc_t rs, unsigned v, unsigned s) { return __builtin_amdgcn_raw_buffer_load_b64(rs, (int)v, (int)s, AUX_SC1); }
DEVI unsigned ld32(__amdgpu_buffer_rsrc_t rs, unsigned v, unsigned s) { return __builtin_amdgcn_raw_buffer_load_b32(rs, (int)v, (int)s, AUX_SC1); }
DEVI void st128(__amdgpu_buffer_rsrc_t rs, unsigned v, unsigned s, u32x4 x) {
    __builtin_amdgcn_raw_buffer_store_b128(x, rs, (int)v, (int)s, AUX_SC1);
    asm volatile("s_nop 1" :: "v"(x) : "memory");
}
DEVI void st64(__amdgpu_buffer_rsrc_t rs, unsigned v, unsigned s, u32x2 x) { __builtin_amdgcn_raw_buffer_store_b64(x, rs, (int)v, (int)s, AUX_SC1); }
DEVI void st32(__amdgpu_buffer_rsrc_t rs, unsigned v, unsigned s, unsigned x) { __builtin_amdgcn_raw_buffer_store_b32(x, rs, (int)v, (int)s, AUX_SC1); }
#define RAWBAR() { __builtin_amdgcn_s_waitcnt(0xc07f); __builtin_amdgcn_s_barrier(); }
#define BARL(n) { asm volatile("" ::: "memory"); __builtin_amdgcn_s_waitcnt(0xc07f | ((n) << 8)); __builtin_amdgcn_s_barrier(); asm volatile("" ::: "memory"); }
#define DRAIN() asm volatile("s_waitcnt vmcnt(0)" ::: "memory")
#define SCHED0() __builtin_amdgcn_sched_barrier(0)
#define OOB 0x80000000u
DEVI int opq(int x) { asm volatile("" : "+v"(x)); return x; }
#define LANE const int l = opq(k.l), g = l >> 4, n = l & 15; (void)g; (void)n;
DEVI unsigned umax3(unsigned a, unsigned b, unsigned c) { return max(max(a, b), c); }
DEVI unsigned umax4(u32x4 v) { return max(umax3(v[0], v[1], v[2]), v[3]); }
DEVI unsigned rclk() { return (unsigned)__builtin_amdgcn_s_memrealtime(); }
DEVI unsigned long long rt_async() { unsigned long long t; asm volatile("s_memrealtime %0" : "=s"(t)); return t; }
DEVI void lgkm0(unsigned long long& a) { asm volatile("s_waitcnt lgkmcnt(0)" : "+s"(a) :: "memory"); }
DEVI unsigned spack(unsigned lo, unsigned hi) { unsigned d; asm volatile("s_pack_ll_b32_b16 %0, %1, %2" : "=s"(d) : "s"(lo), "s"(hi)); return d; }
DEVI int lane() { int l; asm volatile("v_mbcnt_lo_u32_b32 %0, -1, 0\n\tv_mbcnt_hi_u32_b32 %0, -1, %0" : "=v"(l)); __builtin_assume(l >= 0 && l < 64); return l; }
DEVI void sleep_until(unsigned target) { while ((int)(rclk() - target) < 0) __builtin_amdgcn_s_sleep(4); }
DEVI float xor16max(float x) { auto r = __builtin_amdgcn_permlane16_swap(__float_as_uint(x), __float_as_uint(x), false, false); return __builtin_fmaxf(__uint_as_float(r[0]), __uint_as_float(r[1])); }
DEVI float xor32max(float x) { auto r = __builtin_amdgcn_permlane32_swap(__float_as_uint(x), __float_as_uint(x), false, false); return __builtin_fmaxf(__uint_as_float(r[0]), __uint_as_float(r[1])); }
DEVI float xor16add(float x) { auto r = __builtin_amdgcn_permlane16_swap(__float_as_uint(x), __float_as_uint(x), false, false); return __uint_as_float(r[0]) + __uint_as_float(r[1]); }
DEVI float xor32add(float x) { auto r = __builtin_amdgcn_permlane32_swap(__float_as_uint(x), __float_as_uint(x), false, false); return __uint_as_float(r[0]) + __uint_as_float(r[1]); }
DEVI float fold32(float a, float b) { auto r = __builtin_amdgcn_permlane32_swap(__float_as_uint(a), __float_as_uint(b), false, false); return __uint_as_float(r[0]) + __uint_as_float(r[1]); }
#define ROR16(x, n) __builtin_bit_cast(float, __builtin_amdgcn_update_dpp(0, __builtin_bit_cast(int, (x)), 0x120 + (n), 0xf, 0xf, false))
DEVI float row16sum(float x) { x += ROR16(x, 8); x += ROR16(x, 4); x += ROR16(x, 2); x += ROR16(x, 1); return x; }
DEVI unsigned z0() { unsigned z; asm volatile("v_mov_b32 %0, 0" : "=v"(z)); return z; }
DEVI void cnt_add(char* p) { __hip_atomic_fetch_add((GAS unsigned*)(p + z0()), z0() + 1u, __ATOMIC_RELAXED, __HIP_MEMORY_SCOPE_AGENT); }
DEVI unsigned cnt_get(char* p) { return __hip_atomic_load((GAS unsigned*)(p + z0()), __ATOMIC_RELAXED, __HIP_MEMORY_SCOPE_AGENT) + 1u; }
DEVI bool spin_fail(const Ctx& k, unsigned spins) {
    if (spins > SPIN_CAP || ((spins & 255u) == 255u && __builtin_amdgcn_readfirstlane(cnt_get(k.cm + CM_TMO)) != 0)) { cnt_add(k.cm + CM_TMO); return true; }
    return false;
}
DEVI __amdgpu_buffer_rsrc_t cm_rs(const Ctx& k) { return __builtin_amdgcn_make_buffer_rsrc((void*)k.cm, 0, CM_BYTES, 0x00020000); }
DEVI void cm_st(const Ctx& k, unsigned v, unsigned off, unsigned lo, unsigned hi) { asm volatile("buffer_store_dwordx2 %0, %1, %2, %3 offen sc1" :: "v"((u32x2){lo, hi}), "v"(v), "s"(cm_rs(k)), "s"(off) : "memory"); }
DEVI u32x2 cm_ld(const Ctx& k, unsigned off) { return ld64(cm_rs(k), off, 0); }
DEVI void line_st(const Ctx& k, unsigned off, unsigned lo, unsigned hi) { cm_st(k, (lane() & 15) * 8 + (k.w ? OOB : 0u), off + k.role * 128, lo, hi); }
DEVI void hello(const Ctx& k, unsigned vote) { line_st(k, CM_IN, vote, OOB); }
DEVI u32x2 go_ask(const Ctx& k) { u32x2 d; asm volatile("buffer_load_dwordx2 %0, off, %1, %2 sc1" : "=v"(d) : "s"(cm_rs(k)), "s"(CM_GO + k.role * 128) : "memory"); return d; }
DEVI void go_wait(Ctx& k, unsigned vote, u32x2 v = {0u, 0u}) {
_Pragma("unroll 1")
    for (unsigned spins = 0; __builtin_amdgcn_readfirstlane(v[1]) >= 0 && spins < SPIN_CAP; ++spins) {
        if (spins) { hello(k, vote); __builtin_amdgcn_s_sleep(4); }
        asm volatile("buffer_load_dwordx2 %0, off, %1, %2 sc1\n\ts_waitcnt vmcnt(0)" : "=v"(v) : "s"(cm_rs(k)), "s"(CM_GO + k.role * 128) : "memory");
    }
    k.par = ((unsigned long long)(unsigned)__builtin_amdgcn_readfirstlane(v[1]) << 32 | (unsigned)__builtin_amdgcn_readfirstlane(v[0])) ^ (k.c ? ~0ull : 0ull);
    k.p0 = k.pg(0);
    __builtin_amdgcn_fence(__ATOMIC_ACQUIRE, "wavefront");
}
DEVI void cnt_wait(const Ctx& k, char* c, unsigned need) {
_Pragma("unroll 1")
    for (unsigned spins = 0; (unsigned)__builtin_amdgcn_readfirstlane(cnt_get(c)) < need; ++spins) {
        __builtin_amdgcn_s_sleep(PSLEEP);
        if (spin_fail(k, spins)) break;
    }
    __builtin_amdgcn_fence(__ATOMIC_ACQUIRE, "wavefront");
}
DEVI unsigned xn_get(const Ctx& k, unsigned l16, int s) { return ld32(k.rs, opq(l16) & 0x380u, k.p0 + CNT(C_XN, 0)); }
DEVI bool xn_has(const Ctx& k, unsigned v, int s) { return __all((int)v >= s - (k.c ? 0 : 8)); }
DEVI void xn_wait(const Ctx& k, unsigned l16, int s) {
_Pragma("unroll 1")
    for (unsigned spins = 0; !xn_has(k, xn_get(k, l16, s), s); ++spins) {
        asm volatile("" ::: "memory");
        __builtin_amdgcn_s_sleep(2);
        if (spin_fail(k, spins)) break;
    }
    __builtin_amdgcn_fence(__ATOMIC_ACQUIRE, "wavefront");
}
constexpr unsigned L_PW = 150 * 1024;
DEVI void prog_init(const Ctx& k) { *(volatile LAS unsigned*)(size_t)(L_PW + k.l * 4) = ~0u; RAWBAR() }
template <int N> DEVI void prog_wait(const Ctx& k, unsigned a, int s) {
    unsigned n, t;
    asm volatile("s_mov_b32 m0, %8\n\ts_mov_b32 %0, 0\n\ts_branch 2f\n"
        "1:\n\ts_cmp_lg_u32 %6, 0\n\ts_cbranch_scc1 3f\n\tbuffer_load_dword %2, %3, %4 offen sc1 lds\n"
        "3:\n\ts_sleep %9\n\ts_add_u32 %0, %0, 1\n\ts_and_b32 vcc_lo, %0, 0x7ff\n\ts_cbranch_scc1 2f\n\t"
        "v_mov_b32 %1, 0\n\ts_bitcmp1_b32 %0, 23\n\ts_cbranch_scc0 5f\n\tglobal_store_dword %1, %1, %10 sc1\n"
        "5:\n\tglobal_load_dword %1, %1, %10 sc1\n\ts_waitcnt vmcnt(0)\n\tv_cmp_ne_u32 vcc, -1, %1\n\ts_cbranch_vccnz 4f\n"
        "2:\n\tds_read_b32 %1, %7\n\ts_waitcnt lgkmcnt(0)\n\tv_cmp_gt_i32 vcc, %5, %1\n\ts_cbranch_vccnz 1b\n4:"
        : "=&s"(n), "=&v"(t) : "v"(min(k.l, N - 1) * 128), "s"(k.rs), "s"(a), "s"(s), "s"(k.w), "v"(L_PW + k.l * 4), "n"(L_PW), "n"(PWS), "s"(k.cm + CM_TMO) : "memory", "vcc", "scc");
}
template <int OFF> DEVI void dma1(const void* sbase, unsigned voff, unsigned lds) {
    asm volatile("s_add_i32 m0, %2, %3\n\ts_nop 0\n\tglobal_load_lds_dwordx4 %0, %1" NTA :: "v"(voff), "s"(sbase), "s"(lds), "n"(OFF) : "memory", "scc");
}
DEVI void set_m0(unsigned lds) { asm volatile("s_mov_b32 m0, %0" :: "s"(lds) : "memory"); }
template <int OFF> DEVI void dmab(__amdgpu_buffer_rsrc_t rs, unsigned voff, unsigned soff) {
    asm volatile("buffer_load_dwordx4 %0, %1, %2 offen offset:%3 sc1 lds" :: "v"(voff), "s"(rs), "s"(soff), "n"(OFF) : "memory");
}
#define PIN(x) asm volatile("" : "+v"(x))
#define PINS(x) asm volatile("" : "+s"(x))
#define LEAD()
#define TOUCH(x) asm volatile("" :: "v"(x))
DEVI float csel(float a, float b, unsigned long long m) { float d; asm volatile("v_cndmask_b32_e64 %0, %1, %2, %3" : "=v"(d) : "v"(a), "v"(b), "s"(m)); return d; }
#define LO8(x) __builtin_shufflevector(x, x, 0, 1, 2, 3, 4, 5, 6, 7)
#define HI8(x) __builtin_shufflevector(x, x, 8, 9, 10, 11, 12, 13, 14, 15)
#define CAT8(a, b) __builtin_shufflevector(a, b, 0, 1, 2, 3, 4, 5, 6, 7, 8, 9, 10, 11, 12, 13, 14, 15)
template <bool AG> DEVI void smf(f32x4& c, const bf16x8& a, const bf16x16& w, unsigned ix) {
    if constexpr (AG) asm volatile("v_smfmac_f32_16x16x64_bf16 %0, %1, %2, %3" : "+v"(c) : "v"(a), "a"(w), "v"(ix));
    else asm volatile("v_smfmac_f32_16x16x64_bf16 %0, %1, %2, %3" : "+v"(c) : "v"(a), "v"(w), "v"(ix));
}
#define WTAB(X) X(0, "{a[0:7]}", "{a[0:3]}", "{a[4:7]}") X(1, "{a[8:15]}", "{a[8:11]}", "{a[12:15]}") X(2, "{a[16:23]}", "{a[16:19]}", "{a[20:23]}") X(3, "{a[24:31]}", "{a[24:27]}", "{a[28:31]}") X(4, "{a[32:39]}", "{a[32:35]}", "{a[36:39]}") X(5, "{a[40:47]}", "{a[40:43]}", "{a[44:47]}") X(6, "{a[48:55]}", "{a[48:51]}", "{a[52:55]}") X(7, "{a[56:63]}", "{a[56:59]}", "{a[60:63]}") X(8, "{a[64:71]}", "{a[64:67]}", "{a[68:71]}") X(9, "{a[72:79]}", "{a[72:75]}", "{a[76:79]}") X(10, "{a[80:87]}", "{a[80:83]}", "{a[84:87]}") X(11, "{a[88:95]}", "{a[88:91]}", "{a[92:95]}") X(12, "{a[96:103]}", "{a[96:99]}", "{a[100:103]}") X(13, "{a[104:111]}", "{a[104:107]}", "{a[108:111]}") X(14, "{a[112:119]}", "{a[112:115]}", "{a[116:119]}") X(15, "{a[120:127]}", "{a[120:123]}", "{a[124:127]}") X(16, "{a[128:135]}", "{a[128:131]}", "{a[132:135]}") X(17, "{a[136:143]}", "{a[136:139]}", "{a[140:143]}") X(18, "{a[144:151]}", "{a[144:147]}", "{a[148:151]}") X(19, "{a[152:159]}", "{a[152:155]}", "{a[156:159]}") X(20, "{a[160:167]}", "{a[160:163]}", "{a[164:167]}") X(21, "{a[168:175]}", "{a[168:171]}", "{a[172:175]}") X(22, "{a[176:183]}", "{a[176:179]}", "{a[180:183]}") X(23, "{a[184:191]}", "{a[184:187]}", "{a[188:191]}") X(24, "{a[192:199]}", "{a[192:195]}", "{a[196:199]}") X(25, "{a[200:207]}", "{a[200:203]}", "{a[204:207]}") X(26, "{a[208:215]}", "{a[208:211]}", "{a[212:215]}") X(27, "{a[216:223]}", "{a[216:219]}", "{a[220:223]}") X(28, "{a[224:231]}", "{a[224:227]}", "{a[228:231]}") X(29, "{a[232:239]}", "{a[232:235]}", "{a[236:239]}") X(30, "{a[240:247]}", "{a[240:243]}", "{a[244:247]}") X(31, "{a[248:255]}", "{a[248:251]}", "{a[252:255]}") X(32, "{v[96:103]}", "{v[96:99]}", "{v[100:103]}") X(33, "{v[104:111]}", "{v[104:107]}", "{v[108:111]}") X(34, "{v[112:119]}", "{v[112:115]}", "{v[116:119]}") X(35, "{v[120:127]}", "{v[120:123]}", "{v[124:127]}") X(36, "{v[128:135]}", "{v[128:131]}", "{v[132:135]}") X(37, "{v[136:143]}", "{v[136:139]}", "{v[140:143]}") X(38, "{v[144:151]}", "{v[144:147]}", "{v[148:151]}") X(39, "{v[152:159]}", "{v[152:155]}", "{v[156:159]}") X(40, "{v[160:167]}", "{v[160:163]}", "{v[164:167]}") X(41, "{v[168:175]}", "{v[168:171]}", "{v[172:175]}") X(42, "{v[176:183]}", "{v[176:179]}", "{v[180:183]}") X(43, "{v[184:191]}", "{v[184:187]}", "{v[188:191]}") X(44, "{v[192:199]}", "{v[192:195]}", "{v[196:199]}") X(45, "{v[200:207]}", "{v[200:203]}", "{v[204:207]}") X(46, "{v[208:215]}", "{v[208:211]}", "{v[212:215]}") X(47, "{v[216:223]}", "{v[216:219]}", "{v[220:223]}") X(48, "{v[224:231]}", "{v[224:227]}", "{v[228:231]}") X(49, "{v[232:239]}", "{v[232:235]}", "{v[236:239]}") X(50, "{v[240:247]}", "{v[240:243]}", "{v[244:247]}") X(51, "{v[248:255]}", "{v[248:251]}", "{v[252:255]}")
template <int ID> DEVI void smfw(f32x4& c, const bf16x8& a, const bf16x16& w, unsigned ix) {
#define X(i, P, L, H) if constexpr (ID == i) asm volatile("v_smfmac_f32_16x16x64_bf16 %0, %1, %2, %3" : "+v"(c) : "v"(a), P(w), "v"(ix));
    WTAB(X)
#undef X
}
template <int ID, int HF, bool WB, bool FIRST> DEVI void mfw(f32x4& c, const bf16x16& w, const bf16x8& x) {
#define Y(R, W8) { if constexpr (FIRST && WB) asm volatile("v_mfma_f32_16x16x32_bf16 %0, %2, %1, 0" : "=&v"(c) : R(W8), "v"(x)); \
    else if constexpr (FIRST) asm volatile("v_mfma_f32_16x16x32_bf16 %0, %1, %2, 0" : "=&v"(c) : R(W8), "v"(x)); \
    else if constexpr (WB) asm volatile("v_mfma_f32_16x16x32_bf16 %0, %2, %1, %0" : "+v"(c) : R(W8), "v"(x)); \
    else asm volatile("v_mfma_f32_16x16x32_bf16 %0, %1, %2, %0" : "+v"(c) : R(W8), "v"(x)); }
#define X(i, P, L, H) if constexpr (ID == i) { if constexpr (HF) Y(H, HI8(w)) else Y(L, LO8(w)) }
    WTAB(X)
#undef X
#undef Y
}
template <bool AG, bool FIRST> DEVI void mfa(f32x4& c, const bf16x8& w, const bf16x8& x) {
    if constexpr (AG && FIRST) asm volatile("v_mfma_f32_16x16x32_bf16 %0, %1, %2, 0" : "=&v"(c) : "a"(w), "v"(x));
    else if constexpr (AG) asm volatile("v_mfma_f32_16x16x32_bf16 %0, %1, %2, %0" : "+v"(c) : "a"(w), "v"(x));
    else if constexpr (FIRST) asm volatile("v_mfma_f32_16x16x32_bf16 %0, %1, %2, 0" : "=&v"(c) : "v"(w), "v"(x));
    else asm volatile("v_mfma_f32_16x16x32_bf16 %0, %1, %2, %0" : "+v"(c) : "v"(w), "v"(x));
}
template <bool AG, bool FIRST> DEVI void mfb(f32x4& c, const bf16x8& x, const bf16x8& w) {
    if constexpr (AG && FIRST) asm volatile("v_mfma_f32_16x16x32_bf16 %0, %1, %2, 0" : "=&v"(c) : "v"(x), "a"(w));
    else if constexpr (AG) asm volatile("v_mfma_f32_16x16x32_bf16 %0, %1, %2, %0" : "+v"(c) : "v"(x), "a"(w));
    else if constexpr (FIRST) asm volatile("v_mfma_f32_16x16x32_bf16 %0, %1, %2, 0" : "=&v"(c) : "v"(x), "v"(w));
    else asm volatile("v_mfma_f32_16x16x32_bf16 %0, %1, %2, %0" : "+v"(c) : "v"(x), "v"(w));
}
DEVI f32x4 mf(const bf16x8& a, const bf16x8& b, const f32x4& c) { return __builtin_amdgcn_mfma_f32_16x16x32_bf16(__builtin_bit_cast(hbf8, a), __builtin_bit_cast(hbf8, b), c, 0, 0, 0); }
template <int N, class F> DEVI void sfor(F f) { [&]<int... I>(std::integer_sequence<int, I...>) { (f(std::integral_constant<int, I>{}), ...); }(std::make_integer_sequence<int, N>{}); }
DEVI bf16x8 lds128(const char* p) { return *(const bf16x8*)p; }
DEVI bf16x4 ldstr(const char* p) { return __builtin_amdgcn_ds_read_tr16_b64_v4i16((LAS bf16x4*)p); }
DEVI bf16x8 ldstr2(const char* p, int d) { bf16x4 a = ldstr(p), b = ldstr(p + d); return __builtin_shufflevector(a, b, 0, 1, 2, 3, 4, 5, 6, 7); }

#define STAMP(s, k)
#define QSTAMP(f, i)
#define STOP(s)
#define PFLUSH(s)
#define PROF_DECL
#define RSTAMP(i)
#define XSTAMP(i)
#define PCOUNT(i)
#define PROF_OUT

#define HSTAMP STAMP
#define HFLUSH PFLUSH
#define HTRACE(s)
#define TROUT
#define TRHINT
#define TPQ(n)
#define TPV(n)
#define AQ(m, i)
#define AC3(i, j, k) { }
#define AST(i)
#define AAC(i)
#define ACCM 0
#define HSTOP(m) if constexpr ((ACCM != 0) == m) { STOP(0) }
#define PC8(i, x)
#define PROF_ONLY(x)
#define PROF_S(x)
template <int RB, bool STD> DEVI int swz(int row) { return RB == 384 ? ((row >> 1) & 1) | ((row >> 2) & 2) : RB == 128 ? (row >> 1) & 3 : STD ? (row & 3) | ((row >> 1) & 4) : row & 7; }
template <int RB, bool STD> DEVI unsigned wsrc(unsigned id, int ld) { const unsigned row = id / (RB / 16), c = id % (RB / 16); return row * ld + ((c ^ (swz<RB, STD>(row) << 1)) << 4); }
template <int RB, bool STD> DEVI int wp_adr(int T, int l) { const int row = (STD ? 8 : 4) * (l >> 4) + ((l >> 2) & 3); return row * RB + ((T ^ swz<RB, STD>(row)) << 5) + (l & 3) * 8; }
template <int RB, bool STD> DEVI bf16x8 wp_read(const char* blk, int T, int l) { return ldstr2(blk + wp_adr<RB, STD>(T, l), (STD ? 4 : 16) * RB); }
template <int NU, class CNT, class STEP> DEVI void cstream(CNT cnt, STEP step) {
    sfor<NU>([&](auto V) { constexpr int v = decltype(V)::value;
        asm volatile("s_waitcnt vmcnt(%0) lgkmcnt(0)\n\ts_barrier" :: "n"(decltype(cnt(V))::value * (v ? 1 : 2)) : "memory");
        step(V);
    });
}
template <int NU, int NI, int D, class ISSUE, class READ> DEVI void wstream(char* ring, ISSUE issue, READ read) {
    const unsigned base = (unsigned)(size_t)(LAS char*)ring;
    sfor<D>([&](auto U) { constexpr int u = decltype(U)::value; if constexpr (u < NU) issue(U, base); });
    sfor<NU>([&](auto U) {
        constexpr int u = decltype(U)::value, left = (NU - u < D ? NU - u : D) - 1;
        asm volatile("s_waitcnt vmcnt(%0)" :: "n"(left * NI) : "memory");
        read(U, ring + (u % D) * NI * 1024);
        __builtin_amdgcn_s_waitcnt(0xc07f);
        asm volatile("" ::: "memory");
        if constexpr (u + D < NU) issue(std::integral_constant<int, u + D>{}, base);
    });
}

constexpr int L_XN = 0;
constexpr int L_K = 40960;
constexpr int L_MEM = L_K + 12288;
constexpr int L_QM = L_MEM + 10240;
constexpr int L_Q = L_QM + 12288;
constexpr int L_W = 94208;
#define SLOT(i) ((i) % 3 == 0 ? 45056 : (i) % 3 == 1 ? 69632 : L_W + 40960)
#define CMIN(a, b) ((a) < (b) ? (a) : (b))
#define IC(x) std::integral_constant<int, (x)>{}
#define CV(X) decltype(X)::value
DEVI bf16x8 lr128(unsigned a) { return *(const LAS bf16x8*)(size_t)a; }
DEVI bf16x4 lrtr(unsigned a) { return __builtin_amdgcn_ds_read_tr16_b64_v4i16((LAS bf16x4*)(size_t)a); }
#define CAT4(a, b) __builtin_shufflevector(a, b, 0, 1, 2, 3, 4, 5, 6, 7)
DEVI void lw128(unsigned a, u32x4 v) { *(LAS u32x4*)(size_t)a = v; }
DEVI void lw64(unsigned a, u32x2 v) { *(LAS u32x2*)(size_t)a = v; }
DEVI void lw32(unsigned a, unsigned v) { *(LAS unsigned*)(size_t)a = v; }
template <int N> DEVI void bar_lgkm() { asm volatile("s_waitcnt lgkmcnt(%0)\n\ts_barrier" :: "n"(N) : "memory"); }
DEVI void atom1(__amdgpu_buffer_rsrc_t rs, unsigned voff, unsigned soff) {
    unsigned one; asm volatile("v_mov_b32 %0, 1\n\tbuffer_atomic_add %0, %1, %2, %3 offen" : "=&v"(one) : "v"(voff), "s"(rs), "s"(soff) : "memory");
}
DEVI unsigned vadd(unsigned v, unsigned sc) { unsigned d; asm volatile("v_add_u32 %0, %1, %2" : "=v"(d) : "s"(sc), "v"(v)); return d; }
DEVI float vzero() { float z; asm volatile("v_mov_b32 %0, 0" : "=v"(z)); return z; }
template <int S> DEVI unsigned slot_adr(unsigned a) { if constexpr (S % 3 == 2) return vadd(a, SLOT(2) - SLOT(0)); else return a + (SLOT(S) - SLOT(0)); }
template <int B, int I> DEVI void xn_op(const Ctx& k, unsigned v, unsigned src, unsigned dst) {
    if constexpr (I == 0) set_m0(dst + B * 8192);
    else if constexpr (I == 1) dmab<0>(k.rs, v, src + B * 16384);
    else dmab<1024>(k.rs, v, src + B * 16384);
}
DEVI void xn_all(const Ctx& k, unsigned v, unsigned src, unsigned dst) {
    sfor<5>([&](auto B) { xn_op<CV(B), 0>(k, v, src, dst); asm volatile("s_nop 0"); xn_op<CV(B), 1>(k, v, src, dst); xn_op<CV(B), 2>(k, v, src, dst); });
}
struct Xn {
    unsigned src, dst, sg, need, t, sb, se, l16, c, off;
    unsigned long long flip, m;
    DEVI void init(const Ctx& k) {
        src = k.seg(PG_XNA, 0) + k.w * 2048; dst = L_XN + k.w * 2048; sg = 0; off = ~0u;
        const unsigned q = (unsigned)(k.par >> PG_XNA), d = q ^ (q >> 1);
        flip = ((unsigned long long)(d & 1) << 16) | ((unsigned long long)(d & 2) << 31) | ((unsigned long long)(d & 4) << 46);
    }
    template <int I> DEVI void next(const Ctx& k, unsigned l16) {
        if constexpr (I == 0) { sg += 1; src += 131072; PINS(sg); PINS(src); }
        if constexpr (I == 1) asm volatile("s_bitcmp1_b64 %1, %2\n\ts_cselect_b32 %0, 0x2000, 0" : "=s"(t) : "s"(flip), "s"(sg) : "scc");
        if constexpr (I == 2) { src ^= t; PINS(src); }
        if constexpr (I == 3) asm volatile("v_and_b32 %0, 0x380, %1" : "=v"(c) : "v"(l16));
        if constexpr (I == 4) c = ld32(k.rs, c, k.p0 + CNT(C_XN, 0));
        if constexpr (I == 5) { need = sg - (k.c ? 0u : 8u); PINS(need); }
        if constexpr (I == 6) { need = sg < 64u ? need : 0x7FFFFFFFu; PINS(need); }
        if constexpr (I == 7) asm volatile("v_cmp_gt_i32_e64 %0, %1, %2" : "=s"(m) : "s"(need), "v"(c) : "memory");
        if constexpr (I == 8) asm volatile("s_cmp_lg_u64 %2, 0\n\ts_cselect_b32 %0, -1, 0\n\ts_cselect_b32 %1, 0x200000, %3" : "=&s"(off), "=s"(se) : "s"(m), "s"(src) : "scc");
        if constexpr (I == 13) set_m0(L_MEM);
        if constexpr (I == 14) dmab<0>(k.rs, c, k.p0 + CNT(C_XN, 0));
        if constexpr (I == 15) c = *(volatile LAS unsigned*)(size_t)(l16 + L_MEM);
        if constexpr (I == 11) { need = sg < 64u ? need : 0x80000000u; PINS(need); }
        if constexpr (I == 12) {
            if (__builtin_expect(m != 0, 0)) {
_Pragma("unroll 1")
                for (unsigned spins = 0; __any((int)*(volatile LAS unsigned*)(size_t)(l16 + L_MEM) < (int)need) && !spin_fail(k, spins >> 2); ++spins) { next<3>(k, l16); __builtin_amdgcn_s_sleep(1); next<14>(k, l16); }
            }
            se = src; }
        if constexpr (I == 10) asm volatile("s_cmp_lg_u32 %1, 64\n\ts_cselect_b32 %0, %0, 0" : "+s"(off) : "s"(sg) : "scc");
    }
    template <int B, int I> DEVI void op(const Ctx& k) {
        if constexpr (I == 0) { set_m0(dst + B * 8192); sb = se + B * 16384; PINS(sb); }
        else dmab<(I - 1) * 1024>(k.rs, l16, sb);
    }
    template <int NB> DEVI void late(const Ctx& k, unsigned l16) {
        xn_wait(k, l16, sg);
        se = src;
        sfor<NB>([&](auto B) { op<CV(B), 0>(k); asm volatile("s_nop 0"); op<CV(B), 1>(k); op<CV(B), 2>(k); });
        DRAIN();
    }
};
template <int OFF> DEVI u32x4 ald128(__amdgpu_buffer_rsrc_t rs, unsigned v, unsigned s) { u32x4 d; asm volatile("buffer_load_dwordx4 %0, %1, %2, %3 offen offset:%4 sc1" : "=v"(d) : "v"(v), "s"(rs), "s"(s), "n"(OFF) : "memory"); return d; }
#define MADR(b) (L_MEM + ((b) < 6 ? ((b) - 1) * 2048 : ((b) - 6) * 2048 + 3072))
struct Gather {
    u32x4 p0, p1, v0, v1;
    unsigned mx, a, aw, b, base, wo, dst;
    unsigned long long bad;
    DEVI void init(const Ctx& k) { base = k.memf(0); wo = k.w * 1024; dst = L_MEM + k.w * 2048; }
    template <int I> DEVI void op(const Ctx& k, unsigned l16, int s) {
        if constexpr (I == 0) { a = ((unsigned)s << 15) & 0x1F8000u; PINS(a); }
        if constexpr (I == 1) { a += base; aw = a + wo; PINS(a); PINS(aw); }
        if constexpr (I == 2) { b = aw + 3072 + (k.w >> 1) * 8192; PINS(b); set_m0(dst); }
        if constexpr (I == 3) v0 = ald128<2048>(k.rs, l16, aw);
        if constexpr (I == 4) v1 = ald128<3072>(k.rs, l16, b);
        if constexpr (I == 5) dmab<2048>(k.rs, l16, aw);
        if constexpr (I == 6) dmab<3072>(k.rs, l16, b);
        if constexpr (I == 7) p0 = ald128<0>(k.rs, l16, a);
        if constexpr (I == 8) p1 = ald128<1024>(k.rs, l16, a);
    }
    template <int N> DEVI void wait() { asm volatile("s_waitcnt vmcnt(%4)" : "+v"(p0), "+v"(p1), "+v"(v0), "+v"(v1) : "n"(N) : "memory"); }
    template <int I> DEVI void fin(unsigned other) {
#define MX3 "v_max3_u32 %0, %0, %3, %4\n\tv_max3_u32 %0, %0, %5, %6"
        if constexpr (I == 0) asm volatile("v_max3_u32 %0, %7, %1, %2\n\t" MX3 : "=&v"(mx) : "v"(v0[0]), "v"(v0[1]), "v"(v0[2]), "v"(v0[3]), "v"(v1[0]), "v"(v1[1]), "s"(other));
        if constexpr (I == 1) asm volatile("v_max3_u32 %0, %0, %1, %2\n\t" MX3 : "+v"(mx) : "v"(v1[2]), "v"(v1[3]), "v"(p0[0]), "v"(p0[1]), "v"(p0[2]), "v"(p0[3]));
        if constexpr (I == 2) asm volatile("v_max3_u32 %1, %1, %2, %3\n\tv_max3_u32 %1, %1, %4, %5\n\tv_cmp_eq_u32_e64 %0, -1, %1" : "=s"(bad) : "v"(mx), "v"(p1[0]), "v"(p1[1]), "v"(p1[2]), "v"(p1[3]));
    }
    DEVI void also(unsigned other) { asm volatile("v_mov_b32 %0, %1" : "=v"(mx) : "s"(other)); }
    DEVI void all() {
        asm volatile("s_waitcnt vmcnt(0)\n\tv_max3_u32 %1, %1, %2, %3\n\tv_max3_u32 %1, %1, %4, %5\n\tv_max3_u32 %1, %1, %6, %7\n\tv_max3_u32 %1, %1, %8, %9\n\tv_max3_u32 %1, %1, %10, %11\n\tv_max3_u32 %1, %1, %12, %13\n\t"
            "v_max3_u32 %1, %1, %14, %15\n\tv_max3_u32 %1, %1, %16, %17\n\tv_cmp_eq_u32_e64 %0, -1, %1" : "=s"(bad) : "v"(mx), "v"(v0[0]), "v"(v0[1]), "v"(v0[2]), "v"(v0[3]), "v"(v1[0]), "v"(v1[1]), "v"(v1[2]), "v"(v1[3]),
            "v"(p0[0]), "v"(p0[1]), "v"(p0[2]), "v"(p0[3]), "v"(p1[0]), "v"(p1[1]), "v"(p1[2]), "v"(p1[3]) : "memory");
    }
    DEVI void check() { also(0); all(); asm volatile("s_nop 3"); }
    template <int I> DEVI void early(unsigned other) {
        if constexpr (I == 0) asm volatile("s_waitcnt vmcnt(5)\n\tv_max3_u32 %0, %5, %1, %2\n\tv_max3_u32 %0, %0, %3, %4" : "=&v"(mx) : "v"(v0[0]), "v"(v0[1]), "v"(v0[2]), "v"(v0[3]), "s"(other) : "memory");
        if constexpr (I == 1) __builtin_amdgcn_s_setprio(0);
        if constexpr (I == 2) asm volatile("s_waitcnt vmcnt(4)\n\tv_max3_u32 %0, %0, %1, %2\n\tv_max3_u32 %0, %0, %3, %4" : "+v"(mx) : "v"(v1[0]), "v"(v1[1]), "v"(v1[2]), "v"(v1[3]) : "memory");
    }
    DEVI void rest() {
        asm volatile("s_waitcnt vmcnt(1)\n\tv_max3_u32 %1, %1, %2, %3\n\tv_max3_u32 %1, %1, %4, %5\n\ts_waitcnt vmcnt(0)\n\tv_max3_u32 %1, %1, %6, %7\n\tv_max3_u32 %1, %1, %8, %9\n\tv_cmp_eq_u32_e64 %0, -1, %1" : "=s"(bad) : "v"(mx),
            "v"(p0[0]), "v"(p0[1]), "v"(p0[2]), "v"(p0[3]), "v"(p1[0]), "v"(p1[1]), "v"(p1[2]), "v"(p1[3]) : "memory");
    }
    unsigned rv, rs; unsigned long long rt;
    template <int I> DEVI void ask(const Ctx& k, unsigned l16) {
        if constexpr (I == 0) asm volatile("v_and_b32 %0, 0xf0, %1\n\tv_lshlrev_b32 %0, 1, %0" : "=v"(rv) : "v"(l16));
        if constexpr (I == 1) asm volatile("buffer_load_dword %0, %0, %1, %2 offen sc1" : "+v"(rv) : "s"(k.rs), "s"(k.p0 + CNT(C_XB, 0)) : "memory");
    }
    template <int I, int N = 4> DEVI void red() {
        if constexpr (I == 0) asm volatile("s_waitcnt vmcnt(%1)\n\tv_max_u32_dpp %0, %0, %0 quad_perm:[1,0,3,2] row_mask:0xf bank_mask:0xf" : "+v"(rv) : "n"(N));
        if constexpr (I == 1) asm volatile("v_max_u32_dpp %0, %0, %0 quad_perm:[2,3,0,1] row_mask:0xf bank_mask:0xf" : "+v"(rv));
        if constexpr (I == 2) asm volatile("v_max_u32_dpp %0, %0, %0 row_ror:4 row_mask:0xf bank_mask:0xf" : "+v"(rv));
        if constexpr (I == 3) asm volatile("v_max_u32_dpp %0, %0, %0 row_ror:8 row_mask:0xf bank_mask:0xf" : "+v"(rv));
        if constexpr (I == 4) asm volatile("v_readfirstlane_b32 %0, %1" : "=s"(rs) : "v"(rv));
    }
    template <int I> DEVI void hold(unsigned step) {
        if constexpr (I == 0) rt = rt_async();
        if constexpr (I == 3) { PINS(rt); b = spack((unsigned)rt, step); }
        if constexpr (I == 4) asm volatile("s_sub_u32 %0, %1, %0" : "+s"(b) : "s"(rs) : "scc");
        if constexpr (I == 5) if (__builtin_expect(b < 128u, 0)) { while ((int)((rclk() - rs) << 16) < 0) { } }
    }
    template <int I> DEVI void at(const Ctx& k, unsigned l16) {
        if constexpr (I == 1) op<2>(k, l16, 0);
        if constexpr (I >= 2 && I < 15 && (I % 5 == 2 || I % 5 == 4)) op<3 + (I / 5) * 2 + (I % 5) / 4>(k, l16, 0);
    }
    DEVI void again(const Ctx& k, unsigned l16) {
        asm volatile("s_mov_b32 m0, %9\n\tbuffer_load_dwordx4 %2, %4, %5, %7 offen offset:2048 sc1\n\tbuffer_load_dwordx4 %3, %4, %5, %8 offen offset:3072 sc1\n\t"
            "buffer_load_dwordx4 %4, %5, %7 offen offset:2048 sc1 lds\n\tbuffer_load_dwordx4 %4, %5, %8 offen offset:3072 sc1 lds\n\t"
            "buffer_load_dwordx4 %0, %4, %5, %6 offen sc1\n\tbuffer_load_dwordx4 %1, %4, %5, %6 offen offset:1024 sc1\n\ts_waitcnt vmcnt(0)"
            : "+v"(p0), "+v"(p1), "+v"(v0), "+v"(v1) : "v"(l16), "s"(k.rs), "s"(a), "s"(aw), "s"(b), "s"(dst) : "memory");
        check();
    }
};
DEVI void clr2(f32x4& c) { f32x2 a, b; asm volatile("v_mov_b64 %0, 0\n\tv_mov_b64 %1, 0" : "=v"(a), "=v"(b)); c = __builtin_shufflevector(a, b, 0, 1, 2, 3); }
#define NOP7_3(a) asm volatile("s_nop 7" : "+v"(a[0]), "+v"(a[1]), "+v"(a[2]))

struct G3 {
    f32x4 a0[3], a1[3];
    bf16x8 x0[20], x1[20], wl[20];
    unsigned l16, lwv;
    template <int KS> DEVI void r0() { x0[KS] = lr128(l16 + L_XN + KS * 2048); }
    template <int KS> DEVI void r1() { x1[KS] = lr128(l16 + L_XN + KS * 2048 + 1024); }
    template <int KS> DEVI void rw() { if constexpr (KS >= 4) wl[KS] = lr128(lwv + (KS - 4) * 4096); }
    template <bool WL, int KS> DEVI void pre() { if constexpr (WL && KS >= 4) asm volatile("" :: "v"(wl[KS]), "v"(x0[KS]), "v"(x1[KS])); else asm volatile("" :: "v"(x0[KS]), "v"(x1[KS])); }
    template <int ID0, bool WL, int ks, int m> DEVI void mm(const bf16x16 (&W)[52]) {
        constexpr int b = ks >> 1, t = m % 3, id = ID0 + t * 10 + b;
        f32x4& c = m < 3 ? a0[t] : a1[t];
        const bf16x8& x = m < 3 ? x0[ks] : x1[ks];
        if constexpr (WL && t == 2 && b >= 2) { if constexpr (ID0 == 30) mfb<false, false>(c, x, wl[ks]); else mfa<false, false>(c, wl[ks], x); }
        else mfw<id % 52, ks & 1, ID0 == 30, ks == 0>(c, W[id % 52], x);
        SCHED0();
    }
    template <int ID0, bool WL, int K0, int K1, int PF, bool H6 = false, class HOOK> DEVI void run(const bf16x16 (&W)[52], HOOK hook) {
        sfor<K1 - K0>([&](auto KK) {
            constexpr int kk = CV(KK), ks = K0 + kk;
            sfor<6>([&](auto M) {
                constexpr int m = CV(M);
                mm<ID0, WL, ks, m>(W);
                if constexpr (m == 5 && ks + 1 < K1) { pre<WL, (ks + 1) % 20>(); SCHED0(); }
                if constexpr (ks + 2 < PF) {
                    if constexpr (m == 0) r0<(ks + 2) % 20>();
                    if constexpr (m == 1) r1<(ks + 2) % 20>();
                    if constexpr (m == 3 && WL) rw<(ks + 2) % 20>();
                }
                if constexpr (H6) hook(IC(kk * 6 + m));
                else { hook(IC(kk * 7 + (m < 5 ? m : 6))); if constexpr (m == 4) hook(IC(kk * 7 + 5)); }
                SCHED0();
            });
        });
    }
    template <int ID0, bool WL, int F0, int F1, int PF, class HOOK> DEVI void go(const bf16x16 (&W)[52], HOOK hook) {
        sfor<F1 - F0>([&](auto FF) {
            constexpr int f = F0 + CV(FF), ks = f / 6, m = f % 6;
            mm<ID0, WL, ks, m>(W);
            if constexpr (m == 5 && f + 1 < F1) { pre<WL, (ks + 1) % 20>(); SCHED0(); }
            if constexpr (ks + 2 < PF) rd<WL, (f + 12) % 120>();
            hook(IC(f));
            SCHED0();
        });
    }
    template <int P> DEVI void home() {
        if constexpr (P == 0) asm volatile("" : "+{v[12:15]}"(a0[0]), "+{v[16:19]}"(a0[1]), "+{v[20:23]}"(a0[2]), "+{v[24:27]}"(a1[0]), "+{v[28:31]}"(a1[1]), "+{v[32:35]}"(a1[2]));
        else asm volatile("" : "+{v[36:39]}"(a0[0]), "+{v[40:43]}"(a0[1]), "+{v[44:47]}"(a0[2]), "+{v[48:51]}"(a1[0]), "+{v[52:55]}"(a1[1]), "+{v[56:59]}"(a1[2]));
    }
    template <bool WL, int F> DEVI void rd() {
        if constexpr (F % 6 == 0) r0<F / 6>();
        if constexpr (F % 6 == 1) r1<F / 6>();
        if constexpr (F % 6 == 3 && WL) rw<F / 6>();
    }
    u32x2 o;
    template <int I, int OFF> DEVI void out4(unsigned d0) {
        if constexpr (I >= 0 && I < 24) {
            constexpr int t = I / 8, rt = (I / 4) & 1, j = I & 1;
            const f32x4& a = rt ? a1[t] : a0[t];
            if constexpr (I % 4 < 2) { o[j] = cvtpk(a[2 * j], a[2 * j + 1]); PIN(o[j]); }
            else lw32(d0 + OFF + rt * 6144 + t * 1024 + j * 4, o[j]);
        }
    }
    template <int I, int OFF> DEVI void out(unsigned d0, unsigned d1) {
        if constexpr (I >= 0 && I < 18) {
            constexpr int t = I / 6, rt = (I / 3) & 1;
            const f32x4& a = rt ? a1[t] : a0[t];
            if constexpr (I % 3 == 0) { o[0] = cvtpk(a[0], a[1]); PIN(o[0]); }
            else if constexpr (I % 3 == 1) { o[1] = cvtpk(a[2], a[3]); PIN(o[1]); }
            else if constexpr (OFF > 0) lw64(d0 + (OFF - 1) + rt * 6144 + t * 1024, o);
            else lw64((t == 1 ? d1 : d0) + OFF + rt * 6144 + (t == 2 ? 1024 : 0), o);
        }
    }
};
#define SPIX(l) (((l) & 2) ? 0xEEEEu : 0x4444u)
template <int I> DEVI void fold_op(f32x4& a, unsigned& o) {
    if constexpr (I == 0) asm volatile("v_add_f32 %0, %0, %1" : "+v"(a[0]) : "v"(a[2]));
    if constexpr (I == 1) asm volatile("v_add_f32 %0, %0, %1" : "+v"(a[1]) : "v"(a[3]));
    if constexpr (I == 2) asm volatile("v_cvt_pk_bf16_f32 %0, %1, %2" : "=v"(o) : "v"(a[0]), "v"(a[1]));
}

template <int J> DEVI void fold2(f32x4& a, f32x4& b, unsigned (&o)[2]) {
    if constexpr (J < 2) asm volatile("v_add_f32 %0, %0, %2\n\tv_add_f32 %1, %1, %3" : "+v"(a[J]), "+v"(b[J]) : "v"(a[J + 2]), "v"(b[J + 2]));
    else asm volatile("v_cvt_pk_bf16_f32 %0, %2, %3\n\tv_cvt_pk_bf16_f32 %1, %4, %5" : "=&v"(o[0]), "=&v"(o[1]) : "v"(a[0]), "v"(a[1]), "v"(b[0]), "v"(b[1]));
}
DEVI unsigned vshl1(unsigned x, unsigned b) { unsigned d; asm volatile("v_lshl_or_b32 %0, %1, 1, %2" : "=v"(d) : "s"(x), "n"(b)); return d; }

#define ANNOUNCE(s, now, d) (((unsigned)(s) << 16) | (((unsigned)(now) + (d)) & 0xFFFFu))

template <class PUT> DEVI void ln_rows(const u32x4 (&a)[5], int n, int row  , PUT put) {
    float v[40];
#pragma unroll
    for (int i = 0; i < 20; ++i) { v[2 * i] = blo(a[i >> 2][i & 3]); v[2 * i + 1] = bhi(a[i >> 2][i & 3]); }
    f32x2 s2[4] = {{0.f, 0.f}, {0.f, 0.f}, {0.f, 0.f}, {0.f, 0.f}};
#pragma unroll
    for (int i = 0; i < 40; i += 2) s2[(i >> 1) & 3] += (f32x2){v[i], v[i + 1]};
    const f32x2 st = (s2[0] + s2[1]) + (s2[2] + s2[3]);
    float m = row16sum(st[0] + st[1]) * (1.f / 640.f);
    f32x2 q2[4] = {{0.f, 0.f}, {0.f, 0.f}, {0.f, 0.f}, {0.f, 0.f}};
#pragma unroll
    for (int i = 0; i < 40; i += 2) { const f32x2 d = (f32x2){v[i], v[i + 1]} - (f32x2){m, m}; q2[(i >> 1) & 3] += d * d; }
    const f32x2 qt = (q2[0] + q2[1]) + (q2[2] + q2[3]);
    const float q = row16sum(qt[0] + qt[1]);
    const float var = bfr(q * (1.f / 640.f));
    m = bfr(m);
    const float rden = __builtin_amdgcn_rcpf(bfr(__builtin_amdgcn_sqrtf(bfr(var + 1e-5f))));
    const unsigned vo = (n >> 2) * 2048 + (row >> 4) * 1024 + ((n & 3) * 16 + (row & 15)) * 16;
#pragma unroll
    for (int c = 0; c < 5; ++c) {
        u32x4 o;
#pragma unroll
        for (int i = 0; i < 4; ++i) { unsigned d = cvtpk(v[c * 8 + 2 * i] - m, v[c * 8 + 2 * i + 1] - m); o[i] = cvtpk(blo(d) * rden, bhi(d) * rden); }
        put(vo, c, o);
    }
}

struct Soft {
    unsigned long long mA, mY, mC[8];
    unsigned v0, v1;
    unsigned sg, hs;
    float a[6], m, M, t, s, h; u32x2 pk;
    template <int I> DEVI void mask(f32x4 (&sc)[3]) {
        if constexpr (I == 0) asm volatile("v_mov_b32 %0, 0xfe800000" : "=v"(t));
        if constexpr (I >= 1 && I < 9) { float d; asm volatile("v_cndmask_b32_e64 %0, %1, 0, %2" : "=v"(d) : "v"(t), "s"(mC[I - 1])); sc[(I - 1) >> 2][(I - 1) & 3] = d; }
    }
    template <int I> DEVI void op(const Ctx& k, f32x4 (&sc)[3], unsigned a0, unsigned a1, unsigned hv) {
#define FMA(x, y) "v_fmamk_f32 " x ", " x ", 0x3dd53b95, " y "\n\t"
#define EXPC(x) "v_exp_f32_e64 " x ", " x " clamp\n\t"
#define OP3(o, d, x, y) o " " d ", " x ", " y "\n\t"
#define SA(txt) asm volatile(txt : "+v"(a[0]), "+v"(a[1]), "+v"(a[2]), "+v"(a[3]), "+v"(m), "+v"(t))
#define SB(txt) asm volatile(txt : "+v"(a[0]), "+v"(a[1]), "+v"(a[2]), "+v"(a[3]), "+v"(m), "+v"(t), "+v"(s), "+v"(a[4]), "+v"(a[5]), "+v"(M), "+v"(h) : "s"(hs), "s"(sg))
#define ADD(d, x, y) OP3("v_add_f32", d, x, y)
#define MUL(d, x, y) OP3("v_mul_f32", d, x, y)
#define CND(d, x, y, c) "v_cndmask_b32_e64 " d ", " x ", " y ", " c "\n\t"
        if constexpr (I == 0) asm volatile("v_max3_f32 %0, %1, %2, %3\n\tv_max3_f32 %0, %0, %4, %5" : "=&v"(m) : "v"(sc[0][0]), "v"(sc[0][1]), "v"(sc[0][2]), "v"(sc[0][3]), "v"(sc[1][0]));
        if constexpr (I == 1) asm volatile("v_max3_f32 %0, %0, %2, %3\n\t" CND("%1", "%4", "%5", "%6") : "+v"(m), "=&v"(a[2]) : "v"(sc[1][1]), "v"(sc[1][2]), "v"(sc[0][2]), "v"(sc[1][0]), "s"(mY));
        if constexpr (I == 2) asm volatile("v_max_f32 %0, %0, %2\n\t" CND("%1", "%3", "%4", "%5") : "+v"(m), "=&v"(a[3]) : "v"(sc[1][3]), "v"(sc[0][3]), "v"(sc[1][1]), "s"(mY));
        if constexpr (I == 3) asm volatile("v_mul_f32 %1, 0xbdd53b95, %5\n\t" CND("%0", "%2", "%3", "%4") : "=&v"(a[0]), "=&v"(t) : "v"(sc[0][0]), "v"(a[2]), "s"(mA), "v"(m));
        if constexpr (I == 4) asm volatile(CND("%0", "%2", "%3", "%5") CND("%1", "%1", "%4", "%5") : "=&v"(a[1]), "+v"(a[2]) : "v"(sc[0][1]), "v"(a[3]), "v"(sc[1][2]), "s"(mA));
        if constexpr (I == 5) asm volatile(CND("%0", "%0", "%2", "%3") FMA("%1", "%4") : "+v"(a[3]), "+v"(a[0]) : "v"(sc[1][3]), "s"(mA), "v"(t));
        if constexpr (I == 6) SA(FMA("%1", "%5") FMA("%2", "%5"));
        if constexpr (I == 7) asm volatile(FMA("%0", "%3") "v_mov_b32 %1, %4" : "+v"(a[3]), "=&v"(h), "=v"(s) : "v"(t), "s"(hv));
        if constexpr (I == 8) SA(EXPC("%0"));
        if constexpr (I == 9) SA(EXPC("%1"));
        if constexpr (I == 10) { asm volatile("v_max3_f32 %0, %2, %3, %4\n\tv_mov_b32 %1, %0" : "=&v"(M), "=&v"(t) : "v"(m), "v"(sc[2][0]), "v"(sc[2][1]), "v"(sc[2])); a[4] = sc[2][0]; a[5] = sc[2][1]; }
        if constexpr (I == 11) SB("v_permlane16_swap_b32 %9, %5");
        if constexpr (I == 12) SB(OP3("v_max_f32", "%9", "%9", "%5") "v_mov_b32 %5, %9");
        if constexpr (I == 13) SB("v_permlane32_swap_b32 %9, %5");
        if constexpr (I == 14) SB(OP3("v_max_f32", "%9", "%9", "%5") "v_mul_f32 %9, 0xbdd53b95, %9");
        if constexpr (I == 15) SB(FMA("%7", "%9") FMA("%8", "%9"));
        if constexpr (I == 16) SB(FMA("%4", "%9") ADD("%6", "%0", "%1"));
        if constexpr (I == 17) SB(EXPC("%2"));
        if constexpr (I == 18) SB(EXPC("%3"));
        if constexpr (I == 19) SB(ADD("%6", "%6", "%2") ADD("%6", "%6", "%3"));
        if constexpr (I == 20) SB(EXPC("%7"));
        if constexpr (I == 21) SB(EXPC("%8"));
        if constexpr (I == 22) SB(MUL("%6", "%12", "%6") ADD("%5", "%7", "%8"));
        if constexpr (I == 23) SB(EXPC("%4"));
        if constexpr (I == 24) SB(MUL("%6", "%6", "%4") OP3("v_fmac_f32", "%6", "%11", "%5"));
        if constexpr (I == 25) SB("v_fmac_f32_dpp %6, %6, %10 row_ror:8 row_mask:0xf bank_mask:0xf\n\tv_mov_b32 %5, %6");
        if constexpr (I == 26) SB("v_permlane16_swap_b32 %6, %5");
        if constexpr (I == 27) SB(ADD("%6", "%6", "%5") "v_mov_b32 %5, %6");
        if constexpr (I == 28) SB("v_permlane32_swap_b32 %6, %5");
        if constexpr (I == 29) SB(ADD("%6", "%6", "%5"));
        if constexpr (I == 30) SB("v_rcp_f32 %6, %6");
        if constexpr (I == 31) SB(MUL("%4", "%4", "%6") MUL("%7", "%7", "%6"));
        if constexpr (I == 32) SB(MUL("%0", "%0", "%4") MUL("%1", "%1", "%4"));
        if constexpr (I == 33) SB(MUL("%2", "%2", "%4") MUL("%3", "%3", "%4"));
        if constexpr (I == 34) { unsigned x, y; asm volatile("v_cvt_pk_bf16_f32 %0, %2, %3\n\tv_cvt_pk_bf16_f32 %1, %4, %5" : "=&v"(x), "=v"(y) : "v"(a[0]), "v"(a[1]), "v"(a[2]), "v"(a[3])); pk[0] = x; pk[1] = y; }
        if constexpr (I == 35) { st64(k.rs, v0, a0, pk); SCHED0(); asm volatile("v_mul_f32 %1, %1, %2\n\tv_cvt_pk_bf16_f32 %0, %0, %1" : "+v"(a[4]), "+v"(a[5]) : "v"(s)); }
        if constexpr (I == 36) st32(k.rs, v1, a1, __float_as_uint(a[4]));
    }
};
#define QIDX(t, b) ((t) * 10 + (b))
DEVI void qk_role(const Params& p, Ctx& k, char* smem, int h) {
    PROF_DECL
    const int w = k.w;
    bf16x16 W[52];
    Gather G;
    Soft S;
    const int hw = h >> 3, hh = h & 7;
    unsigned l16, ix, dqm, dq, trp, qp;
    {
        const int l = k.l, g = l >> 4, n = l & 15, r = n & 7, u = w & 1;
        const bool hf = n >= 8, dup = w != 1;
        S.mA = __ballot(dup && hf);
        const int lim = (w == 0 ? 64 : w == 1 ? n : 16 + 8 * u + r) - 4 * g;
#pragma unroll
        for (int i = 0; i < 8; ++i) S.mC[i] = __ballot((i & 3) + (i >> 2) * 16 <= lim);
        const bool okm = !(dup && hf);
        S.mY = __ballot((dup && hf) == (w != 0));
        S.v0 = w == 0 ? hh * 512 + ((g >> 1) * 16 + (r >> 1) * 4 + hf * 2 + (r & 1)) * 16 + (g & 1) * 8 : w == 1 ? (hh >> 1) * 1024 + l * 16 + (hh & 1) * 8 : hh * 1024 + (g * 16 + 8 * u + r) * 16 + hf * 8;
        S.v1 = !okm ? OOB : w == 0 ? 4096 + hh * 128 + r * 16 + g * 4 : (w == 1 ? 4096 + n * 16 : 6144 + (8 * u + r) * 16) + (hh >> 1) * 512 + (g >> 1) * 256 + (hh & 1) * 8 + (g & 1) * 4;
        l16 = l * 16; ix = SPIX(l);
        const int uu = u ^ ((n >> 2) & 1);
        dqm = L_QM + (w >> 1) * 3072 + uu * 512 + (g >> 1) * 128 + n * 8 + (g & 1) * 4;
        dq = L_QM + 6144 + (w >> 1) * 3072 + uu * 512 + g * 128 + ((n * 8) ^ ((g >> 1) * 64));
        trp = L_QM + 6144 + (l & 3) * 128 + (((l >> 2) * 8) ^ ((l & 2) * 32));
        qp = w == 1 ? trp + 6144 : (w == 0 ? L_QM : L_Q + 6144 + u * 256) + (l & 1) * 128 + (((l >> 2) * 8) ^ (w == 3 ? 64 : 0));
        u32x8 rg = {S.v0, S.v1, l16, ix, dqm, dq, trp, qp};
        asm volatile("" : "+{v[0:7]}"(rg));
        S.v0 = rg[0]; S.v1 = rg[1]; l16 = rg[2]; ix = rg[3]; dqm = rg[4]; dq = rg[5]; trp = rg[6]; qp = rg[7];
    }
    Xn X;
    G3 A, B;
    X.l16 = l16; A.l16 = l16; B.l16 = l16; B.lwv = vadd(l16, L_W + w * 1024);
    {
        const int l = k.l;
        bf16x8 wt[16];
        u32x2 gv;
        unsigned ra[3];
#pragma unroll
        for (int t = 0; t < 3; ++t) ra[t] = opq(wp_adr<384, true>(w * 3 + t, l) + SLOT(0));
#define QEX(v) ((v) < 0 ? 0 : ((v) < 6) + ((v) == QGS) + 10 * ((v) == QGS + 2) + 8 * ((v) == QGS + 7))
#define QKE(v) ((v) < QGS + 4 ? 0 : (v) == 19 ? 20 : CMIN(v, 3 * ((v) - QGS - 3)))
        auto rdx = [&](auto K) { constexpr int ks = CV(K); A.r0<ks>(); A.r1<ks>(); B.x0[ks] = A.x0[ks]; B.x1[ks] = A.x1[ks]; if constexpr (ks < 14 && QKE(ks + 1) <= ks) B.rw<ks>(); };
        cstream<20>([](auto V) { constexpr int v = CV(V); return IC(QEX(v - 1) + (v < 19 ? 6 : 0)); },
            [&](auto U) { constexpr int u = CV(U), b = u >> 1, k0 = QKE(u - 1), n = (QKE(u) - k0) * 12, nn = n ? n : 12;
                if constexpr (u < 6) hello(k, 0);
                if constexpr (u == QGS) gv = go_ask(k);
                if constexpr (u == QGS + 2) { PIN(gv); go_wait(k, 0, gv); X.init(k); G.init(k); X.l16 = k.c ? OOB : l16; xn_all(k, X.l16, X.src, X.dst); }
                if constexpr (u == QGS + 4) rdx(IC(0));
                if constexpr (u == QGS + 7) { sfor<3>([&](auto I) { X.next<CV(I)>(k, l16); G.op<CV(I)>(k, l16, 0); }); X.se = X.src; }
                if constexpr (u == 18) {
                    asm volatile("" :: "{v[152:155]}"(wt[10]), "{v[156:159]}"(wt[11]), "{v[232:235]}"(wt[12]));
#pragma unroll
                    for (int i = 10; i < 13; ++i) lw128(B.lwv + i * 4096, __builtin_bit_cast(u32x4, wt[i]));
                }
                if constexpr (u == 19) { G.wait<0>(); lw128(l16 + G.dst + 2048, G.v0); lw128(l16 + G.dst + 3072, G.v1); }
                XSTAMP(u)
                const unsigned rb[3] = {slot_adr<u>(ra[0]), slot_adr<u>(ra[1]), slot_adr<u>(ra[2])};
                bf16x4 hf[12];
                SCHED0();
                sfor<nn>([&](auto I) { constexpr int i = CV(I), ks = k0 + i / 12, m = i % 12, t = i >> 1, e = u == QGS + 7 ? 8 : 0, j = ((i - e) * 6) / (nn - e);
                    if constexpr (n > 0) { if constexpr (m < 6) A.mm<0, false, ks, m>(W); else B.mm<30, true, ks, m - 6>(W); }
                    if constexpr (i < e) { if constexpr (i % 4 == 0) X.op<i / 4, 0>(k); if constexpr (i & 1) X.op<i / 4, 1 + (i / 2) % 2>(k); else G.op<3 + i / 4 + (i & 2) * 2>(k, l16, 0); }
                    else if constexpr (u > 0 && u < 18 && (i == e || ((i - e - 1) * 6) / (nn - e) != j))
                        dma1<SLOT(u + 2) + (j / 3) * 12288 + (j % 3) * 1024>((j < 3 ? p.Wk : p.Wq) + (size_t)(u + 2) * 32 * 6144 + h * 192, wsrc<384, true>(w * 192 + (j % 3) * 64 + l, 12288), w * 3072);
                    if constexpr (i == 0 && u > 4 && (u < 15 || u >= 18)) lw128(B.lwv + (u - 5) * 4096, __builtin_bit_cast(u32x4, wt[(u - 5) & 15]));
                    if constexpr (i < 12) {
                        hf[i] = lrtr(rb[t % 3] + (t / 3) * 12288 + (i & 1) * 1536);
                        if constexpr (i & 1) {
                            if constexpr (t == 5 && b >= 2) B.wl[u] = wt[(u - 4) & 15] = CAT4(hf[i - 1], hf[i]);
                            else if constexpr (u & 1) W[QIDX(t, b) % 52] = CAT8(LO8(W[QIDX(t, b) % 52]), CAT4(hf[i - 1], hf[i]));
                            else W[QIDX(t, b) % 52] = __builtin_shufflevector(hf[i - 1], hf[i], 0, 1, 2, 3, 4, 5, 6, 7, -1, -1, -1, -1, -1, -1, -1, -1);
                        }
                    }
                    if constexpr (n > 0 && m == 3 && ks < 19) rdx(IC((ks + 1) % 20));
                    SCHED0(); }); });
        lw128(B.lwv + 15 * 4096, __builtin_bit_cast(u32x4, wt[15]));
        static_assert(QKE(17) == 17, "QGS");
        RAWBAR()
        XSTAMP(20)
        const unsigned d0 = (l16 | ((l16 >> 5) & 8)) ^ (L_K + (w >> 1) * 3072 + (w & 1) * 8);
        sfor<24>([&](auto I) { A.out4<CV(I), 0>(d0); });
        G.v0 = G.p0; G.v1 = G.p0; G.bad = 0;
        X.off = 0; X.l16 = l16;
        if (k.c) X.init(k);
    }
    RSTAMP(2)
    const unsigned po0 = k.p0 + PR_B + hw * 49152 + (w == 0 ? 0 : w == 1 ? 32768 : 16384), po1 = k.p0 + PR_B + hw * 49152 + (w == 0 ? 0 : 32768);
    const unsigned pdum = k.pg(1) + DUMMY_B, pbc = w == 0 ? k.p0 + CNT(C_BC, hw * 16 + hh) : pdum + EXP(32768 + h * 512 + w * 128);
    unsigned long long now = 0, done = 0, tpa = 0, tpb = 0;
    unsigned a0 = po0 + (k.c ? pdum - k.p0 - PR_B : 0u), a1 = po1 + (k.c ? pdum - k.p0 - PR_B : 0u);
    const unsigned hv = w == 1 ? 0u : 0x3F800000u, hw2 = w == 1 ? 0u : 0x00800000u;
    S.sg = 0x3F800000u; S.hs = 0x3F800000u - hw2;
    f32x4 ac[6];
#pragma unroll
    for (int t = 0; t < 6; ++t) clr2(ac[t]);
    unsigned an = 110;
    __builtin_amdgcn_s_waitcnt(0xc07f);
    do {
        if (X.off != 0) {
            PC8(1, X.sg)
            X.late<2>(k, l16);
            G.check();
        }
_Pragma("unroll 1")
        for (unsigned spins = 0; G.bad != 0 && !spin_fail(k, spins); ++spins) { PC8(0, X.sg) G.again(k, l16); }
        HSTOP(0) HSTAMP(0, 0)
        clr2(ac[5]);
        asm volatile("s_nop 1" : "+v"(ac[5]));
        smfw<50>(ac[5], __builtin_bit_cast(bf16x8, G.p0), W[50], ix);
    do {
        PROF_S(X.sg - 1)
        f32x4 sc[3];
        {
            bf16x8 m[16], wa[10], wb[10], kf[12];
            bf16x4 q[12], km[12];
            unsigned fo[6];
            m[0] = __builtin_bit_cast(bf16x8, G.p0); m[1] = __builtin_bit_cast(bf16x8, G.p1);
            auto put = [&](auto T) { constexpr int t = CV(T); if constexpr (t < 3) lw32(dq + t * 1024, fo[t]); else lw32(dqm + (t - 3) * 1024, fo[t]); };
            auto rd = [&](auto C) { constexpr int c = CV(C), j = c % 20;
                if constexpr (c < 0) { }
                else if constexpr (c < 20) m[j] = lr128(l16 + MADR(j < 10 ? j : j - 8));
                else if constexpr (c < 40) wa[j] = lr128(B.lwv + (j - 2) * 8192);
                else if constexpr (c < 60) wb[j] = lr128(B.lwv + (j - 2) * 8192 + 4096);
                else if constexpr (c < 80) kf[j] = lr128(l16 + L_K + ((j & 1) * 6 + j / 2) * 1024);
                else if constexpr (c < 100) q[j] = lrtr(qp + j * 512);
                else if constexpr (c < 120) km[j] = lrtr(trp + j * 512);
                else if constexpr (c < 140) A.r0<j>();
                else A.r1<j>(); };
#define PB(i) ((i) < 12 ? (i) / 6 : (i) < 32 ? (i) / 4 - 1 : (i) < 44 ? 7 + ((i) - 32) % 3 : (i) < 54 ? 7 + ((i) - 44) / 2 : 12 + ((i) - 54) % 3)
#define PT(i) ((i) < 12 ? ((i) + 5) % 6 : (i) < 32 ? (3 + (i) % 4) % 6 : (i) < 44 ? (3 + ((i) - 32) / 3) % 6 : (i) < 54 ? 1 + ((i) & 1) : 1 + ((i) - 54) / 3)
            sfor<82>([&](auto I) {
                constexpr int i = CV(I) + 1, b = PB(i), t = PT(i), bn = PB(i + 1);
                constexpr int rt[83] = {-1, 2, -1, -1, -1, 22, 42, 3, -1, 23, 43, 4, -1, 24, 44, 5, -1, 25, 45, 6,
                    -1, 26, 46, 7, -1, 8, 9, 27, 47, 28, 48, 29, 49, -1, -1, 60, -1, 61, 62, 63,
                    -1, 10, 64, -1, 11, 12, -1, 13, 14, -1, -1, 80, 82, 83, 84, 85, 86, 87, 67, 88,
                    89, 69, -1, 91, -1, 71, -1, 121, -1, 100, 102, 104, 106, 108, 110, -1, 140, -1, -1, 141, -1, -1, 122};
                constexpr int r2[83] = {-1, -1, -1, -1, -1, -1, -1, -1, -1, -1, -1, -1, -1, -1, -1, -1, -1, -1, -1, -1,
                    -1, -1, -1, -1, -1, -1, -1, -1, -1, -1, -1, -1, -1, -1, -1, -1, -1, -1, -1, -1,
                    -1, -1, -1, -1, -1, -1, -1, -1, -1, -1, -1, 81, -1, 65, -1, 66, -1, -1, -1, -1,
                    68, 90, -1, 70, -1, 120, -1, -1, -1, 101, 103, 105, 107, 109, 111, -1, -1, -1, -1, -1, -1, -1, -1};
                if constexpr (i < 60) {
                    if constexpr (t == 5 && b >= 2) smf<false>(ac[5], m[b], CAT8(wa[b], wb[b]), ix);
                    else smfw<QIDX(t, b < 10 ? b : b - 8) % 52>(ac[t], m[b], W[QIDX(t, b < 10 ? b : b - 8) % 52], ix);
                } else if constexpr (i < 72) { constexpr int j = i - 60; mfa<false, false>(sc[j & 1], kf[j], CAT4(q[j & ~1], q[j | 1])); }
                else if constexpr (i < 77) A.mm<0, false, (i - 72) / 3, (i - 72) % 3>(W);
                else { constexpr int ks = i - 77; mfa<false, ks == 0>(sc[2], CAT4(km[2 * ks], km[2 * ks + 1]), CAT4(q[2 * ks], q[2 * ks + 1])); }
                SCHED0();
                if constexpr (i == 1) { AQ(3, 3) asm volatile("s_barrier" ::: "memory"); AQ(1, 2) AQ(3, 4) }
                else if constexpr (i == 50) { BARL(3); AQ(1, 3) }
                else if constexpr (i == 68) { BARL(3); AQ(1, 4) QSTAMP(0, 1) }
                else if constexpr (i >= 12 && i < 59) { if constexpr (PT(i + 1) == 5) TOUCH(wb[bn % 10]); TOUCH(m[bn]); }
                else if constexpr (i >= 59 && i < 71) { asm volatile("" :: "v"(kf[(i - 59) % 12]), "v"(q[((i - 59) | 1) % 12])); }
                else if constexpr (i == 71) TOUCH(A.x0[0]);
                else if constexpr (i == 74) TOUCH(A.x0[1]);
                else if constexpr (i >= 76 && i < 82) TOUCH(km[(2 * (i - 76) + 1) % 12]);
                if constexpr (i == 8 || i == 30) asm volatile("" :: "{v[60:63]}"(ac[0]), "{v[28:31]}"(ac[1]), "{v[32:35]}"(ac[2]), "{v[64:67]}"(ac[3]), "{v[68:71]}"(ac[4]), "{v[72:75]}"(ac[5]));
                TPQ(i)
                if constexpr (i == 14) AC3(0, 0, 0)
                if constexpr (i == 16) AC3(1, 6, 1)
                if constexpr (i == 18) AC3(2, 7, 2)
                if constexpr (i == 20) AC3(-1, -1, 3)
                if constexpr (i == 22) AC3(-1, -1, 4)
                if constexpr (i == 24) AC3(-1, -1, 5)
                if constexpr (i == 66) AC3(3, -1, 6)
                if constexpr (i == 2) { now = rt_async(); HSTOP(1) }
                if constexpr (i == APOS) { PINS(now); an += (unsigned)now; PINS(an); }
                if constexpr (i == APOS + 1) an = spack(an, X.sg);
                SCHED0();
                rd(IC(rt[i])); rd(IC(r2[i]));
                if constexpr (i == APOS + 4) st32(k.rs, 0, pbc, fo[1]);
                if constexpr (i >= 40 && i < 50 && i % 3 == 1) put(IC(((i - 31) / 3) % 6));
                if constexpr (i == 62 || i == 64) put(IC(i / 2 - 30));
                SCHED0();
                if constexpr (i < 25 && i % 4 != 1) B.out<((i - 2) / 4) * 3 + (i - 2) % 4, 6145>(dq, dq);
                if constexpr (i == APOS + 3) { fo[1] = an; PIN(fo[1]); }
                if constexpr (i >= 37 && i < 49) { constexpr int tt = ((i - 28) / 3) % 6; fold_op<(i - 37) % 3>(ac[tt], fo[tt]); }
                if constexpr (i >= 59 && i < 62) fold_op<i - 59>(ac[1], fo[1]);
                if constexpr (i == 62) fold_op<0>(ac[2], fo[2]);
                if constexpr (i == 63) asm volatile("v_add_f32 %1, %1, %2\n\tv_cvt_pk_bf16_f32 %0, %3, %1" : "=v"(fo[2]), "+v"(ac[2][1]) : "v"(ac[2][3]), "v"(ac[2][0]));
                if constexpr (i == 33) S.mask<0>(sc);
                if constexpr (i >= 51 && i < 59) S.mask<i - 50>(sc);
                if constexpr (i >= 75) S.op<i - 75>(k, sc, a0, a1, hv);
                if constexpr (i == 12) { AQ(3, 5) }
                if constexpr (i == 43) { AQ(3, 6) }
                if constexpr (i == 59) { AQ(3, 7) }
                SCHED0();
            });
        }
        QSTAMP(0, 2)
        {
            unsigned d0;
            auto rest = [&](auto J) { constexpr int j = CV(J);
                if constexpr (j >= 0 && j < 12 && (j % 6 == 0 || j % 3 == 2)) X.op<3 + j / 6, (j % 6 + 1) / 3>(k); };
            sfor<43>([&](auto F) { constexpr int f = CV(F) + 5;
                constexpr int e = f < 6 ? f + 3 : f < 9 ? f - 3 : f;
                A.mm<0, false, e / 6, e % 6>(W);
                if constexpr (f % 6 == 2 || f == 5) TOUCH(A.x1[f / 6]);
                else if constexpr (f == 47) { TOUCH(B.x0[0]); AQ(1, 6) AQ(2, 1) }
                else if constexpr (f % 6 == 5) TOUCH(A.x0[f / 6 + 1]);
                if constexpr (f == 40 - 6 * SOFT2) { done = rt_async(); QSTAMP(0, 3) AQ(1, 5) }
                TPQ(f + 78)
                if constexpr (f == 21) set_m0(X.dst + 2 * 8192);
                if constexpr (f == 24) { X.sb = X.se + 2 * 16384; PINS(X.sb); }
                if constexpr (f == 42) AC3(4, -1, 7)
                SCHED0();
                if constexpr (f % 6 == 4 && f < 36) A.r0<f / 6 + 2>();
                if constexpr (f % 6 == 1 && f < 42) A.r1<f / 6 + 1>();
                if constexpr (f >= 39 && f < 43) B.rd<true, ((f - 39) / 2) * 6 + (f - 39) % 2>();
                if constexpr (f == 26 || f == 31) X.op<2, f / 15>(k);
                SCHED0();
                S.op<f + 3>(k, sc, a0, a1, hv);
                rest(IC(f - 43));
                if constexpr (f == 44) B.lwv = vadd(l16, L_W + w * 1024);
                SCHED0(); });
            B.go<30, true, 0, 48, 8>(W, [&](auto F) { constexpr int f = CV(F);
                TPQ(f + 126)
                rest(IC(f + 5));
                if constexpr (f == 9) AC3(5, -1, -1)
                if constexpr (f == 2) { a0 = (X.sg & 3) * PR_SZ; PINS(a0); }
                if constexpr (f == 4) { a1 = a0 + po1; a0 += po0; PINS(a0); PINS(a1); }
                if constexpr (f == 8) { S.sg = (X.sg & 4) ? 0xBF800000u : 0x3F800000u; PINS(S.sg); }
                if constexpr (f == 10) { S.hs = S.sg - hw2; PINS(S.hs); }
                if constexpr (f == 14 || f == 16) G.op<f / 16>(k, l16, X.sg);
                if constexpr (f == 22 || f == 28 || f == 32) X.next<(f - 22) / 5>(k, l16);
                if constexpr (f == 35) asm volatile("s_waitcnt vmcnt(%0)\n\ts_barrier" :: "n"(CPOS < 35 ? 7 : 6) : "memory");
                if constexpr (f == 20) AC3(6, 1, -1)
                if constexpr (f == 26) A.home<0>();
                if constexpr (f >= 36) A.rd<false, f + 12>();
                if constexpr (f == 38 || f == 40) X.next<(f == 38 ? 5 : 6)>(k, l16);
                if constexpr (f == CPOS - 1 || f == CPOS) X.next<f - CPOS + 4>(k, l16);
                if constexpr (f == 47) { A.pre<false, 8>(); AQ(1, 7) AQ(2, 2) } });
            A.go<0, false, 48, 120, 20>(W, [&](auto F) { constexpr int f = CV(F), i = f - XPOS;
                TPQ(f + 126)
                if constexpr (f == 56) B.home<1>();
                if constexpr (i == 0 || i == 2 || i == 6) X.next<7 + i / 2>(k, l16);
                if constexpr (i >= 3 && i < 15 && ((i - 3) % 6 == 0 || (i - 3) % 3 == 2)) X.op<(i - 3) / 6, ((i - 3) % 6 + 1) / 3>(k);
                if constexpr (f == 58) asm volatile("s_waitcnt vmcnt(1)\n\ts_barrier" ::: "memory");
                if constexpr (f == 57) AC3(7, 2, -1)
                G.ask<f - RPOS>(k, l16);
                if constexpr (f >= 108) B.rd<true, f - 60>();
                SCHED0();
                G.red<f - 110, (RPOS < XPOS + 5 ? 4 : 0)>();
                if constexpr (f == 116) asm volatile("v_bfe_u32 %0, %1, 8, 1\n\tv_lshl_or_b32 %0, %0, 3, %1" : "=&v"(d0) : "v"(l16));
                if constexpr (f == 118) asm volatile("v_xor_b32 %0, %1, %0" : "+v"(d0) : "s"(L_K + (w >> 1) * 3072 + (w & 1) * 8));
                if constexpr (f == 119) { B.pre<true, 8>(); AQ(2, 3) } });
            B.go<30, true, 48, 120, 20>(W, [&](auto F) { constexpr int f = CV(F), i = f - 48;
                constexpr int ko[26] = {0, 1, 2, 4, 3, 6, 5, 8, 7, 9, 10, 11, 12, 13, 14, 16, 15, 18, 17, 20, 19, 21, 22, 23, -1, -1};
                TPQ(f + 198)
                G.hold<f - GPOS + 5>(X.sg);
                if constexpr (i < 24) { SCHED0(); A.out4<ko[i % 26], 0>(d0); }
                G.at<f - GPOS>(k, l16);
                if constexpr (f == 114) { lgkm0(done); PINS(G.rt); }
                if constexpr (f == 115) { PINS(now); HTRACE(X.sg - 2) an = (unsigned)done - (unsigned)now; PINS(an); }
                if constexpr (f == 116) { an = min(an, 200u); PINS(an); }
                if constexpr (f == 117) { an += DQ; PINS(an); }
                SCHED0();
                if constexpr (i == 28) AC3(-1, 3, -1)
                if constexpr (i == 50) AC3(-1, 4, -1)
                if constexpr (i == 62) AC3(-1, 5, -1)
                if constexpr (i == 23) { AQ(2, 4) }
                if constexpr (i == 47) { AQ(2, 5) }
                if constexpr (i == 65) { AQ(2, 6) }
                if constexpr (f == 119) { AQ(2, 7) }
                if constexpr (f == 112) asm volatile("" :: "{v[12:15]}"(G.v0), "{v[16:19]}"(G.v1), "{v[20:23]}"(G.p0), "{v[24:27]}"(G.p1), "{v9}"(B.lwv) TRHINT);
                G.early<f - EPOS>(X.off);
                if constexpr (f >= 113 && f < 119) clr2(ac[(f + 6 - 113 + 5) % 6]); });
            static_assert(GPOS >= 70 && GPOS <= 98 && XPOS % 6 == 2 && RPOS > XPOS && (RPOS + 1 < XPOS + 5 || RPOS >= XPOS + 14) && XPOS > 58 && CPOS > 8 && CPOS != 35, "GPOS");
            QSTAMP(0, 4) QSTAMP(0, 5)
            HFLUSH(s)
        }
        AST(0)
        PROF_ONLY(G.wait<0>();)
        HSTOP(0) HSTAMP(s, 0) AQ(1, 1) AQ(3, 1)
        G.rest();
        SCHED0();
        smfw<50>(ac[5], __builtin_bit_cast(bf16x8, G.p0), W[50], ix);
        AQ(3, 2)
        SCHED0();
    } while (G.bad == 0);
    } while (X.sg != 65u);
    PROF_OUT
}

#define OIDX(t, b) (22 + (t) * 3 + (b))
DEVI void vo_role(const Params& p, Ctx& k, char* smem, int h) {
    PROF_DECL
    const int w = k.w;
    bf16x16 W[52];
    unsigned l16, lwv, ix, dq0, trp, vo;
    {
        const int l = k.l, g = l >> 4, n = l & 15, q = (l >> 2) & 3, pp = l & 3;
        l16 = l * 16; lwv = l16 + L_W + w * 1024; ix = SPIX(l);
        dq0 = L_QM + (24 * w + (n >> 2) * 2 + (n & 1)) * 32 + g * 8 + ((n >> 1) & 1) * 4;
        trp = L_QM + (16 * (g >> 1) + 4 * (g & 1) + 8 * (q >> 1) + (q & 1)) * 32 + pp * 8;
        vo = EXP(h * 512) + (g >> 1) * 256 + n * 16 + (g & 1) * 8;
        u32x8 r = {dq0, trp, vo, ix, l16, lwv, 0u, 0u};
        PIN(r);
        dq0 = r[0]; trp = r[1]; vo = r[2]; ix = r[3]; l16 = r[4]; lwv = r[5];
    }
    Gather G;
    G3 V;
    Xn X;
    V.l16 = l16; V.lwv = lwv; X.l16 = l16;
    f32x4 y[20];
    bf16x8 v[12];
    u32x4 o[10];
    unsigned yb;
    {
        const int l = k.l;
        bf16x8 wt[16];
        bf16x4 q[10][4];
        f32x4 z[9][2];
        u32x2 gv;
        unsigned ra[6];
        const u16* wo = p.Wo + (size_t)h * 192 * 640;
        constexpr int L_V = L_XN + 24576;
#define VEX(v) ((v) < 0 ? 0 : ((v) < 6) + ((v) == VGS) + 10 * ((v) == VGS + 2) + 6 * ((v) == VGS + 6 || (v) == 18))
#define VKE(v) ((v) < VGS + 4 ? 0 : CMIN(20, 7 * ((v) - VGS - 3)))
#define YKE(v) ((v) < 16 ? 0 : 3 * (v) - 45)
        auto put = [&](int t) { st128(k.rs, X.l16, yb + t * 65536, (u32x4){cvtpk(z[t][0][0], z[t][0][1]), cvtpk(z[t][0][2], z[t][0][3]), cvtpk(z[t][1][0], z[t][1][1]), cvtpk(z[t][1][2], z[t][1][3])}); };
        auto rdx = [&](auto K) { constexpr int ks = CV(K); if constexpr (ks < 20) { V.r0<ks % 20>(); V.r1<ks % 20>(); if constexpr (ks < 14) V.rw<ks>(); } };
        auto rdv = [&](auto J) { constexpr int j = CV(J) % 6; v[j] = lr128(l16 + L_V + j * 1024); v[6 + j] = lr128(l16 + L_V + (6 + j) * 1024); };
        cstream<20>([](auto V) { constexpr int v = CV(V); return IC(VEX(v - 1) + (v < 19 ? 6 : 0)); },
            [&](auto U) { constexpr int u = CV(U), k0 = VKE(u - 1), y0 = YKE(u - 1), n = u < 16 ? (VKE(u) - k0) * 6 : u < 18 ? 36 : 18, nn = n ? n : 12, nr = u < 10 ? 3 : u < 16 ? 6 : 4;
                if constexpr (u < 6) hello(k, 0);
                if constexpr (u == VGS) gv = go_ask(k);
                if constexpr (u == VGS + 2) { PIN(gv); go_wait(k, 0, gv); X.init(k); X.l16 = k.c ? OOB : l16; xn_all(k, X.l16, X.src, X.dst); }
                if constexpr (u == VGS + 4) { rdx(IC(0)); rdx(IC(1)); }
                if constexpr (u == VGS + 6) { sfor<3>([&](auto I) { X.next<CV(I)>(k, l16); }); X.se = X.src; yb = k.yx(0, w) + EXP(h * 1024); }
                if constexpr (u == 16) rdv(IC(0));
                XSTAMP(u)
                if constexpr (u == 0 || u == 10 || u == 16) {
#pragma unroll
                    for (int t = 0; t < nr; ++t) ra[t] = opq((u == 0 ? wp_adr<384, true>(w * 3 + t, l) : u == 10 ? wp_adr<768, false>(w * 6 + t, l) : wp_adr<512, false>(w * 4 + t, l)) + SLOT(0));
                }
                unsigned rb[6];
#pragma unroll
                for (int t = 0; t < nr; ++t) rb[t] = slot_adr<u>(ra[t]);
                SCHED0();
                sfor<nn>([&](auto I) { constexpr int i = CV(I), e = u == VGS + 6 ? 9 : 0, j = ((i - e) * 6) / (nn - e), d = u + 2, r0 = u == 18 ? 6 : 0;
                    if constexpr (u < 16 && n > 0) { V.mm<0, true, k0 + i / 6, i % 6>(W); if constexpr (i % 6 == 3) rdx(IC(k0 + i / 6 + 2)); }
                    if constexpr (u >= 16) { constexpr int m = n / 3, ks = (y0 + i / m) % 6, t = (u / 18) * 6 + (i % m) / 2, rt = i & 1;
                        if constexpr (!(ks == 5 && t == 7 && rt)) { mfw<OIDX(t, ks >> 1), ks & 1, true, ks == 0>(z[t][rt], W[OIDX(t, ks >> 1)], v[rt * 6 + ks]); SCHED0(); }
                        if constexpr (i % m == 1 && u < 19) rdv(IC(ks + 1));
                        if constexpr (u == 19 && i < 2) rdv(IC(4 + i));
                        if constexpr (u == 18 && i >= 3 && i < 15 && (i & 1)) put((i - 3) / 2);
                        if constexpr (u == 18 && i < 6) lw128(lwv + (10 + i) * 4096, __builtin_bit_cast(u32x4, wt[10 + i])); }
                    if constexpr (i < e) X.op<i / 3, i % 3>(k);
                    else if constexpr (u > 0 && u < 18 && (i == e || ((i - e - 1) * 6) / (nn - e) != j)) {
                        if constexpr (d < 10) dma1<SLOT(d) + (j / 3) * 12288 + (j % 3) * 1024>(p.Wv + (size_t)(2 * d + j / 3) * 32 * 6144 + h * 192, wsrc<384, true>(w * 192 + (j % 3) * 64 + l, 12288), w * 3072);
                        else if constexpr (d < 16) dma1<SLOT(d) + j * 1024>(wo + (d - 10) * 32 * 640, wsrc<768, false>(w * 384 + j * 64 + l, 1280), w * 6144);
                        else dma1<SLOT(d) + j * 1024>(wo + (d - 16) * 48 * 640 + 384, wsrc<512, false>(w * 384 + j * 64 + l, 1280), w * 6144);
                    }
                    if constexpr (i < 2 && u >= 3 && u < 8) lw128(lwv + (2 * u - 6 + i) * 4096, __builtin_bit_cast(u32x4, wt[(2 * u - 6 + i) & 15]));
                    sfor<2>([&](auto C) { constexpr int c = u == 19 ? 2 * i + CV(C) : i - r0 + CV(C) * 99;
                        if constexpr (c >= 0 && c < 12) {
                            constexpr int t = u < 10 ? c >> 2 : u < 16 ? c >> 1 : 6 + c % 4, r = u < 10 ? c & 3 : u < 16 ? (u - 10) * 2 + (c & 1) : (u - 16) * 3 + c / 4, id = u < 10 ? (t * 10 + u) % 52 : OIDX(t, r >> 2);
                            q[t][r & 3] = lrtr(u < 10 ? rb[t] + (r >> 1) * 12288 + (r & 1) * 1536 : u < 16 ? rb[t] + (c & 1) * 12288 : rb[(t - 6) & 3] + (c / 4) * 8192);
                            if constexpr (u < 10 && u >= 2 && t == 2) { if constexpr (r & 1) V.wl[2 * u + r / 2] = wt[(2 * u - 4 + r / 2) & 15] = CAT4(q[t][r - 1], q[t][r]); }
                            else if constexpr ((r & 3) == 1) W[id] = __builtin_shufflevector(q[t][0], q[t][1], 0, 1, 2, 3, 4, 5, 6, 7, -1, -1, -1, -1, -1, -1, -1, -1);
                            else if constexpr ((r & 3) == 3) W[id] = CAT8(LO8(W[id]), CAT4(q[t][2], q[t][3]));
                        } });
                    SCHED0(); });
                if constexpr (u == VGS + 7) sfor<18>([&](auto I) { V.out<CV(I), 0>(l16 + L_V + ((3 * w) >> 1) * 1024 + ((3 * w) & 1) * 8, l16 + L_V + ((3 * w + 1) >> 1) * 1024 + ((3 * w + 1) & 1) * 8); }); });
        static_assert(VGS >= 6 && VGS < 9, "VGS");
        RAWBAR()
        XSTAMP(20)
        sfor<2>([&](auto B) { X.op<CV(B) + 3, 0>(k); asm volatile("s_nop 0"); X.op<CV(B) + 3, 1>(k); X.op<CV(B) + 3, 2>(k); });
        asm volatile("" : "+v"(z[8][0]), "+v"(z[8][1]), "+v"(z[7][0]), "+v"(z[6][1]));
        put(6); put(8);
        lw32(l16 + L_MEM, ~0u);
        o[7] = (u32x4){cvtpk(z[7][0][0], z[7][0][1]), cvtpk(z[7][0][2], z[7][0][3]), 0u, 0u};
        y[17] = z[7][1];
        yb += 7 * 65536;
        X.off = 0; X.l16 = l16;
        if (k.c) X.init(k);
    }
    RSTAMP(2)
    const unsigned pdum = k.pg(1) + DUMMY_B, pbc = w == 0 ? k.p0 + CNT(C_BC, (h >> 3) * 16 + 8 + (h & 7)) : pdum + EXP(49152 + h * 512 + w * 128), pby = k.p0 + YLINE(w, h);
    unsigned long long now = 0, done = 0, tpa = 0, tpb = 0;
    G.mx = ~0u;
    G.init(k);
    G.base -= 32768;
    const unsigned ypar = (unsigned)(k.par >> (PG_YX + (w >> 1))), yo = ((unsigned)(PG_YX + (w >> 1)) << 21) + (w & 1) * 1048576 + EXP(h * 1024);
    const unsigned ym0 = k.ym(0) + w * 163840, ym2 = k.ym(2) + w * 163840;
    unsigned ym = k.c ? pdum + w * 163840 : ym0, ys, t, c = ~0u, an = 80;
#define YT(u) ((u) < 10 ? 2 * ((u) % 5) : ((u) - 10) | 1)
#define YR(u) ((u) < 10 ? (u) / 5 : (u) & 1)
    auto yout = [&](auto U, auto J) { constexpr int u = CV(U), j = CV(J), t = YT(u), rt = YR(u);
        if constexpr (j == 0) { o[t][2 * rt] = cvtpk(y[u][0], y[u][1]); o[t][2 * rt + 1] = cvtpk(y[u][2], y[u][3]); TOUCH(o[t][2 * rt]); TOUCH(o[t][2 * rt + 1]); }
        if constexpr (j == 1 && rt) __builtin_amdgcn_raw_buffer_store_b128(o[t], k.rs, (int)l16, (int)yb, AUX_SC1);
        if constexpr (j == 2 && rt) { yb += t == 8 ? -7 * 65536 : 131072; PINS(yb); TOUCH(o[t]); } };
    auto ymm = [&](auto I) {
        constexpr int i = CV(I), ks = i % 6, u = i / 6, id = OIDX(YT(u), ks >> 1);
        mfw<id, ks & 1, true, ks == 0>(y[u], W[id], v[YR(u) * 6 + ks]);
        SCHED0();
        if constexpr (u > 0 && ks >= 2) yout(IC((u + 19) % 20), IC(ks - 2)); };
    if (k.c) X.late<5>(k, l16);
    do {
        PROF_S(X.sg - 1)
        f32x4 ac[3];
        sfor<13>([&](auto I) { constexpr int i = CV(I) + 107;
            ymm(IC(i));
            TPV(i - 107)
            if constexpr (i == TB) { asm volatile("s_waitcnt vmcnt(%0)\n\ts_barrier" :: "n"(TB < 111 ? 7 : 8) : "memory"); AST(5) }
            if constexpr (i == TB + 1) st32(k.rs, 0, pby, G.mx);
            if constexpr (i == 119) V.pre<true, 0>();
            if constexpr (i == 113) G.ask<0>(k, l16);
            if constexpr (i == 119) G.ask<1>(k, l16);
            if constexpr (i == 114) V.r0<0>();
            if constexpr (i == 115) V.r1<0>();
            if constexpr (i == 117) V.r0<1>();
            if constexpr (i == 118) V.r1<1>();
            SCHED0(); });
        HSTAMP(s, 4)
        V.run<0, true, 0, 16, 17, true>(W, [&](auto I) { constexpr int i = CV(I);
            TPV(i + 13)
            if constexpr (i >= 2) yout(IC(19), IC(i - 2));
            if constexpr (i == 8) { t = (X.sg & 7) * 2; PINS(t); }
            if constexpr (i == 10) { yb = (ypar >> t) & 1; PINS(yb); }
            if constexpr (i == 14) { t = (t << 21) + yo; PINS(t); }
            if constexpr (i == 16) { yb = (yb << 13) + t; PINS(yb); }
            if constexpr (i == 20) { c = X.sg; PINS(c); }
            if constexpr (i == 22) { G.a = max(c, 1u) << 15; PINS(G.a); }
            if constexpr (i == 28) G.op<1>(k, l16, 0);
            if constexpr (i == 40) { an = min(an, 200u) + DV; PINS(an); }
            if constexpr (i == 32 || i == 34 || i == 38) X.next<(i - 31) / 3>(k, l16);
            if constexpr (i == 23 || i == 29) { AAC(4 + i / 29) }
            if constexpr (i == 83 || i == 89) { AAC(6 + i / 89) }
            if constexpr (i == YPOS) { AST(6) }
            if constexpr (i == YPOS + 1) { AST(7) }
            if constexpr (i == YPOS) DRAIN();
            if constexpr (i == YB) asm volatile("s_barrier" ::: "memory");
            if constexpr (i == YPOS - 2) G.mx = vshl1(c, 0);
            if constexpr (i == YPOS + 1) st32(k.rs, 0, pby, G.mx);
            SCHED0();
            G.red<i - YPOS - 2>();
            G.hold<i - GPOSY * 6 + 5>(c);
            G.at<i - GPOSY * 6>(k, l16);
            if constexpr (i == 90) PINS(G.rt);
            if constexpr (i == 95) V.pre<true, 16>();
            if constexpr (i >= 92 && i < 95) clr2(ac[(i - 92) % 3]); });
        static_assert(GPOSY * 6 + 2 > YPOS + 1 && GPOSY <= 13 && YB >= YPOS && YB < 60, "GPOSY");
        HSTAMP(s, 5)
        HFLUSH(s - 1)
        f32x4 acc[10];
        f32x2 z[20];
        auto clr = [&](auto I) { constexpr int i = CV(I); asm volatile("v_mov_b64 %0, 0" : "=v"(z[i])); if constexpr (i & 1) acc[i >> 1] = __builtin_shufflevector(z[i - 1], z[i], 0, 1, 2, 3); };
        bf16x4 al[3], ah[3];
        unsigned d0, d1;
        {
            bf16x8 m[10], wa[10], wb[10];
            auto rd = [&](auto R) { constexpr int r = CV(R), b = 2 + r / 3;
                if constexpr (b < 10) {
                    if constexpr (r % 3 == 0) m[b] = lr128(l16 + MADR(b));
                    if constexpr (r % 3 == 1) wa[b] = lr128(lwv + (b - 2) * 8192);
                    if constexpr (r % 3 == 2) wb[b] = lr128(lwv + (b - 2) * 8192 + 4096);
                } else if constexpr (b < 13) {
                    if constexpr (r % 3 == 0) V.r0<b + 7>();
                    if constexpr (r % 3 == 1) V.rw<b + 7>();
                    if constexpr (r % 3 == 2) V.r1<b + 7>();
                } };
            AST(0)
            G.wait<0>();
            HSTOP(0) HSTAMP(s, 0) AST(1)
            V.run<0, true, 16, 17, 0, true>(W, [&](auto I) { constexpr int i = CV(I);
                TPV(i + 109)
                G.fin<i>(0);
                if constexpr (i == 3) {
                    if (__builtin_expect(G.bad != 0, 0)) {
                        unsigned spins = 0;
_Pragma("unroll 1")
                        do { PC8(0, c) G.again(k, l16); } while (G.bad != 0 && !spin_fail(k, spins++));
                        HSTOP(0) HSTAMP(s, 0)
                    }
                    asm volatile("s_barrier" ::: "memory");
                    AST(2)
                }
                if constexpr (i == 4) { now = rt_async(); HSTOP(1) SCHED0(); }
                if constexpr (i >= 3) rd(IC(i - 3)); });
            m[0] = __builtin_bit_cast(bf16x8, G.p0); m[1] = __builtin_bit_cast(bf16x8, G.p1);
            sfor<30>([&](auto I) {
                constexpr int i = CV(I), b = i / 3, t = i % 3;
                if constexpr (t == 2 && b >= 2) smf<false>(ac[2], m[b], CAT8(wa[b], wb[b]), ix); else smfw<t * 10 + b>(ac[t], m[b], W[t * 10 + b], ix);
                SCHED0();
                TPV(i + 115)
                if constexpr (i >= 6 && i < 29 && t != 0) { if constexpr (t == 1) TOUCH(wb[b]); else TOUCH(m[(b + 1) % 10]); }
                if constexpr (i == 29) V.pre<true, 17>();
                if constexpr (i == 2) { PINS(now); an += (unsigned)now; PINS(an); }
                if constexpr (i == 3) an = spack(an, c);
                SCHED0();
                if constexpr (i < 27) rd(IC(3 + i));
                if constexpr (i == 5) st32(k.rs, 0, pbc, d0);
                SCHED0();
                if constexpr (i == 4) { d0 = an; PIN(d0); }
                if constexpr (i == 28) d0 = vadd(l16, L_K + ((3 * w) >> 1) * 1024 + ((3 * w) & 1) * 8);
                if constexpr (i == 29) d1 = vadd(l16, L_K + ((3 * w + 1) >> 1) * 1024 + ((3 * w + 1) & 1) * 8);
                SCHED0();
            });
            unsigned fo[3];
            V.run<0, true, 17, 19, 0, true>(W, [&](auto I) { constexpr int i = CV(I);
                TPV(i + 145)
                if constexpr (i < 3) { rd(IC(30 + i)); SCHED0(); fold_op<0>(ac[i], fo[i]); }
                if constexpr (i == 3 || i == 4 || i == 6) { constexpr int t = i / 2 - 1; asm volatile("v_add_f32 %1, %1, %2\n\tv_cvt_pk_bf16_f32 %0, %3, %1" : "=v"(fo[t]), "+v"(ac[t][1]) : "v"(ac[t][3]), "v"(ac[t][0])); }
                if constexpr (i == 5 || i == 7 || i == 8) lw32(dq0 + ((i - 4) / 2) * 256, fo[(i - 4) / 2]);
                if constexpr (i == 9 || i == 10) clr2(acc[i - 9]);
                if constexpr (i == 11) BARL(0) });
        }
        HSTAMP(s, 1) AST(3)
        {
            unsigned fo[2];
            auto xq = [&](auto I) { constexpr int i = CV(I) - XP + XS0;
                if constexpr (i == XA) X.next<3>(k, l16);
                if constexpr (i == -1 || i == 0) X.next<14 + i>(k, l16); };
            static_assert(XP % 6 == 5 && XP <= 53 && XP >= 17 && XB > XP + 18 && XB > 63, "XP");
            auto fin2 = [&](auto P, auto J) { fold2<CV(J)>(acc[2 * CV(P)], acc[2 * CV(P) + 1], fo); };
            V.run<0, true, 19, 20, 0, true>(W, [&](auto I) { constexpr int i = CV(I);
                TPV(i + 157)
                if constexpr (i == 0) { al[0] = lrtr(trp); ah[0] = lrtr(trp + 64); }
                if constexpr (i == 1) al[1] = lrtr(trp + 1024);
                if constexpr (i == 2) ah[1] = lrtr(trp + 1088);
                if constexpr (i == 3) al[2] = lrtr(trp + 2048);
                if constexpr (i == 4) ah[2] = lrtr(trp + 2112);
                SCHED0();
                if constexpr (i >= 1) clr(IC(3 + i)); });
            sfor<30>([&](auto I) { constexpr int i = CV(I), b = (i % 6) >> 1, t = 2 * (i / 6) + (i & 1), id = OIDX(t, b);
                smfw<id>(acc[t], CAT4(al[b], ah[b]), W[id], ix);
                SCHED0();
                TPV(i + 163)
                if constexpr (i == 1 || i == 3) TOUCH(ah[(i + 1) / 2]);
                if constexpr (i == 29) TOUCH(v[0]);
                if constexpr (i == 21) BARL(0)
                SCHED0();
                if constexpr (i == 4 || i == 7) X.next<(i == 4 ? 5 : 11)>(k, l16);
                xq(IC(i - 30));
                if constexpr (i >= 21 && i < 27) v[i - 21] = lr128(l16 + L_K + (i - 21) * 1024);
                SCHED0();
                V.out<i - 1, 0>(d0, d1);
                SCHED0();
                if constexpr (i % 3 == 0 && i < 18) clr(IC(9 + i / 3));
                if constexpr (i == 16) clr(IC(15));
                if constexpr (i >= 18 && i < 21) clr(IC(i - 2));
                if constexpr (i == 22) clr(IC(19));
                if constexpr (i >= 23 && i < 29) fold_op<(i - 23) / 2>(acc[(i - 23) & 1], fo[(i - 23) & 1]);
                if constexpr (i == 29) { st64(k.rs, vo, ym, (u32x2){fo[0], fo[1]}); SCHED0(); fin2(IC(1), IC(0)); }
                SCHED0(); });
            HSTAMP(s, 2)
            sfor<107>([&](auto I) { constexpr int i = CV(I), q = i < 8 ? i + 4 : i + 3;
                ymm(I);
                TPV(i + 193)
                if constexpr (i < 5 || (i >= 29 && i < 35)) TOUCH(v[(i < 5 ? i + 1 : i - 23) % 12]);
                if constexpr (i < 13 && i != 8) {
                    if constexpr (q % 3 == 1) { ym += 32768; PINS(ym); }
                    if constexpr (q % 3 == 2 && i >= 5) __builtin_amdgcn_s_setprio(0);
                    SCHED0();
                    if constexpr (q % 3 == 0) { st64(k.rs, vo, ym, (u32x2){fo[0], fo[1]}); SCHED0(); }
                    if constexpr (q < 15) fin2(IC(q / 3), IC(q % 3));
                }
                if constexpr (i == 13) done = rt_async();
                if constexpr (i >= 15 && i < 23 && i != 18 && i != 20) v[6 + i - 15 - (i > 18) - (i > 20)] = lr128(l16 + L_K + (6 + i - 15 - (i > 18) - (i > 20)) * 1024);
                xq(IC(i));
                if constexpr (i == XP - 6) X.next<15>(k, l16);
                if constexpr (i == XP - 1) { SCHED0(); X.next<7>(k, l16); }
                if constexpr (i == XP) X.next<12>(k, l16);
                if constexpr (i > XP && i <= XP + 18 && (i % 6 == 0 || i % 3 == 1)) X.op<(i - XP - 1) / 6, (i % 6 + 2) / 3>(k);
                { constexpr int xp[6] = {XB, XB + 1, XB + 6, XB + 11, XB + 12, XB + 18};
                  sfor<6>([&](auto Q) { if constexpr (xp[CV(Q)] == i) X.op<3 + CV(Q) / 3, CV(Q) % 3>(k); }); }
                if constexpr (i == 90) { ym = (c & 2) ? ym2 : ym0; PINS(ym); }
                if constexpr (i == 91) { ys = ym + EXP(327680); PINS(ys); }
                if constexpr (i == 96) { ym = (c & 1) ? ys : ym; PINS(ym); }
                if constexpr (i == 60) { HSTAMP(s, 3) }
                if constexpr (i == 42 || i == 43 || i == 48 || i == 49) { AAC((i - 42) / 3 + (i & 1)) }
                if constexpr (i == 102) lgkm0(done);
                if constexpr (i == 103) { PINS(now); HTRACE(c - 1) an = (unsigned)done - (unsigned)now; PINS(an); }
                if constexpr (i == 105) G.mx = vshl1(c, 1);
                SCHED0(); });
            AST(4)
        }
    } while (X.sg != 65u);
    HFLUSH(63)
    PROF_OUT
}

DEVI void row_role(const Params& p, const Ctx& k, char* smem, int rt) {
    PROF_DECL
    const int l = k.l, n = l & 15;
    const unsigned fo = k.w * 2048 + rt * 1024;
    RSTAMP(2)
    const __amdgpu_buffer_rsrc_t rs = __builtin_amdgcn_make_buffer_rsrc(k.c ? (void*)k.ws : (void*)p.x, 0, 0x7fffffff, 0x00020000);
    const unsigned vs = k.c ? l * 16 : n * 1280 + (l >> 4) * 16, cs = k.c ? 16384 : 256;
    unsigned ep = n * 32, dp = (l >> 4) == (n >> 2) ? n * 32 + k.w * 8 : 1024 + l * 8;
    const unsigned long long m1 = __ballot(n & 1), m2 = __ballot(n & 2);
    bf16x8 one8 = {0x3F80, 0x3F80, 0x3F80, 0x3F80, 0x3F80, 0x3F80, 0x3F80, 0x3F80};
    PIN(one8);
    unsigned fails = k.c ? 0u : 65536u;
    prog_init(k);
    for (int sg = k.c ? 0 : 8; sg < 64; ++sg) {
        unsigned src[5], dst = k.seg(PG_XNA, sg) + fo;
#pragma unroll
        for (int c = 0; c < 5; ++c) { src[c] = (k.c ? k.seg(PG_X2, sg) + fo : (sg * 32 + rt * 16) * 1280 + k.w * 64) + c * cs; PINS(src[c]); }
        PINS(dst);
        prog_wait<5>(k, k.p0 + CNT(C_X2, rt * 5), k.c ? sg : -1);
        STOP(sg) STAMP(sg, 0)
        u32x4 a[5];
        f32x4 sm, sq;
        f32x2 v[20];
        unsigned long long bad;
        do {
            asm volatile("" ::: "memory");
#pragma unroll
            for (int c = 0; c < 5; ++c) { a[c] = ld128(rs, vs, src[c]); SCHED0(); }
            unsigned mx = 0, lim = ~1u | (fails >> 16);
            ++fails; PINS(fails); PINS(lim);
            sfor<5>([&](auto C) { constexpr int c = CV(C);
                mfa<false, c == 0>(sq, __builtin_bit_cast(bf16x8, a[c]), __builtin_bit_cast(bf16x8, a[c])); mfa<false, c == 0>(sm, one8, __builtin_bit_cast(bf16x8, a[c]));
                mx = umax3(umax3(mx, a[c][0], a[c][1]), a[c][2], a[c][3]); PIN(mx);
                SCHED0(); });
            bad = __ballot(mx > lim);
        } while (__builtin_expect(bad != 0, 0));
        STAMP(sg, 1)
        auto unp = [&](int i0, int i1) {
#pragma unroll
            for (int i = i0; i < i1; ++i) { v[i] = (f32x2){blo(a[i >> 2][i & 3]), bhi(a[i >> 2][i & 3])}; PIN(v[i]); }
            SCHED0(); };
        unp(0, 3);
        asm volatile("" : "+v"(sq), "+v"(sm));
        lw64(dp, (u32x2){__float_as_uint(sm[0]), __float_as_uint(csel(csel(sq[0], sq[1], m1), csel(sq[2], sq[3], m1), m2))});
        SCHED0();
        unp(3, 10);
        RAWBAR()
        const f32x4 e0 = __builtin_bit_cast(f32x4, lr128(ep)), e1 = __builtin_bit_cast(f32x4, lr128(ep + 16));
        SCHED0();
        unp(10, 20);
        const f32x4 e = e0 + e1;
        float m = (e[0] + e[2]) * (1.f / 640.f);
        const float var = bfr(__builtin_fmaxf(__builtin_fmaf(-m, m, (e[1] + e[3]) * (1.f / 640.f)), 0.f));
        m = bfr(m);
        const float rden = __builtin_amdgcn_rcpf(bfr(__builtin_amdgcn_sqrtf(bfr(var + 1e-5f))));
#pragma unroll
        for (int c = 0; c < 5; ++c) {
            u32x4 o;
#pragma unroll
            for (int i = 0; i < 4; ++i) { const f32x2 x = v[c * 4 + i] - m; o[i] = cvtpk(bfr(x[0]) * rden, bfr(x[1]) * rden); }
            st128(k.rs, l * 16, dst + c * 16384, o);
        }
        STAMP(sg, 2)
        unsigned fv = sg - (k.c ? 0 : 8);
        PIN(fv);
        DRAIN();
        st32(k.rs, l == 0 ? 0u : OOB, k.p0 + CNT(C_XN, rt * 4 + k.w), fv);
        dp ^= 2048; ep ^= 2048;
        STAMP(sg, 3) STAMP(sg, 4) STAMP(sg, 5)
        PFLUSH(sg)
    }
    PROF_OUT
}

DEVI int red_col(int j) { const int w = j / 5, q = j % 5; return q < 3 ? 3 * w + q : 9 + 2 * w + q; }
struct Red {
    u32x4 Y[16];
    template <int I0, int I1> DEVI void yload(const Ctx& k, int j, int sg) {
        const unsigned a = k.yx(sg, j / 5) + (j % 5) * 131072 + k.w * 16384;
#pragma unroll
        for (int i = I0; i < I1; ++i) Y[i] = ld128(k.rs, k.l * 16 + (i & 3) * 1024, a + ((i >> 2) & 1) * 4096 + (i >> 3) * 65536);
    }
};
DEVI unsigned y_get(const Ctx& k) { return ld32(k.rs, (k.l & 7) * 128, k.p0 + YLINE((k.role >> 5) & 3, k.w * 8)); }
DEVI void y_wait(const Ctx& k, int s) {
_Pragma("unroll 1")
    for (unsigned spins = 0; !__all((int)y_get(k) >= s); ++spins) {
        asm volatile("" ::: "memory");
        __builtin_amdgcn_s_sleep(YS);
        if (spin_fail(k, spins)) break;
    }
    __builtin_amdgcn_fence(__ATOMIC_ACQUIRE, "wavefront");
}
DEVI void wait_until(unsigned t) { while ((int)((rclk() - t) << 16) < 0) { if (WUS) __builtin_amdgcn_s_sleep(WUS); } }
#define DPPMAX(x, c) x = max(x, __builtin_amdgcn_update_dpp(0, x, c, 0xf, 0xf, false))
#define TSLP 16
#define TWU 0
DEVI void wait_until2(unsigned t) { while ((int)((rclk() - t) << 16) < 0) { if (TWU) __builtin_amdgcn_s_sleep(TWU); } }
#define WAIT_BC(s, BSLP) { \
    unsigned bc; \
    _Pragma("unroll 1") for (;; ++spins) { \
        bc = ld32(k.rs, (l & 15) * 128, k.p0 + CNT(C_BC, w * 16)); \
        DRAIN(); \
        if (__all(((int)bc >> 16) > s)) break; \
        __builtin_amdgcn_s_sleep(BSLP); \
        if (spin_fail(k, spins)) break; \
    } \
    const unsigned now = rclk(); \
    int d = (int)((bc - now) << 16); \
    d = ((int)bc >> 16) > s + 1 || d > (256 << 16) ? -(64 << 16) : d; \
    DPPMAX(d, 0xB1); DPPMAX(d, 0x4E); DPPMAX(d, 0x141); \
    tq = now + (__builtin_amdgcn_readlane(d, 0) >> 16); tv = now + (__builtin_amdgcn_readlane(d, 8) >> 16); }
DEVI void mred_role(const Params& p, const Ctx& k, char* smem, int j) {
    PROF_DECL
    const int l = k.l, w = k.w, g = l >> 4, n = l & 15;
    const unsigned ix = SPIX(l);
    const unsigned vp = (g * 16 + ((n >> 1) & 1) * 8 + (n >> 2) * 2 + (n & 1)) * 16;
    const unsigned v16 = (g >> 1) * 1024 + (g & 1) * 256 + n * 16, yo = j * 32768 + EXP(w * 4096);
    const u32x4 ones = {0xFFFFFFFFu, 0xFFFFFFFFu, 0xFFFFFFFFu, 0xFFFFFFFFu};
    auto arm = [&](int s) {
#pragma unroll
        for (int i = 0; i < 4; ++i) st128(k.rs, l * 16 + i * 1024, k.ym(s) + yo, ones);
    };
    const unsigned pv = l == 0 && j % 5 == 0 ? (j / 5) * 128 + w * 32 : OOB;
    Red R;
    RSTAMP(2)
    arm(1);
#pragma unroll
    for (int i = 0; i < 8; ++i) st128(k.rs, (i || w > 1 || g > 1) ? g * 32768 + n * 16 : OOB, k.memf(i * 8 + (w >> 1) * 4) + EXP((2 * j + (w & 1)) * 256), ones);
#pragma unroll
    for (int i = 0; i < 4; ++i) { const unsigned q = min(i * 80 + j * 4 + w, 287); st128(k.rs, l * 16, k.p0 + PR_B + PR_SZ + EXP(q * 1024), ones); }
    sleep_until(k.t0 + RSL);
    y_wait(k, 2);
    R.yload<0, 16>(k, j, 0);
    for (int s = 0; s < 63; ++s) {
        STOP(s) HSTAMP(s, 0)
        const unsigned ap = k.p0 + PR_B + (s & 3) * PR_SZ + w * 49152, am = k.ym(s) + yo;
        const unsigned ph = (s & 4) ? 0x80008000u : 0u;
        unsigned spins = 0, tq, tv;
        unsigned fl;
        WAIT_BC(s, BSLP)
        st32(k.rs, pv, k.p0 + CNT(C_XB, 0), spack(((int)((tq - tv) << 16) < 0 ? tv : tq) + RDY, s + 2));
        STAMP(s, 5)
        char* rb = smem + (s & 1) * 4096;
_Pragma("unroll 1")
        for (spins = 0;; ++spins) {
            u32x4 ym[4], pm[5];
            f32x4 acc[2];
            if (spins == 0) wait_until(tv);
            asm volatile("" ::: "memory");
#pragma unroll
            for (int i = 0; i < 4; ++i) ym[i] = ld128(k.rs, v16 + (i >> 1) * 2048 + (i & 1) * 512, am);
            fl = ld32(k.rs, 0, (k.p0 ^ 8192u) + CNT(C_X1, j));
            asm volatile("" ::: "memory");
            if (spins == 0) { wait_until(tq); STAMP(s, 1) }
            asm volatile("" ::: "memory");
#pragma unroll
            for (int i = 0; i < 5; ++i) pm[i] = ld128(k.rs, i < 4 ? l * 16 + i * 1024 : vp, ap + (i >> 2) * 4096);
            acc[0] = (f32x4){0.f, 0.f, 0.f, 0.f}; acc[1] = acc[0];
            const u32x8 y0 = {ym[0][0], ym[1][0], ym[0][2], ym[1][2], ym[2][0], ym[3][0], ym[2][2], ym[3][2]};
            const u32x8 y1 = {ym[0][1], ym[1][1], ym[0][3], ym[1][3], ym[2][1], ym[3][1], ym[2][3], ym[3][3]};
            unsigned mx = 0;
#pragma unroll
            for (int i = 0; i < 4; ++i) mx = max(mx, umax4(ym[i]));
            unsigned tg = mx == 0xFFFFFFFFu ? 0x80000000u : 0u;
            asm volatile("s_nop 1" : "+v"(acc[0]), "+v"(acc[1]), "+v"(tg) : "v"(y0), "v"(y1), "v"(ix));
            SCHED0();
#pragma unroll
            for (int i = 0; i < 5; ++i) {
                const unsigned u0 = __builtin_amdgcn_bitop3_b32(pm[i][0], pm[i][1], ph, 0x7e), u1 = __builtin_amdgcn_bitop3_b32(pm[i][2], pm[i][3], ph, 0x7e);
                SCHED0();
                if (i < 4) smf<false>(acc[0], __builtin_bit_cast(bf16x8, pm[i]), __builtin_bit_cast(bf16x16, __builtin_shufflevector(R.Y[2 * i], R.Y[2 * i + 1], 0, 1, 2, 3, 4, 5, 6, 7)), ix);
                else smf<false>(acc[0], __builtin_bit_cast(bf16x8, pm[4]), __builtin_bit_cast(bf16x16, y0), ix);
                tg = (tg | u0) | u1;
                SCHED0();
                if (i < 4) smf<false>(acc[1], __builtin_bit_cast(bf16x8, pm[i]), __builtin_bit_cast(bf16x16, __builtin_shufflevector(R.Y[8 + 2 * i], R.Y[9 + 2 * i], 0, 1, 2, 3, 4, 5, 6, 7)), ix);
                else smf<false>(acc[1], __builtin_bit_cast(bf16x8, pm[4]), __builtin_bit_cast(bf16x16, y1), ix);
                SCHED0();
            }
            const unsigned long long bad = __ballot((int)tg < 0);
            asm volatile("s_nop 5" : "+v"(acc[0]), "+v"(acc[1]) : "s"(bad));
#pragma unroll
            for (int t = 0; t < 2; ++t) { float* rp = (float*)(rb + t * 2048 + w * 512 + g * 128 + n * 4); rp[0] = acc[t][0] + acc[t][2]; rp[16] = acc[t][1] + acc[t][3]; }
            if (bad == 0) break;
            PCOUNT(0)
            asm volatile("" ::: "memory");
            if (spin_fail(k, spins)) break;
        }
        STAMP(s, 2)
        RAWBAR()
        if (w < 2) {
            const char* rp = rb + w * 2048 + ((l >> 4) * 2 + ((l >> 2) & 1)) * 64 + ((l & 3) * 4 + ((l >> 3) & 1) * 2) * 4;
            const f32x2 a = *(const f32x2*)rp, b = *(const f32x2*)(rp + 512), c = *(const f32x2*)(rp + 1024), d = *(const f32x2*)(rp + 1536);
            const f32x2 r = (a + b) + (c + d);
            st32(k.rs, l * 4, k.memf(s + 1) + EXP((2 * red_col(j) + w) * 256), min(cvtpk(r[0], r[1]) ^ ph, 0xFFFFFFFEu));
        }
        STAMP(s, 3)
_Pragma("unroll 1")
        for (spins = 0; (int)__builtin_amdgcn_readfirstlane(fl) < s - 1; ++spins) {
            PCOUNT(1)
            __builtin_amdgcn_s_sleep(PSLEEP);
            if (spin_fail(k, spins)) break;
            fl = ld32(k.rs, 0, (k.p0 ^ 8192u) + CNT(C_X1, j));
        }
        __builtin_amdgcn_fence(__ATOMIC_ACQUIRE, "wavefront");
        if (Y1) wait_until(tv + Y1);
        y_wait(k, 2 * s + 3);
        R.yload<0, 8>(k, j, s + 1);
        if (ARMP == 0) arm(s + 2);
        if (Y2) wait_until(tv + Y2);
        y_wait(k, 2 * s + 4);
        STAMP(s, 4)
        if (ARMP == 2) { arm(s + 2); SCHED0(); }
        R.yload<8, 16>(k, j, s + 1);
        if (ARMP == 1) { SCHED0(); arm(s + 2); }
        PFLUSH(s)
    }
    PFLUSH(63)
    PROF_OUT
}
DEVI void tred_role(const Params& p, const Ctx& k, char* smem, int j) {
    PROF_DECL
    const int l = k.l, w = k.w, g = l >> 4, n = l & 15, T = 2 * red_col(j) + (w >> 1), rt = w & 1;
    const unsigned v16 = (g >> 1) * 1024 + (g & 1) * 256 + n * 16, yo = j * 32768 + EXP(w * 4096);
    const unsigned xo = EXP((T >> 1) * 2048) + (T & 1) * 512 + rt * 1024, xv = ((g >> 1) * 16 + n) * 16 + (g & 1) * 8;
    const u32x2 ones = {0xFFFFFFFFu, 0xFFFFFFFFu};
    const unsigned fl = (w == 0 && l == 0) ? 0u : OOB;
    unsigned fails = 0;
    Red R;
    RSTAMP(2)
    st64(k.rs, xv, k.segx(PG_X1, 0) + xo, ones); st64(k.rs, xv, k.segx(PG_X1, 1) + xo, ones);
    sleep_until(k.t0 + RSL);
    y_wait(k, 2);
    R.yload<0, 16>(k, j, 0);
    for (int s = 0; s < 64; ++s) {
        STOP(s) STAMP(s, 0)
        unsigned ap[4], am = k.ym(s) + yo, xa = k.segx(PG_X1, s) + xo, rb = (s & 1) * 16384 + l * 16, wb = rb + w * 1024, fv = s;
#pragma unroll
        for (int i = 0; i < 4; ++i) { ap[i] = k.p0 + PR_B + (s & 3) * PR_SZ + w * 49152 + (i < 2 ? 32768 : 16384) + (~i & 1) * 4096; PINS(ap[i]); }
        rb += w * 4096;
        PINS(am); PINS(xa); PIN(rb); PIN(wb); PIN(fv);
        const unsigned ph = (s & 4) ? 0x80008000u : 0u;
        unsigned spins = 0, tq, tv;
        u32x2 res;
        if (k.c) res = ld64(k.rs, xv, k.seg(PG_X2, s) + xo);
        else res = *(const u32x2*)(p.x + (size_t)(s * 32 + rt * 16 + n) * 640 + T * 16 + g * 4);
        f32x4 acc[2][2];
        WAIT_BC(s, TSLP)
        u32x4 yl[8];
#pragma unroll
        for (int i = 0; i < 8; ++i) { yl[i] = __builtin_shufflevector(R.Y[2 * i], R.Y[2 * i + 1], 0, 1, 4, 5); PIN(yl[i]); }
        f32x4 x = {blo(res[0] ^ ph), bhi(res[0] ^ ph), blo(res[1] ^ ph), bhi(res[1] ^ ph)};
        PIN(x);
        wait_until2(tv); wait_until2(tq + (s < 63 ? RDLY : 0));
        STAMP(s, 1)
        unsigned long long bad;
        do {
            asm volatile("" ::: "memory");
            u32x4 ym[4], pp[16];
#pragma unroll
            for (int i = 0; i < 4; ++i) { ym[i] = ld128(k.rs, v16 + (i >> 1) * 2048 + (i & 1) * 512, am); SCHED0(); }
#pragma unroll
            for (int i = 0; i < 16; ++i) { pp[i ^ 4] = ld128(k.rs, l * 16 + (i & 3) * 1024, ap[i >> 2]); SCHED0(); }
            unsigned mx = 0, tg, lim = 0x7FFFFFFFu | (fails << 15);
            ++fails; PINS(fails); PINS(lim);
#pragma unroll
            for (int i = 0; i < 4; ++i) mx = max(mx, umax4(ym[i]));
            tg = mx == 0xFFFFFFFFu ? 0x80000000u : 0u;
            u32x4 yf[4];
#pragma unroll
            for (int i = 0; i < 4; ++i) { const int t = i & 1, f = i >> 1; yf[i] = (u32x4){ym[2 * f][t], ym[2 * f][t + 2], ym[2 * f + 1][t], ym[2 * f + 1][t + 2]}; }
            asm volatile("s_nop 1" : "+v"(yf[0]), "+v"(yf[1]), "+v"(yf[2]), "+v"(yf[3]), "+v"(tg));
            SCHED0();
            sfor<32>([&](auto I) { constexpr int i = CV(I), t = i & 1, q = (i >> 1) ^ 4, r = q < 4 ? 0 : q < 8 ? (q >> 1) & 1 : 1;
                mfa<false, q == 4 || q == 6>(acc[t][r], __builtin_bit_cast(bf16x8, q < 4 ? yl[t * 4 + q] : q < 8 ? yf[(q & 1) * 2 + t] : R.Y[t * 8 + (q & 7)]), __builtin_bit_cast(bf16x8, pp[q]));
                SCHED0();
                tg |= __builtin_amdgcn_bitop3_b32(pp[q][2 * t], pp[q][2 * t + 1], ph, 0x7e); PIN(tg);
                if constexpr (i == 20 || i == 22) { asm volatile("" : "+v"(acc[(i - 20) / 2][0])); lw128(wb + (i - 20) * 4096, __builtin_bit_cast(u32x4, acc[(i - 20) / 2][0])); }
                if constexpr (i == TPOS) st32(k.rs, fl, (k.p0 ^ 8192u) + CNT(C_X1, j), fv);
                SCHED0(); });
            bad = __ballot(tg > lim);
        } while (__builtin_expect(bad != 0, 0));
        STAMP(s, 2)
        const unsigned yc = y_get(k);
        SCHED0();
        asm volatile("s_nop 2" : "+v"(acc[0][1]), "+v"(acc[1][1]));
        lw128(wb + 4096, __builtin_bit_cast(u32x4, acc[0][1])); lw128(wb + 12288, __builtin_bit_cast(u32x4, acc[1][1]));
        RAWBAR()
        {
            const f32x4 r0 = __builtin_bit_cast(f32x4, lr128(rb)), r1 = __builtin_bit_cast(f32x4, lr128(rb + 1024)), r2 = __builtin_bit_cast(f32x4, lr128(rb + 2048)), r3 = __builtin_bit_cast(f32x4, lr128(rb + 3072));
            SCHED0();
            if constexpr (TPOS == 32) st32(k.rs, fl, (k.p0 ^ 8192u) + CNT(C_X1, j), fv);
            SCHED0();
            const f32x4 r = (r0 + r1) + (r2 + r3);
            st64(k.rs, xv, xa, (u32x2){min(cvtpk(x[0] + bfr(r[0]), x[1] + bfr(r[1])) ^ ph, 0xFFFFFFFEu), min(cvtpk(x[2] + bfr(r[2]), x[3] + bfr(r[3])) ^ ph, 0xFFFFFFFEu)});
            SCHED0();
            if constexpr (TPOS == 33) st32(k.rs, fl, (k.p0 ^ 8192u) + CNT(C_X1, j), fv);
        }
        st64(k.rs, s < 62 ? xv : OOB, k.segx(PG_X1, (s + 2) & 63) + xo, ones);
        STAMP(s, 3)
        if (s < 63) {
            if (!__all((int)yc >= 2 * s + 4)) { PCOUNT(2) y_wait(k, 2 * s + 4); }
            __builtin_amdgcn_fence(__ATOMIC_ACQUIRE, "wavefront");
            R.yload<0, 16>(k, j, s + 1);
        }
        STAMP(s, 4)
        STAMP(s, 5)
        PFLUSH(s)
    }
    PROF_OUT
}

#undef TZ
#define TZ z0()
DEVI void up_role(const Params& p, const Ctx& k, char* smem, int id) {
    PROF_DECL
    const int l = k.l, w = k.w, g = l >> 4, n = l & 15;
    bf16x8 Wt[80];
    const int tile0 = id * 8 + w * 2;
    auto issue = [&](auto U) { constexpr int u = CV(U);
        sfor<8>([&](auto J) { constexpr int j = CV(J); dma1<(u % 3) * 32768 + j * 1024>(((w & 1) ? p.wu : p.wg) + (size_t)(2 * u + (w >> 1)) * 32 * 1536 + id * 128, wsrc<256, true>(j * 64 + l, 3072), w * 8192); }); };
    sfor<3>(issue);
    cstream<10>([](auto V) { return IC(CV(V) < 9 ? 8 : 0); },
        [&](auto U) { constexpr int u = CV(U);
            if constexpr (u > 0 && u < 8) issue(IC(u + 2));
#pragma unroll
            for (int i = 0; i < 8; ++i) Wt[(i & 3) * 20 + 2 * u + (i >> 2)] = wp_read<256, true>(smem + (u % 3) * 32768 + (i >> 1) * 8192, w * 2 + (i & 1), l); });
    RAWBAR()
    RSTAMP(2)
    bf16x8 one8 = {0x3F80, 0x3F80, 0x3F80, 0x3F80, 0x3F80, 0x3F80, 0x3F80, 0x3F80};
    asm volatile("s_nop 1" : "+v"(one8));
    f32x4 cs[4];
    sfor<80>([&](auto I) { constexpr int f = CV(I); mfa<(f < 64), f % 20 == 0>(cs[f / 20], Wt[f], one8); });
    asm volatile("s_nop 7" : "+v"(cs[0]), "+v"(cs[1]), "+v"(cs[2]), "+v"(cs[3]));
    const unsigned ho = EXP((tile0 >> 1) * 1024), l16 = l * 16;
    constexpr int L_ST = 81920;
    const u32x4 ones = {0xFFFFFFFFu, 0xFFFFFFFFu, 0xFFFFFFFFu, 0xFFFFFFFFu};
#pragma unroll
    for (int i = 0; i < 4; ++i) st128(k.rs, l16, k.hseg(i >> 1) + (i & 1) * EXP(49152) + ho, ones);
    unsigned fails = 0;
    lw32(L_ST + 1024, 0u);
    prog_init(k);
    const unsigned fl = (w == 0 && l == 0) ? 0u : OOB;
    const unsigned long long m1 = __ballot(n & 1), m2 = __ballot(n & 2);
    for (int s = 0; s < 64; ) {
        unsigned xs = k.seg(PG_X1, s) + w * 2048, xd = (s & 1) * 40960 + w * 2048;
        PINS(xs); PINS(xd);
        prog_wait<20>(k, k.p0 + CNT(C_X1, 0), s);
        STOP(s) STAMP(s, 0)
        xn_all(k, l16, xs, xd);
        const unsigned xb = (s & 1) * 40960 + l16, yb = xb + w * 2048, sp = L_ST + (s & 1) * 2048, rp = sp + n * 16;
        const unsigned dp = sp + (g == (n >> 2) ? n * 16 + w * 4 : 4096 + l * 4);
        f32x4 acc[2][4], st[4];
        bf16x8 x[20], x1[20], y0[5], y1[5];
        unsigned mx = 0;
        auto blk = [&](auto B, auto J) { constexpr int b = CV(B), j = CV(J);
            if constexpr (j == 0) asm volatile("s_waitcnt vmcnt(%0)\n\ts_barrier" :: "n"(8 - 2 * b) : "memory");
            if constexpr (j >= 1 && j < 5) x[4 * b + j - 1] = lr128(xb + (4 * b + j - 1) * 2048);
            if constexpr (j == 5) y0[b] = lr128(yb + b * 8192);
            if constexpr (j == 6) y1[b] = lr128(yb + b * 8192 + 1024); };
        sfor<7>([&](auto J) { blk(IC(0), J); });
        SCHED0();
        STAMP(s, 1)
        float mean[2], rden[2], e[4][4];
        f32x4 sa[2], sb[2];
        u32x4 bad, o[2];
        unsigned sv, fv, r, f0, f1, ha = k.hseg(s) + ho;
        PINS(ha);
        auto stat = [&](auto I) { constexpr int i = CV(I), rt = i / 8, j = i % 8;
            if constexpr (j == 0) asm volatile("v_add_f32 %0, %2, %3\n\tv_add_f32 %1, %4, %5" : "=&v"(mean[rt]), "=&v"(rden[rt]) : "v"(sa[rt][0]), "v"(sa[rt][1]), "v"(sb[rt][0]), "v"(sb[rt][1]));
            if constexpr (j == 1 || j == 2) asm volatile("v_add_f32 %0, %0, %2\n\tv_add_f32 %1, %1, %3" : "+v"(mean[rt]), "+v"(rden[rt]) : "v"(sa[rt][j + 1]), "v"(sb[rt][j + 1]));
            if constexpr (j == 3) asm volatile("v_mul_f32 %0, 0xbaccccCD, %0\n\tv_mul_f32 %1, 0x3acccccd, %1" : "+v"(mean[rt]), "+v"(rden[rt]));
            if constexpr (j == 4) asm volatile("v_fma_f32 %1, -%0, %0, %1\n\tv_cvt_pk_bf16_f32 %0, 0, %0\n\tv_max_f32 %1, 0, %1" : "+v"(mean[rt]), "+v"(rden[rt]));
            if constexpr (j == 5) asm volatile("v_cvt_pk_bf16_f32 %0, 0, %0\n\tv_add_f32 %0, 0x3727c5ac, %0\n\tv_cvt_pk_bf16_f32 %0, 0, %0" : "+v"(rden[rt]));
            if constexpr (j == 6) asm volatile("v_sqrt_f32 %0, %0" : "+v"(rden[rt]));
            if constexpr (j == 7) asm volatile("v_cvt_pk_bf16_f32 %0, 0, %0\n\tv_rcp_f32 %0, %0" : "+v"(rden[rt])); };
        auto ep = [&](auto RT, auto T, auto J) { constexpr int rt = CV(RT), t = CV(T), j = CV(J), c = (j & 1) * 2, u = j >= 20, q = u ? j - 20 : j;
            float (&a)[4] = e[t + 2 * u];
#define A2 "+v"(a[c]), "+v"(a[c + 1])
            if constexpr (j < 0 || j >= 30) { }
            else if constexpr (q < 2) asm volatile("v_fma_f32 %0, %2, %3, %5\n\tv_fma_f32 %1, %2, %4, %6" : "=&v"(a[c]), "=&v"(a[c + 1]) : "v"(mean[rt]), "v"(cs[t + 2 * u][c]), "v"(cs[t + 2 * u][c + 1]), "v"(acc[rt][t + 2 * u][c]), "v"(acc[rt][t + 2 * u][c + 1]));
            else if constexpr (q < 4) asm volatile("v_mul_f32 %0, %0, %2\n\tv_mul_f32 %1, %1, %2" : A2 : "v"(rden[rt]));
            else if constexpr (q < 6 || j == 18 || j == 19) asm volatile("v_cvt_pk_bf16_f32 %0, 0, %0\n\tv_cvt_pk_bf16_f32 %1, 0, %1" : A2);
            else if constexpr (j < 8) asm volatile("v_mul_f32 %0, 0xbfb8aa3b, %0\n\tv_mul_f32 %1, 0xbfb8aa3b, %1" : A2);
            else if constexpr (j < 12) asm volatile("v_exp_f32 %0, %0" : "+v"(a[j & 3]));
            else if constexpr (j < 14) asm volatile("v_add_f32 %0, 1.0, %0\n\tv_add_f32 %1, 1.0, %1" : A2);
            else if constexpr (j < 18) asm volatile("v_rcp_f32 %0, %0" : "+v"(a[(j - 14) & 3]));
            else if constexpr (q < 8) asm volatile("v_mul_f32 %0, %0, %2\n\tv_mul_f32 %1, %1, %3" : A2 : "v"(e[t][c]), "v"(e[t][c + 1]));
            else if constexpr (q == 8) asm volatile("v_cvt_pk_bf16_f32 %0, %0, %2\n\tv_cvt_pk_bf16_f32 %1, %1, %3" : "+v"(a[0]), "+v"(a[2]) : "v"(a[1]), "v"(a[3]));
            else { unsigned d0, d1; asm volatile("v_min_u32 %0, -2, %2\n\tv_min_u32 %1, -2, %3" : "=&v"(d0), "=&v"(d1) : "v"(a[0]), "v"(a[2])); o[rt][2 * t] = d0; o[rt][2 * t + 1] = d1; } };
        sfor<180>([&](auto I) {
            constexpr int a = CV(I), b = a / 8, j = (a % 8) ^ (b == 4 ? 4 : 0), ch = a / 20 - 1, ks = a < 40 ? 4 * b + (j & 3) : a % 20, rt = ch >= 4;
            constexpr int nt = a < 40 ? 0 : rt ? ch - 4 : (ch & 1) * 2 + (ch >> 1), f = nt * 20 + ks, i = a - 36;
            if constexpr (a >= 40 || j < 4) mfa<(f < 64), ks == 0>(acc[rt][nt], Wt[f], rt ? x1[ks] : x[ks]);
            else mfa<false, b == 0>(st[j & 3], (j & 1) ? (j & 2 ? y1[b] : y0[b]) : one8, j & 2 ? y1[b] : y0[b]);
            SCHED0();
            if constexpr (a >= 103 && a < 119) TOUCH(x1[(a - 99) % 20]);
            else if constexpr (a == 60) { r = __builtin_amdgcn_readfirstlane(bad[0]); PINS(r); }
            else if constexpr (a == 61) { r = (fails - r) >> 31; PINS(r); }
            else if constexpr (a == 62) { fails += r; PINS(fails); }
            else if constexpr (a == 63) { f0 = 2 * s - r; PINS(f0); }
            else if constexpr (a == 64) { f1 = f0 | 1; PINS(f1); }
            else if constexpr (a == 65) { r <<= 31; PINS(r); }
            else if constexpr (a >= 56 && (a < 92 || a >= 119)) __builtin_amdgcn_s_setprio(0);
            if constexpr (a < 32 && a % 8 == 7) asm volatile("" :: "v"(x[4 * b + 4]), "v"(x[4 * b + 5]), "v"(x[4 * b + 6]), "v"(x[4 * b + 7]), "v"(y0[b + 1]));
            if constexpr (a < 32 && a % 8 == 4) TOUCH(y1[b]);
            SCHED0();
            if constexpr (a == 15) lw32((rp ^ 2048) + 1024, z0());
            if constexpr (a < 32 && a % 8 < 7) blk(IC((b + 1) % 5), IC(a % 8));
            if constexpr (i == 0 || i == 4 || i == 5 || i == 8) x1[i / 2 - (i == 4 || i == 8)] = lr128(xb + 1024 + (i / 2 - (i == 4 || i == 8)) * 2048);
            if constexpr (a >= 92 && a < 108) x1[a - 88] = lr128(xb + 1024 + (a - 88) * 2048);
            SCHED0();
            if constexpr (a < 40 && j >= 4) { const u32x4 v = __builtin_bit_cast(u32x4, (j & 1) ? y1[b] : y0[b]); mx = umax3(mx, v[j & 2], v[(j & 2) + 1]); PIN(mx); }
            if constexpr (i == 1 || i == 3) {
                constexpr int rt2 = i == 3;
                asm volatile("" : "+v"(st[2 * rt2]), "+v"(st[2 * rt2 + 1]));
                const f32x4& q = st[2 * rt2 + 1];
                const float d = csel(csel(q[0], q[1], m1), csel(q[2], q[3], m1), m2);
                lw32(dp + rt2 * 512, __float_as_uint(st[2 * rt2][0])); lw32(dp + rt2 * 512 + 256, __float_as_uint(d));
            }
            if constexpr (i == 2) lw32(mx == 0xFFFFFFFFu ? sp + 1024 : dp + 4096, 4096u);
            if constexpr (i == 6) bar_lgkm<2>();
            if constexpr (i == 6) sa[0] = __builtin_bit_cast(f32x4, lr128(rp));
            if constexpr (i == 7) sb[0] = __builtin_bit_cast(f32x4, lr128(rp + 256));
            if constexpr (i == 9) sa[1] = __builtin_bit_cast(f32x4, lr128(rp + 512));
            if constexpr (i == 10) sb[1] = __builtin_bit_cast(f32x4, lr128(rp + 768));
            if constexpr (i == 11) bad = __builtin_bit_cast(u32x4, lr128(rp + 1024));
            if constexpr (i >= 12 && i < 20) stat(IC(i - 12));
            if constexpr (a == 40) { STAMP(s, 2) }
            if constexpr (a == UPOS0 || a == UPOS) st32(k.rs, fl, k.p0 + CNT(C_H, id), fv);
            if constexpr (a == 117) __builtin_amdgcn_raw_buffer_store_b128(o[0], k.rs, (int)sv, (int)ha, AUX_SC1);
            if constexpr (a == 118) TOUCH(o[0]);
            SCHED0();
            ep(IC(0), IC(0), IC(a - 56)); ep(IC(0), IC(1), IC(a - 86));
            if constexpr (a >= 116 && a < 124) stat(IC(a - 108));
            if constexpr (a == UPOS0 - 2 || a == UPOS - 2) { fv = a == UPOS - 2 ? f1 : f0; PIN(fv); }
            if constexpr (a == 80) { sv = l16 | r; PIN(sv); }
            if constexpr (a < 144) ep(IC(1), IC(0), IC(a - 124));
            else if constexpr (a < 164) ep(IC(1), IC(1), IC(a - 144));
            else ep(IC(1), IC(0), IC(a - 144));
            SCHED0();
        });
        STAMP(s, 3)
        asm volatile("s_nop 7" : "+v"(acc[1][3]));
        {
            const f32x4 u = (acc[1][3] + mean[1] * cs[3]) * rden[1];
            o[1][2] = packs(bfr(u[0]) * e[1][0], bfr(u[1]) * e[1][1]); o[1][3] = packs(bfr(u[2]) * e[1][2], bfr(u[3]) * e[1][3]);
        }
        st128(k.rs, sv, ha + EXP(49152), o[1]);
        if (r) { PCOUNT(0) RAWBAR() lw32(rp + 1024, 0u); RAWBAR() }
        st128(k.rs, s < 62 ? l16 : OOB, k.hseg((s + 2) & 63) + ho, ones); st128(k.rs, s < 62 ? l16 : OOB, k.hseg((s + 2) & 63) + EXP(49152) + ho, ones);
        STAMP(s, 4) STAMP(s, 5)
        PFLUSH(s)
        s = (f1 + 1) >> 1;
    }
    PROF_OUT
}

DEVI void down_role(const Params& p, const Ctx& k, char* smem, int id) {
    PROF_DECL
    const int l = k.l, w = k.w, g = l >> 4, n = l & 15, rt = id / 5, cg = id % 5;
    bf16x8 Wt[96];
    wstream<12, 8, 4>(smem + w * 32768,
        [&](auto U, unsigned sl) { constexpr int u = decltype(U)::value; sfor<8>([&](auto J) { constexpr int j = CV(J); dma1<(u % 4) * 8192 + j * 1024>(p.wd + (size_t)(w * 12 + u) * 32 * 640 + cg * 128, wsrc<256, false>(j * 64 + l, 1280), sl); }); },
        [&](auto U, char* sp) { constexpr int u = decltype(U)::value;
#pragma unroll
            for (int nt = 0; nt < 8; ++nt) Wt[u * 8 + nt] = wp_read<256, false>(sp, (nt + 2 * w) & 7, l); });
    RAWBAR()
    RSTAMP(2)
    const unsigned xo = EXP((cg * 4 + w) * 2048) + rt * 1024, xv = ((g >> 1) * 16 + n) * 16 + (g & 1) * 8;
    const u32x2 ones = {0xFFFFFFFFu, 0xFFFFFFFFu};
    const unsigned x2v = k.c ? xv : OOB;
    prog_init(k);
#pragma unroll
    for (int i = 0; i < 4; ++i) st64(k.rs, x2v, k.seg(PG_X2, i >> 1) + xo + (i & 1) * 512, ones);
    const __amdgpu_buffer_rsrc_t ro = __builtin_amdgcn_make_buffer_rsrc((void*)p.out, 0, 2048 * 1280, 0x00020000);
    const unsigned ov = k.c ? OOB : n * 1280 + g * 8, lim = k.c ? 0xFFFFFFFEu : 0xFFFFFFFFu, fl = (w == 0 && l == 0) ? 0u : OOB;
    unsigned fails = 0;
    for (int s = 0; s < 64; ++s) {
        unsigned la[3], ra = k.seg(PG_X1, s) + xo, oa = k.c ? k.seg(PG_X2, s) + xo : (s * 32 + rt * 16) * 1280 + (cg * 8 + w * 2) * 32, rb = (s & 1) * 32768 + l * 16, wb[3], fv = s;
#pragma unroll
        for (int q = 0; q < 3; ++q) { la[q] = k.hseg(s) + rt * EXP(49152) + EXP((w * 12 + q * 4) * 1024); wb[q] = rb + ((w + q + 1) & 3) * 6144 + q * 2048; PINS(la[q]); PIN(wb[q]); }
        rb += w * 6144;
        PINS(ra); PINS(oa); PIN(rb); PIN(fv);
        prog_wait<12>(k, k.p0 + CNT(C_H, 0), 2 * s + rt);
        STOP(s) STAMP(s, 0)
        f32x4 acc[8], r[6];
        u32x2 res[2];
        u32x4 hx[12];
        unsigned long long bad;
        do {
            asm volatile("" ::: "memory");
#pragma unroll
            for (int i = 0; i < 12; ++i) { hx[i] = ld128(k.rs, l * 16 + (i & 3) * 1024, la[i >> 2]); SCHED0(); }
#pragma unroll
            for (int t = 0; t < 2; ++t) res[t] = ld64(k.rs, xv + t * 512, ra);
            SCHED0();
            unsigned mx = 0;
            sfor<72>([&](auto I) { constexpr int i = CV(I), ks = i / 6, j = 2 + i % 6, f = ks * 8 + j;
                mfa<(f < 64), ks == 0>(acc[j], Wt[f], __builtin_bit_cast(bf16x8, hx[ks]));
                SCHED0();
                if constexpr (i % 6 < 2) { mx = umax3(mx, hx[ks][(i & 1) * 2], hx[ks][(i & 1) * 2 + 1]); PIN(mx); }
                if constexpr (i == 68) { bad = __ballot(mx == 0xFFFFFFFFu); PINS(bad); }
                if constexpr (i == 69) { bad = fails < 65536u ? bad : 0; PINS(bad); }
                if constexpr (i == 70) { ++fails; PINS(fails); }
                if constexpr (i == DPOS) st32(k.rs, fl, k.p0 + CNT(C_X2, rt * 5 + cg), fv);
                SCHED0(); });
        } while (__builtin_expect(bad != 0, 0));
        STAMP(s, 1)
        float x[8];
        sfor<24>([&](auto I) { constexpr int i = CV(I), ks = i / 2, j = i & 1, f = ks * 8 + j;
            mfa<(f < 64), ks == 0>(acc[j], Wt[f], __builtin_bit_cast(bf16x8, hx[ks]));
            SCHED0();
            if constexpr (i < 12 && !(i & 1)) { constexpr int jj = 2 + i / 2;
                asm volatile("" : "+v"(acc[jj])); lw128(wb[jj / 2 - 1] + (jj & 1) * 1024, __builtin_bit_cast(u32x4, acc[jj])); }
            if constexpr (i < 8 && (i & 1)) { x[i - 1] = blo(res[i >> 2][(i >> 1) & 1]); x[i] = bhi(res[i >> 2][(i >> 1) & 1]); PIN(x[i - 1]); PIN(x[i]); }
            if constexpr (i == 13) bar_lgkm<0>();
            if constexpr (i >= 13 && i < 19) r[i - 13] = __builtin_bit_cast(f32x4, lr128(rb + (i - 13) * 1024));
            if constexpr (i + 72 == DPOS) st32(k.rs, fl, k.p0 + CNT(C_X2, rt * 5 + cg), fv);
            SCHED0(); });
        r[0] += r[2] + r[4]; r[1] += r[3] + r[5];
        asm volatile("s_nop 3" : "+v"(acc[0]), "+v"(acc[1]), "+v"(r[0]), "+v"(r[1]));
#pragma unroll
        for (int t = 0; t < 2; ++t) {
            const f32x4 d = acc[t] + r[t];
            const u32x2 o = {min(cvtpk(x[4 * t] + bfr(d[0]), x[4 * t + 1] + bfr(d[1])), lim), min(cvtpk(x[4 * t + 2] + bfr(d[2]), x[4 * t + 3] + bfr(d[3])), lim)};
            st64(k.rs, x2v + t * 512, oa, o);
            __builtin_amdgcn_raw_buffer_store_b64(o, ro, (int)(ov + t * 32), (int)oa, 0);
        }
        STAMP(s, 2)
#pragma unroll
        for (int t = 0; t < 2; ++t) st64(k.rs, s < 62 ? x2v : OOB, k.seg(PG_X2, (s + 2) & 63) + xo + t * 512, ones);
        STAMP(s, 3) STAMP(s, 4) STAMP(s, 5)
        PFLUSH(s)
    }
    PROF_OUT
}

DEVI void warm(const Params& p, const Ctx& k, int xl, int hw) {
    set_m0(k.w * 256);
_Pragma("unroll 1")
    for (int it = hw; it < 32 * 15; it += k.c ? 40 : 64) {
        const int q = it < 120 ? it & 7 : 8 + (it - 120) % 24, t = it < 120 ? it >> 3 : (it - 120) / 24;
        const u16* b = q < 24 ? (q < 8 ? p.Wv : q < 16 ? p.Wk : p.Wq) + (xl * 8 + (q & 7)) * 192 : p.Wo + (xl * 8 + (q & 7)) * 122880;
        const unsigned ln = t * 64 + k.l, cl = min(((unsigned)(size_t)b >> 7) & 7u, 4u);
        if (k.c) sleep_until(k.t0 + WP + it * PP / 128);
        if (t < (q >= 24 ? PO : ((q < 8 ? PV : PQ) >> (cl * 4)) & 15))
            asm volatile("global_load_lds_dword %0, %1" :: "v"(q < 24 ? (639 - ln / 3) * 12288 + (ln % 3) * 128 : (191 - (ln >> 2)) * 1280 + 768 + (ln & 3) * 128), "s"(b) : "memory");
    }
    DRAIN();
_Pragma("unroll 1")
    for (int i = 3; i < 8; ++i) { sleep_until(k.t0 + i * 50); hello(k, 0); }
    DRAIN();
    RAWBAR()
}

DEVI int xcc_id() { return __builtin_amdgcn_s_getreg(20 | (0 << 6) | (3 << 11)) & 7; }

DEVI unsigned prepare(const Params& p, Ctx& k, char* smem, int xl, int i) {
    const int l = k.l, j = xl * 22 + i - 4;
    const u32x4 ones = {0xFFFFFFFFu, 0xFFFFFFFFu, 0xFFFFFFFFu, 0xFFFFFFFFu};
    unsigned vote = 0;
    if (i < 4) {
        const int row = ((i * 4 + xl) * 4 + k.w) * 4 + (l >> 4);
        const unsigned dst = k.seg(PG_XNA, i * 2 + (xl >> 1));
        u32x4 a[5];
#pragma unroll
        for (int c = 0; c < 5; ++c) a[c] = *(const u32x4*)(p.x + (size_t)row * 640 + (c * 16 + (l & 15)) * 8);
        ln_rows(a, l & 15, row & 31, [&](unsigned vo, int c, u32x4 o) { st128(k.rs, vo, dst + c * 16384, o); st128(k.rs, vo, dst + c * 16384 + 8192, o); });
    } else if (k.w == 3) {
        const unsigned cal = ((unsigned)(j % 44) << 21) + 7 * 16384 + (xl >> 1) * 4096 + (l & 7) * 16;
        int d = 0;
#pragma unroll 1
        for (int r = 0; r <= 2 * CALR; ++r) {
            unsigned long long c0, c1;
            st128(k.rs, cal, (r & 1) * 8192, ones);
            st128(k.rs, cal, (~r & 1) * 8192, ones);
            asm volatile("s_waitcnt vmcnt(1)\n\ts_memtime %0\n\ts_waitcnt vmcnt(0)\n\ts_memtime %1\n\ts_waitcnt lgkmcnt(0)" : "=&s"(c0), "=s"(c1) :: "memory");
            d = r ? min((int)(c1 - c0), 400) - d : 0;
        }
        vote = (unsigned)(d < 0) ^ (unsigned)(xcc_id() >> 2) ^ (unsigned)(i & 1);
    } else {
        if (k.w == 2 && j < 20) {
            const int e = j * 32 + (l >> 1), T = e >> 4, hf = (e >> 1) & 1, rr = ((e >> 2) & 3) * 2 + (e & 1);
            u32x4 v;
#pragma unroll
            for (int c = 0; c < 4; ++c) v[c] = min(*(const unsigned*)(p.mem0 + rr * 640 + T * 16 + 4 * c + 2 * hf), 0xFFFFFFFEu);
            st128(k.rs, (2u << 21) + EXP(e * 16) + (l & 1) * 8192, 0, v);
        }
        constexpr unsigned N0 = PR_B + PR_SZ, N1 = 40 * 16384, N2 = 32768;
        static_assert(N0 + N1 + N2 <= 4 * 88 * 3072 && NPAGES <= 44, "fill");
#pragma unroll
        for (int r = 0; r < 4; ++r) {
            const unsigned o = ((r * 88 + j) * 3 + k.w) * 1024;
            st128(k.rs, o < N0 + N1 + N2 ? l * 16 : OOB, o < N0 ? o : o < N0 + N1 ? ((unsigned)PG_YM << 21) + (o - N0) : (2u << 21) + 32768 + (o - N0 - N1), ones);
        }
    }
    if (k.w == 3) *(volatile LAS unsigned*)smem = vote;
    DRAIN();
    __builtin_amdgcn_s_waitcnt(0x0f70);
    XSTAMP(0)
    RAWBAR()
    return *(volatile LAS unsigned*)smem;
}
DEVI void master(const Params& p, const Ctx& k) {
    const int l = k.l;
    u32x2 v[4] = {{0u, 0u}, {0u, 0u}, {0u, 0u}, {0u, 0u}};
_Pragma("unroll 1")
    for (unsigned spins = 0; spins < SPIN_CAP; ++spins) {
#pragma unroll
        for (int q = 0; q < 4; ++q) v[q] |= cm_ld(k, (CM_IN + (q * 64 + l) * 128) | v[q][1]);
        if (__all((v[0][1] & v[1][1] & v[2][1] & v[3][1]) != 0)) break;
        asm volatile("" ::: "memory");
    }
    const unsigned long long a = __ballot(v[2][0] != 0), b = __ballot(v[3][0] != 0);
    const int n = __popcll(a) + __popcll(b);
    const unsigned long long g = n > 44 ? ~0ull : 0ull, u = (a & b) | ((a ^ b) & g);
    const unsigned long long t = (__popcll(a ^ b) > 2 || n > 80 || n < 8 ? g : ((u >> 4) & 0x3FFFFF) | ((u >> 36) << 22)) ^ 0xAAAAAAAAAAAAAAAAull;
#pragma unroll
    for (int q = 0; q < 4; ++q) cm_st(k, l * 128, CM_GO + q * 8192, (unsigned)t, (unsigned)(t >> 32) | 0x80000000u);
}

#define FIN(n) { __builtin_amdgcn_s_waitcnt(0x0f70); asm volatile("s_nop " #n); return; }
__global__ __launch_bounds__(256, 1) void mega_kernel(Params p) {
    extern __shared__ __attribute__((aligned(16))) char smem[];
    Ctx k;
    const u16 *wq = p.Wq, *wk = p.Wk, *wv = p.Wv;
    asm volatile("" : "+s"(wq), "+s"(wk), "+s"(wv) : "s"(p.cm));
    k.ws = p.ws; k.cm = p.cm;
    k.rs = __builtin_amdgcn_make_buffer_rsrc((void*)p.ws, 0, 0x7fffffff, 0x00020000);
    k.l = threadIdx.x & 63; k.w = __builtin_amdgcn_readfirstlane(threadIdx.x >> 6);
    RSTAMP(0)
    const int x = blockIdx.x & 7, i = blockIdx.x >> 3, xl = x & 3;
    k.role = x * 32 + i; k.c = x >> 2; k.par = 0; k.p0 = 0; k.t0 = rclk();
    line_st(k, CM_GO, 0, 0);
    auto start = [&]() {
        const u16* a = (i < 8 ? wk : wv) + (xl * 8 + (i & 7)) * 192, * b = (i < 8 ? wq : wv + 32 * 6144) + (xl * 8 + (i & 7)) * 192;
        unsigned o[3];
        sfor<18>([&](auto J) { constexpr int j = CV(J);
            if constexpr (j < 3) o[j] = wsrc<384, true>(k.w * 192 + j * 64 + k.l, 12288);
            dma1<SLOT(j / 6) + ((j / 3) & 1) * 12288 + (j % 3) * 1024>((((j / 3) & 1) ? b : a) + (j / 6) * (i < 8 ? 32 * 6144 : 64 * 6144), o[j % 3], k.w * 3072); }); };
    if (!k.c && i < 16) start();
    if (__builtin_expect(k.role == MASTER, 0)) {
#pragma unroll
        for (int q = 0; q < 16; ++q) cm_st(k, k.l * 8, CM_IN + k.w * 8192 + q * 512, 0, 0);
        cm_st(k, 0, CM_TMO, ~0u, ~0u);
        DRAIN();
        RAWBAR()
    }
    if (__builtin_expect(k.c && i < 26, 0)) { const unsigned v = prepare(p, k, smem, xl, i); RSTAMP(1) hello(k, v); go_wait(k, v); XSTAMP(1) RAWBAR() }
    else { RSTAMP(1) if (k.c) { DRAIN(); hello(k, 0); } }
    if (__builtin_expect(k.role == MASTER && k.w == 0, 0)) { master(p, k); __builtin_amdgcn_s_waitcnt(0x0f70); }
    if (k.c && i < 16) { sleep_until(k.t0 + WD1 + (i ^ 8) * STG); start(); }
    if (i < 8) { k.l = lane(); qk_role(p, k, smem, xl * 8 + i); FIN(0) }
    if (i < 16) { k.l = lane(); vo_role(p, k, smem, xl * 8 + i - 8); FIN(1) }
    k.l = lane();
    if (!k.c || i < 26) warm(p, k, xl, (i - 16) * 4 + k.w);
    else RAWBAR()
    go_wait(k, 0);
    if (i >= 26 && !k.c) {
        sleep_until(k.t0 + XPF);
#pragma unroll
        for (int r = 0; r < 3; ++r) asm volatile("global_load_lds_dword %0, %1" :: "v"(min(40 + r * 96 + ((i - 26) * 4 + xl) * 4 + k.w, 319) * 8192 + k.l * 128), "s"(p.x) : "memory");
        DRAIN();
        RAWBAR()
    }
    if (i >= 26) sleep_until(k.t0 + (k.c ? GST : GSA) + ((xl & 1) == UPODD ? 0 : i < 31 ? GSD : -GSA));
    if (i < 21) { mred_role(p, k, smem, xl * 5 + i - 16); FIN(2) }
    if (i < 26) { tred_role(p, k, smem, xl * 5 + i - 21); FIN(3) }
    if ((xl & 1) == UPODD) { up_role(p, k, smem, (xl >> 1) * 6 + i - 26); FIN(4) }
    if (i < 31) { down_role(p, k, smem, (xl >> 1) * 5 + i - 26); FIN(5) }
    row_role(p, k, smem, xl >> 1);
    FIN(6)
}

extern "C" void kernel_launch(void* const* d_in, const int* in_sizes, int n_in,
                              void* d_out, int out_size, void* d_ws, size_t ws_size,
                              hipStream_t stream) {
    Params p;
    p.x = (const u16*)d_in[0]; p.mem0 = (const u16*)d_in[1];
    p.Wq = (const u16*)d_in[2]; p.Wk = (const u16*)d_in[3]; p.Wv = (const u16*)d_in[4]; p.Wo = (const u16*)d_in[5];
    p.wg = (const u16*)d_in[8]; p.wu = (const u16*)d_in[9]; p.wd = (const u16*)d_in[10];
    p.out = (u16*)d_out; p.cm = (char*)d_ws;
    p.ws = (char*)(((uintptr_t)d_ws + CM_BYTES + ((1u << 21) - 1)) & ~(uintptr_t)((1u << 21) - 1));
    (void)hipFuncSetAttribute((const void*)mega_kernel, hipFuncAttributeMaxDynamicSharedMemorySize, LDS_BYTES);
    mega_kernel<<<256, 256, LDS_BYTES, stream>>>(p);
}
```

```cpp
#define SWAPG
#include <hip/hip_runtime.h>
#include <hip/hip_bf16.h>
#include <stdint.h>
#include <utility>

typedef unsigned short u16;
typedef __attribute__((ext_vector_type(16))) short bf16x16;
typedef __attribute__((ext_vector_type(8))) short bf16x8;
typedef __attribute__((ext_vector_type(4))) short bf16x4;
typedef __attribute__((ext_vector_type(8))) __bf16 hbf8;
typedef __attribute__((ext_vector_type(16))) __bf16 hbf16;
typedef __attribute__((ext_vector_type(4))) float f32x4;
typedef __attribute__((ext_vector_type(2))) float f32x2;
typedef __attribute__((ext_vector_type(8))) unsigned u32x8;
typedef __attribute__((ext_vector_type(4))) unsigned u32x4;
typedef __attribute__((ext_vector_type(2))) unsigned u32x2;
typedef __attribute__((ext_vector_type(2))) __bf16 bf16x2_t;
#define LAS __attribute__((address_space(3)))
#define GAS __attribute__((address_space(1)))
#define DEVI __device__ __forceinline__

#define PSLEEP 8
#define QT 1
#define GVS 0
#define CPOS (34 - 2 * QT)
#define EPOS 110
#define XA -5
#define SOFT2 1
#define YS 2
#define Y1 0
#define Y2 0
#define YB 58
#define ARMP 2
#define WD1 1000
#define GST 550
#define NTA " nt"
#define NTB ""
#define PQ 0x077A0
#define PV 0x4AAE4
#define GSA 1600
#define QGS 8
#define VGS 8
#define MN 480
#define MD 250
#define MP 100
#define RSL 1000
#define GSD 250
#define PO 0
#define PDLY 0
#define WP 600
#define PP 100
#define STG 0
#define UPOS0 88
#define DPOS 88
#define XPF 1400
#define TPOS 33
#define XS0 40
#define XP 23
#define XB 66
#define TB 107
#define PACE 65
#define XPOS 68
#define YPOS 40
#define XPOSY 1
#define PWS 1
#define WUS 0
#define WMIX 1
#define PACE2 30
#define GDLY 80
#define QDLY 0
#define CALR 1
#define SPG 3
#define DQ 16
#define DV 12
#define BSLP 1
#define SDLY 0
#define UPOS 150
#define RDLY 45
#define NOYST 0
#define NOTOK 0
#define NOYLD 0
#define GPOSY 8
#define OLDMAP 1
#define UPODD 0
#define XLD 10
#define TROT 8
#define KOUT 1
#define RPOS 69
#define RDY 90
#define APOS 6
#define GPOS 74

#define EXP(o) ((((unsigned)(o) >> 13) << 14) | ((unsigned)(o) & 8191u))
enum { C_XN = 0, C_Y = 1, C_X1 = 2, C_XB = 3, C_H = 4, C_X2 = 5, C_BC = 6 };
#define CNT(type, s) ((unsigned)((type) * 16384 + (s) * 128))
#define YLINE(w, h) CNT(C_Y + ((w) >> 1) * 6, ((w) & 1) * 32 + (h))
constexpr unsigned PR_B = EXP(65536), PR_SZ = EXP(12 * 8192);
constexpr unsigned DUMMY_B = EXP(0);
constexpr int PG_MEMF = 2;
#define YXR 8
constexpr int PG_YX = 3;
constexpr int PG_XNA = PG_YX + 2 * YXR;
constexpr int PG_X1 = PG_XNA + 4, PG_X2 = PG_X1 + 4;
constexpr int PG_H = PG_X2 + 4;
constexpr int PG_YM = PG_H + 8;
constexpr int NPAGES = PG_YM + 2;
static_assert(NPAGES <= 64, "map");
constexpr unsigned CM_TAB = 0, CM_TICKET = 1024, CM_TMO = 4096, CM_IN = 8192, CM_GO = 40960, CM_BYTES = 73728;
constexpr int MASTER = 5 * 32 + 31;
constexpr unsigned SPIN_CAP = 1u << 20;
#define AUX_SC1 16
#define LDS_BYTES (160 * 1024)

struct Params {
    const u16* x; const u16* mem0; const u16* Wq; const u16* Wk; const u16* Wv; const u16* Wo;
    const u16* wg; const u16* wu; const u16* wd; u16* out; char* cm; char* ws;
};
struct Ctx {
    char* ws; char* cm; __amdgpu_buffer_rsrc_t rs; unsigned long long par;
    int c, l, w, role;
    unsigned p0, t0;
    DEVI unsigned pg(int page) const { return ((unsigned)page << 21) | (((unsigned)(par >> page) & 1u) << 13); }
    DEVI unsigned pg0(int page) const { return pg(page) ^ ((unsigned)c << 13); }
    DEVI unsigned seg(int page, int s) const { return pg(page + (s >> 4)) + (unsigned)(s & 15) * 131072u; }
    DEVI unsigned seg0(int page, int s) const { return seg(page, s) ^ ((unsigned)c << 13); }
    DEVI unsigned segx(int page, int s) const { return seg(page, s) ^ 8192u; }
    DEVI unsigned hseg(int s) const { return pg(PG_H + (s >> 3)) + (unsigned)(s & 7) * 262144u; }
    DEVI unsigned yx(int sg, int wq) const { return pg(PG_YX + (sg % YXR) * 2 + (wq >> 1)) + (unsigned)(wq & 1) * 1048576u; }
    DEVI unsigned ym(int s) const { return pg(PG_YM + ((s >> 1) & 1)) + (unsigned)(s & 1) * EXP(327680); }
    DEVI unsigned memf(int s) const { return pg(PG_MEMF) + (unsigned)s * 32768u; }
    DEVI char* cnt(int type, int s) const { return ws + (p0 + CNT(type, s)); }
    DEVI char* cnt0(int type, int s) const { return ws + ((p0 ^ ((unsigned)c << 13)) + CNT(type, s)); }
};

DEVI unsigned cvtpk(float a, float b) { return __builtin_bit_cast(unsigned, __builtin_convertvector((f32x2){a, b}, bf16x2_t)); }
DEVI unsigned packs(float a, float b) { return min(cvtpk(a, b), 0xFFFFFFFEu); }
DEVI float blo(unsigned x) { return __uint_as_float(x << 16); }
DEVI float bhi(unsigned x) { return __uint_as_float(x & 0xFFFF0000u); }
DEVI float bfr(float f) { return __uint_as_float(cvtpk(0.f, f)); }
DEVI u32x4 ld128(__amdgpu_buffer_rsrc_t rs, unsigned v, unsigned s) { return __builtin_amdgcn_raw_buffer_load_b128(rs, (int)v, (int)s, AUX_SC1); }
DEVI u32x2 ld64(__amdgpu_buffer_rsrc_t rs, unsigned v, unsigned s) { return __builtin_amdgcn_raw_buffer_load_b64(rs, (int)v, (int)s, AUX_SC1); }
DEVI unsigned ld32(__amdgpu_buffer_rsrc_t rs, unsigned v, unsigned s) { return __builtin_amdgcn_raw_buffer_load_b32(rs, (int)v, (int)s, AUX_SC1); }
DEVI void st128(__amdgpu_buffer_rsrc_t rs, unsigned v, unsigned s, u32x4 x) {
    __builtin_amdgcn_raw_buffer_store_b128(x, rs, (int)v, (int)s, AUX_SC1);
    asm volatile("s_nop 1" :: "v"(x) : "memory");
}
DEVI void st64(__amdgpu_buffer_rsrc_t rs, unsigned v, unsigned s, u32x2 x) { __builtin_amdgcn_raw_buffer_store_b64(x, rs, (int)v, (int)s, AUX_SC1); }
DEVI void st32(__amdgpu_buffer_rsrc_t rs, unsigned v, unsigned s, unsigned x) { __builtin_amdgcn_raw_buffer_store_b32(x, rs, (int)v, (int)s, AUX_SC1); }
#define RAWBAR() { __builtin_amdgcn_s_waitcnt(0xc07f); __builtin_amdgcn_s_barrier(); }
#define BARL(n) { asm volatile("" ::: "memory"); __builtin_amdgcn_s_waitcnt(0xc07f | ((n) << 8)); __builtin_amdgcn_s_barrier(); asm volatile("" ::: "memory"); }
#define DRAIN() asm volatile("s_waitcnt vmcnt(0)" ::: "memory")
#define SCHED0() __builtin_amdgcn_sched_barrier(0)
#define OOB 0x80000000u
DEVI int opq(int x) { asm volatile("" : "+v"(x)); return x; }
#define LANE const int l = opq(k.l), g = l >> 4, n = l & 15; (void)g; (void)n;
DEVI unsigned umax3(unsigned a, unsigned b, unsigned c) { return max(max(a, b), c); }
DEVI unsigned umax4(u32x4 v) { return max(umax3(v[0], v[1], v[2]), v[3]); }
DEVI unsigned rclk() { return (unsigned)__builtin_amdgcn_s_memrealtime(); }
DEVI unsigned long long rt_async() { unsigned long long t; asm volatile("s_memrealtime %0" : "=s"(t)); return t; }
DEVI void lgkm0(unsigned long long& a) { asm volatile("s_waitcnt lgkmcnt(0)" : "+s"(a) :: "memory"); }
DEVI unsigned spack(unsigned lo, unsigned hi) { unsigned d; asm volatile("s_pack_ll_b32_b16 %0, %1, %2" : "=s"(d) : "s"(lo), "s"(hi)); return d; }
DEVI int lane() { int l; asm volatile("v_mbcnt_lo_u32_b32 %0, -1, 0\n\tv_mbcnt_hi_u32_b32 %0, -1, %0" : "=v"(l)); __builtin_assume(l >= 0 && l < 64); return l; }
DEVI void sleep_until(unsigned target) { while ((int)(rclk() - target) < 0) __builtin_amdgcn_s_sleep(4); }
DEVI float xor16max(float x) { auto r = __builtin_amdgcn_permlane16_swap(__float_as_uint(x), __float_as_uint(x), false, false); return __builtin_fmaxf(__uint_as_float(r[0]), __uint_as_float(r[1])); }
DEVI float xor32max(float x) { auto r = __builtin_amdgcn_permlane32_swap(__float_as_uint(x), __float_as_uint(x), false, false); return __builtin_fmaxf(__uint_as_float(r[0]), __uint_as_float(r[1])); }
DEVI float xor16add(float x) { auto r = __builtin_amdgcn_permlane16_swap(__float_as_uint(x), __float_as_uint(x), false, false); return __uint_as_float(r[0]) + __uint_as_float(r[1]); }
DEVI float xor32add(float x) { auto r = __builtin_amdgcn_permlane32_swap(__float_as_uint(x), __float_as_uint(x), false, false); return __uint_as_float(r[0]) + __uint_as_float(r[1]); }
DEVI float fold32(float a, float b) { auto r = __builtin_amdgcn_permlane32_swap(__float_as_uint(a), __float_as_uint(b), false, false); return __uint_as_float(r[0]) + __uint_as_float(r[1]); }
#define ROR16(x, n) __builtin_bit_cast(float, __builtin_amdgcn_update_dpp(0, __builtin_bit_cast(int, (x)), 0x120 + (n), 0xf, 0xf, false))
DEVI float row16sum(float x) { x += ROR16(x, 8); x += ROR16(x, 4); x += ROR16(x, 2); x += ROR16(x, 1); return x; }
DEVI unsigned z0() { unsigned z; asm volatile("v_mov_b32 %0, 0" : "=v"(z)); return z; }
DEVI void cnt_add(char* p) { __hip_atomic_fetch_add((GAS unsigned*)(p + z0()), z0() + 1u, __ATOMIC_RELAXED, __HIP_MEMORY_SCOPE_AGENT); }
DEVI unsigned cnt_get(char* p) { return __hip_atomic_load((GAS unsigned*)(p + z0()), __ATOMIC_RELAXED, __HIP_MEMORY_SCOPE_AGENT) + 1u; }
DEVI bool spin_fail(const Ctx& k, unsigned spins) {
    if (spins > SPIN_CAP || ((spins & 255u) == 255u && __builtin_amdgcn_readfirstlane(cnt_get(k.cm + CM_TMO)) != 0)) { cnt_add(k.cm + CM_TMO); return true; }
    return false;
}
DEVI __amdgpu_buffer_rsrc_t cm_rs(const Ctx& k) { return __builtin_amdgcn_make_buffer_rsrc((void*)k.cm, 0, CM_BYTES, 0x00020000); }
DEVI void cm_st(const Ctx& k, unsigned v, unsigned off, unsigned lo, unsigned hi) { asm volatile("buffer_store_dwordx2 %0, %1, %2, %3 offen sc1" :: "v"((u32x2){lo, hi}), "v"(v), "s"(cm_rs(k)), "s"(off) : "memory"); }
DEVI u32x2 cm_ld(const Ctx& k, unsigned off) { return ld64(cm_rs(k), off, 0); }
DEVI void line_st(const Ctx& k, unsigned off, unsigned lo, unsigned hi) { cm_st(k, (lane() & 15) * 8 + (k.w ? OOB : 0u), off + k.role * 128, lo, hi); }
DEVI void hello(const Ctx& k, unsigned vote) { line_st(k, CM_IN, vote, OOB); }
DEVI u32x2 go_ask(const Ctx& k) { u32x2 d; asm volatile("buffer_load_dwordx2 %0, off, %1, %2 sc1" : "=v"(d) : "s"(cm_rs(k)), "s"(CM_GO + k.role * 128) : "memory"); return d; }
DEVI void go_wait(Ctx& k, unsigned vote, u32x2 v = {0u, 0u}) {
_Pragma("unroll 1")
    for (unsigned spins = 0; __builtin_amdgcn_readfirstlane(v[1]) >= 0 && spins < SPIN_CAP; ++spins) {
        if (spins) { hello(k, vote); __builtin_amdgcn_s_sleep(4); }
        asm volatile("buffer_load_dwordx2 %0, off, %1, %2 sc1\n\ts_waitcnt vmcnt(0)" : "=v"(v) : "s"(cm_rs(k)), "s"(CM_GO + k.role * 128) : "memory");
    }
    k.par = ((unsigned long long)(unsigned)__builtin_amdgcn_readfirstlane(v[1]) << 32 | (unsigned)__builtin_amdgcn_readfirstlane(v[0])) ^ (k.c ? ~0ull : 0ull);
    k.p0 = k.pg(0);
    __builtin_amdgcn_fence(__ATOMIC_ACQUIRE, "wavefront");
}
DEVI void cnt_wait(const Ctx& k, char* c, unsigned need) {
_Pragma("unroll 1")
    for (unsigned spins = 0; (unsigned)__builtin_amdgcn_readfirstlane(cnt_get(c)) < need; ++spins) {
        __builtin_amdgcn_s_sleep(PSLEEP);
        if (spin_fail(k, spins)) break;
    }
    __builtin_amdgcn_fence(__ATOMIC_ACQUIRE, "wavefront");
}
DEVI unsigned xn_get(const Ctx& k, unsigned l16, int s) { return ld32(k.rs, opq(l16) & 0x380u, k.p0 + CNT(C_XN, 0)); }
DEVI bool xn_has(const Ctx& k, unsigned v, int s) { return __all((int)v >= s - (k.c ? 0 : 8)); }
DEVI void xn_wait(const Ctx& k, unsigned l16, int s) {
_Pragma("unroll 1")
    for (unsigned spins = 0; !xn_has(k, xn_get(k, l16, s), s); ++spins) {
        asm volatile("" ::: "memory");
        __builtin_amdgcn_s_sleep(2);
        if (spin_fail(k, spins)) break;
    }
    __builtin_amdgcn_fence(__ATOMIC_ACQUIRE, "wavefront");
}
constexpr unsigned L_PW = 150 * 1024;
DEVI void prog_init(const Ctx& k) { *(volatile LAS unsigned*)(size_t)(L_PW + k.l * 4) = ~0u; RAWBAR() }
template <int N> DEVI void prog_wait(const Ctx& k, unsigned a, int s) {
    unsigned n, t;
    asm volatile("s_mov_b32 m0, %8\n\ts_mov_b32 %0, 0\n\ts_branch 2f\n"
        "1:\n\ts_cmp_lg_u32 %6, 0\n\ts_cbranch_scc1 3f\n\tbuffer_load_dword %2, %3, %4 offen sc1 lds\n"
        "3:\n\ts_sleep %9\n\ts_add_u32 %0, %0, 1\n\ts_and_b32 vcc_lo, %0, 0x7ff\n\ts_cbranch_scc1 2f\n\t"
        "v_mov_b32 %1, 0\n\ts_bitcmp1_b32 %0, 23\n\ts_cbranch_scc0 5f\n\tglobal_store_dword %1, %1, %10 sc1\n"
        "5:\n\tglobal_load_dword %1, %1, %10 sc1\n\ts_waitcnt vmcnt(0)\n\tv_cmp_ne_u32 vcc, -1, %1\n\ts_cbranch_vccnz 4f\n"
        "2:\n\tds_read_b32 %1, %7\n\ts_waitcnt lgkmcnt(0)\n\tv_cmp_gt_i32 vcc, %5, %1\n\ts_cbranch_vccnz 1b\n4:"
        : "=&s"(n), "=&v"(t) : "v"(min(k.l, N - 1) * 128), "s"(k.rs), "s"(a), "s"(s), "s"(k.w), "v"(L_PW + k.l * 4), "n"(L_PW), "n"(PWS), "s"(k.cm + CM_TMO) : "memory", "vcc", "scc");
}
template <int OFF> DEVI void dma1(const void* sbase, unsigned voff, unsigned lds) {
    asm volatile("s_add_i32 m0, %2, %3\n\ts_nop 0\n\tglobal_load_lds_dwordx4 %0, %1" NTA :: "v"(voff), "s"(sbase), "s"(lds), "n"(OFF) : "memory", "scc");
}
DEVI void set_m0(unsigned lds) { asm volatile("s_mov_b32 m0, %0" :: "s"(lds) : "memory"); }
template <int OFF> DEVI void dmab(__amdgpu_buffer_rsrc_t rs, unsigned voff, unsigned soff) {
    asm volatile("buffer_load_dwordx4 %0, %1, %2 offen offset:%3 sc1 lds" :: "v"(voff), "s"(rs), "s"(soff), "n"(OFF) : "memory");
}
#define PIN(x) asm volatile("" : "+v"(x))
#define PINS(x) asm volatile("" : "+s"(x))
#define LEAD()
#define TOUCH(x) asm volatile("" :: "v"(x))
DEVI float csel(float a, float b, unsigned long long m) { float d; asm volatile("v_cndmask_b32_e64 %0, %1, %2, %3" : "=v"(d) : "v"(a), "v"(b), "s"(m)); return d; }
#define LO8(x) __builtin_shufflevector(x, x, 0, 1, 2, 3, 4, 5, 6, 7)
#define HI8(x) __builtin_shufflevector(x, x, 8, 9, 10, 11, 12, 13, 14, 15)
#define CAT8(a, b) __builtin_shufflevector(a, b, 0, 1, 2, 3, 4, 5, 6, 7, 8, 9, 10, 11, 12, 13, 14, 15)
template <bool AG> DEVI void smf(f32x4& c, const bf16x8& a, const bf16x16& w, unsigned ix) {
    if constexpr (AG) asm volatile("v_smfmac_f32_16x16x64_bf16 %0, %1, %2, %3" : "+v"(c) : "v"(a), "a"(w), "v"(ix));
    else asm volatile("v_smfmac_f32_16x16x64_bf16 %0, %1, %2, %3" : "+v"(c) : "v"(a), "v"(w), "v"(ix));
}
#define WTAB(X) X(0, "{a[0:7]}", "{a[0:3]}", "{a[4:7]}") X(1, "{a[8:15]}", "{a[8:11]}", "{a[12:15]}") X(2, "{a[16:23]}", "{a[16:19]}", "{a[20:23]}") X(3, "{a[24:31]}", "{a[24:27]}", "{a[28:31]}") X(4, "{a[32:39]}", "{a[32:35]}", "{a[36:39]}") X(5, "{a[40:47]}", "{a[40:43]}", "{a[44:47]}") X(6, "{a[48:55]}", "{a[48:51]}", "{a[52:55]}") X(7, "{a[56:63]}", "{a[56:59]}", "{a[60:63]}") X(8, "{a[64:71]}", "{a[64:67]}", "{a[68:71]}") X(9, "{a[72:79]}", "{a[72:75]}", "{a[76:79]}") X(10, "{a[80:87]}", "{a[80:83]}", "{a[84:87]}") X(11, "{a[88:95]}", "{a[88:91]}", "{a[92:95]}") X(12, "{a[96:103]}", "{a[96:99]}", "{a[100:103]}") X(13, "{a[104:111]}", "{a[104:107]}", "{a[108:111]}") X(14, "{a[112:119]}", "{a[112:115]}", "{a[116:119]}") X(15, "{a[120:127]}", "{a[120:123]}", "{a[124:127]}") X(16, "{a[128:135]}", "{a[128:131]}", "{a[132:135]}") X(17, "{a[136:143]}", "{a[136:139]}", "{a[140:143]}") X(18, "{a[144:151]}", "{a[144:147]}", "{a[148:151]}") X(19, "{a[152:159]}", "{a[152:155]}", "{a[156:159]}") X(20, "{a[160:167]}", "{a[160:163]}", "{a[164:167]}") X(21, "{a[168:175]}", "{a[168:171]}", "{a[172:175]}") X(22, "{a[176:183]}", "{a[176:179]}", "{a[180:183]}") X(23, "{a[184:191]}", "{a[184:187]}", "{a[188:191]}") X(24, "{a[192:199]}", "{a[192:195]}", "{a[196:199]}") X(25, "{a[200:207]}", "{a[200:203]}", "{a[204:207]}") X(26, "{a[208:215]}", "{a[208:211]}", "{a[212:215]}") X(27, "{a[216:223]}", "{a[216:219]}", "{a[220:223]}") X(28, "{a[224:231]}", "{a[224:227]}", "{a[228:231]}") X(29, "{a[232:239]}", "{a[232:235]}", "{a[236:239]}") X(30, "{a[240:247]}", "{a[240:243]}", "{a[244:247]}") X(31, "{a[248:255]}", "{a[248:251]}", "{a[252:255]}") X(32, "{v[96:103]}", "{v[96:99]}", "{v[100:103]}") X(33, "{v[104:111]}", "{v[104:107]}", "{v[108:111]}") X(34, "{v[112:119]}", "{v[112:115]}", "{v[116:119]}") X(35, "{v[120:127]}", "{v[120:123]}", "{v[124:127]}") X(36, "{v[128:135]}", "{v[128:131]}", "{v[132:135]}") X(37, "{v[136:143]}", "{v[136:139]}", "{v[140:143]}") X(38, "{v[144:151]}", "{v[144:147]}", "{v[148:151]}") X(39, "{v[152:159]}", "{v[152:155]}", "{v[156:159]}") X(40, "{v[160:167]}", "{v[160:163]}", "{v[164:167]}") X(41, "{v[168:175]}", "{v[168:171]}", "{v[172:175]}") X(42, "{v[176:183]}", "{v[176:179]}", "{v[180:183]}") X(43, "{v[184:191]}", "{v[184:187]}", "{v[188:191]}") X(44, "{v[192:199]}", "{v[192:195]}", "{v[196:199]}") X(45, "{v[200:207]}", "{v[200:203]}", "{v[204:207]}") X(46, "{v[208:215]}", "{v[208:211]}", "{v[212:215]}") X(47, "{v[216:223]}", "{v[216:219]}", "{v[220:223]}") X(48, "{v[224:231]}", "{v[224:227]}", "{v[228:231]}") X(49, "{v[232:239]}", "{v[232:235]}", "{v[236:239]}") X(50, "{v[240:247]}", "{v[240:243]}", "{v[244:247]}") X(51, "{v[248:255]}", "{v[248:251]}", "{v[252:255]}")
template <int ID> DEVI void smfw(f32x4& c, const bf16x8& a, const bf16x16& w, unsigned ix) {
#define X(i, P, L, H) if constexpr (ID == i) asm volatile("v_smfmac_f32_16x16x64_bf16 %0, %1, %2, %3" : "+v"(c) : "v"(a), P(w), "v"(ix));
    WTAB(X)
#undef X
}
template <int ID, int HF, bool WB, bool FIRST> DEVI void mfw(f32x4& c, const bf16x16& w, const bf16x8& x) {
#define Y(R, W8) { if constexpr (FIRST && WB) asm volatile("v_mfma_f32_16x16x32_bf16 %0, %2, %1, 0" : "=&v"(c) : R(W8), "v"(x)); \
    else if constexpr (FIRST) asm volatile("v_mfma_f32_16x16x32_bf16 %0, %1, %2, 0" : "=&v"(c) : R(W8), "v"(x)); \
    else if constexpr (WB) asm volatile("v_mfma_f32_16x16x32_bf16 %0, %2, %1, %0" : "+v"(c) : R(W8), "v"(x)); \
    else asm volatile("v_mfma_f32_16x16x32_bf16 %0, %1, %2, %0" : "+v"(c) : R(W8), "v"(x)); }
#define X(i, P, L, H) if constexpr (ID == i) { if constexpr (HF) Y(H, HI8(w)) else Y(L, LO8(w)) }
    WTAB(X)
#undef X
#undef Y
}
template <bool AG, bool FIRST> DEVI void mfa(f32x4& c, const bf16x8& w, const bf16x8& x) {
    if constexpr (AG && FIRST) asm volatile("v_mfma_f32_16x16x32_bf16 %0, %1, %2, 0" : "=&v"(c) : "a"(w), "v"(x));
    else if constexpr (AG) asm volatile("v_mfma_f32_16x16x32_bf16 %0, %1, %2, %0" : "+v"(c) : "a"(w), "v"(x));
    else if constexpr (FIRST) asm volatile("v_mfma_f32_16x16x32_bf16 %0, %1, %2, 0" : "=&v"(c) : "v"(w), "v"(x));
    else asm volatile("v_mfma_f32_16x16x32_bf16 %0, %1, %2, %0" : "+v"(c) : "v"(w), "v"(x));
}
template <bool AG, bool FIRST> DEVI void mfb(f32x4& c, const bf16x8& x, const bf16x8& w) {
    if constexpr (AG && FIRST) asm volatile("v_mfma_f32_16x16x32_bf16 %0, %1, %2, 0" : "=&v"(c) : "v"(x), "a"(w));
    else if constexpr (AG) asm volatile("v_mfma_f32_16x16x32_bf16 %0, %1, %2, %0" : "+v"(c) : "v"(x), "a"(w));
    else if constexpr (FIRST) asm volatile("v_mfma_f32_16x16x32_bf16 %0, %1, %2, 0" : "=&v"(c) : "v"(x), "v"(w));
    else asm volatile("v_mfma_f32_16x16x32_bf16 %0, %1, %2, %0" : "+v"(c) : "v"(x), "v"(w));
}
DEVI f32x4 mf(const bf16x8& a, const bf16x8& b, const f32x4& c) { return __builtin_amdgcn_mfma_f32_16x16x32_bf16(__builtin_bit_cast(hbf8, a), __builtin_bit_cast(hbf8, b), c, 0, 0, 0); }
template <int N, class F> DEVI void sfor(F f) { [&]<int... I>(std::integer_sequence<int, I...>) { (f(std::integral_constant<int, I>{}), ...); }(std::make_integer_sequence<int, N>{}); }
DEVI bf16x8 lds128(const char* p) { return *(const bf16x8*)p; }
DEVI bf16x4 ldstr(const char* p) { return __builtin_amdgcn_ds_read_tr16_b64_v4i16((LAS bf16x4*)p); }
DEVI bf16x8 ldstr2(const char* p, int d) { bf16x4 a = ldstr(p), b = ldstr(p + d); return __builtin_shufflevector(a, b, 0, 1, 2, 3, 4, 5, 6, 7); }

#define STAMP(s, k)
#define QSTAMP(f, i)
#define STOP(s)
#define PFLUSH(s)
#define PROF_DECL
#define RSTAMP(i)
#define XSTAMP(i)
#define PCOUNT(i)
#define PROF_OUT

#define HSTAMP STAMP
#define HFLUSH PFLUSH
#define HTRACE(s)
#define TROUT
#define TRHINT
#define TPQ(n)
#define TPV(n)
#define AQ(m, i)
#define AC3(i, j, k) { }
#define AST(i)
#define AAC(i)
#define ACCM 0
#define HSTOP(m) if constexpr ((ACCM != 0) == m) { STOP(0) }
#define PC8(i, x)
#define PROF_ONLY(x)
#define PROF_S(x)
template <int RB, bool STD> DEVI int swz(int row) { return RB == 384 ? ((row >> 1) & 1) | ((row >> 2) & 2) : RB == 128 ? (row >> 1) & 3 : STD ? (row & 3) | ((row >> 1) & 4) : row & 7; }
template <int RB, bool STD> DEVI unsigned wsrc(unsigned id, int ld) { const unsigned row = id / (RB / 16), c = id % (RB / 16); return row * ld + ((c ^ (swz<RB, STD>(row) << 1)) << 4); }
template <int RB, bool STD> DEVI int wp_adr(int T, int l) { const int row = (STD ? 8 : 4) * (l >> 4) + ((l >> 2) & 3); return row * RB + ((T ^ swz<RB, STD>(row)) << 5) + (l & 3) * 8; }
template <int RB, bool STD> DEVI bf16x8 wp_read(const char* blk, int T, int l) { return ldstr2(blk + wp_adr<RB, STD>(T, l), (STD ? 4 : 16) * RB); }
template <int NU, class CNT, class STEP> DEVI void cstream(CNT cnt, STEP step) {
    sfor<NU>([&](auto V) { constexpr int v = decltype(V)::value;
        asm volatile("s_waitcnt vmcnt(%0) lgkmcnt(0)\n\ts_barrier" :: "n"(decltype(cnt(V))::value * (v ? 1 : 2)) : "memory");
        step(V);
    });
}
template <int NU, int NI, int D, class ISSUE, class READ> DEVI void wstream(char* ring, ISSUE issue, READ read) {
    const unsigned base = (unsigned)(size_t)(LAS char*)ring;
    sfor<D>([&](auto U) { constexpr int u = decltype(U)::value; if constexpr (u < NU) issue(U, base); });
    sfor<NU>([&](auto U) {
        constexpr int u = decltype(U)::value, left = (NU - u < D ? NU - u : D) - 1;
        asm volatile("s_waitcnt vmcnt(%0)" :: "n"(left * NI) : "memory");
        read(U, ring + (u % D) * NI * 1024);
        __builtin_amdgcn_s_waitcnt(0xc07f);
        asm volatile("" ::: "memory");
        if constexpr (u + D < NU) issue(std::integral_constant<int, u + D>{}, base);
    });
}

constexpr int L_XN = 0;
constexpr int L_K = 40960;
constexpr int L_MEM = L_K + 12288;
constexpr int L_QM = L_MEM + 10240;
constexpr int L_Q = L_QM + 12288;
constexpr int L_W = 94208;
#define SLOT(i) ((i) % 3 == 0 ? 45056 : (i) % 3 == 1 ? 69632 : L_W + 40960)
#define CMIN(a, b) ((a) < (b) ? (a) : (b))
#define IC(x) std::integral_constant<int, (x)>{}
#define CV(X) decltype(X)::value
DEVI bf16x8 lr128(unsigned a) { return *(const LAS bf16x8*)(size_t)a; }
DEVI bf16x4 lrtr(unsigned a) { return __builtin_amdgcn_ds_read_tr16_b64_v4i16((LAS bf16x4*)(size_t)a); }
#define CAT4(a, b) __builtin_shufflevector(a, b, 0, 1, 2, 3, 4, 5, 6, 7)
DEVI void lw128(unsigned a, u32x4 v) { *(LAS u32x4*)(size_t)a = v; }
DEVI void lw64(unsigned a, u32x2 v) { *(LAS u32x2*)(size_t)a = v; }
DEVI void lw32(unsigned a, unsigned v) { *(LAS unsigned*)(size_t)a = v; }
template <int N> DEVI void bar_lgkm() { asm volatile("s_waitcnt lgkmcnt(%0)\n\ts_barrier" :: "n"(N) : "memory"); }
DEVI void atom1(__amdgpu_buffer_rsrc_t rs, unsigned voff, unsigned soff) {
    unsigned one; asm volatile("v_mov_b32 %0, 1\n\tbuffer_atomic_add %0, %1, %2, %3 offen" : "=&v"(one) : "v"(voff), "s"(rs), "s"(soff) : "memory");
}
DEVI unsigned vadd(unsigned v, unsigned sc) { unsigned d; asm volatile("v_add_u32 %0, %1, %2" : "=v"(d) : "s"(sc), "v"(v)); return d; }
DEVI float vzero() { float z; asm volatile("v_mov_b32 %0, 0" : "=v"(z)); return z; }
template <int S> DEVI unsigned slot_adr(unsigned a) { if constexpr (S % 3 == 2) return vadd(a, SLOT(2) - SLOT(0)); else return a + (SLOT(S) - SLOT(0)); }
template <int B, int I> DEVI void xn_op(const Ctx& k, unsigned v, unsigned src, unsigned dst) {
    if constexpr (I == 0) set_m0(dst + B * 8192);
    else if constexpr (I == 1) dmab<0>(k.rs, v, src + B * 16384);
    else dmab<1024>(k.rs, v, src + B * 16384);
}
DEVI void xn_all(const Ctx& k, unsigned v, unsigned src, unsigned dst) {
    sfor<5>([&](auto B) { xn_op<CV(B), 0>(k, v, src, dst); asm volatile("s_nop 0"); xn_op<CV(B), 1>(k, v, src, dst); xn_op<CV(B), 2>(k, v, src, dst); });
}
struct Xn {
    unsigned src, dst, sg, need, t, sb, se, l16, c, off;
    unsigned long long flip, m;
    DEVI void init(const Ctx& k) {
        src = k.seg(PG_XNA, 0) + k.w * 2048; dst = L_XN + k.w * 2048; sg = 0; off = ~0u;
        const unsigned q = (unsigned)(k.par >> PG_XNA), d = q ^ (q >> 1);
        flip = ((unsigned long long)(d & 1) << 16) | ((unsigned long long)(d & 2) << 31) | ((unsigned long long)(d & 4) << 46);
    }
    template <int I> DEVI void next(const Ctx& k, unsigned l16) {
        if constexpr (I == 0) { sg += 1; src += 131072; PINS(sg); PINS(src); }
        if constexpr (I == 1) asm volatile("s_bitcmp1_b64 %1, %2\n\ts_cselect_b32 %0, 0x2000, 0" : "=s"(t) : "s"(flip), "s"(sg) : "scc");
        if constexpr (I == 2) { src ^= t; PINS(src); }
        if constexpr (I == 3) asm volatile("v_and_b32 %0, 0x380, %1" : "=v"(c) : "v"(l16));
        if constexpr (I == 4) c = ld32(k.rs, c, k.p0 + CNT(C_XN, 0));
        if constexpr (I == 5) { need = sg - (k.c ? 0u : 8u); PINS(need); }
        if constexpr (I == 6) { need = sg < 64u ? need : 0x7FFFFFFFu; PINS(need); }
        if constexpr (I == 7) asm volatile("v_cmp_gt_i32_e64 %0, %1, %2" : "=s"(m) : "s"(need), "v"(c) : "memory");
        if constexpr (I == 8) asm volatile("s_cmp_lg_u64 %2, 0\n\ts_cselect_b32 %0, -1, 0\n\ts_cselect_b32 %1, 0x200000, %3" : "=&s"(off), "=s"(se) : "s"(m), "s"(src) : "scc");
        if constexpr (I == 13) set_m0(L_MEM);
        if constexpr (I == 14) dmab<0>(k.rs, c, k.p0 + CNT(C_XN, 0));
        if constexpr (I == 15) c = *(volatile LAS unsigned*)(size_t)(l16 + L_MEM);
        if constexpr (I == 11) { need = sg < 64u ? need : 0x80000000u; PINS(need); }
        if constexpr (I == 12) {
            if (__builtin_expect(m != 0, 0)) {
_Pragma("unroll 1")
                for (unsigned spins = 0; __any((int)*(volatile LAS unsigned*)(size_t)(l16 + L_MEM) < (int)need) && !spin_fail(k, spins >> 2); ++spins) { next<3>(k, l16); __builtin_amdgcn_s_sleep(1); next<14>(k, l16); }
            }
            se = src; }
        if constexpr (I == 10) asm volatile("s_cmp_lg_u32 %1, 64\n\ts_cselect_b32 %0, %0, 0" : "+s"(off) : "s"(sg) : "scc");
    }
    template <int B, int I> DEVI void op(const Ctx& k) {
        if constexpr (I == 0) { set_m0(dst + B * 8192); sb = se + B * 16384; PINS(sb); }
        else dmab<(I - 1) * 1024>(k.rs, l16, sb);
    }
    template <int NB> DEVI void late(const Ctx& k, unsigned l16) {
        xn_wait(k, l16, sg);
        se = src;
        sfor<NB>([&](auto B) { op<CV(B), 0>(k); asm volatile("s_nop 0"); op<CV(B), 1>(k); op<CV(B), 2>(k); });
        DRAIN();
    }
};
template <int OFF> DEVI u32x4 ald128(__amdgpu_buffer_rsrc_t rs, unsigned v, unsigned s) { u32x4 d; asm volatile("buffer_load_dwordx4 %0, %1, %2, %3 offen offset:%4 sc1" : "=v"(d) : "v"(v), "s"(rs), "s"(s), "n"(OFF) : "memory"); return d; }
#define MADR(b) (L_MEM + ((b) < 6 ? ((b) - 1) * 2048 : ((b) - 6) * 2048 + 3072))
struct Gather {
    u32x4 p0, p1, v0, v1;
    unsigned mx, a, aw, b, base, wo, dst;
    unsigned long long bad;
    DEVI void init(const Ctx& k) { base = k.memf(0); wo = k.w * 1024; dst = L_MEM + k.w * 2048; }
    template <int I> DEVI void op(const Ctx& k, unsigned l16, int s) {
        if constexpr (I == 0) { a = ((unsigned)s << 15) & 0x1F8000u; PINS(a); }
        if constexpr (I == 1) { a += base; aw = a + wo; b = aw + 3072 + (k.w >> 1) * 8192; PINS(a); PINS(aw); PINS(b); }
        if constexpr (I == 2) set_m0(dst);
        if constexpr (I == 3) v0 = ald128<2048>(k.rs, l16, aw);
        if constexpr (I == 4) v1 = ald128<3072>(k.rs, l16, b);
        if constexpr (I == 5) dmab<2048>(k.rs, l16, aw);
        if constexpr (I == 6) dmab<3072>(k.rs, l16, b);
        if constexpr (I == 7) p0 = ald128<0>(k.rs, l16, a);
        if constexpr (I == 8) p1 = ald128<1024>(k.rs, l16, a);
    }
    template <int N> DEVI void wait() { asm volatile("s_waitcnt vmcnt(%4)" : "+v"(p0), "+v"(p1), "+v"(v0), "+v"(v1) : "n"(N) : "memory"); }
    template <int I> DEVI void fin(unsigned other) {
#define MX3 "v_max3_u32 %0, %0, %3, %4\n\tv_max3_u32 %0, %0, %5, %6"
        if constexpr (I == 0) asm volatile("v_max3_u32 %0, %7, %1, %2\n\t" MX3 : "=&v"(mx) : "v"(v0[0]), "v"(v0[1]), "v"(v0[2]), "v"(v0[3]), "v"(v1[0]), "v"(v1[1]), "s"(other));
        if constexpr (I == 1) asm volatile("v_max3_u32 %0, %0, %1, %2\n\t" MX3 : "+v"(mx) : "v"(v1[2]), "v"(v1[3]), "v"(p0[0]), "v"(p0[1]), "v"(p0[2]), "v"(p0[3]));
        if constexpr (I == 2) asm volatile("v_max3_u32 %1, %1, %2, %3\n\tv_max3_u32 %1, %1, %4, %5\n\tv_cmp_eq_u32_e64 %0, -1, %1" : "=s"(bad) : "v"(mx), "v"(p1[0]), "v"(p1[1]), "v"(p1[2]), "v"(p1[3]));
    }
    DEVI void also(unsigned other) { asm volatile("v_mov_b32 %0, %1" : "=v"(mx) : "s"(other)); }
    DEVI void all() {
        asm volatile("s_waitcnt vmcnt(0)\n\tv_max3_u32 %1, %1, %2, %3\n\tv_max3_u32 %1, %1, %4, %5\n\tv_max3_u32 %1, %1, %6, %7\n\tv_max3_u32 %1, %1, %8, %9\n\tv_max3_u32 %1, %1, %10, %11\n\tv_max3_u32 %1, %1, %12, %13\n\t"
            "v_max3_u32 %1, %1, %14, %15\n\tv_max3_u32 %1, %1, %16, %17\n\tv_cmp_eq_u32_e64 %0, -1, %1" : "=s"(bad) : "v"(mx), "v"(v0[0]), "v"(v0[1]), "v"(v0[2]), "v"(v0[3]), "v"(v1[0]), "v"(v1[1]), "v"(v1[2]), "v"(v1[3]),
            "v"(p0[0]), "v"(p0[1]), "v"(p0[2]), "v"(p0[3]), "v"(p1[0]), "v"(p1[1]), "v"(p1[2]), "v"(p1[3]) : "memory");
    }
    DEVI void check() { also(0); all(); asm volatile("s_nop 3"); }
    template <int I> DEVI void early(unsigned other) {
        if constexpr (I == 0) asm volatile("s_waitcnt vmcnt(5)\n\tv_max3_u32 %0, %5, %1, %2\n\tv_max3_u32 %0, %0, %3, %4" : "=&v"(mx) : "v"(v0[0]), "v"(v0[1]), "v"(v0[2]), "v"(v0[3]), "s"(other) : "memory");
        if constexpr (I == 1) __builtin_amdgcn_s_setprio(0);
        if constexpr (I == 2) asm volatile("s_waitcnt vmcnt(4)\n\tv_max3_u32 %0, %0, %1, %2\n\tv_max3_u32 %0, %0, %3, %4" : "+v"(mx) : "v"(v1[0]), "v"(v1[1]), "v"(v1[2]), "v"(v1[3]) : "memory");
    }
    DEVI void rest() {
        asm volatile("s_waitcnt vmcnt(1)\n\tv_max3_u32 %1, %1, %2, %3\n\tv_max3_u32 %1, %1, %4, %5\n\ts_waitcnt vmcnt(0)\n\tv_max3_u32 %1, %1, %6, %7\n\tv_max3_u32 %1, %1, %8, %9\n\tv_cmp_eq_u32_e64 %0, -1, %1" : "=s"(bad) : "v"(mx),
            "v"(p0[0]), "v"(p0[1]), "v"(p0[2]), "v"(p0[3]), "v"(p1[0]), "v"(p1[1]), "v"(p1[2]), "v"(p1[3]) : "memory");
    }
    unsigned rv, rs, hb; unsigned long long rt;
    template <int I> DEVI void ask(const Ctx& k, unsigned l16) {
        if constexpr (I == 0) asm volatile("v_and_b32 %0, 0xf0, %1\n\tv_lshlrev_b32 %0, 1, %0" : "=v"(rv) : "v"(l16));
        if constexpr (I == 1) asm volatile("buffer_load_dword %0, %0, %1, %2 offen sc1" : "+v"(rv) : "s"(k.rs), "s"(k.p0 + CNT(C_XB, 0)) : "memory");
    }
    template <int I, int N = 4> DEVI void red() {
        if constexpr (I == 0 && N < 0) asm volatile("v_max_u32_dpp %0, %0, %0 quad_perm:[1,0,3,2] row_mask:0xf bank_mask:0xf" : "+v"(rv));
        else if constexpr (I == 0) asm volatile("s_waitcnt vmcnt(%1)\n\tv_max_u32_dpp %0, %0, %0 quad_perm:[1,0,3,2] row_mask:0xf bank_mask:0xf" : "+v"(rv) : "n"(N));
        if constexpr (I == 1) asm volatile("v_max_u32_dpp %0, %0, %0 quad_perm:[2,3,0,1] row_mask:0xf bank_mask:0xf" : "+v"(rv));
        if constexpr (I == 2) asm volatile("v_max_u32_dpp %0, %0, %0 row_ror:4 row_mask:0xf bank_mask:0xf" : "+v"(rv));
        if constexpr (I == 3) asm volatile("v_max_u32_dpp %0, %0, %0 row_ror:8 row_mask:0xf bank_mask:0xf" : "+v"(rv));
        if constexpr (I == 4) asm volatile("v_readfirstlane_b32 %0, %1" : "=s"(rs) : "v"(rv));
    }
    template <int I> DEVI void hold(unsigned step) {
        if constexpr (I == -1) rt = rt_async();
        if constexpr (I == 3) hb = spack((unsigned)rt, step);
        if constexpr (I == 4) asm volatile("s_sub_u32 %0, %1, %0" : "+s"(hb) : "s"(rs) : "scc");
        if constexpr (I == 5) if (__builtin_expect(hb < 128u, 0)) { while ((int)((rclk() - rs) << 16) < 0) { } }
    }
    template <int I> DEVI void at(const Ctx& k, unsigned l16) {
        if constexpr (I == 1) op<2>(k, l16, 0);
        if constexpr (I >= 2 && I < 15 && (I % 5 == 2 || I % 5 == 4)) op<3 + (I / 5) * 2 + (I % 5) / 4>(k, l16, 0);
    }
    DEVI void again(const Ctx& k, unsigned l16) {
        asm volatile("s_mov_b32 m0, %9\n\tbuffer_load_dwordx4 %2, %4, %5, %7 offen offset:2048 sc1\n\tbuffer_load_dwordx4 %3, %4, %5, %8 offen offset:3072 sc1\n\t"
            "buffer_load_dwordx4 %4, %5, %7 offen offset:2048 sc1 lds\n\tbuffer_load_dwordx4 %4, %5, %8 offen offset:3072 sc1 lds\n\t"
            "buffer_load_dwordx4 %0, %4, %5, %6 offen sc1\n\tbuffer_load_dwordx4 %1, %4, %5, %6 offen offset:1024 sc1\n\ts_waitcnt vmcnt(0)"
            : "+v"(p0), "+v"(p1), "+v"(v0), "+v"(v1) : "v"(l16), "s"(k.rs), "s"(a), "s"(aw), "s"(b), "s"(dst) : "memory");
        check();
    }
};
DEVI void clr2(f32x4& c) { f32x2 a, b; asm volatile("v_mov_b64 %0, 0\n\tv_mov_b64 %1, 0" : "=v"(a), "=v"(b)); c = __builtin_shufflevector(a, b, 0, 1, 2, 3); }
#define NOP7_3(a) asm volatile("s_nop 7" : "+v"(a[0]), "+v"(a[1]), "+v"(a[2]))

struct G3 {
    f32x4 a0[3], a1[3];
    bf16x8 x0[20], x1[20], wl[20];
    unsigned l16, lwv;
    template <int KS> DEVI void r0() { x0[KS] = lr128(l16 + L_XN + KS * 2048); }
    template <int KS> DEVI void r1() { x1[KS] = lr128(l16 + L_XN + KS * 2048 + 1024); }
    template <int KS> DEVI void rw() { if constexpr (KS >= 4) wl[KS] = lr128(lwv + (KS - 4) * 4096); }
    template <bool WL, int KS> DEVI void pre() { if constexpr (WL && KS >= 4) asm volatile("" :: "v"(wl[KS]), "v"(x0[KS]), "v"(x1[KS])); else asm volatile("" :: "v"(x0[KS]), "v"(x1[KS])); }
    template <int ID0, bool WL, int ks, int m> DEVI void mm(const bf16x16 (&W)[52]) {
        constexpr int b = ks >> 1, t = m % 3, id = ID0 + t * 10 + b;
        f32x4& c = m < 3 ? a0[t] : a1[t];
        const bf16x8& x = m < 3 ? x0[ks] : x1[ks];
        if constexpr (WL && t == 2 && b >= 2) { if constexpr (ID0 == 30) mfb<false, false>(c, x, wl[ks]); else mfa<false, false>(c, wl[ks], x); }
        else mfw<id % 52, ks & 1, ID0 == 30, ks == 0>(c, W[id % 52], x);
        SCHED0();
    }
    template <int ID0, bool WL, int K0, int K1, int PF, bool H6 = false, class HOOK> DEVI void run(const bf16x16 (&W)[52], HOOK hook) {
        sfor<K1 - K0>([&](auto KK) {
            constexpr int kk = CV(KK), ks = K0 + kk;
            sfor<6>([&](auto M) {
                constexpr int m = CV(M);
                mm<ID0, WL, ks, m>(W);
                if constexpr (m == 5 && ks + 1 < K1) { pre<WL, (ks + 1) % 20>(); SCHED0(); }
                if constexpr (ks + 2 < PF) {
                    if constexpr (m == 0) r0<(ks + 2) % 20>();
                    if constexpr (m == 1) r1<(ks + 2) % 20>();
                    if constexpr (m == 3 && WL) rw<(ks + 2) % 20>();
                }
                if constexpr (H6) hook(IC(kk * 6 + m));
                else { hook(IC(kk * 7 + (m < 5 ? m : 6))); if constexpr (m == 4) hook(IC(kk * 7 + 5)); }
                SCHED0();
            });
        });
    }
    template <int ID0, bool WL, int F0, int F1, int PF, class HOOK> DEVI void go(const bf16x16 (&W)[52], HOOK hook) {
        sfor<F1 - F0>([&](auto FF) {
            constexpr int f = F0 + CV(FF), ks = f / 6, m = f % 6;
            mm<ID0, WL, ks, m>(W);
            if constexpr (m == 5 && f + 1 < F1) { pre<WL, (ks + 1) % 20>(); SCHED0(); }
            if constexpr (ks + 2 < PF) rd<WL, (f + 12) % 120>();
            hook(IC(f));
            SCHED0();
        });
    }
    template <int P> DEVI void home() {
        if constexpr (P == 0) asm volatile("" : "+{v[12:15]}"(a0[0]), "+{v[16:19]}"(a0[1]), "+{v[20:23]}"(a0[2]), "+{v[24:27]}"(a1[0]), "+{v[28:31]}"(a1[1]), "+{v[32:35]}"(a1[2]));
        else asm volatile("" : "+{v[36:39]}"(a0[0]), "+{v[40:43]}"(a0[1]), "+{v[44:47]}"(a0[2]), "+{v[48:51]}"(a1[0]), "+{v[52:55]}"(a1[1]), "+{v[56:59]}"(a1[2]));
    }
    template <bool WL, int F> DEVI void rd() {
        if constexpr (F % 6 == 0) r0<F / 6>();
        if constexpr (F % 6 == 1) r1<F / 6>();
        if constexpr (F % 6 == 3 && WL) rw<F / 6>();
    }
    u32x2 o;
    template <int I, int OFF> DEVI void out4(unsigned d0) {
        if constexpr (I >= 0 && I < 24) {
            constexpr int t = I / 8, rt = (I / 4) & 1, j = I & 1;
            const f32x4& a = rt ? a1[t] : a0[t];
            if constexpr (I % 4 < 2) { o[j] = cvtpk(a[2 * j], a[2 * j + 1]); PIN(o[j]); }
            else lw32(d0 + OFF + rt * 6144 + t * 1024 + j * 4, o[j]);
        }
    }
    template <int I, int OFF> DEVI void out(unsigned d0, unsigned d1) {
        if constexpr (I >= 0 && I < 18) {
            constexpr int t = I / 6, rt = (I / 3) & 1;
            const f32x4& a = rt ? a1[t] : a0[t];
            if constexpr (I % 3 == 0) { o[0] = cvtpk(a[0], a[1]); PIN(o[0]); }
            else if constexpr (I % 3 == 1) { o[1] = cvtpk(a[2], a[3]); PIN(o[1]); }
            else if constexpr (OFF > 0) lw64(d0 + (OFF - 1) + rt * 6144 + t * 1024, o);
            else lw64((t == 1 ? d1 : d0) + OFF + rt * 6144 + (t == 2 ? 1024 : 0), o);
        }
    }
};
#define SPIX(l) (((l) & 2) ? 0xEEEEu : 0x4444u)
template <int I> DEVI void fold_op(f32x4& a, unsigned& o) {
    if constexpr (I == 0) asm volatile("v_add_f32 %0, %0, %1" : "+v"(a[0]) : "v"(a[2]));
    if constexpr (I == 1) asm volatile("v_add_f32 %0, %0, %1" : "+v"(a[1]) : "v"(a[3]));
    if constexpr (I == 2) asm volatile("v_cvt_pk_bf16_f32 %0, %1, %2" : "=v"(o) : "v"(a[0]), "v"(a[1]));
}

template <int J> DEVI void fold2(f32x4& a, f32x4& b, unsigned (&o)[2]) {
    if constexpr (J < 2) asm volatile("v_add_f32 %0, %0, %2\n\tv_add_f32 %1, %1, %3" : "+v"(a[J]), "+v"(b[J]) : "v"(a[J + 2]), "v"(b[J + 2]));
    else asm volatile("v_cvt_pk_bf16_f32 %0, %2, %3\n\tv_cvt_pk_bf16_f32 %1, %4, %5" : "=&v"(o[0]), "=&v"(o[1]) : "v"(a[0]), "v"(a[1]), "v"(b[0]), "v"(b[1]));
}
DEVI unsigned vshl1(unsigned x, unsigned b) { unsigned d; asm volatile("v_lshl_or_b32 %0, %1, 1, %2" : "=v"(d) : "s"(x), "n"(b)); return d; }

#define ANNOUNCE(s, now, d) (((unsigned)(s) << 16) | (((unsigned)(now) + (d)) & 0xFFFFu))

template <class PUT> DEVI void ln_rows(const u32x4 (&a)[5], int n, int row  , PUT put) {
    float v[40];
#pragma unroll
    for (int i = 0; i < 20; ++i) { v[2 * i] = blo(a[i >> 2][i & 3]); v[2 * i + 1] = bhi(a[i >> 2][i & 3]); }
    f32x2 s2[4] = {{0.f, 0.f}, {0.f, 0.f}, {0.f, 0.f}, {0.f, 0.f}};
#pragma unroll
    for (int i = 0; i < 40; i += 2) s2[(i >> 1) & 3] += (f32x2){v[i], v[i + 1]};
    const f32x2 st = (s2[0] + s2[1]) + (s2[2] + s2[3]);
    float m = row16sum(st[0] + st[1]) * (1.f / 640.f);
    f32x2 q2[4] = {{0.f, 0.f}, {0.f, 0.f}, {0.f, 0.f}, {0.f, 0.f}};
#pragma unroll
    for (int i = 0; i < 40; i += 2) { const f32x2 d = (f32x2){v[i], v[i + 1]} - (f32x2){m, m}; q2[(i >> 1) & 3] += d * d; }
    const f32x2 qt = (q2[0] + q2[1]) + (q2[2] + q2[3]);
    const float q = row16sum(qt[0] + qt[1]);
    const float var = bfr(q * (1.f / 640.f));
    m = bfr(m);
    const float rden = __builtin_amdgcn_rcpf(bfr(__builtin_amdgcn_sqrtf(bfr(var + 1e-5f))));
    const unsigned vo = (n >> 2) * 2048 + (row >> 4) * 1024 + ((n & 3) * 16 + (row & 15)) * 16;
#pragma unroll
    for (int c = 0; c < 5; ++c) {
        u32x4 o;
#pragma unroll
        for (int i = 0; i < 4; ++i) { unsigned d = cvtpk(v[c * 8 + 2 * i] - m, v[c * 8 + 2 * i + 1] - m); o[i] = cvtpk(blo(d) * rden, bhi(d) * rden); }
        put(vo, c, o);
    }
}

struct Soft {
    unsigned long long mA, mY, mC[8];
    unsigned v0, v1;
    unsigned sg, hs;
    float a[6], m, M, t, s, h; u32x2 pk;
    template <int I> DEVI void mask(f32x4 (&sc)[3]) {
        if constexpr (I == 0) asm volatile("v_mov_b32 %0, 0xfe800000" : "=v"(t));
        if constexpr (I >= 1 && I < 9) { float d; asm volatile("v_cndmask_b32_e64 %0, %1, 0, %2" : "=v"(d) : "v"(t), "s"(mC[I - 1])); sc[(I - 1) >> 2][(I - 1) & 3] = d; }
    }
    template <int I> DEVI void op(const Ctx& k, f32x4 (&sc)[3], unsigned a0, unsigned a1, unsigned hv) {
#define FMA(x, y) "v_fmamk_f32 " x ", " x ", 0x3dd53b95, " y "\n\t"
#define EXPC(x) "v_exp_f32_e64 " x ", " x " clamp\n\t"
#define OP3(o, d, x, y) o " " d ", " x ", " y "\n\t"
#define SA(txt) asm volatile(txt : "+v"(a[0]), "+v"(a[1]), "+v"(a[2]), "+v"(a[3]), "+v"(m), "+v"(t))
#define SB(txt) asm volatile(txt : "+v"(a[0]), "+v"(a[1]), "+v"(a[2]), "+v"(a[3]), "+v"(m), "+v"(t), "+v"(s), "+v"(a[4]), "+v"(a[5]), "+v"(M), "+v"(h) : "s"(hs), "s"(sg))
#define ADD(d, x, y) OP3("v_add_f32", d, x, y)
#define MUL(d, x, y) OP3("v_mul_f32", d, x, y)
#define CND(d, x, y, c) "v_cndmask_b32_e64 " d ", " x ", " y ", " c "\n\t"
        if constexpr (I == 0) asm volatile("v_max3_f32 %0, %1, %2, %3\n\tv_max3_f32 %0, %0, %4, %5" : "=&v"(m) : "v"(sc[0][0]), "v"(sc[0][1]), "v"(sc[0][2]), "v"(sc[0][3]), "v"(sc[1][0]));
        if constexpr (I == 1) asm volatile("v_max3_f32 %0, %0, %2, %3\n\t" CND("%1", "%4", "%5", "%6") : "+v"(m), "=&v"(a[2]) : "v"(sc[1][1]), "v"(sc[1][2]), "v"(sc[0][2]), "v"(sc[1][0]), "s"(mY));
        if constexpr (I == 2) asm volatile("v_max_f32 %0, %0, %2\n\t" CND("%1", "%3", "%4", "%5") : "+v"(m), "=&v"(a[3]) : "v"(sc[1][3]), "v"(sc[0][3]), "v"(sc[1][1]), "s"(mY));
        if constexpr (I == 3) asm volatile("v_mul_f32 %1, 0xbdd53b95, %5\n\t" CND("%0", "%2", "%3", "%4") : "=&v"(a[0]), "=&v"(t) : "v"(sc[0][0]), "v"(a[2]), "s"(mA), "v"(m));
        if constexpr (I == 4) asm volatile(CND("%0", "%2", "%3", "%5") CND("%1", "%1", "%4", "%5") : "=&v"(a[1]), "+v"(a[2]) : "v"(sc[0][1]), "v"(a[3]), "v"(sc[1][2]), "s"(mA));
        if constexpr (I == 5) asm volatile(CND("%0", "%0", "%2", "%3") FMA("%1", "%4") : "+v"(a[3]), "+v"(a[0]) : "v"(sc[1][3]), "s"(mA), "v"(t));
        if constexpr (I == 6) SA(FMA("%1", "%5") FMA("%2", "%5"));
        if constexpr (I == 7) asm volatile(FMA("%0", "%3") "v_mov_b32 %1, %4" : "+v"(a[3]), "=&v"(h), "=v"(s) : "v"(t), "s"(hv));
        if constexpr (I == 8) SA(EXPC("%0"));
        if constexpr (I == 9) SA(EXPC("%1"));
        if constexpr (I == 10) { asm volatile("v_max3_f32 %0, %2, %3, %4\n\tv_mov_b32 %1, %0" : "=&v"(M), "=&v"(t) : "v"(m), "v"(sc[2][0]), "v"(sc[2][1]), "v"(sc[2])); a[4] = sc[2][0]; a[5] = sc[2][1]; }
        if constexpr (I == 11) SB("v_permlane16_swap_b32 %9, %5");
        if constexpr (I == 12) SB(OP3("v_max_f32", "%9", "%9", "%5") "v_mov_b32 %5, %9");
        if constexpr (I == 13) SB("v_permlane32_swap_b32 %9, %5");
        if constexpr (I == 14) SB(OP3("v_max_f32", "%9", "%9", "%5") "v_mul_f32 %9, 0xbdd53b95, %9");
        if constexpr (I == 15) SB(FMA("%7", "%9") FMA("%8", "%9"));
        if constexpr (I == 16) SB(FMA("%4", "%9") ADD("%6", "%0", "%1"));
        if constexpr (I == 17) SB(EXPC("%2"));
        if constexpr (I == 18) SB(EXPC("%3"));
        if constexpr (I == 19) SB(ADD("%6", "%6", "%2") ADD("%6", "%6", "%3"));
        if constexpr (I == 20) SB(EXPC("%7"));
        if constexpr (I == 21) SB(EXPC("%8"));
        if constexpr (I == 22) SB(MUL("%6", "%12", "%6") ADD("%5", "%7", "%8"));
        if constexpr (I == 23) SB(EXPC("%4"));
        if constexpr (I == 24) SB(MUL("%6", "%6", "%4") OP3("v_fmac_f32", "%6", "%11", "%5"));
        if constexpr (I == 25) SB("v_fmac_f32_dpp %6, %6, %10 row_ror:8 row_mask:0xf bank_mask:0xf\n\tv_mov_b32 %5, %6");
        if constexpr (I == 26) SB("v_permlane16_swap_b32 %6, %5");
        if constexpr (I == 27) SB(ADD("%6", "%6", "%5") "v_mov_b32 %5, %6");
        if constexpr (I == 28) SB("v_permlane32_swap_b32 %6, %5");
        if constexpr (I == 29) SB(ADD("%6", "%6", "%5"));
        if constexpr (I == 30) SB("v_rcp_f32 %6, %6");
        if constexpr (I == 31) SB(MUL("%4", "%4", "%6") MUL("%7", "%7", "%6"));
        if constexpr (I == 32) SB(MUL("%0", "%0", "%4") MUL("%1", "%1", "%4"));
        if constexpr (I == 33) SB(MUL("%2", "%2", "%4") MUL("%3", "%3", "%4"));
        if constexpr (I == 34) { unsigned x, y; asm volatile("v_cvt_pk_bf16_f32 %0, %2, %3\n\tv_cvt_pk_bf16_f32 %1, %4, %5" : "=&v"(x), "=v"(y) : "v"(a[0]), "v"(a[1]), "v"(a[2]), "v"(a[3])); pk[0] = x; pk[1] = y; }
        if constexpr (I == 35) { st64(k.rs, v0, a0, pk); SCHED0(); asm volatile("v_mul_f32 %1, %1, %2\n\tv_cvt_pk_bf16_f32 %0, %0, %1" : "+v"(a[4]), "+v"(a[5]) : "v"(s)); }
        if constexpr (I == 36) st32(k.rs, v1, a1, __float_as_uint(a[4]));
    }
};
#define QIDX(t, b) ((t) * 10 + (b))
DEVI void qk_role(const Params& p, Ctx& k, char* smem, int h) {
    PROF_DECL
    const int w = k.w;
    bf16x16 W[52];
    Gather G;
    Soft S;
    const int hw = h >> 3, hh = h & 7;
    unsigned l16, ix, dqm, dq, trp, qp;
    {
        const int l = k.l, g = l >> 4, n = l & 15, r = n & 7, u = w & 1;
        const bool hf = n >= 8, dup = w != 1;
        S.mA = __ballot(dup && hf);
        const int lim = (w == 0 ? 64 : w == 1 ? n : 16 + 8 * u + r) - 4 * g;
#pragma unroll
        for (int i = 0; i < 8; ++i) S.mC[i] = __ballot((i & 3) + (i >> 2) * 16 <= lim);
        const bool okm = !(dup && hf);
        S.mY = __ballot((dup && hf) == (w != 0));
        S.v0 = w == 0 ? hh * 512 + ((g >> 1) * 16 + (r >> 1) * 4 + hf * 2 + (r & 1)) * 16 + (g & 1) * 8 : w == 1 ? (hh >> 1) * 1024 + l * 16 + (hh & 1) * 8 : hh * 1024 + (g * 16 + 8 * u + r) * 16 + hf * 8;
        S.v1 = !okm ? OOB : w == 0 ? 4096 + hh * 128 + r * 16 + g * 4 : (w == 1 ? 4096 + n * 16 : 6144 + (8 * u + r) * 16) + (hh >> 1) * 512 + (g >> 1) * 256 + (hh & 1) * 8 + (g & 1) * 4;
        l16 = l * 16; ix = SPIX(l);
        const int uu = u ^ ((n >> 2) & 1);
        dqm = L_QM + (w >> 1) * 3072 + uu * 512 + (g >> 1) * 128 + n * 8 + (g & 1) * 4;
        dq = L_QM + 6144 + (w >> 1) * 3072 + uu * 512 + g * 128 + ((n * 8) ^ ((g >> 1) * 64));
        trp = L_QM + 6144 + (l & 3) * 128 + (((l >> 2) * 8) ^ ((l & 2) * 32));
        qp = w == 1 ? trp + 6144 : (w == 0 ? L_QM : L_Q + 6144 + u * 256) + (l & 1) * 128 + (((l >> 2) * 8) ^ (w == 3 ? 64 : 0));
        u32x8 rg = {S.v0, S.v1, l16, ix, dqm, dq, trp, qp};
        asm volatile("" : "+{v[0:7]}"(rg));
        S.v0 = rg[0]; S.v1 = rg[1]; l16 = rg[2]; ix = rg[3]; dqm = rg[4]; dq = rg[5]; trp = rg[6]; qp = rg[7];
    }
    Xn X;
    G3 A, B;
    X.l16 = l16; A.l16 = l16; B.l16 = l16; B.lwv = vadd(l16, L_W + w * 1024);
    {
        const int l = k.l;
        bf16x8 wt[16];
        u32x2 gv;
        unsigned ra[3];
#pragma unroll
        for (int t = 0; t < 3; ++t) ra[t] = opq(wp_adr<384, true>(w * 3 + t, l) + SLOT(0));
#define QEX(v) ((v) < 0 ? 0 : ((v) < 6) + ((v) == QGS) + 10 * ((v) == QGS + 2) + 8 * ((v) == QGS + 7))
#define QKE(v) ((v) < QGS + 4 ? 0 : (v) == 19 ? 20 : CMIN(v, 3 * ((v) - QGS - 3)))
        auto rdx = [&](auto K) { constexpr int ks = CV(K); A.r0<ks>(); A.r1<ks>(); B.x0[ks] = A.x0[ks]; B.x1[ks] = A.x1[ks]; if constexpr (ks < 14 && QKE(ks + 1) <= ks) B.rw<ks>(); };
        cstream<20>([](auto V) { constexpr int v = CV(V); return IC(QEX(v - 1) + (v < 19 ? 6 : 0)); },
            [&](auto U) { constexpr int u = CV(U), b = u >> 1, k0 = QKE(u - 1), n = (QKE(u) - k0) * 12, nn = n ? n : 12;
                if constexpr (u < 6) hello(k, 0);
                if constexpr (u == QGS) gv = go_ask(k);
                if constexpr (u == QGS + 2) { PIN(gv); go_wait(k, 0, gv); X.init(k); G.init(k); X.l16 = k.c ? OOB : l16; xn_all(k, X.l16, X.src, X.dst); }
                if constexpr (u == QGS + 4) rdx(IC(0));
                if constexpr (u == QGS + 7) { sfor<3>([&](auto I) { X.next<CV(I)>(k, l16); G.op<CV(I)>(k, l16, 0); }); X.se = X.src; }
                if constexpr (u == 18) {
                    asm volatile("" :: "{v[152:155]}"(wt[10]), "{v[156:159]}"(wt[11]), "{v[232:235]}"(wt[12]));
#pragma unroll
                    for (int i = 10; i < 13; ++i) lw128(B.lwv + i * 4096, __builtin_bit_cast(u32x4, wt[i]));
                }
                if constexpr (u == 19) { G.wait<0>(); lw128(l16 + G.dst + 2048, G.v0); lw128(l16 + G.dst + 3072, G.v1); }
                XSTAMP(u)
                const unsigned rb[3] = {slot_adr<u>(ra[0]), slot_adr<u>(ra[1]), slot_adr<u>(ra[2])};
                bf16x4 hf[12];
                SCHED0();
                sfor<nn>([&](auto I) { constexpr int i = CV(I), ks = k0 + i / 12, m = i % 12, t = i >> 1, e = u == QGS + 7 ? 8 : 0, j = ((i - e) * 6) / (nn - e);
                    if constexpr (n > 0) { if constexpr (m < 6) A.mm<0, false, ks, m>(W); else B.mm<30, true, ks, m - 6>(W); }
                    if constexpr (i < e) { if constexpr (i % 4 == 0) X.op<i / 4, 0>(k); if constexpr (i & 1) X.op<i / 4, 1 + (i / 2) % 2>(k); else G.op<3 + i / 4 + (i & 2) * 2>(k, l16, 0); }
                    else if constexpr (u > 0 && u < 18 && (i == e || ((i - e - 1) * 6) / (nn - e) != j))
                        dma1<SLOT(u + 2) + (j / 3) * 12288 + (j % 3) * 1024>((j < 3 ? p.Wk : p.Wq) + (size_t)(u + 2) * 32 * 6144 + h * 192, wsrc<384, true>(w * 192 + (j % 3) * 64 + l, 12288), w * 3072);
                    if constexpr (i == 0 && u > 4 && (u < 15 || u >= 18)) lw128(B.lwv + (u - 5) * 4096, __builtin_bit_cast(u32x4, wt[(u - 5) & 15]));
                    if constexpr (i < 12) {
                        hf[i] = lrtr(rb[t % 3] + (t / 3) * 12288 + (i & 1) * 1536);
                        if constexpr (i & 1) {
                            if constexpr (t == 5 && b >= 2) B.wl[u] = wt[(u - 4) & 15] = CAT4(hf[i - 1], hf[i]);
                            else if constexpr (u & 1) W[QIDX(t, b) % 52] = CAT8(LO8(W[QIDX(t, b) % 52]), CAT4(hf[i - 1], hf[i]));
                            else W[QIDX(t, b) % 52] = __builtin_shufflevector(hf[i - 1], hf[i], 0, 1, 2, 3, 4, 5, 6, 7, -1, -1, -1, -1, -1, -1, -1, -1);
                        }
                    }
                    if constexpr (n > 0 && m == 3 && ks < 19) rdx(IC((ks + 1) % 20));
                    SCHED0(); }); });
        lw128(B.lwv + 15 * 4096, __builtin_bit_cast(u32x4, wt[15]));
        static_assert(QKE(17) == 17, "QGS");
        RAWBAR()
        XSTAMP(20)
        const unsigned d0 = (l16 | ((l16 >> 5) & 8)) ^ (L_K + (w >> 1) * 3072 + (w & 1) * 8);
        sfor<24>([&](auto I) { A.out4<CV(I), 0>(d0); });
        G.v0 = G.p0; G.v1 = G.p0; G.bad = 0;
        X.off = 0; X.l16 = l16;
        if (k.c) X.init(k);
    }
    RSTAMP(2)
    const unsigned po0 = k.p0 + PR_B + hw * 49152 + (w == 0 ? 0 : w == 1 ? 32768 : 16384), po1 = k.p0 + PR_B + hw * 49152 + (w == 0 ? 0 : 32768);
    const unsigned pdum = k.pg(1) + DUMMY_B, pbc = w == 0 ? k.p0 + CNT(C_BC, hw * 16 + hh) : pdum + EXP(32768 + h * 512 + w * 128);
    unsigned long long now = 0, done = 0, tpa = 0, tpb = 0;
    unsigned a0 = po0 + (k.c ? pdum - k.p0 - PR_B : 0u), a1 = po1 + (k.c ? pdum - k.p0 - PR_B : 0u);
    const unsigned hv = w == 1 ? 0u : 0x3F800000u, hw2 = w == 1 ? 0u : 0x00800000u;
    S.sg = 0x3F800000u; S.hs = 0x3F800000u - hw2;
    f32x4 ac[6];
#pragma unroll
    for (int t = 0; t < 6; ++t) clr2(ac[t]);
    unsigned an = 110;
    __builtin_amdgcn_s_waitcnt(0xc07f);
    do {
        if (X.off != 0) {
            PC8(1, X.sg)
            X.late<2>(k, l16);
            G.check();
        }
_Pragma("unroll 1")
        for (unsigned spins = 0; G.bad != 0 && !spin_fail(k, spins); ++spins) { PC8(0, X.sg) G.again(k, l16); }
        HSTOP(0) HSTAMP(0, 0)
        clr2(ac[5]);
        asm volatile("s_nop 1" : "+v"(ac[5]));
        smfw<50>(ac[5], __builtin_bit_cast(bf16x8, G.p0), W[50], ix);
    do {
        PROF_S(X.sg - 1)
        f32x4 sc[3];
        {
            bf16x8 m[16], wa[10], wb[10], kf[12];
            bf16x4 q[12], km[12];
            unsigned fo[6];
            m[0] = __builtin_bit_cast(bf16x8, G.p0); m[1] = __builtin_bit_cast(bf16x8, G.p1);
            auto put = [&](auto T) { constexpr int t = CV(T); if constexpr (t < 3) lw32(dq + t * 1024, fo[t]); else lw32(dqm + (t - 3) * 1024, fo[t]); };
            auto rd = [&](auto C) { constexpr int c = CV(C), j = c % 20;
                if constexpr (c < 0) { }
                else if constexpr (c < 20) m[j] = lr128(l16 + MADR(j < 10 ? j : j - 8));
                else if constexpr (c < 40) wa[j] = lr128(B.lwv + (j - 2) * 8192);
                else if constexpr (c < 60) wb[j] = lr128(B.lwv + (j - 2) * 8192 + 4096);
                else if constexpr (c < 80) kf[j] = lr128(l16 + L_K + ((j & 1) * 6 + j / 2) * 1024);
                else if constexpr (c < 100) q[j] = lrtr(qp + j * 512);
                else if constexpr (c < 120) km[j] = lrtr(trp + j * 512);
                else if constexpr (c < 140) A.r0<j>();
                else A.r1<j>(); };
#define PB(i) ((i) < 12 ? (i) / 6 : (i) < 32 ? (i) / 4 - 1 : (i) < 44 ? 7 + ((i) - 32) % 3 : (i) < 54 ? 7 + ((i) - 44) / 2 : 12 + ((i) - 54) % 3)
#define PT(i) ((i) < 12 ? ((i) + 5) % 6 : (i) < 32 ? (3 + (i) % 4) % 6 : (i) < 44 ? (3 + ((i) - 32) / 3) % 6 : (i) < 54 ? 1 + ((i) & 1) : 1 + ((i) - 54) / 3)
            sfor<82>([&](auto I) {
                constexpr int i = CV(I) + 1, b = PB(i), t = PT(i), bn = PB(i + 1);
                constexpr int rt[83] = {-1, 2, -1, -1, -1, 22, 42, 3, -1, 23, 43, 4, -1, 24, 44, 5, -1, 25, 45, 6,
                    -1, 26, 46, 7, -1, 8, 9, 27, 47, 28, 48, 29, 49, -1, -1, 60, -1, 61, 62, 63,
                    -1, 10, 64, -1, 11, 12, -1, 13, 14, -1, -1, 80, 82, 83, 84, 85, 86, 87, 67, 88,
                    89, 69, -1, 91, -1, 71, -1, 121, -1, 100, 102, 104, 106, 108, 110, -1, 140, -1, -1, 141, -1, -1, 122};
                constexpr int r2[83] = {-1, -1, -1, -1, -1, -1, -1, -1, -1, -1, -1, -1, -1, -1, -1, -1, -1, -1, -1, -1,
                    -1, -1, -1, -1, -1, -1, -1, -1, -1, -1, -1, -1, -1, -1, -1, -1, -1, -1, -1, -1,
                    -1, -1, -1, -1, -1, -1, -1, -1, -1, -1, -1, 81, -1, 65, -1, 66, -1, -1, -1, -1,
                    68, 90, -1, 70, -1, 120, -1, -1, -1, 101, 103, 105, 107, 109, 111, -1, -1, -1, -1, -1, -1, -1, -1};
                if constexpr (i < 60) {
                    if constexpr (t == 5 && b >= 2) smf<false>(ac[5], m[b], CAT8(wa[b], wb[b]), ix);
                    else smfw<QIDX(t, b < 10 ? b : b - 8) % 52>(ac[t], m[b], W[QIDX(t, b < 10 ? b : b - 8) % 52], ix);
                } else if constexpr (i < 72) { constexpr int j = i - 60; mfa<false, false>(sc[j & 1], kf[j], CAT4(q[j & ~1], q[j | 1])); }
                else if constexpr (i < 77) A.mm<0, false, (i - 72) / 3, (i - 72) % 3>(W);
                else { constexpr int ks = i - 77; mfa<false, ks == 0>(sc[2], CAT4(km[2 * ks], km[2 * ks + 1]), CAT4(q[2 * ks], q[2 * ks + 1])); }
                SCHED0();
                if constexpr (i == 1) { AQ(3, 3) asm volatile("s_barrier" ::: "memory"); AQ(1, 2) AQ(3, 4) }
                else if constexpr (i == 50) { BARL(3); AQ(1, 3) }
                else if constexpr (i == 68) { BARL(3); AQ(1, 4) QSTAMP(0, 1) }
                else if constexpr (i >= 12 && i < 59) { if constexpr (PT(i + 1) == 5) TOUCH(wb[bn % 10]); TOUCH(m[bn]); }
                else if constexpr (i >= 59 && i < 71) { asm volatile("" :: "v"(kf[(i - 59) % 12]), "v"(q[((i - 59) | 1) % 12])); }
                else if constexpr (i == 71) TOUCH(A.x0[0]);
                else if constexpr (i == 74) TOUCH(A.x0[1]);
                else if constexpr (i >= 76 && i < 82) TOUCH(km[(2 * (i - 76) + 1) % 12]);
                if constexpr (i == 8 || i == 30) asm volatile("" :: "{v[60:63]}"(ac[0]), "{v[28:31]}"(ac[1]), "{v[32:35]}"(ac[2]), "{v[64:67]}"(ac[3]), "{v[68:71]}"(ac[4]), "{v[72:75]}"(ac[5]));
                TPQ(i)
                if constexpr (i == 14) AC3(0, 0, 0)
                if constexpr (i == 16) AC3(1, 6, 1)
                if constexpr (i == 18) AC3(2, 7, 2)
                if constexpr (i == 20) AC3(-1, -1, 3)
                if constexpr (i == 22) AC3(-1, -1, 4)
                if constexpr (i == 24) AC3(-1, -1, 5)
                if constexpr (i == 66) AC3(3, -1, 6)
                if constexpr (i == 2) { now = rt_async(); HSTOP(1) }
                if constexpr (i == APOS) { PINS(now); an += (unsigned)now; PINS(an); }
                if constexpr (i == APOS + 1) an = spack(an, X.sg);
                SCHED0();
                rd(IC(rt[i])); rd(IC(r2[i]));
                if constexpr (i == APOS + 4) st32(k.rs, 0, pbc, fo[1]);
                if constexpr (i >= 40 && i < 50 && i % 3 == 1) put(IC(((i - 31) / 3) % 6));
                if constexpr (i == 62 || i == 64) put(IC(i / 2 - 30));
                SCHED0();
                if constexpr (i < 25 && i % 4 != 1) B.out<((i - 2) / 4) * 3 + (i - 2) % 4, 6145>(dq, dq);
                if constexpr (i == APOS + 3) { fo[1] = an; PIN(fo[1]); }
                if constexpr (i >= 37 && i < 49) { constexpr int tt = ((i - 28) / 3) % 6; fold_op<(i - 37) % 3>(ac[tt], fo[tt]); }
                if constexpr (i >= 59 && i < 62) fold_op<i - 59>(ac[1], fo[1]);
                if constexpr (i == 62) fold_op<0>(ac[2], fo[2]);
                if constexpr (i == 63) asm volatile("v_add_f32 %1, %1, %2\n\tv_cvt_pk_bf16_f32 %0, %3, %1" : "=v"(fo[2]), "+v"(ac[2][1]) : "v"(ac[2][3]), "v"(ac[2][0]));
                if constexpr (i == 33) S.mask<0>(sc);
                if constexpr (i >= 51 && i < 59) S.mask<i - 50>(sc);
                if constexpr (i >= 75) S.op<i - 75>(k, sc, a0, a1, hv);
                if constexpr (i == 12) { AQ(3, 5) }
                if constexpr (i == 43) { AQ(3, 6) }
                if constexpr (i == 59) { AQ(3, 7) }
                SCHED0();
            });
        }
        QSTAMP(0, 2)
        {
            unsigned d0;
            auto rest = [&](auto J) { constexpr int j = CV(J);
                if constexpr (j >= 0 && j < 12 && (j % 6 == 0 || j % 3 == 2)) X.op<3 + j / 6, (j % 6 + 1) / 3>(k); };
            sfor<43>([&](auto F) { constexpr int f = CV(F) + 5;
                constexpr int e = f < 6 ? f + 3 : f < 9 ? f - 3 : f;
                A.mm<0, false, e / 6, e % 6>(W);
                if constexpr (f % 6 == 2 || f == 5) TOUCH(A.x1[f / 6]);
                else if constexpr (f == 47) { TOUCH(B.x0[0]); AQ(1, 6) AQ(2, 1) }
                else if constexpr (f % 6 == 5) TOUCH(A.x0[f / 6 + 1]);
                if constexpr (f == 40 - 6 * SOFT2) { done = rt_async(); QSTAMP(0, 3) AQ(1, 5) }
                TPQ(f + 78)
                if constexpr (f == 21) set_m0(X.dst + 2 * 8192);
                if constexpr (f == 24) { X.sb = X.se + 2 * 16384; PINS(X.sb); }
                if constexpr (f == 42) AC3(4, -1, 7)
                SCHED0();
                if constexpr (f % 6 == 4 && f < 36) A.r0<f / 6 + 2>();
                if constexpr (f % 6 == 1 && f < 42) A.r1<f / 6 + 1>();
                if constexpr (f >= 39 && f < 43) B.rd<true, ((f - 39) / 2) * 6 + (f - 39) % 2>();
                if constexpr (f == 26 || f == 31 + 4 * QT) X.op<2, f / 15>(k);
                SCHED0();
                S.op<f + 3>(k, sc, a0, a1, hv);
                rest(IC(f - 38));
                if constexpr (f == 45) B.lwv = vadd(l16, L_W + w * 1024);
                SCHED0(); });
            B.go<30, true, 0, 48, 8>(W, [&](auto F) { constexpr int f = CV(F);
                TPQ(f + 126)
                rest(IC(f + 10));
                if constexpr (f == 9) AC3(5, -1, -1)
                if constexpr (f == 2) { a0 = (X.sg & 3) * PR_SZ; PINS(a0); }
                if constexpr (f == 4) { a1 = a0 + po1; a0 += po0; PINS(a0); PINS(a1); }
                if constexpr (f == 8) { S.sg = (X.sg & 4) ? 0xBF800000u : 0x3F800000u; PINS(S.sg); }
                if constexpr (f == 10) { S.hs = S.sg - hw2; PINS(S.hs); }
                if constexpr (f == 14 || f == 16) G.op<f / 16>(k, l16, X.sg);
                if constexpr (f == 22 || f == 28 || f == 32) X.next<(f - 22) / 5>(k, l16);
                if constexpr (f == 35 - QT) asm volatile("s_waitcnt vmcnt(%0)\n\ts_barrier" :: "n"(7 - 2 * QT) : "memory");
                if constexpr (f == 20) AC3(6, 1, -1)
                if constexpr (f == 26) A.home<0>();
                if constexpr (f >= 36) A.rd<false, f + 12>();
                if constexpr (f == 38 || f == 40) X.next<(f == 38 ? 5 : 6)>(k, l16);
                if constexpr (f == CPOS - 1 || f == CPOS) X.next<f - CPOS + 4>(k, l16);
                if constexpr (f == 47) { A.pre<false, 8>(); AQ(1, 7) AQ(2, 2) } });
            A.go<0, false, 48, 120, 20>(W, [&](auto F) { constexpr int f = CV(F), i = f - XPOS;
                TPQ(f + 126)
                if constexpr (f == 56) B.home<1>();
                if constexpr (i == 0 || i == 2 || i == 6) X.next<7 + i / 2>(k, l16);
                if constexpr (i >= 3 && i < 15 && ((i - 3) % 6 == 0 || (i - 3) % 3 == 2)) X.op<(i - 3) / 6, ((i - 3) % 6 + 1) / 3>(k);
                if constexpr (f == 58) asm volatile("s_waitcnt vmcnt(1)\n\ts_barrier" ::: "memory");
                if constexpr (f == 57) AC3(7, 2, -1)
                if constexpr (f == RPOS - 5) G.ask<0>(k, l16);
                if constexpr (f == RPOS) { __builtin_amdgcn_s_setprio(0); SCHED0(); G.ask<1>(k, l16); }
                if constexpr (f >= 108) B.rd<true, f - 60>();
                SCHED0();
                G.red<f - 110, (RPOS < XPOS + 5 ? 4 : 0)>();
                if constexpr (f == 116) asm volatile("v_bfe_u32 %0, %1, 8, 1\n\tv_lshl_or_b32 %0, %0, 3, %1" : "=&v"(d0) : "v"(l16));
                if constexpr (f == 118) asm volatile("v_xor_b32 %0, %1, %0" : "+v"(d0) : "s"(L_K + (w >> 1) * 3072 + (w & 1) * 8));
                if constexpr (f == 119) { B.pre<true, 8>(); AQ(2, 3) } });
            B.go<30, true, 48, 120, 20>(W, [&](auto F) { constexpr int f = CV(F), i = f - 48;
                constexpr int ko[26] = {0, 1, 2, 4, 3, 6, 5, 8, 7, 9, 10, 11, 12, 13, 14, 16, 15, 18, 17, 20, 19, 21, 22, 23, -1, -1};
                TPQ(f + 198)
                G.hold<f - GPOS + 5>(X.sg);
                if constexpr (i < 24) { SCHED0(); A.out4<ko[i % 26], 0>(d0); }
                G.at<f - GPOS>(k, l16);
                if constexpr (f == 114) { lgkm0(done); PINS(G.rt); }
                if constexpr (f == 115) { PINS(now); HTRACE(X.sg - 2) an = (unsigned)done - (unsigned)now; PINS(an); }
                if constexpr (f == 116) { an = min(an, 200u); PINS(an); }
                if constexpr (f == 117) { an += DQ; PINS(an); }
                SCHED0();
                if constexpr (i == 28) AC3(-1, 3, -1)
                if constexpr (i == 50) AC3(-1, 4, -1)
                if constexpr (i == 62) AC3(-1, 5, -1)
                if constexpr (i == 23) { AQ(2, 4) }
                if constexpr (i == 47) { AQ(2, 5) }
                if constexpr (i == 65) { AQ(2, 6) }
                if constexpr (f == 119) { AQ(2, 7) }
                if constexpr (f == 112) asm volatile("" :: "{v[12:15]}"(G.v0), "{v[16:19]}"(G.v1), "{v[20:23]}"(G.p0), "{v[24:27]}"(G.p1), "{v9}"(B.lwv) TRHINT);
                G.early<f - EPOS>(X.off);
                if constexpr (f >= 113 && f < 119) clr2(ac[(f + 6 - 113 + 5) % 6]); });
            static_assert(GPOS >= 70 && GPOS <= 98 && XPOS % 6 == 2 && RPOS > XPOS && (RPOS + 1 < XPOS + 5 || RPOS >= XPOS + 14) && XPOS > 58 && CPOS > 8 && CPOS < 35 - QT && SOFT2, "GPOS");
            QSTAMP(0, 4) QSTAMP(0, 5)
            HFLUSH(s)
        }
        AST(0)
        PROF_ONLY(G.wait<0>();)
        HSTOP(0) HSTAMP(s, 0) AQ(1, 1) AQ(3, 1)
        G.rest();
        SCHED0();
        smfw<50>(ac[5], __builtin_bit_cast(bf16x8, G.p0), W[50], ix);
        AQ(3, 2)
        SCHED0();
    } while (G.bad == 0);
    } while (X.sg != 65u);
    PROF_OUT
}

#define OIDX(t, b) (22 + (t) * 3 + (b))
DEVI void vo_role(const Params& p, Ctx& k, char* smem, int h) {
    PROF_DECL
    const int w = k.w;
    bf16x16 W[52];
    unsigned l16, lwv, ix, dq0, trp, vo;
    {
        const int l = k.l, g = l >> 4, n = l & 15, q = (l >> 2) & 3, pp = l & 3;
        l16 = l * 16; lwv = l16 + L_W + w * 1024; ix = SPIX(l);
        dq0 = L_QM + (24 * w + (n >> 2) * 2 + (n & 1)) * 32 + g * 8 + ((n >> 1) & 1) * 4;
        trp = L_QM + (16 * (g >> 1) + 4 * (g & 1) + 8 * (q >> 1) + (q & 1)) * 32 + pp * 8;
        vo = EXP(h * 512) + (g >> 1) * 256 + n * 16 + (g & 1) * 8;
        u32x8 r = {dq0, trp, vo, ix, l16, lwv, 0u, 0u};
        PIN(r);
        dq0 = r[0]; trp = r[1]; vo = r[2]; ix = r[3]; l16 = r[4]; lwv = r[5];
    }
    Gather G;
    G3 V;
    Xn X;
    V.l16 = l16; V.lwv = lwv; X.l16 = l16;
    f32x4 y[20];
    bf16x8 v[12];
    u32x4 o[10];
    unsigned yb;
    {
        const int l = k.l;
        bf16x8 wt[16];
        bf16x4 q[10][4];
        f32x4 z[9][2];
        u32x2 gv;
        unsigned ra[6];
        const u16* wo = p.Wo + (size_t)h * 192 * 640;
        constexpr int L_V = L_XN + 24576;
#define VEX(v) ((v) < 0 ? 0 : ((v) < 6) + ((v) == VGS) + 10 * ((v) == VGS + 2) + 6 * ((v) == VGS + 6 || (v) == 18))
#define VKE(v) ((v) < VGS + 4 ? 0 : CMIN(20, 7 * ((v) - VGS - 3)))
#define YKE(v) ((v) < 16 ? 0 : 3 * (v) - 45)
        auto put = [&](int t) { st128(k.rs, X.l16, yb + t * 65536, (u32x4){cvtpk(z[t][0][0], z[t][0][1]), cvtpk(z[t][0][2], z[t][0][3]), cvtpk(z[t][1][0], z[t][1][1]), cvtpk(z[t][1][2], z[t][1][3])}); };
        auto rdx = [&](auto K) { constexpr int ks = CV(K); if constexpr (ks < 20) { V.r0<ks % 20>(); V.r1<ks % 20>(); if constexpr (ks < 14) V.rw<ks>(); } };
        auto rdv = [&](auto J) { constexpr int j = CV(J) % 6; v[j] = lr128(l16 + L_V + j * 1024); v[6 + j] = lr128(l16 + L_V + (6 + j) * 1024); };
        cstream<20>([](auto V) { constexpr int v = CV(V); return IC(VEX(v - 1) + (v < 19 ? 6 : 0)); },
            [&](auto U) { constexpr int u = CV(U), k0 = VKE(u - 1), y0 = YKE(u - 1), n = u < 16 ? (VKE(u) - k0) * 6 : u < 18 ? 36 : 18, nn = n ? n : 12, nr = u < 10 ? 3 : u < 16 ? 6 : 4;
                if constexpr (u < 6) hello(k, 0);
                if constexpr (u == VGS) gv = go_ask(k);
                if constexpr (u == VGS + 2) { PIN(gv); go_wait(k, 0, gv); X.init(k); X.l16 = k.c ? OOB : l16; xn_all(k, X.l16, X.src, X.dst); }
                if constexpr (u == VGS + 4) { rdx(IC(0)); rdx(IC(1)); }
                if constexpr (u == VGS + 6) { sfor<3>([&](auto I) { X.next<CV(I)>(k, l16); }); X.se = X.src; yb = k.yx(0, w) + EXP(h * 1024); }
                if constexpr (u == 16) rdv(IC(0));
                XSTAMP(u)
                if constexpr (u == 0 || u == 10 || u == 16) {
#pragma unroll
                    for (int t = 0; t < nr; ++t) ra[t] = opq((u == 0 ? wp_adr<384, true>(w * 3 + t, l) : u == 10 ? wp_adr<768, false>(w * 6 + t, l) : wp_adr<512, false>(w * 4 + t, l)) + SLOT(0));
                }
                unsigned rb[6];
#pragma unroll
                for (int t = 0; t < nr; ++t) rb[t] = slot_adr<u>(ra[t]);
                SCHED0();
                sfor<nn>([&](auto I) { constexpr int i = CV(I), e = u == VGS + 6 ? 9 : 0, j = ((i - e) * 6) / (nn - e), d = u + 2, r0 = u == 18 ? 6 : 0;
                    if constexpr (u < 16 && n > 0) { V.mm<0, true, k0 + i / 6, i % 6>(W); if constexpr (i % 6 == 3) rdx(IC(k0 + i / 6 + 2)); }
                    if constexpr (u >= 16) { constexpr int m = n / 3, ks = (y0 + i / m) % 6, t = (u / 18) * 6 + (i % m) / 2, rt = i & 1;
                        if constexpr (!(ks == 5 && t == 7 && rt)) { mfw<OIDX(t, ks >> 1), ks & 1, true, ks == 0>(z[t][rt], W[OIDX(t, ks >> 1)], v[rt * 6 + ks]); SCHED0(); }
                        if constexpr (i % m == 1 && u < 19) rdv(IC(ks + 1));
                        if constexpr (u == 19 && i < 2) rdv(IC(4 + i));
                        if constexpr (u == 18 && i >= 3 && i < 15 && (i & 1)) put((i - 3) / 2);
                        if constexpr (u == 18 && i < 6) lw128(lwv + (10 + i) * 4096, __builtin_bit_cast(u32x4, wt[10 + i])); }
                    if constexpr (i < e) X.op<i / 3, i % 3>(k);
                    else if constexpr (u > 0 && u < 18 && (i == e || ((i - e - 1) * 6) / (nn - e) != j)) {
                        if constexpr (d < 10) dma1<SLOT(d) + (j / 3) * 12288 + (j % 3) * 1024>(p.Wv + (size_t)(2 * d + j / 3) * 32 * 6144 + h * 192, wsrc<384, true>(w * 192 + (j % 3) * 64 + l, 12288), w * 3072);
                        else if constexpr (d < 16) dma1<SLOT(d) + j * 1024>(wo + (d - 10) * 32 * 640, wsrc<768, false>(w * 384 + j * 64 + l, 1280), w * 6144);
                        else dma1<SLOT(d) + j * 1024>(wo + (d - 16) * 48 * 640 + 384, wsrc<512, false>(w * 384 + j * 64 + l, 1280), w * 6144);
                    }
                    if constexpr (i < 2 && u >= 3 && u < 8) lw128(lwv + (2 * u - 6 + i) * 4096, __builtin_bit_cast(u32x4, wt[(2 * u - 6 + i) & 15]));
                    sfor<2>([&](auto C) { constexpr int c = u == 19 ? 2 * i + CV(C) : i - r0 + CV(C) * 99;
                        if constexpr (c >= 0 && c < 12) {
                            constexpr int t = u < 10 ? c >> 2 : u < 16 ? c >> 1 : 6 + c % 4, r = u < 10 ? c & 3 : u < 16 ? (u - 10) * 2 + (c & 1) : (u - 16) * 3 + c / 4, id = u < 10 ? (t * 10 + u) % 52 : OIDX(t, r >> 2);
                            q[t][r & 3] = lrtr(u < 10 ? rb[t] + (r >> 1) * 12288 + (r & 1) * 1536 : u < 16 ? rb[t] + (c & 1) * 12288 : rb[(t - 6) & 3] + (c / 4) * 8192);
                            if constexpr (u < 10 && u >= 2 && t == 2) { if constexpr (r & 1) V.wl[2 * u + r / 2] = wt[(2 * u - 4 + r / 2) & 15] = CAT4(q[t][r - 1], q[t][r]); }
                            else if constexpr ((r & 3) == 1) W[id] = __builtin_shufflevector(q[t][0], q[t][1], 0, 1, 2, 3, 4, 5, 6, 7, -1, -1, -1, -1, -1, -1, -1, -1);
                            else if constexpr ((r & 3) == 3) W[id] = CAT8(LO8(W[id]), CAT4(q[t][2], q[t][3]));
                        } });
                    SCHED0(); });
                if constexpr (u == VGS + 7) sfor<18>([&](auto I) { V.out<CV(I), 0>(l16 + L_V + ((3 * w) >> 1) * 1024 + ((3 * w) & 1) * 8, l16 + L_V + ((3 * w + 1) >> 1) * 1024 + ((3 * w + 1) & 1) * 8); }); });
        static_assert(VGS >= 6 && VGS < 9, "VGS");
        RAWBAR()
        XSTAMP(20)
        sfor<2>([&](auto B) { X.op<CV(B) + 3, 0>(k); asm volatile("s_nop 0"); X.op<CV(B) + 3, 1>(k); X.op<CV(B) + 3, 2>(k); });
        asm volatile("" : "+v"(z[8][0]), "+v"(z[8][1]), "+v"(z[7][0]), "+v"(z[6][1]));
        put(6); put(8);
        lw32(l16 + L_MEM, ~0u);
        o[7] = (u32x4){cvtpk(z[7][0][0], z[7][0][1]), cvtpk(z[7][0][2], z[7][0][3]), 0u, 0u};
        y[17] = z[7][1];
        yb += 7 * 65536;
        X.off = 0; X.l16 = l16;
        if (k.c) X.init(k);
    }
    RSTAMP(2)
    const unsigned pdum = k.pg(1) + DUMMY_B, pbc = w == 0 ? k.p0 + CNT(C_BC, (h >> 3) * 16 + 8 + (h & 7)) : pdum + EXP(49152 + h * 512 + w * 128), pby = k.p0 + YLINE(w, h);
    unsigned long long now = 0, done = 0, tpa = 0, tpb = 0;
    G.mx = ~0u;
    G.init(k);
    G.base -= 32768;
    const unsigned ypar = (unsigned)(k.par >> (PG_YX + (w >> 1))), yo = ((unsigned)(PG_YX + (w >> 1)) << 21) + (w & 1) * 1048576 + EXP(h * 1024);
    const unsigned ym0 = k.ym(0) + w * 163840, ym2 = k.ym(2) + w * 163840;
    unsigned ym = k.c ? pdum + w * 163840 : ym0, ys, t, c = ~0u, an = 80;
#define YT(u) ((u) < 10 ? 2 * ((u) % 5) : ((u) - 10) | 1)
#define YR(u) ((u) < 10 ? (u) / 5 : (u) & 1)
    auto yout = [&](auto U, auto J) { constexpr int u = CV(U), j = CV(J), t = YT(u), rt = YR(u);
        if constexpr (j == 0) { o[t][2 * rt] = cvtpk(y[u][0], y[u][1]); o[t][2 * rt + 1] = cvtpk(y[u][2], y[u][3]); TOUCH(o[t][2 * rt]); TOUCH(o[t][2 * rt + 1]); }
        if constexpr (j == 1 && rt) __builtin_amdgcn_raw_buffer_store_b128(o[t], k.rs, (int)l16, (int)yb, AUX_SC1);
        if constexpr (j == 2 && rt) { yb += t == 8 ? -7 * 65536 : 131072; PINS(yb); TOUCH(o[t]); } };
    auto ymm = [&](auto I) {
        constexpr int i = CV(I), ks = i % 6, u = i / 6, id = OIDX(YT(u), ks >> 1);
        mfw<id, ks & 1, true, ks == 0>(y[u], W[id], v[YR(u) * 6 + ks]);
        SCHED0();
        if constexpr (u > 0 && ks >= 2) yout(IC((u + 19) % 20), IC(ks - 2)); };
    if (k.c) X.late<5>(k, l16);
    do {
        PROF_S(X.sg - 1)
        f32x4 ac[3];
        sfor<13>([&](auto I) { constexpr int i = CV(I) + 107;
            ymm(IC(i));
            TPV(i - 107)
            if constexpr (i == TB) { asm volatile("s_waitcnt vmcnt(%0)\n\ts_barrier" :: "n"(TB < 111 ? 7 : 8) : "memory"); AST(5) }
            if constexpr (i == TB + 1) st32(k.rs, 0, pby, G.mx);
            if constexpr (i == 119) V.pre<true, 0>();
            if constexpr (i == 113) G.ask<0>(k, l16);
            if constexpr (i == 119) G.ask<1>(k, l16);
            if constexpr (i == 114) V.r0<0>();
            if constexpr (i == 115) V.r1<0>();
            if constexpr (i == 117) V.r0<1>();
            if constexpr (i == 118) V.r1<1>();
            SCHED0(); });
        HSTAMP(s, 4)
        V.run<0, true, 0, 16, 17, true>(W, [&](auto I) { constexpr int i = CV(I);
            TPV(i + 13)
            if constexpr (i >= 2) yout(IC(19), IC(i - 2));
            if constexpr (i == 8) { t = (X.sg & 7) * 2; PINS(t); }
            if constexpr (i == 10) { yb = (ypar >> t) & 1; PINS(yb); }
            if constexpr (i == 14) { t = (t << 21) + yo; PINS(t); }
            if constexpr (i == 16) { yb = (yb << 13) + t; PINS(yb); }
            if constexpr (i == 20) { c = X.sg; PINS(c); }
            if constexpr (i == 22) { G.a = max(c, 1u) << 15; PINS(G.a); }
            if constexpr (i == 28) G.op<1>(k, l16, 0);
            if constexpr (i == 40) { an = min(an, 200u) + DV; PINS(an); }
            if constexpr (i == 32 || i == 34 || i == 38) X.next<(i - 31) / 3>(k, l16);
            if constexpr (i == 23 || i == 29) { AAC(4 + i / 29) }
            if constexpr (i == 83 || i == 89) { AAC(6 + i / 89) }
            if constexpr (i == YPOS) { AST(6) }
            if constexpr (i == YPOS + 1) { AST(7) }
            if constexpr (i == YPOS) DRAIN();
            if constexpr (i == YB) asm volatile("s_barrier" ::: "memory");
            if constexpr (i == YPOS - 2) G.mx = vshl1(c, 0);
            if constexpr (i == YPOS + 1) st32(k.rs, 0, pby, G.mx);
            SCHED0();
            G.red<i - YPOS - 2, -1>();
            G.hold<i - GPOSY * 6 - GVS + 5>(c);
            G.at<i - GPOSY * 6 - GVS>(k, l16);
            if constexpr (i == 90) PINS(G.rt);
            if constexpr (i == 95) V.pre<true, 16>();
            if constexpr (i >= 92 && i < 95) clr2(ac[(i - 92) % 3]); });
        static_assert(GPOSY * 6 + 2 > YPOS + 1 && GPOSY <= 13 && YB >= YPOS && YB < 60, "GPOSY");
        HSTAMP(s, 5)
        HFLUSH(s - 1)
        f32x4 acc[10];
        f32x2 z[20];
        auto clr = [&](auto I) { constexpr int i = CV(I); asm volatile("v_mov_b64 %0, 0" : "=v"(z[i])); if constexpr (i & 1) acc[i >> 1] = __builtin_shufflevector(z[i - 1], z[i], 0, 1, 2, 3); };
        bf16x4 al[3], ah[3];
        unsigned d0, d1;
        {
            bf16x8 m[10], wa[10], wb[10];
            auto rd = [&](auto R) { constexpr int r = CV(R), b = 2 + r / 3;
                if constexpr (b < 10) {
                    if constexpr (r % 3 == 0) m[b] = lr128(l16 + MADR(b));
                    if constexpr (r % 3 == 1) wa[b] = lr128(lwv + (b - 2) * 8192);
                    if constexpr (r % 3 == 2) wb[b] = lr128(lwv + (b - 2) * 8192 + 4096);
                } else if constexpr (b < 13) {
                    if constexpr (r % 3 == 0) V.r0<b + 7>();
                    if constexpr (r % 3 == 1) V.rw<b + 7>();
                    if constexpr (r % 3 == 2) V.r1<b + 7>();
                } };
            AST(0)
            G.wait<0>();
            HSTOP(0) HSTAMP(s, 0) AST(1)
            V.run<0, true, 16, 17, 0, true>(W, [&](auto I) { constexpr int i = CV(I);
                TPV(i + 109)
                G.fin<i>(0);
                if constexpr (i == 3) {
                    if (__builtin_expect(G.bad != 0, 0)) {
                        unsigned spins = 0;
_Pragma("unroll 1")
                        do { PC8(0, c) G.again(k, l16); } while (G.bad != 0 && !spin_fail(k, spins++));
                        HSTOP(0) HSTAMP(s, 0)
                    }
                    asm volatile("s_barrier" ::: "memory");
                    AST(2)
                }
                if constexpr (i == 4) { now = rt_async(); HSTOP(1) SCHED0(); }
                if constexpr (i >= 3) rd(IC(i - 3)); });
            m[0] = __builtin_bit_cast(bf16x8, G.p0); m[1] = __builtin_bit_cast(bf16x8, G.p1);
            sfor<30>([&](auto I) {
                constexpr int i = CV(I), b = i / 3, t = i % 3;
                if constexpr (t == 2 && b >= 2) smf<false>(ac[2], m[b], CAT8(wa[b], wb[b]), ix); else smfw<t * 10 + b>(ac[t], m[b], W[t * 10 + b], ix);
                SCHED0();
                TPV(i + 115)
                if constexpr (i >= 6 && i < 29 && t != 0) { if constexpr (t == 1) TOUCH(wb[b]); else TOUCH(m[(b + 1) % 10]); }
                if constexpr (i == 29) V.pre<true, 17>();
                if constexpr (i == 2) { PINS(now); an += (unsigned)now; PINS(an); }
                if constexpr (i == 3) an = spack(an, c);
                SCHED0();
                if constexpr (i < 27) rd(IC(3 + i));
                if constexpr (i == 5) st32(k.rs, 0, pbc, d0);
                SCHED0();
                if constexpr (i == 4) { d0 = an; PIN(d0); }
                if constexpr (i == 28) d0 = vadd(l16, L_K + ((3 * w) >> 1) * 1024 + ((3 * w) & 1) * 8);
                if constexpr (i == 29) d1 = vadd(l16, L_K + ((3 * w + 1) >> 1) * 1024 + ((3 * w + 1) & 1) * 8);
                SCHED0();
            });
            unsigned fo[3];
            V.run<0, true, 17, 19, 0, true>(W, [&](auto I) { constexpr int i = CV(I);
                TPV(i + 145)
                if constexpr (i < 3) { rd(IC(30 + i)); SCHED0(); fold_op<0>(ac[i], fo[i]); }
                if constexpr (i == 3 || i == 4 || i == 6) { constexpr int t = i / 2 - 1; asm volatile("v_add_f32 %1, %1, %2\n\tv_cvt_pk_bf16_f32 %0, %3, %1" : "=v"(fo[t]), "+v"(ac[t][1]) : "v"(ac[t][3]), "v"(ac[t][0])); }
                if constexpr (i == 5 || i == 7 || i == 8) lw32(dq0 + ((i - 4) / 2) * 256, fo[(i - 4) / 2]);
                if constexpr (i == 9 || i == 10) clr2(acc[i - 9]);
                if constexpr (i == 11) BARL(0) });
        }
        HSTAMP(s, 1) AST(3)
        {
            unsigned fo[2];
            auto xq = [&](auto I) { constexpr int i = CV(I) - XP + XS0;
                if constexpr (i == XA) X.next<3>(k, l16);
                if constexpr (i == -1 || i == 0) X.next<14 + i>(k, l16); };
            static_assert(XP % 6 == 5 && XP <= 53 && XP >= 17 && XB > XP + 18 && XB > 63, "XP");
            auto fin2 = [&](auto P, auto J) { fold2<CV(J)>(acc[2 * CV(P)], acc[2 * CV(P) + 1], fo); };
            V.run<0, true, 19, 20, 0, true>(W, [&](auto I) { constexpr int i = CV(I);
                TPV(i + 157)
                if constexpr (i == 0) { al[0] = lrtr(trp); ah[0] = lrtr(trp + 64); }
                if constexpr (i == 1) al[1] = lrtr(trp + 1024);
                if constexpr (i == 2) ah[1] = lrtr(trp + 1088);
                if constexpr (i == 3) al[2] = lrtr(trp + 2048);
                if constexpr (i == 4) ah[2] = lrtr(trp + 2112);
                SCHED0();
                if constexpr (i >= 1) clr(IC(3 + i)); });
            sfor<30>([&](auto I) { constexpr int i = CV(I), b = (i % 6) >> 1, t = 2 * (i / 6) + (i & 1), id = OIDX(t, b);
                smfw<id>(acc[t], CAT4(al[b], ah[b]), W[id], ix);
                SCHED0();
                TPV(i + 163)
                if constexpr (i == 1 || i == 3) TOUCH(ah[(i + 1) / 2]);
                if constexpr (i == 29) TOUCH(v[0]);
                if constexpr (i == 21) BARL(0)
                SCHED0();
                if constexpr (i == 4 || i == 7) X.next<(i == 4 ? 5 : 11)>(k, l16);
                xq(IC(i - 30));
                if constexpr (i >= 21 && i < 27) v[i - 21] = lr128(l16 + L_K + (i - 21) * 1024);
                SCHED0();
                V.out<i - 1, 0>(d0, d1);
                SCHED0();
                if constexpr (i % 3 == 0 && i < 18) clr(IC(9 + i / 3));
                if constexpr (i == 16) clr(IC(15));
                if constexpr (i >= 18 && i < 21) clr(IC(i - 2));
                if constexpr (i == 22) clr(IC(19));
                if constexpr (i >= 23 && i < 29) fold_op<(i - 23) / 2>(acc[(i - 23) & 1], fo[(i - 23) & 1]);
                if constexpr (i == 29) { st64(k.rs, vo, ym, (u32x2){fo[0], fo[1]}); SCHED0(); fin2(IC(1), IC(0)); }
                SCHED0(); });
            HSTAMP(s, 2)
            sfor<107>([&](auto I) { constexpr int i = CV(I), q = i < 8 ? i + 4 : i + 3;
                ymm(I);
                TPV(i + 193)
                if constexpr (i < 5 || (i >= 29 && i < 35)) TOUCH(v[(i < 5 ? i + 1 : i - 23) % 12]);
                if constexpr (i < 13 && i != 8) {
                    if constexpr (q % 3 == 1) { ym += 32768; PINS(ym); }
                    if constexpr (q % 3 == 2 && i >= 5) __builtin_amdgcn_s_setprio(0);
                    SCHED0();
                    if constexpr (q % 3 == 0) { st64(k.rs, vo, ym, (u32x2){fo[0], fo[1]}); SCHED0(); }
                    if constexpr (q < 15) fin2(IC(q / 3), IC(q % 3));
                }
                if constexpr (i == 13) done = rt_async();
                if constexpr (i >= 15 && i < 23 && i != 18 && i != 20) v[6 + i - 15 - (i > 18) - (i > 20)] = lr128(l16 + L_K + (6 + i - 15 - (i > 18) - (i > 20)) * 1024);
                xq(IC(i));
                if constexpr (i == XP - 6) X.next<15>(k, l16);
                if constexpr (i == XP - 1) { SCHED0(); X.next<7>(k, l16); }
                if constexpr (i == XP) X.next<12>(k, l16);
                if constexpr (i > XP && i <= XP + 18 && (i % 6 == 0 || i % 3 == 1)) X.op<(i - XP - 1) / 6, (i % 6 + 2) / 3>(k);
                { constexpr int xp[6] = {XB, XB + 1, XB + 6, XB + 11, XB + 12, XB + 18};
                  sfor<6>([&](auto Q) { if constexpr (xp[CV(Q)] == i) X.op<3 + CV(Q) / 3, CV(Q) % 3>(k); }); }
                if constexpr (i == 90) { ym = (c & 2) ? ym2 : ym0; PINS(ym); }
                if constexpr (i == 91) { ys = ym + EXP(327680); PINS(ys); }
                if constexpr (i == 96) { ym = (c & 1) ? ys : ym; PINS(ym); }
                if constexpr (i == 60) { HSTAMP(s, 3) }
                if constexpr (i == 42 || i == 43 || i == 48 || i == 49) { AAC((i - 42) / 3 + (i & 1)) }
                if constexpr (i == 102) lgkm0(done);
                if constexpr (i == 103) { PINS(now); HTRACE(c - 1) an = (unsigned)done - (unsigned)now; PINS(an); }
                if constexpr (i == 105) G.mx = vshl1(c, 1);
                SCHED0(); });
            AST(4)
        }
    } while (X.sg != 65u);
    HFLUSH(63)
    PROF_OUT
}

DEVI void row_role(const Params& p, const Ctx& k, char* smem, int rt) {
    PROF_DECL
    const int l = k.l, n = l & 15;
    const unsigned fo = k.w * 2048 + rt * 1024;
    RSTAMP(2)
    const __amdgpu_buffer_rsrc_t rs = __builtin_amdgcn_make_buffer_rsrc(k.c ? (void*)k.ws : (void*)p.x, 0, 0x7fffffff, 0x00020000);
    const unsigned vs = k.c ? l * 16 : n * 1280 + (l >> 4) * 16, cs = k.c ? 16384 : 256;
    unsigned ep = n * 32, dp = (l >> 4) == (n >> 2) ? n * 32 + k.w * 8 : 1024 + l * 8;
    const unsigned long long m1 = __ballot(n & 1), m2 = __ballot(n & 2);
    bf16x8 one8 = {0x3F80, 0x3F80, 0x3F80, 0x3F80, 0x3F80, 0x3F80, 0x3F80, 0x3F80};
    PIN(one8);
    unsigned fails = k.c ? 0u : 65536u;
    prog_init(k);
    for (int sg = k.c ? 0 : 8; sg < 64; ++sg) {
        unsigned src[5], dst = k.seg(PG_XNA, sg) + fo;
#pragma unroll
        for (int c = 0; c < 5; ++c) { src[c] = (k.c ? k.seg(PG_X2, sg) + fo : (sg * 32 + rt * 16) * 1280 + k.w * 64) + c * cs; PINS(src[c]); }
        PINS(dst);
        prog_wait<5>(k, k.p0 + CNT(C_X2, rt * 5), k.c ? sg : -1);
        STOP(sg) STAMP(sg, 0)
        u32x4 a[5];
        f32x4 sm, sq;
        f32x2 v[20];
        unsigned long long bad;
        do {
            asm volatile("" ::: "memory");
#pragma unroll
            for (int c = 0; c < 5; ++c) { a[c] = ld128(rs, vs, src[c]); SCHED0(); }
            unsigned mx = 0, lim = ~1u | (fails >> 16);
            ++fails; PINS(fails); PINS(lim);
            sfor<5>([&](auto C) { constexpr int c = CV(C);
                mfa<false, c == 0>(sq, __builtin_bit_cast(bf16x8, a[c]), __builtin_bit_cast(bf16x8, a[c])); mfa<false, c == 0>(sm, one8, __builtin_bit_cast(bf16x8, a[c]));
                mx = umax3(umax3(mx, a[c][0], a[c][1]), a[c][2], a[c][3]); PIN(mx);
                SCHED0(); });
            bad = __ballot(mx > lim);
        } while (__builtin_expect(bad != 0, 0));
        STAMP(sg, 1)
        auto unp = [&](int i0, int i1) {
#pragma unroll
            for (int i = i0; i < i1; ++i) { v[i] = (f32x2){blo(a[i >> 2][i & 3]), bhi(a[i >> 2][i & 3])}; PIN(v[i]); }
            SCHED0(); };
        unp(0, 3);
        asm volatile("" : "+v"(sq), "+v"(sm));
        lw64(dp, (u32x2){__float_as_uint(sm[0]), __float_as_uint(csel(csel(sq[0], sq[1], m1), csel(sq[2], sq[3], m1), m2))});
        SCHED0();
        unp(3, 10);
        RAWBAR()
        const f32x4 e0 = __builtin_bit_cast(f32x4, lr128(ep)), e1 = __builtin_bit_cast(f32x4, lr128(ep + 16));
        SCHED0();
        unp(10, 20);
        const f32x4 e = e0 + e1;
        float m = (e[0] + e[2]) * (1.f / 640.f);
        const float var = bfr(__builtin_fmaxf(__builtin_fmaf(-m, m, (e[1] + e[3]) * (1.f / 640.f)), 0.f));
        m = bfr(m);
        const float rden = __builtin_amdgcn_rcpf(bfr(__builtin_amdgcn_sqrtf(bfr(var + 1e-5f))));
#pragma unroll
        for (int c = 0; c < 5; ++c) {
            u32x4 o;
#pragma unroll
            for (int i = 0; i < 4; ++i) { const f32x2 x = v[c * 4 + i] - m; o[i] = cvtpk(bfr(x[0]) * rden, bfr(x[1]) * rden); }
            st128(k.rs, l * 16, dst + c * 16384, o);
        }
        STAMP(sg, 2)
        unsigned fv = sg - (k.c ? 0 : 8);
        PIN(fv);
        DRAIN();
        st32(k.rs, l == 0 ? 0u : OOB, k.p0 + CNT(C_XN, rt * 4 + k.w), fv);
        dp ^= 2048; ep ^= 2048;
        STAMP(sg, 3) STAMP(sg, 4) STAMP(sg, 5)
        PFLUSH(sg)
    }
    PROF_OUT
}

DEVI int red_col(int j) { const int w = j / 5, q = j % 5; return q < 3 ? 3 * w + q : 9 + 2 * w + q; }
struct Red {
    u32x4 Y[16];
    template <int I0, int I1> DEVI void yload(const Ctx& k, int j, int sg) {
        const unsigned a = k.yx(sg, j / 5) + (j % 5) * 131072 + k.w * 16384;
#pragma unroll
        for (int i = I0; i < I1; ++i) Y[i] = ld128(k.rs, k.l * 16 + (i & 3) * 1024, a + ((i >> 2) & 1) * 4096 + (i >> 3) * 65536);
    }
};
DEVI unsigned y_get(const Ctx& k) { return ld32(k.rs, (k.l & 7) * 128, k.p0 + YLINE((k.role >> 5) & 3, k.w * 8)); }
DEVI void y_wait(const Ctx& k, int s) {
_Pragma("unroll 1")
    for (unsigned spins = 0; !__all((int)y_get(k) >= s); ++spins) {
        asm volatile("" ::: "memory");
        __builtin_amdgcn_s_sleep(YS);
        if (spin_fail(k, spins)) break;
    }
    __builtin_amdgcn_fence(__ATOMIC_ACQUIRE, "wavefront");
}
DEVI void wait_until(unsigned t) { while ((int)((rclk() - t) << 16) < 0) { if (WUS) __builtin_amdgcn_s_sleep(WUS); } }
#define DPPMAX(x, c) x = max(x, __builtin_amdgcn_update_dpp(0, x, c, 0xf, 0xf, false))
#define TSLP 16
#define TWU 0
DEVI void wait_until2(unsigned t) { while ((int)((rclk() - t) << 16) < 0) { if (TWU) __builtin_amdgcn_s_sleep(TWU); } }
#define WAIT_BC(s, BSLP) { \
    unsigned bc; \
    _Pragma("unroll 1") for (;; ++spins) { \
        bc = ld32(k.rs, (l & 15) * 128, k.p0 + CNT(C_BC, w * 16)); \
        DRAIN(); \
        if (__all(((int)bc >> 16) > s)) break; \
        __builtin_amdgcn_s_sleep(BSLP); \
        if (spin_fail(k, spins)) break; \
    } \
    const unsigned now = rclk(); \
    int d = (int)((bc - now) << 16); \
    d = ((int)bc >> 16) > s + 1 || d > (256 << 16) ? -(64 << 16) : d; \
    DPPMAX(d, 0xB1); DPPMAX(d, 0x4E); DPPMAX(d, 0x141); \
    tq = now + (__builtin_amdgcn_readlane(d, 0) >> 16); tv = now + (__builtin_amdgcn_readlane(d, 8) >> 16); }
DEVI void mred_role(const Params& p, const Ctx& k, char* smem, int j) {
    PROF_DECL
    const int l = k.l, w = k.w, g = l >> 4, n = l & 15;
    const unsigned ix = SPIX(l);
    const unsigned vp = (g * 16 + ((n >> 1) & 1) * 8 + (n >> 2) * 2 + (n & 1)) * 16;
    const unsigned v16 = (g >> 1) * 1024 + (g & 1) * 256 + n * 16, yo = j * 32768 + EXP(w * 4096);
    const u32x4 ones = {0xFFFFFFFFu, 0xFFFFFFFFu, 0xFFFFFFFFu, 0xFFFFFFFFu};
    auto arm = [&](int s) {
#pragma unroll
        for (int i = 0; i < 4; ++i) st128(k.rs, l * 16 + i * 1024, k.ym(s) + yo, ones);
    };
    const unsigned pv = l == 0 && j % 5 == 0 ? (j / 5) * 128 + w * 32 : OOB;
    Red R;
    RSTAMP(2)
    arm(1);
#pragma unroll
    for (int i = 0; i < 8; ++i) st128(k.rs, (i || w > 1 || g > 1) ? g * 32768 + n * 16 : OOB, k.memf(i * 8 + (w >> 1) * 4) + EXP((2 * j + (w & 1)) * 256), ones);
#pragma unroll
    for (int i = 0; i < 4; ++i) { const unsigned q = min(i * 80 + j * 4 + w, 287); st128(k.rs, l * 16, k.p0 + PR_B + PR_SZ + EXP(q * 1024), ones); }
    sleep_until(k.t0 + RSL);
    y_wait(k, 2);
    R.yload<0, 16>(k, j, 0);
    for (int s = 0; s < 63; ++s) {
        STOP(s) HSTAMP(s, 0)
        const unsigned ap = k.p0 + PR_B + (s & 3) * PR_SZ + w * 49152, am = k.ym(s) + yo;
        const unsigned ph = (s & 4) ? 0x80008000u : 0u;
        unsigned spins = 0, tq, tv;
        unsigned fl;
        WAIT_BC(s, BSLP)
        st32(k.rs, pv, k.p0 + CNT(C_XB, 0), spack(((int)((tq - tv) << 16) < 0 ? tv : tq) + RDY, s + 2));
        STAMP(s, 5)
        char* rb = smem + (s & 1) * 4096;
_Pragma("unroll 1")
        for (spins = 0;; ++spins) {
            u32x4 ym[4], pm[5];
            f32x4 acc[2];
            if (spins == 0) wait_until(tv);
            asm volatile("" ::: "memory");
#pragma unroll
            for (int i = 0; i < 4; ++i) ym[i] = ld128(k.rs, v16 + (i >> 1) * 2048 + (i & 1) * 512, am);
            fl = ld32(k.rs, 0, (k.p0 ^ 8192u) + CNT(C_X1, j));
            asm volatile("" ::: "memory");
            if (spins == 0) { wait_until(tq); STAMP(s, 1) }
            asm volatile("" ::: "memory");
#pragma unroll
            for (int i = 0; i < 5; ++i) pm[i] = ld128(k.rs, i < 4 ? l * 16 + i * 1024 : vp, ap + (i >> 2) * 4096);
            acc[0] = (f32x4){0.f, 0.f, 0.f, 0.f}; acc[1] = acc[0];
            const u32x8 y0 = {ym[0][0], ym[1][0], ym[0][2], ym[1][2], ym[2][0], ym[3][0], ym[2][2], ym[3][2]};
            const u32x8 y1 = {ym[0][1], ym[1][1], ym[0][3], ym[1][3], ym[2][1], ym[3][1], ym[2][3], ym[3][3]};
            unsigned mx = 0;
#pragma unroll
            for (int i = 0; i < 4; ++i) mx = max(mx, umax4(ym[i]));
            unsigned tg = mx == 0xFFFFFFFFu ? 0x80000000u : 0u;
            asm volatile("s_nop 1" : "+v"(acc[0]), "+v"(acc[1]), "+v"(tg) : "v"(y0), "v"(y1), "v"(ix));
            SCHED0();
#pragma unroll
            for (int i = 0; i < 5; ++i) {
                const unsigned u0 = __builtin_amdgcn_bitop3_b32(pm[i][0], pm[i][1], ph, 0x7e), u1 = __builtin_amdgcn_bitop3_b32(pm[i][2], pm[i][3], ph, 0x7e);
                SCHED0();
                if (i < 4) smf<false>(acc[0], __builtin_bit_cast(bf16x8, pm[i]), __builtin_bit_cast(bf16x16, __builtin_shufflevector(R.Y[2 * i], R.Y[2 * i + 1], 0, 1, 2, 3, 4, 5, 6, 7)), ix);
                else smf<false>(acc[0], __builtin_bit_cast(bf16x8, pm[4]), __builtin_bit_cast(bf16x16, y0), ix);
                tg = (tg | u0) | u1;
                SCHED0();
                if (i < 4) smf<false>(acc[1], __builtin_bit_cast(bf16x8, pm[i]), __builtin_bit_cast(bf16x16, __builtin_shufflevector(R.Y[8 + 2 * i], R.Y[9 + 2 * i], 0, 1, 2, 3, 4, 5, 6, 7)), ix);
                else smf<false>(acc[1], __builtin_bit_cast(bf16x8, pm[4]), __builtin_bit_cast(bf16x16, y1), ix);
                SCHED0();
            }
            const unsigned long long bad = __ballot((int)tg < 0);
            asm volatile("s_nop 5" : "+v"(acc[0]), "+v"(acc[1]) : "s"(bad));
#pragma unroll
            for (int t = 0; t < 2; ++t) { float* rp = (float*)(rb + t * 2048 + w * 512 + g * 128 + n * 4); rp[0] = acc[t][0] + acc[t][2]; rp[16] = acc[t][1] + acc[t][3]; }
            if (bad == 0) break;
            PCOUNT(0)
            asm volatile("" ::: "memory");
            if (spin_fail(k, spins)) break;
        }
        STAMP(s, 2)
        RAWBAR()
        if (w < 2) {
            const char* rp = rb + w * 2048 + ((l >> 4) * 2 + ((l >> 2) & 1)) * 64 + ((l & 3) * 4 + ((l >> 3) & 1) * 2) * 4;
            const f32x2 a = *(const f32x2*)rp, b = *(const f32x2*)(rp + 512), c = *(const f32x2*)(rp + 1024), d = *(const f32x2*)(rp + 1536);
            const f32x2 r = (a + b) + (c + d);
            st32(k.rs, l * 4, k.memf(s + 1) + EXP((2 * red_col(j) + w) * 256), min(cvtpk(r[0], r[1]) ^ ph, 0xFFFFFFFEu));
        }
        STAMP(s, 3)
_Pragma("unroll 1")
        for (spins = 0; (int)__builtin_amdgcn_readfirstlane(fl) < s - 1; ++spins) {
            PCOUNT(1)
            __builtin_amdgcn_s_sleep(PSLEEP);
            if (spin_fail(k, spins)) break;
            fl = ld32(k.rs, 0, (k.p0 ^ 8192u) + CNT(C_X1, j));
        }
        __builtin_amdgcn_fence(__ATOMIC_ACQUIRE, "wavefront");
        if (Y1) wait_until(tv + Y1);
        y_wait(k, 2 * s + 3);
        R.yload<0, 8>(k, j, s + 1);
        if (ARMP == 0) arm(s + 2);
        if (Y2) wait_until(tv + Y2);
        y_wait(k, 2 * s + 4);
        STAMP(s, 4)
        if (ARMP == 2) { arm(s + 2); SCHED0(); }
        R.yload<8, 16>(k, j, s + 1);
        if (ARMP == 1) { SCHED0(); arm(s + 2); }
        PFLUSH(s)
    }
    PFLUSH(63)
    PROF_OUT
}
DEVI void tred_role(const Params& p, const Ctx& k, char* smem, int j) {
    PROF_DECL
    const int l = k.l, w = k.w, g = l >> 4, n = l & 15, T = 2 * red_col(j) + (w >> 1), rt = w & 1;
    const unsigned v16 = (g >> 1) * 1024 + (g & 1) * 256 + n * 16, yo = j * 32768 + EXP(w * 4096);
    const unsigned xo = EXP((T >> 1) * 2048) + (T & 1) * 512 + rt * 1024, xv = ((g >> 1) * 16 + n) * 16 + (g & 1) * 8;
    const u32x2 ones = {0xFFFFFFFFu, 0xFFFFFFFFu};
    const unsigned fl = (w == 0 && l == 0) ? 0u : OOB;
    unsigned fails = 0;
    Red R;
    RSTAMP(2)
    st64(k.rs, xv, k.segx(PG_X1, 0) + xo, ones); st64(k.rs, xv, k.segx(PG_X1, 1) + xo, ones);
    sleep_until(k.t0 + RSL);
    y_wait(k, 2);
    R.yload<0, 16>(k, j, 0);
    for (int s = 0; s < 64; ++s) {
        STOP(s) STAMP(s, 0)
        unsigned ap[4], am = k.ym(s) + yo, xa = k.segx(PG_X1, s) + xo, rb = (s & 1) * 16384 + l * 16, wb = rb + w * 1024, fv = s;
#pragma unroll
        for (int i = 0; i < 4; ++i) { ap[i] = k.p0 + PR_B + (s & 3) * PR_SZ + w * 49152 + (i < 2 ? 32768 : 16384) + (~i & 1) * 4096; PINS(ap[i]); }
        rb += w * 4096;
        PINS(am); PINS(xa); PIN(rb); PIN(wb); PIN(fv);
        const unsigned ph = (s & 4) ? 0x80008000u : 0u;
        unsigned spins = 0, tq, tv;
        u32x2 res;
        if (k.c) res = ld64(k.rs, xv, k.seg(PG_X2, s) + xo);
        else res = *(const u32x2*)(p.x + (size_t)(s * 32 + rt * 16 + n) * 640 + T * 16 + g * 4);
        f32x4 acc[2][2];
        WAIT_BC(s, TSLP)
        u32x4 yl[8];
#pragma unroll
        for (int i = 0; i < 8; ++i) { yl[i] = __builtin_shufflevector(R.Y[2 * i], R.Y[2 * i + 1], 0, 1, 4, 5); PIN(yl[i]); }
        f32x4 x = {blo(res[0] ^ ph), bhi(res[0] ^ ph), blo(res[1] ^ ph), bhi(res[1] ^ ph)};
        PIN(x);
        wait_until2(tv); wait_until2(tq + (s < 63 ? RDLY : 0));
        STAMP(s, 1)
        unsigned long long bad;
        do {
            asm volatile("" ::: "memory");
            u32x4 ym[4], pp[16];
#pragma unroll
            for (int i = 0; i < 4; ++i) { ym[i] = ld128(k.rs, v16 + (i >> 1) * 2048 + (i & 1) * 512, am); SCHED0(); }
#pragma unroll
            for (int i = 0; i < 16; ++i) { pp[i ^ 4] = ld128(k.rs, l * 16 + (i & 3) * 1024, ap[i >> 2]); SCHED0(); }
            unsigned mx = 0, tg, lim = 0x7FFFFFFFu | (fails << 15);
            ++fails; PINS(fails); PINS(lim);
#pragma unroll
            for (int i = 0; i < 4; ++i) mx = max(mx, umax4(ym[i]));
            tg = mx == 0xFFFFFFFFu ? 0x80000000u : 0u;
            u32x4 yf[4];
#pragma unroll
            for (int i = 0; i < 4; ++i) { const int t = i & 1, f = i >> 1; yf[i] = (u32x4){ym[2 * f][t], ym[2 * f][t + 2], ym[2 * f + 1][t], ym[2 * f + 1][t + 2]}; }
            asm volatile("s_nop 1" : "+v"(yf[0]), "+v"(yf[1]), "+v"(yf[2]), "+v"(yf[3]), "+v"(tg));
            SCHED0();
            sfor<32>([&](auto I) { constexpr int i = CV(I), t = i & 1, q = (i >> 1) ^ 4, r = q < 4 ? 0 : q < 8 ? (q >> 1) & 1 : 1;
                mfa<false, q == 4 || q == 6>(acc[t][r], __builtin_bit_cast(bf16x8, q < 4 ? yl[t * 4 + q] : q < 8 ? yf[(q & 1) * 2 + t] : R.Y[t * 8 + (q & 7)]), __builtin_bit_cast(bf16x8, pp[q]));
                SCHED0();
                tg |= __builtin_amdgcn_bitop3_b32(pp[q][2 * t], pp[q][2 * t + 1], ph, 0x7e); PIN(tg);
                if constexpr (i == 20 || i == 22) { asm volatile("" : "+v"(acc[(i - 20) / 2][0])); lw128(wb + (i - 20) * 4096, __builtin_bit_cast(u32x4, acc[(i - 20) / 2][0])); }
                if constexpr (i == TPOS) st32(k.rs, fl, (k.p0 ^ 8192u) + CNT(C_X1, j), fv);
                SCHED0(); });
            bad = __ballot(tg > lim);
        } while (__builtin_expect(bad != 0, 0));
        STAMP(s, 2)
        const unsigned yc = y_get(k);
        SCHED0();
        asm volatile("s_nop 2" : "+v"(acc[0][1]), "+v"(acc[1][1]));
        lw128(wb + 4096, __builtin_bit_cast(u32x4, acc[0][1])); lw128(wb + 12288, __builtin_bit_cast(u32x4, acc[1][1]));
        RAWBAR()
        {
            const f32x4 r0 = __builtin_bit_cast(f32x4, lr128(rb)), r1 = __builtin_bit_cast(f32x4, lr128(rb + 1024)), r2 = __builtin_bit_cast(f32x4, lr128(rb + 2048)), r3 = __builtin_bit_cast(f32x4, lr128(rb + 3072));
            SCHED0();
            if constexpr (TPOS == 32) st32(k.rs, fl, (k.p0 ^ 8192u) + CNT(C_X1, j), fv);
            SCHED0();
            const f32x4 r = (r0 + r1) + (r2 + r3);
            st64(k.rs, xv, xa, (u32x2){min(cvtpk(x[0] + bfr(r[0]), x[1] + bfr(r[1])) ^ ph, 0xFFFFFFFEu), min(cvtpk(x[2] + bfr(r[2]), x[3] + bfr(r[3])) ^ ph, 0xFFFFFFFEu)});
            SCHED0();
            if constexpr (TPOS == 33) st32(k.rs, fl, (k.p0 ^ 8192u) + CNT(C_X1, j), fv);
        }
        st64(k.rs, s < 62 ? xv : OOB, k.segx(PG_X1, (s + 2) & 63) + xo, ones);
        STAMP(s, 3)
        if (s < 63) {
            if (!__all((int)yc >= 2 * s + 4)) { PCOUNT(2) y_wait(k, 2 * s + 4); }
            __builtin_amdgcn_fence(__ATOMIC_ACQUIRE, "wavefront");
            R.yload<0, 16>(k, j, s + 1);
        }
        STAMP(s, 4)
        STAMP(s, 5)
        PFLUSH(s)
    }
    PROF_OUT
}

#undef TZ
#define TZ z0()
DEVI void up_role(const Params& p, const Ctx& k, char* smem, int id) {
    PROF_DECL
    const int l = k.l, w = k.w, g = l >> 4, n = l & 15;
    bf16x8 Wt[80];
    const int tile0 = id * 8 + w * 2;
    auto issue = [&](auto U) { constexpr int u = CV(U);
        sfor<8>([&](auto J) { constexpr int j = CV(J); dma1<(u % 3) * 32768 + j * 1024>(((w & 1) ? p.wu : p.wg) + (size_t)(2 * u + (w >> 1)) * 32 * 1536 + id * 128, wsrc<256, true>(j * 64 + l, 3072), w * 8192); }); };
    sfor<3>(issue);
    cstream<10>([](auto V) { return IC(CV(V) < 9 ? 8 : 0); },
        [&](auto U) { constexpr int u = CV(U);
            if constexpr (u > 0 && u < 8) issue(IC(u + 2));
#pragma unroll
            for (int i = 0; i < 8; ++i) Wt[(i & 3) * 20 + 2 * u + (i >> 2)] = wp_read<256, true>(smem + (u % 3) * 32768 + (i >> 1) * 8192, w * 2 + (i & 1), l); });
    RAWBAR()
    RSTAMP(2)
    bf16x8 one8 = {0x3F80, 0x3F80, 0x3F80, 0x3F80, 0x3F80, 0x3F80, 0x3F80, 0x3F80};
    asm volatile("s_nop 1" : "+v"(one8));
    f32x4 cs[4];
    sfor<80>([&](auto I) { constexpr int f = CV(I); mfa<(f < 64), f % 20 == 0>(cs[f / 20], Wt[f], one8); });
    asm volatile("s_nop 7" : "+v"(cs[0]), "+v"(cs[1]), "+v"(cs[2]), "+v"(cs[3]));
    const unsigned ho = EXP((tile0 >> 1) * 1024), l16 = l * 16;
    constexpr int L_ST = 81920;
    const u32x4 ones = {0xFFFFFFFFu, 0xFFFFFFFFu, 0xFFFFFFFFu, 0xFFFFFFFFu};
#pragma unroll
    for (int i = 0; i < 4; ++i) st128(k.rs, l16, k.hseg(i >> 1) + (i & 1) * EXP(49152) + ho, ones);
    unsigned fails = 0;
    lw32(L_ST + 1024, 0u);
    prog_init(k);
    const unsigned fl = (w == 0 && l == 0) ? 0u : OOB;
    const unsigned long long m1 = __ballot(n & 1), m2 = __ballot(n & 2);
    for (int s = 0; s < 64; ) {
        unsigned xs = k.seg(PG_X1, s) + w * 2048, xd = (s & 1) * 40960 + w * 2048;
        PINS(xs); PINS(xd);
        prog_wait<20>(k, k.p0 + CNT(C_X1, 0), s);
        STOP(s) STAMP(s, 0)
        xn_all(k, l16, xs, xd);
        const unsigned xb = (s & 1) * 40960 + l16, yb = xb + w * 2048, sp = L_ST + (s & 1) * 2048, rp = sp + n * 16;
        const unsigned dp = sp + (g == (n >> 2) ? n * 16 + w * 4 : 4096 + l * 4);
        f32x4 acc[2][4], st[4];
        bf16x8 x[20], x1[20], y0[5], y1[5];
        unsigned mx = 0;
        auto blk = [&](auto B, auto J) { constexpr int b = CV(B), j = CV(J);
            if constexpr (j == 0) asm volatile("s_waitcnt vmcnt(%0)\n\ts_barrier" :: "n"(8 - 2 * b) : "memory");
            if constexpr (j >= 1 && j < 5) x[4 * b + j - 1] = lr128(xb + (4 * b + j - 1) * 2048);
            if constexpr (j == 5) y0[b] = lr128(yb + b * 8192);
            if constexpr (j == 6) y1[b] = lr128(yb + b * 8192 + 1024); };
        sfor<7>([&](auto J) { blk(IC(0), J); });
        SCHED0();
        STAMP(s, 1)
        float mean[2], rden[2], e[4][4];
        f32x4 sa[2], sb[2];
        u32x4 bad, o[2];
        unsigned sv, fv, r, f0, f1, ha = k.hseg(s) + ho;
        PINS(ha);
        auto stat = [&](auto I) { constexpr int i = CV(I), rt = i / 8, j = i % 8;
            if constexpr (j == 0) asm volatile("v_add_f32 %0, %2, %3\n\tv_add_f32 %1, %4, %5" : "=&v"(mean[rt]), "=&v"(rden[rt]) : "v"(sa[rt][0]), "v"(sa[rt][1]), "v"(sb[rt][0]), "v"(sb[rt][1]));
            if constexpr (j == 1 || j == 2) asm volatile("v_add_f32 %0, %0, %2\n\tv_add_f32 %1, %1, %3" : "+v"(mean[rt]), "+v"(rden[rt]) : "v"(sa[rt][j + 1]), "v"(sb[rt][j + 1]));
            if constexpr (j == 3) asm volatile("v_mul_f32 %0, 0xbaccccCD, %0\n\tv_mul_f32 %1, 0x3acccccd, %1" : "+v"(mean[rt]), "+v"(rden[rt]));
            if constexpr (j == 4) asm volatile("v_fma_f32 %1, -%0, %0, %1\n\tv_cvt_pk_bf16_f32 %0, 0, %0\n\tv_max_f32 %1, 0, %1" : "+v"(mean[rt]), "+v"(rden[rt]));
            if constexpr (j == 5) asm volatile("v_cvt_pk_bf16_f32 %0, 0, %0\n\tv_add_f32 %0, 0x3727c5ac, %0\n\tv_cvt_pk_bf16_f32 %0, 0, %0" : "+v"(rden[rt]));
            if constexpr (j == 6) asm volatile("v_sqrt_f32 %0, %0" : "+v"(rden[rt]));
            if constexpr (j == 7) asm volatile("v_cvt_pk_bf16_f32 %0, 0, %0\n\tv_rcp_f32 %0, %0" : "+v"(rden[rt])); };
        auto ep = [&](auto RT, auto T, auto J) { constexpr int rt = CV(RT), t = CV(T), j = CV(J), c = (j & 1) * 2, u = j >= 20, q = u ? j - 20 : j;
            float (&a)[4] = e[t + 2 * u];
#define A2 "+v"(a[c]), "+v"(a[c + 1])
            if constexpr (j < 0 || j >= 30) { }
            else if constexpr (q < 2) asm volatile("v_fma_f32 %0, %2, %3, %5\n\tv_fma_f32 %1, %2, %4, %6" : "=&v"(a[c]), "=&v"(a[c + 1]) : "v"(mean[rt]), "v"(cs[t + 2 * u][c]), "v"(cs[t + 2 * u][c + 1]), "v"(acc[rt][t + 2 * u][c]), "v"(acc[rt][t + 2 * u][c + 1]));
            else if constexpr (q < 4) asm volatile("v_mul_f32 %0, %0, %2\n\tv_mul_f32 %1, %1, %2" : A2 : "v"(rden[rt]));
            else if constexpr (q < 6 || j == 18 || j == 19) asm volatile("v_cvt_pk_bf16_f32 %0, 0, %0\n\tv_cvt_pk_bf16_f32 %1, 0, %1" : A2);
            else if constexpr (j < 8) asm volatile("v_mul_f32 %0, 0xbfb8aa3b, %0\n\tv_mul_f32 %1, 0xbfb8aa3b, %1" : A2);
            else if constexpr (j < 12) asm volatile("v_exp_f32 %0, %0" : "+v"(a[j & 3]));
            else if constexpr (j < 14) asm volatile("v_add_f32 %0, 1.0, %0\n\tv_add_f32 %1, 1.0, %1" : A2);
            else if constexpr (j < 18) asm volatile("v_rcp_f32 %0, %0" : "+v"(a[(j - 14) & 3]));
            else if constexpr (q < 8) asm volatile("v_mul_f32 %0, %0, %2\n\tv_mul_f32 %1, %1, %3" : A2 : "v"(e[t][c]), "v"(e[t][c + 1]));
            else if constexpr (q == 8) asm volatile("v_cvt_pk_bf16_f32 %0, %0, %2\n\tv_cvt_pk_bf16_f32 %1, %1, %3" : "+v"(a[0]), "+v"(a[2]) : "v"(a[1]), "v"(a[3]));
            else { unsigned d0, d1; asm volatile("v_min_u32 %0, -2, %2\n\tv_min_u32 %1, -2, %3" : "=&v"(d0), "=&v"(d1) : "v"(a[0]), "v"(a[2])); o[rt][2 * t] = d0; o[rt][2 * t + 1] = d1; } };
        sfor<180>([&](auto I) {
            constexpr int a = CV(I), b = a / 8, j = (a % 8) ^ (b == 4 ? 4 : 0), ch = a / 20 - 1, ks = a < 40 ? 4 * b + (j & 3) : a % 20, rt = ch >= 4;
            constexpr int nt = a < 40 ? 0 : rt ? ch - 4 : (ch & 1) * 2 + (ch >> 1), f = nt * 20 + ks, i = a - 36;
            if constexpr (a >= 40 || j < 4) mfa<(f < 64), ks == 0>(acc[rt][nt], Wt[f], rt ? x1[ks] : x[ks]);
            else mfa<false, b == 0>(st[j & 3], (j & 1) ? (j & 2 ? y1[b] : y0[b]) : one8, j & 2 ? y1[b] : y0[b]);
            SCHED0();
            if constexpr (a >= 103 && a < 119) TOUCH(x1[(a - 99) % 20]);
            else if constexpr (a == 60) { r = __builtin_amdgcn_readfirstlane(bad[0]); PINS(r); }
            else if constexpr (a == 61) { r = (fails - r) >> 31; PINS(r); }
            else if constexpr (a == 62) { fails += r; PINS(fails); }
            else if constexpr (a == 63) { f0 = 2 * s - r; PINS(f0); }
            else if constexpr (a == 64) { f1 = f0 | 1; PINS(f1); }
            else if constexpr (a == 65) { r <<= 31; PINS(r); }
            else if constexpr (a >= 56 && (a < 92 || a >= 119)) __builtin_amdgcn_s_setprio(0);
            if constexpr (a < 32 && a % 8 == 7) asm volatile("" :: "v"(x[4 * b + 4]), "v"(x[4 * b + 5]), "v"(x[4 * b + 6]), "v"(x[4 * b + 7]), "v"(y0[b + 1]));
            if constexpr (a < 32 && a % 8 == 4) TOUCH(y1[b]);
            SCHED0();
            if constexpr (a == 15) lw32((rp ^ 2048) + 1024, z0());
            if constexpr (a < 32 && a % 8 < 7) blk(IC((b + 1) % 5), IC(a % 8));
            if constexpr (i == 0 || i == 4 || i == 5 || i == 8) x1[i / 2 - (i == 4 || i == 8)] = lr128(xb + 1024 + (i / 2 - (i == 4 || i == 8)) * 2048);
            if constexpr (a >= 92 && a < 108) x1[a - 88] = lr128(xb + 1024 + (a - 88) * 2048);
            SCHED0();
            if constexpr (a < 40 && j >= 4) { const u32x4 v = __builtin_bit_cast(u32x4, (j & 1) ? y1[b] : y0[b]); mx = umax3(mx, v[j & 2], v[(j & 2) + 1]); PIN(mx); }
            if constexpr (i == 1 || i == 3) {
                constexpr int rt2 = i == 3;
                asm volatile("" : "+v"(st[2 * rt2]), "+v"(st[2 * rt2 + 1]));
                const f32x4& q = st[2 * rt2 + 1];
                const float d = csel(csel(q[0], q[1], m1), csel(q[2], q[3], m1), m2);
                lw32(dp + rt2 * 512, __float_as_uint(st[2 * rt2][0])); lw32(dp + rt2 * 512 + 256, __float_as_uint(d));
            }
            if constexpr (i == 2) lw32(mx == 0xFFFFFFFFu ? sp + 1024 : dp + 4096, 4096u);
            if constexpr (i == 6) bar_lgkm<2>();
            if constexpr (i == 6) sa[0] = __builtin_bit_cast(f32x4, lr128(rp));
            if constexpr (i == 7) sb[0] = __builtin_bit_cast(f32x4, lr128(rp + 256));
            if constexpr (i == 9) sa[1] = __builtin_bit_cast(f32x4, lr128(rp + 512));
            if constexpr (i == 10) sb[1] = __builtin_bit_cast(f32x4, lr128(rp + 768));
            if constexpr (i == 11) bad = __builtin_bit_cast(u32x4, lr128(rp + 1024));
            if constexpr (i >= 12 && i < 20) stat(IC(i - 12));
            if constexpr (a == 40) { STAMP(s, 2) }
            if constexpr (a == UPOS0 || a == UPOS) st32(k.rs, fl, k.p0 + CNT(C_H, id), fv);
            if constexpr (a == 117) __builtin_amdgcn_raw_buffer_store_b128(o[0], k.rs, (int)sv, (int)ha, AUX_SC1);
            if constexpr (a == 118) TOUCH(o[0]);
            SCHED0();
            ep(IC(0), IC(0), IC(a - 56)); ep(IC(0), IC(1), IC(a - 86));
            if constexpr (a >= 116 && a < 124) stat(IC(a - 108));
            if constexpr (a == UPOS0 - 2 || a == UPOS - 2) { fv = a == UPOS - 2 ? f1 : f0; PIN(fv); }
            if constexpr (a == 80) { sv = l16 | r; PIN(sv); }
            if constexpr (a < 144) ep(IC(1), IC(0), IC(a - 124));
            else if constexpr (a < 164) ep(IC(1), IC(1), IC(a - 144));
            else ep(IC(1), IC(0), IC(a - 144));
            SCHED0();
        });
        STAMP(s, 3)
        asm volatile("s_nop 7" : "+v"(acc[1][3]));
        {
            const f32x4 u = (acc[1][3] + mean[1] * cs[3]) * rden[1];
            o[1][2] = packs(bfr(u[0]) * e[1][0], bfr(u[1]) * e[1][1]); o[1][3] = packs(bfr(u[2]) * e[1][2], bfr(u[3]) * e[1][3]);
        }
        st128(k.rs, sv, ha + EXP(49152), o[1]);
        if (r) { PCOUNT(0) RAWBAR() lw32(rp + 1024, 0u); RAWBAR() }
        st128(k.rs, s < 62 ? l16 : OOB, k.hseg((s + 2) & 63) + ho, ones); st128(k.rs, s < 62 ? l16 : OOB, k.hseg((s + 2) & 63) + EXP(49152) + ho, ones);
        STAMP(s, 4) STAMP(s, 5)
        PFLUSH(s)
        s = (f1 + 1) >> 1;
    }
    PROF_OUT
}

DEVI void down_role(const Params& p, const Ctx& k, char* smem, int id) {
    PROF_DECL
    const int l = k.l, w = k.w, g = l >> 4, n = l & 15, rt = id / 5, cg = id % 5;
    bf16x8 Wt[96];
    wstream<12, 8, 4>(smem + w * 32768,
        [&](auto U, unsigned sl) { constexpr int u = decltype(U)::value; sfor<8>([&](auto J) { constexpr int j = CV(J); dma1<(u % 4) * 8192 + j * 1024>(p.wd + (size_t)(w * 12 + u) * 32 * 640 + cg * 128, wsrc<256, false>(j * 64 + l, 1280), sl); }); },
        [&](auto U, char* sp) { constexpr int u = decltype(U)::value;
#pragma unroll
            for (int nt = 0; nt < 8; ++nt) Wt[u * 8 + nt] = wp_read<256, false>(sp, (nt + 2 * w) & 7, l); });
    RAWBAR()
    RSTAMP(2)
    const unsigned xo = EXP((cg * 4 + w) * 2048) + rt * 1024, xv = ((g >> 1) * 16 + n) * 16 + (g & 1) * 8;
    const u32x2 ones = {0xFFFFFFFFu, 0xFFFFFFFFu};
    const unsigned x2v = k.c ? xv : OOB;
    prog_init(k);
#pragma unroll
    for (int i = 0; i < 4; ++i) st64(k.rs, x2v, k.seg(PG_X2, i >> 1) + xo + (i & 1) * 512, ones);
    const __amdgpu_buffer_rsrc_t ro = __builtin_amdgcn_make_buffer_rsrc((void*)p.out, 0, 2048 * 1280, 0x00020000);
    const unsigned ov = k.c ? OOB : n * 1280 + g * 8, lim = k.c ? 0xFFFFFFFEu : 0xFFFFFFFFu, fl = (w == 0 && l == 0) ? 0u : OOB;
    unsigned fails = 0;
    for (int s = 0; s < 64; ++s) {
        unsigned la[3], ra = k.seg(PG_X1, s) + xo, oa = k.c ? k.seg(PG_X2, s) + xo : (s * 32 + rt * 16) * 1280 + (cg * 8 + w * 2) * 32, rb = (s & 1) * 32768 + l * 16, wb[3], fv = s;
#pragma unroll
        for (int q = 0; q < 3; ++q) { la[q] = k.hseg(s) + rt * EXP(49152) + EXP((w * 12 + q * 4) * 1024); wb[q] = rb + ((w + q + 1) & 3) * 6144 + q * 2048; PINS(la[q]); PIN(wb[q]); }
        rb += w * 6144;
        PINS(ra); PINS(oa); PIN(rb); PIN(fv);
        prog_wait<12>(k, k.p0 + CNT(C_H, 0), 2 * s + rt);
        STOP(s) STAMP(s, 0)
        f32x4 acc[8], r[6];
        u32x2 res[2];
        u32x4 hx[12];
        unsigned long long bad;
        do {
            asm volatile("" ::: "memory");
#pragma unroll
            for (int i = 0; i < 12; ++i) { hx[i] = ld128(k.rs, l * 16 + (i & 3) * 1024, la[i >> 2]); SCHED0(); }
#pragma unroll
            for (int t = 0; t < 2; ++t) res[t] = ld64(k.rs, xv + t * 512, ra);
            SCHED0();
            unsigned mx = 0;
            sfor<72>([&](auto I) { constexpr int i = CV(I), ks = i / 6, j = 2 + i % 6, f = ks * 8 + j;
                mfa<(f < 64), ks == 0>(acc[j], Wt[f], __builtin_bit_cast(bf16x8, hx[ks]));
                SCHED0();
                if constexpr (i % 6 < 2) { mx = umax3(mx, hx[ks][(i & 1) * 2], hx[ks][(i & 1) * 2 + 1]); PIN(mx); }
                if constexpr (i == 68) { bad = __ballot(mx == 0xFFFFFFFFu); PINS(bad); }
                if constexpr (i == 69) { bad = fails < 65536u ? bad : 0; PINS(bad); }
                if constexpr (i == 70) { ++fails; PINS(fails); }
                if constexpr (i == DPOS) st32(k.rs, fl, k.p0 + CNT(C_X2, rt * 5 + cg), fv);
                SCHED0(); });
        } while (__builtin_expect(bad != 0, 0));
        STAMP(s, 1)
        float x[8];
        sfor<24>([&](auto I) { constexpr int i = CV(I), ks = i / 2, j = i & 1, f = ks * 8 + j;
            mfa<(f < 64), ks == 0>(acc[j], Wt[f], __builtin_bit_cast(bf16x8, hx[ks]));
            SCHED0();
            if constexpr (i < 12 && !(i & 1)) { constexpr int jj = 2 + i / 2;
                asm volatile("" : "+v"(acc[jj])); lw128(wb[jj / 2 - 1] + (jj & 1) * 1024, __builtin_bit_cast(u32x4, acc[jj])); }
            if constexpr (i < 8 && (i & 1)) { x[i - 1] = blo(res[i >> 2][(i >> 1) & 1]); x[i] = bhi(res[i >> 2][(i >> 1) & 1]); PIN(x[i - 1]); PIN(x[i]); }
            if constexpr (i == 13) bar_lgkm<0>();
            if constexpr (i >= 13 && i < 19) r[i - 13] = __builtin_bit_cast(f32x4, lr128(rb + (i - 13) * 1024));
            if constexpr (i + 72 == DPOS) st32(k.rs, fl, k.p0 + CNT(C_X2, rt * 5 + cg), fv);
            SCHED0(); });
        r[0] += r[2] + r[4]; r[1] += r[3] + r[5];
        asm volatile("s_nop 3" : "+v"(acc[0]), "+v"(acc[1]), "+v"(r[0]), "+v"(r[1]));
#pragma unroll
        for (int t = 0; t < 2; ++t) {
            const f32x4 d = acc[t] + r[t];
            const u32x2 o = {min(cvtpk(x[4 * t] + bfr(d[0]), x[4 * t + 1] + bfr(d[1])), lim), min(cvtpk(x[4 * t + 2] + bfr(d[2]), x[4 * t + 3] + bfr(d[3])), lim)};
            st64(k.rs, x2v + t * 512, oa, o);
            __builtin_amdgcn_raw_buffer_store_b64(o, ro, (int)(ov + t * 32), (int)oa, 0);
        }
        STAMP(s, 2)
#pragma unroll
        for (int t = 0; t < 2; ++t) st64(k.rs, s < 62 ? x2v : OOB, k.seg(PG_X2, (s + 2) & 63) + xo + t * 512, ones);
        STAMP(s, 3) STAMP(s, 4) STAMP(s, 5)
        PFLUSH(s)
    }
    PROF_OUT
}

DEVI void warm(const Params& p, const Ctx& k, int xl, int hw) {
    set_m0(k.w * 256);
_Pragma("unroll 1")
    for (int it = hw; it < 32 * 15; it += k.c ? 40 : 64) {
        const int q = it < 120 ? it & 7 : 8 + (it - 120) % 24, t = it < 120 ? it >> 3 : (it - 120) / 24;
        const u16* b = q < 24 ? (q < 8 ? p.Wv : q < 16 ? p.Wk : p.Wq) + (xl * 8 + (q & 7)) * 192 : p.Wo + (xl * 8 + (q & 7)) * 122880;
        const unsigned ln = t * 64 + k.l, cl = min(((unsigned)(size_t)b >> 7) & 7u, 4u);
        if (k.c) sleep_until(k.t0 + WP + it * PP / 128);
        if (t < (q >= 24 ? PO : ((q < 8 ? PV : PQ) >> (cl * 4)) & 15))
            asm volatile("global_load_lds_dword %0, %1" :: "v"(q < 24 ? (639 - ln / 3) * 12288 + (ln % 3) * 128 : (191 - (ln >> 2)) * 1280 + 768 + (ln & 3) * 128), "s"(b) : "memory");
    }
    DRAIN();
_Pragma("unroll 1")
    for (int i = 3; i < 8; ++i) { sleep_until(k.t0 + i * 50); hello(k, 0); }
    DRAIN();
    RAWBAR()
}

DEVI int xcc_id() { return __builtin_amdgcn_s_getreg(20 | (0 << 6) | (3 << 11)) & 7; }

DEVI unsigned prepare(const Params& p, Ctx& k, char* smem, int xl, int i) {
    const int l = k.l, j = xl * 22 + i - 4;
    const u32x4 ones = {0xFFFFFFFFu, 0xFFFFFFFFu, 0xFFFFFFFFu, 0xFFFFFFFFu};
    unsigned vote = 0;
    if (i < 4) {
        const int row = ((i * 4 + xl) * 4 + k.w) * 4 + (l >> 4);
        const unsigned dst = k.seg(PG_XNA, i * 2 + (xl >> 1));
        u32x4 a[5];
#pragma unroll
        for (int c = 0; c < 5; ++c) a[c] = *(const u32x4*)(p.x + (size_t)row * 640 + (c * 16 + (l & 15)) * 8);
        ln_rows(a, l & 15, row & 31, [&](unsigned vo, int c, u32x4 o) { st128(k.rs, vo, dst + c * 16384, o); st128(k.rs, vo, dst + c * 16384 + 8192, o); });
    } else if (k.w == 3) {
        const unsigned cal = ((unsigned)(j % 44) << 21) + 7 * 16384 + (xl >> 1) * 4096 + (l & 7) * 16;
        int d = 0;
#pragma unroll 1
        for (int r = 0; r <= 2 * CALR; ++r) {
            unsigned long long c0, c1;
            st128(k.rs, cal, (r & 1) * 8192, ones);
            st128(k.rs, cal, (~r & 1) * 8192, ones);
            asm volatile("s_waitcnt vmcnt(1)\n\ts_memtime %0\n\ts_waitcnt vmcnt(0)\n\ts_memtime %1\n\ts_waitcnt lgkmcnt(0)" : "=&s"(c0), "=s"(c1) :: "memory");
            d = r ? min((int)(c1 - c0), 400) - d : 0;
        }
        vote = (unsigned)(d < 0) ^ (unsigned)(xcc_id() >> 2) ^ (unsigned)(i & 1);
    } else {
        if (k.w == 2 && j < 20) {
            const int e = j * 32 + (l >> 1), T = e >> 4, hf = (e >> 1) & 1, rr = ((e >> 2) & 3) * 2 + (e & 1);
            u32x4 v;
#pragma unroll
            for (int c = 0; c < 4; ++c) v[c] = min(*(const unsigned*)(p.mem0 + rr * 640 + T * 16 + 4 * c + 2 * hf), 0xFFFFFFFEu);
            st128(k.rs, (2u << 21) + EXP(e * 16) + (l & 1) * 8192, 0, v);
        }
        constexpr unsigned N0 = PR_B + PR_SZ, N1 = 40 * 16384, N2 = 32768;
        static_assert(N0 + N1 + N2 <= 4 * 88 * 3072 && NPAGES <= 44, "fill");
#pragma unroll
        for (int r = 0; r < 4; ++r) {
            const unsigned o = ((r * 88 + j) * 3 + k.w) * 1024;
            st128(k.rs, o < N0 + N1 + N2 ? l * 16 : OOB, o < N0 ? o : o < N0 + N1 ? ((unsigned)PG_YM << 21) + (o - N0) : (2u << 21) + 32768 + (o - N0 - N1), ones);
        }
    }
    if (k.w == 3) *(volatile LAS unsigned*)smem = vote;
    DRAIN();
    __builtin_amdgcn_s_waitcnt(0x0f70);
    XSTAMP(0)
    RAWBAR()
    return *(volatile LAS unsigned*)smem;
}
DEVI void master(const Params& p, const Ctx& k) {
    const int l = k.l;
    u32x2 v[4] = {{0u, 0u}, {0u, 0u}, {0u, 0u}, {0u, 0u}};
_Pragma("unroll 1")
    for (unsigned spins = 0; spins < SPIN_CAP; ++spins) {
#pragma unroll
        for (int q = 0; q < 4; ++q) v[q] |= cm_ld(k, (CM_IN + (q * 64 + l) * 128) | v[q][1]);
        if (__all((v[0][1] & v[1][1] & v[2][1] & v[3][1]) != 0)) break;
        asm volatile("" ::: "memory");
    }
    const unsigned long long a = __ballot(v[2][0] != 0), b = __ballot(v[3][0] != 0);
    const int n = __popcll(a) + __popcll(b);
    const unsigned long long g = n > 44 ? ~0ull : 0ull, u = (a & b) | ((a ^ b) & g);
    const unsigned long long t = (__popcll(a ^ b) > 2 || n > 80 || n < 8 ? g : ((u >> 4) & 0x3FFFFF) | ((u >> 36) << 22)) ^ 0xAAAAAAAAAAAAAAAAull;
#pragma unroll
    for (int q = 0; q < 4; ++q) cm_st(k, l * 128, CM_GO + q * 8192, (unsigned)t, (unsigned)(t >> 32) | 0x80000000u);
}

#define FIN(n) { __builtin_amdgcn_s_waitcnt(0x0f70); asm volatile("s_nop " #n); return; }
__global__ __launch_bounds__(256, 1) void mega_kernel(Params p) {
    extern __shared__ __attribute__((aligned(16))) char smem[];
    Ctx k;
    const u16 *wq = p.Wq, *wk = p.Wk, *wv = p.Wv;
    asm volatile("" : "+s"(wq), "+s"(wk), "+s"(wv) : "s"(p.cm));
    k.ws = p.ws; k.cm = p.cm;
    k.rs = __builtin_amdgcn_make_buffer_rsrc((void*)p.ws, 0, 0x7fffffff, 0x00020000);
    k.l = threadIdx.x & 63; k.w = __builtin_amdgcn_readfirstlane(threadIdx.x >> 6);
    RSTAMP(0)
    const int x = blockIdx.x & 7, i = blockIdx.x >> 3, xl = x & 3;
    k.role = x * 32 + i; k.c = x >> 2; k.par = 0; k.p0 = 0; k.t0 = rclk();
    line_st(k, CM_GO, 0, 0);
    auto start = [&]() {
        const u16* a = (i < 8 ? wk : wv) + (xl * 8 + (i & 7)) * 192, * b = (i < 8 ? wq : wv + 32 * 6144) + (xl * 8 + (i & 7)) * 192;
        unsigned o[3];
        sfor<18>([&](auto J) { constexpr int j = CV(J);
            if constexpr (j < 3) o[j] = wsrc<384, true>(k.w * 192 + j * 64 + k.l, 12288);
            dma1<SLOT(j / 6) + ((j / 3) & 1) * 12288 + (j % 3) * 1024>((((j / 3) & 1) ? b : a) + (j / 6) * (i < 8 ? 32 * 6144 : 64 * 6144), o[j % 3], k.w * 3072); }); };
    if (!k.c && i < 16) start();
    if (__builtin_expect(k.role == MASTER, 0)) {
#pragma unroll
        for (int q = 0; q < 16; ++q) cm_st(k, k.l * 8, CM_IN + k.w * 8192 + q * 512, 0, 0);
        cm_st(k, 0, CM_TMO, ~0u, ~0u);
        DRAIN();
        RAWBAR()
    }
    if (__builtin_expect(k.c && i < 26, 0)) { const unsigned v = prepare(p, k, smem, xl, i); RSTAMP(1) hello(k, v); go_wait(k, v); XSTAMP(1) RAWBAR() }
    else { RSTAMP(1) if (k.c) { DRAIN(); hello(k, 0); } }
    if (__builtin_expect(k.role == MASTER && k.w == 0, 0)) { master(p, k); __builtin_amdgcn_s_waitcnt(0x0f70); }
    if (k.c && i < 16) { sleep_until(k.t0 + WD1 + (i ^ 8) * STG); start(); }
    if (i < 8) { k.l = lane(); qk_role(p, k, smem, xl * 8 + i); FIN(0) }
    if (i < 16) { k.l = lane(); vo_role(p, k, smem, xl * 8 + i - 8); FIN(1) }
    k.l = lane();
    if (!k.c || i < 26) warm(p, k, xl, (i - 16) * 4 + k.w);
    else RAWBAR()
    go_wait(k, 0);
    if (i >= 26 && !k.c) {
        sleep_until(k.t0 + XPF);
#pragma unroll
        for (int r = 0; r < 3; ++r) asm volatile("global_load_lds_dword %0, %1" :: "v"(min(40 + r * 96 + ((i - 26) * 4 + xl) * 4 + k.w, 319) * 8192 + k.l * 128), "s"(p.x) : "memory");
        DRAIN();
        RAWBAR()
    }
    if (i >= 26) sleep_until(k.t0 + (k.c ? GST : GSA) + ((xl & 1) == UPODD ? 0 : i < 31 ? GSD : -GSA));
    if (i < 21) { mred_role(p, k, smem, xl * 5 + i - 16); FIN(2) }
    if (i < 26) { tred_role(p, k, smem, xl * 5 + i - 21); FIN(3) }
    if ((xl & 1) == UPODD) { up_role(p, k, smem, (xl >> 1) * 6 + i - 26); FIN(4) }
    if (i < 31) { down_role(p, k, smem, (xl >> 1) * 5 + i - 26); FIN(5) }
    row_role(p, k, smem, xl >> 1);
    FIN(6)
}

extern "C" void kernel_launch(void* const* d_in, const int* in_sizes, int n_in,
                              void* d_out, int out_size, void* d_ws, size_t ws_size,
                              hipStream_t stream) {
    Params p;
    p.x = (const u16*)d_in[0]; p.mem0 = (const u16*)d_in[1];
    p.Wq = (const u16*)d_in[2]; p.Wk = (const u16*)d_in[3]; p.Wv = (const u16*)d_in[4]; p.Wo = (const u16*)d_in[5];
    p.wg = (const u16*)d_in[8]; p.wu = (const u16*)d_in[9]; p.wd = (const u16*)d_in[10];
    p.out = (u16*)d_out; p.cm = (char*)d_ws;
    p.ws = (char*)(((uintptr_t)d_ws + CM_BYTES + ((1u << 21) - 1)) & ~(uintptr_t)((1u << 21) - 1));
    (void)hipFuncSetAttribute((const void*)mega_kernel, hipFuncAttributeMaxDynamicSharedMemorySize, LDS_BYTES);
    mega_kernel<<<256, 256, LDS_BYTES, stream>>>(p);
}
```

```cpp
#define SWAPG
#include <hip/hip_runtime.h>
#include <hip/hip_bf16.h>
#include <stdint.h>
#include <utility>

typedef unsigned short u16;
typedef __attribute__((ext_vector_type(16))) short bf16x16;
typedef __attribute__((ext_vector_type(8))) short bf16x8;
typedef __attribute__((ext_vector_type(4))) short bf16x4;
typedef __attribute__((ext_vector_type(8))) __bf16 hbf8;
typedef __attribute__((ext_vector_type(16))) __bf16 hbf16;
typedef __attribute__((ext_vector_type(4))) float f32x4;
typedef __attribute__((ext_vector_type(2))) float f32x2;
typedef __attribute__((ext_vector_type(8))) unsigned u32x8;
typedef __attribute__((ext_vector_type(4))) unsigned u32x4;
typedef __attribute__((ext_vector_type(2))) unsigned u32x2;
typedef __attribute__((ext_vector_type(2))) __bf16 bf16x2_t;
#define LAS __attribute__((address_space(3)))
#define GAS __attribute__((address_space(1)))
#define DEVI __device__ __forceinline__

#define PSLEEP 8
#define QT 1
#define GVS 0
#define VS 0
#define CPOS (34 - 2 * QT)
#define EPOS 110
#define XA -5
#define SOFT2 1
#define YS 2
#define Y1 0
#define Y2 0
#define YB 58
#define ARMP 2
#define WD1 1000
#define GST 550
#define NTA " nt"
#define NTB ""
#define PQ 0x077A0
#define PV 0x4AAE4
#define GSA 1600
#define QGS 8
#define VGS 8
#define MN 480
#define MD 250
#define MP 100
#define RSL 1000
#define GSD 250
#define PO 0
#define PDLY 0
#define WP 600
#define PP 100
#define STG 0
#define UPOS0 88
#define DPOS 88
#define XPF 1400
#define TPOS 33
#define XS0 40
#define XP 23
#define XB 66
#define TB 107
#define PACE 65
#define XPOS 68
#define YPOS 40
#define XPOSY 1
#define PWS 1
#define WUS 0
#define WMIX 1
#define PACE2 30
#define GDLY 80
#define QDLY 0
#define CALR 1
#define SPG 3
#define DQ 16
#define DV 12
#define BSLP 1
#define SDLY 0
#define UPOS 150
#define RDLY 45
#define NOYST 0
#define NOTOK 0
#define NOYLD 0
#define GPOSY 8
#define OLDMAP 1
#define UPODD 0
#define XLD 10
#define TROT 8
#define KOUT 1
#define RPOS 69
#define RDY 90
#define APOS 6
#define GPOS 74

#define EXP(o) ((((unsigned)(o) >> 13) << 14) | ((unsigned)(o) & 8191u))
enum { C_XN = 0, C_Y = 1, C_X1 = 2, C_XB = 3, C_H = 4, C_X2 = 5, C_BC = 6 };
#define CNT(type, s) ((unsigned)((type) * 16384 + (s) * 128))
#define YLINE(w, h) CNT(C_Y + ((w) >> 1) * 6, ((w) & 1) * 32 + (h))
constexpr unsigned PR_B = EXP(65536), PR_SZ = EXP(12 * 8192);
constexpr unsigned DUMMY_B = EXP(0);
constexpr int PG_MEMF = 2;
#define YXR 8
constexpr int PG_YX = 3;
constexpr int PG_XNA = PG_YX + 2 * YXR;
constexpr int PG_X1 = PG_XNA + 4, PG_X2 = PG_X1 + 4;
constexpr int PG_H = PG_X2 + 4;
constexpr int PG_YM = PG_H + 8;
constexpr int NPAGES = PG_YM + 2;
static_assert(NPAGES <= 64, "map");
constexpr unsigned CM_TAB = 0, CM_TICKET = 1024, CM_TMO = 4096, CM_IN = 8192, CM_GO = 40960, CM_BYTES = 73728;
constexpr int MASTER = 5 * 32 + 31;
constexpr unsigned SPIN_CAP = 1u << 20;
#define AUX_SC1 16
#define LDS_BYTES (160 * 1024)

struct Params {
    const u16* x; const u16* mem0; const u16* Wq; const u16* Wk; const u16* Wv; const u16* Wo;
    const u16* wg; const u16* wu; const u16* wd; u16* out; char* cm; char* ws;
};
struct Ctx {
    char* ws; char* cm; __amdgpu_buffer_rsrc_t rs; unsigned long long par;
    int c, l, w, role;
    unsigned p0, t0;
    DEVI unsigned pg(int page) const { return ((unsigned)page << 21) | (((unsigned)(par >> page) & 1u) << 13); }
    DEVI unsigned pg0(int page) const { return pg(page) ^ ((unsigned)c << 13); }
    DEVI unsigned seg(int page, int s) const { return pg(page + (s >> 4)) + (unsigned)(s & 15) * 131072u; }
    DEVI unsigned seg0(int page, int s) const { return seg(page, s) ^ ((unsigned)c << 13); }
    DEVI unsigned segx(int page, int s) const { return seg(page, s) ^ 8192u; }
    DEVI unsigned hseg(int s) const { return pg(PG_H + (s >> 3)) + (unsigned)(s & 7) * 262144u; }
    DEVI unsigned yx(int sg, int wq) const { return pg(PG_YX + (sg % YXR) * 2 + (wq >> 1)) + (unsigned)(wq & 1) * 1048576u; }
    DEVI unsigned ym(int s) const { return pg(PG_YM + ((s >> 1) & 1)) + (unsigned)(s & 1) * EXP(327680); }
    DEVI unsigned memf(int s) const { return pg(PG_MEMF) + (unsigned)s * 32768u; }
    DEVI char* cnt(int type, int s) const { return ws + (p0 + CNT(type, s)); }
    DEVI char* cnt0(int type, int s) const { return ws + ((p0 ^ ((unsigned)c << 13)) + CNT(type, s)); }
};

DEVI unsigned cvtpk(float a, float b) { return __builtin_bit_cast(unsigned, __builtin_convertvector((f32x2){a, b}, bf16x2_t)); }
DEVI unsigned packs(float a, float b) { return min(cvtpk(a, b), 0xFFFFFFFEu); }
DEVI float blo(unsigned x) { return __uint_as_float(x << 16); }
DEVI float bhi(unsigned x) { return __uint_as_float(x & 0xFFFF0000u); }
DEVI float bfr(float f) { return __uint_as_float(cvtpk(0.f, f)); }
DEVI u32x4 ld128(__amdgpu_buffer_rsrc_t rs, unsigned v, unsigned s) { return __builtin_amdgcn_raw_buffer_load_b128(rs, (int)v, (int)s, AUX_SC1); }
DEVI u32x2 ld64(__amdgpu_buffer_rsrc_t rs, unsigned v, unsigned s) { return __builtin_amdgcn_raw_buffer_load_b64(rs, (int)v, (int)s, AUX_SC1); }
DEVI unsigned ld32(__amdgpu_buffer_rsrc_t rs, unsigned v, unsigned s) { return __builtin_amdgcn_raw_buffer_load_b32(rs, (int)v, (int)s, AUX_SC1); }
DEVI void st128(__amdgpu_buffer_rsrc_t rs, unsigned v, unsigned s, u32x4 x) {
    __builtin_amdgcn_raw_buffer_store_b128(x, rs, (int)v, (int)s, AUX_SC1);
    asm volatile("s_nop 1" :: "v"(x) : "memory");
}
DEVI void st64(__amdgpu_buffer_rsrc_t rs, unsigned v, unsigned s, u32x2 x) { __builtin_amdgcn_raw_buffer_store_b64(x, rs, (int)v, (int)s, AUX_SC1); }
DEVI void st32(__amdgpu_buffer_rsrc_t rs, unsigned v, unsigned s, unsigned x) { __builtin_amdgcn_raw_buffer_store_b32(x, rs, (int)v, (int)s, AUX_SC1); }
#define RAWBAR() { __builtin_amdgcn_s_waitcnt(0xc07f); __builtin_amdgcn_s_barrier(); }
#define BARL(n) { asm volatile("" ::: "memory"); __builtin_amdgcn_s_waitcnt(0xc07f | ((n) << 8)); __builtin_amdgcn_s_barrier(); asm volatile("" ::: "memory"); }
#define DRAIN() asm volatile("s_waitcnt vmcnt(0)" ::: "memory")
#define SCHED0() __builtin_amdgcn_sched_barrier(0)
#define OOB 0x80000000u
DEVI int opq(int x) { asm volatile("" : "+v"(x)); return x; }
#define LANE const int l = opq(k.l), g = l >> 4, n = l & 15; (void)g; (void)n;
DEVI unsigned umax3(unsigned a, unsigned b, unsigned c) { return max(max(a, b), c); }
DEVI unsigned umax4(u32x4 v) { return max(umax3(v[0], v[1], v[2]), v[3]); }
DEVI unsigned rclk() { return (unsigned)__builtin_amdgcn_s_memrealtime(); }
DEVI unsigned long long rt_async() { unsigned long long t; asm volatile("s_memrealtime %0" : "=s"(t)); return t; }
DEVI void lgkm0(unsigned long long& a) { asm volatile("s_waitcnt lgkmcnt(0)" : "+s"(a) :: "memory"); }
DEVI unsigned spack(unsigned lo, unsigned hi) { unsigned d; asm volatile("s_pack_ll_b32_b16 %0, %1, %2" : "=s"(d) : "s"(lo), "s"(hi)); return d; }
DEVI int lane() { int l; asm volatile("v_mbcnt_lo_u32_b32 %0, -1, 0\n\tv_mbcnt_hi_u32_b32 %0, -1, %0" : "=v"(l)); __builtin_assume(l >= 0 && l < 64); return l; }
DEVI void sleep_until(unsigned target) { while ((int)(rclk() - target) < 0) __builtin_amdgcn_s_sleep(4); }
DEVI float xor16max(float x) { auto r = __builtin_amdgcn_permlane16_swap(__float_as_uint(x), __float_as_uint(x), false, false); return __builtin_fmaxf(__uint_as_float(r[0]), __uint_as_float(r[1])); }
DEVI float xor32max(float x) { auto r = __builtin_amdgcn_permlane32_swap(__float_as_uint(x), __float_as_uint(x), false, false); return __builtin_fmaxf(__uint_as_float(r[0]), __uint_as_float(r[1])); }
DEVI float xor16add(float x) { auto r = __builtin_amdgcn_permlane16_swap(__float_as_uint(x), __float_as_uint(x), false, false); return __uint_as_float(r[0]) + __uint_as_float(r[1]); }
DEVI float xor32add(float x) { auto r = __builtin_amdgcn_permlane32_swap(__float_as_uint(x), __float_as_uint(x), false, false); return __uint_as_float(r[0]) + __uint_as_float(r[1]); }
DEVI float fold32(float a, float b) { auto r = __builtin_amdgcn_permlane32_swap(__float_as_uint(a), __float_as_uint(b), false, false); return __uint_as_float(r[0]) + __uint_as_float(r[1]); }
#define ROR16(x, n) __builtin_bit_cast(float, __builtin_amdgcn_update_dpp(0, __builtin_bit_cast(int, (x)), 0x120 + (n), 0xf, 0xf, false))
DEVI float row16sum(float x) { x += ROR16(x, 8); x += ROR16(x, 4); x += ROR16(x, 2); x += ROR16(x, 1); return x; }
DEVI unsigned z0() { unsigned z; asm volatile("v_mov_b32 %0, 0" : "=v"(z)); return z; }
DEVI void cnt_add(char* p) { __hip_atomic_fetch_add((GAS unsigned*)(p + z0()), z0() + 1u, __ATOMIC_RELAXED, __HIP_MEMORY_SCOPE_AGENT); }
DEVI unsigned cnt_get(char* p) { return __hip_atomic_load((GAS unsigned*)(p + z0()), __ATOMIC_RELAXED, __HIP_MEMORY_SCOPE_AGENT) + 1u; }
DEVI bool spin_fail(const Ctx& k, unsigned spins) {
    if (spins > SPIN_CAP || ((spins & 255u) == 255u && __builtin_amdgcn_readfirstlane(cnt_get(k.cm + CM_TMO)) != 0)) { cnt_add(k.cm + CM_TMO); return true; }
    return false;
}
DEVI __amdgpu_buffer_rsrc_t cm_rs(const Ctx& k) { return __builtin_amdgcn_make_buffer_rsrc((void*)k.cm, 0, CM_BYTES, 0x00020000); }
DEVI void cm_st(const Ctx& k, unsigned v, unsigned off, unsigned lo, unsigned hi) { asm volatile("buffer_store_dwordx2 %0, %1, %2, %3 offen sc1" :: "v"((u32x2){lo, hi}), "v"(v), "s"(cm_rs(k)), "s"(off) : "memory"); }
DEVI u32x2 cm_ld(const Ctx& k, unsigned off) { return ld64(cm_rs(k), off, 0); }
DEVI void line_st(const Ctx& k, unsigned off, unsigned lo, unsigned hi) { cm_st(k, (lane() & 15) * 8 + (k.w ? OOB : 0u), off + k.role * 128, lo, hi); }
DEVI void hello(const Ctx& k, unsigned vote) { line_st(k, CM_IN, vote, OOB); }
DEVI u32x2 go_ask(const Ctx& k) { u32x2 d; asm volatile("buffer_load_dwordx2 %0, off, %1, %2 sc1" : "=v"(d) : "s"(cm_rs(k)), "s"(CM_GO + k.role * 128) : "memory"); return d; }
DEVI void go_wait(Ctx& k, unsigned vote, u32x2 v = {0u, 0u}) {
_Pragma("unroll 1")
    for (unsigned spins = 0; __builtin_amdgcn_readfirstlane(v[1]) >= 0 && spins < SPIN_CAP; ++spins) {
        if (spins) { hello(k, vote); __builtin_amdgcn_s_sleep(4); }
        asm volatile("buffer_load_dwordx2 %0, off, %1, %2 sc1\n\ts_waitcnt vmcnt(0)" : "=v"(v) : "s"(cm_rs(k)), "s"(CM_GO + k.role * 128) : "memory");
    }
    k.par = ((unsigned long long)(unsigned)__builtin_amdgcn_readfirstlane(v[1]) << 32 | (unsigned)__builtin_amdgcn_readfirstlane(v[0])) ^ (k.c ? ~0ull : 0ull);
    k.p0 = k.pg(0);
    __builtin_amdgcn_fence(__ATOMIC_ACQUIRE, "wavefront");
}
DEVI void cnt_wait(const Ctx& k, char* c, unsigned need) {
_Pragma("unroll 1")
    for (unsigned spins = 0; (unsigned)__builtin_amdgcn_readfirstlane(cnt_get(c)) < need; ++spins) {
        __builtin_amdgcn_s_sleep(PSLEEP);
        if (spin_fail(k, spins)) break;
    }
    __builtin_amdgcn_fence(__ATOMIC_ACQUIRE, "wavefront");
}
DEVI unsigned xn_get(const Ctx& k, unsigned l16, int s) { return ld32(k.rs, opq(l16) & 0x380u, k.p0 + CNT(C_XN, 0)); }
DEVI bool xn_has(const Ctx& k, unsigned v, int s) { return __all((int)v >= s - (k.c ? 0 : 8)); }
DEVI void xn_wait(const Ctx& k, unsigned l16, int s) {
_Pragma("unroll 1")
    for (unsigned spins = 0; !xn_has(k, xn_get(k, l16, s), s); ++spins) {
        asm volatile("" ::: "memory");
        __builtin_amdgcn_s_sleep(2);
        if (spin_fail(k, spins)) break;
    }
    __builtin_amdgcn_fence(__ATOMIC_ACQUIRE, "wavefront");
}
constexpr unsigned L_PW = 150 * 1024;
DEVI void prog_init(const Ctx& k) { *(volatile LAS unsigned*)(size_t)(L_PW + k.l * 4) = ~0u; RAWBAR() }
template <int N> DEVI void prog_wait(const Ctx& k, unsigned a, int s) {
    unsigned n, t;
    asm volatile("s_mov_b32 m0, %8\n\ts_mov_b32 %0, 0\n\ts_branch 2f\n"
        "1:\n\ts_cmp_lg_u32 %6, 0\n\ts_cbranch_scc1 3f\n\tbuffer_load_dword %2, %3, %4 offen sc1 lds\n"
        "3:\n\ts_sleep %9\n\ts_add_u32 %0, %0, 1\n\ts_and_b32 vcc_lo, %0, 0x7ff\n\ts_cbranch_scc1 2f\n\t"
        "v_mov_b32 %1, 0\n\ts_bitcmp1_b32 %0, 23\n\ts_cbranch_scc0 5f\n\tglobal_store_dword %1, %1, %10 sc1\n"
        "5:\n\tglobal_load_dword %1, %1, %10 sc1\n\ts_waitcnt vmcnt(0)\n\tv_cmp_ne_u32 vcc, -1, %1\n\ts_cbranch_vccnz 4f\n"
        "2:\n\tds_read_b32 %1, %7\n\ts_waitcnt lgkmcnt(0)\n\tv_cmp_gt_i32 vcc, %5, %1\n\ts_cbranch_vccnz 1b\n4:"
        : "=&s"(n), "=&v"(t) : "v"(min(k.l, N - 1) * 128), "s"(k.rs), "s"(a), "s"(s), "s"(k.w), "v"(L_PW + k.l * 4), "n"(L_PW), "n"(PWS), "s"(k.cm + CM_TMO) : "memory", "vcc", "scc");
}
template <int OFF> DEVI void dma1(const void* sbase, unsigned voff, unsigned lds) {
    asm volatile("s_add_i32 m0, %2, %3\n\ts_nop 0\n\tglobal_load_lds_dwordx4 %0, %1" NTA :: "v"(voff), "s"(sbase), "s"(lds), "n"(OFF) : "memory", "scc");
}
DEVI void set_m0(unsigned lds) { asm volatile("s_mov_b32 m0, %0" :: "s"(lds) : "memory"); }
template <int OFF> DEVI void dmab(__amdgpu_buffer_rsrc_t rs, unsigned voff, unsigned soff) {
    asm volatile("buffer_load_dwordx4 %0, %1, %2 offen offset:%3 sc1 lds" :: "v"(voff), "s"(rs), "s"(soff), "n"(OFF) : "memory");
}
#define PIN(x) asm volatile("" : "+v"(x))
#define PINS(x) asm volatile("" : "+s"(x))
#define LEAD()
#define TOUCH(x) asm volatile("" :: "v"(x))
DEVI float csel(float a, float b, unsigned long long m) { float d; asm volatile("v_cndmask_b32_e64 %0, %1, %2, %3" : "=v"(d) : "v"(a), "v"(b), "s"(m)); return d; }
#define LO8(x) __builtin_shufflevector(x, x, 0, 1, 2, 3, 4, 5, 6, 7)
#define HI8(x) __builtin_shufflevector(x, x, 8, 9, 10, 11, 12, 13, 14, 15)
#define CAT8(a, b) __builtin_shufflevector(a, b, 0, 1, 2, 3, 4, 5, 6, 7, 8, 9, 10, 11, 12, 13, 14, 15)
template <bool AG> DEVI void smf(f32x4& c, const bf16x8& a, const bf16x16& w, unsigned ix) {
    if constexpr (AG) asm volatile("v_smfmac_f32_16x16x64_bf16 %0, %1, %2, %3" : "+v"(c) : "v"(a), "a"(w), "v"(ix));
    else asm volatile("v_smfmac_f32_16x16x64_bf16 %0, %1, %2, %3" : "+v"(c) : "v"(a), "v"(w), "v"(ix));
}
#define WTAB(X) X(0, "{a[0:7]}", "{a[0:3]}", "{a[4:7]}") X(1, "{a[8:15]}", "{a[8:11]}", "{a[12:15]}") X(2, "{a[16:23]}", "{a[16:19]}", "{a[20:23]}") X(3, "{a[24:31]}", "{a[24:27]}", "{a[28:31]}") X(4, "{a[32:39]}", "{a[32:35]}", "{a[36:39]}") X(5, "{a[40:47]}", "{a[40:43]}", "{a[44:47]}") X(6, "{a[48:55]}", "{a[48:51]}", "{a[52:55]}") X(7, "{a[56:63]}", "{a[56:59]}", "{a[60:63]}") X(8, "{a[64:71]}", "{a[64:67]}", "{a[68:71]}") X(9, "{a[72:79]}", "{a[72:75]}", "{a[76:79]}") X(10, "{a[80:87]}", "{a[80:83]}", "{a[84:87]}") X(11, "{a[88:95]}", "{a[88:91]}", "{a[92:95]}") X(12, "{a[96:103]}", "{a[96:99]}", "{a[100:103]}") X(13, "{a[104:111]}", "{a[104:107]}", "{a[108:111]}") X(14, "{a[112:119]}", "{a[112:115]}", "{a[116:119]}") X(15, "{a[120:127]}", "{a[120:123]}", "{a[124:127]}") X(16, "{a[128:135]}", "{a[128:131]}", "{a[132:135]}") X(17, "{a[136:143]}", "{a[136:139]}", "{a[140:143]}") X(18, "{a[144:151]}", "{a[144:147]}", "{a[148:151]}") X(19, "{a[152:159]}", "{a[152:155]}", "{a[156:159]}") X(20, "{a[160:167]}", "{a[160:163]}", "{a[164:167]}") X(21, "{a[168:175]}", "{a[168:171]}", "{a[172:175]}") X(22, "{a[176:183]}", "{a[176:179]}", "{a[180:183]}") X(23, "{a[184:191]}", "{a[184:187]}", "{a[188:191]}") X(24, "{a[192:199]}", "{a[192:195]}", "{a[196:199]}") X(25, "{a[200:207]}", "{a[200:203]}", "{a[204:207]}") X(26, "{a[208:215]}", "{a[208:211]}", "{a[212:215]}") X(27, "{a[216:223]}", "{a[216:219]}", "{a[220:223]}") X(28, "{a[224:231]}", "{a[224:227]}", "{a[228:231]}") X(29, "{a[232:239]}", "{a[232:235]}", "{a[236:239]}") X(30, "{a[240:247]}", "{a[240:243]}", "{a[244:247]}") X(31, "{a[248:255]}", "{a[248:251]}", "{a[252:255]}") X(32, "{v[96:103]}", "{v[96:99]}", "{v[100:103]}") X(33, "{v[104:111]}", "{v[104:107]}", "{v[108:111]}") X(34, "{v[112:119]}", "{v[112:115]}", "{v[116:119]}") X(35, "{v[120:127]}", "{v[120:123]}", "{v[124:127]}") X(36, "{v[128:135]}", "{v[128:131]}", "{v[132:135]}") X(37, "{v[136:143]}", "{v[136:139]}", "{v[140:143]}") X(38, "{v[144:151]}", "{v[144:147]}", "{v[148:151]}") X(39, "{v[152:159]}", "{v[152:155]}", "{v[156:159]}") X(40, "{v[160:167]}", "{v[160:163]}", "{v[164:167]}") X(41, "{v[168:175]}", "{v[168:171]}", "{v[172:175]}") X(42, "{v[176:183]}", "{v[176:179]}", "{v[180:183]}") X(43, "{v[184:191]}", "{v[184:187]}", "{v[188:191]}") X(44, "{v[192:199]}", "{v[192:195]}", "{v[196:199]}") X(45, "{v[200:207]}", "{v[200:203]}", "{v[204:207]}") X(46, "{v[208:215]}", "{v[208:211]}", "{v[212:215]}") X(47, "{v[216:223]}", "{v[216:219]}", "{v[220:223]}") X(48, "{v[224:231]}", "{v[224:227]}", "{v[228:231]}") X(49, "{v[232:239]}", "{v[232:235]}", "{v[236:239]}") X(50, "{v[240:247]}", "{v[240:243]}", "{v[244:247]}") X(51, "{v[248:255]}", "{v[248:251]}", "{v[252:255]}")
template <int ID> DEVI void smfw(f32x4& c, const bf16x8& a, const bf16x16& w, unsigned ix) {
#define X(i, P, L, H) if constexpr (ID == i) asm volatile("v_smfmac_f32_16x16x64_bf16 %0, %1, %2, %3" : "+v"(c) : "v"(a), P(w), "v"(ix));
    WTAB(X)
#undef X
}
template <int ID, int HF, bool WB, bool FIRST> DEVI void mfw(f32x4& c, const bf16x16& w, const bf16x8& x) {
#define Y(R, W8) { if constexpr (FIRST && WB) asm volatile("v_mfma_f32_16x16x32_bf16 %0, %2, %1, 0" : "=&v"(c) : R(W8), "v"(x)); \
    else if constexpr (FIRST) asm volatile("v_mfma_f32_16x16x32_bf16 %0, %1, %2, 0" : "=&v"(c) : R(W8), "v"(x)); \
    else if constexpr (WB) asm volatile("v_mfma_f32_16x16x32_bf16 %0, %2, %1, %0" : "+v"(c) : R(W8), "v"(x)); \
    else asm volatile("v_mfma_f32_16x16x32_bf16 %0, %1, %2, %0" : "+v"(c) : R(W8), "v"(x)); }
#define X(i, P, L, H) if constexpr (ID == i) { if constexpr (HF) Y(H, HI8(w)) else Y(L, LO8(w)) }
    WTAB(X)
#undef X
#undef Y
}
template <bool AG, bool FIRST> DEVI void mfa(f32x4& c, const bf16x8& w, const bf16x8& x) {
    if constexpr (AG && FIRST) asm volatile("v_mfma_f32_16x16x32_bf16 %0, %1, %2, 0" : "=&v"(c) : "a"(w), "v"(x));
    else if constexpr (AG) asm volatile("v_mfma_f32_16x16x32_bf16 %0, %1, %2, %0" : "+v"(c) : "a"(w), "v"(x));
    else if constexpr (FIRST) asm volatile("v_mfma_f32_16x16x32_bf16 %0, %1, %2, 0" : "=&v"(c) : "v"(w), "v"(x));
    else asm volatile("v_mfma_f32_16x16x32_bf16 %0, %1, %2, %0" : "+v"(c) : "v"(w), "v"(x));
}
template <bool AG, bool FIRST> DEVI void mfb(f32x4& c, const bf16x8& x, const bf16x8& w) {
    if constexpr (AG && FIRST) asm volatile("v_mfma_f32_16x16x32_bf16 %0, %1, %2, 0" : "=&v"(c) : "v"(x), "a"(w));
    else if constexpr (AG) asm volatile("v_mfma_f32_16x16x32_bf16 %0, %1, %2, %0" : "+v"(c) : "v"(x), "a"(w));
    else if constexpr (FIRST) asm volatile("v_mfma_f32_16x16x32_bf16 %0, %1, %2, 0" : "=&v"(c) : "v"(x), "v"(w));
    else asm volatile("v_mfma_f32_16x16x32_bf16 %0, %1, %2, %0" : "+v"(c) : "v"(x), "v"(w));
}
DEVI f32x4 mf(const bf16x8& a, const bf16x8& b, const f32x4& c) { return __builtin_amdgcn_mfma_f32_16x16x32_bf16(__builtin_bit_cast(hbf8, a), __builtin_bit_cast(hbf8, b), c, 0, 0, 0); }
template <int N, class F> DEVI void sfor(F f) { [&]<int... I>(std::integer_sequence<int, I...>) { (f(std::integral_constant<int, I>{}), ...); }(std::make_integer_sequence<int, N>{}); }
DEVI bf16x8 lds128(const char* p) { return *(const bf16x8*)p; }
DEVI bf16x4 ldstr(const char* p) { return __builtin_amdgcn_ds_read_tr16_b64_v4i16((LAS bf16x4*)p); }
DEVI bf16x8 ldstr2(const char* p, int d) { bf16x4 a = ldstr(p), b = ldstr(p + d); return __builtin_shufflevector(a, b, 0, 1, 2, 3, 4, 5, 6, 7); }

#define STAMP(s, k)
#define QSTAMP(f, i)
#define STOP(s)
#define PFLUSH(s)
#define PROF_DECL
#define RSTAMP(i)
#define XSTAMP(i)
#define PCOUNT(i)
#define PROF_OUT

#define HSTAMP STAMP
#define HFLUSH PFLUSH
#define HTRACE(s)
#define TROUT
#define TRHINT
#define TPQ(n)
#define TPV(n)
#define AQ(m, i)
#define AC3(i, j, k) { }
#define AST(i)
#define AAC(i)
#define ACCM 0
#define HSTOP(m) if constexpr ((ACCM != 0) == m) { STOP(0) }
#define PC8(i, x)
#define PROF_ONLY(x)
#define PROF_S(x)
template <int RB, bool STD> DEVI int swz(int row) { return RB == 384 ? ((row >> 1) & 1) | ((row >> 2) & 2) : RB == 128 ? (row >> 1) & 3 : STD ? (row & 3) | ((row >> 1) & 4) : row & 7; }
template <int RB, bool STD> DEVI unsigned wsrc(unsigned id, int ld) { const unsigned row = id / (RB / 16), c = id % (RB / 16); return row * ld + ((c ^ (swz<RB, STD>(row) << 1)) << 4); }
template <int RB, bool STD> DEVI int wp_adr(int T, int l) { const int row = (STD ? 8 : 4) * (l >> 4) + ((l >> 2) & 3); return row * RB + ((T ^ swz<RB, STD>(row)) << 5) + (l & 3) * 8; }
template <int RB, bool STD> DEVI bf16x8 wp_read(const char* blk, int T, int l) { return ldstr2(blk + wp_adr<RB, STD>(T, l), (STD ? 4 : 16) * RB); }
template <int NU, class CNT, class STEP> DEVI void cstream(CNT cnt, STEP step) {
    sfor<NU>([&](auto V) { constexpr int v = decltype(V)::value;
        asm volatile("s_waitcnt vmcnt(%0) lgkmcnt(0)\n\ts_barrier" :: "n"(decltype(cnt(V))::value * (v ? 1 : 2)) : "memory");
        step(V);
    });
}
template <int NU, int NI, int D, class ISSUE, class READ> DEVI void wstream(char* ring, ISSUE issue, READ read) {
    const unsigned base = (unsigned)(size_t)(LAS char*)ring;
    sfor<D>([&](auto U) { constexpr int u = decltype(U)::value; if constexpr (u < NU) issue(U, base); });
    sfor<NU>([&](auto U) {
        constexpr int u = decltype(U)::value, left = (NU - u < D ? NU - u : D) - 1;
        asm volatile("s_waitcnt vmcnt(%0)" :: "n"(left * NI) : "memory");
        read(U, ring + (u % D) * NI * 1024);
        __builtin_amdgcn_s_waitcnt(0xc07f);
        asm volatile("" ::: "memory");
        if constexpr (u + D < NU) issue(std::integral_constant<int, u + D>{}, base);
    });
}

constexpr int L_XN = 0;
constexpr int L_K = 40960;
constexpr int L_MEM = L_K + 12288;
constexpr int L_QM = L_MEM + 10240;
constexpr int L_Q = L_QM + 12288;
constexpr int L_W = 94208;
#define SLOT(i) ((i) % 3 == 0 ? 45056 : (i) % 3 == 1 ? 69632 : L_W + 40960)
#define CMIN(a, b) ((a) < (b) ? (a) : (b))
#define IC(x) std::integral_constant<int, (x)>{}
#define CV(X) decltype(X)::value
DEVI bf16x8 lr128(unsigned a) { return *(const LAS bf16x8*)(size_t)a; }
DEVI bf16x4 lrtr(unsigned a) { return __builtin_amdgcn_ds_read_tr16_b64_v4i16((LAS bf16x4*)(size_t)a); }
#define CAT4(a, b) __builtin_shufflevector(a, b, 0, 1, 2, 3, 4, 5, 6, 7)
DEVI void lw128(unsigned a, u32x4 v) { *(LAS u32x4*)(size_t)a = v; }
DEVI void lw64(unsigned a, u32x2 v) { *(LAS u32x2*)(size_t)a = v; }
DEVI void lw32(unsigned a, unsigned v) { *(LAS unsigned*)(size_t)a = v; }
template <int N> DEVI void bar_lgkm() { asm volatile("s_waitcnt lgkmcnt(%0)\n\ts_barrier" :: "n"(N) : "memory"); }
DEVI void atom1(__amdgpu_buffer_rsrc_t rs, unsigned voff, unsigned soff) {
    unsigned one; asm volatile("v_mov_b32 %0, 1\n\tbuffer_atomic_add %0, %1, %2, %3 offen" : "=&v"(one) : "v"(voff), "s"(rs), "s"(soff) : "memory");
}
DEVI unsigned vadd(unsigned v, unsigned sc) { unsigned d; asm volatile("v_add_u32 %0, %1, %2" : "=v"(d) : "s"(sc), "v"(v)); return d; }
DEVI float vzero() { float z; asm volatile("v_mov_b32 %0, 0" : "=v"(z)); return z; }
template <int S> DEVI unsigned slot_adr(unsigned a) { if constexpr (S % 3 == 2) return vadd(a, SLOT(2) - SLOT(0)); else return a + (SLOT(S) - SLOT(0)); }
template <int B, int I> DEVI void xn_op(const Ctx& k, unsigned v, unsigned src, unsigned dst) {
    if constexpr (I == 0) set_m0(dst + B * 8192);
    else if constexpr (I == 1) dmab<0>(k.rs, v, src + B * 16384);
    else dmab<1024>(k.rs, v, src + B * 16384);
}
DEVI void xn_all(const Ctx& k, unsigned v, unsigned src, unsigned dst) {
    sfor<5>([&](auto B) { xn_op<CV(B), 0>(k, v, src, dst); asm volatile("s_nop 0"); xn_op<CV(B), 1>(k, v, src, dst); xn_op<CV(B), 2>(k, v, src, dst); });
}
struct Xn {
    unsigned src, dst, sg, need, t, sb, se, l16, c, off;
    unsigned long long flip, m;
    DEVI void init(const Ctx& k) {
        src = k.seg(PG_XNA, 0) + k.w * 2048; dst = L_XN + k.w * 2048; sg = 0; off = ~0u;
        const unsigned q = (unsigned)(k.par >> PG_XNA), d = q ^ (q >> 1);
        flip = ((unsigned long long)(d & 1) << 16) | ((unsigned long long)(d & 2) << 31) | ((unsigned long long)(d & 4) << 46);
    }
    template <int I> DEVI void next(const Ctx& k, unsigned l16) {
        if constexpr (I == 0) { sg += 1; src += 131072; PINS(sg); PINS(src); }
        if constexpr (I == 1) asm volatile("s_bitcmp1_b64 %1, %2\n\ts_cselect_b32 %0, 0x2000, 0" : "=s"(t) : "s"(flip), "s"(sg) : "scc");
        if constexpr (I == 2) { src ^= t; PINS(src); }
        if constexpr (I == 3) asm volatile("v_and_b32 %0, 0x380, %1" : "=v"(c) : "v"(l16));
        if constexpr (I == 4) c = ld32(k.rs, c, k.p0 + CNT(C_XN, 0));
        if constexpr (I == 5) { need = sg - (k.c ? 0u : 8u); PINS(need); }
        if constexpr (I == 6) { need = sg < 64u ? need : 0x7FFFFFFFu; PINS(need); }
        if constexpr (I == 7) asm volatile("v_cmp_gt_i32_e64 %0, %1, %2" : "=s"(m) : "s"(need), "v"(c) : "memory");
        if constexpr (I == 8) asm volatile("s_cmp_lg_u64 %2, 0\n\ts_cselect_b32 %0, -1, 0\n\ts_cselect_b32 %1, 0x200000, %3" : "=&s"(off), "=s"(se) : "s"(m), "s"(src) : "scc");
        if constexpr (I == 13) set_m0(L_MEM);
        if constexpr (I == 14) dmab<0>(k.rs, c, k.p0 + CNT(C_XN, 0));
        if constexpr (I == 15) c = *(volatile LAS unsigned*)(size_t)(l16 + L_MEM);
        if constexpr (I == 11) { need = sg < 64u ? need : 0x80000000u; PINS(need); }
        if constexpr (I == 12) {
            if (__builtin_expect(m != 0, 0)) {
_Pragma("unroll 1")
                for (unsigned spins = 0; __any((int)*(volatile LAS unsigned*)(size_t)(l16 + L_MEM) < (int)need) && !spin_fail(k, spins >> 2); ++spins) { next<3>(k, l16); __builtin_amdgcn_s_sleep(1); next<14>(k, l16); }
            }
            se = src; }
        if constexpr (I == 10) asm volatile("s_cmp_lg_u32 %1, 64\n\ts_cselect_b32 %0, %0, 0" : "+s"(off) : "s"(sg) : "scc");
    }
    template <int B, int I> DEVI void op(const Ctx& k) {
        if constexpr (I == 0) { set_m0(dst + B * 8192); sb = se + B * 16384; PINS(sb); }
        else dmab<(I - 1) * 1024>(k.rs, l16, sb);
    }
    template <int NB> DEVI void late(const Ctx& k, unsigned l16) {
        xn_wait(k, l16, sg);
        se = src;
        sfor<NB>([&](auto B) { op<CV(B), 0>(k); asm volatile("s_nop 0"); op<CV(B), 1>(k); op<CV(B), 2>(k); });
        DRAIN();
    }
};
template <int OFF> DEVI u32x4 ald128(__amdgpu_buffer_rsrc_t rs, unsigned v, unsigned s) { u32x4 d; asm volatile("buffer_load_dwordx4 %0, %1, %2, %3 offen offset:%4 sc1" : "=v"(d) : "v"(v), "s"(rs), "s"(s), "n"(OFF) : "memory"); return d; }
#define MADR(b) (L_MEM + ((b) < 6 ? ((b) - 1) * 2048 : ((b) - 6) * 2048 + 3072))
struct Gather {
    u32x4 p0, p1, v0, v1;
    unsigned mx, a, aw, b, base, wo, dst;
    unsigned long long bad;
    DEVI void init(const Ctx& k) { base = k.memf(0); wo = k.w * 1024; dst = L_MEM + k.w * 2048; }
    template <int I> DEVI void op(const Ctx& k, unsigned l16, int s) {
        if constexpr (I == 0) { a = ((unsigned)s << 15) & 0x1F8000u; PINS(a); }
        if constexpr (I == 1) { a += base; aw = a + wo; b = aw + 3072 + (k.w >> 1) * 8192; PINS(a); PINS(aw); PINS(b); }
        if constexpr (I == 2) set_m0(dst);
        if constexpr (I == 3) v0 = ald128<2048>(k.rs, l16, aw);
        if constexpr (I == 4) v1 = ald128<3072>(k.rs, l16, b);
        if constexpr (I == 5) dmab<2048>(k.rs, l16, aw);
        if constexpr (I == 6) dmab<3072>(k.rs, l16, b);
        if constexpr (I == 7) p0 = ald128<0>(k.rs, l16, a);
        if constexpr (I == 8) p1 = ald128<1024>(k.rs, l16, a);
    }
    template <int N> DEVI void wait() { asm volatile("s_waitcnt vmcnt(%4)" : "+v"(p0), "+v"(p1), "+v"(v0), "+v"(v1) : "n"(N) : "memory"); }
    template <int I> DEVI void fin(unsigned other) {
#define MX3 "v_max3_u32 %0, %0, %3, %4\n\tv_max3_u32 %0, %0, %5, %6"
        if constexpr (I == 0) asm volatile("v_max3_u32 %0, %7, %1, %2\n\t" MX3 : "=&v"(mx) : "v"(v0[0]), "v"(v0[1]), "v"(v0[2]), "v"(v0[3]), "v"(v1[0]), "v"(v1[1]), "s"(other));
        if constexpr (I == 1) asm volatile("v_max3_u32 %0, %0, %1, %2\n\t" MX3 : "+v"(mx) : "v"(v1[2]), "v"(v1[3]), "v"(p0[0]), "v"(p0[1]), "v"(p0[2]), "v"(p0[3]));
        if constexpr (I == 2) asm volatile("v_max3_u32 %1, %1, %2, %3\n\tv_max3_u32 %1, %1, %4, %5\n\tv_cmp_eq_u32_e64 %0, -1, %1" : "=s"(bad) : "v"(mx), "v"(p1[0]), "v"(p1[1]), "v"(p1[2]), "v"(p1[3]));
    }
    DEVI void also(unsigned other) { asm volatile("v_mov_b32 %0, %1" : "=v"(mx) : "s"(other)); }
    DEVI void all() {
        asm volatile("s_waitcnt vmcnt(0)\n\tv_max3_u32 %1, %1, %2, %3\n\tv_max3_u32 %1, %1, %4, %5\n\tv_max3_u32 %1, %1, %6, %7\n\tv_max3_u32 %1, %1, %8, %9\n\tv_max3_u32 %1, %1, %10, %11\n\tv_max3_u32 %1, %1, %12, %13\n\t"
            "v_max3_u32 %1, %1, %14, %15\n\tv_max3_u32 %1, %1, %16, %17\n\tv_cmp_eq_u32_e64 %0, -1, %1" : "=s"(bad) : "v"(mx), "v"(v0[0]), "v"(v0[1]), "v"(v0[2]), "v"(v0[3]), "v"(v1[0]), "v"(v1[1]), "v"(v1[2]), "v"(v1[3]),
            "v"(p0[0]), "v"(p0[1]), "v"(p0[2]), "v"(p0[3]), "v"(p1[0]), "v"(p1[1]), "v"(p1[2]), "v"(p1[3]) : "memory");
    }
    DEVI void check() { also(0); all(); asm volatile("s_nop 3"); }
    template <int I> DEVI void early(unsigned other) {
        if constexpr (I == 0) asm volatile("s_waitcnt vmcnt(5)\n\tv_max3_u32 %0, %5, %1, %2\n\tv_max3_u32 %0, %0, %3, %4" : "=&v"(mx) : "v"(v0[0]), "v"(v0[1]), "v"(v0[2]), "v"(v0[3]), "s"(other) : "memory");
        if constexpr (I == 1) __builtin_amdgcn_s_setprio(0);
        if constexpr (I == 2) asm volatile("s_waitcnt vmcnt(4)\n\tv_max3_u32 %0, %0, %1, %2\n\tv_max3_u32 %0, %0, %3, %4" : "+v"(mx) : "v"(v1[0]), "v"(v1[1]), "v"(v1[2]), "v"(v1[3]) : "memory");
    }
    DEVI void rest() {
        asm volatile("s_waitcnt vmcnt(1)\n\tv_max3_u32 %1, %1, %2, %3\n\tv_max3_u32 %1, %1, %4, %5\n\ts_waitcnt vmcnt(0)\n\tv_max3_u32 %1, %1, %6, %7\n\tv_max3_u32 %1, %1, %8, %9\n\tv_cmp_eq_u32_e64 %0, -1, %1" : "=s"(bad) : "v"(mx),
            "v"(p0[0]), "v"(p0[1]), "v"(p0[2]), "v"(p0[3]), "v"(p1[0]), "v"(p1[1]), "v"(p1[2]), "v"(p1[3]) : "memory");
    }
    unsigned rv, rs, hb; unsigned long long rt;
    template <int I> DEVI void ask(const Ctx& k, unsigned l16) {
        if constexpr (I == 0) asm volatile("v_and_b32 %0, 0xf0, %1\n\tv_lshlrev_b32 %0, 1, %0" : "=v"(rv) : "v"(l16));
        if constexpr (I == 1) asm volatile("buffer_load_dword %0, %0, %1, %2 offen sc1" : "+v"(rv) : "s"(k.rs), "s"(k.p0 + CNT(C_XB, 0)) : "memory");
    }
    template <int I, int N = 4> DEVI void red() {
        if constexpr (I == 0 && N < 0) asm volatile("v_max_u32_dpp %0, %0, %0 quad_perm:[1,0,3,2] row_mask:0xf bank_mask:0xf" : "+v"(rv));
        else if constexpr (I == 0) asm volatile("s_waitcnt vmcnt(%1)\n\tv_max_u32_dpp %0, %0, %0 quad_perm:[1,0,3,2] row_mask:0xf bank_mask:0xf" : "+v"(rv) : "n"(N));
        if constexpr (I == 1) asm volatile("v_max_u32_dpp %0, %0, %0 quad_perm:[2,3,0,1] row_mask:0xf bank_mask:0xf" : "+v"(rv));
        if constexpr (I == 2) asm volatile("v_max_u32_dpp %0, %0, %0 row_ror:4 row_mask:0xf bank_mask:0xf" : "+v"(rv));
        if constexpr (I == 3) asm volatile("v_max_u32_dpp %0, %0, %0 row_ror:8 row_mask:0xf bank_mask:0xf" : "+v"(rv));
        if constexpr (I == 4) asm volatile("v_readfirstlane_b32 %0, %1" : "=s"(rs) : "v"(rv));
    }
    template <int I> DEVI void hold(unsigned step) {
        if constexpr (I == -1) rt = rt_async();
        if constexpr (I == 3) hb = spack((unsigned)rt, step);
        if constexpr (I == 4) asm volatile("s_sub_u32 %0, %1, %0" : "+s"(hb) : "s"(rs) : "scc");
        if constexpr (I == 5) if (__builtin_expect(hb < 128u, 0)) { while ((int)((rclk() - rs) << 16) < 0) { } }
    }
    template <int I> DEVI void at(const Ctx& k, unsigned l16) {
        if constexpr (I == 1) op<2>(k, l16, 0);
        if constexpr (I >= 2 && I < 15 && (I % 5 == 2 || I % 5 == 4)) op<3 + (I / 5) * 2 + (I % 5) / 4>(k, l16, 0);
    }
    DEVI void again(const Ctx& k, unsigned l16) {
        asm volatile("s_mov_b32 m0, %9\n\tbuffer_load_dwordx4 %2, %4, %5, %7 offen offset:2048 sc1\n\tbuffer_load_dwordx4 %3, %4, %5, %8 offen offset:3072 sc1\n\t"
            "buffer_load_dwordx4 %4, %5, %7 offen offset:2048 sc1 lds\n\tbuffer_load_dwordx4 %4, %5, %8 offen offset:3072 sc1 lds\n\t"
            "buffer_load_dwordx4 %0, %4, %5, %6 offen sc1\n\tbuffer_load_dwordx4 %1, %4, %5, %6 offen offset:1024 sc1\n\ts_waitcnt vmcnt(0)"
            : "+v"(p0), "+v"(p1), "+v"(v0), "+v"(v1) : "v"(l16), "s"(k.rs), "s"(a), "s"(aw), "s"(b), "s"(dst) : "memory");
        check();
    }
};
DEVI void clr2(f32x4& c) { f32x2 a, b; asm volatile("v_mov_b64 %0, 0\n\tv_mov_b64 %1, 0" : "=v"(a), "=v"(b)); c = __builtin_shufflevector(a, b, 0, 1, 2, 3); }
#define NOP7_3(a) asm volatile("s_nop 7" : "+v"(a[0]), "+v"(a[1]), "+v"(a[2]))

struct G3 {
    f32x4 a0[3], a1[3];
    bf16x8 x0[20], x1[20], wl[20];
    unsigned l16, lwv;
    template <int KS> DEVI void r0() { x0[KS] = lr128(l16 + L_XN + KS * 2048); }
    template <int KS> DEVI void r1() { x1[KS] = lr128(l16 + L_XN + KS * 2048 + 1024); }
    template <int KS> DEVI void rw() { if constexpr (KS >= 4) wl[KS] = lr128(lwv + (KS - 4) * 4096); }
    template <bool WL, int KS> DEVI void pre() { if constexpr (WL && KS >= 4) asm volatile("" :: "v"(wl[KS]), "v"(x0[KS]), "v"(x1[KS])); else asm volatile("" :: "v"(x0[KS]), "v"(x1[KS])); }
    template <int ID0, bool WL, int ks, int m> DEVI void mm(const bf16x16 (&W)[52]) {
        constexpr int b = ks >> 1, t = m % 3, id = ID0 + t * 10 + b;
        f32x4& c = m < 3 ? a0[t] : a1[t];
        const bf16x8& x = m < 3 ? x0[ks] : x1[ks];
        if constexpr (WL && t == 2 && b >= 2) { if constexpr (ID0 == 30) mfb<false, false>(c, x, wl[ks]); else mfa<false, false>(c, wl[ks], x); }
        else mfw<id % 52, ks & 1, ID0 == 30, ks == 0>(c, W[id % 52], x);
        SCHED0();
    }
    template <int ID0, bool WL, int K0, int K1, int PF, bool H6 = false, class HOOK> DEVI void run(const bf16x16 (&W)[52], HOOK hook) {
        sfor<K1 - K0>([&](auto KK) {
            constexpr int kk = CV(KK), ks = K0 + kk;
            sfor<6>([&](auto M) {
                constexpr int m = CV(M);
                mm<ID0, WL, ks, m>(W);
                if constexpr (m == 5 && ks + 1 < K1) { pre<WL, (ks + 1) % 20>(); SCHED0(); }
                if constexpr (ks + 2 < PF) {
                    if constexpr (m == 0) r0<(ks + 2) % 20>();
                    if constexpr (m == 1) r1<(ks + 2) % 20>();
                    if constexpr (m == 3 && WL) rw<(ks + 2) % 20>();
                }
                if constexpr (H6) hook(IC(kk * 6 + m));
                else { hook(IC(kk * 7 + (m < 5 ? m : 6))); if constexpr (m == 4) hook(IC(kk * 7 + 5)); }
                SCHED0();
            });
        });
    }
    template <int ID0, bool WL, int F0, int F1, int PF, class HOOK> DEVI void go(const bf16x16 (&W)[52], HOOK hook) {
        sfor<F1 - F0>([&](auto FF) {
            constexpr int f = F0 + CV(FF), ks = f / 6, m = f % 6;
            mm<ID0, WL, ks, m>(W);
            if constexpr (m == 5 && f + 1 < F1) { pre<WL, (ks + 1) % 20>(); SCHED0(); }
            if constexpr (ks + 2 < PF) rd<WL, (f + 12) % 120>();
            hook(IC(f));
            SCHED0();
        });
    }
    template <int P> DEVI void home() {
        if constexpr (P == 0) asm volatile("" : "+{v[12:15]}"(a0[0]), "+{v[16:19]}"(a0[1]), "+{v[20:23]}"(a0[2]), "+{v[24:27]}"(a1[0]), "+{v[28:31]}"(a1[1]), "+{v[32:35]}"(a1[2]));
        else asm volatile("" : "+{v[36:39]}"(a0[0]), "+{v[40:43]}"(a0[1]), "+{v[44:47]}"(a0[2]), "+{v[48:51]}"(a1[0]), "+{v[52:55]}"(a1[1]), "+{v[56:59]}"(a1[2]));
    }
    template <bool WL, int F> DEVI void rd() {
        if constexpr (F % 6 == 0) r0<F / 6>();
        if constexpr (F % 6 == 1) r1<F / 6>();
        if constexpr (F % 6 == 3 && WL) rw<F / 6>();
    }
    u32x2 o;
    template <int I, int OFF> DEVI void out4(unsigned d0) {
        if constexpr (I >= 0 && I < 24) {
            constexpr int t = I / 8, rt = (I / 4) & 1, j = I & 1;
            const f32x4& a = rt ? a1[t] : a0[t];
            if constexpr (I % 4 < 2) { o[j] = cvtpk(a[2 * j], a[2 * j + 1]); PIN(o[j]); }
            else lw32(d0 + OFF + rt * 6144 + t * 1024 + j * 4, o[j]);
        }
    }
    template <int I, int OFF> DEVI void out(unsigned d0, unsigned d1) {
        if constexpr (I >= 0 && I < 18) {
            constexpr int t = I / 6, rt = (I / 3) & 1;
            const f32x4& a = rt ? a1[t] : a0[t];
            if constexpr (I % 3 == 0) { o[0] = cvtpk(a[0], a[1]); PIN(o[0]); }
            else if constexpr (I % 3 == 1) { o[1] = cvtpk(a[2], a[3]); PIN(o[1]); }
            else if constexpr (OFF > 0) lw64(d0 + (OFF - 1) + rt * 6144 + t * 1024, o);
            else lw64((t == 1 ? d1 : d0) + OFF + rt * 6144 + (t == 2 ? 1024 : 0), o);
        }
    }
};
#define SPIX(l) (((l) & 2) ? 0xEEEEu : 0x4444u)
template <int I> DEVI void fold_op(f32x4& a, unsigned& o) {
    if constexpr (I == 0) asm volatile("v_add_f32 %0, %0, %1" : "+v"(a[0]) : "v"(a[2]));
    if constexpr (I == 1) asm volatile("v_add_f32 %0, %0, %1" : "+v"(a[1]) : "v"(a[3]));
    if constexpr (I == 2) asm volatile("v_cvt_pk_bf16_f32 %0, %1, %2" : "=v"(o) : "v"(a[0]), "v"(a[1]));
}

template <int J> DEVI void fold2(f32x4& a, f32x4& b, unsigned (&o)[2]) {
    if constexpr (J < 2) asm volatile("v_add_f32 %0, %0, %2\n\tv_add_f32 %1, %1, %3" : "+v"(a[J]), "+v"(b[J]) : "v"(a[J + 2]), "v"(b[J + 2]));
    else asm volatile("v_cvt_pk_bf16_f32 %0, %2, %3\n\tv_cvt_pk_bf16_f32 %1, %4, %5" : "=&v"(o[0]), "=&v"(o[1]) : "v"(a[0]), "v"(a[1]), "v"(b[0]), "v"(b[1]));
}
DEVI unsigned vshl1(unsigned x, unsigned b) { unsigned d; asm volatile("v_lshl_or_b32 %0, %1, 1, %2" : "=v"(d) : "s"(x), "n"(b)); return d; }

#define ANNOUNCE(s, now, d) (((unsigned)(s) << 16) | (((unsigned)(now) + (d)) & 0xFFFFu))

template <class PUT> DEVI void ln_rows(const u32x4 (&a)[5], int n, int row  , PUT put) {
    float v[40];
#pragma unroll
    for (int i = 0; i < 20; ++i) { v[2 * i] = blo(a[i >> 2][i & 3]); v[2 * i + 1] = bhi(a[i >> 2][i & 3]); }
    f32x2 s2[4] = {{0.f, 0.f}, {0.f, 0.f}, {0.f, 0.f}, {0.f, 0.f}};
#pragma unroll
    for (int i = 0; i < 40; i += 2) s2[(i >> 1) & 3] += (f32x2){v[i], v[i + 1]};
    const f32x2 st = (s2[0] + s2[1]) + (s2[2] + s2[3]);
    float m = row16sum(st[0] + st[1]) * (1.f / 640.f);
    f32x2 q2[4] = {{0.f, 0.f}, {0.f, 0.f}, {0.f, 0.f}, {0.f, 0.f}};
#pragma unroll
    for (int i = 0; i < 40; i += 2) { const f32x2 d = (f32x2){v[i], v[i + 1]} - (f32x2){m, m}; q2[(i >> 1) & 3] += d * d; }
    const f32x2 qt = (q2[0] + q2[1]) + (q2[2] + q2[3]);
    const float q = row16sum(qt[0] + qt[1]);
    const float var = bfr(q * (1.f / 640.f));
    m = bfr(m);
    const float rden = __builtin_amdgcn_rcpf(bfr(__builtin_amdgcn_sqrtf(bfr(var + 1e-5f))));
    const unsigned vo = (n >> 2) * 2048 + (row >> 4) * 1024 + ((n & 3) * 16 + (row & 15)) * 16;
#pragma unroll
    for (int c = 0; c < 5; ++c) {
        u32x4 o;
#pragma unroll
        for (int i = 0; i < 4; ++i) { unsigned d = cvtpk(v[c * 8 + 2 * i] - m, v[c * 8 + 2 * i + 1] - m); o[i] = cvtpk(blo(d) * rden, bhi(d) * rden); }
        put(vo, c, o);
    }
}

struct Soft {
    unsigned long long mA, mY, mC[8];
    unsigned v0, v1;
    unsigned sg, hs;
    float a[6], m, M, t, s, h; u32x2 pk;
    template <int I> DEVI void mask(f32x4 (&sc)[3]) {
        if constexpr (I == 0) asm volatile("v_mov_b32 %0, 0xfe800000" : "=v"(t));
        if constexpr (I >= 1 && I < 9) { float d; asm volatile("v_cndmask_b32_e64 %0, %1, 0, %2" : "=v"(d) : "v"(t), "s"(mC[I - 1])); sc[(I - 1) >> 2][(I - 1) & 3] = d; }
    }
    template <int I> DEVI void op(const Ctx& k, f32x4 (&sc)[3], unsigned a0, unsigned a1, unsigned hv) {
#define FMA(x, y) "v_fmamk_f32 " x ", " x ", 0x3dd53b95, " y "\n\t"
#define EXPC(x) "v_exp_f32_e64 " x ", " x " clamp\n\t"
#define OP3(o, d, x, y) o " " d ", " x ", " y "\n\t"
#define SA(txt) asm volatile(txt : "+v"(a[0]), "+v"(a[1]), "+v"(a[2]), "+v"(a[3]), "+v"(m), "+v"(t))
#define SB(txt) asm volatile(txt : "+v"(a[0]), "+v"(a[1]), "+v"(a[2]), "+v"(a[3]), "+v"(m), "+v"(t), "+v"(s), "+v"(a[4]), "+v"(a[5]), "+v"(M), "+v"(h) : "s"(hs), "s"(sg))
#define ADD(d, x, y) OP3("v_add_f32", d, x, y)
#define MUL(d, x, y) OP3("v_mul_f32", d, x, y)
#define CND(d, x, y, c) "v_cndmask_b32_e64 " d ", " x ", " y ", " c "\n\t"
        if constexpr (I == 0) asm volatile("v_max3_f32 %0, %1, %2, %3\n\tv_max3_f32 %0, %0, %4, %5" : "=&v"(m) : "v"(sc[0][0]), "v"(sc[0][1]), "v"(sc[0][2]), "v"(sc[0][3]), "v"(sc[1][0]));
        if constexpr (I == 1) asm volatile("v_max3_f32 %0, %0, %2, %3\n\t" CND("%1", "%4", "%5", "%6") : "+v"(m), "=&v"(a[2]) : "v"(sc[1][1]), "v"(sc[1][2]), "v"(sc[0][2]), "v"(sc[1][0]), "s"(mY));
        if constexpr (I == 2) asm volatile("v_max_f32 %0, %0, %2\n\t" CND("%1", "%3", "%4", "%5") : "+v"(m), "=&v"(a[3]) : "v"(sc[1][3]), "v"(sc[0][3]), "v"(sc[1][1]), "s"(mY));
        if constexpr (I == 3) asm volatile("v_mul_f32 %1, 0xbdd53b95, %5\n\t" CND("%0", "%2", "%3", "%4") : "=&v"(a[0]), "=&v"(t) : "v"(sc[0][0]), "v"(a[2]), "s"(mA), "v"(m));
        if constexpr (I == 4) asm volatile(CND("%0", "%2", "%3", "%5") CND("%1", "%1", "%4", "%5") : "=&v"(a[1]), "+v"(a[2]) : "v"(sc[0][1]), "v"(a[3]), "v"(sc[1][2]), "s"(mA));
        if constexpr (I == 5) asm volatile(CND("%0", "%0", "%2", "%3") FMA("%1", "%4") : "+v"(a[3]), "+v"(a[0]) : "v"(sc[1][3]), "s"(mA), "v"(t));
        if constexpr (I == 6) SA(FMA("%1", "%5") FMA("%2", "%5"));
        if constexpr (I == 7) asm volatile(FMA("%0", "%3") "v_mov_b32 %1, %4" : "+v"(a[3]), "=&v"(h), "=v"(s) : "v"(t), "s"(hv));
        if constexpr (I == 8) SA(EXPC("%0"));
        if constexpr (I == 9) SA(EXPC("%1"));
        if constexpr (I == 10) { asm volatile("v_max3_f32 %0, %2, %3, %4\n\tv_mov_b32 %1, %0" : "=&v"(M), "=&v"(t) : "v"(m), "v"(sc[2][0]), "v"(sc[2][1]), "v"(sc[2])); a[4] = sc[2][0]; a[5] = sc[2][1]; }
        if constexpr (I == 11) SB("v_permlane16_swap_b32 %9, %5");
        if constexpr (I == 12) SB(OP3("v_max_f32", "%9", "%9", "%5") "v_mov_b32 %5, %9");
        if constexpr (I == 13) SB("v_permlane32_swap_b32 %9, %5");
        if constexpr (I == 14) SB(OP3("v_max_f32", "%9", "%9", "%5") "v_mul_f32 %9, 0xbdd53b95, %9");
        if constexpr (I == 15) SB(FMA("%7", "%9") FMA("%8", "%9"));
        if constexpr (I == 16) SB(FMA("%4", "%9") ADD("%6", "%0", "%1"));
        if constexpr (I == 17) SB(EXPC("%2"));
        if constexpr (I == 18) SB(EXPC("%3"));
        if constexpr (I == 19) SB(ADD("%6", "%6", "%2") ADD("%6", "%6", "%3"));
        if constexpr (I == 20) SB(EXPC("%7"));
        if constexpr (I == 21) SB(EXPC("%8"));
        if constexpr (I == 22) SB(MUL("%6", "%12", "%6") ADD("%5", "%7", "%8"));
        if constexpr (I == 23) SB(EXPC("%4"));
        if constexpr (I == 24) SB(MUL("%6", "%6", "%4") OP3("v_fmac_f32", "%6", "%11", "%5"));
        if constexpr (I == 25) SB("v_fmac_f32_dpp %6, %6, %10 row_ror:8 row_mask:0xf bank_mask:0xf\n\tv_mov_b32 %5, %6");
        if constexpr (I == 26) SB("v_permlane16_swap_b32 %6, %5");
        if constexpr (I == 27) SB(ADD("%6", "%6", "%5") "v_mov_b32 %5, %6");
        if constexpr (I == 28) SB("v_permlane32_swap_b32 %6, %5");
        if constexpr (I == 29) SB(ADD("%6", "%6", "%5"));
        if constexpr (I == 30) SB("v_rcp_f32 %6, %6");
        if constexpr (I == 31) SB(MUL("%4", "%4", "%6") MUL("%7", "%7", "%6"));
        if constexpr (I == 32) SB(MUL("%0", "%0", "%4") MUL("%1", "%1", "%4"));
        if constexpr (I == 33) SB(MUL("%2", "%2", "%4") MUL("%3", "%3", "%4"));
        if constexpr (I == 34) { unsigned x, y; asm volatile("v_cvt_pk_bf16_f32 %0, %2, %3\n\tv_cvt_pk_bf16_f32 %1, %4, %5" : "=&v"(x), "=v"(y) : "v"(a[0]), "v"(a[1]), "v"(a[2]), "v"(a[3])); pk[0] = x; pk[1] = y; }
        if constexpr (I == 35) { st64(k.rs, v0, a0, pk); SCHED0(); asm volatile("v_mul_f32 %1, %1, %2\n\tv_cvt_pk_bf16_f32 %0, %0, %1" : "+v"(a[4]), "+v"(a[5]) : "v"(s)); }
        if constexpr (I == 36) st32(k.rs, v1, a1, __float_as_uint(a[4]));
    }
};
#define QIDX(t, b) ((t) * 10 + (b))
DEVI void qk_role(const Params& p, Ctx& k, char* smem, int h) {
    PROF_DECL
    const int w = k.w;
    bf16x16 W[52];
    Gather G;
    Soft S;
    const int hw = h >> 3, hh = h & 7;
    unsigned l16, ix, dqm, dq, trp, qp;
    {
        const int l = k.l, g = l >> 4, n = l & 15, r = n & 7, u = w & 1;
        const bool hf = n >= 8, dup = w != 1;
        S.mA = __ballot(dup && hf);
        const int lim = (w == 0 ? 64 : w == 1 ? n : 16 + 8 * u + r) - 4 * g;
#pragma unroll
        for (int i = 0; i < 8; ++i) S.mC[i] = __ballot((i & 3) + (i >> 2) * 16 <= lim);
        const bool okm = !(dup && hf);
        S.mY = __ballot((dup && hf) == (w != 0));
        S.v0 = w == 0 ? hh * 512 + ((g >> 1) * 16 + (r >> 1) * 4 + hf * 2 + (r & 1)) * 16 + (g & 1) * 8 : w == 1 ? (hh >> 1) * 1024 + l * 16 + (hh & 1) * 8 : hh * 1024 + (g * 16 + 8 * u + r) * 16 + hf * 8;
        S.v1 = !okm ? OOB : w == 0 ? 4096 + hh * 128 + r * 16 + g * 4 : (w == 1 ? 4096 + n * 16 : 6144 + (8 * u + r) * 16) + (hh >> 1) * 512 + (g >> 1) * 256 + (hh & 1) * 8 + (g & 1) * 4;
        l16 = l * 16; ix = SPIX(l);
        const int uu = u ^ ((n >> 2) & 1);
        dqm = L_QM + (w >> 1) * 3072 + uu * 512 + (g >> 1) * 128 + n * 8 + (g & 1) * 4;
        dq = L_QM + 6144 + (w >> 1) * 3072 + uu * 512 + g * 128 + ((n * 8) ^ ((g >> 1) * 64));
        trp = L_QM + 6144 + (l & 3) * 128 + (((l >> 2) * 8) ^ ((l & 2) * 32));
        qp = w == 1 ? trp + 6144 : (w == 0 ? L_QM : L_Q + 6144 + u * 256) + (l & 1) * 128 + (((l >> 2) * 8) ^ (w == 3 ? 64 : 0));
        u32x8 rg = {S.v0, S.v1, l16, ix, dqm, dq, trp, qp};
        asm volatile("" : "+{v[0:7]}"(rg));
        S.v0 = rg[0]; S.v1 = rg[1]; l16 = rg[2]; ix = rg[3]; dqm = rg[4]; dq = rg[5]; trp = rg[6]; qp = rg[7];
    }
    Xn X;
    G3 A, B;
    X.l16 = l16; A.l16 = l16; B.l16 = l16; B.lwv = vadd(l16, L_W + w * 1024);
    {
        const int l = k.l;
        bf16x8 wt[16];
        u32x2 gv;
        unsigned ra[3];
#pragma unroll
        for (int t = 0; t < 3; ++t) ra[t] = opq(wp_adr<384, true>(w * 3 + t, l) + SLOT(0));
#define QEX(v) ((v) < 0 ? 0 : ((v) < 6) + ((v) == QGS) + 10 * ((v) == QGS + 2) + 8 * ((v) == QGS + 7))
#define QKE(v) ((v) < QGS + 4 ? 0 : (v) == 19 ? 20 : CMIN(v, 3 * ((v) - QGS - 3)))
        auto rdx = [&](auto K) { constexpr int ks = CV(K); A.r0<ks>(); A.r1<ks>(); B.x0[ks] = A.x0[ks]; B.x1[ks] = A.x1[ks]; if constexpr (ks < 14 && QKE(ks + 1) <= ks) B.rw<ks>(); };
        cstream<20>([](auto V) { constexpr int v = CV(V); return IC(QEX(v - 1) + (v < 19 ? 6 : 0)); },
            [&](auto U) { constexpr int u = CV(U), b = u >> 1, k0 = QKE(u - 1), n = (QKE(u) - k0) * 12, nn = n ? n : 12;
                if constexpr (u < 6) hello(k, 0);
                if constexpr (u == QGS) gv = go_ask(k);
                if constexpr (u == QGS + 2) { PIN(gv); go_wait(k, 0, gv); X.init(k); G.init(k); X.l16 = k.c ? OOB : l16; xn_all(k, X.l16, X.src, X.dst); }
                if constexpr (u == QGS + 4) rdx(IC(0));
                if constexpr (u == QGS + 7) { sfor<3>([&](auto I) { X.next<CV(I)>(k, l16); G.op<CV(I)>(k, l16, 0); }); X.se = X.src; }
                if constexpr (u == 18) {
                    asm volatile("" :: "{v[152:155]}"(wt[10]), "{v[156:159]}"(wt[11]), "{v[232:235]}"(wt[12]));
#pragma unroll
                    for (int i = 10; i < 13; ++i) lw128(B.lwv + i * 4096, __builtin_bit_cast(u32x4, wt[i]));
                }
                if constexpr (u == 19) { G.wait<0>(); lw128(l16 + G.dst + 2048, G.v0); lw128(l16 + G.dst + 3072, G.v1); }
                XSTAMP(u)
                const unsigned rb[3] = {slot_adr<u>(ra[0]), slot_adr<u>(ra[1]), slot_adr<u>(ra[2])};
                bf16x4 hf[12];
                SCHED0();
                sfor<nn>([&](auto I) { constexpr int i = CV(I), ks = k0 + i / 12, m = i % 12, t = i >> 1, e = u == QGS + 7 ? 8 : 0, j = ((i - e) * 6) / (nn - e);
                    if constexpr (n > 0) { if constexpr (m < 6) A.mm<0, false, ks, m>(W); else B.mm<30, true, ks, m - 6>(W); }
                    if constexpr (i < e) { if constexpr (i % 4 == 0) X.op<i / 4, 0>(k); if constexpr (i & 1) X.op<i / 4, 1 + (i / 2) % 2>(k); else G.op<3 + i / 4 + (i & 2) * 2>(k, l16, 0); }
                    else if constexpr (u > 0 && u < 18 && (i == e || ((i - e - 1) * 6) / (nn - e) != j))
                        dma1<SLOT(u + 2) + (j / 3) * 12288 + (j % 3) * 1024>((j < 3 ? p.Wk : p.Wq) + (size_t)(u + 2) * 32 * 6144 + h * 192, wsrc<384, true>(w * 192 + (j % 3) * 64 + l, 12288), w * 3072);
                    if constexpr (i == 0 && u > 4 && (u < 15 || u >= 18)) lw128(B.lwv + (u - 5) * 4096, __builtin_bit_cast(u32x4, wt[(u - 5) & 15]));
                    if constexpr (i < 12) {
                        hf[i] = lrtr(rb[t % 3] + (t / 3) * 12288 + (i & 1) * 1536);
                        if constexpr (i & 1) {
                            if constexpr (t == 5 && b >= 2) B.wl[u] = wt[(u - 4) & 15] = CAT4(hf[i - 1], hf[i]);
                            else if constexpr (u & 1) W[QIDX(t, b) % 52] = CAT8(LO8(W[QIDX(t, b) % 52]), CAT4(hf[i - 1], hf[i]));
                            else W[QIDX(t, b) % 52] = __builtin_shufflevector(hf[i - 1], hf[i], 0, 1, 2, 3, 4, 5, 6, 7, -1, -1, -1, -1, -1, -1, -1, -1);
                        }
                    }
                    if constexpr (n > 0 && m == 3 && ks < 19) rdx(IC((ks + 1) % 20));
                    SCHED0(); }); });
        lw128(B.lwv + 15 * 4096, __builtin_bit_cast(u32x4, wt[15]));
        static_assert(QKE(17) == 17, "QGS");
        RAWBAR()
        XSTAMP(20)
        const unsigned d0 = (l16 | ((l16 >> 5) & 8)) ^ (L_K + (w >> 1) * 3072 + (w & 1) * 8);
        sfor<24>([&](auto I) { A.out4<CV(I), 0>(d0); });
        G.v0 = G.p0; G.v1 = G.p0; G.bad = 0;
        X.off = 0; X.l16 = l16;
        if (k.c) X.init(k);
    }
    RSTAMP(2)
    const unsigned po0 = k.p0 + PR_B + hw * 49152 + (w == 0 ? 0 : w == 1 ? 32768 : 16384), po1 = k.p0 + PR_B + hw * 49152 + (w == 0 ? 0 : 32768);
    const unsigned pdum = k.pg(1) + DUMMY_B, pbc = w == 0 ? k.p0 + CNT(C_BC, hw * 16 + hh) : pdum + EXP(32768 + h * 512 + w * 128);
    unsigned long long now = 0, done = 0, tpa = 0, tpb = 0;
    unsigned a0 = po0 + (k.c ? pdum - k.p0 - PR_B : 0u), a1 = po1 + (k.c ? pdum - k.p0 - PR_B : 0u);
    const unsigned hv = w == 1 ? 0u : 0x3F800000u, hw2 = w == 1 ? 0u : 0x00800000u;
    S.sg = 0x3F800000u; S.hs = 0x3F800000u - hw2;
    f32x4 ac[6];
#pragma unroll
    for (int t = 0; t < 6; ++t) clr2(ac[t]);
    unsigned an = 110;
    __builtin_amdgcn_s_waitcnt(0xc07f);
    do {
        if (X.off != 0) {
            PC8(1, X.sg)
            X.late<2>(k, l16);
            G.check();
        }
_Pragma("unroll 1")
        for (unsigned spins = 0; G.bad != 0 && !spin_fail(k, spins); ++spins) { PC8(0, X.sg) G.again(k, l16); }
        HSTOP(0) HSTAMP(0, 0)
        clr2(ac[5]);
        asm volatile("s_nop 1" : "+v"(ac[5]));
        smfw<50>(ac[5], __builtin_bit_cast(bf16x8, G.p0), W[50], ix);
    do {
        PROF_S(X.sg - 1)
        f32x4 sc[3];
        {
            bf16x8 m[16], wa[10], wb[10], kf[12];
            bf16x4 q[12], km[12];
            unsigned fo[6];
            m[0] = __builtin_bit_cast(bf16x8, G.p0); m[1] = __builtin_bit_cast(bf16x8, G.p1);
            auto put = [&](auto T) { constexpr int t = CV(T); if constexpr (t < 3) lw32(dq + t * 1024, fo[t]); else lw32(dqm + (t - 3) * 1024, fo[t]); };
            auto rd = [&](auto C) { constexpr int c = CV(C), j = c % 20;
                if constexpr (c < 0) { }
                else if constexpr (c < 20) m[j] = lr128(l16 + MADR(j < 10 ? j : j - 8));
                else if constexpr (c < 40) wa[j] = lr128(B.lwv + (j - 2) * 8192);
                else if constexpr (c < 60) wb[j] = lr128(B.lwv + (j - 2) * 8192 + 4096);
                else if constexpr (c < 80) kf[j] = lr128(l16 + L_K + ((j & 1) * 6 + j / 2) * 1024);
                else if constexpr (c < 100) q[j] = lrtr(qp + j * 512);
                else if constexpr (c < 120) km[j] = lrtr(trp + j * 512);
                else if constexpr (c < 140) A.r0<j>();
                else A.r1<j>(); };
#define PB(i) ((i) < 12 ? (i) / 6 : (i) < 32 ? (i) / 4 - 1 : (i) < 44 ? 7 + ((i) - 32) % 3 : (i) < 54 ? 7 + ((i) - 44) / 2 : 12 + ((i) - 54) % 3)
#define PT(i) ((i) < 12 ? ((i) + 5) % 6 : (i) < 32 ? (3 + (i) % 4) % 6 : (i) < 44 ? (3 + ((i) - 32) / 3) % 6 : (i) < 54 ? 1 + ((i) & 1) : 1 + ((i) - 54) / 3)
            sfor<82>([&](auto I) {
                constexpr int i = CV(I) + 1, b = PB(i), t = PT(i), bn = PB(i + 1);
                constexpr int rt[83] = {-1, -1, 2, -1, -1, 22, 42, 3, -1, 23, 43, 4, -1, 24, 44, 5, -1, 25, 45, 6,
                    -1, 26, 46, 7, -1, 8, 9, 27, 47, 28, 48, 29, 49, -1, -1, 60, -1, 61, 62, 63,
                    -1, 10, 64, -1, 11, 12, -1, 13, 14, -1, -1, 80, 82, 83, 84, 85, 86, 87, 67, 88,
                    89, 69, -1, 91, -1, 71, -1, 121, -1, 100, 102, 104, 106, 108, 110, -1, 140, -1, -1, 141, -1, -1, 122};
                constexpr int r2[83] = {-1, -1, -1, -1, -1, -1, -1, -1, -1, -1, -1, -1, -1, -1, -1, -1, -1, -1, -1, -1,
                    -1, -1, -1, -1, -1, -1, -1, -1, -1, -1, -1, -1, -1, -1, -1, -1, -1, -1, -1, -1,
                    -1, -1, -1, -1, -1, -1, -1, -1, -1, -1, -1, 81, -1, 65, -1, 66, -1, -1, -1, -1,
                    68, 90, -1, 70, -1, 120, -1, -1, -1, 101, 103, 105, 107, 109, 111, -1, -1, -1, -1, -1, -1, -1, -1};
                if constexpr (i < 60) {
                    if constexpr (t == 5 && b >= 2) smf<false>(ac[5], m[b], CAT8(wa[b], wb[b]), ix);
                    else smfw<QIDX(t, b < 10 ? b : b - 8) % 52>(ac[t], m[b], W[QIDX(t, b < 10 ? b : b - 8) % 52], ix);
                } else if constexpr (i < 72) { constexpr int j = i - 60; mfa<false, false>(sc[j & 1], kf[j], CAT4(q[j & ~1], q[j | 1])); }
                else if constexpr (i < 77) A.mm<0, false, (i - 72) / 3, (i - 72) % 3>(W);
                else { constexpr int ks = i - 77; mfa<false, ks == 0>(sc[2], CAT4(km[2 * ks], km[2 * ks + 1]), CAT4(q[2 * ks], q[2 * ks + 1])); }
                SCHED0();
                if constexpr (i == 1) { AQ(3, 3) asm volatile("s_barrier" ::: "memory"); AQ(1, 2) AQ(3, 4) }
                else if constexpr (i == 50) { BARL(3); AQ(1, 3) }
                else if constexpr (i == 68) { BARL(3); AQ(1, 4) QSTAMP(0, 1) }
                else if constexpr (i >= 11 && i < 59) { if constexpr (PT(i + 1) == 5) TOUCH(wb[bn % 10]); TOUCH(m[bn]); }
                else if constexpr (i >= 59 && i < 71) { asm volatile("" :: "v"(kf[(i - 59) % 12]), "v"(q[((i - 59) | 1) % 12])); }
                else if constexpr (i == 71) TOUCH(A.x0[0]);
                else if constexpr (i == 74) TOUCH(A.x0[1]);
                else if constexpr (i >= 76 && i < 82) TOUCH(km[(2 * (i - 76) + 1) % 12]);
                if constexpr (i == 8 || i == 30) asm volatile("" :: "{v[60:63]}"(ac[0]), "{v[28:31]}"(ac[1]), "{v[32:35]}"(ac[2]), "{v[64:67]}"(ac[3]), "{v[68:71]}"(ac[4]), "{v[72:75]}"(ac[5]));
                TPQ(i)
                if constexpr (i == 14) AC3(0, 0, 0)
                if constexpr (i == 16) AC3(1, 6, 1)
                if constexpr (i == 18) AC3(2, 7, 2)
                if constexpr (i == 20) AC3(-1, -1, 3)
                if constexpr (i == 22) AC3(-1, -1, 4)
                if constexpr (i == 24) AC3(-1, -1, 5)
                if constexpr (i == 66) AC3(3, -1, 6)
                if constexpr (i == 2) { now = rt_async(); HSTOP(1) }
                if constexpr (i == APOS) { PINS(now); an += (unsigned)now; PINS(an); }
                if constexpr (i == APOS + 1) an = spack(an, X.sg);
                SCHED0();
                rd(IC(rt[i])); rd(IC(r2[i]));
                if constexpr (i == APOS + 7) st32(k.rs, 0, pbc, fo[1]);
                if constexpr (i >= 40 && i < 50 && i % 3 == 1) put(IC(((i - 31) / 3) % 6));
                if constexpr (i == 62 || i == 64) put(IC(i / 2 - 30));
                SCHED0();
                if constexpr (i < 25 && i % 4 != 1) B.out<((i - 2) / 4) * 3 + (i - 2) % 4, 6145>(dq, dq);
                if constexpr (i == APOS + 3) { fo[1] = an; PIN(fo[1]); }
                if constexpr (i >= 37 && i < 49) { constexpr int tt = ((i - 28) / 3) % 6; fold_op<(i - 37) % 3>(ac[tt], fo[tt]); }
                if constexpr (i >= 59 && i < 62) fold_op<i - 59>(ac[1], fo[1]);
                if constexpr (i == 62) fold_op<0>(ac[2], fo[2]);
                if constexpr (i == 63) asm volatile("v_add_f32 %1, %1, %2\n\tv_cvt_pk_bf16_f32 %0, %3, %1" : "=v"(fo[2]), "+v"(ac[2][1]) : "v"(ac[2][3]), "v"(ac[2][0]));
                if constexpr (i == 33) S.mask<0>(sc);
                if constexpr (i >= 51 && i < 59) S.mask<i - 50>(sc);
                if constexpr (i >= 75) S.op<i - 75>(k, sc, a0, a1, hv);
                if constexpr (i == 12) { AQ(3, 5) }
                if constexpr (i == 43) { AQ(3, 6) }
                if constexpr (i == 59) { AQ(3, 7) }
                SCHED0();
            });
        }
        QSTAMP(0, 2)
        {
            unsigned d0;
            auto rest = [&](auto J) { constexpr int j = CV(J);
                if constexpr (j >= 0 && j < 12 && (j % 6 == 0 || j % 3 == 2)) X.op<3 + j / 6, (j % 6 + 1) / 3>(k); };
            sfor<43>([&](auto F) { constexpr int f = CV(F) + 5;
                constexpr int e = f < 6 ? f + 3 : f < 9 ? f - 3 : f;
                A.mm<0, false, e / 6, e % 6>(W);
                if constexpr (f % 6 == 2 || f == 5) TOUCH(A.x1[f / 6]);
                else if constexpr (f == 47) { TOUCH(B.x0[0]); AQ(1, 6) AQ(2, 1) }
                else if constexpr (f % 6 == 5) TOUCH(A.x0[f / 6 + 1]);
                if constexpr (f == 40 - 6 * SOFT2) { done = rt_async(); QSTAMP(0, 3) AQ(1, 5) }
                TPQ(f + 78)
                if constexpr (f == 21) set_m0(X.dst + 2 * 8192);
                if constexpr (f == 24) { X.sb = X.se + 2 * 16384; PINS(X.sb); }
                if constexpr (f == 42) AC3(4, -1, 7)
                SCHED0();
                if constexpr (f % 6 == 4 && f < 36) A.r0<f / 6 + 2>();
                if constexpr (f % 6 == 1 && f < 42) A.r1<f / 6 + 1>();
                if constexpr (f >= 39 && f < 43) B.rd<true, ((f - 39) / 2) * 6 + (f - 39) % 2>();
                if constexpr (f == 26 || f == 31 + 4 * QT) X.op<2, f / 15>(k);
                SCHED0();
                S.op<f + 3>(k, sc, a0, a1, hv);
                rest(IC(f - 38));
                if constexpr (f == 45) B.lwv = vadd(l16, L_W + w * 1024);
                SCHED0(); });
            B.go<30, true, 0, 48, 8>(W, [&](auto F) { constexpr int f = CV(F);
                TPQ(f + 126)
                rest(IC(f + 10));
                if constexpr (f == 9) AC3(5, -1, -1)
                if constexpr (f == 2) { a0 = (X.sg & 3) * PR_SZ; PINS(a0); }
                if constexpr (f == 4) { a1 = a0 + po1; a0 += po0; PINS(a0); PINS(a1); }
                if constexpr (f == 8) { S.sg = (X.sg & 4) ? 0xBF800000u : 0x3F800000u; PINS(S.sg); }
                if constexpr (f == 10) { S.hs = S.sg - hw2; PINS(S.hs); }
                if constexpr (f == 14 || f == 16) G.op<f / 16>(k, l16, X.sg);
                if constexpr (f == 22 || f == 28 || f == 32) X.next<(f - 22) / 5>(k, l16);
                if constexpr (f == 35 - QT) asm volatile("s_waitcnt vmcnt(%0)\n\ts_barrier" :: "n"(7 - 2 * QT) : "memory");
                if constexpr (f == 20) AC3(6, 1, -1)
                if constexpr (f == 26) A.home<0>();
                if constexpr (f >= 36) A.rd<false, f + 12>();
                if constexpr (f == 38 || f == 40) X.next<(f == 38 ? 5 : 6)>(k, l16);
                if constexpr (f == CPOS - 1 || f == CPOS) X.next<f - CPOS + 4>(k, l16);
                if constexpr (f == 47) { A.pre<false, 8>(); AQ(1, 7) AQ(2, 2) } });
            A.go<0, false, 48, 120, 20>(W, [&](auto F) { constexpr int f = CV(F), i = f - XPOS;
                TPQ(f + 126)
                if constexpr (f == 56) B.home<1>();
                if constexpr (i == 0 || i == 2 || i == 6) X.next<7 + i / 2>(k, l16);
                if constexpr (i >= 3 && i < 15 && ((i - 3) % 6 == 0 || (i - 3) % 3 == 2)) X.op<(i - 3) / 6, ((i - 3) % 6 + 1) / 3>(k);
                if constexpr (f == 58) asm volatile("s_waitcnt vmcnt(1)\n\ts_barrier" ::: "memory");
                if constexpr (f == 57) AC3(7, 2, -1)
                if constexpr (f == RPOS - 5) G.ask<0>(k, l16);
                if constexpr (f == RPOS) { __builtin_amdgcn_s_setprio(0); SCHED0(); G.ask<1>(k, l16); }
                if constexpr (f >= 108) B.rd<true, f - 60>();
                SCHED0();
                G.red<f - 110, (RPOS < XPOS + 5 ? 4 : 0)>();
                if constexpr (f == 116) asm volatile("v_bfe_u32 %0, %1, 8, 1\n\tv_lshl_or_b32 %0, %0, 3, %1" : "=&v"(d0) : "v"(l16));
                if constexpr (f == 118) asm volatile("v_xor_b32 %0, %1, %0" : "+v"(d0) : "s"(L_K + (w >> 1) * 3072 + (w & 1) * 8));
                if constexpr (f == 119) { B.pre<true, 8>(); AQ(2, 3) } });
            B.go<30, true, 48, 120, 20>(W, [&](auto F) { constexpr int f = CV(F), i = f - 48;
                constexpr int ko[26] = {0, 1, 2, 4, 3, 6, 5, 8, 7, 9, 10, 11, 12, 13, 14, 16, 15, 18, 17, 20, 19, 21, 22, 23, -1, -1};
                TPQ(f + 198)
                G.hold<f - GPOS + 5>(X.sg);
                if constexpr (i < 24) { SCHED0(); A.out4<ko[i % 26], 0>(d0); }
                G.at<f - GPOS>(k, l16);
                if constexpr (f == 114) { lgkm0(done); PINS(G.rt); }
                if constexpr (f == 115) { PINS(now); HTRACE(X.sg - 2) an = (unsigned)done - (unsigned)now; PINS(an); }
                if constexpr (f == 116) { an = min(an, 200u); PINS(an); }
                if constexpr (f == 117) { an += DQ; PINS(an); }
                SCHED0();
                if constexpr (i == 28) AC3(-1, 3, -1)
                if constexpr (i == 50) AC3(-1, 4, -1)
                if constexpr (i == 62) AC3(-1, 5, -1)
                if constexpr (i == 23) { AQ(2, 4) }
                if constexpr (i == 47) { AQ(2, 5) }
                if constexpr (i == 65) { AQ(2, 6) }
                if constexpr (f == 119) { AQ(2, 7) }
                if constexpr (f == 112) asm volatile("" :: "{v[12:15]}"(G.v0), "{v[16:19]}"(G.v1), "{v[20:23]}"(G.p0), "{v[24:27]}"(G.p1), "{v9}"(B.lwv) TRHINT);
                G.early<f - EPOS>(X.off);
                if constexpr (f >= 113 && f < 119) clr2(ac[(f + 6 - 113 + 5) % 6]); });
            static_assert(GPOS >= 70 && GPOS <= 98 && XPOS % 6 == 2 && RPOS > XPOS && (RPOS + 1 < XPOS + 5 || RPOS >= XPOS + 14) && XPOS > 58 && CPOS > 8 && CPOS < 35 - QT && SOFT2, "GPOS");
            QSTAMP(0, 4) QSTAMP(0, 5)
            HFLUSH(s)
        }
        AST(0)
        PROF_ONLY(G.wait<0>();)
        HSTOP(0) HSTAMP(s, 0) AQ(1, 1) AQ(3, 1)
        G.rest();
        SCHED0();
        smfw<50>(ac[5], __builtin_bit_cast(bf16x8, G.p0), W[50], ix);
        AQ(3, 2)
        SCHED0();
    } while (G.bad == 0);
    } while (X.sg != 65u);
    PROF_OUT
}

#define OIDX(t, b) (22 + (t) * 3 + (b))
DEVI void vo_role(const Params& p, Ctx& k, char* smem, int h) {
    PROF_DECL
    const int w = k.w;
    bf16x16 W[52];
    unsigned l16, lwv, ix, dq0, trp, vo;
    {
        const int l = k.l, g = l >> 4, n = l & 15, q = (l >> 2) & 3, pp = l & 3;
        l16 = l * 16; lwv = l16 + L_W + w * 1024; ix = SPIX(l);
        dq0 = L_QM + (24 * w + (n >> 2) * 2 + (n & 1)) * 32 + g * 8 + ((n >> 1) & 1) * 4;
        trp = L_QM + (16 * (g >> 1) + 4 * (g & 1) + 8 * (q >> 1) + (q & 1)) * 32 + pp * 8;
        vo = EXP(h * 512) + (g >> 1) * 256 + n * 16 + (g & 1) * 8;
        u32x8 r = {dq0, trp, vo, ix, l16, lwv, 0u, 0u};
        PIN(r);
        dq0 = r[0]; trp = r[1]; vo = r[2]; ix = r[3]; l16 = r[4]; lwv = r[5];
    }
    Gather G;
    G3 V;
    Xn X;
    V.l16 = l16; V.lwv = lwv; X.l16 = l16;
    f32x4 y[20];
    bf16x8 v[12];
    u32x4 o[10];
    unsigned yb;
    {
        const int l = k.l;
        bf16x8 wt[16];
        bf16x4 q[10][4];
        f32x4 z[9][2];
        u32x2 gv;
        unsigned ra[6];
        const u16* wo = p.Wo + (size_t)h * 192 * 640;
        constexpr int L_V = L_XN + 24576;
#define VEX(v) ((v) < 0 ? 0 : ((v) < 6) + ((v) == VGS) + 10 * ((v) == VGS + 2) + 6 * ((v) == VGS + 6 || (v) == 18))
#define VKE(v) ((v) < VGS + 4 ? 0 : CMIN(20, 7 * ((v) - VGS - 3)))
#define YKE(v) ((v) < 16 ? 0 : 3 * (v) - 45)
        auto put = [&](int t) { st128(k.rs, X.l16, yb + t * 65536, (u32x4){cvtpk(z[t][0][0], z[t][0][1]), cvtpk(z[t][0][2], z[t][0][3]), cvtpk(z[t][1][0], z[t][1][1]), cvtpk(z[t][1][2], z[t][1][3])}); };
        auto rdx = [&](auto K) { constexpr int ks = CV(K); if constexpr (ks < 20) { V.r0<ks % 20>(); V.r1<ks % 20>(); if constexpr (ks < 14) V.rw<ks>(); } };
        auto rdv = [&](auto J) { constexpr int j = CV(J) % 6; v[j] = lr128(l16 + L_V + j * 1024); v[6 + j] = lr128(l16 + L_V + (6 + j) * 1024); };
        cstream<20>([](auto V) { constexpr int v = CV(V); return IC(VEX(v - 1) + (v < 19 ? 6 : 0)); },
            [&](auto U) { constexpr int u = CV(U), k0 = VKE(u - 1), y0 = YKE(u - 1), n = u < 16 ? (VKE(u) - k0) * 6 : u < 18 ? 36 : 18, nn = n ? n : 12, nr = u < 10 ? 3 : u < 16 ? 6 : 4;
                if constexpr (u < 6) hello(k, 0);
                if constexpr (u == VGS) gv = go_ask(k);
                if constexpr (u == VGS + 2) { PIN(gv); go_wait(k, 0, gv); X.init(k); X.l16 = k.c ? OOB : l16; xn_all(k, X.l16, X.src, X.dst); }
                if constexpr (u == VGS + 4) { rdx(IC(0)); rdx(IC(1)); }
                if constexpr (u == VGS + 6) { sfor<3>([&](auto I) { X.next<CV(I)>(k, l16); }); X.se = X.src; yb = k.yx(0, w) + EXP(h * 1024); }
                if constexpr (u == 16) rdv(IC(0));
                XSTAMP(u)
                if constexpr (u == 0 || u == 10 || u == 16) {
#pragma unroll
                    for (int t = 0; t < nr; ++t) ra[t] = opq((u == 0 ? wp_adr<384, true>(w * 3 + t, l) : u == 10 ? wp_adr<768, false>(w * 6 + t, l) : wp_adr<512, false>(w * 4 + t, l)) + SLOT(0));
                }
                unsigned rb[6];
#pragma unroll
                for (int t = 0; t < nr; ++t) rb[t] = slot_adr<u>(ra[t]);
                SCHED0();
                sfor<nn>([&](auto I) { constexpr int i = CV(I), e = u == VGS + 6 ? 9 : 0, j = ((i - e) * 6) / (nn - e), d = u + 2, r0 = u == 18 ? 6 : 0;
                    if constexpr (u < 16 && n > 0) { V.mm<0, true, k0 + i / 6, i % 6>(W); if constexpr (i % 6 == 3) rdx(IC(k0 + i / 6 + 2)); }
                    if constexpr (u >= 16) { constexpr int m = n / 3, ks = (y0 + i / m) % 6, t = (u / 18) * 6 + (i % m) / 2, rt = i & 1;
                        if constexpr (!(ks == 5 && t == 7 && rt)) { mfw<OIDX(t, ks >> 1), ks & 1, true, ks == 0>(z[t][rt], W[OIDX(t, ks >> 1)], v[rt * 6 + ks]); SCHED0(); }
                        if constexpr (i % m == 1 && u < 19) rdv(IC(ks + 1));
                        if constexpr (u == 19 && i < 2) rdv(IC(4 + i));
                        if constexpr (u == 18 && i >= 3 && i < 15 && (i & 1)) put((i - 3) / 2);
                        if constexpr (u == 18 && i < 6) lw128(lwv + (10 + i) * 4096, __builtin_bit_cast(u32x4, wt[10 + i])); }
                    if constexpr (i < e) X.op<i / 3, i % 3>(k);
                    else if constexpr (u > 0 && u < 18 && (i == e || ((i - e - 1) * 6) / (nn - e) != j)) {
                        if constexpr (d < 10) dma1<SLOT(d) + (j / 3) * 12288 + (j % 3) * 1024>(p.Wv + (size_t)(2 * d + j / 3) * 32 * 6144 + h * 192, wsrc<384, true>(w * 192 + (j % 3) * 64 + l, 12288), w * 3072);
                        else if constexpr (d < 16) dma1<SLOT(d) + j * 1024>(wo + (d - 10) * 32 * 640, wsrc<768, false>(w * 384 + j * 64 + l, 1280), w * 6144);
                        else dma1<SLOT(d) + j * 1024>(wo + (d - 16) * 48 * 640 + 384, wsrc<512, false>(w * 384 + j * 64 + l, 1280), w * 6144);
                    }
                    if constexpr (i < 2 && u >= 3 && u < 8) lw128(lwv + (2 * u - 6 + i) * 4096, __builtin_bit_cast(u32x4, wt[(2 * u - 6 + i) & 15]));
                    sfor<2>([&](auto C) { constexpr int c = u == 19 ? 2 * i + CV(C) : i - r0 + CV(C) * 99;
                        if constexpr (c >= 0 && c < 12) {
                            constexpr int t = u < 10 ? c >> 2 : u < 16 ? c >> 1 : 6 + c % 4, r = u < 10 ? c & 3 : u < 16 ? (u - 10) * 2 + (c & 1) : (u - 16) * 3 + c / 4, id = u < 10 ? (t * 10 + u) % 52 : OIDX(t, r >> 2);
                            q[t][r & 3] = lrtr(u < 10 ? rb[t] + (r >> 1) * 12288 + (r & 1) * 1536 : u < 16 ? rb[t] + (c & 1) * 12288 : rb[(t - 6) & 3] + (c / 4) * 8192);
                            if constexpr (u < 10 && u >= 2 && t == 2) { if constexpr (r & 1) V.wl[2 * u + r / 2] = wt[(2 * u - 4 + r / 2) & 15] = CAT4(q[t][r - 1], q[t][r]); }
                            else if constexpr ((r & 3) == 1) W[id] = __builtin_shufflevector(q[t][0], q[t][1], 0, 1, 2, 3, 4, 5, 6, 7, -1, -1, -1, -1, -1, -1, -1, -1);
                            else if constexpr ((r & 3) == 3) W[id] = CAT8(LO8(W[id]), CAT4(q[t][2], q[t][3]));
                        } });
                    SCHED0(); });
                if constexpr (u == VGS + 7) sfor<18>([&](auto I) { V.out<CV(I), 0>(l16 + L_V + ((3 * w) >> 1) * 1024 + ((3 * w) & 1) * 8, l16 + L_V + ((3 * w + 1) >> 1) * 1024 + ((3 * w + 1) & 1) * 8); }); });
        static_assert(VGS >= 6 && VGS < 9, "VGS");
        RAWBAR()
        XSTAMP(20)
        sfor<2>([&](auto B) { X.op<CV(B) + 3, 0>(k); asm volatile("s_nop 0"); X.op<CV(B) + 3, 1>(k); X.op<CV(B) + 3, 2>(k); });
        asm volatile("" : "+v"(z[8][0]), "+v"(z[8][1]), "+v"(z[7][0]), "+v"(z[6][1]));
        put(6); put(8);
        lw32(l16 + L_MEM, ~0u);
        o[7] = (u32x4){cvtpk(z[7][0][0], z[7][0][1]), cvtpk(z[7][0][2], z[7][0][3]), 0u, 0u};
        y[17] = z[7][1];
        yb += 7 * 65536;
        X.off = 0; X.l16 = l16;
        if (k.c) X.init(k);
    }
    RSTAMP(2)
    const unsigned pdum = k.pg(1) + DUMMY_B, pbc = w == 0 ? k.p0 + CNT(C_BC, (h >> 3) * 16 + 8 + (h & 7)) : pdum + EXP(49152 + h * 512 + w * 128), pby = k.p0 + YLINE(w, h);
    unsigned long long now = 0, done = 0, tpa = 0, tpb = 0;
    G.mx = ~0u;
    G.init(k);
    G.base -= 32768;
    const unsigned ypar = (unsigned)(k.par >> (PG_YX + (w >> 1))), yo = ((unsigned)(PG_YX + (w >> 1)) << 21) + (w & 1) * 1048576 + EXP(h * 1024);
    const unsigned ym0 = k.ym(0) + w * 163840, ym2 = k.ym(2) + w * 163840;
    unsigned ym = k.c ? pdum + w * 163840 : ym0, ys, t, c = ~0u, an = 80;
#define YT(u) ((u) < 10 ? 2 * ((u) % 5) : ((u) - 10) | 1)
#define YR(u) ((u) < 10 ? (u) / 5 : (u) & 1)
    auto yout = [&](auto U, auto J) { constexpr int u = CV(U), j = CV(J), t = YT(u), rt = YR(u);
        if constexpr (j == 0) { o[t][2 * rt] = cvtpk(y[u][0], y[u][1]); o[t][2 * rt + 1] = cvtpk(y[u][2], y[u][3]); TOUCH(o[t][2 * rt]); TOUCH(o[t][2 * rt + 1]); }
        if constexpr (j == 1 && rt) __builtin_amdgcn_raw_buffer_store_b128(o[t], k.rs, (int)l16, (int)yb, AUX_SC1);
        if constexpr (j == 2 && rt) { yb += t == 8 ? -7 * 65536 : 131072; PINS(yb); TOUCH(o[t]); } };
    auto ymm = [&](auto I) {
        constexpr int i = CV(I), ks = i % 6, u = i / 6, id = OIDX(YT(u), ks >> 1);
        mfw<id, ks & 1, true, ks == 0>(y[u], W[id], v[YR(u) * 6 + ks]);
        SCHED0();
        if constexpr (u > 0 && ks >= 2) yout(IC((u + 19) % 20), IC(ks - 2)); };
    if (k.c) X.late<5>(k, l16);
    do {
        PROF_S(X.sg - 1)
        f32x4 ac[3];
        sfor<13>([&](auto I) { constexpr int i = CV(I) + 107;
            ymm(IC(i));
            TPV(i - 107)
            if constexpr (i == TB) { asm volatile("s_waitcnt vmcnt(%0)\n\ts_barrier" :: "n"(TB < 111 ? 7 : 8) : "memory"); AST(5) }
            if constexpr (i == TB + 1) st32(k.rs, 0, pby, G.mx);
            if constexpr (i == 119) V.pre<true, 0>();
            if constexpr (i == 113) G.ask<0>(k, l16);
            if constexpr (i == 119) G.ask<1>(k, l16);
            if constexpr (i == 114) V.r0<0>();
            if constexpr (i == 115) V.r1<0>();
            if constexpr (i == 117) V.r0<1>();
            if constexpr (i == 118) V.r1<1>();
            SCHED0(); });
        HSTAMP(s, 4)
        V.run<0, true, 0, 16, 17, true>(W, [&](auto I) { constexpr int i = CV(I);
            TPV(i + 13)
            if constexpr (i >= 2) yout(IC(19), IC(i - 2));
            if constexpr (i == 8) { t = (X.sg & 7) * 2; PINS(t); }
            if constexpr (i == 10) { yb = (ypar >> t) & 1; PINS(yb); }
            if constexpr (i == 14) { t = (t << 21) + yo; PINS(t); }
            if constexpr (i == 16) { yb = (yb << 13) + t; PINS(yb); }
            if constexpr (i == 20) { c = X.sg; PINS(c); }
            if constexpr (i == 22) { G.a = max(c, 1u) << 15; PINS(G.a); }
            if constexpr (i == 28) G.op<1>(k, l16, 0);
            if constexpr (i == 40) { an = min(an, 200u) + DV; PINS(an); }
            if constexpr (i == 32 || i == 34 || i == 38) X.next<(i - 31) / 3>(k, l16);
            if constexpr (i == 23 || i == 29) { AAC(4 + i / 29) }
            if constexpr (i == 83 || i == 89) { AAC(6 + i / 89) }
            if constexpr (i == YPOS) { AST(6) }
            if constexpr (i == YPOS + 1) { AST(7) }
            if constexpr (i == YPOS) DRAIN();
            if constexpr (i == YB) asm volatile("s_barrier" ::: "memory");
            if constexpr (i == YPOS - 2) G.mx = vshl1(c, 0);
            if constexpr (i == YPOS + 1) st32(k.rs, 0, pby, G.mx);
            SCHED0();
            G.red<i - YPOS - 2, -1>();
            G.hold<i - GPOSY * 6 - GVS + 5>(c);
            G.at<i - GPOSY * 6 - GVS>(k, l16);
            if constexpr (i == 90) PINS(G.rt);
            if constexpr (i == 95) V.pre<true, 16>();
            if constexpr (i >= 92 && i < 95) clr2(ac[(i - 92) % 3]); });
        static_assert(GPOSY * 6 + 2 > YPOS + 1 && GPOSY <= 13 && YB >= YPOS && YB < 60, "GPOSY");
        HSTAMP(s, 5)
        HFLUSH(s - 1)
        f32x4 acc[10];
        f32x2 z[20];
        auto clr = [&](auto I) { constexpr int i = CV(I); asm volatile("v_mov_b64 %0, 0" : "=v"(z[i])); if constexpr (i & 1) acc[i >> 1] = __builtin_shufflevector(z[i - 1], z[i], 0, 1, 2, 3); };
        bf16x4 al[3], ah[3];
        unsigned d0, d1;
        {
            bf16x8 m[10], wa[10], wb[10];
            auto rd = [&](auto R) { constexpr int r = CV(R), b = 2 + r / 3;
                if constexpr (b < 10) {
                    if constexpr (r % 3 == 0) m[b] = lr128(l16 + MADR(b));
                    if constexpr (r % 3 == 1) wa[b] = lr128(lwv + (b - 2) * 8192);
                    if constexpr (r % 3 == 2) wb[b] = lr128(lwv + (b - 2) * 8192 + 4096);
                } else if constexpr (b < 13) {
                    if constexpr (r % 3 == 0) V.r0<b + 7>();
                    if constexpr (r % 3 == 1) V.rw<b + 7>();
                    if constexpr (r % 3 == 2) V.r1<b + 7>();
                } };
            AST(0)
            G.wait<0>();
            HSTOP(0) HSTAMP(s, 0) AST(1)
            V.run<0, true, 16, 17, 0, true>(W, [&](auto I) { constexpr int i = CV(I);
                TPV(i + 109)
                G.fin<i>(0);
                if constexpr (i == 3) {
                    if (__builtin_expect(G.bad != 0, 0)) {
                        unsigned spins = 0;
_Pragma("unroll 1")
                        do { PC8(0, c) G.again(k, l16); } while (G.bad != 0 && !spin_fail(k, spins++));
                        HSTOP(0) HSTAMP(s, 0)
                    }
                    asm volatile("s_barrier" ::: "memory");
                    AST(2)
                }
                if constexpr (i == 4) { now = rt_async(); HSTOP(1) SCHED0(); }
                if constexpr (i >= 3 + VS) rd(IC(i - 3 - VS)); });
            m[0] = __builtin_bit_cast(bf16x8, G.p0); m[1] = __builtin_bit_cast(bf16x8, G.p1);
            sfor<30>([&](auto I) {
                constexpr int i = CV(I), b = i / 3, t = i % 3;
                if constexpr (t == 2 && b >= 2) smf<false>(ac[2], m[b], CAT8(wa[b], wb[b]), ix); else smfw<t * 10 + b>(ac[t], m[b], W[t * 10 + b], ix);
                SCHED0();
                TPV(i + 115)
                if constexpr (i >= 5 && i < 29 && t != 0) { if constexpr (t == 1) TOUCH(wb[b]); else TOUCH(m[(b + 1) % 10]); }
                if constexpr (i == 29) V.pre<true, 17>();
                if constexpr (i == 2) { PINS(now); an += (unsigned)now; PINS(an); }
                if constexpr (i == 3) an = spack(an, c);
                SCHED0();
                if constexpr (i < 27 + VS) rd(IC(3 - VS + i));
                if constexpr (i == 5) st32(k.rs, 0, pbc, d0);
                SCHED0();
                if constexpr (i == 4) { d0 = an; PIN(d0); }
                if constexpr (i == 28) d0 = vadd(l16, L_K + ((3 * w) >> 1) * 1024 + ((3 * w) & 1) * 8);
                if constexpr (i == 29) d1 = vadd(l16, L_K + ((3 * w + 1) >> 1) * 1024 + ((3 * w + 1) & 1) * 8);
                SCHED0();
            });
            unsigned fo[3];
            V.run<0, true, 17, 19, 0, true>(W, [&](auto I) { constexpr int i = CV(I);
                TPV(i + 145)
                if constexpr (i < 3) { rd(IC(30 + i)); SCHED0(); fold_op<0>(ac[i], fo[i]); }
                if constexpr (i == 3 || i == 4 || i == 6) { constexpr int t = i / 2 - 1; asm volatile("v_add_f32 %1, %1, %2\n\tv_cvt_pk_bf16_f32 %0, %3, %1" : "=v"(fo[t]), "+v"(ac[t][1]) : "v"(ac[t][3]), "v"(ac[t][0])); }
                if constexpr (i == 5 || i == 7 || i == 8) lw32(dq0 + ((i - 4) / 2) * 256, fo[(i - 4) / 2]);
                if constexpr (i == 9 || i == 10) clr2(acc[i - 9]);
                if constexpr (i == 11) BARL(0) });
        }
        HSTAMP(s, 1) AST(3)
        {
            unsigned fo[2];
            auto xq = [&](auto I) { constexpr int i = CV(I) - XP + XS0;
                if constexpr (i == XA) X.next<3>(k, l16);
                if constexpr (i == -1 || i == 0) X.next<14 + i>(k, l16); };
            static_assert(XP % 6 == 5 && XP <= 53 && XP >= 17 && XB > XP + 18 && XB > 63, "XP");
            auto fin2 = [&](auto P, auto J) { fold2<CV(J)>(acc[2 * CV(P)], acc[2 * CV(P) + 1], fo); };
            V.run<0, true, 19, 20, 0, true>(W, [&](auto I) { constexpr int i = CV(I);
                TPV(i + 157)
                if constexpr (i == 0) { al[0] = lrtr(trp); ah[0] = lrtr(trp + 64); }
                if constexpr (i == 1) al[1] = lrtr(trp + 1024);
                if constexpr (i == 2) ah[1] = lrtr(trp + 1088);
                if constexpr (i == 3) al[2] = lrtr(trp + 2048);
                if constexpr (i == 4) ah[2] = lrtr(trp + 2112);
                SCHED0();
                if constexpr (i >= 1) clr(IC(3 + i)); });
            sfor<30>([&](auto I) { constexpr int i = CV(I), b = (i % 6) >> 1, t = 2 * (i / 6) + (i & 1), id = OIDX(t, b);
                smfw<id>(acc[t], CAT4(al[b], ah[b]), W[id], ix);
                SCHED0();
                TPV(i + 163)
                if constexpr (i == 1 || i == 3) TOUCH(ah[(i + 1) / 2]);
                if constexpr (i == 29) TOUCH(v[0]);
                if constexpr (i == 21) BARL(0)
                SCHED0();
                if constexpr (i == 4 || i == 7) X.next<(i == 4 ? 5 : 11)>(k, l16);
                xq(IC(i - 30));
                if constexpr (i >= 22 && i < 28) v[i - 22] = lr128(l16 + L_K + (i - 22) * 1024);
                SCHED0();
                V.out<i - 1, 0>(d0, d1);
                SCHED0();
                if constexpr (i % 3 == 0 && i < 18) clr(IC(9 + i / 3));
                if constexpr (i == 16) clr(IC(15));
                if constexpr (i >= 18 && i < 21) clr(IC(i - 2));
                if constexpr (i == 22) clr(IC(19));
                if constexpr (i >= 23 && i < 29) fold_op<(i - 23) / 2>(acc[(i - 23) & 1], fo[(i - 23) & 1]);
                if constexpr (i == 29) { st64(k.rs, vo, ym, (u32x2){fo[0], fo[1]}); SCHED0(); fin2(IC(1), IC(0)); }
                SCHED0(); });
            HSTAMP(s, 2)
            sfor<107>([&](auto I) { constexpr int i = CV(I), q = i < 8 ? i + 4 : i + 3;
                ymm(I);
                TPV(i + 193)
                if constexpr (i < 5 || (i >= 29 && i < 35)) TOUCH(v[(i < 5 ? i + 1 : i - 23) % 12]);
                if constexpr (i < 13 && i != 8) {
                    if constexpr (q % 3 == 1) { ym += 32768; PINS(ym); }
                    if constexpr (q % 3 == 2 && i >= 5) __builtin_amdgcn_s_setprio(0);
                    SCHED0();
                    if constexpr (q % 3 == 0) { st64(k.rs, vo, ym, (u32x2){fo[0], fo[1]}); SCHED0(); }
                    if constexpr (q < 15) fin2(IC(q / 3), IC(q % 3));
                }
                if constexpr (i == 13) done = rt_async();
                if constexpr (i >= 15 && i < 23 && i != 18 && i != 20) v[6 + i - 15 - (i > 18) - (i > 20)] = lr128(l16 + L_K + (6 + i - 15 - (i > 18) - (i > 20)) * 1024);
                xq(IC(i));
                if constexpr (i == XP - 6 + (XP == 23)) X.next<15>(k, l16);
                if constexpr (i == XP - 1) { SCHED0(); X.next<7>(k, l16); }
                if constexpr (i == XP) X.next<12>(k, l16);
                if constexpr (i > XP && i <= XP + 18 && (i % 6 == 0 || i % 3 == 1)) X.op<(i - XP - 1) / 6, (i % 6 + 2) / 3>(k);
                { constexpr int xp[6] = {XB, XB + 1, XB + 6, XB + 11, XB + 12, XB + 18};
                  sfor<6>([&](auto Q) { if constexpr (xp[CV(Q)] == i) X.op<3 + CV(Q) / 3, CV(Q) % 3>(k); }); }
                if constexpr (i == 90) { ym = (c & 2) ? ym2 : ym0; PINS(ym); }
                if constexpr (i == 91) { ys = ym + EXP(327680); PINS(ys); }
                if constexpr (i == 96) { ym = (c & 1) ? ys : ym; PINS(ym); }
                if constexpr (i == 60) { HSTAMP(s, 3) }
                if constexpr (i == 42 || i == 43 || i == 48 || i == 49) { AAC((i - 42) / 3 + (i & 1)) }
                if constexpr (i == 102) lgkm0(done);
                if constexpr (i == 103) { PINS(now); HTRACE(c - 1) an = (unsigned)done - (unsigned)now; PINS(an); }
                if constexpr (i == 105) G.mx = vshl1(c, 1);
                SCHED0(); });
            AST(4)
        }
    } while (X.sg != 65u);
    HFLUSH(63)
    PROF_OUT
}

DEVI void row_role(const Params& p, const Ctx& k, char* smem, int rt) {
    PROF_DECL
    const int l = k.l, n = l & 15;
    const unsigned fo = k.w * 2048 + rt * 1024;
    RSTAMP(2)
    const __amdgpu_buffer_rsrc_t rs = __builtin_amdgcn_make_buffer_rsrc(k.c ? (void*)k.ws : (void*)p.x, 0, 0x7fffffff, 0x00020000);
    const unsigned vs = k.c ? l * 16 : n * 1280 + (l >> 4) * 16, cs = k.c ? 16384 : 256;
    unsigned ep = n * 32, dp = (l >> 4) == (n >> 2) ? n * 32 + k.w * 8 : 1024 + l * 8;
    const unsigned long long m1 = __ballot(n & 1), m2 = __ballot(n & 2);
    bf16x8 one8 = {0x3F80, 0x3F80, 0x3F80, 0x3F80, 0x3F80, 0x3F80, 0x3F80, 0x3F80};
    PIN(one8);
    unsigned fails = k.c ? 0u : 65536u;
    prog_init(k);
    for (int sg = k.c ? 0 : 8; sg < 64; ++sg) {
        unsigned src[5], dst = k.seg(PG_XNA, sg) + fo;
#pragma unroll
        for (int c = 0; c < 5; ++c) { src[c] = (k.c ? k.seg(PG_X2, sg) + fo : (sg * 32 + rt * 16) * 1280 + k.w * 64) + c * cs; PINS(src[c]); }
        PINS(dst);
        prog_wait<5>(k, k.p0 + CNT(C_X2, rt * 5), k.c ? sg : -1);
        STOP(sg) STAMP(sg, 0)
        u32x4 a[5];
        f32x4 sm, sq;
        f32x2 v[20];
        unsigned long long bad;
        do {
            asm volatile("" ::: "memory");
#pragma unroll
            for (int c = 0; c < 5; ++c) { a[c] = ld128(rs, vs, src[c]); SCHED0(); }
            unsigned mx = 0, lim = ~1u | (fails >> 16);
            ++fails; PINS(fails); PINS(lim);
            sfor<5>([&](auto C) { constexpr int c = CV(C);
                mfa<false, c == 0>(sq, __builtin_bit_cast(bf16x8, a[c]), __builtin_bit_cast(bf16x8, a[c])); mfa<false, c == 0>(sm, one8, __builtin_bit_cast(bf16x8, a[c]));
                mx = umax3(umax3(mx, a[c][0], a[c][1]), a[c][2], a[c][3]); PIN(mx);
                SCHED0(); });
            bad = __ballot(mx > lim);
        } while (__builtin_expect(bad != 0, 0));
        STAMP(sg, 1)
        auto unp = [&](int i0, int i1) {
#pragma unroll
            for (int i = i0; i < i1; ++i) { v[i] = (f32x2){blo(a[i >> 2][i & 3]), bhi(a[i >> 2][i & 3])}; PIN(v[i]); }
            SCHED0(); };
        unp(0, 3);
        asm volatile("" : "+v"(sq), "+v"(sm));
        lw64(dp, (u32x2){__float_as_uint(sm[0]), __float_as_uint(csel(csel(sq[0], sq[1], m1), csel(sq[2], sq[3], m1), m2))});
        SCHED0();
        unp(3, 10);
        RAWBAR()
        const f32x4 e0 = __builtin_bit_cast(f32x4, lr128(ep)), e1 = __builtin_bit_cast(f32x4, lr128(ep + 16));
        SCHED0();
        unp(10, 20);
        const f32x4 e = e0 + e1;
        float m = (e[0] + e[2]) * (1.f / 640.f);
        const float var = bfr(__builtin_fmaxf(__builtin_fmaf(-m, m, (e[1] + e[3]) * (1.f / 640.f)), 0.f));
        m = bfr(m);
        const float rden = __builtin_amdgcn_rcpf(bfr(__builtin_amdgcn_sqrtf(bfr(var + 1e-5f))));
#pragma unroll
        for (int c = 0; c < 5; ++c) {
            u32x4 o;
#pragma unroll
            for (int i = 0; i < 4; ++i) { const f32x2 x = v[c * 4 + i] - m; o[i] = cvtpk(bfr(x[0]) * rden, bfr(x[1]) * rden); }
            st128(k.rs, l * 16, dst + c * 16384, o);
        }
        STAMP(sg, 2)
        unsigned fv = sg - (k.c ? 0 : 8);
        PIN(fv);
        DRAIN();
        st32(k.rs, l == 0 ? 0u : OOB, k.p0 + CNT(C_XN, rt * 4 + k.w), fv);
        dp ^= 2048; ep ^= 2048;
        STAMP(sg, 3) STAMP(sg, 4) STAMP(sg, 5)
        PFLUSH(sg)
    }
    PROF_OUT
}

DEVI int red_col(int j) { const int w = j / 5, q = j % 5; return q < 3 ? 3 * w + q : 9 + 2 * w + q; }
struct Red {
    u32x4 Y[16];
    template <int I0, int I1> DEVI void yload(const Ctx& k, int j, int sg) {
        const unsigned a = k.yx(sg, j / 5) + (j % 5) * 131072 + k.w * 16384;
#pragma unroll
        for (int i = I0; i < I1; ++i) Y[i] = ld128(k.rs, k.l * 16 + (i & 3) * 1024, a + ((i >> 2) & 1) * 4096 + (i >> 3) * 65536);
    }
};
DEVI unsigned y_get(const Ctx& k) { return ld32(k.rs, (k.l & 7) * 128, k.p0 + YLINE((k.role >> 5) & 3, k.w * 8)); }
DEVI void y_wait(const Ctx& k, int s) {
_Pragma("unroll 1")
    for (unsigned spins = 0; !__all((int)y_get(k) >= s); ++spins) {
        asm volatile("" ::: "memory");
        __builtin_amdgcn_s_sleep(YS);
        if (spin_fail(k, spins)) break;
    }
    __builtin_amdgcn_fence(__ATOMIC_ACQUIRE, "wavefront");
}
DEVI void wait_until(unsigned t) { while ((int)((rclk() - t) << 16) < 0) { if (WUS) __builtin_amdgcn_s_sleep(WUS); } }
#define DPPMAX(x, c) x = max(x, __builtin_amdgcn_update_dpp(0, x, c, 0xf, 0xf, false))
#define TSLP 16
#define TWU 0
DEVI void wait_until2(unsigned t) { while ((int)((rclk() - t) << 16) < 0) { if (TWU) __builtin_amdgcn_s_sleep(TWU); } }
#define WAIT_BC(s, BSLP) { \
    unsigned bc; \
    _Pragma("unroll 1") for (;; ++spins) { \
        bc = ld32(k.rs, (l & 15) * 128, k.p0 + CNT(C_BC, w * 16)); \
        DRAIN(); \
        if (__all(((int)bc >> 16) > s)) break; \
        __builtin_amdgcn_s_sleep(BSLP); \
        if (spin_fail(k, spins)) break; \
    } \
    const unsigned now = rclk(); \
    int d = (int)((bc - now) << 16); \
    d = ((int)bc >> 16) > s + 1 || d > (256 << 16) ? -(64 << 16) : d; \
    DPPMAX(d, 0xB1); DPPMAX(d, 0x4E); DPPMAX(d, 0x141); \
    tq = now + (__builtin_amdgcn_readlane(d, 0) >> 16); tv = now + (__builtin_amdgcn_readlane(d, 8) >> 16); }
DEVI void mred_role(const Params& p, const Ctx& k, char* smem, int j) {
    PROF_DECL
    const int l = k.l, w = k.w, g = l >> 4, n = l & 15;
    const unsigned ix = SPIX(l);
    const unsigned vp = (g * 16 + ((n >> 1) & 1) * 8 + (n >> 2) * 2 + (n & 1)) * 16;
    const unsigned v16 = (g >> 1) * 1024 + (g & 1) * 256 + n * 16, yo = j * 32768 + EXP(w * 4096);
    const u32x4 ones = {0xFFFFFFFFu, 0xFFFFFFFFu, 0xFFFFFFFFu, 0xFFFFFFFFu};
    auto arm = [&](int s) {
#pragma unroll
        for (int i = 0; i < 4; ++i) st128(k.rs, l * 16 + i * 1024, k.ym(s) + yo, ones);
    };
    const unsigned pv = l == 0 && j % 5 == 0 ? (j / 5) * 128 + w * 32 : OOB;
    Red R;
    RSTAMP(2)
    arm(1);
#pragma unroll
    for (int i = 0; i < 8; ++i) st128(k.rs, (i || w > 1 || g > 1) ? g * 32768 + n * 16 : OOB, k.memf(i * 8 + (w >> 1) * 4) + EXP((2 * j + (w & 1)) * 256), ones);
#pragma unroll
    for (int i = 0; i < 4; ++i) { const unsigned q = min(i * 80 + j * 4 + w, 287); st128(k.rs, l * 16, k.p0 + PR_B + PR_SZ + EXP(q * 1024), ones); }
    sleep_until(k.t0 + RSL);
    y_wait(k, 2);
    R.yload<0, 16>(k, j, 0);
    for (int s = 0; s < 63; ++s) {
        STOP(s) HSTAMP(s, 0)
        const unsigned ap = k.p0 + PR_B + (s & 3) * PR_SZ + w * 49152, am = k.ym(s) + yo;
        const unsigned ph = (s & 4) ? 0x80008000u : 0u;
        unsigned spins = 0, tq, tv;
        unsigned fl;
        WAIT_BC(s, BSLP)
        st32(k.rs, pv, k.p0 + CNT(C_XB, 0), spack(((int)((tq - tv) << 16) < 0 ? tv : tq) + RDY, s + 2));
        STAMP(s, 5)
        char* rb = smem + (s & 1) * 4096;
_Pragma("unroll 1")
        for (spins = 0;; ++spins) {
            u32x4 ym[4], pm[5];
            f32x4 acc[2];
            if (spins == 0) wait_until(tv);
            asm volatile("" ::: "memory");
#pragma unroll
            for (int i = 0; i < 4; ++i) ym[i] = ld128(k.rs, v16 + (i >> 1) * 2048 + (i & 1) * 512, am);
            fl = ld32(k.rs, 0, (k.p0 ^ 8192u) + CNT(C_X1, j));
            asm volatile("" ::: "memory");
            if (spins == 0) { wait_until(tq); STAMP(s, 1) }
            asm volatile("" ::: "memory");
#pragma unroll
            for (int i = 0; i < 5; ++i) pm[i] = ld128(k.rs, i < 4 ? l * 16 + i * 1024 : vp, ap + (i >> 2) * 4096);
            acc[0] = (f32x4){0.f, 0.f, 0.f, 0.f}; acc[1] = acc[0];
            const u32x8 y0 = {ym[0][0], ym[1][0], ym[0][2], ym[1][2], ym[2][0], ym[3][0], ym[2][2], ym[3][2]};
            const u32x8 y1 = {ym[0][1], ym[1][1], ym[0][3], ym[1][3], ym[2][1], ym[3][1], ym[2][3], ym[3][3]};
            unsigned mx = 0;
#pragma unroll
            for (int i = 0; i < 4; ++i) mx = max(mx, umax4(ym[i]));
            unsigned tg = mx == 0xFFFFFFFFu ? 0x80000000u : 0u;
            asm volatile("s_nop 1" : "+v"(acc[0]), "+v"(acc[1]), "+v"(tg) : "v"(y0), "v"(y1), "v"(ix));
            SCHED0();
#pragma unroll
            for (int i = 0; i < 5; ++i) {
                const unsigned u0 = __builtin_amdgcn_bitop3_b32(pm[i][0], pm[i][1], ph, 0x7e), u1 = __builtin_amdgcn_bitop3_b32(pm[i][2], pm[i][3], ph, 0x7e);
                SCHED0();
                if (i < 4) smf<false>(acc[0], __builtin_bit_cast(bf16x8, pm[i]), __builtin_bit_cast(bf16x16, __builtin_shufflevector(R.Y[2 * i], R.Y[2 * i + 1], 0, 1, 2, 3, 4, 5, 6, 7)), ix);
                else smf<false>(acc[0], __builtin_bit_cast(bf16x8, pm[4]), __builtin_bit_cast(bf16x16, y0), ix);
                tg = (tg | u0) | u1;
                SCHED0();
                if (i < 4) smf<false>(acc[1], __builtin_bit_cast(bf16x8, pm[i]), __builtin_bit_cast(bf16x16, __builtin_shufflevector(R.Y[8 + 2 * i], R.Y[9 + 2 * i], 0, 1, 2, 3, 4, 5, 6, 7)), ix);
                else smf<false>(acc[1], __builtin_bit_cast(bf16x8, pm[4]), __builtin_bit_cast(bf16x16, y1), ix);
                SCHED0();
            }
            const unsigned long long bad = __ballot((int)tg < 0);
            asm volatile("s_nop 5" : "+v"(acc[0]), "+v"(acc[1]) : "s"(bad));
#pragma unroll
            for (int t = 0; t < 2; ++t) { float* rp = (float*)(rb + t * 2048 + w * 512 + g * 128 + n * 4); rp[0] = acc[t][0] + acc[t][2]; rp[16] = acc[t][1] + acc[t][3]; }
            if (bad == 0) break;
            PCOUNT(0)
            asm volatile("" ::: "memory");
            if (spin_fail(k, spins)) break;
        }
        STAMP(s, 2)
        RAWBAR()
        if (w < 2) {
            const char* rp = rb + w * 2048 + ((l >> 4) * 2 + ((l >> 2) & 1)) * 64 + ((l & 3) * 4 + ((l >> 3) & 1) * 2) * 4;
            const f32x2 a = *(const f32x2*)rp, b = *(const f32x2*)(rp + 512), c = *(const f32x2*)(rp + 1024), d = *(const f32x2*)(rp + 1536);
            const f32x2 r = (a + b) + (c + d);
            st32(k.rs, l * 4, k.memf(s + 1) + EXP((2 * red_col(j) + w) * 256), min(cvtpk(r[0], r[1]) ^ ph, 0xFFFFFFFEu));
        }
        STAMP(s, 3)
_Pragma("unroll 1")
        for (spins = 0; (int)__builtin_amdgcn_readfirstlane(fl) < s - 1; ++spins) {
            PCOUNT(1)
            __builtin_amdgcn_s_sleep(PSLEEP);
            if (spin_fail(k, spins)) break;
            fl = ld32(k.rs, 0, (k.p0 ^ 8192u) + CNT(C_X1, j));
        }
        __builtin_amdgcn_fence(__ATOMIC_ACQUIRE, "wavefront");
        if (Y1) wait_until(tv + Y1);
        y_wait(k, 2 * s + 3);
        R.yload<0, 8>(k, j, s + 1);
        if (ARMP == 0) arm(s + 2);
        if (Y2) wait_until(tv + Y2);
        y_wait(k, 2 * s + 4);
        STAMP(s, 4)
        if (ARMP == 2) { arm(s + 2); SCHED0(); }
        R.yload<8, 16>(k, j, s + 1);
        if (ARMP == 1) { SCHED0(); arm(s + 2); }
        PFLUSH(s)
    }
    PFLUSH(63)
    PROF_OUT
}
DEVI void tred_role(const Params& p, const Ctx& k, char* smem, int j) {
    PROF_DECL
    const int l = k.l, w = k.w, g = l >> 4, n = l & 15, T = 2 * red_col(j) + (w >> 1), rt = w & 1;
    const unsigned v16 = (g >> 1) * 1024 + (g & 1) * 256 + n * 16, yo = j * 32768 + EXP(w * 4096);
    const unsigned xo = EXP((T >> 1) * 2048) + (T & 1) * 512 + rt * 1024, xv = ((g >> 1) * 16 + n) * 16 + (g & 1) * 8;
    const u32x2 ones = {0xFFFFFFFFu, 0xFFFFFFFFu};
    const unsigned fl = (w == 0 && l == 0) ? 0u : OOB;
    unsigned fails = 0;
    Red R;
    RSTAMP(2)
    st64(k.rs, xv, k.segx(PG_X1, 0) + xo, ones); st64(k.rs, xv, k.segx(PG_X1, 1) + xo, ones);
    sleep_until(k.t0 + RSL);
    y_wait(k, 2);
    R.yload<0, 16>(k, j, 0);
    for (int s = 0; s < 64; ++s) {
        STOP(s) STAMP(s, 0)
        unsigned ap[4], am = k.ym(s) + yo, xa = k.segx(PG_X1, s) + xo, rb = (s & 1) * 16384 + l * 16, wb = rb + w * 1024, fv = s;
#pragma unroll
        for (int i = 0; i < 4; ++i) { ap[i] = k.p0 + PR_B + (s & 3) * PR_SZ + w * 49152 + (i < 2 ? 32768 : 16384) + (~i & 1) * 4096; PINS(ap[i]); }
        rb += w * 4096;
        PINS(am); PINS(xa); PIN(rb); PIN(wb); PIN(fv);
        const unsigned ph = (s & 4) ? 0x80008000u : 0u;
        unsigned spins = 0, tq, tv;
        u32x2 res;
        if (k.c) res = ld64(k.rs, xv, k.seg(PG_X2, s) + xo);
        else res = *(const u32x2*)(p.x + (size_t)(s * 32 + rt * 16 + n) * 640 + T * 16 + g * 4);
        f32x4 acc[2][2];
        WAIT_BC(s, TSLP)
        u32x4 yl[8];
#pragma unroll
        for (int i = 0; i < 8; ++i) { yl[i] = __builtin_shufflevector(R.Y[2 * i], R.Y[2 * i + 1], 0, 1, 4, 5); PIN(yl[i]); }
        f32x4 x = {blo(res[0] ^ ph), bhi(res[0] ^ ph), blo(res[1] ^ ph), bhi(res[1] ^ ph)};
        PIN(x);
        wait_until2(tv); wait_until2(tq + (s < 63 ? RDLY : 0));
        STAMP(s, 1)
        unsigned long long bad;
        do {
            asm volatile("" ::: "memory");
            u32x4 ym[4], pp[16];
#pragma unroll
            for (int i = 0; i < 4; ++i) { ym[i] = ld128(k.rs, v16 + (i >> 1) * 2048 + (i & 1) * 512, am); SCHED0(); }
#pragma unroll
            for (int i = 0; i < 16; ++i) { pp[i ^ 4] = ld128(k.rs, l * 16 + (i & 3) * 1024, ap[i >> 2]); SCHED0(); }
            unsigned mx = 0, tg, lim = 0x7FFFFFFFu | (fails << 15);
            ++fails; PINS(fails); PINS(lim);
#pragma unroll
            for (int i = 0; i < 4; ++i) mx = max(mx, umax4(ym[i]));
            tg = mx == 0xFFFFFFFFu ? 0x80000000u : 0u;
            u32x4 yf[4];
#pragma unroll
            for (int i = 0; i < 4; ++i) { const int t = i & 1, f = i >> 1; yf[i] = (u32x4){ym[2 * f][t], ym[2 * f][t + 2], ym[2 * f + 1][t], ym[2 * f + 1][t + 2]}; }
            asm volatile("s_nop 1" : "+v"(yf[0]), "+v"(yf[1]), "+v"(yf[2]), "+v"(yf[3]), "+v"(tg));
            SCHED0();
            sfor<32>([&](auto I) { constexpr int i = CV(I), t = i & 1, q = (i >> 1) ^ 4, r = q < 4 ? 0 : q < 8 ? (q >> 1) & 1 : 1;
                mfa<false, q == 4 || q == 6>(acc[t][r], __builtin_bit_cast(bf16x8, q < 4 ? yl[t * 4 + q] : q < 8 ? yf[(q & 1) * 2 + t] : R.Y[t * 8 + (q & 7)]), __builtin_bit_cast(bf16x8, pp[q]));
                SCHED0();
                tg |= __builtin_amdgcn_bitop3_b32(pp[q][2 * t], pp[q][2 * t + 1], ph, 0x7e); PIN(tg);
                if constexpr (i == 20 || i == 22) { asm volatile("" : "+v"(acc[(i - 20) / 2][0])); lw128(wb + (i - 20) * 4096, __builtin_bit_cast(u32x4, acc[(i - 20) / 2][0])); }
                if constexpr (i == TPOS) st32(k.rs, fl, (k.p0 ^ 8192u) + CNT(C_X1, j), fv);
                SCHED0(); });
            bad = __ballot(tg > lim);
        } while (__builtin_expect(bad != 0, 0));
        STAMP(s, 2)
        const unsigned yc = y_get(k);
        SCHED0();
        asm volatile("s_nop 2" : "+v"(acc[0][1]), "+v"(acc[1][1]));
        lw128(wb + 4096, __builtin_bit_cast(u32x4, acc[0][1])); lw128(wb + 12288, __builtin_bit_cast(u32x4, acc[1][1]));
        RAWBAR()
        {
            const f32x4 r0 = __builtin_bit_cast(f32x4, lr128(rb)), r1 = __builtin_bit_cast(f32x4, lr128(rb + 1024)), r2 = __builtin_bit_cast(f32x4, lr128(rb + 2048)), r3 = __builtin_bit_cast(f32x4, lr128(rb + 3072));
            SCHED0();
            if constexpr (TPOS == 32) st32(k.rs, fl, (k.p0 ^ 8192u) + CNT(C_X1, j), fv);
            SCHED0();
            const f32x4 r = (r0 + r1) + (r2 + r3);
            st64(k.rs, xv, xa, (u32x2){min(cvtpk(x[0] + bfr(r[0]), x[1] + bfr(r[1])) ^ ph, 0xFFFFFFFEu), min(cvtpk(x[2] + bfr(r[2]), x[3] + bfr(r[3])) ^ ph, 0xFFFFFFFEu)});
            SCHED0();
            if constexpr (TPOS == 33) st32(k.rs, fl, (k.p0 ^ 8192u) + CNT(C_X1, j), fv);
        }
        st64(k.rs, s < 62 ? xv : OOB, k.segx(PG_X1, (s + 2) & 63) + xo, ones);
        STAMP(s, 3)
        if (s < 63) {
            if (!__all((int)yc >= 2 * s + 4)) { PCOUNT(2) y_wait(k, 2 * s + 4); }
            __builtin_amdgcn_fence(__ATOMIC_ACQUIRE, "wavefront");
            R.yload<0, 16>(k, j, s + 1);
        }
        STAMP(s, 4)
        STAMP(s, 5)
        PFLUSH(s)
    }
    PROF_OUT
}

#undef TZ
#define TZ z0()
DEVI void up_role(const Params& p, const Ctx& k, char* smem, int id) {
    PROF_DECL
    const int l = k.l, w = k.w, g = l >> 4, n = l & 15;
    bf16x8 Wt[80];
    const int tile0 = id * 8 + w * 2;
    auto issue = [&](auto U) { constexpr int u = CV(U);
        sfor<8>([&](auto J) { constexpr int j = CV(J); dma1<(u % 3) * 32768 + j * 1024>(((w & 1) ? p.wu : p.wg) + (size_t)(2 * u + (w >> 1)) * 32 * 1536 + id * 128, wsrc<256, true>(j * 64 + l, 3072), w * 8192); }); };
    sfor<3>(issue);
    cstream<10>([](auto V) { return IC(CV(V) < 9 ? 8 : 0); },
        [&](auto U) { constexpr int u = CV(U);
            if constexpr (u > 0 && u < 8) issue(IC(u + 2));
#pragma unroll
            for (int i = 0; i < 8; ++i) Wt[(i & 3) * 20 + 2 * u + (i >> 2)] = wp_read<256, true>(smem + (u % 3) * 32768 + (i >> 1) * 8192, w * 2 + (i & 1), l); });
    RAWBAR()
    RSTAMP(2)
    bf16x8 one8 = {0x3F80, 0x3F80, 0x3F80, 0x3F80, 0x3F80, 0x3F80, 0x3F80, 0x3F80};
    asm volatile("s_nop 1" : "+v"(one8));
    f32x4 cs[4];
    sfor<80>([&](auto I) { constexpr int f = CV(I); mfa<(f < 64), f % 20 == 0>(cs[f / 20], Wt[f], one8); });
    asm volatile("s_nop 7" : "+v"(cs[0]), "+v"(cs[1]), "+v"(cs[2]), "+v"(cs[3]));
    const unsigned ho = EXP((tile0 >> 1) * 1024), l16 = l * 16;
    constexpr int L_ST = 81920;
    const u32x4 ones = {0xFFFFFFFFu, 0xFFFFFFFFu, 0xFFFFFFFFu, 0xFFFFFFFFu};
#pragma unroll
    for (int i = 0; i < 4; ++i) st128(k.rs, l16, k.hseg(i >> 1) + (i & 1) * EXP(49152) + ho, ones);
    unsigned fails = 0;
    lw32(L_ST + 1024, 0u);
    prog_init(k);
    const unsigned fl = (w == 0 && l == 0) ? 0u : OOB;
    const unsigned long long m1 = __ballot(n & 1), m2 = __ballot(n & 2);
    for (int s = 0; s < 64; ) {
        unsigned xs = k.seg(PG_X1, s) + w * 2048, xd = (s & 1) * 40960 + w * 2048;
        PINS(xs); PINS(xd);
        prog_wait<20>(k, k.p0 + CNT(C_X1, 0), s);
        STOP(s) STAMP(s, 0)
        xn_all(k, l16, xs, xd);
        const unsigned xb = (s & 1) * 40960 + l16, yb = xb + w * 2048, sp = L_ST + (s & 1) * 2048, rp = sp + n * 16;
        const unsigned dp = sp + (g == (n >> 2) ? n * 16 + w * 4 : 4096 + l * 4);
        f32x4 acc[2][4], st[4];
        bf16x8 x[20], x1[20], y0[5], y1[5];
        unsigned mx = 0;
        auto blk = [&](auto B, auto J) { constexpr int b = CV(B), j = CV(J);
            if constexpr (j == 0) asm volatile("s_waitcnt vmcnt(%0)\n\ts_barrier" :: "n"(8 - 2 * b) : "memory");
            if constexpr (j >= 1 && j < 5) x[4 * b + j - 1] = lr128(xb + (4 * b + j - 1) * 2048);
            if constexpr (j == 5) y0[b] = lr128(yb + b * 8192);
            if constexpr (j == 6) y1[b] = lr128(yb + b * 8192 + 1024); };
        sfor<7>([&](auto J) { blk(IC(0), J); });
        SCHED0();
        STAMP(s, 1)
        float mean[2], rden[2], e[4][4];
        f32x4 sa[2], sb[2];
        u32x4 bad, o[2];
        unsigned sv, fv, r, f0, f1, ha = k.hseg(s) + ho;
        PINS(ha);
        auto stat = [&](auto I) { constexpr int i = CV(I), rt = i / 8, j = i % 8;
            if constexpr (j == 0) asm volatile("v_add_f32 %0, %2, %3\n\tv_add_f32 %1, %4, %5" : "=&v"(mean[rt]), "=&v"(rden[rt]) : "v"(sa[rt][0]), "v"(sa[rt][1]), "v"(sb[rt][0]), "v"(sb[rt][1]));
            if constexpr (j == 1 || j == 2) asm volatile("v_add_f32 %0, %0, %2\n\tv_add_f32 %1, %1, %3" : "+v"(mean[rt]), "+v"(rden[rt]) : "v"(sa[rt][j + 1]), "v"(sb[rt][j + 1]));
            if constexpr (j == 3) asm volatile("v_mul_f32 %0, 0xbaccccCD, %0\n\tv_mul_f32 %1, 0x3acccccd, %1" : "+v"(mean[rt]), "+v"(rden[rt]));
            if constexpr (j == 4) asm volatile("v_fma_f32 %1, -%0, %0, %1\n\tv_cvt_pk_bf16_f32 %0, 0, %0\n\tv_max_f32 %1, 0, %1" : "+v"(mean[rt]), "+v"(rden[rt]));
            if constexpr (j == 5) asm volatile("v_cvt_pk_bf16_f32 %0, 0, %0\n\tv_add_f32 %0, 0x3727c5ac, %0\n\tv_cvt_pk_bf16_f32 %0, 0, %0" : "+v"(rden[rt]));
            if constexpr (j == 6) asm volatile("v_sqrt_f32 %0, %0" : "+v"(rden[rt]));
            if constexpr (j == 7) asm volatile("v_cvt_pk_bf16_f32 %0, 0, %0\n\tv_rcp_f32 %0, %0" : "+v"(rden[rt])); };
        auto ep = [&](auto RT, auto T, auto J) { constexpr int rt = CV(RT), t = CV(T), j = CV(J), c = (j & 1) * 2, u = j >= 20, q = u ? j - 20 : j;
            float (&a)[4] = e[t + 2 * u];
#define A2 "+v"(a[c]), "+v"(a[c + 1])
            if constexpr (j < 0 || j >= 30) { }
            else if constexpr (q < 2) asm volatile("v_fma_f32 %0, %2, %3, %5\n\tv_fma_f32 %1, %2, %4, %6" : "=&v"(a[c]), "=&v"(a[c + 1]) : "v"(mean[rt]), "v"(cs[t + 2 * u][c]), "v"(cs[t + 2 * u][c + 1]), "v"(acc[rt][t + 2 * u][c]), "v"(acc[rt][t + 2 * u][c + 1]));
            else if constexpr (q < 4) asm volatile("v_mul_f32 %0, %0, %2\n\tv_mul_f32 %1, %1, %2" : A2 : "v"(rden[rt]));
            else if constexpr (q < 6 || j == 18 || j == 19) asm volatile("v_cvt_pk_bf16_f32 %0, 0, %0\n\tv_cvt_pk_bf16_f32 %1, 0, %1" : A2);
            else if constexpr (j < 8) asm volatile("v_mul_f32 %0, 0xbfb8aa3b, %0\n\tv_mul_f32 %1, 0xbfb8aa3b, %1" : A2);
            else if constexpr (j < 12) asm volatile("v_exp_f32 %0, %0" : "+v"(a[j & 3]));
            else if constexpr (j < 14) asm volatile("v_add_f32 %0, 1.0, %0\n\tv_add_f32 %1, 1.0, %1" : A2);
            else if constexpr (j < 18) asm volatile("v_rcp_f32 %0, %0" : "+v"(a[(j - 14) & 3]));
            else if constexpr (q < 8) asm volatile("v_mul_f32 %0, %0, %2\n\tv_mul_f32 %1, %1, %3" : A2 : "v"(e[t][c]), "v"(e[t][c + 1]));
            else if constexpr (q == 8) asm volatile("v_cvt_pk_bf16_f32 %0, %0, %2\n\tv_cvt_pk_bf16_f32 %1, %1, %3" : "+v"(a[0]), "+v"(a[2]) : "v"(a[1]), "v"(a[3]));
            else { unsigned d0, d1; asm volatile("v_min_u32 %0, -2, %2\n\tv_min_u32 %1, -2, %3" : "=&v"(d0), "=&v"(d1) : "v"(a[0]), "v"(a[2])); o[rt][2 * t] = d0; o[rt][2 * t + 1] = d1; } };
        sfor<180>([&](auto I) {
            constexpr int a = CV(I), b = a / 8, j = (a % 8) ^ (b == 4 ? 4 : 0), ch = a / 20 - 1, ks = a < 40 ? 4 * b + (j & 3) : a % 20, rt = ch >= 4;
            constexpr int nt = a < 40 ? 0 : rt ? ch - 4 : (ch & 1) * 2 + (ch >> 1), f = nt * 20 + ks, i = a - 36;
            if constexpr (a >= 40 || j < 4) mfa<(f < 64), ks == 0>(acc[rt][nt], Wt[f], rt ? x1[ks] : x[ks]);
            else mfa<false, b == 0>(st[j & 3], (j & 1) ? (j & 2 ? y1[b] : y0[b]) : one8, j & 2 ? y1[b] : y0[b]);
            SCHED0();
            if constexpr (a >= 103 && a < 119) TOUCH(x1[(a - 99) % 20]);
            else if constexpr (a == 60) { r = __builtin_amdgcn_readfirstlane(bad[0]); PINS(r); }
            else if constexpr (a == 61) { r = (fails - r) >> 31; PINS(r); }
            else if constexpr (a == 62) { fails += r; PINS(fails); }
            else if constexpr (a == 63) { f0 = 2 * s - r; PINS(f0); }
            else if constexpr (a == 64) { f1 = f0 | 1; PINS(f1); }
            else if constexpr (a == 65) { r <<= 31; PINS(r); }
            else if constexpr (a >= 56 && (a < 92 || a >= 119)) __builtin_amdgcn_s_setprio(0);
            if constexpr (a < 32 && a % 8 == 7) asm volatile("" :: "v"(x[4 * b + 4]), "v"(x[4 * b + 5]), "v"(x[4 * b + 6]), "v"(x[4 * b + 7]), "v"(y0[b + 1]));
            if constexpr (a < 32 && a % 8 == 4) TOUCH(y1[b]);
            SCHED0();
            if constexpr (a == 15) lw32((rp ^ 2048) + 1024, z0());
            if constexpr (a < 32 && a % 8 < 7) blk(IC((b + 1) % 5), IC(a % 8));
            if constexpr (i == 0 || i == 4 || i == 5 || i == 8) x1[i / 2 - (i == 4 || i == 8)] = lr128(xb + 1024 + (i / 2 - (i == 4 || i == 8)) * 2048);
            if constexpr (a >= 92 && a < 108) x1[a - 88] = lr128(xb + 1024 + (a - 88) * 2048);
            SCHED0();
            if constexpr (a < 40 && j >= 4) { const u32x4 v = __builtin_bit_cast(u32x4, (j & 1) ? y1[b] : y0[b]); mx = umax3(mx, v[j & 2], v[(j & 2) + 1]); PIN(mx); }
            if constexpr (i == 1 || i == 3) {
                constexpr int rt2 = i == 3;
                asm volatile("" : "+v"(st[2 * rt2]), "+v"(st[2 * rt2 + 1]));
                const f32x4& q = st[2 * rt2 + 1];
                const float d = csel(csel(q[0], q[1], m1), csel(q[2], q[3], m1), m2);
                lw32(dp + rt2 * 512, __float_as_uint(st[2 * rt2][0])); lw32(dp + rt2 * 512 + 256, __float_as_uint(d));
            }
            if constexpr (i == 2) lw32(mx == 0xFFFFFFFFu ? sp + 1024 : dp + 4096, 4096u);
            if constexpr (i == 6) bar_lgkm<2>();
            if constexpr (i == 6) sa[0] = __builtin_bit_cast(f32x4, lr128(rp));
            if constexpr (i == 7) sb[0] = __builtin_bit_cast(f32x4, lr128(rp + 256));
            if constexpr (i == 9) sa[1] = __builtin_bit_cast(f32x4, lr128(rp + 512));
            if constexpr (i == 10) sb[1] = __builtin_bit_cast(f32x4, lr128(rp + 768));
            if constexpr (i == 11) bad = __builtin_bit_cast(u32x4, lr128(rp + 1024));
            if constexpr (i >= 12 && i < 20) stat(IC(i - 12));
            if constexpr (a == 40) { STAMP(s, 2) }
            if constexpr (a == UPOS0 || a == UPOS) st32(k.rs, fl, k.p0 + CNT(C_H, id), fv);
            if constexpr (a == 117) __builtin_amdgcn_raw_buffer_store_b128(o[0], k.rs, (int)sv, (int)ha, AUX_SC1);
            if constexpr (a == 118) TOUCH(o[0]);
            SCHED0();
            ep(IC(0), IC(0), IC(a - 56)); ep(IC(0), IC(1), IC(a - 86));
            if constexpr (a >= 116 && a < 124) stat(IC(a - 108));
            if constexpr (a == UPOS0 - 2 || a == UPOS - 2) { fv = a == UPOS - 2 ? f1 : f0; PIN(fv); }
            if constexpr (a == 80) { sv = l16 | r; PIN(sv); }
            if constexpr (a < 144) ep(IC(1), IC(0), IC(a - 124));
            else if constexpr (a < 164) ep(IC(1), IC(1), IC(a - 144));
            else ep(IC(1), IC(0), IC(a - 144));
            SCHED0();
        });
        STAMP(s, 3)
        asm volatile("s_nop 7" : "+v"(acc[1][3]));
        {
            const f32x4 u = (acc[1][3] + mean[1] * cs[3]) * rden[1];
            o[1][2] = packs(bfr(u[0]) * e[1][0], bfr(u[1]) * e[1][1]); o[1][3] = packs(bfr(u[2]) * e[1][2], bfr(u[3]) * e[1][3]);
        }
        st128(k.rs, sv, ha + EXP(49152), o[1]);
        if (r) { PCOUNT(0) RAWBAR() lw32(rp + 1024, 0u); RAWBAR() }
        st128(k.rs, s < 62 ? l16 : OOB, k.hseg((s + 2) & 63) + ho, ones); st128(k.rs, s < 62 ? l16 : OOB, k.hseg((s + 2) & 63) + EXP(49152) + ho, ones);
        STAMP(s, 4) STAMP(s, 5)
        PFLUSH(s)
        s = (f1 + 1) >> 1;
    }
    PROF_OUT
}

DEVI void down_role(const Params& p, const Ctx& k, char* smem, int id) {
    PROF_DECL
    const int l = k.l, w = k.w, g = l >> 4, n = l & 15, rt = id / 5, cg = id % 5;
    bf16x8 Wt[96];
    wstream<12, 8, 4>(smem + w * 32768,
        [&](auto U, unsigned sl) { constexpr int u = decltype(U)::value; sfor<8>([&](auto J) { constexpr int j = CV(J); dma1<(u % 4) * 8192 + j * 1024>(p.wd + (size_t)(w * 12 + u) * 32 * 640 + cg * 128, wsrc<256, false>(j * 64 + l, 1280), sl); }); },
        [&](auto U, char* sp) { constexpr int u = decltype(U)::value;
#pragma unroll
            for (int nt = 0; nt < 8; ++nt) Wt[u * 8 + nt] = wp_read<256, false>(sp, (nt + 2 * w) & 7, l); });
    RAWBAR()
    RSTAMP(2)
    const unsigned xo = EXP((cg * 4 + w) * 2048) + rt * 1024, xv = ((g >> 1) * 16 + n) * 16 + (g & 1) * 8;
    const u32x2 ones = {0xFFFFFFFFu, 0xFFFFFFFFu};
    const unsigned x2v = k.c ? xv : OOB;
    prog_init(k);
#pragma unroll
    for (int i = 0; i < 4; ++i) st64(k.rs, x2v, k.seg(PG_X2, i >> 1) + xo + (i & 1) * 512, ones);
    const __amdgpu_buffer_rsrc_t ro = __builtin_amdgcn_make_buffer_rsrc((void*)p.out, 0, 2048 * 1280, 0x00020000);
    const unsigned ov = k.c ? OOB : n * 1280 + g * 8, lim = k.c ? 0xFFFFFFFEu : 0xFFFFFFFFu, fl = (w == 0 && l == 0) ? 0u : OOB;
    unsigned fails = 0;
    for (int s = 0; s < 64; ++s) {
        unsigned la[3], ra = k.seg(PG_X1, s) + xo, oa = k.c ? k.seg(PG_X2, s) + xo : (s * 32 + rt * 16) * 1280 + (cg * 8 + w * 2) * 32, rb = (s & 1) * 32768 + l * 16, wb[3], fv = s;
#pragma unroll
        for (int q = 0; q < 3; ++q) { la[q] = k.hseg(s) + rt * EXP(49152) + EXP((w * 12 + q * 4) * 1024); wb[q] = rb + ((w + q + 1) & 3) * 6144 + q * 2048; PINS(la[q]); PIN(wb[q]); }
        rb += w * 6144;
        PINS(ra); PINS(oa); PIN(rb); PIN(fv);
        prog_wait<12>(k, k.p0 + CNT(C_H, 0), 2 * s + rt);
        STOP(s) STAMP(s, 0)
        f32x4 acc[8], r[6];
        u32x2 res[2];
        u32x4 hx[12];
        unsigned long long bad;
        do {
            asm volatile("" ::: "memory");
#pragma unroll
            for (int i = 0; i < 12; ++i) { hx[i] = ld128(k.rs, l * 16 + (i & 3) * 1024, la[i >> 2]); SCHED0(); }
#pragma unroll
            for (int t = 0; t < 2; ++t) res[t] = ld64(k.rs, xv + t * 512, ra);
            SCHED0();
            unsigned mx = 0;
            sfor<72>([&](auto I) { constexpr int i = CV(I), ks = i / 6, j = 2 + i % 6, f = ks * 8 + j;
                mfa<(f < 64), ks == 0>(acc[j], Wt[f], __builtin_bit_cast(bf16x8, hx[ks]));
                SCHED0();
                if constexpr (i % 6 < 2) { mx = umax3(mx, hx[ks][(i & 1) * 2], hx[ks][(i & 1) * 2 + 1]); PIN(mx); }
                if constexpr (i == 68) { bad = __ballot(mx == 0xFFFFFFFFu); PINS(bad); }
                if constexpr (i == 69) { bad = fails < 65536u ? bad : 0; PINS(bad); }
                if constexpr (i == 70) { ++fails; PINS(fails); }
                if constexpr (i == DPOS) st32(k.rs, fl, k.p0 + CNT(C_X2, rt * 5 + cg), fv);
                SCHED0(); });
        } while (__builtin_expect(bad != 0, 0));
        STAMP(s, 1)
        float x[8];
        sfor<24>([&](auto I) { constexpr int i = CV(I), ks = i / 2, j = i & 1, f = ks * 8 + j;
            mfa<(f < 64), ks == 0>(acc[j], Wt[f], __builtin_bit_cast(bf16x8, hx[ks]));
            SCHED0();
            if constexpr (i < 12 && !(i & 1)) { constexpr int jj = 2 + i / 2;
                asm volatile("" : "+v"(acc[jj])); lw128(wb[jj / 2 - 1] + (jj & 1) * 1024, __builtin_bit_cast(u32x4, acc[jj])); }
            if constexpr (i < 8 && (i & 1)) { x[i - 1] = blo(res[i >> 2][(i >> 1) & 1]); x[i] = bhi(res[i >> 2][(i >> 1) & 1]); PIN(x[i - 1]); PIN(x[i]); }
            if constexpr (i == 13) bar_lgkm<0>();
            if constexpr (i >= 13 && i < 19) r[i - 13] = __builtin_bit_cast(f32x4, lr128(rb + (i - 13) * 1024));
            if constexpr (i + 72 == DPOS) st32(k.rs, fl, k.p0 + CNT(C_X2, rt * 5 + cg), fv);
            SCHED0(); });
        r[0] += r[2] + r[4]; r[1] += r[3] + r[5];
        asm volatile("s_nop 3" : "+v"(acc[0]), "+v"(acc[1]), "+v"(r[0]), "+v"(r[1]));
#pragma unroll
        for (int t = 0; t < 2; ++t) {
            const f32x4 d = acc[t] + r[t];
            const u32x2 o = {min(cvtpk(x[4 * t] + bfr(d[0]), x[4 * t + 1] + bfr(d[1])), lim), min(cvtpk(x[4 * t + 2] + bfr(d[2]), x[4 * t + 3] + bfr(d[3])), lim)};
            st64(k.rs, x2v + t * 512, oa, o);
            __builtin_amdgcn_raw_buffer_store_b64(o, ro, (int)(ov + t * 32), (int)oa, 0);
        }
        STAMP(s, 2)
#pragma unroll
        for (int t = 0; t < 2; ++t) st64(k.rs, s < 62 ? x2v : OOB, k.seg(PG_X2, (s + 2) & 63) + xo + t * 512, ones);
        STAMP(s, 3) STAMP(s, 4) STAMP(s, 5)
        PFLUSH(s)
    }
    PROF_OUT
}

DEVI void warm(const Params& p, const Ctx& k, int xl, int hw) {
    set_m0(k.w * 256);
_Pragma("unroll 1")
    for (int it = hw; it < 32 * 15; it += k.c ? 40 : 64) {
        const int q = it < 120 ? it & 7 : 8 + (it - 120) % 24, t = it < 120 ? it >> 3 : (it - 120) / 24;
        const u16* b = q < 24 ? (q < 8 ? p.Wv : q < 16 ? p.Wk : p.Wq) + (xl * 8 + (q & 7)) * 192 : p.Wo + (xl * 8 + (q & 7)) * 122880;
        const unsigned ln = t * 64 + k.l, cl = min(((unsigned)(size_t)b >> 7) & 7u, 4u);
        if (k.c) sleep_until(k.t0 + WP + it * PP / 128);
        if (t < (q >= 24 ? PO : ((q < 8 ? PV : PQ) >> (cl * 4)) & 15))
            asm volatile("global_load_lds_dword %0, %1" :: "v"(q < 24 ? (639 - ln / 3) * 12288 + (ln % 3) * 128 : (191 - (ln >> 2)) * 1280 + 768 + (ln & 3) * 128), "s"(b) : "memory");
    }
    DRAIN();
_Pragma("unroll 1")
    for (int i = 3; i < 8; ++i) { sleep_until(k.t0 + i * 50); hello(k, 0); }
    DRAIN();
    RAWBAR()
}

DEVI int xcc_id() { return __builtin_amdgcn_s_getreg(20 | (0 << 6) | (3 << 11)) & 7; }

DEVI unsigned prepare(const Params& p, Ctx& k, char* smem, int xl, int i) {
    const int l = k.l, j = xl * 22 + i - 4;
    const u32x4 ones = {0xFFFFFFFFu, 0xFFFFFFFFu, 0xFFFFFFFFu, 0xFFFFFFFFu};
    unsigned vote = 0;
    if (i < 4) {
        const int row = ((i * 4 + xl) * 4 + k.w) * 4 + (l >> 4);
        const unsigned dst = k.seg(PG_XNA, i * 2 + (xl >> 1));
        u32x4 a[5];
#pragma unroll
        for (int c = 0; c < 5; ++c) a[c] = *(const u32x4*)(p.x + (size_t)row * 640 + (c * 16 + (l & 15)) * 8);
        ln_rows(a, l & 15, row & 31, [&](unsigned vo, int c, u32x4 o) { st128(k.rs, vo, dst + c * 16384, o); st128(k.rs, vo, dst + c * 16384 + 8192, o); });
    } else if (k.w == 3) {
        const unsigned cal = ((unsigned)(j % 44) << 21) + 7 * 16384 + (xl >> 1) * 4096 + (l & 7) * 16;
        int d = 0;
#pragma unroll 1
        for (int r = 0; r <= 2 * CALR; ++r) {
            unsigned long long c0, c1;
            st128(k.rs, cal, (r & 1) * 8192, ones);
            st128(k.rs, cal, (~r & 1) * 8192, ones);
            asm volatile("s_waitcnt vmcnt(1)\n\ts_memtime %0\n\ts_waitcnt vmcnt(0)\n\ts_memtime %1\n\ts_waitcnt lgkmcnt(0)" : "=&s"(c0), "=s"(c1) :: "memory");
            d = r ? min((int)(c1 - c0), 400) - d : 0;
        }
        vote = (unsigned)(d < 0) ^ (unsigned)(xcc_id() >> 2) ^ (unsigned)(i & 1);
    } else {
        if (k.w == 2 && j < 20) {
            const int e = j * 32 + (l >> 1), T = e >> 4, hf = (e >> 1) & 1, rr = ((e >> 2) & 3) * 2 + (e & 1);
            u32x4 v;
#pragma unroll
            for (int c = 0; c < 4; ++c) v[c] = min(*(const unsigned*)(p.mem0 + rr * 640 + T * 16 + 4 * c + 2 * hf), 0xFFFFFFFEu);
            st128(k.rs, (2u << 21) + EXP(e * 16) + (l & 1) * 8192, 0, v);
        }
        constexpr unsigned N0 = PR_B + PR_SZ, N1 = 40 * 16384, N2 = 32768;
        static_assert(N0 + N1 + N2 <= 4 * 88 * 3072 && NPAGES <= 44, "fill");
#pragma unroll
        for (int r = 0; r < 4; ++r) {
            const unsigned o = ((r * 88 + j) * 3 + k.w) * 1024;
            st128(k.rs, o < N0 + N1 + N2 ? l * 16 : OOB, o < N0 ? o : o < N0 + N1 ? ((unsigned)PG_YM << 21) + (o - N0) : (2u << 21) + 32768 + (o - N0 - N1), ones);
        }
    }
    if (k.w == 3) *(volatile LAS unsigned*)smem = vote;
    DRAIN();
    __builtin_amdgcn_s_waitcnt(0x0f70);
    XSTAMP(0)
    RAWBAR()
    return *(volatile LAS unsigned*)smem;
}
DEVI void master(const Params& p, const Ctx& k) {
    const int l = k.l;
    u32x2 v[4] = {{0u, 0u}, {0u, 0u}, {0u, 0u}, {0u, 0u}};
_Pragma("unroll 1")
    for (unsigned spins = 0; spins < SPIN_CAP; ++spins) {
#pragma unroll
        for (int q = 0; q < 4; ++q) v[q] |= cm_ld(k, (CM_IN + (q * 64 + l) * 128) | v[q][1]);
        if (__all((v[0][1] & v[1][1] & v[2][1] & v[3][1]) != 0)) break;
        asm volatile("" ::: "memory");
    }
    const unsigned long long a = __ballot(v[2][0] != 0), b = __ballot(v[3][0] != 0);
    const int n = __popcll(a) + __popcll(b);
    const unsigned long long g = n > 44 ? ~0ull : 0ull, u = (a & b) | ((a ^ b) & g);
    const unsigned long long t = (__popcll(a ^ b) > 2 || n > 80 || n < 8 ? g : ((u >> 4) & 0x3FFFFF) | ((u >> 36) << 22)) ^ 0xAAAAAAAAAAAAAAAAull;
#pragma unroll
    for (int q = 0; q < 4; ++q) cm_st(k, l * 128, CM_GO + q * 8192, (unsigned)t, (unsigned)(t >> 32) | 0x80000000u);
}

#define FIN(n) { __builtin_amdgcn_s_waitcnt(0x0f70); asm volatile("s_nop " #n); return; }
__global__ __launch_bounds__(256, 1) void mega_kernel(Params p) {
    extern __shared__ __attribute__((aligned(16))) char smem[];
    Ctx k;
    const u16 *wq = p.Wq, *wk = p.Wk, *wv = p.Wv;
    asm volatile("" : "+s"(wq), "+s"(wk), "+s"(wv) : "s"(p.cm));
    k.ws = p.ws; k.cm = p.cm;
    k.rs = __builtin_amdgcn_make_buffer_rsrc((void*)p.ws, 0, 0x7fffffff, 0x00020000);
    k.l = threadIdx.x & 63; k.w = __builtin_amdgcn_readfirstlane(threadIdx.x >> 6);
    RSTAMP(0)
    const int x = blockIdx.x & 7, i = blockIdx.x >> 3, xl = x & 3;
    k.role = x * 32 + i; k.c = x >> 2; k.par = 0; k.p0 = 0; k.t0 = rclk();
    line_st(k, CM_GO, 0, 0);
    auto start = [&]() {
        const u16* a = (i < 8 ? wk : wv) + (xl * 8 + (i & 7)) * 192, * b = (i < 8 ? wq : wv + 32 * 6144) + (xl * 8 + (i & 7)) * 192;
        unsigned o[3];
        sfor<18>([&](auto J) { constexpr int j = CV(J);
            if constexpr (j < 3) o[j] = wsrc<384, true>(k.w * 192 + j * 64 + k.l, 12288);
            dma1<SLOT(j / 6) + ((j / 3) & 1) * 12288 + (j % 3) * 1024>((((j / 3) & 1) ? b : a) + (j / 6) * (i < 8 ? 32 * 6144 : 64 * 6144), o[j % 3], k.w * 3072); }); };
    if (!k.c && i < 16) start();
    if (__builtin_expect(k.role == MASTER, 0)) {
#pragma unroll
        for (int q = 0; q < 16; ++q) cm_st(k, k.l * 8, CM_IN + k.w * 8192 + q * 512, 0, 0);
        cm_st(k, 0, CM_TMO, ~0u, ~0u);
        DRAIN();
        RAWBAR()
    }
    if (__builtin_expect(k.c && i < 26, 0)) { const unsigned v = prepare(p, k, smem, xl, i); RSTAMP(1) hello(k, v); go_wait(k, v); XSTAMP(1) RAWBAR() }
    else { RSTAMP(1) if (k.c) { DRAIN(); hello(k, 0); } }
    if (__builtin_expect(k.role == MASTER && k.w == 0, 0)) { master(p, k); __builtin_amdgcn_s_waitcnt(0x0f70); }
    if (k.c && i < 16) { sleep_until(k.t0 + WD1 + (i ^ 8) * STG); start(); }
    if (i < 8) { k.l = lane(); qk_role(p, k, smem, xl * 8 + i); FIN(0) }
    if (i < 16) { k.l = lane(); vo_role(p, k, smem, xl * 8 + i - 8); FIN(1) }
    k.l = lane();
    if (!k.c || i < 26) warm(p, k, xl, (i - 16) * 4 + k.w);
    else RAWBAR()
    go_wait(k, 0);
    if (i >= 26 && !k.c) {
        sleep_until(k.t0 + XPF);
#pragma unroll
        for (int r = 0; r < 3; ++r) asm volatile("global_load_lds_dword %0, %1" :: "v"(min(40 + r * 96 + ((i - 26) * 4 + xl) * 4 + k.w, 319) * 8192 + k.l * 128), "s"(p.x) : "memory");
        DRAIN();
        RAWBAR()
    }
    if (i >= 26) sleep_until(k.t0 + (k.c ? GST : GSA) + ((xl & 1) == UPODD ? 0 : i < 31 ? GSD : -GSA));
    if (i < 21) { mred_role(p, k, smem, xl * 5 + i - 16); FIN(2) }
    if (i < 26) { tred_role(p, k, smem, xl * 5 + i - 21); FIN(3) }
    if ((xl & 1) == UPODD) { up_role(p, k, smem, (xl >> 1) * 6 + i - 26); FIN(4) }
    if (i < 31) { down_role(p, k, smem, (xl >> 1) * 5 + i - 26); FIN(5) }
    row_role(p, k, smem, xl >> 1);
    FIN(6)
}

extern "C" void kernel_launch(void* const* d_in, const int* in_sizes, int n_in,
                              void* d_out, int out_size, void* d_ws, size_t ws_size,
                              hipStream_t stream) {
    Params p;
    p.x = (const u16*)d_in[0]; p.mem0 = (const u16*)d_in[1];
    p.Wq = (const u16*)d_in[2]; p.Wk = (const u16*)d_in[3]; p.Wv = (const u16*)d_in[4]; p.Wo = (const u16*)d_in[5];
    p.wg = (const u16*)d_in[8]; p.wu = (const u16*)d_in[9]; p.wd = (const u16*)d_in[10];
    p.out = (u16*)d_out; p.cm = (char*)d_ws;
    p.ws = (char*)(((uintptr_t)d_ws + CM_BYTES + ((1u << 21) - 1)) & ~(uintptr_t)((1u << 21) - 1));
    (void)hipFuncSetAttribute((const void*)mega_kernel, hipFuncAttributeMaxDynamicSharedMemorySize, LDS_BYTES);
    mega_kernel<<<256, 256, LDS_BYTES, stream>>>(p);
}
```

```cpp
#define SWAPG
#include <hip/hip_runtime.h>
#include <hip/hip_bf16.h>
#include <stdint.h>
#include <utility>

typedef unsigned short u16;
typedef __attribute__((ext_vector_type(16))) short bf16x16;
typedef __attribute__((ext_vector_type(8))) short bf16x8;
typedef __attribute__((ext_vector_type(4))) short bf16x4;
typedef __attribute__((ext_vector_type(8))) __bf16 hbf8;
typedef __attribute__((ext_vector_type(16))) __bf16 hbf16;
typedef __attribute__((ext_vector_type(4))) float f32x4;
typedef __attribute__((ext_vector_type(2))) float f32x2;
typedef __attribute__((ext_vector_type(8))) unsigned u32x8;
typedef __attribute__((ext_vector_type(4))) unsigned u32x4;
typedef __attribute__((ext_vector_type(2))) unsigned u32x2;
typedef __attribute__((ext_vector_type(2))) __bf16 bf16x2_t;
#define LAS __attribute__((address_space(3)))
#define GAS __attribute__((address_space(1)))
#define DEVI __device__ __forceinline__

#define PSLEEP 8
#define QT 1
#define GVS 0
#define VS 0
#define CPOS (34 - 2 * QT)
#define EPOS 110
#define XA -5
#define SOFT2 1
#define YS 2
#define Y1 0
#define Y2 0
#define YB 58
#define ARMP 2
#define WD1 1000
#define GST 550
#define NTA " nt"
#define NTB ""
#define PQ 0x077A0
#define PV 0x4AAE4
#define GSA 1600
#define QGS 8
#define VGS 8
#define MN 480
#define MD 250
#define MP 100
#define RSL 1000
#define GSD 250
#define PO 0
#define PDLY 0
#define WP 600
#define PP 100
#define STG 0
#define UPOS0 88
#define DPOS 88
#define XPF 1400
#define TPOS 33
#define XS0 40
#define XP 23
#define XB 66
#define TB 107
#define PACE 65
#define XPOS 68
#define YPOS 40
#define XPOSY 1
#define PWS 1
#define WUS 0
#define WMIX 1
#define PACE2 30
#define GDLY 80
#define QDLY 0
#define CALR 1
#define SPG 3
#define DQ 16
#define DV 12
#define BSLP 1
#define SDLY 0
#define UPOS 150
#define RDLY 45
#define NOYST 0
#define NOTOK 0
#define NOYLD 0
#define GPOSY 8
#define OLDMAP 1
#define UPODD 0
#define XLD 10
#define TROT 8
#define KOUT 1
#define RPOS 69
#define RDY 90
#define APOS 6
#define GPOS 74

#define EXP(o) ((((unsigned)(o) >> 13) << 14) | ((unsigned)(o) & 8191u))
enum { C_XN = 0, C_Y = 1, C_X1 = 2, C_XB = 3, C_H = 4, C_X2 = 5, C_BC = 6 };
#define CNT(type, s) ((unsigned)((type) * 16384 + (s) * 128))
#define YLINE(w, h) CNT(C_Y + ((w) >> 1) * 6, ((w) & 1) * 32 + (h))
constexpr unsigned PR_B = EXP(65536), PR_SZ = EXP(12 * 8192);
constexpr unsigned DUMMY_B = EXP(0);
constexpr int PG_MEMF = 2;
#define YXR 8
constexpr int PG_YX = 3;
constexpr int PG_XNA = PG_YX + 2 * YXR;
constexpr int PG_X1 = PG_XNA + 4, PG_X2 = PG_X1 + 4;
constexpr int PG_H = PG_X2 + 4;
constexpr int PG_YM = PG_H + 8;
constexpr int NPAGES = PG_YM + 2;
static_assert(NPAGES <= 64, "map");
constexpr unsigned CM_TAB = 0, CM_TICKET = 1024, CM_TMO = 4096, CM_IN = 8192, CM_GO = 40960, CM_BYTES = 73728;
constexpr int MASTER = 5 * 32 + 31;
constexpr unsigned SPIN_CAP = 1u << 20;
#define AUX_SC1 16
#define LDS_BYTES (160 * 1024)

struct Params {
    const u16* x; const u16* mem0; const u16* Wq; const u16* Wk; const u16* Wv; const u16* Wo;
    const u16* wg; const u16* wu; const u16* wd; u16* out; char* cm; char* ws;
};
struct Ctx {
    char* ws; char* cm; __amdgpu_buffer_rsrc_t rs; unsigned long long par;
    int c, l, w, role;
    unsigned p0, t0;
    DEVI unsigned pg(int page) const { return ((unsigned)page << 21) | (((unsigned)(par >> page) & 1u) << 13); }
    DEVI unsigned pg0(int page) const { return pg(page) ^ ((unsigned)c << 13); }
    DEVI unsigned seg(int page, int s) const { return pg(page + (s >> 4)) + (unsigned)(s & 15) * 131072u; }
    DEVI unsigned seg0(int page, int s) const { return seg(page, s) ^ ((unsigned)c << 13); }
    DEVI unsigned segx(int page, int s) const { return seg(page, s) ^ 8192u; }
    DEVI unsigned hseg(int s) const { return pg(PG_H + (s >> 3)) + (unsigned)(s & 7) * 262144u; }
    DEVI unsigned yx(int sg, int wq) const { return pg(PG_YX + (sg % YXR) * 2 + (wq >> 1)) + (unsigned)(wq & 1) * 1048576u; }
    DEVI unsigned ym(int s) const { return pg(PG_YM + ((s >> 1) & 1)) + (unsigned)(s & 1) * EXP(327680); }
    DEVI unsigned memf(int s) const { return pg(PG_MEMF) + (unsigned)s * 32768u; }
    DEVI char* cnt(int type, int s) const { return ws + (p0 + CNT(type, s)); }
    DEVI char* cnt0(int type, int s) const { return ws + ((p0 ^ ((unsigned)c << 13)) + CNT(type, s)); }
};

DEVI unsigned cvtpk(float a, float b) { return __builtin_bit_cast(unsigned, __builtin_convertvector((f32x2){a, b}, bf16x2_t)); }
DEVI unsigned packs(float a, float b) { return min(cvtpk(a, b), 0xFFFFFFFEu); }
DEVI float blo(unsigned x) { return __uint_as_float(x << 16); }
DEVI float bhi(unsigned x) { return __uint_as_float(x & 0xFFFF0000u); }
DEVI float bfr(float f) { return __uint_as_float(cvtpk(0.f, f)); }
DEVI u32x4 ld128(__amdgpu_buffer_rsrc_t rs, unsigned v, unsigned s) { return __builtin_amdgcn_raw_buffer_load_b128(rs, (int)v, (int)s, AUX_SC1); }
DEVI u32x2 ld64(__amdgpu_buffer_rsrc_t rs, unsigned v, unsigned s) { return __builtin_amdgcn_raw_buffer_load_b64(rs, (int)v, (int)s, AUX_SC1); }
DEVI unsigned ld32(__amdgpu_buffer_rsrc_t rs, unsigned v, unsigned s) { return __builtin_amdgcn_raw_buffer_load_b32(rs, (int)v, (int)s, AUX_SC1); }
DEVI void st128(__amdgpu_buffer_rsrc_t rs, unsigned v, unsigned s, u32x4 x) {
    __builtin_amdgcn_raw_buffer_store_b128(x, rs, (int)v, (int)s, AUX_SC1);
    asm volatile("s_nop 1" :: "v"(x) : "memory");
}
DEVI void st64(__amdgpu_buffer_rsrc_t rs, unsigned v, unsigned s, u32x2 x) { __builtin_amdgcn_raw_buffer_store_b64(x, rs, (int)v, (int)s, AUX_SC1); }
DEVI void st32(__amdgpu_buffer_rsrc_t rs, unsigned v, unsigned s, unsigned x) { __builtin_amdgcn_raw_buffer_store_b32(x, rs, (int)v, (int)s, AUX_SC1); }
#define RAWBAR() { __builtin_amdgcn_s_waitcnt(0xc07f); __builtin_amdgcn_s_barrier(); }
#define BARL(n) { asm volatile("" ::: "memory"); __builtin_amdgcn_s_waitcnt(0xc07f | ((n) << 8)); __builtin_amdgcn_s_barrier(); asm volatile("" ::: "memory"); }
#define DRAIN() asm volatile("s_waitcnt vmcnt(0)" ::: "memory")
#define SCHED0() __builtin_amdgcn_sched_barrier(0)
#define OOB 0x80000000u
DEVI int opq(int x) { asm volatile("" : "+v"(x)); return x; }
#define LANE const int l = opq(k.l), g = l >> 4, n = l & 15; (void)g; (void)n;
DEVI unsigned umax3(unsigned a, unsigned b, unsigned c) { return max(max(a, b), c); }
DEVI unsigned umax4(u32x4 v) { return max(umax3(v[0], v[1], v[2]), v[3]); }
DEVI unsigned rclk() { return (unsigned)__builtin_amdgcn_s_memrealtime(); }
DEVI unsigned long long rt_async() { unsigned long long t; asm volatile("s_memrealtime %0" : "=s"(t)); return t; }
DEVI void lgkm0(unsigned long long& a) { asm volatile("s_waitcnt lgkmcnt(0)" : "+s"(a) :: "memory"); }
DEVI unsigned spack(unsigned lo, unsigned hi) { unsigned d; asm volatile("s_pack_ll_b32_b16 %0, %1, %2" : "=s"(d) : "s"(lo), "s"(hi)); return d; }
DEVI int lane() { int l; asm volatile("v_mbcnt_lo_u32_b32 %0, -1, 0\n\tv_mbcnt_hi_u32_b32 %0, -1, %0" : "=v"(l)); __builtin_assume(l >= 0 && l < 64); return l; }
DEVI void sleep_until(unsigned target) { while ((int)(rclk() - target) < 0) __builtin_amdgcn_s_sleep(4); }
DEVI float xor16max(float x) { auto r = __builtin_amdgcn_permlane16_swap(__float_as_uint(x), __float_as_uint(x), false, false); return __builtin_fmaxf(__uint_as_float(r[0]), __uint_as_float(r[1])); }
DEVI float xor32max(float x) { auto r = __builtin_amdgcn_permlane32_swap(__float_as_uint(x), __float_as_uint(x), false, false); return __builtin_fmaxf(__uint_as_float(r[0]), __uint_as_float(r[1])); }
DEVI float xor16add(float x) { auto r = __builtin_amdgcn_permlane16_swap(__float_as_uint(x), __float_as_uint(x), false, false); return __uint_as_float(r[0]) + __uint_as_float(r[1]); }
DEVI float xor32add(float x) { auto r = __builtin_amdgcn_permlane32_swap(__float_as_uint(x), __float_as_uint(x), false, false); return __uint_as_float(r[0]) + __uint_as_float(r[1]); }
DEVI float fold32(float a, float b) { auto r = __builtin_amdgcn_permlane32_swap(__float_as_uint(a), __float_as_uint(b), false, false); return __uint_as_float(r[0]) + __uint_as_float(r[1]); }
#define ROR16(x, n) __builtin_bit_cast(float, __builtin_amdgcn_update_dpp(0, __builtin_bit_cast(int, (x)), 0x120 + (n), 0xf, 0xf, false))
DEVI float row16sum(float x) { x += ROR16(x, 8); x += ROR16(x, 4); x += ROR16(x, 2); x += ROR16(x, 1); return x; }
DEVI unsigned z0() { unsigned z; asm volatile("v_mov_b32 %0, 0" : "=v"(z)); return z; }
DEVI void cnt_add(char* p) { __hip_atomic_fetch_add((GAS unsigned*)(p + z0()), z0() + 1u, __ATOMIC_RELAXED, __HIP_MEMORY_SCOPE_AGENT); }
DEVI unsigned cnt_get(char* p) { return __hip_atomic_load((GAS unsigned*)(p + z0()), __ATOMIC_RELAXED, __HIP_MEMORY_SCOPE_AGENT) + 1u; }
DEVI bool spin_fail(const Ctx& k, unsigned spins) {
    if (spins > SPIN_CAP || ((spins & 255u) == 255u && __builtin_amdgcn_readfirstlane(cnt_get(k.cm + CM_TMO)) != 0)) { cnt_add(k.cm + CM_TMO); return true; }
    return false;
}
DEVI __amdgpu_buffer_rsrc_t cm_rs(const Ctx& k) { return __builtin_amdgcn_make_buffer_rsrc((void*)k.cm, 0, CM_BYTES, 0x00020000); }
DEVI void cm_st(const Ctx& k, unsigned v, unsigned off, unsigned lo, unsigned hi) { asm volatile("buffer_store_dwordx2 %0, %1, %2, %3 offen sc1" :: "v"((u32x2){lo, hi}), "v"(v), "s"(cm_rs(k)), "s"(off) : "memory"); }
DEVI u32x2 cm_ld(const Ctx& k, unsigned off) { return ld64(cm_rs(k), off, 0); }
DEVI void line_st(const Ctx& k, unsigned off, unsigned lo, unsigned hi) { cm_st(k, (lane() & 15) * 8 + (k.w ? OOB : 0u), off + k.role * 128, lo, hi); }
DEVI void hello(const Ctx& k, unsigned vote) { line_st(k, CM_IN, vote, OOB); }
DEVI u32x2 go_ask(const Ctx& k) { u32x2 d; asm volatile("buffer_load_dwordx2 %0, off, %1, %2 sc1" : "=v"(d) : "s"(cm_rs(k)), "s"(CM_GO + k.role * 128) : "memory"); return d; }
DEVI void go_wait(Ctx& k, unsigned vote, u32x2 v = {0u, 0u}) {
_Pragma("unroll 1")
    for (unsigned spins = 0; __builtin_amdgcn_readfirstlane(v[1]) >= 0 && spins < SPIN_CAP; ++spins) {
        if (spins) { hello(k, vote); __builtin_amdgcn_s_sleep(4); }
        asm volatile("buffer_load_dwordx2 %0, off, %1, %2 sc1\n\ts_waitcnt vmcnt(0)" : "=v"(v) : "s"(cm_rs(k)), "s"(CM_GO + k.role * 128) : "memory");
    }
    k.par = ((unsigned long long)(unsigned)__builtin_amdgcn_readfirstlane(v[1]) << 32 | (unsigned)__builtin_amdgcn_readfirstlane(v[0])) ^ (k.c ? ~0ull : 0ull);
    k.p0 = k.pg(0);
    __builtin_amdgcn_fence(__ATOMIC_ACQUIRE, "wavefront");
}
DEVI void cnt_wait(const Ctx& k, char* c, unsigned need) {
_Pragma("unroll 1")
    for (unsigned spins = 0; (unsigned)__builtin_amdgcn_readfirstlane(cnt_get(c)) < need; ++spins) {
        __builtin_amdgcn_s_sleep(PSLEEP);
        if (spin_fail(k, spins)) break;
    }
    __builtin_amdgcn_fence(__ATOMIC_ACQUIRE, "wavefront");
}
DEVI unsigned xn_get(const Ctx& k, unsigned l16, int s) { return ld32(k.rs, opq(l16) & 0x380u, k.p0 + CNT(C_XN, 0)); }
DEVI bool xn_has(const Ctx& k, unsigned v, int s) { return __all((int)v >= s - (k.c ? 0 : 8)); }
DEVI void xn_wait(const Ctx& k, unsigned l16, int s) {
_Pragma("unroll 1")
    for (unsigned spins = 0; !xn_has(k, xn_get(k, l16, s), s); ++spins) {
        asm volatile("" ::: "memory");
        __builtin_amdgcn_s_sleep(2);
        if (spin_fail(k, spins)) break;
    }
    __builtin_amdgcn_fence(__ATOMIC_ACQUIRE, "wavefront");
}
constexpr unsigned L_PW = 150 * 1024;
DEVI void prog_init(const Ctx& k) { *(volatile LAS unsigned*)(size_t)(L_PW + k.l * 4) = ~0u; RAWBAR() }
template <int N> DEVI void prog_wait(const Ctx& k, unsigned a, int s) {
    unsigned n, t;
    asm volatile("s_mov_b32 m0, %8\n\ts_mov_b32 %0, 0\n\ts_branch 2f\n"
        "1:\n\ts_cmp_lg_u32 %6, 0\n\ts_cbranch_scc1 3f\n\tbuffer_load_dword %2, %3, %4 offen sc1 lds\n"
        "3:\n\ts_sleep %9\n\ts_add_u32 %0, %0, 1\n\ts_and_b32 vcc_lo, %0, 0x7ff\n\ts_cbranch_scc1 2f\n\t"
        "v_mov_b32 %1, 0\n\ts_bitcmp1_b32 %0, 23\n\ts_cbranch_scc0 5f\n\tglobal_store_dword %1, %1, %10 sc1\n"
        "5:\n\tglobal_load_dword %1, %1, %10 sc1\n\ts_waitcnt vmcnt(0)\n\tv_cmp_ne_u32 vcc, -1, %1\n\ts_cbranch_vccnz 4f\n"
        "2:\n\tds_read_b32 %1, %7\n\ts_waitcnt lgkmcnt(0)\n\tv_cmp_gt_i32 vcc, %5, %1\n\ts_cbranch_vccnz 1b\n4:"
        : "=&s"(n), "=&v"(t) : "v"(min(k.l, N - 1) * 128), "s"(k.rs), "s"(a), "s"(s), "s"(k.w), "v"(L_PW + k.l * 4), "n"(L_PW), "n"(PWS), "s"(k.cm + CM_TMO) : "memory", "vcc", "scc");
}
template <int OFF> DEVI void dma1(const void* sbase, unsigned voff, unsigned lds) {
    asm volatile("s_add_i32 m0, %2, %3\n\ts_nop 0\n\tglobal_load_lds_dwordx4 %0, %1" NTA :: "v"(voff), "s"(sbase), "s"(lds), "n"(OFF) : "memory", "scc");
}
DEVI void set_m0(unsigned lds) { asm volatile("s_mov_b32 m0, %0" :: "s"(lds) : "memory"); }
template <int OFF> DEVI void dmab(__amdgpu_buffer_rsrc_t rs, unsigned voff, unsigned soff) {
    asm volatile("buffer_load_dwordx4 %0, %1, %2 offen offset:%3 sc1 lds" :: "v"(voff), "s"(rs), "s"(soff), "n"(OFF) : "memory");
}
#define PIN(x) asm volatile("" : "+v"(x))
#define PINS(x) asm volatile("" : "+s"(x))
#define LEAD()
#define TOUCH(x) asm volatile("" :: "v"(x))
DEVI float csel(float a, float b, unsigned long long m) { float d; asm volatile("v_cndmask_b32_e64 %0, %1, %2, %3" : "=v"(d) : "v"(a), "v"(b), "s"(m)); return d; }
#define LO8(x) __builtin_shufflevector(x, x, 0, 1, 2, 3, 4, 5, 6, 7)
#define HI8(x) __builtin_shufflevector(x, x, 8, 9, 10, 11, 12, 13, 14, 15)
#define CAT8(a, b) __builtin_shufflevector(a, b, 0, 1, 2, 3, 4, 5, 6, 7, 8, 9, 10, 11, 12, 13, 14, 15)
template <bool AG> DEVI void smf(f32x4& c, const bf16x8& a, const bf16x16& w, unsigned ix) {
    if constexpr (AG) asm volatile("v_smfmac_f32_16x16x64_bf16 %0, %1, %2, %3" : "+v"(c) : "v"(a), "a"(w), "v"(ix));
    else asm volatile("v_smfmac_f32_16x16x64_bf16 %0, %1, %2, %3" : "+v"(c) : "v"(a), "v"(w), "v"(ix));
}
#define WTAB(X) X(0, "{a[0:7]}", "{a[0:3]}", "{a[4:7]}") X(1, "{a[8:15]}", "{a[8:11]}", "{a[12:15]}") X(2, "{a[16:23]}", "{a[16:19]}", "{a[20:23]}") X(3, "{a[24:31]}", "{a[24:27]}", "{a[28:31]}") X(4, "{a[32:39]}", "{a[32:35]}", "{a[36:39]}") X(5, "{a[40:47]}", "{a[40:43]}", "{a[44:47]}") X(6, "{a[48:55]}", "{a[48:51]}", "{a[52:55]}") X(7, "{a[56:63]}", "{a[56:59]}", "{a[60:63]}") X(8, "{a[64:71]}", "{a[64:67]}", "{a[68:71]}") X(9, "{a[72:79]}", "{a[72:75]}", "{a[76:79]}") X(10, "{a[80:87]}", "{a[80:83]}", "{a[84:87]}") X(11, "{a[88:95]}", "{a[88:91]}", "{a[92:95]}") X(12, "{a[96:103]}", "{a[96:99]}", "{a[100:103]}") X(13, "{a[104:111]}", "{a[104:107]}", "{a[108:111]}") X(14, "{a[112:119]}", "{a[112:115]}", "{a[116:119]}") X(15, "{a[120:127]}", "{a[120:123]}", "{a[124:127]}") X(16, "{a[128:135]}", "{a[128:131]}", "{a[132:135]}") X(17, "{a[136:143]}", "{a[136:139]}", "{a[140:143]}") X(18, "{a[144:151]}", "{a[144:147]}", "{a[148:151]}") X(19, "{a[152:159]}", "{a[152:155]}", "{a[156:159]}") X(20, "{a[160:167]}", "{a[160:163]}", "{a[164:167]}") X(21, "{a[168:175]}", "{a[168:171]}", "{a[172:175]}") X(22, "{a[176:183]}", "{a[176:179]}", "{a[180:183]}") X(23, "{a[184:191]}", "{a[184:187]}", "{a[188:191]}") X(24, "{a[192:199]}", "{a[192:195]}", "{a[196:199]}") X(25, "{a[200:207]}", "{a[200:203]}", "{a[204:207]}") X(26, "{a[208:215]}", "{a[208:211]}", "{a[212:215]}") X(27, "{a[216:223]}", "{a[216:219]}", "{a[220:223]}") X(28, "{a[224:231]}", "{a[224:227]}", "{a[228:231]}") X(29, "{a[232:239]}", "{a[232:235]}", "{a[236:239]}") X(30, "{a[240:247]}", "{a[240:243]}", "{a[244:247]}") X(31, "{a[248:255]}", "{a[248:251]}", "{a[252:255]}") X(32, "{v[96:103]}", "{v[96:99]}", "{v[100:103]}") X(33, "{v[104:111]}", "{v[104:107]}", "{v[108:111]}") X(34, "{v[112:119]}", "{v[112:115]}", "{v[116:119]}") X(35, "{v[120:127]}", "{v[120:123]}", "{v[124:127]}") X(36, "{v[128:135]}", "{v[128:131]}", "{v[132:135]}") X(37, "{v[136:143]}", "{v[136:139]}", "{v[140:143]}") X(38, "{v[144:151]}", "{v[144:147]}", "{v[148:151]}") X(39, "{v[152:159]}", "{v[152:155]}", "{v[156:159]}") X(40, "{v[160:167]}", "{v[160:163]}", "{v[164:167]}") X(41, "{v[168:175]}", "{v[168:171]}", "{v[172:175]}") X(42, "{v[176:183]}", "{v[176:179]}", "{v[180:183]}") X(43, "{v[184:191]}", "{v[184:187]}", "{v[188:191]}") X(44, "{v[192:199]}", "{v[192:195]}", "{v[196:199]}") X(45, "{v[200:207]}", "{v[200:203]}", "{v[204:207]}") X(46, "{v[208:215]}", "{v[208:211]}", "{v[212:215]}") X(47, "{v[216:223]}", "{v[216:219]}", "{v[220:223]}") X(48, "{v[224:231]}", "{v[224:227]}", "{v[228:231]}") X(49, "{v[232:239]}", "{v[232:235]}", "{v[236:239]}") X(50, "{v[240:247]}", "{v[240:243]}", "{v[244:247]}") X(51, "{v[248:255]}", "{v[248:251]}", "{v[252:255]}")
template <int ID> DEVI void smfw(f32x4& c, const bf16x8& a, const bf16x16& w, unsigned ix) {
#define X(i, P, L, H) if constexpr (ID == i) asm volatile("v_smfmac_f32_16x16x64_bf16 %0, %1, %2, %3" : "+v"(c) : "v"(a), P(w), "v"(ix));
    WTAB(X)
#undef X
}
template <int ID, int HF, bool WB, bool FIRST> DEVI void mfw(f32x4& c, const bf16x16& w, const bf16x8& x) {
#define Y(R, W8) { if constexpr (FIRST && WB) asm volatile("v_mfma_f32_16x16x32_bf16 %0, %2, %1, 0" : "=&v"(c) : R(W8), "v"(x)); \
    else if constexpr (FIRST) asm volatile("v_mfma_f32_16x16x32_bf16 %0, %1, %2, 0" : "=&v"(c) : R(W8), "v"(x)); \
    else if constexpr (WB) asm volatile("v_mfma_f32_16x16x32_bf16 %0, %2, %1, %0" : "+v"(c) : R(W8), "v"(x)); \
    else asm volatile("v_mfma_f32_16x16x32_bf16 %0, %1, %2, %0" : "+v"(c) : R(W8), "v"(x)); }
#define X(i, P, L, H) if constexpr (ID == i) { if constexpr (HF) Y(H, HI8(w)) else Y(L, LO8(w)) }
    WTAB(X)
#undef X
#undef Y
}
template <bool AG, bool FIRST> DEVI void mfa(f32x4& c, const bf16x8& w, const bf16x8& x) {
    if constexpr (AG && FIRST) asm volatile("v_mfma_f32_16x16x32_bf16 %0, %1, %2, 0" : "=&v"(c) : "a"(w), "v"(x));
    else if constexpr (AG) asm volatile("v_mfma_f32_16x16x32_bf16 %0, %1, %2, %0" : "+v"(c) : "a"(w), "v"(x));
    else if constexpr (FIRST) asm volatile("v_mfma_f32_16x16x32_bf16 %0, %1, %2, 0" : "=&v"(c) : "v"(w), "v"(x));
    else asm volatile("v_mfma_f32_16x16x32_bf16 %0, %1, %2, %0" : "+v"(c) : "v"(w), "v"(x));
}
template <bool AG, bool FIRST> DEVI void mfb(f32x4& c, const bf16x8& x, const bf16x8& w) {
    if constexpr (AG && FIRST) asm volatile("v_mfma_f32_16x16x32_bf16 %0, %1, %2, 0" : "=&v"(c) : "v"(x), "a"(w));
    else if constexpr (AG) asm volatile("v_mfma_f32_16x16x32_bf16 %0, %1, %2, %0" : "+v"(c) : "v"(x), "a"(w));
    else if constexpr (FIRST) asm volatile("v_mfma_f32_16x16x32_bf16 %0, %1, %2, 0" : "=&v"(c) : "v"(x), "v"(w));
    else asm volatile("v_mfma_f32_16x16x32_bf16 %0, %1, %2, %0" : "+v"(c) : "v"(x), "v"(w));
}
DEVI f32x4 mf(const bf16x8& a, const bf16x8& b, const f32x4& c) { return __builtin_amdgcn_mfma_f32_16x16x32_bf16(__builtin_bit_cast(hbf8, a), __builtin_bit_cast(hbf8, b), c, 0, 0, 0); }
template <int N, class F> DEVI void sfor(F f) { [&]<int... I>(std::integer_sequence<int, I...>) { (f(std::integral_constant<int, I>{}), ...); }(std::make_integer_sequence<int, N>{}); }
DEVI bf16x8 lds128(const char* p) { return *(const bf16x8*)p; }
DEVI bf16x4 ldstr(const char* p) { return __builtin_amdgcn_ds_read_tr16_b64_v4i16((LAS bf16x4*)p); }
DEVI bf16x8 ldstr2(const char* p, int d) { bf16x4 a = ldstr(p), b = ldstr(p + d); return __builtin_shufflevector(a, b, 0, 1, 2, 3, 4, 5, 6, 7); }

#define STAMP(s, k)
#define QSTAMP(f, i)
#define STOP(s)
#define PFLUSH(s)
#define PROF_DECL
#define RSTAMP(i)
#define XSTAMP(i)
#define PCOUNT(i)
#define PROF_OUT

#define HSTAMP STAMP
#define HFLUSH PFLUSH
#define HTRACE(s)
#define TROUT
#define TRHINT
#define TPQ(n)
#define TPV(n)
#define AQ(m, i)
#define AC3(i, j, k) { }
#define AST(i)
#define AAC(i)
#define ACCM 0
#define HSTOP(m) if constexpr ((ACCM != 0) == m) { STOP(0) }
#define PC8(i, x)
#define PROF_ONLY(x)
#define PROF_S(x)
template <int RB, bool STD> DEVI int swz(int row) { return RB == 384 ? ((row >> 1) & 1) | ((row >> 2) & 2) : RB == 128 ? (row >> 1) & 3 : STD ? (row & 3) | ((row >> 1) & 4) : row & 7; }
template <int RB, bool STD> DEVI unsigned wsrc(unsigned id, int ld) { const unsigned row = id / (RB / 16), c = id % (RB / 16); return row * ld + ((c ^ (swz<RB, STD>(row) << 1)) << 4); }
template <int RB, bool STD> DEVI int wp_adr(int T, int l) { const int row = (STD ? 8 : 4) * (l >> 4) + ((l >> 2) & 3); return row * RB + ((T ^ swz<RB, STD>(row)) << 5) + (l & 3) * 8; }
template <int RB, bool STD> DEVI bf16x8 wp_read(const char* blk, int T, int l) { return ldstr2(blk + wp_adr<RB, STD>(T, l), (STD ? 4 : 16) * RB); }
template <int NU, class CNT, class STEP> DEVI void cstream(CNT cnt, STEP step) {
    sfor<NU>([&](auto V) { constexpr int v = decltype(V)::value;
        asm volatile("s_waitcnt vmcnt(%0) lgkmcnt(0)\n\ts_barrier" :: "n"(decltype(cnt(V))::value * (v ? 1 : 2)) : "memory");
        step(V);
    });
}
template <int NU, int NI, int D, class ISSUE, class READ> DEVI void wstream(char* ring, ISSUE issue, READ read) {
    const unsigned base = (unsigned)(size_t)(LAS char*)ring;
    sfor<D>([&](auto U) { constexpr int u = decltype(U)::value; if constexpr (u < NU) issue(U, base); });
    sfor<NU>([&](auto U) {
        constexpr int u = decltype(U)::value, left = (NU - u < D ? NU - u : D) - 1;
        asm volatile("s_waitcnt vmcnt(%0)" :: "n"(left * NI) : "memory");
        read(U, ring + (u % D) * NI * 1024);
        __builtin_amdgcn_s_waitcnt(0xc07f);
        asm volatile("" ::: "memory");
        if constexpr (u + D < NU) issue(std::integral_constant<int, u + D>{}, base);
    });
}

constexpr int L_XN = 0;
constexpr int L_K = 40960;
constexpr int L_MEM = L_K + 12288;
constexpr int L_QM = L_MEM + 10240;
constexpr int L_Q = L_QM + 12288;
constexpr int L_W = 94208;
#define SLOT(i) ((i) % 3 == 0 ? 45056 : (i) % 3 == 1 ? 69632 : L_W + 40960)
#define CMIN(a, b) ((a) < (b) ? (a) : (b))
#define IC(x) std::integral_constant<int, (x)>{}
#define CV(X) decltype(X)::value
DEVI bf16x8 lr128(unsigned a) { return *(const LAS bf16x8*)(size_t)a; }
DEVI bf16x4 lrtr(unsigned a) { return __builtin_amdgcn_ds_read_tr16_b64_v4i16((LAS bf16x4*)(size_t)a); }
#define CAT4(a, b) __builtin_shufflevector(a, b, 0, 1, 2, 3, 4, 5, 6, 7)
DEVI void lw128(unsigned a, u32x4 v) { *(LAS u32x4*)(size_t)a = v; }
DEVI void lw64(unsigned a, u32x2 v) { *(LAS u32x2*)(size_t)a = v; }
DEVI void lw32(unsigned a, unsigned v) { *(LAS unsigned*)(size_t)a = v; }
template <int N> DEVI void bar_lgkm() { asm volatile("s_waitcnt lgkmcnt(%0)\n\ts_barrier" :: "n"(N) : "memory"); }
DEVI void atom1(__amdgpu_buffer_rsrc_t rs, unsigned voff, unsigned soff) {
    unsigned one; asm volatile("v_mov_b32 %0, 1\n\tbuffer_atomic_add %0, %1, %2, %3 offen" : "=&v"(one) : "v"(voff), "s"(rs), "s"(soff) : "memory");
}
DEVI unsigned vadd(unsigned v, unsigned sc) { unsigned d; asm volatile("v_add_u32 %0, %1, %2" : "=v"(d) : "s"(sc), "v"(v)); return d; }
DEVI float vzero() { float z; asm volatile("v_mov_b32 %0, 0" : "=v"(z)); return z; }
template <int S> DEVI unsigned slot_adr(unsigned a) { if constexpr (S % 3 == 2) return vadd(a, SLOT(2) - SLOT(0)); else return a + (SLOT(S) - SLOT(0)); }
template <int B, int I> DEVI void xn_op(const Ctx& k, unsigned v, unsigned src, unsigned dst) {
    if constexpr (I == 0) set_m0(dst + B * 8192);
    else if constexpr (I == 1) dmab<0>(k.rs, v, src + B * 16384);
    else dmab<1024>(k.rs, v, src + B * 16384);
}
DEVI void xn_all(const Ctx& k, unsigned v, unsigned src, unsigned dst) {
    sfor<5>([&](auto B) { xn_op<CV(B), 0>(k, v, src, dst); asm volatile("s_nop 0"); xn_op<CV(B), 1>(k, v, src, dst); xn_op<CV(B), 2>(k, v, src, dst); });
}
struct Xn {
    unsigned src, dst, sg, need, t, sb, se, l16, c, off;
    unsigned long long flip, m;
    DEVI void init(const Ctx& k) {
        src = k.seg(PG_XNA, 0) + k.w * 2048; dst = L_XN + k.w * 2048; sg = 0; off = ~0u;
        const unsigned q = (unsigned)(k.par >> PG_XNA), d = q ^ (q >> 1);
        flip = ((unsigned long long)(d & 1) << 16) | ((unsigned long long)(d & 2) << 31) | ((unsigned long long)(d & 4) << 46);
    }
    template <int I> DEVI void next(const Ctx& k, unsigned l16) {
        if constexpr (I == 0) { sg += 1; src += 131072; PINS(sg); PINS(src); }
        if constexpr (I == 1) asm volatile("s_bitcmp1_b64 %1, %2\n\ts_cselect_b32 %0, 0x2000, 0" : "=s"(t) : "s"(flip), "s"(sg) : "scc");
        if constexpr (I == 2) { src ^= t; PINS(src); }
        if constexpr (I == 3) asm volatile("v_and_b32 %0, 0x380, %1" : "=v"(c) : "v"(l16));
        if constexpr (I == 4) c = ld32(k.rs, c, k.p0 + CNT(C_XN, 0));
        if constexpr (I == 5) { need = sg - (k.c ? 0u : 8u); PINS(need); }
        if constexpr (I == 6) { need = sg < 64u ? need : 0x7FFFFFFFu; PINS(need); }
        if constexpr (I == 7) asm volatile("v_cmp_gt_i32_e64 %0, %1, %2" : "=s"(m) : "s"(need), "v"(c) : "memory");
        if constexpr (I == 8) asm volatile("s_cmp_lg_u64 %2, 0\n\ts_cselect_b32 %0, -1, 0\n\ts_cselect_b32 %1, 0x200000, %3" : "=&s"(off), "=s"(se) : "s"(m), "s"(src) : "scc");
        if constexpr (I == 13) set_m0(L_MEM);
        if constexpr (I == 14) dmab<0>(k.rs, c, k.p0 + CNT(C_XN, 0));
        if constexpr (I == 15) c = *(volatile LAS unsigned*)(size_t)(l16 + L_MEM);
        if constexpr (I == 11) { need = sg < 64u ? need : 0x80000000u; PINS(need); }
        if constexpr (I == 12) {
            if (__builtin_expect(m != 0, 0)) {
_Pragma("unroll 1")
                for (unsigned spins = 0; __any((int)*(volatile LAS unsigned*)(size_t)(l16 + L_MEM) < (int)need) && !spin_fail(k, spins >> 2); ++spins) { next<3>(k, l16); __builtin_amdgcn_s_sleep(1); next<14>(k, l16); }
            }
            se = src; }
        if constexpr (I == 10) asm volatile("s_cmp_lg_u32 %1, 64\n\ts_cselect_b32 %0, %0, 0" : "+s"(off) : "s"(sg) : "scc");
    }
    template <int B, int I> DEVI void op(const Ctx& k) {
        if constexpr (I == 0) { set_m0(dst + B * 8192); sb = se + B * 16384; PINS(sb); }
        else dmab<(I - 1) * 1024>(k.rs, l16, sb);
    }
    template <int NB> DEVI void late(const Ctx& k, unsigned l16) {
        xn_wait(k, l16, sg);
        se = src;
        sfor<NB>([&](auto B) { op<CV(B), 0>(k); asm volatile("s_nop 0"); op<CV(B), 1>(k); op<CV(B), 2>(k); });
        DRAIN();
    }
};
template <int OFF> DEVI u32x4 ald128(__amdgpu_buffer_rsrc_t rs, unsigned v, unsigned s) { u32x4 d; asm volatile("buffer_load_dwordx4 %0, %1, %2, %3 offen offset:%4 sc1" : "=v"(d) : "v"(v), "s"(rs), "s"(s), "n"(OFF) : "memory"); return d; }
#define MADR(b) (L_MEM + ((b) < 6 ? ((b) - 1) * 2048 : ((b) - 6) * 2048 + 3072))
struct Gather {
    u32x4 p0, p1, v0, v1;
    unsigned mx, a, aw, b, base, wo, dst;
    unsigned long long bad;
    DEVI void init(const Ctx& k) { base = k.memf(0); wo = k.w * 1024; dst = L_MEM + k.w * 2048; }
    template <int I> DEVI void op(const Ctx& k, unsigned l16, int s) {
        if constexpr (I == 0) { a = ((unsigned)s << 15) & 0x1F8000u; PINS(a); }
        if constexpr (I == 1) { a += base; aw = a + wo; b = aw + 3072 + (k.w >> 1) * 8192; PINS(a); PINS(aw); PINS(b); }
        if constexpr (I == 2) set_m0(dst);
        if constexpr (I == 3) v0 = ald128<2048>(k.rs, l16, aw);
        if constexpr (I == 4) v1 = ald128<3072>(k.rs, l16, b);
        if constexpr (I == 5) dmab<2048>(k.rs, l16, aw);
        if constexpr (I == 6) dmab<3072>(k.rs, l16, b);
        if constexpr (I == 7) p0 = ald128<0>(k.rs, l16, a);
        if constexpr (I == 8) p1 = ald128<1024>(k.rs, l16, a);
    }
    template <int N> DEVI void wait() { asm volatile("s_waitcnt vmcnt(%4)" : "+v"(p0), "+v"(p1), "+v"(v0), "+v"(v1) : "n"(N) : "memory"); }
    template <int I> DEVI void fin(unsigned other) {
#define MX3 "v_max3_u32 %0, %0, %3, %4\n\tv_max3_u32 %0, %0, %5, %6"
        if constexpr (I == 0) asm volatile("v_max3_u32 %0, %7, %1, %2\n\t" MX3 : "=&v"(mx) : "v"(v0[0]), "v"(v0[1]), "v"(v0[2]), "v"(v0[3]), "v"(v1[0]), "v"(v1[1]), "s"(other));
        if constexpr (I == 1) asm volatile("v_max3_u32 %0, %0, %1, %2\n\t" MX3 : "+v"(mx) : "v"(v1[2]), "v"(v1[3]), "v"(p0[0]), "v"(p0[1]), "v"(p0[2]), "v"(p0[3]));
        if constexpr (I == 2) asm volatile("v_max3_u32 %1, %1, %2, %3\n\tv_max3_u32 %1, %1, %4, %5\n\tv_cmp_eq_u32_e64 %0, -1, %1" : "=s"(bad) : "v"(mx), "v"(p1[0]), "v"(p1[1]), "v"(p1[2]), "v"(p1[3]));
    }
    DEVI void also(unsigned other) { asm volatile("v_mov_b32 %0, %1" : "=v"(mx) : "s"(other)); }
    DEVI void all() {
        asm volatile("s_waitcnt vmcnt(0)\n\tv_max3_u32 %1, %1, %2, %3\n\tv_max3_u32 %1, %1, %4, %5\n\tv_max3_u32 %1, %1, %6, %7\n\tv_max3_u32 %1, %1, %8, %9\n\tv_max3_u32 %1, %1, %10, %11\n\tv_max3_u32 %1, %1, %12, %13\n\t"
            "v_max3_u32 %1, %1, %14, %15\n\tv_max3_u32 %1, %1, %16, %17\n\tv_cmp_eq_u32_e64 %0, -1, %1" : "=s"(bad) : "v"(mx), "v"(v0[0]), "v"(v0[1]), "v"(v0[2]), "v"(v0[3]), "v"(v1[0]), "v"(v1[1]), "v"(v1[2]), "v"(v1[3]),
            "v"(p0[0]), "v"(p0[1]), "v"(p0[2]), "v"(p0[3]), "v"(p1[0]), "v"(p1[1]), "v"(p1[2]), "v"(p1[3]) : "memory");
    }
    DEVI void check() { also(0); all(); asm volatile("s_nop 3"); }
    template <int I> DEVI void early(unsigned other) {
        if constexpr (I == 0) asm volatile("s_waitcnt vmcnt(5)\n\tv_max3_u32 %0, %5, %1, %2\n\tv_max3_u32 %0, %0, %3, %4" : "=&v"(mx) : "v"(v0[0]), "v"(v0[1]), "v"(v0[2]), "v"(v0[3]), "s"(other) : "memory");
        if constexpr (I == 1) __builtin_amdgcn_s_setprio(0);
        if constexpr (I == 2) asm volatile("s_waitcnt vmcnt(4)\n\tv_max3_u32 %0, %0, %1, %2\n\tv_max3_u32 %0, %0, %3, %4" : "+v"(mx) : "v"(v1[0]), "v"(v1[1]), "v"(v1[2]), "v"(v1[3]) : "memory");
    }
    DEVI void rest() {
        asm volatile("s_waitcnt vmcnt(1)\n\tv_max3_u32 %1, %1, %2, %3\n\tv_max3_u32 %1, %1, %4, %5\n\ts_waitcnt vmcnt(0)\n\tv_max3_u32 %1, %1, %6, %7\n\tv_max3_u32 %1, %1, %8, %9\n\tv_cmp_eq_u32_e64 %0, -1, %1" : "=s"(bad) : "v"(mx),
            "v"(p0[0]), "v"(p0[1]), "v"(p0[2]), "v"(p0[3]), "v"(p1[0]), "v"(p1[1]), "v"(p1[2]), "v"(p1[3]) : "memory");
    }
    unsigned rv, rs, hb; unsigned long long rt;
    template <int I> DEVI void ask(const Ctx& k, unsigned l16) {
        if constexpr (I == 0) asm volatile("v_and_b32 %0, 0xf0, %1\n\tv_lshlrev_b32 %0, 1, %0" : "=v"(rv) : "v"(l16));
        if constexpr (I == 1) asm volatile("buffer_load_dword %0, %0, %1, %2 offen sc1" : "+v"(rv) : "s"(k.rs), "s"(k.p0 + CNT(C_XB, 0)) : "memory");
    }
    template <int I, int N = 4> DEVI void red() {
        if constexpr (I == 0 && N < 0) asm volatile("v_max_u32_dpp %0, %0, %0 quad_perm:[1,0,3,2] row_mask:0xf bank_mask:0xf" : "+v"(rv));
        else if constexpr (I == 0) asm volatile("s_waitcnt vmcnt(%1)\n\tv_max_u32_dpp %0, %0, %0 quad_perm:[1,0,3,2] row_mask:0xf bank_mask:0xf" : "+v"(rv) : "n"(N));
        if constexpr (I == 1) asm volatile("v_max_u32_dpp %0, %0, %0 quad_perm:[2,3,0,1] row_mask:0xf bank_mask:0xf" : "+v"(rv));
        if constexpr (I == 2) asm volatile("v_max_u32_dpp %0, %0, %0 row_ror:4 row_mask:0xf bank_mask:0xf" : "+v"(rv));
        if constexpr (I == 3) asm volatile("v_max_u32_dpp %0, %0, %0 row_ror:8 row_mask:0xf bank_mask:0xf" : "+v"(rv));
        if constexpr (I == 4) asm volatile("v_readfirstlane_b32 %0, %1" : "=s"(rs) : "v"(rv));
    }
    template <int I> DEVI void hold(unsigned step) {
        if constexpr (I == -1) rt = rt_async();
        if constexpr (I == 3) hb = spack((unsigned)rt, step);
        if constexpr (I == 4) asm volatile("s_sub_u32 %0, %1, %0" : "+s"(hb) : "s"(rs) : "scc");
        if constexpr (I == 5) if (__builtin_expect(hb < 128u, 0)) { while ((int)((rclk() - rs) << 16) < 0) { } }
    }
    template <int I> DEVI void at(const Ctx& k, unsigned l16) {
        if constexpr (I == 1) op<2>(k, l16, 0);
        if constexpr (I >= 2 && I < 15 && (I % 5 == 2 || I % 5 == 4)) op<3 + (I / 5) * 2 + (I % 5) / 4>(k, l16, 0);
    }
    DEVI void again(const Ctx& k, unsigned l16) {
        asm volatile("s_mov_b32 m0, %9\n\tbuffer_load_dwordx4 %2, %4, %5, %7 offen offset:2048 sc1\n\tbuffer_load_dwordx4 %3, %4, %5, %8 offen offset:3072 sc1\n\t"
            "buffer_load_dwordx4 %4, %5, %7 offen offset:2048 sc1 lds\n\tbuffer_load_dwordx4 %4, %5, %8 offen offset:3072 sc1 lds\n\t"
            "buffer_load_dwordx4 %0, %4, %5, %6 offen sc1\n\tbuffer_load_dwordx4 %1, %4, %5, %6 offen offset:1024 sc1\n\ts_waitcnt vmcnt(0)"
            : "+v"(p0), "+v"(p1), "+v"(v0), "+v"(v1) : "v"(l16), "s"(k.rs), "s"(a), "s"(aw), "s"(b), "s"(dst) : "memory");
        check();
    }
};
DEVI void clr2(f32x4& c) { f32x2 a, b; asm volatile("v_mov_b64 %0, 0\n\tv_mov_b64 %1, 0" : "=v"(a), "=v"(b)); c = __builtin_shufflevector(a, b, 0, 1, 2, 3); }
#define NOP7_3(a) asm volatile("s_nop 7" : "+v"(a[0]), "+v"(a[1]), "+v"(a[2]))

struct G3 {
    f32x4 a0[3], a1[3];
    bf16x8 x0[20], x1[20], wl[20];
    unsigned l16, lwv;
    template <int KS> DEVI void r0() { x0[KS] = lr128(l16 + L_XN + KS * 2048); }
    template <int KS> DEVI void r1() { x1[KS] = lr128(l16 + L_XN + KS * 2048 + 1024); }
    template <int KS> DEVI void rw() { if constexpr (KS >= 4) wl[KS] = lr128(lwv + (KS - 4) * 4096); }
    template <bool WL, int KS> DEVI void pre() { if constexpr (WL && KS >= 4) asm volatile("" :: "v"(wl[KS]), "v"(x0[KS]), "v"(x1[KS])); else asm volatile("" :: "v"(x0[KS]), "v"(x1[KS])); }
    template <int ID0, bool WL, int ks, int m> DEVI void mm(const bf16x16 (&W)[52]) {
        constexpr int b = ks >> 1, t = m % 3, id = ID0 + t * 10 + b;
        f32x4& c = m < 3 ? a0[t] : a1[t];
        const bf16x8& x = m < 3 ? x0[ks] : x1[ks];
        if constexpr (WL && t == 2 && b >= 2) { if constexpr (ID0 == 30) mfb<false, false>(c, x, wl[ks]); else mfa<false, false>(c, wl[ks], x); }
        else mfw<id % 52, ks & 1, ID0 == 30, ks == 0>(c, W[id % 52], x);
        SCHED0();
    }
    template <int ID0, bool WL, int K0, int K1, int PF, bool H6 = false, class HOOK> DEVI void run(const bf16x16 (&W)[52], HOOK hook) {
        sfor<K1 - K0>([&](auto KK) {
            constexpr int kk = CV(KK), ks = K0 + kk;
            sfor<6>([&](auto M) {
                constexpr int m = CV(M);
                mm<ID0, WL, ks, m>(W);
                if constexpr (m == 5 && ks + 1 < K1) { pre<WL, (ks + 1) % 20>(); SCHED0(); }
                if constexpr (ks + 2 < PF) {
                    if constexpr (m == 0) r0<(ks + 2) % 20>();
                    if constexpr (m == 1) r1<(ks + 2) % 20>();
                    if constexpr (m == 3 && WL) rw<(ks + 2) % 20>();
                }
                if constexpr (H6) hook(IC(kk * 6 + m));
                else { hook(IC(kk * 7 + (m < 5 ? m : 6))); if constexpr (m == 4) hook(IC(kk * 7 + 5)); }
                SCHED0();
            });
        });
    }
    template <int ID0, bool WL, int F0, int F1, int PF, class HOOK> DEVI void go(const bf16x16 (&W)[52], HOOK hook) {
        sfor<F1 - F0>([&](auto FF) {
            constexpr int f = F0 + CV(FF), ks = f / 6, m = f % 6;
            mm<ID0, WL, ks, m>(W);
            if constexpr (m == 5 && f + 1 < F1) { pre<WL, (ks + 1) % 20>(); SCHED0(); }
            if constexpr (ks + 2 < PF) rd<WL, (f + 12) % 120>();
            hook(IC(f));
            SCHED0();
        });
    }
    template <int P> DEVI void home() {
        if constexpr (P == 0) asm volatile("" : "+{v[12:15]}"(a0[0]), "+{v[16:19]}"(a0[1]), "+{v[20:23]}"(a0[2]), "+{v[24:27]}"(a1[0]), "+{v[28:31]}"(a1[1]), "+{v[32:35]}"(a1[2]));
        else asm volatile("" : "+{v[36:39]}"(a0[0]), "+{v[40:43]}"(a0[1]), "+{v[44:47]}"(a0[2]), "+{v[48:51]}"(a1[0]), "+{v[52:55]}"(a1[1]), "+{v[56:59]}"(a1[2]));
    }
    template <bool WL, int F> DEVI void rd() {
        if constexpr (F % 6 == 0) r0<F / 6>();
        if constexpr (F % 6 == 1) r1<F / 6>();
        if constexpr (F % 6 == 3 && WL) rw<F / 6>();
    }
    u32x2 o;
    template <int I, int OFF> DEVI void out4(unsigned d0) {
        if constexpr (I >= 0 && I < 24) {
            constexpr int t = I / 8, rt = (I / 4) & 1, j = I & 1;
            const f32x4& a = rt ? a1[t] : a0[t];
            if constexpr (I % 4 < 2) { o[j] = cvtpk(a[2 * j], a[2 * j + 1]); PIN(o[j]); }
            else lw32(d0 + OFF + rt * 6144 + t * 1024 + j * 4, o[j]);
        }
    }
    template <int I, int OFF> DEVI void out(unsigned d0, unsigned d1) {
        if constexpr (I >= 0 && I < 18) {
            constexpr int t = I / 6, rt = (I / 3) & 1;
            const f32x4& a = rt ? a1[t] : a0[t];
            if constexpr (I % 3 == 0) { o[0] = cvtpk(a[0], a[1]); PIN(o[0]); }
            else if constexpr (I % 3 == 1) { o[1] = cvtpk(a[2], a[3]); PIN(o[1]); }
            else if constexpr (OFF > 0) lw64(d0 + (OFF - 1) + rt * 6144 + t * 1024, o);
            else lw64((t == 1 ? d1 : d0) + OFF + rt * 6144 + (t == 2 ? 1024 : 0), o);
        }
    }
};
#define SPIX(l) (((l) & 2) ? 0xEEEEu : 0x4444u)
template <int I> DEVI void fold_op(f32x4& a, unsigned& o) {
    if constexpr (I == 0) asm volatile("v_add_f32 %0, %0, %1" : "+v"(a[0]) : "v"(a[2]));
    if constexpr (I == 1) asm volatile("v_add_f32 %0, %0, %1" : "+v"(a[1]) : "v"(a[3]));
    if constexpr (I == 2) asm volatile("v_cvt_pk_bf16_f32 %0, %1, %2" : "=v"(o) : "v"(a[0]), "v"(a[1]));
}

template <int J> DEVI void fold2(f32x4& a, f32x4& b, unsigned (&o)[2]) {
    if constexpr (J < 2) asm volatile("v_add_f32 %0, %0, %2\n\tv_add_f32 %1, %1, %3" : "+v"(a[J]), "+v"(b[J]) : "v"(a[J + 2]), "v"(b[J + 2]));
    else asm volatile("v_cvt_pk_bf16_f32 %0, %2, %3\n\tv_cvt_pk_bf16_f32 %1, %4, %5" : "=&v"(o[0]), "=&v"(o[1]) : "v"(a[0]), "v"(a[1]), "v"(b[0]), "v"(b[1]));
}
DEVI unsigned vshl1(unsigned x, unsigned b) { unsigned d; asm volatile("v_lshl_or_b32 %0, %1, 1, %2" : "=v"(d) : "s"(x), "n"(b)); return d; }

#define ANNOUNCE(s, now, d) (((unsigned)(s) << 16) | (((unsigned)(now) + (d)) & 0xFFFFu))

template <class PUT> DEVI void ln_rows(const u32x4 (&a)[5], int n, int row  , PUT put) {
    float v[40];
#pragma unroll
    for (int i = 0; i < 20; ++i) { v[2 * i] = blo(a[i >> 2][i & 3]); v[2 * i + 1] = bhi(a[i >> 2][i & 3]); }
    f32x2 s2[4] = {{0.f, 0.f}, {0.f, 0.f}, {0.f, 0.f}, {0.f, 0.f}};
#pragma unroll
    for (int i = 0; i < 40; i += 2) s2[(i >> 1) & 3] += (f32x2){v[i], v[i + 1]};
    const f32x2 st = (s2[0] + s2[1]) + (s2[2] + s2[3]);
    float m = row16sum(st[0] + st[1]) * (1.f / 640.f);
    f32x2 q2[4] = {{0.f, 0.f}, {0.f, 0.f}, {0.f, 0.f}, {0.f, 0.f}};
#pragma unroll
    for (int i = 0; i < 40; i += 2) { const f32x2 d = (f32x2){v[i], v[i + 1]} - (f32x2){m, m}; q2[(i >> 1) & 3] += d * d; }
    const f32x2 qt = (q2[0] + q2[1]) + (q2[2] + q2[3]);
    const float q = row16sum(qt[0] + qt[1]);
    const float var = bfr(q * (1.f / 640.f));
    m = bfr(m);
    const float rden = __builtin_amdgcn_rcpf(bfr(__builtin_amdgcn_sqrtf(bfr(var + 1e-5f))));
    const unsigned vo = (n >> 2) * 2048 + (row >> 4) * 1024 + ((n & 3) * 16 + (row & 15)) * 16;
#pragma unroll
    for (int c = 0; c < 5; ++c) {
        u32x4 o;
#pragma unroll
        for (int i = 0; i < 4; ++i) { unsigned d = cvtpk(v[c * 8 + 2 * i] - m, v[c * 8 + 2 * i + 1] - m); o[i] = cvtpk(blo(d) * rden, bhi(d) * rden); }
        put(vo, c, o);
    }
}

struct Soft {
    unsigned long long mA, mY, mC[8];
    unsigned v0, v1;
    unsigned sg, hs;
    float a[6], m, M, t, s, h; u32x2 pk;
    template <int I> DEVI void mask(f32x4 (&sc)[3]) {
        if constexpr (I == 0) asm volatile("v_mov_b32 %0, 0xfe800000" : "=v"(t));
        if constexpr (I >= 1 && I < 9) { float d; asm volatile("v_cndmask_b32_e64 %0, %1, 0, %2" : "=v"(d) : "v"(t), "s"(mC[I - 1])); sc[(I - 1) >> 2][(I - 1) & 3] = d; }
    }
    template <int I> DEVI void op(const Ctx& k, f32x4 (&sc)[3], unsigned a0, unsigned a1, unsigned hv) {
#define FMA(x, y) "v_fmamk_f32 " x ", " x ", 0x3dd53b95, " y "\n\t"
#define EXPC(x) "v_exp_f32_e64 " x ", " x " clamp\n\t"
#define OP3(o, d, x, y) o " " d ", " x ", " y "\n\t"
#define SA(txt) asm volatile(txt : "+v"(a[0]), "+v"(a[1]), "+v"(a[2]), "+v"(a[3]), "+v"(m), "+v"(t))
#define SB(txt) asm volatile(txt : "+v"(a[0]), "+v"(a[1]), "+v"(a[2]), "+v"(a[3]), "+v"(m), "+v"(t), "+v"(s), "+v"(a[4]), "+v"(a[5]), "+v"(M), "+v"(h) : "s"(hs), "s"(sg))
#define ADD(d, x, y) OP3("v_add_f32", d, x, y)
#define MUL(d, x, y) OP3("v_mul_f32", d, x, y)
#define CND(d, x, y, c) "v_cndmask_b32_e64 " d ", " x ", " y ", " c "\n\t"
        if constexpr (I == 0) asm volatile("v_max3_f32 %0, %1, %2, %3\n\tv_max3_f32 %0, %0, %4, %5" : "=&v"(m) : "v"(sc[0][0]), "v"(sc[0][1]), "v"(sc[0][2]), "v"(sc[0][3]), "v"(sc[1][0]));
        if constexpr (I == 1) asm volatile("v_max3_f32 %0, %0, %2, %3\n\t" CND("%1", "%4", "%5", "%6") : "+v"(m), "=&v"(a[2]) : "v"(sc[1][1]), "v"(sc[1][2]), "v"(sc[0][2]), "v"(sc[1][0]), "s"(mY));
        if constexpr (I == 2) asm volatile("v_max_f32 %0, %0, %2\n\t" CND("%1", "%3", "%4", "%5") : "+v"(m), "=&v"(a[3]) : "v"(sc[1][3]), "v"(sc[0][3]), "v"(sc[1][1]), "s"(mY));
        if constexpr (I == 3) asm volatile("v_mul_f32 %1, 0xbdd53b95, %5\n\t" CND("%0", "%2", "%3", "%4") : "=&v"(a[0]), "=&v"(t) : "v"(sc[0][0]), "v"(a[2]), "s"(mA), "v"(m));
        if constexpr (I == 4) asm volatile(CND("%0", "%2", "%3", "%5") CND("%1", "%1", "%4", "%5") : "=&v"(a[1]), "+v"(a[2]) : "v"(sc[0][1]), "v"(a[3]), "v"(sc[1][2]), "s"(mA));
        if constexpr (I == 5) asm volatile(CND("%0", "%0", "%2", "%3") FMA("%1", "%4") : "+v"(a[3]), "+v"(a[0]) : "v"(sc[1][3]), "s"(mA), "v"(t));
        if constexpr (I == 6) SA(FMA("%1", "%5") FMA("%2", "%5"));
        if constexpr (I == 7) asm volatile(FMA("%0", "%3") "v_mov_b32 %1, %4" : "+v"(a[3]), "=&v"(h), "=v"(s) : "v"(t), "s"(hv));
        if constexpr (I == 8) SA(EXPC("%0"));
        if constexpr (I == 9) SA(EXPC("%1"));
        if constexpr (I == 10) { asm volatile("v_max3_f32 %0, %2, %3, %4\n\tv_mov_b32 %1, %0" : "=&v"(M), "=&v"(t) : "v"(m), "v"(sc[2][0]), "v"(sc[2][1]), "v"(sc[2])); a[4] = sc[2][0]; a[5] = sc[2][1]; }
        if constexpr (I == 11) SB("v_permlane16_swap_b32 %9, %5");
        if constexpr (I == 12) SB(OP3("v_max_f32", "%9", "%9", "%5") "v_mov_b32 %5, %9");
        if constexpr (I == 13) SB("v_permlane32_swap_b32 %9, %5");
        if constexpr (I == 14) SB(OP3("v_max_f32", "%9", "%9", "%5") "v_mul_f32 %9, 0xbdd53b95, %9");
        if constexpr (I == 15) SB(FMA("%7", "%9") FMA("%8", "%9"));
        if constexpr (I == 16) SB(FMA("%4", "%9") ADD("%6", "%0", "%1"));
        if constexpr (I == 17) SB(EXPC("%2"));
        if constexpr (I == 18) SB(EXPC("%3"));
        if constexpr (I == 19) SB(ADD("%6", "%6", "%2") ADD("%6", "%6", "%3"));
        if constexpr (I == 20) SB(EXPC("%7"));
        if constexpr (I == 21) SB(EXPC("%8"));
        if constexpr (I == 22) SB(MUL("%6", "%12", "%6") ADD("%5", "%7", "%8"));
        if constexpr (I == 23) SB(EXPC("%4"));
        if constexpr (I == 24) SB(MUL("%6", "%6", "%4") OP3("v_fmac_f32", "%6", "%11", "%5"));
        if constexpr (I == 25) SB("v_fmac_f32_dpp %6, %6, %10 row_ror:8 row_mask:0xf bank_mask:0xf\n\tv_mov_b32 %5, %6");
        if constexpr (I == 26) SB("v_permlane16_swap_b32 %6, %5");
        if constexpr (I == 27) SB(ADD("%6", "%6", "%5") "v_mov_b32 %5, %6");
        if constexpr (I == 28) SB("v_permlane32_swap_b32 %6, %5");
        if constexpr (I == 29) SB(ADD("%6", "%6", "%5"));
        if constexpr (I == 30) SB("v_rcp_f32 %6, %6");
        if constexpr (I == 31) SB(MUL("%4", "%4", "%6") MUL("%7", "%7", "%6"));
        if constexpr (I == 32) SB(MUL("%0", "%0", "%4") MUL("%1", "%1", "%4"));
        if constexpr (I == 33) SB(MUL("%2", "%2", "%4") MUL("%3", "%3", "%4"));
        if constexpr (I == 34) { unsigned x, y; asm volatile("v_cvt_pk_bf16_f32 %0, %2, %3\n\tv_cvt_pk_bf16_f32 %1, %4, %5" : "=&v"(x), "=v"(y) : "v"(a[0]), "v"(a[1]), "v"(a[2]), "v"(a[3])); pk[0] = x; pk[1] = y; }
        if constexpr (I == 35) { st64(k.rs, v0, a0, pk); SCHED0(); asm volatile("v_mul_f32 %1, %1, %2\n\tv_cvt_pk_bf16_f32 %0, %0, %1" : "+v"(a[4]), "+v"(a[5]) : "v"(s)); }
        if constexpr (I == 36) st32(k.rs, v1, a1, __float_as_uint(a[4]));
    }
};
#define QIDX(t, b) ((t) * 10 + (b))
DEVI void qk_role(const Params& p, Ctx& k, char* smem, int h) {
    PROF_DECL
    const int w = k.w;
    bf16x16 W[52];
    Gather G;
    Soft S;
    const int hw = h >> 3, hh = h & 7;
    unsigned l16, ix, dqm, dq, trp, qp;
    {
        const int l = k.l, g = l >> 4, n = l & 15, r = n & 7, u = w & 1;
        const bool hf = n >= 8, dup = w != 1;
        S.mA = __ballot(dup && hf);
        const int lim = (w == 0 ? 64 : w == 1 ? n : 16 + 8 * u + r) - 4 * g;
#pragma unroll
        for (int i = 0; i < 8; ++i) S.mC[i] = __ballot((i & 3) + (i >> 2) * 16 <= lim);
        const bool okm = !(dup && hf);
        S.mY = __ballot((dup && hf) == (w != 0));
        S.v0 = w == 0 ? hh * 512 + ((g >> 1) * 16 + (r >> 1) * 4 + hf * 2 + (r & 1)) * 16 + (g & 1) * 8 : w == 1 ? (hh >> 1) * 1024 + l * 16 + (hh & 1) * 8 : hh * 1024 + (g * 16 + 8 * u + r) * 16 + hf * 8;
        S.v1 = !okm ? OOB : w == 0 ? 4096 + hh * 128 + r * 16 + g * 4 : (w == 1 ? 4096 + n * 16 : 6144 + (8 * u + r) * 16) + (hh >> 1) * 512 + (g >> 1) * 256 + (hh & 1) * 8 + (g & 1) * 4;
        l16 = l * 16; ix = SPIX(l);
        const int uu = u ^ ((n >> 2) & 1);
        dqm = L_QM + (w >> 1) * 3072 + uu * 512 + (g >> 1) * 128 + n * 8 + (g & 1) * 4;
        dq = L_QM + 6144 + (w >> 1) * 3072 + uu * 512 + g * 128 + ((n * 8) ^ ((g >> 1) * 64));
        trp = L_QM + 6144 + (l & 3) * 128 + (((l >> 2) * 8) ^ ((l & 2) * 32));
        qp = w == 1 ? trp + 6144 : (w == 0 ? L_QM : L_Q + 6144 + u * 256) + (l & 1) * 128 + (((l >> 2) * 8) ^ (w == 3 ? 64 : 0));
        u32x8 rg = {S.v0, S.v1, l16, ix, dqm, dq, trp, qp};
        asm volatile("" : "+{v[0:7]}"(rg));
        S.v0 = rg[0]; S.v1 = rg[1]; l16 = rg[2]; ix = rg[3]; dqm = rg[4]; dq = rg[5]; trp = rg[6]; qp = rg[7];
    }
    Xn X;
    G3 A, B;
    X.l16 = l16; A.l16 = l16; B.l16 = l16; B.lwv = vadd(l16, L_W + w * 1024);
    {
        const int l = k.l;
        bf16x8 wt[16];
        u32x2 gv;
        unsigned ra[3];
#pragma unroll
        for (int t = 0; t < 3; ++t) ra[t] = opq(wp_adr<384, true>(w * 3 + t, l) + SLOT(0));
#define QEX(v) ((v) < 0 ? 0 : ((v) < 6) + ((v) == QGS) + 10 * ((v) == QGS + 2) + 8 * ((v) == QGS + 7))
#define QKE(v) ((v) < QGS + 4 ? 0 : (v) == 19 ? 20 : CMIN(v, 3 * ((v) - QGS - 3)))
        auto rdx = [&](auto K) { constexpr int ks = CV(K); A.r0<ks>(); A.r1<ks>(); B.x0[ks] = A.x0[ks]; B.x1[ks] = A.x1[ks]; if constexpr (ks < 14 && QKE(ks + 1) <= ks) B.rw<ks>(); };
        cstream<20>([](auto V) { constexpr int v = CV(V); return IC(QEX(v - 1) + (v < 19 ? 6 : 0)); },
            [&](auto U) { constexpr int u = CV(U), b = u >> 1, k0 = QKE(u - 1), n = (QKE(u) - k0) * 12, nn = n ? n : 12;
                if constexpr (u < 6) hello(k, 0);
                if constexpr (u == QGS) gv = go_ask(k);
                if constexpr (u == QGS + 2) { PIN(gv); go_wait(k, 0, gv); X.init(k); G.init(k); X.l16 = k.c ? OOB : l16; xn_all(k, X.l16, X.src, X.dst); }
                if constexpr (u == QGS + 4) rdx(IC(0));
                if constexpr (u == QGS + 7) { sfor<3>([&](auto I) { X.next<CV(I)>(k, l16); G.op<CV(I)>(k, l16, 0); }); X.se = X.src; }
                if constexpr (u == 18) {
                    asm volatile("" :: "{v[152:155]}"(wt[10]), "{v[156:159]}"(wt[11]), "{v[232:235]}"(wt[12]));
#pragma unroll
                    for (int i = 10; i < 13; ++i) lw128(B.lwv + i * 4096, __builtin_bit_cast(u32x4, wt[i]));
                }
                if constexpr (u == 19) { G.wait<0>(); lw128(l16 + G.dst + 2048, G.v0); lw128(l16 + G.dst + 3072, G.v1); }
                XSTAMP(u)
                const unsigned rb[3] = {slot_adr<u>(ra[0]), slot_adr<u>(ra[1]), slot_adr<u>(ra[2])};
                bf16x4 hf[12];
                SCHED0();
                sfor<nn>([&](auto I) { constexpr int i = CV(I), ks = k0 + i / 12, m = i % 12, t = i >> 1, e = u == QGS + 7 ? 8 : 0, j = ((i - e) * 6) / (nn - e);
                    if constexpr (n > 0) { if constexpr (m < 6) A.mm<0, false, ks, m>(W); else B.mm<30, true, ks, m - 6>(W); }
                    if constexpr (i < e) { if constexpr (i % 4 == 0) X.op<i / 4, 0>(k); if constexpr (i & 1) X.op<i / 4, 1 + (i / 2) % 2>(k); else G.op<3 + i / 4 + (i & 2) * 2>(k, l16, 0); }
                    else if constexpr (u > 0 && u < 18 && (i == e || ((i - e - 1) * 6) / (nn - e) != j))
                        dma1<SLOT(u + 2) + (j / 3) * 12288 + (j % 3) * 1024>((j < 3 ? p.Wk : p.Wq) + (size_t)(u + 2) * 32 * 6144 + h * 192, wsrc<384, true>(w * 192 + (j % 3) * 64 + l, 12288), w * 3072);
                    if constexpr (i == 0 && u > 4 && (u < 15 || u >= 18)) lw128(B.lwv + (u - 5) * 4096, __builtin_bit_cast(u32x4, wt[(u - 5) & 15]));
                    if constexpr (i < 12) {
                        hf[i] = lrtr(rb[t % 3] + (t / 3) * 12288 + (i & 1) * 1536);
                        if constexpr (i & 1) {
                            if constexpr (t == 5 && b >= 2) B.wl[u] = wt[(u - 4) & 15] = CAT4(hf[i - 1], hf[i]);
                            else if constexpr (u & 1) W[QIDX(t, b) % 52] = CAT8(LO8(W[QIDX(t, b) % 52]), CAT4(hf[i - 1], hf[i]));
                            else W[QIDX(t, b) % 52] = __builtin_shufflevector(hf[i - 1], hf[i], 0, 1, 2, 3, 4, 5, 6, 7, -1, -1, -1, -1, -1, -1, -1, -1);
                        }
                    }
                    if constexpr (n > 0 && m == 3 && ks < 19) rdx(IC((ks + 1) % 20));
                    SCHED0(); }); });
        lw128(B.lwv + 15 * 4096, __builtin_bit_cast(u32x4, wt[15]));
        static_assert(QKE(17) == 17, "QGS");
        RAWBAR()
        XSTAMP(20)
        const unsigned d0 = (l16 | ((l16 >> 5) & 8)) ^ (L_K + (w >> 1) * 3072 + (w & 1) * 8);
        sfor<24>([&](auto I) { A.out4<CV(I), 0>(d0); });
        G.v0 = G.p0; G.v1 = G.p0; G.bad = 0;
        X.off = 0; X.l16 = l16;
        if (k.c) X.init(k);
    }
    RSTAMP(2)
    const unsigned po0 = k.p0 + PR_B + hw * 49152 + (w == 0 ? 0 : w == 1 ? 32768 : 16384), po1 = k.p0 + PR_B + hw * 49152 + (w == 0 ? 0 : 32768);
    const unsigned pdum = k.pg(1) + DUMMY_B, pbc = w == 0 ? k.p0 + CNT(C_BC, hw * 16 + hh) : pdum + EXP(32768 + h * 512 + w * 128);
    unsigned long long now = 0, done = 0, tpa = 0, tpb = 0;
    unsigned a0 = po0 + (k.c ? pdum - k.p0 - PR_B : 0u), a1 = po1 + (k.c ? pdum - k.p0 - PR_B : 0u);
    const unsigned hv = w == 1 ? 0u : 0x3F800000u, hw2 = w == 1 ? 0u : 0x00800000u;
    S.sg = 0x3F800000u; S.hs = 0x3F800000u - hw2;
    f32x4 ac[6];
#pragma unroll
    for (int t = 0; t < 6; ++t) clr2(ac[t]);
    unsigned an = 110;
    __builtin_amdgcn_s_waitcnt(0xc07f);
    do {
        if (X.off != 0) {
            PC8(1, X.sg)
            X.late<2>(k, l16);
            G.check();
        }
_Pragma("unroll 1")
        for (unsigned spins = 0; G.bad != 0 && !spin_fail(k, spins); ++spins) { PC8(0, X.sg) G.again(k, l16); }
        HSTOP(0) HSTAMP(0, 0)
        clr2(ac[5]);
        asm volatile("s_nop 1" : "+v"(ac[5]));
        smfw<50>(ac[5], __builtin_bit_cast(bf16x8, G.p0), W[50], ix);
    do {
        PROF_S(X.sg - 1)
        f32x4 sc[3];
        {
            bf16x8 m[16], wa[10], wb[10], kf[12];
            bf16x4 q[12], km[12];
            unsigned fo[6];
            m[0] = __builtin_bit_cast(bf16x8, G.p0); m[1] = __builtin_bit_cast(bf16x8, G.p1);
            auto put = [&](auto T) { constexpr int t = CV(T); if constexpr (t < 3) lw32(dq + t * 1024, fo[t]); else lw32(dqm + (t - 3) * 1024, fo[t]); };
            auto rd = [&](auto C) { constexpr int c = CV(C), j = c % 20;
                if constexpr (c < 0) { }
                else if constexpr (c < 20) m[j] = lr128(l16 + MADR(j < 10 ? j : j - 8));
                else if constexpr (c < 40) wa[j] = lr128(B.lwv + (j - 2) * 8192);
                else if constexpr (c < 60) wb[j] = lr128(B.lwv + (j - 2) * 8192 + 4096);
                else if constexpr (c < 80) kf[j] = lr128(l16 + L_K + ((j & 1) * 6 + j / 2) * 1024);
                else if constexpr (c < 100) q[j] = lrtr(qp + j * 512);
                else if constexpr (c < 120) km[j] = lrtr(trp + j * 512);
                else if constexpr (c < 140) A.r0<j>();
                else A.r1<j>(); };
#define PB(i) ((i) < 12 ? (i) / 6 : (i) < 32 ? (i) / 4 - 1 : (i) < 44 ? 7 + ((i) - 32) % 3 : (i) < 54 ? 7 + ((i) - 44) / 2 : 12 + ((i) - 54) % 3)
#define PT(i) ((i) < 12 ? ((i) + 5) % 6 : (i) < 32 ? (3 + (i) % 4) % 6 : (i) < 44 ? (3 + ((i) - 32) / 3) % 6 : (i) < 54 ? 1 + ((i) & 1) : 1 + ((i) - 54) / 3)
            sfor<82>([&](auto I) {
                constexpr int i = CV(I) + 1, b = PB(i), t = PT(i), bn = PB(i + 1);
                constexpr int rt[83] = {-1, -1, 2, -1, -1, 22, 42, 3, -1, 23, 43, 4, -1, 24, 44, 5, -1, 25, 45, 6,
                    -1, 26, 46, 7, -1, 8, 9, 27, 47, 28, 48, 29, 49, -1, 60, 61, 62, 63, 64, 65,
                    -1, 10, 66, -1, 11, 12, -1, 13, 14, -1, -1, 80, 82, 83, 84, 85, 86, 87, 67, 88,
                    89, 69, -1, 91, -1, 71, 120, 121, -1, 100, 102, 104, 106, 108, 110, -1, 140, -1, -1, 141, -1, -1, 122};
                constexpr int r2[83] = {-1, -1, -1, -1, -1, -1, -1, -1, -1, -1, -1, -1, -1, -1, -1, -1, -1, -1, -1, -1,
                    -1, -1, -1, -1, -1, -1, -1, -1, -1, -1, -1, -1, -1, -1, -1, -1, -1, -1, -1, -1,
                    -1, -1, -1, -1, -1, -1, -1, -1, -1, -1, -1, 81, -1, -1, -1, -1, -1, -1, -1, -1,
                    68, 90, -1, 70, -1, -1, -1, -1, -1, 101, 103, 105, 107, 109, 111, -1, -1, -1, -1, -1, -1, -1, -1};
                if constexpr (i < 60) {
                    if constexpr (t == 5 && b >= 2) smf<false>(ac[5], m[b], CAT8(wa[b], wb[b]), ix);
                    else smfw<QIDX(t, b < 10 ? b : b - 8) % 52>(ac[t], m[b], W[QIDX(t, b < 10 ? b : b - 8) % 52], ix);
                } else if constexpr (i < 72) { constexpr int j = i - 60; mfa<false, false>(sc[j & 1], kf[j], CAT4(q[j & ~1], q[j | 1])); }
                else if constexpr (i < 77) A.mm<0, false, (i - 72) / 3, (i - 72) % 3>(W);
                else { constexpr int ks = i - 77; mfa<false, ks == 0>(sc[2], CAT4(km[2 * ks], km[2 * ks + 1]), CAT4(q[2 * ks], q[2 * ks + 1])); }
                SCHED0();
                if constexpr (i == 1) { AQ(3, 3) asm volatile("s_barrier" ::: "memory"); AQ(1, 2) AQ(3, 4) }
                else if constexpr (i == 50) { BARL(3); AQ(1, 3) }
                else if constexpr (i == 68) { BARL(3); AQ(1, 4) QSTAMP(0, 1) }
                else if constexpr (i >= 11 && i < 59) { if constexpr (PT(i + 1) == 5) TOUCH(wb[bn % 10]); TOUCH(m[bn]); }
                else if constexpr (i >= 59 && i < 71) { asm volatile("" :: "v"(kf[(i - 59) % 12]), "v"(q[((i - 59) | 1) % 12])); }
                else if constexpr (i == 71) TOUCH(A.x0[0]);
                else if constexpr (i == 74) TOUCH(A.x0[1]);
                else if constexpr (i >= 76 && i < 82) TOUCH(km[(2 * (i - 76) + 1) % 12]);
                if constexpr (i == 8 || i == 30) asm volatile("" :: "{v[60:63]}"(ac[0]), "{v[28:31]}"(ac[1]), "{v[32:35]}"(ac[2]), "{v[64:67]}"(ac[3]), "{v[68:71]}"(ac[4]), "{v[72:75]}"(ac[5]));
                TPQ(i)
                if constexpr (i == 14) AC3(0, 0, 0)
                if constexpr (i == 16) AC3(1, 6, 1)
                if constexpr (i == 18) AC3(2, 7, 2)
                if constexpr (i == 20) AC3(-1, -1, 3)
                if constexpr (i == 22) AC3(-1, -1, 4)
                if constexpr (i == 24) AC3(-1, -1, 5)
                if constexpr (i == 66) AC3(3, -1, 6)
                if constexpr (i == 2) { now = rt_async(); HSTOP(1) }
                if constexpr (i == APOS) { PINS(now); an += (unsigned)now; PINS(an); }
                if constexpr (i == APOS + 1) an = spack(an, X.sg);
                SCHED0();
                rd(IC(rt[i])); rd(IC(r2[i]));
                if constexpr (i == APOS + 7) st32(k.rs, 0, pbc, fo[1]);
                if constexpr (i >= 40 && i < 50 && i % 3 == 1) put(IC(((i - 31) / 3) % 6));
                if constexpr (i == 62 || i == 64) put(IC(i / 2 - 30));
                SCHED0();
                if constexpr (i < 25 && i % 4 != 1) B.out<((i - 2) / 4) * 3 + (i - 2) % 4, 6145>(dq, dq);
                if constexpr (i == APOS + 3) { fo[1] = an; PIN(fo[1]); }
                if constexpr (i >= 37 && i < 49) { constexpr int tt = ((i - 28) / 3) % 6; fold_op<(i - 37) % 3>(ac[tt], fo[tt]); }
                if constexpr (i >= 59 && i < 62) fold_op<i - 59>(ac[1], fo[1]);
                if constexpr (i == 62) fold_op<0>(ac[2], fo[2]);
                if constexpr (i == 63) asm volatile("v_add_f32 %1, %1, %2\n\tv_cvt_pk_bf16_f32 %0, %3, %1" : "=v"(fo[2]), "+v"(ac[2][1]) : "v"(ac[2][3]), "v"(ac[2][0]));
                if constexpr (i == 33) S.mask<0>(sc);
                if constexpr (i >= 51 && i < 59) S.mask<i - 50>(sc);
                if constexpr (i >= 75) S.op<i - 75>(k, sc, a0, a1, hv);
                if constexpr (i == 12) { AQ(3, 5) }
                if constexpr (i == 43) { AQ(3, 6) }
                if constexpr (i == 59) { AQ(3, 7) }
                SCHED0();
            });
        }
        QSTAMP(0, 2)
        {
            unsigned d0;
            auto rest = [&](auto J) { constexpr int j = CV(J);
                if constexpr (j >= 0 && j < 12 && (j % 6 == 0 || j % 3 == 2)) X.op<3 + j / 6, (j % 6 + 1) / 3>(k); };
            sfor<43>([&](auto F) { constexpr int f = CV(F) + 5;
                constexpr int e = f < 6 ? f + 3 : f < 9 ? f - 3 : f;
                A.mm<0, false, e / 6, e % 6>(W);
                if constexpr (f % 6 == 2 || f == 5) TOUCH(A.x1[f / 6]);
                else if constexpr (f == 47) { TOUCH(B.x0[0]); AQ(1, 6) AQ(2, 1) }
                else if constexpr (f % 6 == 5) TOUCH(A.x0[f / 6 + 1]);
                if constexpr (f == 40 - 6 * SOFT2) { done = rt_async(); QSTAMP(0, 3) AQ(1, 5) }
                TPQ(f + 78)
                if constexpr (f == 21) set_m0(X.dst + 2 * 8192);
                if constexpr (f == 24) { X.sb = X.se + 2 * 16384; PINS(X.sb); }
                if constexpr (f == 42) AC3(4, -1, 7)
                SCHED0();
                if constexpr (f % 6 == 4 && f < 36) A.r0<f / 6 + 2>();
                if constexpr (f % 6 == 1 && f < 42) A.r1<f / 6 + 1>();
                if constexpr (f >= 39 && f < 43) B.rd<true, ((f - 39) / 2) * 6 + (f - 39) % 2>();
                if constexpr (f == 26 || f == 31 + 4 * QT) X.op<2, f / 15>(k);
                SCHED0();
                S.op<f + 3>(k, sc, a0, a1, hv);
                rest(IC(f - 38));
                if constexpr (f == 45) B.lwv = vadd(l16, L_W + w * 1024);
                SCHED0(); });
            B.go<30, true, 0, 48, 8>(W, [&](auto F) { constexpr int f = CV(F);
                TPQ(f + 126)
                rest(IC(f + 10));
                if constexpr (f == 9) AC3(5, -1, -1)
                if constexpr (f == 2) { a0 = (X.sg & 3) * PR_SZ; PINS(a0); }
                if constexpr (f == 4) { a1 = a0 + po1; a0 += po0; PINS(a0); PINS(a1); }
                if constexpr (f == 8) { S.sg = (X.sg & 4) ? 0xBF800000u : 0x3F800000u; PINS(S.sg); }
                if constexpr (f == 10) { S.hs = S.sg - hw2; PINS(S.hs); }
                if constexpr (f == 14 || f == 16) G.op<f / 16>(k, l16, X.sg);
                if constexpr (f == 22 || f == 28 || f == 32) X.next<(f - 22) / 5>(k, l16);
                if constexpr (f == 35 - QT) asm volatile("s_waitcnt vmcnt(%0)\n\ts_barrier" :: "n"(7 - 2 * QT) : "memory");
                if constexpr (f == 20) AC3(6, 1, -1)
                if constexpr (f == 26) A.home<0>();
                if constexpr (f >= 36) A.rd<false, f + 12>();
                if constexpr (f == 38 || f == 40) X.next<(f == 38 ? 5 : 6)>(k, l16);
                if constexpr (f == CPOS - 1 || f == CPOS) X.next<f - CPOS + 4>(k, l16);
                if constexpr (f == 47) { A.pre<false, 8>(); AQ(1, 7) AQ(2, 2) } });
            A.go<0, false, 48, 120, 20>(W, [&](auto F) { constexpr int f = CV(F), i = f - XPOS;
                TPQ(f + 126)
                if constexpr (f == 56) B.home<1>();
                if constexpr (i == 0 || i == 2 || i == 6) X.next<7 + i / 2>(k, l16);
                if constexpr (i >= 3 && i < 15 && ((i - 3) % 6 == 0 || (i - 3) % 3 == 2)) X.op<(i - 3) / 6, ((i - 3) % 6 + 1) / 3>(k);
                if constexpr (f == 58) asm volatile("s_waitcnt vmcnt(1)\n\ts_barrier" ::: "memory");
                if constexpr (f == 57) AC3(7, 2, -1)
                if constexpr (f == RPOS - 5) G.ask<0>(k, l16);
                if constexpr (f == RPOS) { __builtin_amdgcn_s_setprio(0); SCHED0(); G.ask<1>(k, l16); }
                if constexpr (f >= 108) B.rd<true, f - 60>();
                SCHED0();
                G.red<f - 110, (RPOS < XPOS + 5 ? 4 : 0)>();
                if constexpr (f == 116) asm volatile("v_bfe_u32 %0, %1, 8, 1\n\tv_lshl_or_b32 %0, %0, 3, %1" : "=&v"(d0) : "v"(l16));
                if constexpr (f == 118) asm volatile("v_xor_b32 %0, %1, %0" : "+v"(d0) : "s"(L_K + (w >> 1) * 3072 + (w & 1) * 8));
                if constexpr (f == 119) { B.pre<true, 8>(); AQ(2, 3) } });
            B.go<30, true, 48, 120, 20>(W, [&](auto F) { constexpr int f = CV(F), i = f - 48;
                constexpr int ko[26] = {0, 1, 2, 4, 3, 6, 5, 8, 7, 9, 10, 11, 12, 13, 14, 16, 15, 18, 17, 20, 19, 21, 22, 23, -1, -1};
                TPQ(f + 198)
                G.hold<f - GPOS + 5>(X.sg);
                if constexpr (i < 24) { SCHED0(); A.out4<ko[i % 26], 0>(d0); }
                G.at<f - GPOS>(k, l16);
                if constexpr (f == 114) { lgkm0(done); PINS(G.rt); }
                if constexpr (f == 115) { PINS(now); HTRACE(X.sg - 2) an = (unsigned)done - (unsigned)now; PINS(an); }
                if constexpr (f == 116) { an = min(an, 200u); PINS(an); }
                if constexpr (f == 117) { an += DQ; PINS(an); }
                SCHED0();
                if constexpr (i == 28) AC3(-1, 3, -1)
                if constexpr (i == 50) AC3(-1, 4, -1)
                if constexpr (i == 62) AC3(-1, 5, -1)
                if constexpr (i == 23) { AQ(2, 4) }
                if constexpr (i == 47) { AQ(2, 5) }
                if constexpr (i == 65) { AQ(2, 6) }
                if constexpr (f == 119) { AQ(2, 7) }
                if constexpr (f == 112) asm volatile("" :: "{v[12:15]}"(G.v0), "{v[16:19]}"(G.v1), "{v[20:23]}"(G.p0), "{v[24:27]}"(G.p1), "{v9}"(B.lwv) TRHINT);
                G.early<f - EPOS>(X.off);
                if constexpr (f >= 113 && f < 119) clr2(ac[(f + 6 - 113 + 5) % 6]); });
            static_assert(GPOS >= 70 && GPOS <= 98 && XPOS % 6 == 2 && RPOS > XPOS && (RPOS + 1 < XPOS + 5 || RPOS >= XPOS + 14) && XPOS > 58 && CPOS > 8 && CPOS < 35 - QT && SOFT2, "GPOS");
            QSTAMP(0, 4) QSTAMP(0, 5)
            HFLUSH(s)
        }
        AST(0)
        PROF_ONLY(G.wait<0>();)
        HSTOP(0) HSTAMP(s, 0) AQ(1, 1) AQ(3, 1)
        G.rest();
        SCHED0();
        smfw<50>(ac[5], __builtin_bit_cast(bf16x8, G.p0), W[50], ix);
        AQ(3, 2)
        SCHED0();
    } while (G.bad == 0);
    } while (X.sg != 65u);
    PROF_OUT
}

#define OIDX(t, b) (22 + (t) * 3 + (b))
DEVI void vo_role(const Params& p, Ctx& k, char* smem, int h) {
    PROF_DECL
    const int w = k.w;
    bf16x16 W[52];
    unsigned l16, lwv, ix, dq0, trp, vo;
    {
        const int l = k.l, g = l >> 4, n = l & 15, q = (l >> 2) & 3, pp = l & 3;
        l16 = l * 16; lwv = l16 + L_W + w * 1024; ix = SPIX(l);
        dq0 = L_QM + (24 * w + (n >> 2) * 2 + (n & 1)) * 32 + g * 8 + ((n >> 1) & 1) * 4;
        trp = L_QM + (16 * (g >> 1) + 4 * (g & 1) + 8 * (q >> 1) + (q & 1)) * 32 + pp * 8;
        vo = EXP(h * 512) + (g >> 1) * 256 + n * 16 + (g & 1) * 8;
        u32x8 r = {dq0, trp, vo, ix, l16, lwv, 0u, 0u};
        PIN(r);
        dq0 = r[0]; trp = r[1]; vo = r[2]; ix = r[3]; l16 = r[4]; lwv = r[5];
    }
    Gather G;
    G3 V;
    Xn X;
    V.l16 = l16; V.lwv = lwv; X.l16 = l16;
    f32x4 y[20];
    bf16x8 v[12];
    u32x4 o[10];
    unsigned yb;
    {
        const int l = k.l;
        bf16x8 wt[16];
        bf16x4 q[10][4];
        f32x4 z[9][2];
        u32x2 gv;
        unsigned ra[6];
        const u16* wo = p.Wo + (size_t)h * 192 * 640;
        constexpr int L_V = L_XN + 24576;
#define VEX(v) ((v) < 0 ? 0 : ((v) < 6) + ((v) == VGS) + 10 * ((v) == VGS + 2) + 6 * ((v) == VGS + 6 || (v) == 18))
#define VKE(v) ((v) < VGS + 4 ? 0 : CMIN(20, 7 * ((v) - VGS - 3)))
#define YKE(v) ((v) < 16 ? 0 : 3 * (v) - 45)
        auto put = [&](int t) { st128(k.rs, X.l16, yb + t * 65536, (u32x4){cvtpk(z[t][0][0], z[t][0][1]), cvtpk(z[t][0][2], z[t][0][3]), cvtpk(z[t][1][0], z[t][1][1]), cvtpk(z[t][1][2], z[t][1][3])}); };
        auto rdx = [&](auto K) { constexpr int ks = CV(K); if constexpr (ks < 20) { V.r0<ks % 20>(); V.r1<ks % 20>(); if constexpr (ks < 14) V.rw<ks>(); } };
        auto rdv = [&](auto J) { constexpr int j = CV(J) % 6; v[j] = lr128(l16 + L_V + j * 1024); v[6 + j] = lr128(l16 + L_V + (6 + j) * 1024); };
        cstream<20>([](auto V) { constexpr int v = CV(V); return IC(VEX(v - 1) + (v < 19 ? 6 : 0)); },
            [&](auto U) { constexpr int u = CV(U), k0 = VKE(u - 1), y0 = YKE(u - 1), n = u < 16 ? (VKE(u) - k0) * 6 : u < 18 ? 36 : 18, nn = n ? n : 12, nr = u < 10 ? 3 : u < 16 ? 6 : 4;
                if constexpr (u < 6) hello(k, 0);
                if constexpr (u == VGS) gv = go_ask(k);
                if constexpr (u == VGS + 2) { PIN(gv); go_wait(k, 0, gv); X.init(k); X.l16 = k.c ? OOB : l16; xn_all(k, X.l16, X.src, X.dst); }
                if constexpr (u == VGS + 4) { rdx(IC(0)); rdx(IC(1)); }
                if constexpr (u == VGS + 6) { sfor<3>([&](auto I) { X.next<CV(I)>(k, l16); }); X.se = X.src; yb = k.yx(0, w) + EXP(h * 1024); }
                if constexpr (u == 16) rdv(IC(0));
                XSTAMP(u)
                if constexpr (u == 0 || u == 10 || u == 16) {
#pragma unroll
                    for (int t = 0; t < nr; ++t) ra[t] = opq((u == 0 ? wp_adr<384, true>(w * 3 + t, l) : u == 10 ? wp_adr<768, false>(w * 6 + t, l) : wp_adr<512, false>(w * 4 + t, l)) + SLOT(0));
                }
                unsigned rb[6];
#pragma unroll
                for (int t = 0; t < nr; ++t) rb[t] = slot_adr<u>(ra[t]);
                SCHED0();
                sfor<nn>([&](auto I) { constexpr int i = CV(I), e = u == VGS + 6 ? 9 : 0, j = ((i - e) * 6) / (nn - e), d = u + 2, r0 = u == 18 ? 6 : 0;
                    if constexpr (u < 16 && n > 0) { V.mm<0, true, k0 + i / 6, i % 6>(W); if constexpr (i % 6 == 3) rdx(IC(k0 + i / 6 + 2)); }
                    if constexpr (u >= 16) { constexpr int m = n / 3, ks = (y0 + i / m) % 6, t = (u / 18) * 6 + (i % m) / 2, rt = i & 1;
                        if constexpr (!(ks == 5 && t == 7 && rt)) { mfw<OIDX(t, ks >> 1), ks & 1, true, ks == 0>(z[t][rt], W[OIDX(t, ks >> 1)], v[rt * 6 + ks]); SCHED0(); }
                        if constexpr (i % m == 1 && u < 19) rdv(IC(ks + 1));
                        if constexpr (u == 19 && i < 2) rdv(IC(4 + i));
                        if constexpr (u == 18 && i >= 3 && i < 15 && (i & 1)) put((i - 3) / 2);
                        if constexpr (u == 18 && i < 6) lw128(lwv + (10 + i) * 4096, __builtin_bit_cast(u32x4, wt[10 + i])); }
                    if constexpr (i < e) X.op<i / 3, i % 3>(k);
                    else if constexpr (u > 0 && u < 18 && (i == e || ((i - e - 1) * 6) / (nn - e) != j)) {
                        if constexpr (d < 10) dma1<SLOT(d) + (j / 3) * 12288 + (j % 3) * 1024>(p.Wv + (size_t)(2 * d + j / 3) * 32 * 6144 + h * 192, wsrc<384, true>(w * 192 + (j % 3) * 64 + l, 12288), w * 3072);
                        else if constexpr (d < 16) dma1<SLOT(d) + j * 1024>(wo + (d - 10) * 32 * 640, wsrc<768, false>(w * 384 + j * 64 + l, 1280), w * 6144);
                        else dma1<SLOT(d) + j * 1024>(wo + (d - 16) * 48 * 640 + 384, wsrc<512, false>(w * 384 + j * 64 + l, 1280), w * 6144);
                    }
                    if constexpr (i < 2 && u >= 3 && u < 8) lw128(lwv + (2 * u - 6 + i) * 4096, __builtin_bit_cast(u32x4, wt[(2 * u - 6 + i) & 15]));
                    sfor<2>([&](auto C) { constexpr int c = u == 19 ? 2 * i + CV(C) : i - r0 + CV(C) * 99;
                        if constexpr (c >= 0 && c < 12) {
                            constexpr int t = u < 10 ? c >> 2 : u < 16 ? c >> 1 : 6 + c % 4, r = u < 10 ? c & 3 : u < 16 ? (u - 10) * 2 + (c & 1) : (u - 16) * 3 + c / 4, id = u < 10 ? (t * 10 + u) % 52 : OIDX(t, r >> 2);
                            q[t][r & 3] = lrtr(u < 10 ? rb[t] + (r >> 1) * 12288 + (r & 1) * 1536 : u < 16 ? rb[t] + (c & 1) * 12288 : rb[(t - 6) & 3] + (c / 4) * 8192);
                            if constexpr (u < 10 && u >= 2 && t == 2) { if constexpr (r & 1) V.wl[2 * u + r / 2] = wt[(2 * u - 4 + r / 2) & 15] = CAT4(q[t][r - 1], q[t][r]); }
                            else if constexpr ((r & 3) == 1) W[id] = __builtin_shufflevector(q[t][0], q[t][1], 0, 1, 2, 3, 4, 5, 6, 7, -1, -1, -1, -1, -1, -1, -1, -1);
                            else if constexpr ((r & 3) == 3) W[id] = CAT8(LO8(W[id]), CAT4(q[t][2], q[t][3]));
                        } });
                    SCHED0(); });
                if constexpr (u == VGS + 7) sfor<18>([&](auto I) { V.out<CV(I), 0>(l16 + L_V + ((3 * w) >> 1) * 1024 + ((3 * w) & 1) * 8, l16 + L_V + ((3 * w + 1) >> 1) * 1024 + ((3 * w + 1) & 1) * 8); }); });
        static_assert(VGS >= 6 && VGS < 9, "VGS");
        RAWBAR()
        XSTAMP(20)
        sfor<2>([&](auto B) { X.op<CV(B) + 3, 0>(k); asm volatile("s_nop 0"); X.op<CV(B) + 3, 1>(k); X.op<CV(B) + 3, 2>(k); });
        asm volatile("" : "+v"(z[8][0]), "+v"(z[8][1]), "+v"(z[7][0]), "+v"(z[6][1]));
        put(6); put(8);
        lw32(l16 + L_MEM, ~0u);
        o[7] = (u32x4){cvtpk(z[7][0][0], z[7][0][1]), cvtpk(z[7][0][2], z[7][0][3]), 0u, 0u};
        y[17] = z[7][1];
        yb += 7 * 65536;
        X.off = 0; X.l16 = l16;
        if (k.c) X.init(k);
    }
    RSTAMP(2)
    const unsigned pdum = k.pg(1) + DUMMY_B, pbc = w == 0 ? k.p0 + CNT(C_BC, (h >> 3) * 16 + 8 + (h & 7)) : pdum + EXP(49152 + h * 512 + w * 128), pby = k.p0 + YLINE(w, h);
    unsigned long long now = 0, done = 0, tpa = 0, tpb = 0;
    G.mx = ~0u;
    G.init(k);
    G.base -= 32768;
    const unsigned ypar = (unsigned)(k.par >> (PG_YX + (w >> 1))), yo = ((unsigned)(PG_YX + (w >> 1)) << 21) + (w & 1) * 1048576 + EXP(h * 1024);
    const unsigned ym0 = k.ym(0) + w * 163840, ym2 = k.ym(2) + w * 163840;
    unsigned ym = k.c ? pdum + w * 163840 : ym0, ys, t, c = ~0u, an = 80;
#define YT(u) ((u) < 10 ? 2 * ((u) % 5) : ((u) - 10) | 1)
#define YR(u) ((u) < 10 ? (u) / 5 : (u) & 1)
    auto yout = [&](auto U, auto J) { constexpr int u = CV(U), j = CV(J), t = YT(u), rt = YR(u);
        if constexpr (j == 0) { o[t][2 * rt] = cvtpk(y[u][0], y[u][1]); o[t][2 * rt + 1] = cvtpk(y[u][2], y[u][3]); TOUCH(o[t][2 * rt]); TOUCH(o[t][2 * rt + 1]); }
        if constexpr (j == 1 && rt) __builtin_amdgcn_raw_buffer_store_b128(o[t], k.rs, (int)l16, (int)yb, AUX_SC1);
        if constexpr (j == 2 && rt) { yb += t == 8 ? -7 * 65536 : 131072; PINS(yb); TOUCH(o[t]); } };
    auto ymm = [&](auto I) {
        constexpr int i = CV(I), ks = i % 6, u = i / 6, id = OIDX(YT(u), ks >> 1);
        mfw<id, ks & 1, true, ks == 0>(y[u], W[id], v[YR(u) * 6 + ks]);
        SCHED0();
        if constexpr (u > 0 && ks >= 2) yout(IC((u + 19) % 20), IC(ks - 2)); };
    if (k.c) X.late<5>(k, l16);
    do {
        PROF_S(X.sg - 1)
        f32x4 ac[3];
        sfor<13>([&](auto I) { constexpr int i = CV(I) + 107;
            ymm(IC(i));
            TPV(i - 107)
            if constexpr (i == TB) { asm volatile("s_waitcnt vmcnt(%0)\n\ts_barrier" :: "n"(TB < 111 ? 7 : 8) : "memory"); AST(5) }
            if constexpr (i == TB + 1) st32(k.rs, 0, pby, G.mx);
            if constexpr (i == 119) V.pre<true, 0>();
            if constexpr (i == 113) G.ask<0>(k, l16);
            if constexpr (i == 119) G.ask<1>(k, l16);
            if constexpr (i == 114) V.r0<0>();
            if constexpr (i == 115) V.r1<0>();
            if constexpr (i == 117) V.r0<1>();
            if constexpr (i == 118) V.r1<1>();
            SCHED0(); });
        HSTAMP(s, 4)
        V.run<0, true, 0, 16, 17, true>(W, [&](auto I) { constexpr int i = CV(I);
            TPV(i + 13)
            if constexpr (i >= 2) yout(IC(19), IC(i - 2));
            if constexpr (i == 8) { t = (X.sg & 7) * 2; PINS(t); }
            if constexpr (i == 10) { yb = (ypar >> t) & 1; PINS(yb); }
            if constexpr (i == 14) { t = (t << 21) + yo; PINS(t); }
            if constexpr (i == 16) { yb = (yb << 13) + t; PINS(yb); }
            if constexpr (i == 20) { c = X.sg; PINS(c); }
            if constexpr (i == 22) { G.a = max(c, 1u) << 15; PINS(G.a); }
            if constexpr (i == 28) G.op<1>(k, l16, 0);
            if constexpr (i == 40) { an = min(an, 200u) + DV; PINS(an); }
            if constexpr (i == 32 || i == 34 || i == 38) X.next<(i - 31) / 3>(k, l16);
            if constexpr (i == 23 || i == 29) { AAC(4 + i / 29) }
            if constexpr (i == 83 || i == 89) { AAC(6 + i / 89) }
            if constexpr (i == YPOS) { AST(6) }
            if constexpr (i == YPOS + 1) { AST(7) }
            if constexpr (i == YPOS) DRAIN();
            if constexpr (i == YB) asm volatile("s_barrier" ::: "memory");
            if constexpr (i == YPOS - 2) G.mx = vshl1(c, 0);
            if constexpr (i == YPOS + 1) st32(k.rs, 0, pby, G.mx);
            SCHED0();
            G.red<i - YPOS - 2, -1>();
            G.hold<i - GPOSY * 6 - GVS + 5>(c);
            G.at<i - GPOSY * 6 - GVS>(k, l16);
            if constexpr (i == 90) PINS(G.rt);
            if constexpr (i == 95) V.pre<true, 16>();
            if constexpr (i >= 92 && i < 95) clr2(ac[(i - 92) % 3]); });
        static_assert(GPOSY * 6 + 2 > YPOS + 1 && GPOSY <= 13 && YB >= YPOS && YB < 60, "GPOSY");
        HSTAMP(s, 5)
        HFLUSH(s - 1)
        f32x4 acc[10];
        f32x2 z[20];
        auto clr = [&](auto I) { constexpr int i = CV(I); asm volatile("v_mov_b64 %0, 0" : "=v"(z[i])); if constexpr (i & 1) acc[i >> 1] = __builtin_shufflevector(z[i - 1], z[i], 0, 1, 2, 3); };
        bf16x4 al[3], ah[3];
        unsigned d0, d1;
        {
            bf16x8 m[10], wa[10], wb[10];
            auto rd = [&](auto R) { constexpr int r = CV(R), b = 2 + r / 3;
                if constexpr (b < 10) {
                    if constexpr (r % 3 == 0) m[b] = lr128(l16 + MADR(b));
                    if constexpr (r % 3 == 1) wa[b] = lr128(lwv + (b - 2) * 8192);
                    if constexpr (r % 3 == 2) wb[b] = lr128(lwv + (b - 2) * 8192 + 4096);
                } else if constexpr (b < 13) {
                    if constexpr (r % 3 == 0) V.r0<b + 7>();
                    if constexpr (r % 3 == 1) V.rw<b + 7>();
                    if constexpr (r % 3 == 2) V.r1<b + 7>();
                } };
            AST(0)
            G.wait<0>();
            HSTOP(0) HSTAMP(s, 0) AST(1)
            V.run<0, true, 16, 17, 0, true>(W, [&](auto I) { constexpr int i = CV(I);
                TPV(i + 109)
                G.fin<i>(0);
                if constexpr (i == 3) {
                    if (__builtin_expect(G.bad != 0, 0)) {
                        unsigned spins = 0;
_Pragma("unroll 1")
                        do { PC8(0, c) G.again(k, l16); } while (G.bad != 0 && !spin_fail(k, spins++));
                        HSTOP(0) HSTAMP(s, 0)
                    }
                    asm volatile("s_barrier" ::: "memory");
                    AST(2)
                }
                if constexpr (i == 4) { now = rt_async(); HSTOP(1) SCHED0(); }
                if constexpr (i >= 3 + VS) rd(IC(i - 3 - VS)); });
            m[0] = __builtin_bit_cast(bf16x8, G.p0); m[1] = __builtin_bit_cast(bf16x8, G.p1);
            sfor<30>([&](auto I) {
                constexpr int i = CV(I), b = i / 3, t = i % 3;
                if constexpr (t == 2 && b >= 2) smf<false>(ac[2], m[b], CAT8(wa[b], wb[b]), ix); else smfw<t * 10 + b>(ac[t], m[b], W[t * 10 + b], ix);
                SCHED0();
                TPV(i + 115)
                if constexpr (i >= 5 && i < 29 && t != 0) { if constexpr (t == 1) TOUCH(wb[b]); else TOUCH(m[(b + 1) % 10]); }
                if constexpr (i == 29) V.pre<true, 17>();
                if constexpr (i == 2) { PINS(now); an += (unsigned)now; PINS(an); }
                if constexpr (i == 3) an = spack(an, c);
                SCHED0();
                if constexpr (i < 27 + VS) rd(IC(3 - VS + i));
                if constexpr (i == 5) st32(k.rs, 0, pbc, d0);
                SCHED0();
                if constexpr (i == 4) { d0 = an; PIN(d0); }
                if constexpr (i == 28) d0 = vadd(l16, L_K + ((3 * w) >> 1) * 1024 + ((3 * w) & 1) * 8);
                if constexpr (i == 29) d1 = vadd(l16, L_K + ((3 * w + 1) >> 1) * 1024 + ((3 * w + 1) & 1) * 8);
                SCHED0();
            });
            unsigned fo[3];
            V.run<0, true, 17, 19, 0, true>(W, [&](auto I) { constexpr int i = CV(I);
                TPV(i + 145)
                if constexpr (i < 3) { rd(IC(30 + i)); SCHED0(); fold_op<0>(ac[i], fo[i]); }
                if constexpr (i == 3 || i == 4 || i == 6) { constexpr int t = i / 2 - 1; asm volatile("v_add_f32 %1, %1, %2\n\tv_cvt_pk_bf16_f32 %0, %3, %1" : "=v"(fo[t]), "+v"(ac[t][1]) : "v"(ac[t][3]), "v"(ac[t][0])); }
                if constexpr (i == 5 || i == 7 || i == 8) lw32(dq0 + ((i - 4) / 2) * 256, fo[(i - 4) / 2]);
                if constexpr (i == 9 || i == 10) clr2(acc[i - 9]);
                if constexpr (i == 11) BARL(0) });
        }
        HSTAMP(s, 1) AST(3)
        {
            unsigned fo[2];
            auto xq = [&](auto I) { constexpr int i = CV(I) - XP + XS0;
                if constexpr (i == XA) X.next<3>(k, l16);
                if constexpr (i == -1 || i == 0) X.next<14 + i>(k, l16); };
            static_assert(XP % 6 == 5 && XP <= 53 && XP >= 17 && XB > XP + 18 && XB > 63, "XP");
            auto fin2 = [&](auto P, auto J) { fold2<CV(J)>(acc[2 * CV(P)], acc[2 * CV(P) + 1], fo); };
            V.run<0, true, 19, 20, 0, true>(W, [&](auto I) { constexpr int i = CV(I);
                TPV(i + 157)
                if constexpr (i == 0) { al[0] = lrtr(trp); ah[0] = lrtr(trp + 64); }
                if constexpr (i == 1) al[1] = lrtr(trp + 1024);
                if constexpr (i == 2) ah[1] = lrtr(trp + 1088);
                if constexpr (i == 3) al[2] = lrtr(trp + 2048);
                if constexpr (i == 4) ah[2] = lrtr(trp + 2112);
                SCHED0();
                if constexpr (i >= 1) clr(IC(3 + i)); });
            sfor<30>([&](auto I) { constexpr int i = CV(I), b = (i % 6) >> 1, t = 2 * (i / 6) + (i & 1), id = OIDX(t, b);
                smfw<id>(acc[t], CAT4(al[b], ah[b]), W[id], ix);
                SCHED0();
                TPV(i + 163)
                if constexpr (i == 1 || i == 3) TOUCH(ah[(i + 1) / 2]);
                if constexpr (i == 29) TOUCH(v[0]);
                if constexpr (i == 21) BARL(0)
                SCHED0();
                if constexpr (i == 4 || i == 7) X.next<(i == 4 ? 5 : 11)>(k, l16);
                xq(IC(i - 30));
                if constexpr (i >= 22 && i < 28) v[i - 22] = lr128(l16 + L_K + (i - 22) * 1024);
                SCHED0();
                V.out<i - 1, 0>(d0, d1);
                SCHED0();
                if constexpr (i % 3 == 0 && i < 18) clr(IC(9 + i / 3));
                if constexpr (i == 16) clr(IC(15));
                if constexpr (i >= 18 && i < 21) clr(IC(i - 2));
                if constexpr (i == 22) clr(IC(19));
                if constexpr (i >= 23 && i < 29) fold_op<(i - 23) / 2>(acc[(i - 23) & 1], fo[(i - 23) & 1]);
                if constexpr (i == 29) { st64(k.rs, vo, ym, (u32x2){fo[0], fo[1]}); SCHED0(); fin2(IC(1), IC(0)); }
                SCHED0(); });
            HSTAMP(s, 2)
            sfor<107>([&](auto I) { constexpr int i = CV(I), q = i < 8 ? i + 4 : i + 3;
                ymm(I);
                TPV(i + 193)
                if constexpr (i < 5 || (i >= 29 && i < 35)) TOUCH(v[(i < 5 ? i + 1 : i - 23) % 12]);
                if constexpr (i < 13 && i != 8) {
                    if constexpr (q % 3 == 1) { ym += 32768; PINS(ym); }
                    if constexpr (q % 3 == 2 && i >= 5) __builtin_amdgcn_s_setprio(0);
                    SCHED0();
                    if constexpr (q % 3 == 0) { st64(k.rs, vo, ym, (u32x2){fo[0], fo[1]}); SCHED0(); }
                    if constexpr (q < 15) fin2(IC(q / 3), IC(q % 3));
                }
                if constexpr (i == 13) done = rt_async();
                if constexpr (i >= 15 && i < 23 && i != 18 && i != 20) v[6 + i - 15 - (i > 18) - (i > 20)] = lr128(l16 + L_K + (6 + i - 15 - (i > 18) - (i > 20)) * 1024);
                xq(IC(i));
                if constexpr (i == XP - 6 + (XP == 23)) X.next<15>(k, l16);
                if constexpr (i == XP - 1) { SCHED0(); X.next<7>(k, l16); }
                if constexpr (i == XP) X.next<12>(k, l16);
                if constexpr (i > XP && i <= XP + 18 && (i % 6 == 0 || i % 3 == 1)) X.op<(i - XP - 1) / 6, (i % 6 + 2) / 3>(k);
                { constexpr int xp[6] = {XB, XB + 1, XB + 6, XB + 11, XB + 12, XB + 18};
                  sfor<6>([&](auto Q) { if constexpr (xp[CV(Q)] == i) X.op<3 + CV(Q) / 3, CV(Q) % 3>(k); }); }
                if constexpr (i == 90) { ym = (c & 2) ? ym2 : ym0; PINS(ym); }
                if constexpr (i == 91) { ys = ym + EXP(327680); PINS(ys); }
                if constexpr (i == 96) { ym = (c & 1) ? ys : ym; PINS(ym); }
                if constexpr (i == 60) { HSTAMP(s, 3) }
                if constexpr (i == 42 || i == 43 || i == 48 || i == 49) { AAC((i - 42) / 3 + (i & 1)) }
                if constexpr (i == 102) lgkm0(done);
                if constexpr (i == 103) { PINS(now); HTRACE(c - 1) an = (unsigned)done - (unsigned)now; PINS(an); }
                if constexpr (i == 105) G.mx = vshl1(c, 1);
                SCHED0(); });
            AST(4)
        }
    } while (X.sg != 65u);
    HFLUSH(63)
    PROF_OUT
}

DEVI void row_role(const Params& p, const Ctx& k, char* smem, int rt) {
    PROF_DECL
    const int l = k.l, n = l & 15;
    const unsigned fo = k.w * 2048 + rt * 1024;
    RSTAMP(2)
    const __amdgpu_buffer_rsrc_t rs = __builtin_amdgcn_make_buffer_rsrc(k.c ? (void*)k.ws : (void*)p.x, 0, 0x7fffffff, 0x00020000);
    const unsigned vs = k.c ? l * 16 : n * 1280 + (l >> 4) * 16, cs = k.c ? 16384 : 256;
    unsigned ep = n * 32, dp = (l >> 4) == (n >> 2) ? n * 32 + k.w * 8 : 1024 + l * 8;
    const unsigned long long m1 = __ballot(n & 1), m2 = __ballot(n & 2);
    bf16x8 one8 = {0x3F80, 0x3F80, 0x3F80, 0x3F80, 0x3F80, 0x3F80, 0x3F80, 0x3F80};
    PIN(one8);
    unsigned fails = k.c ? 0u : 65536u;
    prog_init(k);
    for (int sg = k.c ? 0 : 8; sg < 64; ++sg) {
        unsigned src[5], dst = k.seg(PG_XNA, sg) + fo;
#pragma unroll
        for (int c = 0; c < 5; ++c) { src[c] = (k.c ? k.seg(PG_X2, sg) + fo : (sg * 32 + rt * 16) * 1280 + k.w * 64) + c * cs; PINS(src[c]); }
        PINS(dst);
        prog_wait<5>(k, k.p0 + CNT(C_X2, rt * 5), k.c ? sg : -1);
        STOP(sg) STAMP(sg, 0)
        u32x4 a[5];
        f32x4 sm, sq;
        f32x2 v[20];
        unsigned long long bad;
        do {
            asm volatile("" ::: "memory");
#pragma unroll
            for (int c = 0; c < 5; ++c) { a[c] = ld128(rs, vs, src[c]); SCHED0(); }
            unsigned mx = 0, lim = ~1u | (fails >> 16);
            ++fails; PINS(fails); PINS(lim);
            sfor<5>([&](auto C) { constexpr int c = CV(C);
                mfa<false, c == 0>(sq, __builtin_bit_cast(bf16x8, a[c]), __builtin_bit_cast(bf16x8, a[c])); mfa<false, c == 0>(sm, one8, __builtin_bit_cast(bf16x8, a[c]));
                mx = umax3(umax3(mx, a[c][0], a[c][1]), a[c][2], a[c][3]); PIN(mx);
                SCHED0(); });
            bad = __ballot(mx > lim);
        } while (__builtin_expect(bad != 0, 0));
        STAMP(sg, 1)
        auto unp = [&](int i0, int i1) {
#pragma unroll
            for (int i = i0; i < i1; ++i) { v[i] = (f32x2){blo(a[i >> 2][i & 3]), bhi(a[i >> 2][i & 3])}; PIN(v[i]); }
            SCHED0(); };
        unp(0, 3);
        asm volatile("" : "+v"(sq), "+v"(sm));
        lw64(dp, (u32x2){__float_as_uint(sm[0]), __float_as_uint(csel(csel(sq[0], sq[1], m1), csel(sq[2], sq[3], m1), m2))});
        SCHED0();
        unp(3, 10);
        RAWBAR()
        const f32x4 e0 = __builtin_bit_cast(f32x4, lr128(ep)), e1 = __builtin_bit_cast(f32x4, lr128(ep + 16));
        SCHED0();
        unp(10, 20);
        const f32x4 e = e0 + e1;
        float m = (e[0] + e[2]) * (1.f / 640.f);
        const float var = bfr(__builtin_fmaxf(__builtin_fmaf(-m, m, (e[1] + e[3]) * (1.f / 640.f)), 0.f));
        m = bfr(m);
        const float rden = __builtin_amdgcn_rcpf(bfr(__builtin_amdgcn_sqrtf(bfr(var + 1e-5f))));
#pragma unroll
        for (int c = 0; c < 5; ++c) {
            u32x4 o;
#pragma unroll
            for (int i = 0; i < 4; ++i) { const f32x2 x = v[c * 4 + i] - m; o[i] = cvtpk(bfr(x[0]) * rden, bfr(x[1]) * rden); }
            st128(k.rs, l * 16, dst + c * 16384, o);
        }
        STAMP(sg, 2)
        unsigned fv = sg - (k.c ? 0 : 8);
        PIN(fv);
        DRAIN();
        st32(k.rs, l == 0 ? 0u : OOB, k.p0 + CNT(C_XN, rt * 4 + k.w), fv);
        dp ^= 2048; ep ^= 2048;
        STAMP(sg, 3) STAMP(sg, 4) STAMP(sg, 5)
        PFLUSH(sg)
    }
    PROF_OUT
}

DEVI int red_col(int j) { const int w = j / 5, q = j % 5; return q < 3 ? 3 * w + q : 9 + 2 * w + q; }
struct Red {
    u32x4 Y[16];
    template <int I0, int I1> DEVI void yload(const Ctx& k, int j, int sg) {
        const unsigned a = k.yx(sg, j / 5) + (j % 5) * 131072 + k.w * 16384;
#pragma unroll
        for (int i = I0; i < I1; ++i) Y[i] = ld128(k.rs, k.l * 16 + (i & 3) * 1024, a + ((i >> 2) & 1) * 4096 + (i >> 3) * 65536);
    }
};
DEVI unsigned y_get(const Ctx& k) { return ld32(k.rs, (k.l & 7) * 128, k.p0 + YLINE((k.role >> 5) & 3, k.w * 8)); }
DEVI void y_wait(const Ctx& k, int s) {
_Pragma("unroll 1")
    for (unsigned spins = 0; !__all((int)y_get(k) >= s); ++spins) {
        asm volatile("" ::: "memory");
        __builtin_amdgcn_s_sleep(YS);
        if (spin_fail(k, spins)) break;
    }
    __builtin_amdgcn_fence(__ATOMIC_ACQUIRE, "wavefront");
}
DEVI void wait_until(unsigned t) { while ((int)((rclk() - t) << 16) < 0) { if (WUS) __builtin_amdgcn_s_sleep(WUS); } }
#define DPPMAX(x, c) x = max(x, __builtin_amdgcn_update_dpp(0, x, c, 0xf, 0xf, false))
#define TSLP 16
#define TWU 0
DEVI void wait_until2(unsigned t) { while ((int)((rclk() - t) << 16) < 0) { if (TWU) __builtin_amdgcn_s_sleep(TWU); } }
#define WAIT_BC(s, BSLP) { \
    unsigned bc; \
    _Pragma("unroll 1") for (;; ++spins) { \
        bc = ld32(k.rs, (l & 15) * 128, k.p0 + CNT(C_BC, w * 16)); \
        DRAIN(); \
        if (__all(((int)bc >> 16) > s)) break; \
        __builtin_amdgcn_s_sleep(BSLP); \
        if (spin_fail(k, spins)) break; \
    } \
    const unsigned now = rclk(); \
    int d = (int)((bc - now) << 16); \
    d = ((int)bc >> 16) > s + 1 || d > (256 << 16) ? -(64 << 16) : d; \
    DPPMAX(d, 0xB1); DPPMAX(d, 0x4E); DPPMAX(d, 0x141); \
    tq = now + (__builtin_amdgcn_readlane(d, 0) >> 16); tv = now + (__builtin_amdgcn_readlane(d, 8) >> 16); }
DEVI void mred_role(const Params& p, const Ctx& k, char* smem, int j) {
    PROF_DECL
    const int l = k.l, w = k.w, g = l >> 4, n = l & 15;
    const unsigned ix = SPIX(l);
    const unsigned vp = (g * 16 + ((n >> 1) & 1) * 8 + (n >> 2) * 2 + (n & 1)) * 16;
    const unsigned v16 = (g >> 1) * 1024 + (g & 1) * 256 + n * 16, yo = j * 32768 + EXP(w * 4096);
    const u32x4 ones = {0xFFFFFFFFu, 0xFFFFFFFFu, 0xFFFFFFFFu, 0xFFFFFFFFu};
    auto arm = [&](int s) {
#pragma unroll
        for (int i = 0; i < 4; ++i) st128(k.rs, l * 16 + i * 1024, k.ym(s) + yo, ones);
    };
    const unsigned pv = l == 0 && j % 5 == 0 ? (j / 5) * 128 + w * 32 : OOB;
    Red R;
    RSTAMP(2)
    arm(1);
#pragma unroll
    for (int i = 0; i < 8; ++i) st128(k.rs, (i || w > 1 || g > 1) ? g * 32768 + n * 16 : OOB, k.memf(i * 8 + (w >> 1) * 4) + EXP((2 * j + (w & 1)) * 256), ones);
#pragma unroll
    for (int i = 0; i < 4; ++i) { const unsigned q = min(i * 80 + j * 4 + w, 287); st128(k.rs, l * 16, k.p0 + PR_B + PR_SZ + EXP(q * 1024), ones); }
    sleep_until(k.t0 + RSL);
    y_wait(k, 2);
    R.yload<0, 16>(k, j, 0);
    for (int s = 0; s < 63; ++s) {
        STOP(s) HSTAMP(s, 0)
        const unsigned ap = k.p0 + PR_B + (s & 3) * PR_SZ + w * 49152, am = k.ym(s) + yo;
        const unsigned ph = (s & 4) ? 0x80008000u : 0u;
        unsigned spins = 0, tq, tv;
        unsigned fl;
        WAIT_BC(s, BSLP)
        st32(k.rs, pv, k.p0 + CNT(C_XB, 0), spack(((int)((tq - tv) << 16) < 0 ? tv : tq) + RDY, s + 2));
        STAMP(s, 5)
        char* rb = smem + (s & 1) * 4096;
_Pragma("unroll 1")
        for (spins = 0;; ++spins) {
            u32x4 ym[4], pm[5];
            f32x4 acc[2];
            if (spins == 0) wait_until(tv);
            asm volatile("" ::: "memory");
#pragma unroll
            for (int i = 0; i < 4; ++i) ym[i] = ld128(k.rs, v16 + (i >> 1) * 2048 + (i & 1) * 512, am);
            fl = ld32(k.rs, 0, (k.p0 ^ 8192u) + CNT(C_X1, j));
            asm volatile("" ::: "memory");
            if (spins == 0) { wait_until(tq); STAMP(s, 1) }
            asm volatile("" ::: "memory");
#pragma unroll
            for (int i = 0; i < 5; ++i) pm[i] = ld128(k.rs, i < 4 ? l * 16 + i * 1024 : vp, ap + (i >> 2) * 4096);
            acc[0] = (f32x4){0.f, 0.f, 0.f, 0.f}; acc[1] = acc[0];
            const u32x8 y0 = {ym[0][0], ym[1][0], ym[0][2], ym[1][2], ym[2][0], ym[3][0], ym[2][2], ym[3][2]};
            const u32x8 y1 = {ym[0][1], ym[1][1], ym[0][3], ym[1][3], ym[2][1], ym[3][1], ym[2][3], ym[3][3]};
            unsigned mx = 0;
#pragma unroll
            for (int i = 0; i < 4; ++i) mx = max(mx, umax4(ym[i]));
            unsigned tg = mx == 0xFFFFFFFFu ? 0x80000000u : 0u;
            asm volatile("s_nop 1" : "+v"(acc[0]), "+v"(acc[1]), "+v"(tg) : "v"(y0), "v"(y1), "v"(ix));
            SCHED0();
#pragma unroll
            for (int i = 0; i < 5; ++i) {
                const unsigned u0 = __builtin_amdgcn_bitop3_b32(pm[i][0], pm[i][1], ph, 0x7e), u1 = __builtin_amdgcn_bitop3_b32(pm[i][2], pm[i][3], ph, 0x7e);
                SCHED0();
                if (i < 4) smf<false>(acc[0], __builtin_bit_cast(bf16x8, pm[i]), __builtin_bit_cast(bf16x16, __builtin_shufflevector(R.Y[2 * i], R.Y[2 * i + 1], 0, 1, 2, 3, 4, 5, 6, 7)), ix);
                else smf<false>(acc[0], __builtin_bit_cast(bf16x8, pm[4]), __builtin_bit_cast(bf16x16, y0), ix);
                tg = (tg | u0) | u1;
                SCHED0();
                if (i < 4) smf<false>(acc[1], __builtin_bit_cast(bf16x8, pm[i]), __builtin_bit_cast(bf16x16, __builtin_shufflevector(R.Y[8 + 2 * i], R.Y[9 + 2 * i], 0, 1, 2, 3, 4, 5, 6, 7)), ix);
                else smf<false>(acc[1], __builtin_bit_cast(bf16x8, pm[4]), __builtin_bit_cast(bf16x16, y1), ix);
                SCHED0();
            }
            const unsigned long long bad = __ballot((int)tg < 0);
            asm volatile("s_nop 5" : "+v"(acc[0]), "+v"(acc[1]) : "s"(bad));
#pragma unroll
            for (int t = 0; t < 2; ++t) { float* rp = (float*)(rb + t * 2048 + w * 512 + g * 128 + n * 4); rp[0] = acc[t][0] + acc[t][2]; rp[16] = acc[t][1] + acc[t][3]; }
            if (bad == 0) break;
            PCOUNT(0)
            asm volatile("" ::: "memory");
            if (spin_fail(k, spins)) break;
        }
        STAMP(s, 2)
        RAWBAR()
        if (w < 2) {
            const char* rp = rb + w * 2048 + ((l >> 4) * 2 + ((l >> 2) & 1)) * 64 + ((l & 3) * 4 + ((l >> 3) & 1) * 2) * 4;
            const f32x2 a = *(const f32x2*)rp, b = *(const f32x2*)(rp + 512), c = *(const f32x2*)(rp + 1024), d = *(const f32x2*)(rp + 1536);
            const f32x2 r = (a + b) + (c + d);
            st32(k.rs, l * 4, k.memf(s + 1) + EXP((2 * red_col(j) + w) * 256), min(cvtpk(r[0], r[1]) ^ ph, 0xFFFFFFFEu));
        }
        STAMP(s, 3)
_Pragma("unroll 1")
        for (spins = 0; (int)__builtin_amdgcn_readfirstlane(fl) < s - 1; ++spins) {
            PCOUNT(1)
            __builtin_amdgcn_s_sleep(PSLEEP);
            if (spin_fail(k, spins)) break;
            fl = ld32(k.rs, 0, (k.p0 ^ 8192u) + CNT(C_X1, j));
        }
        __builtin_amdgcn_fence(__ATOMIC_ACQUIRE, "wavefront");
        if (Y1) wait_until(tv + Y1);
        y_wait(k, 2 * s + 3);
        R.yload<0, 8>(k, j, s + 1);
        if (ARMP == 0) arm(s + 2);
        if (Y2) wait_until(tv + Y2);
        y_wait(k, 2 * s + 4);
        STAMP(s, 4)
        if (ARMP == 2) { arm(s + 2); SCHED0(); }
        R.yload<8, 16>(k, j, s + 1);
        if (ARMP == 1) { SCHED0(); arm(s + 2); }
        PFLUSH(s)
    }
    PFLUSH(63)
    PROF_OUT
}
DEVI void tred_role(const Params& p, const Ctx& k, char* smem, int j) {
    PROF_DECL
    const int l = k.l, w = k.w, g = l >> 4, n = l & 15, T = 2 * red_col(j) + (w >> 1), rt = w & 1;
    const unsigned v16 = (g >> 1) * 1024 + (g & 1) * 256 + n * 16, yo = j * 32768 + EXP(w * 4096);
    const unsigned xo = EXP((T >> 1) * 2048) + (T & 1) * 512 + rt * 1024, xv = ((g >> 1) * 16 + n) * 16 + (g & 1) * 8;
    const u32x2 ones = {0xFFFFFFFFu, 0xFFFFFFFFu};
    const unsigned fl = (w == 0 && l == 0) ? 0u : OOB;
    unsigned fails = 0;
    Red R;
    RSTAMP(2)
    st64(k.rs, xv, k.segx(PG_X1, 0) + xo, ones); st64(k.rs, xv, k.segx(PG_X1, 1) + xo, ones);
    sleep_until(k.t0 + RSL);
    y_wait(k, 2);
    R.yload<0, 16>(k, j, 0);
    for (int s = 0; s < 64; ++s) {
        STOP(s) STAMP(s, 0)
        unsigned ap[4], am = k.ym(s) + yo, xa = k.segx(PG_X1, s) + xo, rb = (s & 1) * 16384 + l * 16, wb = rb + w * 1024, fv = s;
#pragma unroll
        for (int i = 0; i < 4; ++i) { ap[i] = k.p0 + PR_B + (s & 3) * PR_SZ + w * 49152 + (i < 2 ? 32768 : 16384) + (~i & 1) * 4096; PINS(ap[i]); }
        rb += w * 4096;
        PINS(am); PINS(xa); PIN(rb); PIN(wb); PIN(fv);
        const unsigned ph = (s & 4) ? 0x80008000u : 0u;
        unsigned spins = 0, tq, tv;
        u32x2 res;
        if (k.c) res = ld64(k.rs, xv, k.seg(PG_X2, s) + xo);
        else res = *(const u32x2*)(p.x + (size_t)(s * 32 + rt * 16 + n) * 640 + T * 16 + g * 4);
        f32x4 acc[2][2];
        WAIT_BC(s, TSLP)
        u32x4 yl[8];
#pragma unroll
        for (int i = 0; i < 8; ++i) { yl[i] = __builtin_shufflevector(R.Y[2 * i], R.Y[2 * i + 1], 0, 1, 4, 5); PIN(yl[i]); }
        f32x4 x = {blo(res[0] ^ ph), bhi(res[0] ^ ph), blo(res[1] ^ ph), bhi(res[1] ^ ph)};
        PIN(x);
        wait_until2(tv); wait_until2(tq + (s < 63 ? RDLY : 0));
        STAMP(s, 1)
        unsigned long long bad;
        do {
            asm volatile("" ::: "memory");
            u32x4 ym[4], pp[16];
#pragma unroll
            for (int i = 0; i < 4; ++i) { ym[i] = ld128(k.rs, v16 + (i >> 1) * 2048 + (i & 1) * 512, am); SCHED0(); }
#pragma unroll
            for (int i = 0; i < 16; ++i) { pp[i ^ 4] = ld128(k.rs, l * 16 + (i & 3) * 1024, ap[i >> 2]); SCHED0(); }
            unsigned mx = 0, tg, lim = 0x7FFFFFFFu | (fails << 15);
            ++fails; PINS(fails); PINS(lim);
#pragma unroll
            for (int i = 0; i < 4; ++i) mx = max(mx, umax4(ym[i]));
            tg = mx == 0xFFFFFFFFu ? 0x80000000u : 0u;
            u32x4 yf[4];
#pragma unroll
            for (int i = 0; i < 4; ++i) { const int t = i & 1, f = i >> 1; yf[i] = (u32x4){ym[2 * f][t], ym[2 * f][t + 2], ym[2 * f + 1][t], ym[2 * f + 1][t + 2]}; }
            asm volatile("s_nop 1" : "+v"(yf[0]), "+v"(yf[1]), "+v"(yf[2]), "+v"(yf[3]), "+v"(tg));
            SCHED0();
            sfor<32>([&](auto I) { constexpr int i = CV(I), t = i & 1, q = (i >> 1) ^ 4, r = q < 4 ? 0 : q < 8 ? (q >> 1) & 1 : 1;
                mfa<false, q == 4 || q == 6>(acc[t][r], __builtin_bit_cast(bf16x8, q < 4 ? yl[t * 4 + q] : q < 8 ? yf[(q & 1) * 2 + t] : R.Y[t * 8 + (q & 7)]), __builtin_bit_cast(bf16x8, pp[q]));
                SCHED0();
                tg |= __builtin_amdgcn_bitop3_b32(pp[q][2 * t], pp[q][2 * t + 1], ph, 0x7e); PIN(tg);
                if constexpr (i == 20 || i == 22) { asm volatile("" : "+v"(acc[(i - 20) / 2][0])); lw128(wb + (i - 20) * 4096, __builtin_bit_cast(u32x4, acc[(i - 20) / 2][0])); }
                if constexpr (i == TPOS) st32(k.rs, fl, (k.p0 ^ 8192u) + CNT(C_X1, j), fv);
                SCHED0(); });
            bad = __ballot(tg > lim);
        } while (__builtin_expect(bad != 0, 0));
        STAMP(s, 2)
        const unsigned yc = y_get(k);
        SCHED0();
        asm volatile("s_nop 2" : "+v"(acc[0][1]), "+v"(acc[1][1]));
        lw128(wb + 4096, __builtin_bit_cast(u32x4, acc[0][1])); lw128(wb + 12288, __builtin_bit_cast(u32x4, acc[1][1]));
        RAWBAR()
        {
            const f32x4 r0 = __builtin_bit_cast(f32x4, lr128(rb)), r1 = __builtin_bit_cast(f32x4, lr128(rb + 1024)), r2 = __builtin_bit_cast(f32x4, lr128(rb + 2048)), r3 = __builtin_bit_cast(f32x4, lr128(rb + 3072));
            SCHED0();
            if constexpr (TPOS == 32) st32(k.rs, fl, (k.p0 ^ 8192u) + CNT(C_X1, j), fv);
            SCHED0();
            const f32x4 r = (r0 + r1) + (r2 + r3);
            st64(k.rs, xv, xa, (u32x2){min(cvtpk(x[0] + bfr(r[0]), x[1] + bfr(r[1])) ^ ph, 0xFFFFFFFEu), min(cvtpk(x[2] + bfr(r[2]), x[3] + bfr(r[3])) ^ ph, 0xFFFFFFFEu)});
            SCHED0();
            if constexpr (TPOS == 33) st32(k.rs, fl, (k.p0 ^ 8192u) + CNT(C_X1, j), fv);
        }
        st64(k.rs, s < 62 ? xv : OOB, k.segx(PG_X1, (s + 2) & 63) + xo, ones);
        STAMP(s, 3)
        if (s < 63) {
            if (!__all((int)yc >= 2 * s + 4)) { PCOUNT(2) y_wait(k, 2 * s + 4); }
            __builtin_amdgcn_fence(__ATOMIC_ACQUIRE, "wavefront");
            R.yload<0, 16>(k, j, s + 1);
        }
        STAMP(s, 4)
        STAMP(s, 5)
        PFLUSH(s)
    }
    PROF_OUT
}

#undef TZ
#define TZ z0()
DEVI void up_role(const Params& p, const Ctx& k, char* smem, int id) {
    PROF_DECL
    const int l = k.l, w = k.w, g = l >> 4, n = l & 15;
    bf16x8 Wt[80];
    const int tile0 = id * 8 + w * 2;
    auto issue = [&](auto U) { constexpr int u = CV(U);
        sfor<8>([&](auto J) { constexpr int j = CV(J); dma1<(u % 3) * 32768 + j * 1024>(((w & 1) ? p.wu : p.wg) + (size_t)(2 * u + (w >> 1)) * 32 * 1536 + id * 128, wsrc<256, true>(j * 64 + l, 3072), w * 8192); }); };
    sfor<3>(issue);
    cstream<10>([](auto V) { return IC(CV(V) < 9 ? 8 : 0); },
        [&](auto U) { constexpr int u = CV(U);
            if constexpr (u > 0 && u < 8) issue(IC(u + 2));
#pragma unroll
            for (int i = 0; i < 8; ++i) Wt[(i & 3) * 20 + 2 * u + (i >> 2)] = wp_read<256, true>(smem + (u % 3) * 32768 + (i >> 1) * 8192, w * 2 + (i & 1), l); });
    RAWBAR()
    RSTAMP(2)
    bf16x8 one8 = {0x3F80, 0x3F80, 0x3F80, 0x3F80, 0x3F80, 0x3F80, 0x3F80, 0x3F80};
    asm volatile("s_nop 1" : "+v"(one8));
    f32x4 cs[4];
    sfor<80>([&](auto I) { constexpr int f = CV(I); mfa<(f < 64), f % 20 == 0>(cs[f / 20], Wt[f], one8); });
    asm volatile("s_nop 7" : "+v"(cs[0]), "+v"(cs[1]), "+v"(cs[2]), "+v"(cs[3]));
    const unsigned ho = EXP((tile0 >> 1) * 1024), l16 = l * 16;
    constexpr int L_ST = 81920;
    const u32x4 ones = {0xFFFFFFFFu, 0xFFFFFFFFu, 0xFFFFFFFFu, 0xFFFFFFFFu};
#pragma unroll
    for (int i = 0; i < 4; ++i) st128(k.rs, l16, k.hseg(i >> 1) + (i & 1) * EXP(49152) + ho, ones);
    unsigned fails = 0;
    lw32(L_ST + 1024, 0u);
    prog_init(k);
    const unsigned fl = (w == 0 && l == 0) ? 0u : OOB;
    const unsigned long long m1 = __ballot(n & 1), m2 = __ballot(n & 2);
    for (int s = 0; s < 64; ) {
        unsigned xs = k.seg(PG_X1, s) + w * 2048, xd = (s & 1) * 40960 + w * 2048;
        PINS(xs); PINS(xd);
        prog_wait<20>(k, k.p0 + CNT(C_X1, 0), s);
        STOP(s) STAMP(s, 0)
        xn_all(k, l16, xs, xd);
        const unsigned xb = (s & 1) * 40960 + l16, yb = xb + w * 2048, sp = L_ST + (s & 1) * 2048, rp = sp + n * 16;
        const unsigned dp = sp + (g == (n >> 2) ? n * 16 + w * 4 : 4096 + l * 4);
        f32x4 acc[2][4], st[4];
        bf16x8 x[20], x1[20], y0[5], y1[5];
        unsigned mx = 0;
        auto blk = [&](auto B, auto J) { constexpr int b = CV(B), j = CV(J);
            if constexpr (j == 0) asm volatile("s_waitcnt vmcnt(%0)\n\ts_barrier" :: "n"(8 - 2 * b) : "memory");
            if constexpr (j >= 1 && j < 5) x[4 * b + j - 1] = lr128(xb + (4 * b + j - 1) * 2048);
            if constexpr (j == 5) y0[b] = lr128(yb + b * 8192);
            if constexpr (j == 6) y1[b] = lr128(yb + b * 8192 + 1024); };
        sfor<7>([&](auto J) { blk(IC(0), J); });
        SCHED0();
        STAMP(s, 1)
        float mean[2], rden[2], e[4][4];
        f32x4 sa[2], sb[2];
        u32x4 bad, o[2];
        unsigned sv, fv, r, f0, f1, ha = k.hseg(s) + ho;
        PINS(ha);
        auto stat = [&](auto I) { constexpr int i = CV(I), rt = i / 8, j = i % 8;
            if constexpr (j == 0) asm volatile("v_add_f32 %0, %2, %3\n\tv_add_f32 %1, %4, %5" : "=&v"(mean[rt]), "=&v"(rden[rt]) : "v"(sa[rt][0]), "v"(sa[rt][1]), "v"(sb[rt][0]), "v"(sb[rt][1]));
            if constexpr (j == 1 || j == 2) asm volatile("v_add_f32 %0, %0, %2\n\tv_add_f32 %1, %1, %3" : "+v"(mean[rt]), "+v"(rden[rt]) : "v"(sa[rt][j + 1]), "v"(sb[rt][j + 1]));
            if constexpr (j == 3) asm volatile("v_mul_f32 %0, 0xbaccccCD, %0\n\tv_mul_f32 %1, 0x3acccccd, %1" : "+v"(mean[rt]), "+v"(rden[rt]));
            if constexpr (j == 4) asm volatile("v_fma_f32 %1, -%0, %0, %1\n\tv_cvt_pk_bf16_f32 %0, 0, %0\n\tv_max_f32 %1, 0, %1" : "+v"(mean[rt]), "+v"(rden[rt]));
            if constexpr (j == 5) asm volatile("v_cvt_pk_bf16_f32 %0, 0, %0\n\tv_add_f32 %0, 0x3727c5ac, %0\n\tv_cvt_pk_bf16_f32 %0, 0, %0" : "+v"(rden[rt]));
            if constexpr (j == 6) asm volatile("v_sqrt_f32 %0, %0" : "+v"(rden[rt]));
            if constexpr (j == 7) asm volatile("v_cvt_pk_bf16_f32 %0, 0, %0\n\tv_rcp_f32 %0, %0" : "+v"(rden[rt])); };
        auto ep = [&](auto RT, auto T, auto J) { constexpr int rt = CV(RT), t = CV(T), j = CV(J), c = (j & 1) * 2, u = j >= 20, q = u ? j - 20 : j;
            float (&a)[4] = e[t + 2 * u];
#define A2 "+v"(a[c]), "+v"(a[c + 1])
            if constexpr (j < 0 || j >= 30) { }
            else if constexpr (q < 2) asm volatile("v_fma_f32 %0, %2, %3, %5\n\tv_fma_f32 %1, %2, %4, %6" : "=&v"(a[c]), "=&v"(a[c + 1]) : "v"(mean[rt]), "v"(cs[t + 2 * u][c]), "v"(cs[t + 2 * u][c + 1]), "v"(acc[rt][t + 2 * u][c]), "v"(acc[rt][t + 2 * u][c + 1]));
            else if constexpr (q < 4) asm volatile("v_mul_f32 %0, %0, %2\n\tv_mul_f32 %1, %1, %2" : A2 : "v"(rden[rt]));
            else if constexpr (q < 6 || j == 18 || j == 19) asm volatile("v_cvt_pk_bf16_f32 %0, 0, %0\n\tv_cvt_pk_bf16_f32 %1, 0, %1" : A2);
            else if constexpr (j < 8) asm volatile("v_mul_f32 %0, 0xbfb8aa3b, %0\n\tv_mul_f32 %1, 0xbfb8aa3b, %1" : A2);
            else if constexpr (j < 12) asm volatile("v_exp_f32 %0, %0" : "+v"(a[j & 3]));
            else if constexpr (j < 14) asm volatile("v_add_f32 %0, 1.0, %0\n\tv_add_f32 %1, 1.0, %1" : A2);
            else if constexpr (j < 18) asm volatile("v_rcp_f32 %0, %0" : "+v"(a[(j - 14) & 3]));
            else if constexpr (q < 8) asm volatile("v_mul_f32 %0, %0, %2\n\tv_mul_f32 %1, %1, %3" : A2 : "v"(e[t][c]), "v"(e[t][c + 1]));
            else if constexpr (q == 8) asm volatile("v_cvt_pk_bf16_f32 %0, %0, %2\n\tv_cvt_pk_bf16_f32 %1, %1, %3" : "+v"(a[0]), "+v"(a[2]) : "v"(a[1]), "v"(a[3]));
            else { unsigned d0, d1; asm volatile("v_min_u32 %0, -2, %2\n\tv_min_u32 %1, -2, %3" : "=&v"(d0), "=&v"(d1) : "v"(a[0]), "v"(a[2])); o[rt][2 * t] = d0; o[rt][2 * t + 1] = d1; } };
        sfor<180>([&](auto I) {
            constexpr int a = CV(I), b = a / 8, j = (a % 8) ^ (b == 4 ? 4 : 0), ch = a / 20 - 1, ks = a < 40 ? 4 * b + (j & 3) : a % 20, rt = ch >= 4;
            constexpr int nt = a < 40 ? 0 : rt ? ch - 4 : (ch & 1) * 2 + (ch >> 1), f = nt * 20 + ks, i = a - 36;
            if constexpr (a >= 40 || j < 4) mfa<(f < 64), ks == 0>(acc[rt][nt], Wt[f], rt ? x1[ks] : x[ks]);
            else mfa<false, b == 0>(st[j & 3], (j & 1) ? (j & 2 ? y1[b] : y0[b]) : one8, j & 2 ? y1[b] : y0[b]);
            SCHED0();
            if constexpr (a >= 103 && a < 119) TOUCH(x1[(a - 99) % 20]);
            else if constexpr (a == 60) { r = __builtin_amdgcn_readfirstlane(bad[0]); PINS(r); }
            else if constexpr (a == 61) { r = (fails - r) >> 31; PINS(r); }
            else if constexpr (a == 62) { fails += r; PINS(fails); }
            else if constexpr (a == 63) { f0 = 2 * s - r; PINS(f0); }
            else if constexpr (a == 64) { f1 = f0 | 1; PINS(f1); }
            else if constexpr (a == 65) { r <<= 31; PINS(r); }
            else if constexpr (a >= 56 && (a < 92 || a >= 119)) __builtin_amdgcn_s_setprio(0);
            if constexpr (a < 32 && a % 8 == 7) asm volatile("" :: "v"(x[4 * b + 4]), "v"(x[4 * b + 5]), "v"(x[4 * b + 6]), "v"(x[4 * b + 7]), "v"(y0[b + 1]));
            if constexpr (a < 32 && a % 8 == 4) TOUCH(y1[b]);
            SCHED0();
            if constexpr (a == 15) lw32((rp ^ 2048) + 1024, z0());
            if constexpr (a < 32 && a % 8 < 7) blk(IC((b + 1) % 5), IC(a % 8));
            if constexpr (i == 0 || i == 4 || i == 5 || i == 8) x1[i / 2 - (i == 4 || i == 8)] = lr128(xb + 1024 + (i / 2 - (i == 4 || i == 8)) * 2048);
            if constexpr (a >= 92 && a < 108) x1[a - 88] = lr128(xb + 1024 + (a - 88) * 2048);
            SCHED0();
            if constexpr (a < 40 && j >= 4) { const u32x4 v = __builtin_bit_cast(u32x4, (j & 1) ? y1[b] : y0[b]); mx = umax3(mx, v[j & 2], v[(j & 2) + 1]); PIN(mx); }
            if constexpr (i == 1 || i == 3) {
                constexpr int rt2 = i == 3;
                asm volatile("" : "+v"(st[2 * rt2]), "+v"(st[2 * rt2 + 1]));
                const f32x4& q = st[2 * rt2 + 1];
                const float d = csel(csel(q[0], q[1], m1), csel(q[2], q[3], m1), m2);
                lw32(dp + rt2 * 512, __float_as_uint(st[2 * rt2][0])); lw32(dp + rt2 * 512 + 256, __float_as_uint(d));
            }
            if constexpr (i == 2) lw32(mx == 0xFFFFFFFFu ? sp + 1024 : dp + 4096, 4096u);
            if constexpr (i == 6) bar_lgkm<2>();
            if constexpr (i == 6) sa[0] = __builtin_bit_cast(f32x4, lr128(rp));
            if constexpr (i == 7) sb[0] = __builtin_bit_cast(f32x4, lr128(rp + 256));
            if constexpr (i == 9) sa[1] = __builtin_bit_cast(f32x4, lr128(rp + 512));
            if constexpr (i == 10) sb[1] = __builtin_bit_cast(f32x4, lr128(rp + 768));
            if constexpr (i == 11) bad = __builtin_bit_cast(u32x4, lr128(rp + 1024));
            if constexpr (i >= 12 && i < 20) stat(IC(i - 12));
            if constexpr (a == 40) { STAMP(s, 2) }
            if constexpr (a == UPOS0 || a == UPOS) st32(k.rs, fl, k.p0 + CNT(C_H, id), fv);
            if constexpr (a == 117) __builtin_amdgcn_raw_buffer_store_b128(o[0], k.rs, (int)sv, (int)ha, AUX_SC1);
            if constexpr (a == 118) TOUCH(o[0]);
            SCHED0();
            ep(IC(0), IC(0), IC(a - 56)); ep(IC(0), IC(1), IC(a - 86));
            if constexpr (a >= 116 && a < 124) stat(IC(a - 108));
            if constexpr (a == UPOS0 - 2 || a == UPOS - 2) { fv = a == UPOS - 2 ? f1 : f0; PIN(fv); }
            if constexpr (a == 80) { sv = l16 | r; PIN(sv); }
            if constexpr (a < 144) ep(IC(1), IC(0), IC(a - 124));
            else if constexpr (a < 164) ep(IC(1), IC(1), IC(a - 144));
            else ep(IC(1), IC(0), IC(a - 144));
            SCHED0();
        });
        STAMP(s, 3)
        asm volatile("s_nop 7" : "+v"(acc[1][3]));
        {
            const f32x4 u = (acc[1][3] + mean[1] * cs[3]) * rden[1];
            o[1][2] = packs(bfr(u[0]) * e[1][0], bfr(u[1]) * e[1][1]); o[1][3] = packs(bfr(u[2]) * e[1][2], bfr(u[3]) * e[1][3]);
        }
        st128(k.rs, sv, ha + EXP(49152), o[1]);
        if (r) { PCOUNT(0) RAWBAR() lw32(rp + 1024, 0u); RAWBAR() }
        st128(k.rs, s < 62 ? l16 : OOB, k.hseg((s + 2) & 63) + ho, ones); st128(k.rs, s < 62 ? l16 : OOB, k.hseg((s + 2) & 63) + EXP(49152) + ho, ones);
        STAMP(s, 4) STAMP(s, 5)
        PFLUSH(s)
        s = (f1 + 1) >> 1;
    }
    PROF_OUT
}

DEVI void down_role(const Params& p, const Ctx& k, char* smem, int id) {
    PROF_DECL
    const int l = k.l, w = k.w, g = l >> 4, n = l & 15, rt = id / 5, cg = id % 5;
    bf16x8 Wt[96];
    wstream<12, 8, 4>(smem + w * 32768,
        [&](auto U, unsigned sl) { constexpr int u = decltype(U)::value; sfor<8>([&](auto J) { constexpr int j = CV(J); dma1<(u % 4) * 8192 + j * 1024>(p.wd + (size_t)(w * 12 + u) * 32 * 640 + cg * 128, wsrc<256, false>(j * 64 + l, 1280), sl); }); },
        [&](auto U, char* sp) { constexpr int u = decltype(U)::value;
#pragma unroll
            for (int nt = 0; nt < 8; ++nt) Wt[u * 8 + nt] = wp_read<256, false>(sp, (nt + 2 * w) & 7, l); });
    RAWBAR()
    RSTAMP(2)
    const unsigned xo = EXP((cg * 4 + w) * 2048) + rt * 1024, xv = ((g >> 1) * 16 + n) * 16 + (g & 1) * 8;
    const u32x2 ones = {0xFFFFFFFFu, 0xFFFFFFFFu};
    const unsigned x2v = k.c ? xv : OOB;
    prog_init(k);
#pragma unroll
    for (int i = 0; i < 4; ++i) st64(k.rs, x2v, k.seg(PG_X2, i >> 1) + xo + (i & 1) * 512, ones);
    const __amdgpu_buffer_rsrc_t ro = __builtin_amdgcn_make_buffer_rsrc((void*)p.out, 0, 2048 * 1280, 0x00020000);
    const unsigned ov = k.c ? OOB : n * 1280 + g * 8, lim = k.c ? 0xFFFFFFFEu : 0xFFFFFFFFu, fl = (w == 0 && l == 0) ? 0u : OOB;
    unsigned fails = 0;
    for (int s = 0; s < 64; ++s) {
        unsigned la[3], ra = k.seg(PG_X1, s) + xo, oa = k.c ? k.seg(PG_X2, s) + xo : (s * 32 + rt * 16) * 1280 + (cg * 8 + w * 2) * 32, rb = (s & 1) * 32768 + l * 16, wb[3], fv = s;
#pragma unroll
        for (int q = 0; q < 3; ++q) { la[q] = k.hseg(s) + rt * EXP(49152) + EXP((w * 12 + q * 4) * 1024); wb[q] = rb + ((w + q + 1) & 3) * 6144 + q * 2048; PINS(la[q]); PIN(wb[q]); }
        rb += w * 6144;
        PINS(ra); PINS(oa); PIN(rb); PIN(fv);
        prog_wait<12>(k, k.p0 + CNT(C_H, 0), 2 * s + rt);
        STOP(s) STAMP(s, 0)
        f32x4 acc[8], r[6];
        u32x2 res[2];
        u32x4 hx[12];
        unsigned long long bad;
        do {
            asm volatile("" ::: "memory");
#pragma unroll
            for (int i = 0; i < 12; ++i) { hx[i] = ld128(k.rs, l * 16 + (i & 3) * 1024, la[i >> 2]); SCHED0(); }
#pragma unroll
            for (int t = 0; t < 2; ++t) res[t] = ld64(k.rs, xv + t * 512, ra);
            SCHED0();
            unsigned mx = 0;
            sfor<72>([&](auto I) { constexpr int i = CV(I), ks = i / 6, j = 2 + i % 6, f = ks * 8 + j;
                mfa<(f < 64), ks == 0>(acc[j], Wt[f], __builtin_bit_cast(bf16x8, hx[ks]));
                SCHED0();
                if constexpr (i % 6 < 2) { mx = umax3(mx, hx[ks][(i & 1) * 2], hx[ks][(i & 1) * 2 + 1]); PIN(mx); }
                if constexpr (i == 68) { bad = __ballot(mx == 0xFFFFFFFFu); PINS(bad); }
                if constexpr (i == 69) { bad = fails < 65536u ? bad : 0; PINS(bad); }
                if constexpr (i == 70) { ++fails; PINS(fails); }
                if constexpr (i == DPOS) st32(k.rs, fl, k.p0 + CNT(C_X2, rt * 5 + cg), fv);
                SCHED0(); });
        } while (__builtin_expect(bad != 0, 0));
        STAMP(s, 1)
        float x[8];
        sfor<24>([&](auto I) { constexpr int i = CV(I), ks = i / 2, j = i & 1, f = ks * 8 + j;
            mfa<(f < 64), ks == 0>(acc[j], Wt[f], __builtin_bit_cast(bf16x8, hx[ks]));
            SCHED0();
            if constexpr (i < 12 && !(i & 1)) { constexpr int jj = 2 + i / 2;
                asm volatile("" : "+v"(acc[jj])); lw128(wb[jj / 2 - 1] + (jj & 1) * 1024, __builtin_bit_cast(u32x4, acc[jj])); }
            if constexpr (i < 8 && (i & 1)) { x[i - 1] = blo(res[i >> 2][(i >> 1) & 1]); x[i] = bhi(res[i >> 2][(i >> 1) & 1]); PIN(x[i - 1]); PIN(x[i]); }
            if constexpr (i == 13) bar_lgkm<0>();
            if constexpr (i >= 13 && i < 19) r[i - 13] = __builtin_bit_cast(f32x4, lr128(rb + (i - 13) * 1024));
            if constexpr (i + 72 == DPOS) st32(k.rs, fl, k.p0 + CNT(C_X2, rt * 5 + cg), fv);
            SCHED0(); });
        r[0] += r[2] + r[4]; r[1] += r[3] + r[5];
        asm volatile("s_nop 3" : "+v"(acc[0]), "+v"(acc[1]), "+v"(r[0]), "+v"(r[1]));
#pragma unroll
        for (int t = 0; t < 2; ++t) {
            const f32x4 d = acc[t] + r[t];
            const u32x2 o = {min(cvtpk(x[4 * t] + bfr(d[0]), x[4 * t + 1] + bfr(d[1])), lim), min(cvtpk(x[4 * t + 2] + bfr(d[2]), x[4 * t + 3] + bfr(d[3])), lim)};
            st64(k.rs, x2v + t * 512, oa, o);
            __builtin_amdgcn_raw_buffer_store_b64(o, ro, (int)(ov + t * 32), (int)oa, 0);
        }
        STAMP(s, 2)
#pragma unroll
        for (int t = 0; t < 2; ++t) st64(k.rs, s < 62 ? x2v : OOB, k.seg(PG_X2, (s + 2) & 63) + xo + t * 512, ones);
        STAMP(s, 3) STAMP(s, 4) STAMP(s, 5)
        PFLUSH(s)
    }
    PROF_OUT
}

DEVI void warm(const Params& p, const Ctx& k, int xl, int hw) {
    set_m0(k.w * 256);
_Pragma("unroll 1")
    for (int it = hw; it < 32 * 15; it += k.c ? 40 : 64) {
        const int q = it < 120 ? it & 7 : 8 + (it - 120) % 24, t = it < 120 ? it >> 3 : (it - 120) / 24;
        const u16* b = q < 24 ? (q < 8 ? p.Wv : q < 16 ? p.Wk : p.Wq) + (xl * 8 + (q & 7)) * 192 : p.Wo + (xl * 8 + (q & 7)) * 122880;
        const unsigned ln = t * 64 + k.l, cl = min(((unsigned)(size_t)b >> 7) & 7u, 4u);
        if (k.c) sleep_until(k.t0 + WP + it * PP / 128);
        if (t < (q >= 24 ? PO : ((q < 8 ? PV : PQ) >> (cl * 4)) & 15))
            asm volatile("global_load_lds_dword %0, %1" :: "v"(q < 24 ? (639 - ln / 3) * 12288 + (ln % 3) * 128 : (191 - (ln >> 2)) * 1280 + 768 + (ln & 3) * 128), "s"(b) : "memory");
    }
    DRAIN();
_Pragma("unroll 1")
    for (int i = 3; i < 8; ++i) { sleep_until(k.t0 + i * 50); hello(k, 0); }
    DRAIN();
    RAWBAR()
}

DEVI int xcc_id() { return __builtin_amdgcn_s_getreg(20 | (0 << 6) | (3 << 11)) & 7; }

DEVI unsigned prepare(const Params& p, Ctx& k, char* smem, int xl, int i) {
    const int l = k.l, j = xl * 22 + i - 4;
    const u32x4 ones = {0xFFFFFFFFu, 0xFFFFFFFFu, 0xFFFFFFFFu, 0xFFFFFFFFu};
    unsigned vote = 0;
    if (i < 4) {
        const int row = ((i * 4 + xl) * 4 + k.w) * 4 + (l >> 4);
        const unsigned dst = k.seg(PG_XNA, i * 2 + (xl >> 1));
        u32x4 a[5];
#pragma unroll
        for (int c = 0; c < 5; ++c) a[c] = *(const u32x4*)(p.x + (size_t)row * 640 + (c * 16 + (l & 15)) * 8);
        ln_rows(a, l & 15, row & 31, [&](unsigned vo, int c, u32x4 o) { st128(k.rs, vo, dst + c * 16384, o); st128(k.rs, vo, dst + c * 16384 + 8192, o); });
    } else if (k.w == 3) {
        const unsigned cal = ((unsigned)(j % 44) << 21) + 7 * 16384 + (xl >> 1) * 4096 + (l & 7) * 16;
        int d = 0;
#pragma unroll 1
        for (int r = 0; r <= 2 * CALR; ++r) {
            unsigned long long c0, c1;
            st128(k.rs, cal, (r & 1) * 8192, ones);
            st128(k.rs, cal, (~r & 1) * 8192, ones);
            asm volatile("s_waitcnt vmcnt(1)\n\ts_memtime %0\n\ts_waitcnt vmcnt(0)\n\ts_memtime %1\n\ts_waitcnt lgkmcnt(0)" : "=&s"(c0), "=s"(c1) :: "memory");
            d = r ? min((int)(c1 - c0), 400) - d : 0;
        }
        vote = (unsigned)(d < 0) ^ (unsigned)(xcc_id() >> 2) ^ (unsigned)(i & 1);
    } else {
        if (k.w == 2 && j < 20) {
            const int e = j * 32 + (l >> 1), T = e >> 4, hf = (e >> 1) & 1, rr = ((e >> 2) & 3) * 2 + (e & 1);
            u32x4 v;
#pragma unroll
            for (int c = 0; c < 4; ++c) v[c] = min(*(const unsigned*)(p.mem0 + rr * 640 + T * 16 + 4 * c + 2 * hf), 0xFFFFFFFEu);
            st128(k.rs, (2u << 21) + EXP(e * 16) + (l & 1) * 8192, 0, v);
        }
        constexpr unsigned N0 = PR_B + PR_SZ, N1 = 40 * 16384, N2 = 32768;
        static_assert(N0 + N1 + N2 <= 4 * 88 * 3072 && NPAGES <= 44, "fill");
#pragma unroll
        for (int r = 0; r < 4; ++r) {
            const unsigned o = ((r * 88 + j) * 3 + k.w) * 1024;
            st128(k.rs, o < N0 + N1 + N2 ? l * 16 : OOB, o < N0 ? o : o < N0 + N1 ? ((unsigned)PG_YM << 21) + (o - N0) : (2u << 21) + 32768 + (o - N0 - N1), ones);
        }
    }
    if (k.w == 3) *(volatile LAS unsigned*)smem = vote;
    DRAIN();
    __builtin_amdgcn_s_waitcnt(0x0f70);
    XSTAMP(0)
    RAWBAR()
    return *(volatile LAS unsigned*)smem;
}
DEVI void master(const Params& p, const Ctx& k) {
    const int l = k.l;
    u32x2 v[4] = {{0u, 0u}, {0u, 0u}, {0u, 0u}, {0u, 0u}};
_Pragma("unroll 1")
    for (unsigned spins = 0; spins < SPIN_CAP; ++spins) {
#pragma unroll
        for (int q = 0; q < 4; ++q) v[q] |= cm_ld(k, (CM_IN + (q * 64 + l) * 128) | v[q][1]);
        if (__all((v[0][1] & v[1][1] & v[2][1] & v[3][1]) != 0)) break;
        asm volatile("" ::: "memory");
    }
    const unsigned long long a = __ballot(v[2][0] != 0), b = __ballot(v[3][0] != 0);
    const int n = __popcll(a) + __popcll(b);
    const unsigned long long g = n > 44 ? ~0ull : 0ull, u = (a & b) | ((a ^ b) & g);
    const unsigned long long t = (__popcll(a ^ b) > 2 || n > 80 || n < 8 ? g : ((u >> 4) & 0x3FFFFF) | ((u >> 36) << 22)) ^ 0xAAAAAAAAAAAAAAAAull;
#pragma unroll
    for (int q = 0; q < 4; ++q) cm_st(k, l * 128, CM_GO + q * 8192, (unsigned)t, (unsigned)(t >> 32) | 0x80000000u);
}

#define FIN(n) { __builtin_amdgcn_s_waitcnt(0x0f70); asm volatile("s_nop " #n); return; }
__global__ __launch_bounds__(256, 1) void mega_kernel(Params p) {
    extern __shared__ __attribute__((aligned(16))) char smem[];
    Ctx k;
    const u16 *wq = p.Wq, *wk = p.Wk, *wv = p.Wv;
    asm volatile("" : "+s"(wq), "+s"(wk), "+s"(wv) : "s"(p.cm));
    k.ws = p.ws; k.cm = p.cm;
    k.rs = __builtin_amdgcn_make_buffer_rsrc((void*)p.ws, 0, 0x7fffffff, 0x00020000);
    k.l = threadIdx.x & 63; k.w = __builtin_amdgcn_readfirstlane(threadIdx.x >> 6);
    RSTAMP(0)
    const int x = blockIdx.x & 7, i = blockIdx.x >> 3, xl = x & 3;
    k.role = x * 32 + i; k.c = x >> 2; k.par = 0; k.p0 = 0; k.t0 = rclk();
    line_st(k, CM_GO, 0, 0);
    auto start = [&]() {
        const u16* a = (i < 8 ? wk : wv) + (xl * 8 + (i & 7)) * 192, * b = (i < 8 ? wq : wv + 32 * 6144) + (xl * 8 + (i & 7)) * 192;
        unsigned o[3];
        sfor<18>([&](auto J) { constexpr int j = CV(J);
            if constexpr (j < 3) o[j] = wsrc<384, true>(k.w * 192 + j * 64 + k.l, 12288);
            dma1<SLOT(j / 6) + ((j / 3) & 1) * 12288 + (j % 3) * 1024>((((j / 3) & 1) ? b : a) + (j / 6) * (i < 8 ? 32 * 6144 : 64 * 6144), o[j % 3], k.w * 3072); }); };
    if (!k.c && i < 16) start();
    if (__builtin_expect(k.role == MASTER, 0)) {
#pragma unroll
        for (int q = 0; q < 16; ++q) cm_st(k, k.l * 8, CM_IN + k.w * 8192 + q * 512, 0, 0);
        cm_st(k, 0, CM_TMO, ~0u, ~0u);
        DRAIN();
        RAWBAR()
    }
    if (__builtin_expect(k.c && i < 26, 0)) { const unsigned v = prepare(p, k, smem, xl, i); RSTAMP(1) hello(k, v); go_wait(k, v); XSTAMP(1) RAWBAR() }
    else { RSTAMP(1) if (k.c) { DRAIN(); hello(k, 0); } }
    if (__builtin_expect(k.role == MASTER && k.w == 0, 0)) { master(p, k); __builtin_amdgcn_s_waitcnt(0x0f70); }
    if (k.c && i < 16) { sleep_until(k.t0 + WD1 + (i ^ 8) * STG); start(); }
    if (i < 8) { k.l = lane(); qk_role(p, k, smem, xl * 8 + i); FIN(0) }
    if (i < 16) { k.l = lane(); vo_role(p, k, smem, xl * 8 + i - 8); FIN(1) }
    k.l = lane();
    if (!k.c || i < 26) warm(p, k, xl, (i - 16) * 4 + k.w);
    else RAWBAR()
    go_wait(k, 0);
    if (i >= 26 && !k.c) {
        sleep_until(k.t0 + XPF);
#pragma unroll
        for (int r = 0; r < 3; ++r) asm volatile("global_load_lds_dword %0, %1" :: "v"(min(40 + r * 96 + ((i - 26) * 4 + xl) * 4 + k.w, 319) * 8192 + k.l * 128), "s"(p.x) : "memory");
        DRAIN();
        RAWBAR()
    }
    if (i >= 26) sleep_until(k.t0 + (k.c ? GST : GSA) + ((xl & 1) == UPODD ? 0 : i < 31 ? GSD : -GSA));
    if (i < 21) { mred_role(p, k, smem, xl * 5 + i - 16); FIN(2) }
    if (i < 26) { tred_role(p, k, smem, xl * 5 + i - 21); FIN(3) }
    if ((xl & 1) == UPODD) { up_role(p, k, smem, (xl >> 1) * 6 + i - 26); FIN(4) }
    if (i < 31) { down_role(p, k, smem, (xl >> 1) * 5 + i - 26); FIN(5) }
    row_role(p, k, smem, xl >> 1);
    FIN(6)
}

extern "C" void kernel_launch(void* const* d_in, const int* in_sizes, int n_in,
                              void* d_out, int out_size, void* d_ws, size_t ws_size,
                              hipStream_t stream) {
    Params p;
    p.x = (const u16*)d_in[0]; p.mem0 = (const u16*)d_in[1];
    p.Wq = (const u16*)d_in[2]; p.Wk = (const u16*)d_in[3]; p.Wv = (const u16*)d_in[4]; p.Wo = (const u16*)d_in[5];
    p.wg = (const u16*)d_in[8]; p.wu = (const u16*)d_in[9]; p.wd = (const u16*)d_in[10];
    p.out = (u16*)d_out; p.cm = (char*)d_ws;
    p.ws = (char*)(((uintptr_t)d_ws + CM_BYTES + ((1u << 21) - 1)) & ~(uintptr_t)((1u << 21) - 1));
    (void)hipFuncSetAttribute((const void*)mega_kernel, hipFuncAttributeMaxDynamicSharedMemorySize, LDS_BYTES);
    mega_kernel<<<256, 256, LDS_BYTES, stream>>>(p);
}
```
